```python
import torch

def _softmax(x, axis=-1):
    return torch.softmax(x, dim=axis)

def _gelu(x):
    return torch.nn.functional.gelu(x, approximate='tanh')

def _silu(x):
    return torch.nn.functional.silu(x)

def _sigmoid(x):
    return torch.sigmoid(x)

def _layernorm(x, axis=-1, eps=1e-05):
    m = x.mean(dim=axis, keepdim=True)
    v = x.var(dim=axis, keepdim=True, unbiased=False)
    return (x - m) / torch.sqrt(v + eps)

def _scatter_add(tgt, idx, src, axis=0):
    return tgt.clone().index_add_(axis, idx, src)

def _segment_sum(src, seg, n):
    out = torch.zeros((n,) + src.shape[1:], dtype=src.dtype)
    return out.index_add_(0, seg, src)

def _pad(x, pw):
    flat = tuple((v for pair in reversed(pw) for v in pair))
    return torch.nn.functional.pad(x, flat)
kernel_name = 'adaLN_cond__ssm_conv_h3__mixer_gmlp__prompt'

def setup_inputs():

    _w = lambda a: (lambda t: t.to(torch.bfloat16) if t.is_floating_point() else t)(torch.as_tensor(a))

    def _A_setup():
        B, S, D = (1, 2048, 640)
        ks = 4
        return {'q': _w((lambda *s: torch.randn(*s, dtype=torch.bfloat16) * 0.05)(B, S, D)), 'k': _w((lambda *s: torch.randn(*s, dtype=torch.bfloat16) * 1.6)(B, S, D)), 'v': _w((lambda *s: torch.randn(*s, dtype=torch.bfloat16) * 1.6)(B, S, D)), 'shift_k': _w((lambda *s: torch.randn(*s, dtype=torch.bfloat16) * 1.6)(D, ks) * 0.5), 'ssm_k': _w((lambda *s: torch.randn(*s, dtype=torch.bfloat16) * 1.6)(D, S) * (1.0 / S ** 0.5)), 'ssm_D': _w((lambda *s: torch.randn(*s, dtype=torch.bfloat16) * 1.6)(D))}
    _A_in = _A_setup()

    def _B_setup():
        B, S, D, F = (1, 2048, 640, 1728)
        Fh = F // 2
        return {'x': _w((lambda *s: torch.randn(*s, dtype=torch.bfloat16) * 0.05)(B, S, D)), 'w_in': _w((lambda *s: torch.randn(*s, dtype=torch.bfloat16) * 0.8)(D, 2 * Fh)), 'w_sp': _w(0.01 * (lambda *s: torch.randn(*s, dtype=torch.bfloat16) * 0.8)(S, S)), 'b_sp': _w(1.0 + 0.01 * (lambda *s: torch.randn(*s, dtype=torch.bfloat16) * 0.1)(S)), 'w_out': _w((lambda *s: torch.randn(*s, dtype=torch.bfloat16) * 0.1)(Fh, D))}
    _B_in = _B_setup()
    return {'A_q': _A_in['q'], 'A_k': _A_in['k'], 'A_v': _A_in['v'], 'A_shift_k': _A_in['shift_k'], 'A_ssm_k': _A_in['ssm_k'], 'A_ssm_D': _A_in['ssm_D'], 'B_x': _B_in['x'], 'B_w_in': _B_in['w_in'], 'B_w_sp': _B_in['w_sp'], 'B_b_sp': _B_in['b_sp'], 'B_w_out': _B_in['w_out'], 'c': _w((lambda *s: torch.randn(*s, dtype=torch.bfloat16) * 0.1)(1, 640) * 8.0), 'w_mod': _w((lambda *s: torch.randn(*s, dtype=torch.bfloat16) * 0.1)(640, 6 * 640) * 640 ** (-0.5) * 8.0)}

def reference(A_q, A_k, A_v, A_shift_k, A_ssm_k, A_ssm_D, B_x, B_w_in, B_w_sp, B_b_sp, B_w_out, c, w_mod):

    def _A_ref(q, k, v, shift_k, ssm_k, ssm_D):
        B, S, D = q.shape
        ks = shift_k.shape[-1]
        k_pad = _pad(k, ((0, 0), (ks - 1, 0), (0, 0)))
        k_shift = torch.zeros_like(k)
        for t in range(ks):
            k_shift = k_shift + k_pad[:, t:t + S, :] * shift_k[:, ks - 1 - t][None, None, :]
        kv = k_shift * v
        L = 2 * S
        kv_pad = _pad(kv, ((0, 0), (0, S), (0, 0)))
        kf_pad = _pad(ssm_k, ((0, 0), (0, S)))
        kv_f = (lambda x, axes=(-2, -1): torch.fft.rfft2(x.float(), dim=axes))(kv_pad[:, None, :, :], axes=(1, 2))
        kf_f = (lambda x, axes=(-2, -1): torch.fft.rfft2(x.float(), dim=axes))(kf_pad[:, None, :], axes=(1, 2))
        kf_f = (lambda x, *d: x.permute(*d))(kf_f, 1, 2, 0)
        y_f = kv_f * kf_f[None, :, :, :]
        y = (lambda x, s, axes=(-2, -1): torch.fft.irfft2(x, s=s, dim=axes).to(torch.bfloat16))(y_f, (1, L), axes=(1, 2))[:, 0, :S, :]
        y = y + kv * ssm_D[None, None, :]
        return q * y

    def _B_ref(x, w_in, w_sp, b_sp, w_out):
        B, S, D = x.shape
        F = w_in.shape[-1]
        Fh = F // 2
        z = _gelu(torch.einsum('bsd,df->bsf', x, w_in))
        z1 = z[..., :Fh].clone()
        z2 = _layernorm(z[..., Fh:], axis=-1)
        g = torch.einsum('ts,bsf->btf', w_sp, z2) + b_sp[None, :, None]
        return torch.einsum('bsf,fd->bsd', z1 * g, w_out)
    x = A_q
    mod = (_silu(c) @ w_mod).reshape(c.shape[0], 6, 1, -1)
    sa, ca, ga, sb, cb, gb = (mod[:, 0].clone(), mod[:, 1].clone(), mod[:, 2].clone(), mod[:, 3].clone(), mod[:, 4].clone(), mod[:, 5].clone())
    h = _layernorm(x, axis=-1) * (1 + ca) + sa
    x = x + ga * _A_ref(h, A_k, A_v, A_shift_k, A_ssm_k, A_ssm_D)
    h = _layernorm(x, axis=-1) * (1 + cb) + sb
    x = x + gb * _B_ref(h, B_w_in, B_w_sp, B_b_sp, B_w_out)
    return x
```

```cpp
#include <hip/hip_runtime.h>
#include <stdint.h>

typedef unsigned short u16;
typedef __attribute__((ext_vector_type(8))) short bf16x8;
typedef __attribute__((ext_vector_type(4))) short bf16x4;
typedef __attribute__((ext_vector_type(4))) float f32x4;
typedef __attribute__((ext_vector_type(2))) float f32x2;
typedef __attribute__((ext_vector_type(4))) unsigned int u32x4;
typedef __attribute__((ext_vector_type(2))) unsigned int u32x2;
#define LAS __attribute__((address_space(3)))
typedef __amdgpu_buffer_rsrc_t rsrc_t;
typedef __attribute__((ext_vector_type(4))) short s16x4;

constexpr int S_ = 2048, D_ = 640, F_ = 1728, FH = 864, CB = 108;

constexpr unsigned WS_EPOCH = 0;
constexpr unsigned WS_FWI = 4096;
constexpr unsigned WS_FZ = WS_FWI + 384 * 128;
constexpr unsigned WS_FWO = WS_FZ + 256 * 128;
constexpr unsigned F4S = 896 * 128;
constexpr unsigned WS_CS = WS_FZ + 256 * 128;
constexpr unsigned WS_MOD = 131072;
constexpr unsigned WS_MODP = WS_MOD + 81920;
constexpr unsigned WS_ST1 = 393216;
constexpr unsigned WS_PART = 524288;
constexpr unsigned WS_CSP = WS_PART + 262144;
constexpr unsigned WS_FX = WS_CSP + 655360;
constexpr unsigned WS_X1B = WS_FX + 16 * 4 * 160 * 128;
constexpr unsigned WS_QB = WS_X1B + 2621440;
constexpr unsigned WS_KV3 = WS_QB + 2621440;
constexpr unsigned WS_MODQ = WS_KV3 + 1310720;
constexpr unsigned WBLK = 143360;
constexpr unsigned WS_WINF = WS_MODQ + 245760;
constexpr unsigned WS_WOUTF = WS_WINF + 16 * WBLK;
constexpr unsigned WS_Z2F = WS_WOUTF + 8 * WBLK;
constexpr unsigned WS_UF = WS_Z2F + 8 * 64 * 7168;
constexpr unsigned WS_F4 = WS_UF + 32 * 4 * 8 * 5120;
constexpr unsigned WS_END = WS_F4 + 4 * F4S;
constexpr unsigned WS_STAMP = 200u << 20;

constexpr int G_COPY = 4160;
constexpr int L_U = WBLK;
constexpr int U_CH = (256 + S_) * 2 + 32;
constexpr int L_FLG = L_U + 4 * U_CH;
constexpr int L_RT = 16384;
constexpr int L_CONST = WBLK;
constexpr int L_PST = L_CONST + 2048;
constexpr int L_Z = L_CONST + 5120;
constexpr int L_ST2 = L_Z + 14336;
constexpr int L_MISC = L_ST2 + 512;
constexpr int LDS_TOTAL = 163840;

#define AUX_T 610
#define RT2_T 360
#define CB_T 380
#define W_T 730
#define CS_T 830
#define Y_T 930
#define K_T 460
#ifndef M_T
#define M_T 520
#endif
#define WA_T 580
constexpr int SC1 = 16, NT = 2;
constexpr unsigned SPIN_CAP = 1u << 16;

__device__ __forceinline__ float bflo(unsigned x) { return __uint_as_float(x << 16); }
__device__ __forceinline__ float bfhi(unsigned x) { return __uint_as_float(x & 0xffff0000u); }
__device__ __forceinline__ unsigned f2bf(float x) {
  __bf16 b = (__bf16)x;
  return (unsigned)__builtin_bit_cast(unsigned short, b);
}
__device__ __forceinline__ float rbf(float x) { return bflo(f2bf(x)); }
typedef __attribute__((ext_vector_type(2))) __bf16 bf2_t;
__device__ __forceinline__ unsigned pack2(float lo, float hi) {
  f32x2 v = {lo, hi};
  return __builtin_bit_cast(unsigned, __builtin_convertvector(v, bf2_t));
}
__device__ __forceinline__ f32x2 rbf2(f32x2 v) {
  const unsigned p = __builtin_bit_cast(unsigned, __builtin_convertvector(v, bf2_t));
  return (f32x2){__uint_as_float(p << 16), __uint_as_float(p & 0xffff0000u)};
}
__device__ __forceinline__ f32x4 rbf4(f32x4 v) {
  const f32x2 a = rbf2((f32x2){v[0], v[1]}), b = rbf2((f32x2){v[2], v[3]});
  return (f32x4){a[0], a[1], b[0], b[1]};
}
template <int CTRL>
__device__ __forceinline__ float dpp_f(float v) {
  return __int_as_float(__builtin_amdgcn_update_dpp(0, __float_as_int(v), CTRL, 0xf, 0xf, true));
}
__device__ __forceinline__ float sum8(float v) {
  v += dpp_f<0xB1>(v);
  v += dpp_f<0x4E>(v);
  v += dpp_f<0x141>(v);
  return v;
}
__device__ __forceinline__ float sum16(float v) {
  v = sum8(v);
  v += dpp_f<0x140>(v);
  return v;
}
__device__ __forceinline__ float sum_rows(float v) {
  auto a = __builtin_amdgcn_permlane16_swap(__float_as_uint(v), __float_as_uint(v), false, false);
  v = __uint_as_float(a[0]) + __uint_as_float(a[1]);
  auto b = __builtin_amdgcn_permlane32_swap(__float_as_uint(v), __float_as_uint(v), false, false);
  return __uint_as_float(b[0]) + __uint_as_float(b[1]);
}
#define MFMA16(a, b, c) __builtin_amdgcn_mfma_f32_16x16x32_bf16(__builtin_bit_cast(bf16x8, a), __builtin_bit_cast(bf16x8, b), c, 0, 0, 0)
#define MFMA16K16(a, b, c) __builtin_amdgcn_mfma_f32_16x16x16bf16_1k(__builtin_bit_cast(bf16x4, a), __builtin_bit_cast(bf16x4, b), c, 0, 0, 0)
__device__ __forceinline__ u32x2 trd(const char* p) {
  return __builtin_bit_cast(u32x2, __builtin_amdgcn_ds_read_tr16_b64_v4i16((LAS s16x4*)p));
}
__device__ __forceinline__ u32x4 trfrag(const char* p, int pitch) {
  u32x2 a = trd(p), b = trd(p + 16 * pitch);
  return (u32x4){a[0], a[1], b[0], b[1]};
}
#define BAR() asm volatile("s_waitcnt lgkmcnt(0)\n\ts_barrier" ::: "memory")
#define DRAIN() do { __builtin_amdgcn_s_waitcnt(0x0F70); asm volatile("" ::: "memory"); } while (0)
#define SCHED() __builtin_amdgcn_sched_barrier(0)
template <int N> struct IC { static constexpr int value = N; };
template <int I, int N, class F>
__device__ __forceinline__ void sfor(F&& f) {
  if constexpr (I < N) { f(IC<I>()); sfor<I + 1, N>(f); }
}

__device__ __forceinline__ u32x4 ld16(rsrc_t r, unsigned off) { return __builtin_amdgcn_raw_buffer_load_b128(r, off, 0, SC1); }
__device__ __forceinline__ u32x2 ld8(rsrc_t r, unsigned off) { return __builtin_amdgcn_raw_buffer_load_b64(r, off, 0, SC1); }
__device__ __forceinline__ unsigned ld4(rsrc_t r, unsigned off) { return __builtin_amdgcn_raw_buffer_load_b32(r, off, 0, SC1); }
#define ST_AUX 16
__device__ __forceinline__ void st16(rsrc_t r, unsigned off, u32x4 v) { __builtin_amdgcn_raw_buffer_store_b128(v, r, off, 0, ST_AUX); }
__device__ __forceinline__ void st8(rsrc_t r, unsigned off, u32x2 v) { __builtin_amdgcn_raw_buffer_store_b64(v, r, off, 0, SC1); }
__device__ __forceinline__ void st_flag4(rsrc_t r, unsigned off, unsigned tag, int l) {
  if (l < 32) st16(r, WS_F4 - WS_FWI + off + (unsigned)(l >> 3) * F4S + (unsigned)(l & 7) * 16u, (u32x4){tag, tag, tag, tag});
}
__device__ __forceinline__ void st4(rsrc_t r, unsigned off, unsigned v) { __builtin_amdgcn_raw_buffer_store_b32(v, r, off, 0, SC1); }
#define BULK_AUX 16
__device__ __forceinline__ u32x4 ld16p(rsrc_t r, unsigned off) { return __builtin_amdgcn_raw_buffer_load_b128(r, off, 0, BULK_AUX); }
__device__ __forceinline__ u32x2 ld8p(rsrc_t r, unsigned off) { return __builtin_amdgcn_raw_buffer_load_b64(r, off, 0, BULK_AUX); }
__device__ __forceinline__ void dma1k(rsrc_t r, char* lds_dst, unsigned l16, unsigned soff) {
  __builtin_amdgcn_raw_ptr_buffer_load_lds(r, (LAS void*)lds_dst, 16, l16, soff, 0, BULK_AUX);
}

#define STAMP(i)
#define STAMPF(i)
#define STAMPT(i) STAMP(i)

template <int N, int SLP>
__device__ __forceinline__ void wait_lines(rsrc_t r, const unsigned (&off)[N], unsigned tag) {
  bool need[N];
#pragma unroll
  for (int i = 0; i < N; ++i) need[i] = off[i] != ~0u;
  _Pragma("unroll 1") for (unsigned spins = 0; spins < SPIN_CAP; ++spins) {
    unsigned v[N];
#pragma unroll
    for (int i = 0; i < N; ++i) {
      v[i] = tag;
      if (need[i]) v[i] = ld4(r, off[i]);
    }
    bool any = false;
#pragma unroll
    for (int i = 0; i < N; ++i) { need[i] = v[i] != tag; any = any || need[i]; }
    if (!__any(any)) break;
    __builtin_amdgcn_s_sleep(SLP);
  }
  asm volatile("" ::: "memory");
}

__device__ __forceinline__ int tau(int rt, int n) { return 32 * (rt >> 1) + 8 * (n >> 2) + 4 * (rt & 1) + (n & 3); }
__device__ __forceinline__ int psi(int n) { return 2 * (n & 7) + (n >> 3); }
#define QX_CM(d, s0, s1, P) "v_cndmask_b32_dpp " d ", " s0 ", " s1 ", vcc quad_perm:" P " row_mask:0xf bank_mask:0xf\n\t"
#define QX_BODY(M, P) \
  asm("s_mov_b32 vcc_lo, " M "\n\ts_mov_b32 vcc_hi, " M "\n\t" \
      QX_CM("%0", "%12", "%8", P) "v_xor_b32 %4, %8, %0\n\t" QX_CM("%1", "%13", "%9", P) "v_xor_b32 %5, %9, %1\n\t" \
      QX_CM("%2", "%14", "%10", P) "v_xor_b32 %6, %10, %2\n\t" QX_CM("%3", "%15", "%11", P) "v_xor_b32 %7, %11, %3" \
      : "=&v"(t[0]), "=&v"(t[1]), "=&v"(t[2]), "=&v"(t[3]), "=&v"(u[0]), "=&v"(u[1]), "=&v"(u[2]), "=&v"(u[3]) \
      : "v"(a[0]), "v"(a[1]), "v"(a[2]), "v"(a[3]), "v"(b[0]), "v"(b[1]), "v"(b[2]), "v"(b[3]) : "vcc")
template <int BIT>
__device__ __forceinline__ void quad_xchg3(u32x4& a, u32x4& b) {
  unsigned t[4], u[4];
  if (BIT == 0) QX_BODY("0x55555555", "[1,0,3,2]");
  else QX_BODY("0x33333333", "[2,3,0,1]");
  a = (u32x4){t[0], t[1], t[2], t[3]};
#pragma unroll
  for (int d = 0; d < 4; ++d) b[d] ^= (unsigned)__builtin_amdgcn_update_dpp(0, (int)u[d], BIT == 0 ? 0xB1 : 0x4E, 0xf, 0xf, true);
}
__device__ __forceinline__ void quad_transpose(u32x4& x0, u32x4& x1, u32x4& x2, u32x4& x3) {
  quad_xchg3<0>(x0, x1);
  quad_xchg3<0>(x2, x3);
  quad_xchg3<1>(x0, x2);
  quad_xchg3<1>(x1, x3);
  asm volatile("" : "+v"(x0), "+v"(x1), "+v"(x2), "+v"(x3));
}

struct Params {
  const u16 *q, *k, *v, *shift_k, *ssm_k, *ssm_D, *w_in, *w_sp, *b_sp, *w_out, *cvec, *w_mod;
  u16* out;
  char* ws;
};

__global__ __launch_bounds__(512, 2) void mega(Params p) {
  __shared__ __attribute__((aligned(16))) char lds[LDS_TOTAL];
  {
    unsigned long long pc0;
    unsigned vt;
    asm volatile("v_readfirstlane_b32 vcc_lo, %3\n\ts_cmpk_lt_u32 %2, 192\n\ts_cbranch_scc1 4f\n\ts_cmpk_lt_u32 vcc_lo, 64\n\ts_cbranch_scc0 4f\n\t"
                 "s_lshr_b32 vcc_hi, %2, 3\n\ts_sub_u32 vcc_hi, vcc_hi, 24\n\ts_lshl_b32 vcc_hi, vcc_hi, 13\n\t"
                 "v_lshlrev_b32 %1, 7, %3\n\tv_add_u32 %1, vcc_hi, %1\n\ts_getpc_b64 %0\n.Lmega_here:\n\t"
                 "v_min_u32 %1, mega_tail-.Lmega_here-4, %1\n\ts_movk_i32 m0, 0x2000\n\ts_nop 1\n\tglobal_load_lds_dword %1, %0\n4:\n\t"
                 "s_cmpk_lt_u32 %2, 160\n\ts_cbranch_scc0 1f\n\ts_cmpk_lt_u32 vcc_lo, 256\n\ts_cbranch_scc1 3f\n\t"
                 "s_call_b64 vcc, .Ltouch1\n\ts_branch 3f\n1:\n\t"
                 "s_cmpk_lt_u32 vcc_lo, 256\n\ts_cbranch_scc1 2f\n\ts_cmpk_lt_u32 %2, 224\n\ts_cbranch_scc0 2f\n\ts_call_b64 vcc, .Ltouch3\n\ts_branch 3f\n"
                 "2:\n\ts_call_b64 vcc, .Ltouch2\n3:"
                 : "=&s"(pc0), "=&v"(vt) : "s"(blockIdx.x), "v"(threadIdx.x) : "vcc", "scc", "memory");
  }
  SCHED();
  const int tid = threadIdx.x, l = tid & 63;
  const int w = __builtin_amdgcn_readfirstlane(tid >> 6);
  const int bid = blockIdx.x, xcd = bid & 7, idx = bid >> 3;
#define TOUCH_AT(n) asm volatile("s_branch 1f\n.Ltouch" #n ":\n\ts_setpc_b64 vcc\n1:")
  char* const ws = p.ws;
  asm volatile("" ::"s"(p.q), "s"(p.k), "s"(p.v), "s"(p.shift_k), "s"(p.ssm_k), "s"(p.ssm_D), "s"(p.w_in), "s"(p.w_sp), "s"(p.b_sp),
               "s"(p.w_out), "s"(p.cvec), "s"(p.w_mod), "s"(p.out), "s"(p.ws));
  unsigned epoch, tag, t_start;
  unsigned long long t64;
  asm volatile("s_load_dword %0, %2, 0x0\n\ts_memrealtime %1" : "=&s"(epoch), "=&s"(t64) : "s"(ws));
#define TAG_READY() do { SCHED(); \
    asm volatile("s_waitcnt lgkmcnt(0)\n\ts_mul_i32 %0, %2, 0x9e3779b1\n\ts_add_i32 %0, %0, 0x9e3779b9\n\ts_mov_b64 %1, %3" \
                 : "=&s"(tag), "=&s"(t64r) : "s"(epoch), "s"(t64) : "scc"); t_start = (unsigned)t64r; SCHED(); } while (0)
  unsigned long long t64r;
  const rsrc_t rs = __builtin_amdgcn_make_buffer_rsrc(ws, 0, WS_END, 0x00020000);
  const unsigned l16 = (unsigned)l * 16u;
  STAMP(0);
  const bool is_conv = idx < 20;
  const int quad = xcd * 20 + idx;
  const int R = is_conv ? (xcd & 3) * 5 + (idx >> 2) : (xcd & 3) * 3 + 15 + (idx >> 2);
  const int c = (xcd >> 2) * 4 + (idx & 3);
  const unsigned fwi = WS_F4 + (unsigned)(xcd & 3) * F4S, fz = fwi + (WS_FZ - WS_FWI);
  const int n16 = l & 15, h4 = l >> 4;
  u32x4 wsp[4][8];
  const int rotg = (idx >> 4) & 1, rotb = (idx >> 2) & 3;
  const rsrc_t rsp = __builtin_amdgcn_make_buffer_rsrc((void*)p.w_sp, 0, S_ * S_ * 2, 0x00020000);
  const unsigned wsp_vo = (unsigned)(((l >> 2) & 3) * 8 * 4096 + h4 * 64 + ((l + rotb) & 3) * 16);
#define LDW(k) wsp[((k) >> 2) & 3][((k) >> 4) * 4 + ((k) & 3)] = __builtin_amdgcn_raw_buffer_load_b128(rsp, wsp_vo, \
    (unsigned)((R * 64 + 32 * (((k) >> 3) & 1) + 4 * (((k) >> 2) & 1) + ((k) & 3)) * 4096 + w * 512 + ((((k) >> 4) ^ rotg) * 256)), 0)
#define WSP_TR(g) quad_transpose(wsp[(g) & 3][((g) >> 2) * 4], wsp[(g) & 3][((g) >> 2) * 4 + 1], wsp[(g) & 3][((g) >> 2) * 4 + 2], wsp[(g) & 3][((g) >> 2) * 4 + 3])
  const rsrc_t rwi = __builtin_amdgcn_make_buffer_rsrc((void*)p.w_in, 0, D_ * F_ * 2, 0x00020000);
  const rsrc_t rwo = __builtin_amdgcn_make_buffer_rsrc((void*)p.w_out, 0, FH * D_ * 2, 0x00020000);
  const rsrc_t rq = __builtin_amdgcn_make_buffer_rsrc((void*)p.q, 0, S_ * D_ * 2, 0x00020000);
  auto hold = [&](unsigned tt) { while ((unsigned)__builtin_amdgcn_s_memrealtime() - t_start < tt) __builtin_amdgcn_s_sleep(2); };
  volatile LAS unsigned* lf = (volatile LAS unsigned*)(lds + L_FLG);
  auto lf_set = [&](int i) { asm volatile("s_waitcnt lgkmcnt(0)" ::: "memory"); if (l == 0) lf[i] = tag; };
  auto lf_wait = [&](int i0, int n) {
    _Pragma("unroll 1") for (unsigned spins = 0; spins < (SPIN_CAP << 4); ++spins) {
      const unsigned v = lf[i0 + (l & (n - 1))];
      if (__all(v == tag)) break;
      __builtin_amdgcn_s_sleep(1);
    }
    asm volatile("" ::: "memory");
  };
  auto xflag = [&](int s) {
    st16(rs, WS_FX + (unsigned)(((4 * s + (w & 3)) * 4 + ((l >> 3) & 3)) * 160 + quad) * 128u + (unsigned)(l & 7) * 16u, (u32x4){tag, tag, tag, tag});
    SCHED();
  };
  auto load_b2 = [&]() {
#pragma unroll
    for (int k = 0; k < 35; ++k) {
      int i = k + (idx >> 2) * 4;
      i = (i >= 35 ? i - 35 : i) + 35 * w;
      dma1k(rs, lds + i * 1024, l16, WS_WINF + (unsigned)(8 + c) * WBLK + (unsigned)i * 1024u);
    }
  };
  char* const wo_st = lds + 122880;
  auto wo_ld = [&]() {
    const int U = R < 28 ? R : 0, u2 = U >> 1, pt = u2 / 6, f6 = u2 % 6;
    const int c0 = 4 * (U & 1) + 2 * (pt < 2 ? f6 / 3 : f6), fi = f6 % 3;
    const int b0 = pt < 2 ? CB * (c0 + (fi >> 1)) + 48 * pt + 2 * (fi & 1) : CB * c0 + 96, d1 = pt < 2 ? 16 : 2, d2 = pt < 2 ? 32 : CB;
    const int b3 = pt < 2 ? CB * (c0 + 1) + 48 * pt + 16 * fi + 2 : b0 + CB + 2;
#pragma unroll
    for (int r = 0; r < 5; ++r) {
      const int g = r * 64 + l, rho = g / 10;
      const int fb = (rho & 16) ? ((rho & 2) ? b3 : b0 + d2) : ((rho & 2) ? b0 + d1 : b0);
      __builtin_amdgcn_raw_ptr_buffer_load_lds(rwo, (LAS void*)(wo_st + r * 1024), 16, (unsigned)((fb + (rho & 13)) * (D_ * 2) + (g % 10) * 16 + c * 160), 0, 0, 0);
    }
  };
  auto rt_ld = [&](int i0, char* st) {
    const int cb = (i0 < 160 ? 8 : 0) + (i0 & 7), Q = (i0 < 160 ? i0 : i0 - 160) >> 3;
#pragma unroll
    for (int r = 0; r < 7; ++r) {
      const int g = r * 64 + l;
      __builtin_amdgcn_raw_ptr_buffer_load_lds(rwi, (LAS void*)(st + r * 1024), 16, (unsigned)((g / 14) * (F_ * 2) + (g % 14) * 16 + Q * 32 * F_ * 2 + cb * CB * 2),
                                               0, 0, NT);
    }
  };
  auto rt_job = [&](int i0, char* st) {
    const int cb = (i0 < 160 ? 8 : 0) + (i0 & 7), Q = (i0 < 160 ? i0 : i0 - 160) >> 3;
    const int col = 640 * (l >> 5) + 32 * Q + (l & 31);
    const unsigned po = WS_MODP + (unsigned)col * 8u;
    u32x2 pp8[8];
    hold(CB_T);
    _Pragma("unroll 1") for (unsigned spins = 0; spins < SPIN_CAP; ++spins) {
      unsigned dd = 0u;
#pragma unroll
      for (int i = 0; i < 8; ++i) pp8[i] = ld8(rs, po + (unsigned)i * 10240u);
#pragma unroll
      for (int i = 0; i < 8; ++i) dd |= pp8[i][0] ^ tag;
      if (__all(dd == 0u)) break;
      __builtin_amdgcn_s_sleep(4);
    }
    STAMPF(9);
    {
      float t = 0.f;
#pragma unroll
      for (int i = 0; i < 8; ++i) t += __uint_as_float(pp8[i][1]);
      if (l >= 32) *(float*)(st + 7168 + (l - 32) * 4) = rbf(1.0f + rbf(t));
      else *(u16*)(st + 7296 + l * 2) = (u16)f2bf(t);
    }
    DRAIN();
    asm volatile("s_waitcnt lgkmcnt(0)" ::: "memory");
    const f32x4 ca = *(const f32x4*)(st + 7168 + h4 * 16), cc = *(const f32x4*)(st + 7168 + 64 + h4 * 16);
    const char* bp = st + (4 * h4 + (n16 >> 2)) * 224 + (n16 & 3) * 8;
    u32x4 x[7], o[7];
#pragma unroll
    for (int j = 0; j < 7; ++j) {
      x[j] = trfrag(bp + j * 32, 224);
      o[j] = (u32x4){pack2(bflo(x[j][0]) * ca[0], bfhi(x[j][0]) * ca[1]), pack2(bflo(x[j][1]) * ca[2], bfhi(x[j][1]) * ca[3]),
                     pack2(bflo(x[j][2]) * cc[0], bfhi(x[j][2]) * cc[1]), pack2(bflo(x[j][3]) * cc[2], bfhi(x[j][3]) * cc[3])};
      if (j == 6 && n16 == 12) o[j] = (u32x4){0x3F803F80u, 0x3F803F80u, 0x3F803F80u, 0x3F803F80u};
      st16(rs, WS_WINF + (unsigned)cb * WBLK + (unsigned)(Q * 7 + j) * 1024u + l16, o[j]);
    }
    STAMPF(11);
    const u32x2 e0 = *(const u32x2*)(st + 7296 + h4 * 8), e1 = *(const u32x2*)(st + 7328 + h4 * 8);
    f32x4 d[2] = {{0.f, 0.f, 0.f, 0.f}, {0.f, 0.f, 0.f, 0.f}};
#pragma unroll
    for (int j = 0; j < 7; ++j) {
      const unsigned one = n16 == 4 * (j & 3) ? 0x3F803F80u : 0u;
      const bool sr = n16 == 4 * (j & 3) + 1;
      d[j >> 2] = MFMA16(((u32x4){one, one, one, one}), o[j], d[j >> 2]);
      d[j >> 2] = MFMA16(((u32x4){sr ? e0[0] : 0u, sr ? e0[1] : 0u, sr ? e1[0] : 0u, sr ? e1[1] : 0u}), x[j], d[j >> 2]);
    }
#pragma unroll
    for (int m = 0; m < 2; ++m)
      st16(rs, WS_CSP + (unsigned)((cb * 20 + Q) * 2 + m) * 1024u + l16, (u32x4){tag, __float_as_uint(d[m][0]), tag, __float_as_uint(d[m][1])});
    STAMPF(13);
    asm volatile("s_waitcnt vmcnt(2)" ::: "memory");
    st_flag4(rs, WS_FWI + (unsigned)(cb * 20 + Q) * 128u, tag, l);
  };

  auto red8 = [&](float v) { v += dpp_f<0x128>(v); return sum_rows(v); };
  const rsrc_t rwm = __builtin_amdgcn_make_buffer_rsrc((void*)p.w_mod, 0, D_ * 3840 * 2, 0x00020000);
  auto gemv_load = [&](u32x4* pre, int blk, int sl, int np) {
    pre[10][0] = *(const unsigned*)(p.cvec + 8 * np * sl + 2 * (l < 4 * np ? l : 0));
#pragma unroll
    for (int pp = 0; pp < np; ++pp)
      pre[pp] = __builtin_amdgcn_raw_buffer_load_b128(rwm, (unsigned)((l >> 3) * 7680 + (l & 7) * 16), (unsigned)((8 * np * sl + pp * 8) * 7680 + blk * 128), NT);
  };
  auto gemv_fin = [&](float* acc, const u32x4* pre, int np, char* scr) {
    const unsigned cv2 = pre[10][0];
    unsigned* svt = (unsigned*)scr + w * 96;
#pragma unroll
    for (int e = 0; e < 2; ++e) {
      const int row = 2 * l + e;
      const float cv = e ? bfhi(cv2) : bflo(cv2);
      if (l < 4 * np) svt[(row & 7) * 12 + (row >> 3)] = __float_as_uint(rbf(cv * __builtin_amdgcn_rcpf(1.0f + __expf(-cv))));
    }
    asm volatile("s_waitcnt lgkmcnt(0)" ::: "memory");
    u32x4 sv[3];
#pragma unroll
    for (int i = 0; i < (np + 3) / 4; ++i) sv[i] = *(const u32x4*)(svt + (l >> 3) * 12 + 4 * i);
    SCHED();
#pragma unroll
    for (int e = 0; e < 8; ++e) acc[e] = 0.f;
#pragma unroll
    for (int pp = 0; pp < np; ++pp) {
      const unsigned sh = sv[pp >> 2][pp & 3], sl2 = sh >> 16;
#pragma unroll
      for (int e = 0; e < 4; ++e) {
        const unsigned x = pre[pp][e];
        acc[2 * e] = __builtin_amdgcn_fdot2_f32_bf16(__builtin_bit_cast(bf2_t, x), __builtin_bit_cast(bf2_t, sl2), acc[2 * e], false);
        acc[2 * e + 1] = __builtin_amdgcn_fdot2_f32_bf16(__builtin_bit_cast(bf2_t, x), __builtin_bit_cast(bf2_t, sh), acc[2 * e + 1], false);
      }
    }
#pragma unroll
    for (int e = 0; e < 8; ++e) acc[e] = red8(acc[e]);
  };
  auto gemv_put = [&](const float* acc, unsigned off) {
    float v = acc[0];
#pragma unroll
    for (int e = 1; e < 8; ++e) { v = (l >> 3) == e ? acc[e] : v; asm("" : "+v"(v)); }
    st8(rs, off + (unsigned)l * 8u, (u32x2){tag, (unsigned)__builtin_amdgcn_ds_bpermute(((l & 7) * 8 + (l >> 3)) * 4, (int)__float_as_uint(v))});
  };
  typedef const __attribute__((address_space(4))) unsigned* cptr_t;

  if (__builtin_expect(is_conv, 1)) {
    if (w >= 4) {
      TOUCH_AT(1);
      const int pw = w - 4;
      const rsrc_t ra = __builtin_amdgcn_make_buffer_rsrc((void*)((idx & 2) ? p.v : p.k), 0, S_ * D_ * 2, 0x00020000);
      const rsrc_t rb = __builtin_amdgcn_make_buffer_rsrc((void*)((idx & 2) ? p.k : p.v), 0, S_ * D_ * 2, 0x00020000);
      const rsrc_t rk = __builtin_amdgcn_make_buffer_rsrc((void*)p.k, 0, S_ * D_ * 2, 0x00020000);
      const unsigned vo = (unsigned)((128 * pw + 2 * l) * (D_ * 2));
      const unsigned so0 = (unsigned)((idx & 1) * (D_ * 2) + quad * 8), so1 = (unsigned)((1 - (idx & 1)) * (D_ * 2) + quad * 8);
      u32x2 g[3][4], kh;
      auto gl = [&](int s, int j0, int j1) {
        __builtin_amdgcn_s_setprio(0);
#pragma unroll
        for (int j = j0; j < j1; ++j) {
          g[s][j] = __builtin_amdgcn_raw_buffer_load_b64(j < 2 ? ra : rb, vo, ((j & 1) ? so1 : so0) + (unsigned)(s * 512 * D_ * 2), 0);
          SCHED();
        }
        __builtin_amdgcn_s_setprio(3);
      };
      const cptr_t sp = (cptr_t)(p.shift_k + quad * 16);
      unsigned spv[8];
#pragma unroll
      for (int i = 0; i < 8; ++i) spv[i] = sp[i];
      __builtin_amdgcn_s_sleep(3);
      gl(0, 0, 4);
      {
        const int row = 512 * (l >> 2) + 128 * pw - 3 + (l & 3);
        kh = __builtin_amdgcn_raw_buffer_load_b64(rk, (l >= 12 || (l & 3) == 3 || row < 0) ? 0x80000000u : (unsigned)(row * D_ * 2 + quad * 8), 0, 0);
      }
      TAG_READY();
      f32x2 wt[2][4];
#pragma unroll
      for (int d = 0; d < 2; ++d) {
        wt[d][0] = (f32x2){bflo(spv[4 * d]), bflo(spv[4 * d + 2])}; wt[d][1] = (f32x2){bfhi(spv[4 * d]), bfhi(spv[4 * d + 2])};
        wt[d][2] = (f32x2){bflo(spv[4 * d + 1]), bflo(spv[4 * d + 3])}; wt[d][3] = (f32x2){bfhi(spv[4 * d + 1]), bfhi(spv[4 * d + 3])};
        asm volatile("" : "+s"(wt[d][0]), "+s"(wt[d][1]), "+s"(wt[d][2]), "+s"(wt[d][3]));
      }
      SCHED();
      {
        const int j = idx * 4 + pw;
        const unsigned o = (unsigned)((512 + 64 * (j >> 2) + l) * (D_ * 2) + (((160 * xcd) >> 7) + ((j >> 1) & 1)) * 128);
        __builtin_amdgcn_raw_ptr_buffer_load_lds((j & 1) ? ra : rb, (LAS void*)(lds + 98304), 4, j < 64 ? o : 0x80000000u, 0, 0, 0);
      }
      SCHED();
      gl(1, 0, 4);
      STAMP(21);
      char* up = lds + L_U + 512 + 256 * pw + (l >> 2) * 16 + (3 - (l & 3)) * 4;
      auto kv = [&](auto S__, auto N__) {
        constexpr int s = decltype(S__)::value;
        u32x2* x = g[s];
        SCHED();
        asm volatile("s_waitcnt vmcnt(%5)" : "+v"(x[0]), "+v"(x[1]), "+v"(x[2]), "+v"(x[3]), "+v"(kh) : "n"(decltype(N__)::value));
        if (s == 2) STAMP(27);
        if (idx & 1) { asm volatile(""); const u32x2 t = x[0], u = x[2]; x[0] = x[1]; x[1] = t; x[2] = x[3]; x[3] = u; }
        asm volatile("" : "+v"(x[0]), "+v"(x[1]), "+v"(x[2]), "+v"(x[3]));
        if (idx & 2) { asm volatile(""); const u32x2 t = x[0], u = x[1]; x[0] = x[2]; x[1] = x[3]; x[2] = t; x[3] = u; }
        asm volatile("" : "+v"(x[0]), "+v"(x[1]), "+v"(x[2]), "+v"(x[3]));
        f32x2 kf[2][5], o[2][2];
#pragma unroll
        for (int d = 0; d < 2; ++d) {
          unsigned kk[5];
          kk[3] = x[0][d]; kk[4] = x[1][d];
          kk[2] = __builtin_amdgcn_update_dpp((unsigned)__builtin_amdgcn_readlane(kh[d], 4 * s + 2), kk[4], 0x138, 0xf, 0xf, false);
          kk[1] = __builtin_amdgcn_update_dpp((unsigned)__builtin_amdgcn_readlane(kh[d], 4 * s + 1), kk[3], 0x138, 0xf, 0xf, false);
          kk[0] = __builtin_amdgcn_update_dpp((unsigned)__builtin_amdgcn_readlane(kh[d], 4 * s), kk[2], 0x138, 0xf, 0xf, false);
#pragma unroll
          for (int j = 0; j < 5; ++j) kf[d][j] = (f32x2){bflo(kk[j]), bfhi(kk[j])};
        }
#pragma unroll
        for (int t = 0; t < 5; ++t) {
#pragma unroll
          for (int i = 0; i < 4; ++i) {
            const int d = i >> 1, r = i & 1;
            if (t == 4) o[d][r] *= (f32x2){bflo(x[2 + r][d]), bfhi(x[2 + r][d])};
            else if (t == 0) o[d][r] = kf[d][r + 3] * wt[d][0];
            else o[d][r] = __builtin_elementwise_fma(kf[d][r + 3 - t], wt[d][t], o[d][r]);
          }
          asm volatile("" : "+v"(o[0][0]), "+v"(o[0][1]), "+v"(o[1][0]), "+v"(o[1][1]));
        }
#pragma unroll
        for (int i = 0; i < 4; ++i) *(unsigned*)(up + i * U_CH + s * 1024) = pack2(o[i >> 1][1][i & 1], o[i >> 1][0][i & 1]);
        lf_set(4 * s + pw);
        SCHED();
      };
      const char* yr = lds + L_U + (n16 >> 2) * U_CH + (128 * pw + 32 * h4 + 8 * (n16 & 1) + 4 * ((n16 >> 1) & 1)) * 2;
      const unsigned yo = (unsigned)(8 * pw + (l >> 3)) * 20480u + (unsigned)quad * 128u + (unsigned)(l & 7) * 16u;
      const unsigned so = WS_ST1 + (unsigned)xcd * 16384u + (unsigned)pw * 1024u + l16;
      const unsigned mo = WS_MODQ + (unsigned)((n16 < 12 ? n16 >> 2 : 0) * D_ + quad * 4 + (l & 3)) * 8u + (unsigned)h4 * 40960u;
      u32x4 sv[4], qv[4];
      u32x2 mv[2];
      auto ld_e = [&]() {
#pragma unroll
        for (int s = 0; s < 4; ++s) sv[s] = ld16(rs, so + (unsigned)s * 4096u);
        SCHED();
      };
      auto s_ok = [&]() {
        unsigned d = 0u;
#pragma unroll
        for (int s = 0; s < 4; ++s) d |= (sv[s][1] ^ tag) | (sv[s][3] ^ tag);
        return __all(d == 0u);
      };
      kv(IC<0>(), IC<5>());
      STAMP(23);
      gl(2, 0, 4);
      kv(IC<1>(), IC<4>());
      STAMPF(9);
      u32x4 k3[2];
      const unsigned k3o = WS_KV3 + (unsigned)(16 * pw + (l >> 2)) * 20480u + (unsigned)(((l >> 1) & 1) * 10240 + (2 * quad + (l & 1)) * 16);
      hold(K_T);
      k3[0] = ld16(rs, k3o); k3[1] = ld16(rs, k3o + 5120u);
      SCHED();
      ld_e();
      kv(IC<2>(), IC<6>());
      STAMPF(11);
      auto k_ok = [&]() { return __all(((k3[0][1] ^ tag) | (k3[0][3] ^ tag) | (k3[1][1] ^ tag) | (k3[1][3] ^ tag)) == 0u); };
      asm volatile("" : "+v"(k3[0]), "+v"(k3[1]));
      if (!k_ok()) {
        _Pragma("unroll 1") for (unsigned spins = 0; spins < SPIN_CAP; ++spins) {
          __builtin_amdgcn_s_sleep(4);
          k3[0] = ld16(rs, k3o); k3[1] = ld16(rs, k3o + 5120u);
          if (k_ok()) break;
        }
      }
      *(u32x4*)(lds + L_U + (2 * (l & 1) + ((l >> 1) & 1)) * U_CH + 512 + (1536 + 128 * pw + 8 * (l >> 2)) * 2) = (u32x4){k3[0][0], k3[0][2], k3[1][0], k3[1][2]};
      lf_set(12 + pw);
      __builtin_amdgcn_s_setprio(3);
      STAMPF(18);
      asm volatile("" : "+v"(sv[0]), "+v"(sv[1]), "+v"(sv[2]), "+v"(sv[3]));
      if (!s_ok()) {
        _Pragma("unroll 1") for (unsigned spins = 0; spins < SPIN_CAP; ++spins) {
          __builtin_amdgcn_s_sleep(4);
          ld_e();
          if (s_ok()) break;
        }
      }
      asm volatile("" ::: "memory");
      SCHED();
      STAMPF(17);
#pragma unroll
      for (int s = 0; s < 4; ++s) qv[s] = ld16p(rs, WS_QB + yo + (unsigned)s * (32u * 20480u));
      hold(M_T);
      mv[0] = ld8(rs, mo); mv[1] = ld8(rs, mo + 20480u);
      SCHED();
      STAMP(2);
      u32x2 yv[4][2];
      auto ld_y = [&](int s) {
#pragma unroll
        for (int e = 0; e < 2; ++e) yv[s][e] = trd(yr + 1024 * s + 32 * e);
      };
      lf_wait(20, 4);
      ld_y(0); ld_y(1);
      SCHED();
      asm volatile("" : "+v"(mv[0]), "+v"(mv[1]), "+v"(qv[0]), "+v"(qv[1]), "+v"(qv[2]), "+v"(qv[3]));
      if (!__all(((mv[0][0] ^ tag) | (mv[1][0] ^ tag)) == 0u)) {
        _Pragma("unroll 1") for (unsigned spins = 0; spins < SPIN_CAP; ++spins) {
          __builtin_amdgcn_s_sleep(4);
          mv[0] = ld8(rs, mo); mv[1] = ld8(rs, mo + 20480u);
          if (__all(((mv[0][0] ^ tag) | (mv[1][0] ^ tag)) == 0u)) break;
        }
      }
      STAMPF(13);
      f32x4 al, be;
      {
        const float mgf = rbf(sum_rows(__uint_as_float(mv[0][1]) + __uint_as_float(mv[1][1])));
        auto rl = [&](int i) { return __uint_as_float(__builtin_amdgcn_readlane(__float_as_uint(mgf), i)); };
        const f32x4 sa = {rl(0), rl(1), rl(2), rl(3)}, ca = {rl(4), rl(5), rl(6), rl(7)}, ga = {rl(8), rl(9), rl(10), rl(11)};
        al = rbf4(ca + 1.0f) * ga;
        be = sa * ga;
      }
      f32x4 qf[2][2], P[2][2];
      auto xpre = [&](int s) {
#pragma unroll
        for (int e = 0; e < 2; ++e) {
          const float r1 = __builtin_amdgcn_rcpf(bfhi(sv[s][2 * e])), m1 = -bflo(sv[s][2 * e]) * r1;
          const f32x4 qq = {bflo(qv[s][2 * e]), bfhi(qv[s][2 * e]), bflo(qv[s][2 * e + 1]), bfhi(qv[s][2 * e + 1])};
          qf[s & 1][e] = qq;
          P[s & 1][e] = __builtin_elementwise_fma(__builtin_elementwise_fma(qq, (f32x4){r1, r1, r1, r1}, (f32x4){m1, m1, m1, m1}), al, be);
        }
        asm volatile("" : "+v"(qf[s & 1][0]), "+v"(qf[s & 1][1]), "+v"(P[s & 1][0]), "+v"(P[s & 1][1]));
      };
      auto xfin = [&](int s) {
        u32x4 o;
#pragma unroll
        for (int e = 0; e < 2; ++e) {
          const u32x2 y = yv[s][e];
          const f32x4 yf = {bflo(y[0]), bfhi(y[0]), bflo(y[1]), bfhi(y[1])};
          const f32x4 x = __builtin_elementwise_fma(P[s & 1][e], yf, qf[s & 1][e]);
          o[2 * e] = pack2(x[0], x[1]);
          o[2 * e + 1] = pack2(x[2], x[3]);
        }
        st16(rs, WS_X1B + yo + (unsigned)s * (32u * 20480u), o);
      };
      xpre(0); xfin(0);
      xpre(1); xfin(1);
      SCHED();
      xpre(2); xpre(3);
      lf_wait(24, 4);
      __builtin_amdgcn_s_setprio(3);
      ld_y(2); ld_y(3);
      xfin(2); xfin(3);
      STAMP(28);
      asm volatile("s_waitcnt vmcnt(2)" ::: "memory");
      xflag(l >> 5);
      asm volatile("s_waitcnt vmcnt(1)" ::: "memory");
      xflag(2 + (l >> 5));
      STAMP(5);
    } else {
      const int ch = w;
      char* U = lds + L_U + ch * U_CH;
      char* G = lds + ch * (4 * G_COPY);
      {
        const rsrc_t rf = __builtin_amdgcn_make_buffer_rsrc((void*)(p.ssm_k + (size_t)(quad * 4 + ch) * S_), 0, S_ * 2, 0x00020000);
        u32x4 fa[4];
        u32x2 fb[4];
        __builtin_amdgcn_s_setprio(3);
#pragma unroll
        for (int k = 0; k < 4; ++k) {
          fa[k] = __builtin_amdgcn_raw_buffer_load_b128(rf, l16 + (unsigned)k * 1024u, 0, NT);
        }
        __builtin_amdgcn_s_setprio(1);
        TAG_READY();
#pragma unroll
        for (int k = 0; k < 4; ++k) {
#pragma unroll
          for (int d = 0; d < 2; ++d)
            fb[k][d] = __builtin_amdgcn_update_dpp(k < 3 ? (unsigned)__builtin_amdgcn_readfirstlane(fa[k < 3 ? k + 1 : k][d]) : 0u, fa[k][d], 0x130, 0xf, 0xf, false);
          const unsigned x[6] = {fa[k][0], fa[k][1], fa[k][2], fa[k][3], fb[k][0], fb[k][1]};
          unsigned y[5];
#pragma unroll
          for (int d = 0; d < 5; ++d) y[d] = __builtin_amdgcn_alignbit(x[d + 1], x[d], 16);
          char* gp = G + 32 + k * 1024 + l16;
          *(u32x4*)(gp) = fa[k];
          *(u32x4*)(gp + G_COPY) = (u32x4){y[0], y[1], y[2], y[3]};
          *(u32x4*)(gp + 2 * G_COPY) = (u32x4){x[1], x[2], x[3], x[4]};
          *(u32x4*)(gp + 3 * G_COPY) = (u32x4){y[1], y[2], y[3], y[4]};
          if (k == 0) {
            const unsigned f0 = __builtin_amdgcn_readfirstlane(x[0]), f1 = __builtin_amdgcn_readfirstlane(x[1]);
            const unsigned long long f64 = (unsigned long long)f0 | ((unsigned long long)f1 << 32);
            const unsigned long long hi = l ? (f64 << (64 - 16 * (l & 3))) : 0ull;
            if (l < 4) {
              *(u32x4*)(G + l * G_COPY) = (u32x4){0u, 0u, 0u, 0u};
              *(u32x4*)(G + l * G_COPY + 16) = (u32x4){0u, 0u, (unsigned)hi, (unsigned)(hi >> 32)};
            }
            if (l >= 32) *(u32x4*)(U + (l - 32) * 16) = (u32x4){0u, 0u, 0u, 0u};
          }
        }
        asm volatile("s_waitcnt lgkmcnt(0)" ::: "memory");
        STAMP(1);
      }
      const int a = n16, h = h4, jj = h >> 1, e2 = h & 1;
      const char* ap = G + ((a + 1) & 3) * G_COPY + 2 * (16 * jj + a - 8 * e2 + 9 - ((a + 1) & 3));
      const char* bp = U + 512 + 32 * (a - jj) + 16 * e2;
      auto lda = [&](u32x4* A, int r, int p0, int np) {
#pragma unroll
        for (int pp = 0; pp < np; ++pp) {
          u32x2 lo = *(const volatile LAS u32x2*)(ap + 512 * (p0 + pp) + 64 * r);
          u32x2 hi = *(const volatile LAS u32x2*)(ap + 512 * (p0 + pp) + 64 * r + 8);
          A[pp] = (u32x4){lo[0], lo[1], hi[0], hi[1]};
        }
      };
      u32x4 AD;
      {
        const unsigned dDr = (*(cptr_t)(p.ssm_D + quad * 4 + (ch & 2)) >> (16 * (ch & 1))) & 0xffffu;
        const unsigned dv = h == (a >> 3) ? ((a & 1) ? dDr : dDr << 16) : 0u;
        const int dw = (7 - (a & 7)) >> 1;
#pragma unroll
        for (int d = 0; d < 4; ++d) AD[d] = dw == d ? dv : 0u;
      }
      u32x4 A0[8][4];
      f32x4 acc[8];
#pragma unroll
      for (int n = 0; n < 8; ++n) acc[n] = (f32x4){0.f, 0.f, 0.f, 0.f};
#pragma unroll
      for (int r = 0; r < 8; ++r) lda(A0[r], r, 0, 4);
      auto yput = [&](int s) {
#pragma unroll
        for (int e = 0; e < 2; ++e)
          *(u32x2*)(U + 1024 * s + 512 * e + (16 * a + 4 * h) * 2) = (u32x2){pack2(acc[2 * s + e][0], acc[2 * s + e][1]), pack2(acc[2 * s + e][2], acc[2 * s + e][3])};
        lf_set(16 + 4 * s + ch);
      };
      auto stage = [&](auto S__) {
        constexpr int s = decltype(S__)::value, NH = s < 2 ? 4 - 2 * s : 0, NP = 8 - 2 * s, NB = s < 2 ? 2 : 8;
        u32x4 A1[3][NH > 0 ? NH : 1], B[NB][2];
        auto ldb = [&](int r) {
#pragma unroll
          for (int e = 0; e < 2; ++e) B[r % NB][e] = *(const u32x4*)(bp + 512 * (2 * s + e) - 64 * r);
        };
        if (NH) { lda(A1[0], 0, 4, NH); lda(A1[1], 1, 4, NH); }
        SCHED();
        if (s > 0) yput(s - 1);
        lf_wait(4 * s, 4);
        if (s == 0) STAMP(3);
#pragma unroll
        for (int r = 0; r < NB; ++r) ldb(r);
        SCHED();
#pragma unroll
        for (int e = 0; e < 2; ++e) acc[2 * s + e] = MFMA16(AD, B[0][e], acc[2 * s + e]);
#pragma unroll
        for (int r = 0; r < 8; ++r) {
          if (NH && r + 2 < 8) lda(A1[(r + 2) % 3], r + 2, 4, NH);
          SCHED();
#pragma unroll
          for (int e = 0; e < 2; ++e)
#pragma unroll
            for (int pp = 0; pp < NP - e; ++pp) acc[2 * s + e + pp] = MFMA16(pp < 4 ? A0[r][pp] : A1[r % 3][pp < 4 ? 0 : pp - 4], B[r % NB][e], acc[2 * s + e + pp]);
          SCHED();
          if (r + NB < 8) ldb(r + NB);
        }
      };
      stage(IC<0>());
      STAMP(4);
      stage(IC<1>());
      STAMPF(9);
      const unsigned fo[1] = {fwi + (unsigned)((8 + c) * 20 + 5 * w + (l < 5 ? l : 0)) * 128u};
      unsigned fv = ~tag;
      {
        u32x4 B[4][4];
        auto ldb = [&](int r) {
#pragma unroll
          for (int m = 0; m < 4; ++m) B[r & 3][m] = *(const u32x4*)(bp + 512 * (4 + m) - 64 * r);
        };
        yput(1);
        if (w == 3) wo_ld();
        __builtin_amdgcn_s_setprio(3);
        lf_wait(8, 8);
        STAMP(22);
#pragma unroll
        for (int r = 0; r < 4; ++r) ldb(r);
        SCHED();
#pragma unroll
        for (int m = 0; m < 4; ++m) acc[4 + m] = MFMA16(AD, B[0][m], acc[4 + m]);
#pragma unroll
        for (int r = 0; r < 8; ++r) {
#pragma unroll
          for (int m = 0; m < 4; ++m)
#pragma unroll
            for (int pp = 0; pp < 4 - m; ++pp) acc[4 + m + pp] = MFMA16(A0[r][pp], B[r & 3][m], acc[4 + m + pp]);
          SCHED();
          if (r + 4 < 8) ldb(r + 4);
          if (r == 1) fv = ld4(rs, fo[0]);
          SCHED();
        }
#pragma unroll
        for (int e = 0; e < 4; ++e)
          *(u32x2*)(U + 2048 + 512 * e + (16 * a + 4 * h) * 2) = (u32x2){pack2(acc[4 + e][0], acc[4 + e][1]), pack2(acc[4 + e][2], acc[4 + e][3])};
        lf_set(24 + ch);
      }
      STAMP(28);
      __builtin_amdgcn_s_setprio(0);
      if (!__all(fv == tag)) wait_lines<1, 4>(rs, fo, tag);
      lf_wait(20, 4);
    }
    __builtin_amdgcn_s_setprio(0);
  } else {
    const int at = idx - 20;
    char* const scr = lds + WBLK + 14848;
    auto gv_blk = [&](int j) { return j < 160 ? 30 + (j >> 3) : j < 400 ? (j >> 3) - 20 : j >> 3; };
    auto gv_fin = [&](const u32x4* pre, int j) {
      float acc[8];
      gemv_fin(acc, pre, 10, scr);
      gemv_put(acc, j < 160 ? WS_MODP + (unsigned)((j & 7) * 1280 + (j >> 3) * 64) * 8u : WS_MODQ + (unsigned)((j & 7) * 2560 + ((j >> 3) - 20) * 64) * 8u);
    };
    const int ja = xcd * 12 + at, ai = xcd * 8 + at, bi = xcd * 4 + at - 8, g = w >> 2, wj = w & 3;
    const int rj = w == 0 ? ja : w == 1 ? (at >= 4 ? 92 + ai : -1) : w == 4 ? 160 + ja : w == 5 && at < 8 ? 256 + ai : -1;
    char* const rst = lds + (g ? WBLK : 65536) + wj * 7424;
    u32x4 pre[11];
    if (at >= 8 || g == 0) {
      TOUCH_AT(2);
      const int T = at >= 8 ? 64 + 2 * bi + g : ai;
      unsigned* mds = (unsigned*)(lds + 2048) + 16 * g;
      char* qt = lds + L_RT + g * (16 * 1312);
      u32x4 ld[5];
      const bool e1 = at >= 8 && w == 3;
      if (e1) gemv_load(pre, gv_blk(128 + bi), bi & 7, 10);
      else __builtin_amdgcn_s_sleep(8);
#pragma unroll
      for (int j = 0; j < 5; ++j)
        ld[j] = __builtin_amdgcn_raw_buffer_load_b128(rq, (unsigned)(h4 * D_ * 2 + n16 * 16 + j * 256), (unsigned)((32 * (T >> 1) + 8 * wj + 4 * (T & 1)) * D_ * 2), NT);
      TAG_READY();
      {
        const bf2_t ONE2 = {(__bf16)1.0f, (__bf16)1.0f};
        char* dst = qt + (4 * wj + h4) * 1312 + n16 * 16;
        float sa = 0.f, va = 0.f;
#pragma unroll
        for (int j = 0; j < 5; ++j) {
          *(u32x4*)(dst + j * 256) = ld[j];
#pragma unroll
          for (int d = 0; d < 4; ++d) {
            const unsigned a0 = ld[j][d];
            const bf2_t xa = __builtin_bit_cast(bf2_t, a0);
            sa = __builtin_amdgcn_fdot2_f32_bf16(xa, ONE2, sa, false); va = __builtin_amdgcn_fdot2_f32_bf16(xa, xa, va, false);
          }
        }
        lf_set(8 + w);
        const float ma = sum16(sa) * (1.0f / D_);
        va = __builtin_fmaxf(sum16(va) * (1.0f / D_) - ma * ma, 0.f);
        if (n16 == 0) mds[4 * wj + h4] = pack2(ma, __builtin_amdgcn_sqrtf(rbf(rbf(va) + 1e-5f)));
      }
      if (e1) gv_fin(pre, 128 + bi);
      lf_wait(8 + 4 * g, 4);
      STAMP(23);
      {
        const char* src = qt + (l & 7) * 1312 + (40 * wj + (l >> 3)) * 8;
#pragma unroll
        for (int j = 0; j < 5; ++j) {
          const u32x2 x = *(const u32x2*)(src + j * 64), y = *(const u32x2*)(src + j * 64 + 8 * 1312);
          st16(rs, WS_QB + (unsigned)T * 20480u + (unsigned)(5 * wj + j) * 1024u + l16, (u32x4){x[0], x[1], y[0], y[1]});
        }
      }
      SCHED();
      const int g2 = at < 8 || w < 3 || w == 4 ? 400 : w == 3 ? 384 + bi : 256 + wj * 32 + bi;
      if (g2 < 400) {
        gemv_load(pre, gv_blk(g2), g2 & 7, 10);
        asm volatile("s_waitcnt vmcnt(10)" ::: "memory");
      } else if (w < 2 && rj >= 0) {
        rt_ld(rj, rst);
        asm volatile("s_waitcnt vmcnt(7)" ::: "memory");
      } else {
        DRAIN();
      }
      lf_set(16 + w);
      if (wj == 2) {
        lf_wait(16 + 4 * g, 4);
        st16(rs, WS_ST1 + (unsigned)(l >> 3) * 16384u + (unsigned)T * 128u + (unsigned)(l & 7) * 16u, (u32x4){mds[l & 7], tag, mds[8 + (l & 7)], tag});
        STAMP(22);
      }
      if (w >= 4 && w < 7) lf_set(w);
      if (g2 < 400) gv_fin(pre, g2);
      STAMP(25);
    } else {
      constexpr int KB = L_RT + 16 * 1312;
      TOUCH_AT(3);
      const int g1 = w < 6 ? 2 * ai + wj : 32 + wj * 64 + ai;
      if (w >= 6) {
        const rsrc_t rk = __builtin_amdgcn_make_buffer_rsrc((void*)(w == 6 ? p.k : p.v), 0, S_ * D_ * 2, 0x00020000);
        const unsigned ko = (unsigned)((1536 + 8 * ai) * (D_ * 2) - (w == 6 ? 4096 : 0));
        char* dst = lds + KB + (w == 6 ? 0 : 14336);
        __builtin_amdgcn_s_sleep(3);
#pragma unroll
        for (int n = 0; n < 10; ++n) __builtin_amdgcn_raw_ptr_buffer_load_lds(rk, (LAS void*)(dst + n * 1024), 16, l16, ko + (unsigned)n * 1024u, 0, NT);
        if (w == 6) {
#pragma unroll
          for (int n = 10; n < 14; ++n) __builtin_amdgcn_raw_ptr_buffer_load_lds(rk, (LAS void*)(dst + n * 1024), 16, l16, ko + (unsigned)n * 1024u, 0, NT);
        }
      }
      SCHED();
      u32x4 swt[2];
      swt[0] = *(const u32x4*)(p.shift_k + (tid - 256) * 8);
      swt[1] = *(const u32x4*)(p.shift_k + (256 + l) * 8);
      SCHED();
      if (w >= 6) __builtin_amdgcn_s_sleep(8);
      gemv_load(pre, gv_blk(g1), g1 & 7, 10);
      TAG_READY();
      asm volatile("s_waitcnt vmcnt(11)" : "+v"(swt[0]), "+v"(swt[1]) :: "memory");
      lf_set(8 + w);
      if (w < 6) gv_fin(pre, g1);
      STAMP(25);
      lf_wait(12, 4);
      STAMP(23);
#pragma unroll 1
      for (int n = 0; n < (w == 7 ? 2 : 1); ++n) {
        {
          const int pr = n ? 256 + l : tid - 256;
          const char* kp = lds + KB + 4096 + pr * 4;
          unsigned kk[11], vv[8];
#pragma unroll
          for (int i = 0; i < 11; ++i) kk[i] = *(const unsigned*)(kp + (i - 3) * 1280);
#pragma unroll
          for (int i = 0; i < 8; ++i) vv[i] = *(const unsigned*)(kp + 10240 + i * 1280);
          const u32x4 sw = n ? swt[1] : swt[0];
          const f32x2 wt[4] = {{bflo(sw[0]), bflo(sw[2])}, {bfhi(sw[0]), bfhi(sw[2])}, {bflo(sw[1]), bflo(sw[3])}, {bfhi(sw[1]), bfhi(sw[3])}};
          f32x2 o[8];
#pragma unroll
          for (int i = 0; i < 8; ++i) {
            f32x2 t = (f32x2){bflo(kk[i + 3]), bfhi(kk[i + 3])} * wt[0];
#pragma unroll
            for (int j = 1; j < 4; ++j) t = __builtin_elementwise_fma((f32x2){bflo(kk[i + 3 - j]), bfhi(kk[i + 3 - j])}, wt[j], t);
            o[i] = t * (f32x2){bflo(vv[i]), bfhi(vv[i])};
          }
#pragma unroll
          for (int e = 0; e < 4; ++e) {
            const int i = 7 - 4 * (e & 1);
            st16(rs, WS_KV3 + (unsigned)ai * 20480u + (unsigned)(e * 5120 + pr * 16),
                 (u32x4){pack2(o[i][e >> 1], o[i - 1][e >> 1]), tag, pack2(o[i - 2][e >> 1], o[i - 3][e >> 1]), tag});
          }
        }
      }
      STAMP(22);
      if (w < 7) lf_set(w);
      if (w >= 6) gv_fin(pre, g1);
      STAMPF(30);
    }
    if (w == 6 && ja < 80) {
      hold(AUX_T);
      gemv_load(pre, gv_blk(400 + ja), ja & 7, 10);
      gv_fin(pre, 400 + ja);
    }
    if (rj >= 0) {
      if (w >= 2) { hold(RT2_T); rt_ld(rj, rst); }
      rt_job(rj, rst);
    }
    STAMP(26);
    lf_set(w);
    if (w == 3) { hold(WA_T); wo_ld(); }
    STAMP(3);
    if (w < 4) {
      {
        const unsigned fo[1] = {l < 5 ? fwi + (unsigned)((8 + c) * 20 + 5 * w + l) * 128u : ~0u};
        hold(W_T);
        wait_lines<1, 8>(rs, fo, tag);
      }
      lf_wait(0, 8);
      STAMP(4);
    }
    if ((at == 8 || at == 9) && w == 5 && xcd * 2 + at - 8 < 10) {
      const unsigned o = WS_MODQ + (unsigned)(h4 * 5120 + 1920 + (xcd * 2 + at - 8) * 64 + n16 * 4) * 8u;
      u32x4 v[4];
      hold(CS_T);
      _Pragma("unroll 1") for (unsigned spins = 0; spins < SPIN_CAP; ++spins) {
        unsigned dd = 0u;
#pragma unroll
        for (int k = 0; k < 4; ++k) v[k] = ld16(rs, o + (unsigned)((k >> 1) * 20480 + (k & 1) * 16));
#pragma unroll
        for (int k = 0; k < 4; ++k) dd |= (v[k][0] ^ tag) | (v[k][2] ^ tag);
        if (__all(dd == 0u)) break;
        __builtin_amdgcn_s_sleep(16);
      }
      float t[4];
#pragma unroll
      for (int k = 0; k < 4; ++k) t[k] = sum_rows(__uint_as_float(v[k >> 1][1 + 2 * (k & 1)]) + __uint_as_float(v[2 + (k >> 1)][1 + 2 * (k & 1)]));
#pragma unroll
      for (int e = 0; e < 2; ++e)
        st16(rs, WS_MOD + (unsigned)(4 * e + h4) * 2560u + (unsigned)((xcd * 2 + at - 8) * 16 + n16) * 16u, (u32x4){tag, pack2(t[0], t[1]), tag, pack2(t[2], t[3])});
    }
    if (w == 4 || (w == 5 && at >= 10)) {
      const int cj = w == 4 ? ja : 86 + xcd * 2 + at, cb = cj / 7, j = cj % 7;
      const unsigned o = WS_CSP + (unsigned)((cb * 20 + 5 * h4) * 2 + (j >> 2)) * 1024u + (unsigned)((j & 3) * 16 + n16) * 16u;
      u32x4 v[5];
      hold(CS_T);
      __builtin_amdgcn_s_setprio(3);
      _Pragma("unroll 1") for (unsigned spins = 0; spins < SPIN_CAP; ++spins) {
        unsigned dd = 0u;
#pragma unroll
        for (int k = 0; k < 5; ++k) v[k] = ld16(rs, o + (unsigned)k * 2048u);
#pragma unroll
        for (int k = 0; k < 5; ++k) dd |= (v[k][0] ^ tag) | (v[k][2] ^ tag);
        if (__all(dd == 0u)) break;
        __builtin_amdgcn_s_sleep(8);
      }
      float t0 = 0.f, t1 = 0.f;
#pragma unroll
      for (int k = 0; k < 5; ++k) { t0 += __uint_as_float(v[k][1]); t1 += __uint_as_float(v[k][3]); }
      t0 = sum_rows(t0); t1 = sum_rows(t1);
      if (l < 16) st16(rs, WS_CS + (unsigned)(cb * 2048 + j * 256) + l16, (u32x4){tag, __float_as_uint(t0), tag, __float_as_uint(t1)});
      __builtin_amdgcn_s_setprio(0);
      STAMP(24);
    }
  }

  STAMPF(16);
  u32x4 wox[5];
  DRAIN();
  if (w == 3) {
    const char* bp = wo_st + (4 * h4 + (n16 >> 2)) * 160 + (n16 & 3) * 8;
#pragma unroll
    for (int dj = 0; dj < 5; ++dj) wox[dj] = trfrag(bp + dj * 32, 160);
    asm volatile("s_waitcnt lgkmcnt(0)" : "+v"(wox[0]), "+v"(wox[1]), "+v"(wox[2]), "+v"(wox[3]), "+v"(wox[4]) :: "memory");
  }
  auto wo_put = [&]() {
    if (w == 3 && R < 28) {
      const unsigned base = WS_WOUTF + (unsigned)c * WBLK + (unsigned)((R >> 1) * 10240 + (R & 1) * 1024);
#pragma unroll
      for (int dj = 0; dj < 5; ++dj) st16(rs, base + (unsigned)dj * 2048u + l16, wox[dj]);
    }
  };
  if (w < 4) load_b2();
  STAMPF(29);
  u32x4 k_c;
  unsigned kc_o;
  {
    const int i = tid < 224 ? tid : 0;
    const int col = i >= 112 ? (8 + c) * 128 + i - 112 : c * 128 + i;
    kc_o = w < 4 ? WS_CS + (unsigned)col * 16u : WS_MOD + (unsigned)xcd * 2560u + (unsigned)(20 * c + (tid < 276 ? tid - 256 : 0)) * 16u;
  }
  auto consts_put = [&]() {
    if (!__all(k_c[0] == tag && k_c[2] == tag)) {
      _Pragma("unroll 1") for (unsigned spins = 0; spins < SPIN_CAP; ++spins) {
        __builtin_amdgcn_s_sleep(8);
        k_c = ld16(rs, kc_o);
        if (__all(k_c[0] == tag && k_c[2] == tag)) break;
      }
    }
    if (tid < 224) *(u32x2*)(lds + L_CONST + tid * 8) = (u32x2){k_c[1], k_c[3]};
    if (tid >= 256 && tid < 276) *(u32x2*)(lds + L_MISC + (tid - 256) * 8) = (u32x2){k_c[1], k_c[3]};
  };
  const int rt1 = w & 3, kh1 = w >> 2;
  unsigned bo0 = (unsigned)(kh1 * 71680) + l16, bo1 = bo0 + 35840u;
  asm volatile("" : "+v"(bo0), "+v"(bo1));
#define BFRAG(q, j) (*(const u32x4*)(lds + ((q) < 5 ? bo0 : bo1) + ((q) % 5) * 7168 + (j) * 1024))
  u32x4 h2[10];
  f32x4 acc[7];
  float mu_n, rs_n;
  {
    if (w == 6) {
      const unsigned fo[1] = {l < 20 ? fwi + (unsigned)(c * 20 + l) * 128u : ~0u};
      hold(CS_T);
      wait_lines<1, 8>(rs, fo, tag);
    }
    if (w == 7) {
      const unsigned fb = WS_FX + (unsigned)(((R >> 1) * 4 + (R & 1) * 2 + (c >> 2)) * 160 + l) * 128u;
      const unsigned fo[3] = {fb, fb + 8192u, l < 32 ? fb + 16384u : ~0u};
      if (!is_conv) hold(Y_T);
      wait_lines<3, 2>(rs, fo, tag);
      if (l == 0) lf[33] = tag;
    }
    lf_wait(33, 1);
    STAMP(7);
    const unsigned xo = (unsigned)(R * 4 + rt1) * 20480u + (unsigned)(10 * kh1) * 1024u +
                        (unsigned)((4 * ((l >> 3) & 1) + h4) * 128 + (l & 7) * 16);
    SCHED();
#pragma unroll
    for (int q = 0; q < 3; ++q) h2[q] = ld16p(rs, WS_X1B + xo + (unsigned)q * 1024u);
    SCHED();
    __builtin_amdgcn_s_waitcnt(0x0F73);
    BAR();
    STAMP(8);
#pragma unroll
    for (int j = 0; j < 7; ++j) acc[j] = (f32x4){0.f, 0.f, 0.f, 0.f};
    f32x4 accg = {0.f, 0.f, 0.f, 0.f};
    u32x4 bf[7];
#pragma unroll
    for (int j = 0; j < 7; ++j) bf[j] = BFRAG(0, j);
    SCHED();
#pragma unroll
    for (int q = 3; q < 6; ++q) h2[q] = ld16p(rs, WS_X1B + xo + (unsigned)q * 1024u);
    SCHED();
#pragma unroll
    for (int q = 0; q < 10; ++q) {
      {
#pragma unroll
        for (int e = 0; e < 6; ++e) {
          const int d = e & 1;
          if ((e >> 1) == 1) h2[q][2 + d] ^= (unsigned)__builtin_amdgcn_update_dpp(0, (int)h2[q][d], 0x128, 0xf, 0x3, false);
          else h2[q][d] ^= (unsigned)__builtin_amdgcn_update_dpp(0, (int)h2[q][2 + d], 0x128, 0xf, 0xC, false);
        }
      }
      if (q == 5) { __builtin_amdgcn_s_barrier(); asm volatile("" ::: "memory"); }
      SCHED();
      accg = MFMA16(h2[q], h2[q], accg);
      SCHED();
#pragma unroll
      for (int j = 0; j < 7; ++j) {
        acc[j] = MFMA16(h2[q], bf[j], acc[j]);
        if (q == 9 && j == 1 && h4 == (n16 >> 2)) *(f32x4*)(lds + L_PST + 512 + (w * 16 + n16) * 16) = accg;
        if (q < 9) bf[j] = BFRAG(q + 1, j);
        if (q >= 2 && j == 2) LDW(q - 2);
        if ((j == 0 || j == 4) && 6 + 2 * q + (j >> 2) < 10) h2[6 + 2 * q + (j >> 2)] = ld16p(rs, WS_X1B + xo + (unsigned)(6 + 2 * q + (j >> 2)) * 1024u);
        if (q == 4 && j == 0) k_c = ld16(rs, kc_o);
        if (q >= 5 && j == 5) {
          const int i = (5 + q) * 7 + (w < 7 ? w : 0);
          dma1k(rs, lds + i * 1024, l16, WS_WINF + (unsigned)c * WBLK + (unsigned)i * 1024u);
          if (q == 9) { const int i2 = 28 + (w < 7 ? w : 0); dma1k(rs, lds + i2 * 1024, l16, WS_WINF + (unsigned)c * WBLK + (unsigned)i2 * 1024u); }
        }
        SCHED();
      }
    }
    STAMP(31);
  }
  auto gelu_n = [&](f32x2* x, auto N_) {
    constexpr int N = decltype(N_)::value;
    const float c0 = -2.0f * 0.7978845608028654f * 1.4426950408889634f, c1 = c0 * 0.044715f;
    f32x2 k0 = {c0, c0}, k1 = {c1, c1}, k2 = {1.0f, 1.0f};
    asm volatile("" : "+v"(k0), "+v"(k1), "+v"(k2));
    f32x2 t[N];
#define GST(e) do { _Pragma("unroll") for (int i = 0; i < N; ++i) { t[i] = (e); asm volatile("" : "+v"(t[i])); } } while (0)
    GST(x[i] * x[i]);
    GST(__builtin_elementwise_fma(t[i], k1, k0));
    GST(t[i] * x[i]);
    GST(((f32x2){__builtin_amdgcn_exp2f(t[i][0]), __builtin_amdgcn_exp2f(t[i][1])}));
    GST(t[i] + k2);
    GST(((f32x2){__builtin_amdgcn_rcpf(t[i][0]), __builtin_amdgcn_rcpf(t[i][1])}));
#pragma unroll
    for (int i = 0; i < N; ++i) { x[i] *= t[i]; asm volatile("" : "+v"(x[i])); }
  };
  {
    f32x2* red = (f32x2*)lds;
    f32x2 mine[7];
    if (kh1) {
#pragma unroll
      for (int j = 0; j < 7; ++j) { red[(w * 7 + j) * 64 + l] = (f32x2){acc[j][0], acc[j][1]}; mine[j] = (f32x2){acc[j][2], acc[j][3]}; }
    } else {
#pragma unroll
      for (int j = 0; j < 7; ++j) { red[(w * 7 + j) * 64 + l] = (f32x2){acc[j][2], acc[j][3]}; mine[j] = (f32x2){acc[j][0], acc[j][1]}; }
    }
    f32x2 csb[7];
    unsigned cso = (unsigned)(L_CONST + (112 + n16) * 8);
    asm volatile("" : "+v"(cso));
    consts_put();
    unsigned pso[3], pqo[3], oto = (unsigned)(((w ^ 4) * 7 * 64 + l) * 8);
#pragma unroll
    for (int e = 0; e < 3; ++e) {
      const int row = rt1 * 16 + (e < 2 ? 4 * h4 + 2 * kh1 + e : n16);
      pso[e] = (unsigned)(L_PST + row * 4); pqo[e] = (unsigned)(L_PST + 512 + row * 16 + (row & 3) * 4);
    }
    asm volatile("" : "+v"(pso[0]), "+v"(pso[2]), "+v"(pqo[0]), "+v"(pqo[1]), "+v"(pqo[2]), "+v"(oto));
    pso[1] = pso[0] + 4u;
    if (n16 == 12) *(f32x4*)(lds + L_PST + (w * 16 + 4 * h4) * 4) = acc[6];
    if (!kh1) {
#pragma unroll
      for (int k = 8; k < 16; ++k) LDW(k);
    }
    BAR();
    if (kh1) {
#pragma unroll
      for (int k = 8; k < 16; ++k) LDW(k);
    }
    __builtin_amdgcn_s_setprio(3);
    STAMPT(9);
    f32x2 oth[7];
    f32x2 pa[3], pb[3];
#pragma unroll
    for (int e = 0; e < 3; ++e) {
      pa[e] = (f32x2){*(const float*)(lds + pso[e]), *(const float*)(lds + pqo[e])};
      pb[e] = (f32x2){*(const float*)(lds + pso[e] + 256), *(const float*)(lds + pqo[e] + 1024)};
    }
#pragma unroll
    for (int j = 0; j < 7; ++j) { oth[j] = *(const f32x2*)(lds + oto + j * 512); csb[j] = *(const f32x2*)(lds + cso + 128 * j); }
    SCHED();
    float mu2[2], rs2[2];
#pragma unroll
    for (int e = 0; e < 3; ++e) {
      const float mean = (pa[e][0] + pb[e][0]) * (1.0f / D_);
      const float var = __builtin_fmaxf((pa[e][1] + pb[e][1]) * (1.0f / D_) - mean * mean, 0.f);
      const float rstd = __builtin_amdgcn_rsqf(var + 1e-5f);
      if (e < 2) { mu2[e] = mean; rs2[e] = rstd; } else { mu_n = mean; rs_n = rstd; }
    }
    f32x2 zz[7];
    const f32x2 nmu = {-mu2[0], -mu2[1]}, rsv = {rs2[0], rs2[1]};
#pragma unroll
    for (int j = 0; j < 7; ++j)
      zz[j] = __builtin_elementwise_fma(__builtin_elementwise_fma(nmu, (f32x2){csb[j][0], csb[j][0]}, mine[j] + oth[j]), rsv, (f32x2){csb[j][1], csb[j][1]});
    gelu_n(zz, IC<7>());
    if (n16 >= 12) zz[6] = (f32x2){0.f, 0.f};
    f32x2 s1 = ((zz[0] + zz[1]) + (zz[2] + zz[3])) + ((zz[4] + zz[5]) + zz[6]);
    f32x2 s2 = zz[0] * zz[0], s3 = zz[1] * zz[1];
#pragma unroll
    for (int j = 2; j < 7; ++j) { if (j & 1) s3 = __builtin_elementwise_fma(zz[j], zz[j], s3); else s2 = __builtin_elementwise_fma(zz[j], zz[j], s2); }
    s2 += s3;
    asm("" : "+v"(s1), "+v"(s2));
    float a0 = s1[0] + dpp_f<0xB1>(s1[0]), a1 = s1[1] + dpp_f<0xB1>(s1[1]), b0 = s2[0] + dpp_f<0xB1>(s2[0]);
    a0 = (l & 1) ? a1 : a0;
    const float b1 = s2[1] + dpp_f<0xB1>(s2[1]);
    b0 = (l & 1) ? b1 : b0;
    a0 += dpp_f<0x4E>(a0); b0 += dpp_f<0x4E>(b0);
    a0 += dpp_f<0x124>(a0); b0 += dpp_f<0x124>(b0);
    a0 += dpp_f<0x128>(a0); b0 += dpp_f<0x128>(b0);
    {
      const int src = ((l >> 1) & 3) * 64 + (l & 1) * 4;
      const unsigned pa0 = __builtin_amdgcn_ds_bpermute(src, __float_as_uint(a0)), pb0 = __builtin_amdgcn_ds_bpermute(src, __float_as_uint(b0));
      if (l < 8) st16(rs, WS_PART + (unsigned)((R * 8 + c) * 8 + w) * 128u + l16, (u32x4){tag, pa0, tag, pb0});
    }
    SCHED();
    __builtin_amdgcn_s_setprio(0);
    STAMP(10);
#pragma unroll
    for (int k = 0; k < 10; ++k) {
      if (k < 9) {
        int x = k * 8 + w;
        x = x < 70 ? x : 69;
        const int i = x + (x < 35 ? 35 : 70);
        dma1k(rs, lds + i * 1024, l16, WS_WINF + (unsigned)c * WBLK + (unsigned)i * 1024u);
      }
    }
    wo_put();
#pragma unroll
    for (int j = 0; j < 7; ++j)
      *(unsigned*)(lds + L_Z + (((rt1 >> 1) * 7 + j) * 64 + l) * 16 + (4 * (rt1 & 1) + 2 * kh1) * 2) = pack2(zz[j][0], zz[j][1]);
    SCHED();
    u32x4 v[8];
    const unsigned po = WS_PART + (unsigned)(R * 8) * 1024u + l16;
#pragma unroll
    for (int g = 0; g < 4; ++g) WSP_TR(g);
    if (w == 2) {
      __builtin_amdgcn_s_setprio(3);
      unsigned need = 0xffu;
      if (need != 0u) {
        _Pragma("unroll 1") for (unsigned spins = 0; spins < SPIN_CAP; ++spins) {
#pragma unroll
          for (int k = 0; k < 8; ++k)
            if (need & (1u << k)) v[k] = ld16(rs, po + (unsigned)k * 1024u);
#pragma unroll
          for (int k = 0; k < 8; ++k)
            if (__all(v[k][0] == tag && v[k][2] == tag)) need &= ~(1u << k);
          if (need == 0u) break;
          __builtin_amdgcn_s_sleep(1);
        }
      }
      float t1 = 0.f, t2 = 0.f;
#pragma unroll
      for (int k = 0; k < 8; ++k) { t1 += __uint_as_float(v[k][1]); t2 += __uint_as_float(v[k][3]); }
      const float mean = t1 * (1.0f / FH);
      const float var = __builtin_fmaxf(t2 * (1.0f / FH) - mean * mean, 0.f);
      const float rstd = __builtin_amdgcn_rsqf(var + 1e-5f);
      const int sg = 16 * ((l >> 3) & 3) + 4 * ((l >> 1) & 3) + 2 * (l >> 5) + (l & 1);
      *(f32x2*)(lds + L_ST2 + sg * 8) = (f32x2){rstd, -mean * rstd};
    }
    STAMPT(11);
    DRAIN();
    if (w == 3) st_flag4(rs, WS_FWO + (unsigned)(c * 32 + R) * 128u, tag, l);
    BAR();
    __builtin_amdgcn_s_setprio(3);
    STAMP(12);
    u32x4 zf[2];
#pragma unroll
    for (int m = 0; m < 2; ++m) zf[m] = *(const u32x4*)(lds + L_Z + ((m * 7 + (w < 7 ? w : 0)) * 64 + l) * 16);
    if (w < 7) {
#pragma unroll
      for (int m = 0; m < 2; ++m) {
        const f32x4* sp = (const f32x4*)(lds + L_ST2 + (32 * m + 4 * h4) * 8);
        u32x4 o;
#pragma unroll
        for (int e = 0; e < 4; ++e) {
          f32x4 st = sp[(e & 1) + 8 * (e >> 1)];
          o[e] = pack2(__builtin_fmaf(bflo(zf[m][e]), st[0], st[1]), __builtin_fmaf(bfhi(zf[m][e]), st[2], st[3]));
        }
        st16(rs, WS_Z2F + (unsigned)((c * 64 + 4 * (R >> 1) + h4) * 7 + w) * 1024u + (unsigned)((2 * (R & 1) + m) * 256 + n16 * 16), o);
      }
    }
    SCHED();
    __builtin_amdgcn_s_setprio(0);
#pragma unroll
    for (int k = 0; k < 4; ++k)
      if (w < 7) dma1k(rs, lds + (k * 7 + w) * 1024, l16, WS_WINF + (unsigned)c * WBLK + (unsigned)(k * 7 + w) * 1024u);
    SCHED();
    STAMPT(13);
  }
  unsigned bs_raw;
  unsigned z1d[7];
  constexpr int L_RED = 28672;
#define WOA(i) (((i) < 28 ? (i) : (i) < 108 ? (i) + 32 : (i) < 120 ? (i) + 37 : (i) - 92) * 1024)
#define RBAR() do { __builtin_amdgcn_s_barrier(); asm volatile("" ::: "memory"); } while (0)
#define ZAUX 16
#define NW 14
  auto wo_dma = [&](int k) {
    int i = k < 8 ? (k + (idx >> 2)) & 7 : k + (idx >> 2);
    i = (i >= 15 ? i - 7 : i) * 8 + w;
    const int a = i + ((27 - i) >> 31 & 32) + ((107 - i) >> 31 & 5);
    dma1k(rs, lds + a * 1024, l16, WS_WOUTF + (unsigned)c * WBLK + (unsigned)i * 1024u);
  };
  unsigned zs[8];
#pragma unroll
  for (int t = 0; t < 8; ++t) zs[t] = WS_Z2F + (unsigned)(c * 64 + 8 * w + 4 * ((t >> 2) ^ rotg) + ((t + rotb) & 3)) * 7168u;
  u32x4 zw[NW];
#define LDZ(f) do { if (((f) >> 3) == 4) { zs[(f) & 7] += 4096u; asm volatile("" : "+s"(zs[(f) & 7])); } \
    zw[(f) % NW] = __builtin_amdgcn_raw_buffer_load_b128(rs, l16 + (unsigned)(((f) >> 3) & 3) * 1024u, zs[(f) & 7], ZAUX); } while (0)
  {
    f32x4 acc[7];
#pragma unroll
    for (int j = 0; j < 7; ++j) acc[j] = (f32x4){0.f, 0.f, 0.f, 0.f};
    u32x4 bf[7];
#pragma unroll
    for (int j = 0; j < 7; ++j) bf[j] = BFRAG(4, j);
#pragma unroll
    for (int qi = 0; qi < 10; ++qi) {
      const int q = (qi + 4) % 10, qn = (qi + 5) % 10;
      if (qi == 5) {
        asm volatile("s_waitcnt vmcnt(8)" ::: "memory");
        __builtin_amdgcn_s_barrier();
        asm volatile("" ::: "memory");
        if (w == 7) st_flag4(rs, WS_FZ + (unsigned)(c * 32 + R) * 128u, tag, l);
        STAMP(6);
#pragma unroll
        for (int k = 24; k < 32; ++k) LDW(k);
        bs_raw = p.b_sp[R * 64 + tau(rt1, n16)];
      }
      SCHED();
#pragma unroll
      for (int j = 0; j < 7; ++j) {
        acc[j] = MFMA16(bf[j], h2[q], acc[j]);
        if (qi < 9) bf[j] = BFRAG(qn, j);
        if (qi >= 1 && qi < 5 && (j == 1 || j == 4)) LDW(16 + 2 * (qi - 1) + (j >> 2));
        SCHED();
      }
    }
    STAMPT(29);
    f32x2* red = (f32x2*)(lds + L_RED);
    f32x2 mine[7];
    if (kh1) {
#pragma unroll
      for (int j = 0; j < 7; ++j) { red[(w * 7 + j) * 64 + l] = (f32x2){acc[j][0], acc[j][1]}; mine[j] = (f32x2){acc[j][2], acc[j][3]}; }
    } else {
#pragma unroll
      for (int j = 0; j < 7; ++j) { red[(w * 7 + j) * 64 + l] = (f32x2){acc[j][2], acc[j][3]}; mine[j] = (f32x2){acc[j][0], acc[j][1]}; }
    }
#define WOE 15
#define TRA 6
#define POLLP 1
#pragma unroll
    for (int g = 4; g < TRA; ++g) WSP_TR(g);
    if (WOE > 0 && w == 3) {
      const unsigned fo[1] = {l < 28 ? fwi + (WS_FWO - WS_FWI) + (unsigned)(c * 32 + l) * 128u : ~0u};
      wait_lines<1, 1>(rs, fo, tag);
    }
    BAR();
    STAMP(14);
    f32x2 oth[7];
    f32x4 cc[7];
#pragma unroll
    for (int j = 0; j < 7; ++j) {
      oth[j] = red[((w ^ 4) * 7 + j) * 64 + l];
      cc[j] = *(const f32x4*)(lds + L_CONST + (16 * j + 4 * h4 + 2 * kh1) * 8);
    }
    SCHED();
#pragma unroll
    for (int k = 0; k < WOE; ++k) wo_dma(k);
    SCHED();
    f32x2 z[7];
#pragma unroll
    for (int j = 0; j < 7; ++j) {
      z[j] = __builtin_elementwise_fma(__builtin_elementwise_fma((f32x2){-mu_n, -mu_n}, (f32x2){cc[j][0], cc[j][2]}, oth[j] + mine[j]),
                                       (f32x2){rs_n, rs_n}, (f32x2){cc[j][1], cc[j][3]});
      asm volatile("" : "+v"(z[j]));
    }
    SCHED();
    auto z_start = [&]() {
      const unsigned fo[1] = {l < 4 ? fz + (unsigned)(c * 32 + 4 * w + l) * 128u : ~0u};
      wait_lines<1, 1>(rs, fo, tag);
      STAMPT(23);
#pragma unroll
      for (int f = 0; f < NW; ++f) { LDZ(f); SCHED(); }
    };
    auto z1_fin = [&]() {
      gelu_n(z, IC<7>());
#pragma unroll
      for (int j = 0; j < 7; ++j) z1d[j] = pack2(z[j][0], z[j][1]);
      if (h4 == 3) z1d[6] = 0u;
      asm volatile("" : "+v"(z1d[0]), "+v"(z1d[1]), "+v"(z1d[2]), "+v"(z1d[3]), "+v"(z1d[4]), "+v"(z1d[5]), "+v"(z1d[6]));
      SCHED();
      STAMPT(30);
    };
    if (POLLP == 1) { z_start(); z1_fin(); }
    else if (POLLP == 0) { z1_fin(); z_start(); }
    else if (kh1) { z_start(); z1_fin(); }
    else { z1_fin(); z_start(); }
  }

  {
    SCHED();
#pragma unroll
    for (int g = TRA; g < 8; ++g) WSP_TR(g);
    SCHED();
    STAMP(20);
    const int rt = rt1, kh = kh1;
    unsigned rw = (unsigned)(L_RED + w * 512 + l * 8), rr = (unsigned)(L_RED + w * 4096 + l * 8);
    asm volatile("" : "+v"(rw), "+v"(rr));
    const float bs = bflo(bs_raw);
    const unsigned uo = WS_UF + (unsigned)((R * 4 + rt) * 8 + c) * 5120u + (unsigned)kh * 2560u;
    const unsigned ui = WS_UF + (unsigned)((R * 4 + rt) * 8 + 4 * kh) * 5120u;
    unsigned l8;
    f32x4 acc2[2][4];
    float rv[8][2];
    u32x4 ud;
    u32x4 P[2][8];
    u32x2 P3[8], xp[3];
    auto ld_p = [&](int pt, int i) { P[pt][i] = __builtin_amdgcn_raw_buffer_load_b128(rs, l16 + (unsigned)pt * 1024u, ui + (unsigned)i * 2560u, SC1); };
    auto ld_p3 = [&](int i) { P3[i] = __builtin_amdgcn_raw_buffer_load_b64(rs, l8 + 2048u, ui + (unsigned)i * 2560u, SC1); };
    auto red_put = [&](int j, int r) {
      *(f32x2*)(lds + rw + r * 4096) = (f32x2){acc2[j & 1][r][0], acc2[j & 1][r][1]};
      *(f32x2*)(lds + rw + (r + 4) * 4096) = (f32x2){acc2[j & 1][r][2], acc2[j & 1][r][3]};
    };
    auto red_put1 = [&](int j, int i) { *(volatile LAS float*)(lds + rw + (i >> 2) * 4096 + ((i >> 1) & 1) * 16384 + (i & 1) * 4) = acc2[j & 1][i >> 2][i & 3]; };
    auto red_get = [&](int i) { const f32x2 x = *(const volatile LAS f32x2*)(lds + rr + i * 512); rv[i][0] = x[0]; rv[i][1] = x[1]; };
    float zz[2], gg[2];
    auto red_fin = [&](int j, int k) {
      const int e = k & 1, i = k >> 1;
#define VADD(d, x, y) asm volatile("v_add_f32 %0, %1, %2" : "=v"(d) : "v"(x), "v"(y))
      if (i < 4) VADD(rv[2 * i][e], rv[2 * i][e], rv[2 * i + 1][e]);
      if (i == 4 || i == 5) VADD(rv[4 * (i & 1)][e], rv[4 * (i & 1)][e], rv[4 * (i & 1) + 2][e]);
      if (i == 6) VADD(rv[0][e], rv[0][e], bs);
      if (i == 7) VADD(gg[e], rv[0][e], rv[4][e]);
      if (i == 8) { zz[e] = e ? bfhi(z1d[j]) : bflo(z1d[j]); asm volatile("" : "+v"(zz[e])); }
      if (i == 9) { zz[e] *= gg[e]; asm volatile("" : "+v"(zz[e])); }
      if (k == 20) { ud[j % 3] = pack2(zz[0], zz[1]); asm volatile("" : "+v"(ud[j % 3])); }
      if (k == 21) {
        if (j % 3 == 0) ud[3] = ud[0] + tag; else ud[3] += ud[j % 3];
        asm volatile("" : "+v"(ud[3]));
        if (j == 6) __builtin_amdgcn_raw_buffer_store_b64((u32x2){ud[0], ud[3]}, rs, l8 + 2048u, uo, SC1);
        else if (j % 3 == 2) __builtin_amdgcn_raw_buffer_store_b128(ud, rs, l16 + (unsigned)(j / 3) * 1024u, uo, SC1);
      }
    };
#define G2S 0
    struct Sch { int wide, lp, prio, lf, b1, b2, wp[16], rp[8], vp[22]; };
    constexpr Sch SC[4] = {
      {0, 2, 8, 1, 18, 29, {0, 1, 2, 3, 4, 5, 6, 7, 8, 9, 10, 11, 12, 13, 14, 15}, {19, 19, 20, 20, 21, 21, 22, 22}, {29, 29, 30, 30, 31, 31, 32, 33, 35, 36, 37, 38, 39, 40, 41, 42, 43, 44, 45, 46, 47, 48}},
      {1, 3, 4, 0, 18, 28, {3, 7, 11, 15, 99, 99, 99, 99, 99, 99, 99, 99, 99, 99, 99, 99}, {20, 20, 21, 21, 22, 22, 24, 24}, {28, 28, 29, 29, 30, 30, 31, 31, 32, 32, 33, 33, 34, 34, 35, 36, 36, 37, 37, 38, 38, 39}},
      {0, 1, 4, 1, 17, 29, {0, 1, 2, 3, 4, 5, 6, 7, 8, 9, 10, 11, 12, 13, 14, 15}, {17, 17, 18, 18, 19, 19, 20, 20}, {29, 29, 30, 30, 31, 31, 32, 34, 35, 36, 38, 39, 40, 42, 43, 44, 45, 46, 47, 48, 48, 49}},
      {0, 2, 0, 1, 17, 25, {0, 1, 2, 3, 4, 5, 6, 7, 8, 9, 10, 11, 12, 13, 14, 15}, {17, 17, 18, 18, 19, 19, 20, 20}, {25, 25, 26, 26, 27, 27, 28, 28, 29, 29, 30, 30, 31, 31, 32, 33, 23, 24, 34, 35, 36, 37}}};
    constexpr Sch S = SC[G2S];
    sfor<0, 224>([&](auto M_) __attribute__((always_inline)) {
      constexpr int mm = decltype(M_)::value, f = mm >> 2, r = mm & 3, j = f >> 3, t = f & 7, m = mm & 31;
      if (t == 0) asm volatile("v_mfma_f32_16x16x32_bf16 %0, %1, %2, 0" : "=&v"(acc2[j & 1][r]) : "v"(zw[f % NW]), "v"(wsp[r][t]), "v"(zw[(f | 1) % NW]));
      else asm volatile("v_mfma_f32_16x16x32_bf16 %0, %1, %2, %0" : "+v"(acc2[j & 1][r]) : "v"(zw[f % NW]), "v"(wsp[r][t]), "v"(zw[(f | 1) % NW]));
      SCHED();
      if (S.prio && (m + 31 - S.lp) % (S.prio ? S.prio : 1) == 0) __builtin_amdgcn_s_setprio(1);
      auto ld = [&]() {
        if (r == S.lp) {
          if (S.prio && (m + 32 - S.lp) % (S.prio ? S.prio : 1) == 0) __builtin_amdgcn_s_setprio(0);
          constexpr int g = S.lp == 3 ? f : f - 1;
          if (g >= 0 && g + NW < 56) LDZ(g + NW);
          constexpr int n1 = WOE < 8 ? 8 - WOE : 0, n2 = WOE < 8 ? 7 : 15 - WOE, per = n1 + n2 + 8 > 13 ? 2 : 1, y0 = (g - (per == 2 ? 56 - NW : 54 - n1 - n2 - 8)) * per;
          sfor<0, per>([&](auto E_) __attribute__((always_inline)) {
            constexpr int y = y0 + decltype(E_)::value;
            if (y >= 0 && y < n1) wo_dma(WOE + y);
            if (y >= n1 && y < n1 + 8) ld_p(0, (y - n1) & 7);
            if (y >= n1 + 8 && y < n1 + 8 + n2) wo_dma(15 - n2 + y - n1 - 8);
          });
          SCHED();
        }
      };
      if (!S.lf) ld();
      if ((m == S.b1 && j > 0) || m == S.b2) { asm volatile("" ::: "memory"); __builtin_amdgcn_s_waitcnt(0xC07F); RBAR(); SCHED(); }
      sfor<0, 16>([&](auto I_) __attribute__((always_inline)) {
        constexpr int i = decltype(I_)::value;
        if (j > 0 && S.wp[i] == m) { if (S.wide) red_put(j + 1, i & 3); else red_put1(j + 1, i); SCHED(); }
        if (j > 0 && i < 8 && S.rp[i & 7] == m) { red_get(i & 7); SCHED(); }
      });
      sfor<0, 22>([&](auto K_) __attribute__((always_inline)) {
        constexpr int k = decltype(K_)::value;
        if (j > 0 && S.vp[k] == m) { red_fin(j > 0 ? j - 1 : 0, k); SCHED(); }
        if (j > 1 && S.vp[k] == m + 32) { red_fin(j > 1 ? j - 2 : 0, k); SCHED(); }
      });
      if (S.lf) ld();
    });
    sfor<0, 22>([&](auto K_) __attribute__((always_inline)) { if (S.vp[decltype(K_)::value] >= 32) red_fin(5, decltype(K_)::value); });
    asm volatile("v_lshrrev_b32 %0, 1, %1\n\ts_nop 3" : "=v"(l8) : "v"(l16));
    const int lr = (int)(l8 >> 3), nr = lr & 15, hr = lr >> 4;
    SCHED();
    __builtin_amdgcn_s_setprio(3);
#pragma unroll
    for (int r = 0; r < 4; ++r) red_put(6, r);
    STAMP(15);
    asm volatile("s_waitcnt vmcnt(%0)" :: "n"(8 + (WOE < 8 ? 7 : 15 - WOE)) : "memory");
    BAR();
#pragma unroll
    for (int i = 0; i < 8; ++i) red_get(i);
    unsigned wb[3];
#pragma unroll
    for (int i = 0; i < 3; ++i) { wb[i] = (unsigned)(kh * 1024 + i * 65536) + l16; asm volatile("" : "+v"(wb[i])); }
    u32x4 af[30];
#define LDA(m) af[(m) % 30] = *(const u32x4*)(lds + wb[WOA(2 * (m)) >> 16] + (WOA(2 * (m)) & 65535))
    SCHED();
#pragma unroll
    for (int m = 0; m < 7; ++m) LDA(m);
    SCHED();
#pragma unroll
    for (int k = 0; k < 22; ++k) red_fin(6, k < 2 ? 16 + k : k < 18 ? k - 2 : k);
    SCHED();
    RBAR();
    __builtin_amdgcn_s_setprio(0);
    STAMPT(16);
#pragma unroll
    for (int k = 0; k < 3; ++k) {
      const int i = 120 + (k < 2 || w < 4 ? w + 8 * k : w);
      dma1k(rs, lds + (i - 92) * 1024, l16, WS_WOUTF + (unsigned)c * WBLK + (unsigned)i * 1024u);
    }
#pragma unroll
    for (int x = 0; x < 3; ++x)
      xp[x] = __builtin_amdgcn_raw_buffer_load_b64(rs, (unsigned)(hr * 128 + psi(nr) * 8), WS_X1B + (unsigned)(R * 4 + rt) * 20480u + (unsigned)(5 * c + (kh ? (3 + (x < 2 ? x : 1)) : x)) * 512u, BULK_AUX);
#pragma unroll
    for (int m = 7; m < 14; ++m) LDA(m);
    auto p_ok = [&](int pt) {
      unsigned d = 0u;
#pragma unroll
      for (int i = 0; i < 8; ++i) d |= ((P[pt][i][0] + P[pt][i][1]) + (P[pt][i][2] + tag)) ^ P[pt][i][3];
      return __all(d == 0u);
    };
    auto p_get = [&](int pt) {
      if (!p_ok(pt)) {
        _Pragma("unroll 1") for (unsigned spins = 0; spins < SPIN_CAP; ++spins) {
          __builtin_amdgcn_s_sleep(1);
#pragma unroll
          for (int i = 0; i < 8; ++i) ld_p(pt, i);
          if (p_ok(pt)) break;
        }
      }
#pragma unroll
      for (int i = 0; i < 6; ++i) P[pt][4 * (i / 3) + i % 3][3] = P[pt][4 * (i / 3) + 3][i % 3];
    };
    f32x4 acc[5];
#define P2M 8
#define P3M 40
    u32x2 gbv[3];
    u32x4 F3[2];
    auto g3 = [&](auto M0_, auto M1_) {
#pragma unroll
      for (int m = decltype(M0_)::value; m < decltype(M1_)::value; ++m) {
        if (m == 16 && WOE < 15) {
          asm volatile("s_waitcnt vmcnt(%0)" :: "n"(8 + (P2M < 16 ? (16 - P2M > 8 ? 8 : 16 - P2M) : 0)) : "memory");
          RBAR();
        }
        SCHED();
        const int u2 = m / 5, f6 = u2 % 6;
        acc[m % 5] = MFMA16(af[m % 30], u2 < 12 ? P[u2 < 12 ? u2 / 6 : 0][4 * (f6 / 3) + f6 % 3] : F3[u2 & 1], m < 5 ? ((f32x4){0.f, 0.f, 0.f, 0.f}) : acc[m % 5]);
        if (m + 14 < 70) LDA(m + 14);
        if (m >= P2M && m < P2M + 8) ld_p(1, m - P2M);
        if (m >= P3M && m < P3M + 8) ld_p3(m - P3M);
        if (m >= 56 && m < 59) gbv[m - 56] = *(const u32x2*)(lds + L_MISC + (4 * (kh ? 3 + (m < 58 ? m - 56 : 1) : m - 56) + hr) * 8);
        SCHED();
      }
    };
    p_get(0);
    g3(IC<0>(), IC<30>());
    STAMPT(17);
    p_get(1);
    RBAR();
    STAMPT(25);
    g3(IC<30>(), IC<60>());
    auto ok3 = [&]() {
      unsigned d = 0u;
#pragma unroll
      for (int i = 0; i < 8; ++i) d |= (P3[i][0] + tag) ^ P3[i][1];
      return __all(d == 0u);
    };
    if (!ok3()) {
      _Pragma("unroll 1") for (unsigned spins = 0; spins < SPIN_CAP; ++spins) {
        __builtin_amdgcn_s_sleep(1);
#pragma unroll
        for (int i = 0; i < 8; ++i) ld_p3(i);
        if (ok3()) break;
      }
    }
#pragma unroll
    for (int i = 0; i < 2; ++i) F3[i] = (u32x4){P3[4 * i][0], P3[4 * i + 1][0], P3[4 * i + 2][0], P3[4 * i + 3][0]};
    STAMPT(26);
    g3(IC<60>(), IC<70>());
    STAMPT(18);
    f32x4* X = (f32x4*)lds + rt * (5 * 64) + lr;
    f32x4 m4[3];
    if (kh == 0) {
      X[3 * 64] = acc[3]; X[4 * 64] = acc[4];
      m4[0] = acc[0]; m4[1] = acc[1]; m4[2] = acc[2];
    } else {
      X[0] = acc[0]; X[64] = acc[1]; X[128] = acc[2];
      m4[0] = acc[3]; m4[1] = acc[4]; m4[2] = acc[4];
    }
    BAR();
    if (bid == 0 && w == 5 && lr == 0) st4(rs, WS_EPOCH, tag);
    const int t = R * 64 + tau(rt, nr);
    f32x4 o4[3];
#pragma unroll
    for (int j = 0; j < 3; ++j) o4[j] = X[(kh ? 3 + (j < 2 ? j : 1) : j) * 64];
    SCHED();
#pragma unroll
    for (int j = 0; j < 3; ++j) {
      const f32x4 v = rbf4((f32x4){bflo(gbv[j][0]), bfhi(gbv[j][0]), bflo(gbv[j][1]), bfhi(gbv[j][1])} * rbf4(o4[j] + m4[j]));
      const u32x2 o = {pack2(bflo(xp[j][0]) + v[0], bfhi(xp[j][0]) + v[1]), pack2(bflo(xp[j][1]) + v[2], bfhi(xp[j][1]) + v[3])};
      if (j < 2 || kh == 0)
        __builtin_amdgcn_raw_buffer_store_b64(o, __builtin_amdgcn_make_buffer_rsrc((void*)p.out, 0, S_ * D_ * 2, 0x00020000),
                                              (unsigned)(t * D_ + 80 * c + 16 * (kh ? 3 + j : j) + 4 * hr) * 2u, 0, 0);
    }
  }
  STAMP(19);
}

extern "C" __device__ __attribute__((used, noinline, visibility("hidden"))) void mega_tail() {}

extern "C" void kernel_launch(void* const* d_in, const int* in_sizes, int n_in, void* d_out, int out_size,
                              void* d_ws, size_t ws_size, hipStream_t stream) {
  Params p;
  p.q = (const u16*)d_in[0];
  p.k = (const u16*)d_in[1];
  p.v = (const u16*)d_in[2];
  p.shift_k = (const u16*)d_in[3];
  p.ssm_k = (const u16*)d_in[4];
  p.ssm_D = (const u16*)d_in[5];
  p.w_in = (const u16*)d_in[7];
  p.w_sp = (const u16*)d_in[8];
  p.b_sp = (const u16*)d_in[9];
  p.w_out = (const u16*)d_in[10];
  p.cvec = (const u16*)d_in[11];
  p.w_mod = (const u16*)d_in[12];
  p.out = (u16*)d_out;
  p.ws = (char*)d_ws;
  mega<<<256, 512, 0, stream>>>(p);
}
```

```cpp
#define STAMPS
#define FRONT
#define STMASK ((1u << 0) | (1u << 0))
#define CYC_A 0
#define CYC_B 0
#define CYC_MODE 0
#define CYC_BID 100
#define STW 4
#include <hip/hip_runtime.h>
#include <stdint.h>

typedef unsigned short u16;
typedef __attribute__((ext_vector_type(8))) short bf16x8;
typedef __attribute__((ext_vector_type(4))) short bf16x4;
typedef __attribute__((ext_vector_type(4))) float f32x4;
typedef __attribute__((ext_vector_type(2))) float f32x2;
typedef __attribute__((ext_vector_type(4))) unsigned int u32x4;
typedef __attribute__((ext_vector_type(2))) unsigned int u32x2;
#define LAS __attribute__((address_space(3)))
typedef __amdgpu_buffer_rsrc_t rsrc_t;
typedef __attribute__((ext_vector_type(4))) short s16x4;

constexpr int S_ = 2048, D_ = 640, F_ = 1728, FH = 864, CB = 108;

constexpr unsigned WS_EPOCH = 0;
constexpr unsigned WS_FWI = 4096;
constexpr unsigned WS_FZ = WS_FWI + 384 * 128;
constexpr unsigned WS_CS = WS_FZ + 256 * 128;
constexpr unsigned WS_MOD = 131072;
constexpr unsigned WS_MODP = WS_MOD + 81920;
constexpr unsigned WS_ST1 = 393216;
constexpr unsigned WS_PART = 524288;
constexpr unsigned WS_CSP = WS_PART + 262144;
constexpr unsigned WS_FX = WS_CSP + 655360;
constexpr unsigned WS_X1B = WS_FX + 16 * 4 * 160 * 128;
constexpr unsigned WS_QB = WS_X1B + 2621440;
constexpr unsigned WS_KV3 = WS_QB + 2621440;
constexpr unsigned WS_MODQ = WS_KV3 + 1310720;
constexpr unsigned WBLK = 143360;
constexpr unsigned WS_WINF = WS_MODQ + 245760;
constexpr unsigned WS_WOUTF = WS_WINF + 16 * WBLK;
constexpr unsigned WS_Z2F = WS_WOUTF + 8 * WBLK;
constexpr unsigned WS_UF = WS_Z2F + 8 * 64 * 7168;
constexpr unsigned WS_F4 = WS_UF + 32 * 4 * 8 * 4096;
constexpr unsigned WS_END = WS_F4 + 4 * 81920;
constexpr unsigned WS_STAMP = 200u << 20;

constexpr int G_COPY = 4160;
constexpr int L_U = WBLK;
constexpr int U_CH = (256 + S_) * 2 + 32;
constexpr int L_FLG = L_U + 4 * U_CH;
constexpr int L_RT = 16384;
constexpr int L_CONST = WBLK;
constexpr int L_PST = L_CONST + 2048;
constexpr int L_Z = L_CONST + 5120;
constexpr int L_ST2 = L_Z + 14336;
constexpr int L_MISC = L_ST2 + 512;
constexpr int LDS_TOTAL = 163840;

#define AUX_T 630
#define RT2_T 380
#define CB_T 400
#define W_T 750
#define CS_T 850
#define Y_T 950
#define K_T 480
#ifndef M_T
#define M_T 540
#endif
#define WA_T 600
constexpr int SC1 = 16, NT = 2;
constexpr unsigned SPIN_CAP = 1u << 16;

__device__ __forceinline__ float bflo(unsigned x) { return __uint_as_float(x << 16); }
__device__ __forceinline__ float bfhi(unsigned x) { return __uint_as_float(x & 0xffff0000u); }
__device__ __forceinline__ unsigned f2bf(float x) {
  __bf16 b = (__bf16)x;
  return (unsigned)__builtin_bit_cast(unsigned short, b);
}
__device__ __forceinline__ float rbf(float x) { return bflo(f2bf(x)); }
typedef __attribute__((ext_vector_type(2))) __bf16 bf2_t;
__device__ __forceinline__ unsigned pack2(float lo, float hi) {
  f32x2 v = {lo, hi};
  return __builtin_bit_cast(unsigned, __builtin_convertvector(v, bf2_t));
}
__device__ __forceinline__ f32x2 rbf2(f32x2 v) {
  const unsigned p = __builtin_bit_cast(unsigned, __builtin_convertvector(v, bf2_t));
  return (f32x2){__uint_as_float(p << 16), __uint_as_float(p & 0xffff0000u)};
}
__device__ __forceinline__ f32x4 rbf4(f32x4 v) {
  const f32x2 a = rbf2((f32x2){v[0], v[1]}), b = rbf2((f32x2){v[2], v[3]});
  return (f32x4){a[0], a[1], b[0], b[1]};
}
template <int CTRL>
__device__ __forceinline__ float dpp_f(float v) {
  return __int_as_float(__builtin_amdgcn_update_dpp(0, __float_as_int(v), CTRL, 0xf, 0xf, true));
}
__device__ __forceinline__ float sum8(float v) {
  v += dpp_f<0xB1>(v);
  v += dpp_f<0x4E>(v);
  v += dpp_f<0x141>(v);
  return v;
}
__device__ __forceinline__ float sum16(float v) {
  v = sum8(v);
  v += dpp_f<0x140>(v);
  return v;
}
__device__ __forceinline__ float sum_rows(float v) {
  auto a = __builtin_amdgcn_permlane16_swap(__float_as_uint(v), __float_as_uint(v), false, false);
  v = __uint_as_float(a[0]) + __uint_as_float(a[1]);
  auto b = __builtin_amdgcn_permlane32_swap(__float_as_uint(v), __float_as_uint(v), false, false);
  return __uint_as_float(b[0]) + __uint_as_float(b[1]);
}
#define MFMA16(a, b, c) __builtin_amdgcn_mfma_f32_16x16x32_bf16(__builtin_bit_cast(bf16x8, a), __builtin_bit_cast(bf16x8, b), c, 0, 0, 0)
#define MFMA16K16(a, b, c) __builtin_amdgcn_mfma_f32_16x16x16bf16_1k(__builtin_bit_cast(bf16x4, a), __builtin_bit_cast(bf16x4, b), c, 0, 0, 0)
__device__ __forceinline__ u32x2 trd(const char* p) {
  return __builtin_bit_cast(u32x2, __builtin_amdgcn_ds_read_tr16_b64_v4i16((LAS s16x4*)p));
}
__device__ __forceinline__ u32x4 trfrag(const char* p, int pitch) {
  u32x2 a = trd(p), b = trd(p + 16 * pitch);
  return (u32x4){a[0], a[1], b[0], b[1]};
}
#define BAR() asm volatile("s_waitcnt lgkmcnt(0)\n\ts_barrier" ::: "memory")
#define DRAIN() do { __builtin_amdgcn_s_waitcnt(0x0F70); asm volatile("" ::: "memory"); } while (0)
#define SCHED() __builtin_amdgcn_sched_barrier(0)
template <int N> struct IC { static constexpr int value = N; };

__device__ __forceinline__ u32x4 ld16(rsrc_t r, unsigned off) { return __builtin_amdgcn_raw_buffer_load_b128(r, off, 0, SC1); }
__device__ __forceinline__ u32x2 ld8(rsrc_t r, unsigned off) { return __builtin_amdgcn_raw_buffer_load_b64(r, off, 0, SC1); }
__device__ __forceinline__ unsigned ld4(rsrc_t r, unsigned off) { return __builtin_amdgcn_raw_buffer_load_b32(r, off, 0, SC1); }
#define ST_AUX 16
__device__ __forceinline__ void st16(rsrc_t r, unsigned off, u32x4 v) { __builtin_amdgcn_raw_buffer_store_b128(v, r, off, 0, ST_AUX); }
__device__ __forceinline__ void st8(rsrc_t r, unsigned off, u32x2 v) { __builtin_amdgcn_raw_buffer_store_b64(v, r, off, 0, SC1); }
__device__ __forceinline__ void st_flag4(rsrc_t r, unsigned off, unsigned tag, int l) {
  if (l < 32) st16(r, WS_F4 - WS_FWI + off + (unsigned)(l >> 3) * 81920u + (unsigned)(l & 7) * 16u, (u32x4){tag, tag, tag, tag});
}
__device__ __forceinline__ void st4(rsrc_t r, unsigned off, unsigned v) { __builtin_amdgcn_raw_buffer_store_b32(v, r, off, 0, SC1); }
#define BULK_AUX 16
__device__ __forceinline__ u32x4 ld16p(rsrc_t r, unsigned off) { return __builtin_amdgcn_raw_buffer_load_b128(r, off, 0, BULK_AUX); }
__device__ __forceinline__ u32x2 ld8p(rsrc_t r, unsigned off) { return __builtin_amdgcn_raw_buffer_load_b64(r, off, 0, BULK_AUX); }
__device__ __forceinline__ void dma1k(rsrc_t r, char* lds_dst, unsigned l16, unsigned soff) {
  __builtin_amdgcn_raw_ptr_buffer_load_lds(r, (LAS void*)lds_dst, 16, l16, soff, 0, BULK_AUX);
}

#define STAMP(i) do { if ((STMASK >> (i)) & 1u) { SCHED(); stv[i] = (unsigned)(CYC_MODE ? __builtin_amdgcn_s_memtime() : __builtin_amdgcn_s_memrealtime()); SCHED(); } } while (0)
#define STAMPF(i) STAMP(i)
#define STAMPT(i)

template <int N, int SLP>
__device__ __forceinline__ void wait_lines(rsrc_t r, const unsigned (&off)[N], unsigned tag) {
  bool need[N];
#pragma unroll
  for (int i = 0; i < N; ++i) need[i] = off[i] != ~0u;
  _Pragma("unroll 1") for (unsigned spins = 0; spins < SPIN_CAP; ++spins) {
    unsigned v[N];
#pragma unroll
    for (int i = 0; i < N; ++i) {
      v[i] = tag;
      if (need[i]) v[i] = ld4(r, off[i]);
    }
    bool any = false;
#pragma unroll
    for (int i = 0; i < N; ++i) { need[i] = v[i] != tag; any = any || need[i]; }
    if (!__any(any)) break;
    __builtin_amdgcn_s_sleep(SLP);
  }
  asm volatile("" ::: "memory");
}

__device__ __forceinline__ int tau(int rt, int n) { return 32 * (rt >> 1) + 8 * (n >> 2) + 4 * (rt & 1) + (n & 3); }
__device__ __forceinline__ int psi(int n) { return 2 * (n & 7) + (n >> 3); }
#define QX_CM(d, s0, s1, P) "v_cndmask_b32_dpp " d ", " s0 ", " s1 ", vcc quad_perm:" P " row_mask:0xf bank_mask:0xf\n\t"
#define QX_BODY(M, P) \
  asm("s_mov_b32 vcc_lo, " M "\n\ts_mov_b32 vcc_hi, " M "\n\t" \
      QX_CM("%0", "%12", "%8", P) "v_xor_b32 %4, %8, %0\n\t" QX_CM("%1", "%13", "%9", P) "v_xor_b32 %5, %9, %1\n\t" \
      QX_CM("%2", "%14", "%10", P) "v_xor_b32 %6, %10, %2\n\t" QX_CM("%3", "%15", "%11", P) "v_xor_b32 %7, %11, %3" \
      : "=&v"(t[0]), "=&v"(t[1]), "=&v"(t[2]), "=&v"(t[3]), "=&v"(u[0]), "=&v"(u[1]), "=&v"(u[2]), "=&v"(u[3]) \
      : "v"(a[0]), "v"(a[1]), "v"(a[2]), "v"(a[3]), "v"(b[0]), "v"(b[1]), "v"(b[2]), "v"(b[3]) : "vcc")
template <int BIT>
__device__ __forceinline__ void quad_xchg3(u32x4& a, u32x4& b) {
  unsigned t[4], u[4];
  if (BIT == 0) QX_BODY("0x55555555", "[1,0,3,2]");
  else QX_BODY("0x33333333", "[2,3,0,1]");
  a = (u32x4){t[0], t[1], t[2], t[3]};
#pragma unroll
  for (int d = 0; d < 4; ++d) b[d] ^= (unsigned)__builtin_amdgcn_update_dpp(0, (int)u[d], BIT == 0 ? 0xB1 : 0x4E, 0xf, 0xf, true);
}
__device__ __forceinline__ void quad_transpose(u32x4& x0, u32x4& x1, u32x4& x2, u32x4& x3) {
  quad_xchg3<0>(x0, x1);
  quad_xchg3<0>(x2, x3);
  quad_xchg3<1>(x0, x2);
  quad_xchg3<1>(x1, x3);
  asm volatile("" : "+v"(x0), "+v"(x1), "+v"(x2), "+v"(x3));
}

struct Params {
  const u16 *q, *k, *v, *shift_k, *ssm_k, *ssm_D, *w_in, *w_sp, *b_sp, *w_out, *cvec, *w_mod;
  u16* out;
  char* ws;
};

__global__ __launch_bounds__(512, 2) void mega(Params p) {
  __shared__ __attribute__((aligned(16))) char lds[LDS_TOTAL];
  const int tid = threadIdx.x, l = tid & 63;
  const int w = __builtin_amdgcn_readfirstlane(tid >> 6);
  const int bid = blockIdx.x, xcd = bid & 7, idx = bid >> 3;
  char* const ws = p.ws;
  asm volatile("" ::"s"(p.q), "s"(p.k), "s"(p.v), "s"(p.shift_k), "s"(p.ssm_k), "s"(p.ssm_D), "s"(p.w_in), "s"(p.w_sp), "s"(p.b_sp),
               "s"(p.w_out), "s"(p.cvec), "s"(p.w_mod), "s"(p.out), "s"(p.ws));
  unsigned epoch, tag;
  asm volatile("s_load_dword %0, %1, 0x0" : "=s"(epoch) : "s"(ws));
#define TAG_READY() do { SCHED(); asm volatile("s_waitcnt lgkmcnt(0)\n\ts_mul_i32 %0, %1, 0x9e3779b1\n\ts_add_i32 %0, %0, 0x9e3779b9" : "=s"(tag) : "s"(epoch) : "scc"); SCHED(); } while (0)
  const rsrc_t rs = __builtin_amdgcn_make_buffer_rsrc(ws, 0, WS_END, 0x00020000);
  const unsigned l16 = (unsigned)l * 16u;
  unsigned stv[32];
#pragma unroll
  for (int i = 0; i < 32; ++i) stv[i] = 0u;
  STAMP(0);
  const unsigned t_start = (unsigned)__builtin_amdgcn_s_memrealtime();
  unsigned long long pc0;
  unsigned clen;
  asm volatile("s_getpc_b64 %0\n.Lmega_here:\n\ts_mov_b32 %1, mega_tail-.Lmega_here" : "=s"(pc0), "=s"(clen));
  if (idx >= 24) {
    const rsrc_t rc = __builtin_amdgcn_make_buffer_rsrc((void*)(pc0 & ~15ull), 0, clen & ~15u, 0x00020000);
    __builtin_amdgcn_raw_ptr_buffer_load_lds(rc, (LAS void*)(lds + 8192), 16, (unsigned)(((idx - 24) * 8 + w) * 1024) + l16, 0, 0, 0);
  }

  const bool is_conv = idx < 20;
  const int quad = xcd * 20 + idx;
  const int R = is_conv ? (xcd & 3) * 5 + (idx >> 2) : (xcd & 3) * 3 + 15 + (idx >> 2);
  const int c = (xcd >> 2) * 4 + (idx & 3);
  const unsigned fwi = WS_F4 + (unsigned)(xcd & 3) * 81920u, fz = fwi + (WS_FZ - WS_FWI);
  const int n16 = l & 15, h4 = l >> 4;
  const int rp = w & 1, kq = w >> 1;
  u32x4 wsp[2][16];
  const int rot = idx & 12;
  const rsrc_t rsp = __builtin_amdgcn_make_buffer_rsrc((void*)p.w_sp, 0, S_ * S_ * 2, 0x00020000);
  const unsigned wsp_vo = (unsigned)(((l >> 2) & 3) * 8 * 4096 + h4 * 64 + (l & 3) * 16);
#define LDW(k) wsp[((k) >> 2) & 1][((k) >> 3) * 4 + ((k) & 3)] = __builtin_amdgcn_raw_buffer_load_b128(rsp, wsp_vo, \
    (unsigned)((R * 64 + 32 * rp + 4 * (((k) >> 2) & 1) + ((k) & 3)) * 4096 + kq * 1024 + (((((k) >> 3) * 4 + rot) & 15) * 64)), 0)
#define WSP_TR(g) quad_transpose(wsp[(g) & 1][((g) >> 1) * 4], wsp[(g) & 1][((g) >> 1) * 4 + 1], wsp[(g) & 1][((g) >> 1) * 4 + 2], wsp[(g) & 1][((g) >> 1) * 4 + 3])
  const rsrc_t rwi = __builtin_amdgcn_make_buffer_rsrc((void*)p.w_in, 0, D_ * F_ * 2, 0x00020000);
  const rsrc_t rwo = __builtin_amdgcn_make_buffer_rsrc((void*)p.w_out, 0, FH * D_ * 2, 0x00020000);
  const rsrc_t rq = __builtin_amdgcn_make_buffer_rsrc((void*)p.q, 0, S_ * D_ * 2, 0x00020000);
  auto hold = [&](unsigned tt) { while ((unsigned)__builtin_amdgcn_s_memrealtime() - t_start < tt) __builtin_amdgcn_s_sleep(2); };
  volatile LAS unsigned* lf = (volatile LAS unsigned*)(lds + L_FLG);
  auto lf_set = [&](int i) { asm volatile("s_waitcnt lgkmcnt(0)" ::: "memory"); if (l == 0) lf[i] = tag; };
  auto lf_wait = [&](int i0, int n) {
    _Pragma("unroll 1") for (unsigned spins = 0; spins < (SPIN_CAP << 4); ++spins) {
      const unsigned v = lf[i0 + (l & (n - 1))];
      if (__all(v == tag)) break;
      __builtin_amdgcn_s_sleep(1);
    }
    asm volatile("" ::: "memory");
  };
  auto xflag = [&](int s) {
    st16(rs, WS_FX + (unsigned)(((4 * s + (w & 3)) * 4 + ((l >> 3) & 3)) * 160 + quad) * 128u + (unsigned)(l & 7) * 16u, (u32x4){tag, tag, tag, tag});
    SCHED();
  };
  auto load_b2 = [&]() {
#pragma unroll
    for (int k = 0; k < 35; ++k) {
      int i = k + (idx >> 2) * 4;
      i = (i >= 35 ? i - 35 : i) + 35 * w;
      dma1k(rs, lds + i * 1024, l16, WS_WINF + (unsigned)(8 + c) * WBLK + (unsigned)i * 1024u);
    }
  };
  char* const wo_st = lds + 122880;
  auto wo_ld = [&]() {
#pragma unroll
    for (int r = 0; r < 5; ++r) {
      const int g = r * 64 + l;
      __builtin_amdgcn_raw_ptr_buffer_load_lds(rwo, (LAS void*)(wo_st + r * 1024), 16, (unsigned)((g / 10 + (R >> 2) * CB + (R & 3) * 32) * (D_ * 2) + (g % 10) * 16 + c * 160), 0, 0, 0);
    }
  };
  auto rt_ld = [&](int i0, char* st) {
    const int cb = (i0 < 160 ? 8 : 0) + (i0 & 7), Q = (i0 < 160 ? i0 : i0 - 160) >> 3;
#pragma unroll
    for (int r = 0; r < 7; ++r) {
      const int g = r * 64 + l;
      __builtin_amdgcn_raw_ptr_buffer_load_lds(rwi, (LAS void*)(st + r * 1024), 16, (unsigned)((g / 14) * (F_ * 2) + (g % 14) * 16 + Q * 32 * F_ * 2 + cb * CB * 2),
                                               0, 0, NT);
    }
  };
  auto rt_job = [&](int i0, char* st) {
    const int cb = (i0 < 160 ? 8 : 0) + (i0 & 7), Q = (i0 < 160 ? i0 : i0 - 160) >> 3;
    const int col = 640 * (l >> 5) + 32 * Q + (l & 31);
    const unsigned po = WS_MODP + (unsigned)col * 8u;
    u32x2 pp8[8];
    hold(CB_T);
    _Pragma("unroll 1") for (unsigned spins = 0; spins < SPIN_CAP; ++spins) {
      unsigned dd = 0u;
#pragma unroll
      for (int i = 0; i < 8; ++i) pp8[i] = ld8(rs, po + (unsigned)i * 10240u);
#pragma unroll
      for (int i = 0; i < 8; ++i) dd |= pp8[i][0] ^ tag;
      if (__all(dd == 0u)) break;
      __builtin_amdgcn_s_sleep(4);
    }
    STAMPF(9);
    {
      float t = 0.f;
#pragma unroll
      for (int i = 0; i < 8; ++i) t += __uint_as_float(pp8[i][1]);
      if (l >= 32) *(float*)(st + 7168 + (l - 32) * 4) = rbf(1.0f + rbf(t));
      else *(u16*)(st + 7296 + l * 2) = (u16)f2bf(t);
    }
    DRAIN();
    asm volatile("s_waitcnt lgkmcnt(0)" ::: "memory");
    const f32x4 ca = *(const f32x4*)(st + 7168 + h4 * 16), cc = *(const f32x4*)(st + 7168 + 64 + h4 * 16);
    const char* bp = st + (4 * h4 + (n16 >> 2)) * 224 + (n16 & 3) * 8;
    u32x4 x[7], o[7];
#pragma unroll
    for (int j = 0; j < 7; ++j) {
      x[j] = trfrag(bp + j * 32, 224);
      o[j] = (u32x4){pack2(bflo(x[j][0]) * ca[0], bfhi(x[j][0]) * ca[1]), pack2(bflo(x[j][1]) * ca[2], bfhi(x[j][1]) * ca[3]),
                     pack2(bflo(x[j][2]) * cc[0], bfhi(x[j][2]) * cc[1]), pack2(bflo(x[j][3]) * cc[2], bfhi(x[j][3]) * cc[3])};
      if (j == 6 && n16 == 12) o[j] = (u32x4){0x3F803F80u, 0x3F803F80u, 0x3F803F80u, 0x3F803F80u};
      st16(rs, WS_WINF + (unsigned)cb * WBLK + (unsigned)(Q * 7 + j) * 1024u + l16, o[j]);
    }
    STAMPF(11);
    const u32x2 e0 = *(const u32x2*)(st + 7296 + h4 * 8), e1 = *(const u32x2*)(st + 7328 + h4 * 8);
    f32x4 d[2] = {{0.f, 0.f, 0.f, 0.f}, {0.f, 0.f, 0.f, 0.f}};
#pragma unroll
    for (int j = 0; j < 7; ++j) {
      const unsigned one = n16 == 4 * (j & 3) ? 0x3F803F80u : 0u;
      const bool sr = n16 == 4 * (j & 3) + 1;
      d[j >> 2] = MFMA16(((u32x4){one, one, one, one}), o[j], d[j >> 2]);
      d[j >> 2] = MFMA16(((u32x4){sr ? e0[0] : 0u, sr ? e0[1] : 0u, sr ? e1[0] : 0u, sr ? e1[1] : 0u}), x[j], d[j >> 2]);
    }
#pragma unroll
    for (int m = 0; m < 2; ++m)
      st16(rs, WS_CSP + (unsigned)((cb * 20 + Q) * 2 + m) * 1024u + l16, (u32x4){tag, __float_as_uint(d[m][0]), tag, __float_as_uint(d[m][1])});
    STAMPF(13);
    asm volatile("s_waitcnt vmcnt(2)" ::: "memory");
    st_flag4(rs, WS_FWI + (unsigned)(cb * 20 + Q) * 128u, tag, l);
  };

  auto red8 = [&](float v) { v += dpp_f<0x128>(v); return sum_rows(v); };
  const rsrc_t rwm = __builtin_amdgcn_make_buffer_rsrc((void*)p.w_mod, 0, D_ * 3840 * 2, 0x00020000);
  auto gemv_load = [&](u32x4* pre, int blk, int sl, int np) {
    pre[10][0] = *(const unsigned*)(p.cvec + 8 * np * sl + 2 * (l < 4 * np ? l : 0));
#pragma unroll
    for (int pp = 0; pp < np; ++pp)
      pre[pp] = __builtin_amdgcn_raw_buffer_load_b128(rwm, (unsigned)((l >> 3) * 7680 + (l & 7) * 16), (unsigned)((8 * np * sl + pp * 8) * 7680 + blk * 128), NT);
  };
  auto gemv_fin = [&](float* acc, const u32x4* pre, int np, char* scr) {
    const unsigned cv2 = pre[10][0];
    unsigned* svt = (unsigned*)scr + w * 96;
#pragma unroll
    for (int e = 0; e < 2; ++e) {
      const int row = 2 * l + e;
      const float cv = e ? bfhi(cv2) : bflo(cv2);
      if (l < 4 * np) svt[(row & 7) * 12 + (row >> 3)] = __float_as_uint(rbf(cv * __builtin_amdgcn_rcpf(1.0f + __expf(-cv))));
    }
    asm volatile("s_waitcnt lgkmcnt(0)" ::: "memory");
    u32x4 sv[3];
#pragma unroll
    for (int i = 0; i < (np + 3) / 4; ++i) sv[i] = *(const u32x4*)(svt + (l >> 3) * 12 + 4 * i);
    SCHED();
#pragma unroll
    for (int e = 0; e < 8; ++e) acc[e] = 0.f;
#pragma unroll
    for (int pp = 0; pp < np; ++pp) {
      const unsigned sh = sv[pp >> 2][pp & 3], sl2 = sh >> 16;
#pragma unroll
      for (int e = 0; e < 4; ++e) {
        const unsigned x = pre[pp][e];
        acc[2 * e] = __builtin_amdgcn_fdot2_f32_bf16(__builtin_bit_cast(bf2_t, x), __builtin_bit_cast(bf2_t, sl2), acc[2 * e], false);
        acc[2 * e + 1] = __builtin_amdgcn_fdot2_f32_bf16(__builtin_bit_cast(bf2_t, x), __builtin_bit_cast(bf2_t, sh), acc[2 * e + 1], false);
      }
    }
#pragma unroll
    for (int e = 0; e < 8; ++e) acc[e] = red8(acc[e]);
  };
  auto gemv_put = [&](const float* acc, unsigned off) {
    float v = acc[0];
#pragma unroll
    for (int e = 1; e < 8; ++e) { v = (l >> 3) == e ? acc[e] : v; asm("" : "+v"(v)); }
    st8(rs, off + (unsigned)l * 8u, (u32x2){tag, (unsigned)__builtin_amdgcn_ds_bpermute(((l & 7) * 8 + (l >> 3)) * 4, (int)__float_as_uint(v))});
  };
  typedef const __attribute__((address_space(4))) unsigned* cptr_t;

  if (__builtin_expect(is_conv, 1)) {
    if (w >= 4) {
      const int pw = w - 4;
      const rsrc_t ra = __builtin_amdgcn_make_buffer_rsrc((void*)((idx & 2) ? p.v : p.k), 0, S_ * D_ * 2, 0x00020000);
      const rsrc_t rb = __builtin_amdgcn_make_buffer_rsrc((void*)((idx & 2) ? p.k : p.v), 0, S_ * D_ * 2, 0x00020000);
      const rsrc_t rk = __builtin_amdgcn_make_buffer_rsrc((void*)p.k, 0, S_ * D_ * 2, 0x00020000);
      const unsigned vo = (unsigned)((128 * pw + 2 * l) * (D_ * 2));
      const unsigned so0 = (unsigned)((idx & 1) * (D_ * 2) + quad * 8), so1 = (unsigned)((1 - (idx & 1)) * (D_ * 2) + quad * 8);
      u32x2 g[3][4], kh;
      auto gl = [&](int s, int j0, int j1) {
        __builtin_amdgcn_s_setprio(0);
#pragma unroll
        for (int j = j0; j < j1; ++j) {
          g[s][j] = __builtin_amdgcn_raw_buffer_load_b64(j < 2 ? ra : rb, vo, ((j & 1) ? so1 : so0) + (unsigned)(s * 512 * D_ * 2), 0);
          SCHED();
        }
        __builtin_amdgcn_s_setprio(3);
      };
      const cptr_t sp = (cptr_t)(p.shift_k + quad * 16);
      unsigned spv[8];
#pragma unroll
      for (int i = 0; i < 8; ++i) spv[i] = sp[i];
      STAMPF(29);
      __builtin_amdgcn_s_sleep(3);
      gl(0, 0, 4);
      {
        const int row = 512 * (l >> 2) + 128 * pw - 3 + (l & 3);
        kh = __builtin_amdgcn_raw_buffer_load_b64(rk, (l >= 12 || (l & 3) == 3 || row < 0) ? 0x80000000u : (unsigned)(row * D_ * 2 + quad * 8), 0, 0);
      }
      TAG_READY();
      f32x2 wt[2][4];
#pragma unroll
      for (int d = 0; d < 2; ++d) {
        wt[d][0] = (f32x2){bflo(spv[4 * d]), bflo(spv[4 * d + 2])}; wt[d][1] = (f32x2){bfhi(spv[4 * d]), bfhi(spv[4 * d + 2])};
        wt[d][2] = (f32x2){bflo(spv[4 * d + 1]), bflo(spv[4 * d + 3])}; wt[d][3] = (f32x2){bfhi(spv[4 * d + 1]), bfhi(spv[4 * d + 3])};
        asm volatile("" : "+s"(wt[d][0]), "+s"(wt[d][1]), "+s"(wt[d][2]), "+s"(wt[d][3]));
      }
      SCHED();
      {
        const int j = idx * 4 + pw;
        const unsigned o = (unsigned)((512 + 64 * (j >> 2) + l) * (D_ * 2) + (((160 * xcd) >> 7) + ((j >> 1) & 1)) * 128);
        __builtin_amdgcn_raw_ptr_buffer_load_lds((j & 1) ? ra : rb, (LAS void*)(lds + 98304), 4, j < 64 ? o : 0x80000000u, 0, 0, 0);
      }
      SCHED();
      gl(1, 0, 4);
      STAMP(21);
      char* up = lds + L_U + 512 + 256 * pw + (l >> 2) * 16 + (3 - (l & 3)) * 4;
      auto kv = [&](auto S__, auto N__) {
        constexpr int s = decltype(S__)::value;
        u32x2* x = g[s];
        SCHED();
        asm volatile("s_waitcnt vmcnt(%5)" : "+v"(x[0]), "+v"(x[1]), "+v"(x[2]), "+v"(x[3]), "+v"(kh) : "n"(decltype(N__)::value));
        if (s == 2) STAMP(27);
        if (idx & 1) { asm volatile(""); const u32x2 t = x[0], u = x[2]; x[0] = x[1]; x[1] = t; x[2] = x[3]; x[3] = u; }
        asm volatile("" : "+v"(x[0]), "+v"(x[1]), "+v"(x[2]), "+v"(x[3]));
        if (idx & 2) { asm volatile(""); const u32x2 t = x[0], u = x[1]; x[0] = x[2]; x[1] = x[3]; x[2] = t; x[3] = u; }
        asm volatile("" : "+v"(x[0]), "+v"(x[1]), "+v"(x[2]), "+v"(x[3]));
        f32x2 kf[2][5], o[2][2];
#pragma unroll
        for (int d = 0; d < 2; ++d) {
          unsigned kk[5];
          kk[3] = x[0][d]; kk[4] = x[1][d];
          kk[2] = __builtin_amdgcn_update_dpp((unsigned)__builtin_amdgcn_readlane(kh[d], 4 * s + 2), kk[4], 0x138, 0xf, 0xf, false);
          kk[1] = __builtin_amdgcn_update_dpp((unsigned)__builtin_amdgcn_readlane(kh[d], 4 * s + 1), kk[3], 0x138, 0xf, 0xf, false);
          kk[0] = __builtin_amdgcn_update_dpp((unsigned)__builtin_amdgcn_readlane(kh[d], 4 * s), kk[2], 0x138, 0xf, 0xf, false);
#pragma unroll
          for (int j = 0; j < 5; ++j) kf[d][j] = (f32x2){bflo(kk[j]), bfhi(kk[j])};
        }
#pragma unroll
        for (int t = 0; t < 5; ++t) {
#pragma unroll
          for (int i = 0; i < 4; ++i) {
            const int d = i >> 1, r = i & 1;
            if (t == 4) o[d][r] *= (f32x2){bflo(x[2 + r][d]), bfhi(x[2 + r][d])};
            else if (t == 0) o[d][r] = kf[d][r + 3] * wt[d][0];
            else o[d][r] = __builtin_elementwise_fma(kf[d][r + 3 - t], wt[d][t], o[d][r]);
          }
          asm volatile("" : "+v"(o[0][0]), "+v"(o[0][1]), "+v"(o[1][0]), "+v"(o[1][1]));
        }
#pragma unroll
        for (int i = 0; i < 4; ++i) *(unsigned*)(up + i * U_CH + s * 1024) = pack2(o[i >> 1][1][i & 1], o[i >> 1][0][i & 1]);
        lf_set(4 * s + pw);
        SCHED();
      };
      const char* yr = lds + L_U + (n16 >> 2) * U_CH + (128 * pw + 32 * h4 + 8 * (n16 & 1) + 4 * ((n16 >> 1) & 1)) * 2;
      const unsigned yo = (unsigned)(8 * pw + (l >> 3)) * 20480u + (unsigned)quad * 128u + (unsigned)(l & 7) * 16u;
      const unsigned so = WS_ST1 + (unsigned)xcd * 16384u + (unsigned)pw * 1024u + l16;
      const unsigned mo = WS_MODQ + (unsigned)((n16 < 12 ? n16 >> 2 : 0) * D_ + quad * 4 + (l & 3)) * 8u + (unsigned)h4 * 40960u;
      u32x4 sv[4], qv[4];
      u32x2 mv[2];
      auto ld_e = [&]() {
#pragma unroll
        for (int s = 0; s < 4; ++s) sv[s] = ld16(rs, so + (unsigned)s * 4096u);
        SCHED();
      };
      auto s_ok = [&]() {
        unsigned d = 0u;
#pragma unroll
        for (int s = 0; s < 4; ++s) d |= (sv[s][1] ^ tag) | (sv[s][3] ^ tag);
        return __all(d == 0u);
      };
      kv(IC<0>(), IC<5>());
      STAMP(23);
      gl(2, 0, 4);
      kv(IC<1>(), IC<4>());
      STAMPF(9);
      u32x4 k3[2];
      const unsigned k3o = WS_KV3 + (unsigned)(16 * pw + (l >> 2)) * 20480u + (unsigned)(((l >> 1) & 1) * 10240 + (2 * quad + (l & 1)) * 16);
      hold(K_T);
      k3[0] = ld16(rs, k3o); k3[1] = ld16(rs, k3o + 5120u);
      SCHED();
      ld_e();
      kv(IC<2>(), IC<6>());
      STAMPF(11);
      auto k_ok = [&]() { return __all(((k3[0][1] ^ tag) | (k3[0][3] ^ tag) | (k3[1][1] ^ tag) | (k3[1][3] ^ tag)) == 0u); };
      asm volatile("" : "+v"(k3[0]), "+v"(k3[1]));
      if (!k_ok()) {
        _Pragma("unroll 1") for (unsigned spins = 0; spins < SPIN_CAP; ++spins) {
          __builtin_amdgcn_s_sleep(4);
          k3[0] = ld16(rs, k3o); k3[1] = ld16(rs, k3o + 5120u);
          if (k_ok()) break;
        }
      }
      *(u32x4*)(lds + L_U + (2 * (l & 1) + ((l >> 1) & 1)) * U_CH + 512 + (1536 + 128 * pw + 8 * (l >> 2)) * 2) = (u32x4){k3[0][0], k3[0][2], k3[1][0], k3[1][2]};
      lf_set(12 + pw);
      __builtin_amdgcn_s_setprio(3);
      STAMPF(18);
      asm volatile("" : "+v"(sv[0]), "+v"(sv[1]), "+v"(sv[2]), "+v"(sv[3]));
      if (!s_ok()) {
        _Pragma("unroll 1") for (unsigned spins = 0; spins < SPIN_CAP; ++spins) {
          __builtin_amdgcn_s_sleep(4);
          ld_e();
          if (s_ok()) break;
        }
      }
      asm volatile("" ::: "memory");
      SCHED();
      STAMPF(17);
#pragma unroll
      for (int s = 0; s < 4; ++s) qv[s] = ld16p(rs, WS_QB + yo + (unsigned)s * (32u * 20480u));
      hold(M_T);
      mv[0] = ld8(rs, mo); mv[1] = ld8(rs, mo + 20480u);
      SCHED();
      STAMP(2);
      u32x2 yv[4][2];
      auto ld_y = [&](int s) {
#pragma unroll
        for (int e = 0; e < 2; ++e) yv[s][e] = trd(yr + 1024 * s + 32 * e);
      };
      lf_wait(20, 4);
      ld_y(0); ld_y(1);
      SCHED();
      asm volatile("" : "+v"(mv[0]), "+v"(mv[1]), "+v"(qv[0]), "+v"(qv[1]), "+v"(qv[2]), "+v"(qv[3]));
      if (!__all(((mv[0][0] ^ tag) | (mv[1][0] ^ tag)) == 0u)) {
        _Pragma("unroll 1") for (unsigned spins = 0; spins < SPIN_CAP; ++spins) {
          __builtin_amdgcn_s_sleep(4);
          mv[0] = ld8(rs, mo); mv[1] = ld8(rs, mo + 20480u);
          if (__all(((mv[0][0] ^ tag) | (mv[1][0] ^ tag)) == 0u)) break;
        }
      }
      STAMPF(13);
      f32x4 al, be;
      {
        const float mgf = rbf(sum_rows(__uint_as_float(mv[0][1]) + __uint_as_float(mv[1][1])));
        auto rl = [&](int i) { return __uint_as_float(__builtin_amdgcn_readlane(__float_as_uint(mgf), i)); };
        const f32x4 sa = {rl(0), rl(1), rl(2), rl(3)}, ca = {rl(4), rl(5), rl(6), rl(7)}, ga = {rl(8), rl(9), rl(10), rl(11)};
        al = rbf4(ca + 1.0f) * ga;
        be = sa * ga;
      }
      f32x4 qf[2][2], P[2][2];
      auto xpre = [&](int s) {
#pragma unroll
        for (int e = 0; e < 2; ++e) {
          const float r1 = __builtin_amdgcn_rcpf(bfhi(sv[s][2 * e])), m1 = -bflo(sv[s][2 * e]) * r1;
          const f32x4 qq = {bflo(qv[s][2 * e]), bfhi(qv[s][2 * e]), bflo(qv[s][2 * e + 1]), bfhi(qv[s][2 * e + 1])};
          qf[s & 1][e] = qq;
          P[s & 1][e] = __builtin_elementwise_fma(__builtin_elementwise_fma(qq, (f32x4){r1, r1, r1, r1}, (f32x4){m1, m1, m1, m1}), al, be);
        }
        asm volatile("" : "+v"(qf[s & 1][0]), "+v"(qf[s & 1][1]), "+v"(P[s & 1][0]), "+v"(P[s & 1][1]));
      };
      auto xfin = [&](int s) {
        u32x4 o;
#pragma unroll
        for (int e = 0; e < 2; ++e) {
          const u32x2 y = yv[s][e];
          const f32x4 yf = {bflo(y[0]), bfhi(y[0]), bflo(y[1]), bfhi(y[1])};
          const f32x4 x = __builtin_elementwise_fma(P[s & 1][e], yf, qf[s & 1][e]);
          o[2 * e] = pack2(x[0], x[1]);
          o[2 * e + 1] = pack2(x[2], x[3]);
        }
        st16(rs, WS_X1B + yo + (unsigned)s * (32u * 20480u), o);
      };
      xpre(0); xfin(0);
      xpre(1); xfin(1);
      SCHED();
      xpre(2); xpre(3);
      lf_wait(24, 4);
      __builtin_amdgcn_s_setprio(3);
      ld_y(2); ld_y(3);
      xfin(2); xfin(3);
      STAMP(28);
      asm volatile("s_waitcnt vmcnt(2)" ::: "memory");
      xflag(l >> 5);
      asm volatile("s_waitcnt vmcnt(1)" ::: "memory");
      xflag(2 + (l >> 5));
      STAMP(5);
    } else {
      const int ch = w;
      char* U = lds + L_U + ch * U_CH;
      char* G = lds + ch * (4 * G_COPY);
      {
        const rsrc_t rf = __builtin_amdgcn_make_buffer_rsrc((void*)(p.ssm_k + (size_t)(quad * 4 + ch) * S_), 0, S_ * 2, 0x00020000);
        u32x4 fa[4];
        u32x2 fb[4];
        STAMPF(29);
        __builtin_amdgcn_s_setprio(3);
#pragma unroll
        for (int k = 0; k < 4; ++k) {
          fa[k] = __builtin_amdgcn_raw_buffer_load_b128(rf, l16 + (unsigned)k * 1024u, 0, NT);
        }
        __builtin_amdgcn_s_setprio(1);
        TAG_READY();
#pragma unroll
        for (int k = 0; k < 4; ++k) {
#pragma unroll
          for (int d = 0; d < 2; ++d)
            fb[k][d] = __builtin_amdgcn_update_dpp(k < 3 ? (unsigned)__builtin_amdgcn_readfirstlane(fa[k < 3 ? k + 1 : k][d]) : 0u, fa[k][d], 0x130, 0xf, 0xf, false);
          const unsigned x[6] = {fa[k][0], fa[k][1], fa[k][2], fa[k][3], fb[k][0], fb[k][1]};
          unsigned y[5];
#pragma unroll
          for (int d = 0; d < 5; ++d) y[d] = __builtin_amdgcn_alignbit(x[d + 1], x[d], 16);
          char* gp = G + 32 + k * 1024 + l16;
          *(u32x4*)(gp) = fa[k];
          *(u32x4*)(gp + G_COPY) = (u32x4){y[0], y[1], y[2], y[3]};
          *(u32x4*)(gp + 2 * G_COPY) = (u32x4){x[1], x[2], x[3], x[4]};
          *(u32x4*)(gp + 3 * G_COPY) = (u32x4){y[1], y[2], y[3], y[4]};
          if (k == 0) {
            const unsigned f0 = __builtin_amdgcn_readfirstlane(x[0]), f1 = __builtin_amdgcn_readfirstlane(x[1]);
            const unsigned long long f64 = (unsigned long long)f0 | ((unsigned long long)f1 << 32);
            const unsigned long long hi = l ? (f64 << (64 - 16 * (l & 3))) : 0ull;
            if (l < 4) {
              *(u32x4*)(G + l * G_COPY) = (u32x4){0u, 0u, 0u, 0u};
              *(u32x4*)(G + l * G_COPY + 16) = (u32x4){0u, 0u, (unsigned)hi, (unsigned)(hi >> 32)};
            }
            if (l >= 32) *(u32x4*)(U + (l - 32) * 16) = (u32x4){0u, 0u, 0u, 0u};
          }
        }
        asm volatile("s_waitcnt lgkmcnt(0)" ::: "memory");
        STAMP(1);
      }
      const int a = n16, h = h4, jj = h >> 1, e2 = h & 1;
      const char* ap = G + ((a + 1) & 3) * G_COPY + 2 * (16 * jj + a - 8 * e2 + 9 - ((a + 1) & 3));
      const char* bp = U + 512 + 32 * (a - jj) + 16 * e2;
      auto lda = [&](u32x4* A, int r, int p0, int np) {
#pragma unroll
        for (int pp = 0; pp < np; ++pp) {
          u32x2 lo = *(const volatile LAS u32x2*)(ap + 512 * (p0 + pp) + 64 * r);
          u32x2 hi = *(const volatile LAS u32x2*)(ap + 512 * (p0 + pp) + 64 * r + 8);
          A[pp] = (u32x4){lo[0], lo[1], hi[0], hi[1]};
        }
      };
      u32x4 AD;
      {
        const unsigned dDr = (*(cptr_t)(p.ssm_D + quad * 4 + (ch & 2)) >> (16 * (ch & 1))) & 0xffffu;
        const unsigned dv = h == (a >> 3) ? ((a & 1) ? dDr : dDr << 16) : 0u;
        const int dw = (7 - (a & 7)) >> 1;
#pragma unroll
        for (int d = 0; d < 4; ++d) AD[d] = dw == d ? dv : 0u;
      }
      u32x4 A0[8][4];
      f32x4 acc[8];
#pragma unroll
      for (int n = 0; n < 8; ++n) acc[n] = (f32x4){0.f, 0.f, 0.f, 0.f};
#pragma unroll
      for (int r = 0; r < 8; ++r) lda(A0[r], r, 0, 4);
      auto yput = [&](int s) {
#pragma unroll
        for (int e = 0; e < 2; ++e)
          *(u32x2*)(U + 1024 * s + 512 * e + (16 * a + 4 * h) * 2) = (u32x2){pack2(acc[2 * s + e][0], acc[2 * s + e][1]), pack2(acc[2 * s + e][2], acc[2 * s + e][3])};
        lf_set(16 + 4 * s + ch);
      };
      auto stage = [&](auto S__) {
        constexpr int s = decltype(S__)::value, NH = s < 2 ? 4 - 2 * s : 0, NP = 8 - 2 * s, NB = s < 2 ? 2 : 8;
        u32x4 A1[3][NH > 0 ? NH : 1], B[NB][2];
        auto ldb = [&](int r) {
#pragma unroll
          for (int e = 0; e < 2; ++e) B[r % NB][e] = *(const u32x4*)(bp + 512 * (2 * s + e) - 64 * r);
        };
        if (NH) { lda(A1[0], 0, 4, NH); lda(A1[1], 1, 4, NH); }
        SCHED();
        if (s > 0) yput(s - 1);
        lf_wait(4 * s, 4);
        if (s == 0) STAMP(3);
#pragma unroll
        for (int r = 0; r < NB; ++r) ldb(r);
        SCHED();
#pragma unroll
        for (int e = 0; e < 2; ++e) acc[2 * s + e] = MFMA16(AD, B[0][e], acc[2 * s + e]);
#pragma unroll
        for (int r = 0; r < 8; ++r) {
          if (NH && r + 2 < 8) lda(A1[(r + 2) % 3], r + 2, 4, NH);
          SCHED();
#pragma unroll
          for (int e = 0; e < 2; ++e)
#pragma unroll
            for (int pp = 0; pp < NP - e; ++pp) acc[2 * s + e + pp] = MFMA16(pp < 4 ? A0[r][pp] : A1[r % 3][pp < 4 ? 0 : pp - 4], B[r % NB][e], acc[2 * s + e + pp]);
          SCHED();
          if (r + NB < 8) ldb(r + NB);
        }
      };
      stage(IC<0>());
      STAMP(4);
      stage(IC<1>());
      STAMPF(9);
      const unsigned fo[1] = {fwi + (unsigned)((8 + c) * 20 + 5 * w + (l < 5 ? l : 0)) * 128u};
      unsigned fv = ~tag;
      {
        u32x4 B[4][4];
        auto ldb = [&](int r) {
#pragma unroll
          for (int m = 0; m < 4; ++m) B[r & 3][m] = *(const u32x4*)(bp + 512 * (4 + m) - 64 * r);
        };
        yput(1);
        if (w == 3) wo_ld();
        __builtin_amdgcn_s_setprio(3);
        lf_wait(8, 8);
        STAMP(22);
#pragma unroll
        for (int r = 0; r < 4; ++r) ldb(r);
        SCHED();
#pragma unroll
        for (int m = 0; m < 4; ++m) acc[4 + m] = MFMA16(AD, B[0][m], acc[4 + m]);
#pragma unroll
        for (int r = 0; r < 8; ++r) {
#pragma unroll
          for (int m = 0; m < 4; ++m)
#pragma unroll
            for (int pp = 0; pp < 4 - m; ++pp) acc[4 + m + pp] = MFMA16(A0[r][pp], B[r & 3][m], acc[4 + m + pp]);
          SCHED();
          if (r + 4 < 8) ldb(r + 4);
          if (r == 1) fv = ld4(rs, fo[0]);
          SCHED();
        }
#pragma unroll
        for (int e = 0; e < 4; ++e)
          *(u32x2*)(U + 2048 + 512 * e + (16 * a + 4 * h) * 2) = (u32x2){pack2(acc[4 + e][0], acc[4 + e][1]), pack2(acc[4 + e][2], acc[4 + e][3])};
        lf_set(24 + ch);
      }
      STAMP(28);
      __builtin_amdgcn_s_setprio(0);
      if (!__all(fv == tag)) wait_lines<1, 4>(rs, fo, tag);
      lf_wait(20, 4);
    }
    __builtin_amdgcn_s_setprio(0);
  } else {
    const int at = idx - 20;
    char* const scr = lds + WBLK + 14848;
    auto gv_blk = [&](int j) { return j < 160 ? 30 + (j >> 3) : j < 400 ? (j >> 3) - 20 : j >> 3; };
    auto gv_fin = [&](const u32x4* pre, int j) {
      float acc[8];
      gemv_fin(acc, pre, 10, scr);
      gemv_put(acc, j < 160 ? WS_MODP + (unsigned)((j & 7) * 1280 + (j >> 3) * 64) * 8u : WS_MODQ + (unsigned)((j & 7) * 2560 + ((j >> 3) - 20) * 64) * 8u);
    };
    const int ja = xcd * 12 + at, ai = xcd * 8 + at, bi = xcd * 4 + at - 8, g = w >> 2, wj = w & 3;
    const int rj = w == 0 ? ja : w == 1 ? (at >= 4 ? 92 + ai : -1) : w == 4 ? 160 + ja : w == 5 && at < 8 ? 256 + ai : -1;
    char* const rst = lds + (g ? WBLK : 65536) + wj * 7424;
    u32x4 pre[11];
    if (at >= 8 || g == 0) {
      const int T = at >= 8 ? 64 + 2 * bi + g : ai;
      unsigned* mds = (unsigned*)(lds + 2048) + 16 * g;
      char* qt = lds + L_RT + g * (16 * 1312);
      u32x4 ld[5];
      STAMPF(29);
      const bool e1 = at >= 8 && w == 3;
      if (e1) gemv_load(pre, gv_blk(128 + bi), bi & 7, 10);
      else __builtin_amdgcn_s_sleep(8);
#pragma unroll
      for (int j = 0; j < 5; ++j)
        ld[j] = __builtin_amdgcn_raw_buffer_load_b128(rq, (unsigned)(h4 * D_ * 2 + n16 * 16 + j * 256), (unsigned)((32 * (T >> 1) + 8 * wj + 4 * (T & 1)) * D_ * 2), NT);
      TAG_READY();
      {
        const bf2_t ONE2 = {(__bf16)1.0f, (__bf16)1.0f};
        char* dst = qt + (4 * wj + h4) * 1312 + n16 * 16;
        float sa = 0.f, va = 0.f;
#pragma unroll
        for (int j = 0; j < 5; ++j) {
          *(u32x4*)(dst + j * 256) = ld[j];
#pragma unroll
          for (int d = 0; d < 4; ++d) {
            const unsigned a0 = ld[j][d];
            const bf2_t xa = __builtin_bit_cast(bf2_t, a0);
            sa = __builtin_amdgcn_fdot2_f32_bf16(xa, ONE2, sa, false); va = __builtin_amdgcn_fdot2_f32_bf16(xa, xa, va, false);
          }
        }
        lf_set(8 + w);
        const float ma = sum16(sa) * (1.0f / D_);
        va = __builtin_fmaxf(sum16(va) * (1.0f / D_) - ma * ma, 0.f);
        if (n16 == 0) mds[4 * wj + h4] = pack2(ma, __builtin_amdgcn_sqrtf(rbf(rbf(va) + 1e-5f)));
      }
      if (e1) gv_fin(pre, 128 + bi);
      lf_wait(8 + 4 * g, 4);
      STAMP(23);
      {
        const char* src = qt + (l & 7) * 1312 + (40 * wj + (l >> 3)) * 8;
#pragma unroll
        for (int j = 0; j < 5; ++j) {
          const u32x2 x = *(const u32x2*)(src + j * 64), y = *(const u32x2*)(src + j * 64 + 8 * 1312);
          st16(rs, WS_QB + (unsigned)T * 20480u + (unsigned)(5 * wj + j) * 1024u + l16, (u32x4){x[0], x[1], y[0], y[1]});
        }
      }
      SCHED();
      const int g2 = at < 8 || w < 3 || w == 4 ? 400 : w == 3 ? 384 + bi : 256 + wj * 32 + bi;
      if (g2 < 400) {
        gemv_load(pre, gv_blk(g2), g2 & 7, 10);
        asm volatile("s_waitcnt vmcnt(10)" ::: "memory");
      } else if (w < 2 && rj >= 0) {
        rt_ld(rj, rst);
        asm volatile("s_waitcnt vmcnt(7)" ::: "memory");
      } else {
        DRAIN();
      }
      lf_set(16 + w);
      if (wj == 2) {
        lf_wait(16 + 4 * g, 4);
        st16(rs, WS_ST1 + (unsigned)(l >> 3) * 16384u + (unsigned)T * 128u + (unsigned)(l & 7) * 16u, (u32x4){mds[l & 7], tag, mds[8 + (l & 7)], tag});
        STAMP(22);
      }
      if (w >= 4 && w < 7) lf_set(w);
      if (g2 < 400) gv_fin(pre, g2);
      STAMP(25);
    } else {
      constexpr int KB = L_RT + 16 * 1312;
      const int g1 = w < 6 ? 2 * ai + wj : 32 + wj * 64 + ai;
      if (w >= 6) {
        const rsrc_t rk = __builtin_amdgcn_make_buffer_rsrc((void*)(w == 6 ? p.k : p.v), 0, S_ * D_ * 2, 0x00020000);
        const unsigned ko = (unsigned)((1536 + 8 * ai) * (D_ * 2) - (w == 6 ? 4096 : 0));
        char* dst = lds + KB + (w == 6 ? 0 : 14336);
        __builtin_amdgcn_s_sleep(3);
#pragma unroll
        for (int n = 0; n < 10; ++n) __builtin_amdgcn_raw_ptr_buffer_load_lds(rk, (LAS void*)(dst + n * 1024), 16, l16, ko + (unsigned)n * 1024u, 0, NT);
        if (w == 6) {
#pragma unroll
          for (int n = 10; n < 14; ++n) __builtin_amdgcn_raw_ptr_buffer_load_lds(rk, (LAS void*)(dst + n * 1024), 16, l16, ko + (unsigned)n * 1024u, 0, NT);
        }
      }
      STAMPF(29);
      SCHED();
      u32x4 swt[2];
      swt[0] = *(const u32x4*)(p.shift_k + (tid - 256) * 8);
      swt[1] = *(const u32x4*)(p.shift_k + (256 + l) * 8);
      SCHED();
      if (w >= 6) __builtin_amdgcn_s_sleep(8);
      gemv_load(pre, gv_blk(g1), g1 & 7, 10);
      TAG_READY();
      asm volatile("s_waitcnt vmcnt(11)" : "+v"(swt[0]), "+v"(swt[1]) :: "memory");
      lf_set(8 + w);
      if (w < 6) gv_fin(pre, g1);
      STAMP(25);
      lf_wait(12, 4);
      STAMP(23);
#pragma unroll 1
      for (int n = 0; n < (w == 7 ? 2 : 1); ++n) {
        {
          const int pr = n ? 256 + l : tid - 256;
          const char* kp = lds + KB + 4096 + pr * 4;
          unsigned kk[11], vv[8];
#pragma unroll
          for (int i = 0; i < 11; ++i) kk[i] = *(const unsigned*)(kp + (i - 3) * 1280);
#pragma unroll
          for (int i = 0; i < 8; ++i) vv[i] = *(const unsigned*)(kp + 10240 + i * 1280);
          const u32x4 sw = n ? swt[1] : swt[0];
          const f32x2 wt[4] = {{bflo(sw[0]), bflo(sw[2])}, {bfhi(sw[0]), bfhi(sw[2])}, {bflo(sw[1]), bflo(sw[3])}, {bfhi(sw[1]), bfhi(sw[3])}};
          f32x2 o[8];
#pragma unroll
          for (int i = 0; i < 8; ++i) {
            f32x2 t = (f32x2){bflo(kk[i + 3]), bfhi(kk[i + 3])} * wt[0];
#pragma unroll
            for (int j = 1; j < 4; ++j) t = __builtin_elementwise_fma((f32x2){bflo(kk[i + 3 - j]), bfhi(kk[i + 3 - j])}, wt[j], t);
            o[i] = t * (f32x2){bflo(vv[i]), bfhi(vv[i])};
          }
#pragma unroll
          for (int e = 0; e < 4; ++e) {
            const int i = 7 - 4 * (e & 1);
            st16(rs, WS_KV3 + (unsigned)ai * 20480u + (unsigned)(e * 5120 + pr * 16),
                 (u32x4){pack2(o[i][e >> 1], o[i - 1][e >> 1]), tag, pack2(o[i - 2][e >> 1], o[i - 3][e >> 1]), tag});
          }
        }
      }
      STAMP(22);
      if (w < 7) lf_set(w);
      if (w >= 6) gv_fin(pre, g1);
      STAMPF(30);
    }
    if (w == 6 && ja < 80) {
      hold(AUX_T);
      gemv_load(pre, gv_blk(400 + ja), ja & 7, 10);
      gv_fin(pre, 400 + ja);
    }
    if (rj >= 0) {
      if (w >= 2) { hold(RT2_T); rt_ld(rj, rst); }
      rt_job(rj, rst);
    }
    STAMP(26);
    lf_set(w);
    if (w == 3) { hold(WA_T); wo_ld(); }
    STAMP(3);
    if (w < 4) {
      {
        const unsigned fo[1] = {l < 5 ? fwi + (unsigned)((8 + c) * 20 + 5 * w + l) * 128u : ~0u};
        hold(W_T);
        wait_lines<1, 8>(rs, fo, tag);
      }
      lf_wait(0, 8);
      STAMP(4);
    }
    if ((at == 8 || at == 9) && w == 5 && xcd * 2 + at - 8 < 10) {
      const unsigned o = WS_MODQ + (unsigned)(h4 * 5120 + 1920 + (xcd * 2 + at - 8) * 64 + n16 * 4) * 8u;
      u32x4 v[4];
      hold(CS_T);
      _Pragma("unroll 1") for (unsigned spins = 0; spins < SPIN_CAP; ++spins) {
        unsigned dd = 0u;
#pragma unroll
        for (int k = 0; k < 4; ++k) v[k] = ld16(rs, o + (unsigned)((k >> 1) * 20480 + (k & 1) * 16));
#pragma unroll
        for (int k = 0; k < 4; ++k) dd |= (v[k][0] ^ tag) | (v[k][2] ^ tag);
        if (__all(dd == 0u)) break;
        __builtin_amdgcn_s_sleep(16);
      }
      float t[4];
#pragma unroll
      for (int k = 0; k < 4; ++k) t[k] = sum_rows(__uint_as_float(v[k >> 1][1 + 2 * (k & 1)]) + __uint_as_float(v[2 + (k >> 1)][1 + 2 * (k & 1)]));
#pragma unroll
      for (int e = 0; e < 2; ++e)
        st16(rs, WS_MOD + (unsigned)(4 * e + h4) * 2560u + (unsigned)((xcd * 2 + at - 8) * 16 + n16) * 16u, (u32x4){tag, pack2(t[0], t[1]), tag, pack2(t[2], t[3])});
    }
    if (w == 4 || (w == 5 && at >= 10)) {
      const int cj = w == 4 ? ja : 86 + xcd * 2 + at, cb = cj / 7, j = cj % 7;
      const unsigned o = WS_CSP + (unsigned)((cb * 20 + 5 * h4) * 2 + (j >> 2)) * 1024u + (unsigned)((j & 3) * 16 + n16) * 16u;
      u32x4 v[5];
      hold(CS_T);
      __builtin_amdgcn_s_setprio(3);
      _Pragma("unroll 1") for (unsigned spins = 0; spins < SPIN_CAP; ++spins) {
        unsigned dd = 0u;
#pragma unroll
        for (int k = 0; k < 5; ++k) v[k] = ld16(rs, o + (unsigned)k * 2048u);
#pragma unroll
        for (int k = 0; k < 5; ++k) dd |= (v[k][0] ^ tag) | (v[k][2] ^ tag);
        if (__all(dd == 0u)) break;
        __builtin_amdgcn_s_sleep(8);
      }
      float t0 = 0.f, t1 = 0.f;
#pragma unroll
      for (int k = 0; k < 5; ++k) { t0 += __uint_as_float(v[k][1]); t1 += __uint_as_float(v[k][3]); }
      t0 = sum_rows(t0); t1 = sum_rows(t1);
      if (l < 16) st16(rs, WS_CS + (unsigned)(cb * 2048 + j * 256) + l16, (u32x4){tag, __float_as_uint(t0), tag, __float_as_uint(t1)});
      __builtin_amdgcn_s_setprio(0);
      STAMP(24);
    }
  }

  STAMPF(16);
  u32x4 wox[5];
  DRAIN();
  if (w == 3) {
    const char* bp = wo_st + (4 * h4 + (n16 >> 2)) * 160 + (n16 & 3) * 8;
#pragma unroll
    for (int dj = 0; dj < 5; ++dj) wox[dj] = trfrag(bp + dj * 32, 160);
    asm volatile("s_waitcnt lgkmcnt(0)" : "+v"(wox[0]), "+v"(wox[1]), "+v"(wox[2]), "+v"(wox[3]), "+v"(wox[4]) :: "memory");
  }
  auto wo_put = [&]() {
    if (w == 3) {
      const unsigned base = WS_WOUTF + (unsigned)c * WBLK + (unsigned)((R >> 2) * 17920 + (R & 3) * 5120);
      if ((R & 3) < 3) {
#pragma unroll
        for (int dj = 0; dj < 5; ++dj) st16(rs, base + (unsigned)dj * 1024u + l16, wox[dj]);
      } else {
#pragma unroll
        for (int dj = 0; dj < 5; ++dj) st8(rs, base + (unsigned)dj * 512u + (unsigned)l * 8u, (u32x2){wox[dj][0], wox[dj][1]});
      }
    }
  };
  if (w < 4) load_b2();
  u32x4 k_c;
  unsigned kc_o;
  {
    const int i = tid < 224 ? tid : 0;
    const int col = i >= 112 ? (8 + c) * 128 + i - 112 : c * 128 + i;
    kc_o = w < 4 ? WS_CS + (unsigned)col * 16u : WS_MOD + (unsigned)xcd * 2560u + (unsigned)(20 * c + (tid < 276 ? tid - 256 : 0)) * 16u;
  }
  auto consts_put = [&]() {
    if (!__all(k_c[0] == tag && k_c[2] == tag)) {
      _Pragma("unroll 1") for (unsigned spins = 0; spins < SPIN_CAP; ++spins) {
        __builtin_amdgcn_s_sleep(8);
        k_c = ld16(rs, kc_o);
        if (__all(k_c[0] == tag && k_c[2] == tag)) break;
      }
    }
    if (tid < 224) *(u32x2*)(lds + L_CONST + tid * 8) = (u32x2){k_c[1], k_c[3]};
    if (tid >= 256 && tid < 276) *(u32x2*)(lds + L_MISC + (tid - 256) * 8) = (u32x2){k_c[1], k_c[3]};
  };
  const int rt1 = w & 3, kh1 = w >> 2;
  unsigned bo0 = (unsigned)(kh1 * 71680) + l16, bo1 = bo0 + 35840u;
  asm volatile("" : "+v"(bo0), "+v"(bo1));
#define BFRAG(q, j) (*(const u32x4*)(lds + ((q) < 5 ? bo0 : bo1) + ((q) % 5) * 7168 + (j) * 1024))
  u32x4 h2[10];
  f32x4 acc[7];
  float mu_n, rs_n;
  {
    if (w == 6) {
      const unsigned fo[1] = {l < 20 ? fwi + (unsigned)(c * 20 + l) * 128u : ~0u};
      hold(CS_T);
      wait_lines<1, 8>(rs, fo, tag);
    }
    if (w == 7) {
      const unsigned fb = WS_FX + (unsigned)(((R >> 1) * 4 + (R & 1) * 2 + (c >> 2)) * 160 + l) * 128u;
      const unsigned fo[3] = {fb, fb + 8192u, l < 32 ? fb + 16384u : ~0u};
      if (!is_conv) hold(Y_T);
      wait_lines<3, 2>(rs, fo, tag);
      if (l == 0) lf[33] = tag;
    }
    lf_wait(33, 1);
    STAMP(7);
    const unsigned xo = (unsigned)(R * 4 + rt1) * 20480u + (unsigned)(10 * kh1) * 1024u +
                        (unsigned)((4 * ((l >> 3) & 1) + h4) * 128 + (l & 7) * 16);
    SCHED();
#pragma unroll
    for (int q = 0; q < 3; ++q) h2[q] = ld16p(rs, WS_X1B + xo + (unsigned)q * 1024u);
    SCHED();
    __builtin_amdgcn_s_waitcnt(0x0F73);
    BAR();
    STAMP(8);
#pragma unroll
    for (int j = 0; j < 7; ++j) acc[j] = (f32x4){0.f, 0.f, 0.f, 0.f};
    f32x4 accg = {0.f, 0.f, 0.f, 0.f};
    u32x4 bf[7];
#pragma unroll
    for (int j = 0; j < 7; ++j) bf[j] = BFRAG(0, j);
    SCHED();
#pragma unroll
    for (int q = 3; q < 6; ++q) h2[q] = ld16p(rs, WS_X1B + xo + (unsigned)q * 1024u);
    SCHED();
#pragma unroll
    for (int q = 0; q < 10; ++q) {
      {
#pragma unroll
        for (int e = 0; e < 6; ++e) {
          const int d = e & 1;
          if ((e >> 1) == 1) h2[q][2 + d] ^= (unsigned)__builtin_amdgcn_update_dpp(0, (int)h2[q][d], 0x128, 0xf, 0x3, false);
          else h2[q][d] ^= (unsigned)__builtin_amdgcn_update_dpp(0, (int)h2[q][2 + d], 0x128, 0xf, 0xC, false);
        }
      }
      if (q == 5) { __builtin_amdgcn_s_barrier(); asm volatile("" ::: "memory"); }
      SCHED();
      accg = MFMA16(h2[q], h2[q], accg);
      SCHED();
#pragma unroll
      for (int j = 0; j < 7; ++j) {
        acc[j] = MFMA16(h2[q], bf[j], acc[j]);
        if (q == 9 && j == 1 && h4 == (n16 >> 2)) *(f32x4*)(lds + L_PST + 512 + (w * 16 + n16) * 16) = accg;
        if (q < 9) bf[j] = BFRAG(q + 1, j);
        if (q >= 2 && j == 2) LDW(q - 2);
        if ((j == 0 || j == 4) && 6 + 2 * q + (j >> 2) < 10) h2[6 + 2 * q + (j >> 2)] = ld16p(rs, WS_X1B + xo + (unsigned)(6 + 2 * q + (j >> 2)) * 1024u);
        if (q == 4 && j == 0) k_c = ld16(rs, kc_o);
        if (q >= 5 && j == 5) {
          const int i = (5 + q) * 7 + (w < 7 ? w : 0);
          dma1k(rs, lds + i * 1024, l16, WS_WINF + (unsigned)c * WBLK + (unsigned)i * 1024u);
          if (q == 9) { const int i2 = 28 + (w < 7 ? w : 0); dma1k(rs, lds + i2 * 1024, l16, WS_WINF + (unsigned)c * WBLK + (unsigned)i2 * 1024u); }
        }
        SCHED();
      }
    }
    STAMP(31);
  }
  auto gelu_n = [&](f32x2* x, auto N_) {
    constexpr int N = decltype(N_)::value;
    const float c0 = -2.0f * 0.7978845608028654f * 1.4426950408889634f, c1 = c0 * 0.044715f;
    f32x2 k0 = {c0, c0}, k1 = {c1, c1}, k2 = {1.0f, 1.0f};
    asm volatile("" : "+v"(k0), "+v"(k1), "+v"(k2));
    f32x2 t[N];
#define GST(e) do { _Pragma("unroll") for (int i = 0; i < N; ++i) { t[i] = (e); asm volatile("" : "+v"(t[i])); } } while (0)
    GST(x[i] * x[i]);
    GST(__builtin_elementwise_fma(t[i], k1, k0));
    GST(t[i] * x[i]);
    GST(((f32x2){__builtin_amdgcn_exp2f(t[i][0]), __builtin_amdgcn_exp2f(t[i][1])}));
    GST(t[i] + k2);
    GST(((f32x2){__builtin_amdgcn_rcpf(t[i][0]), __builtin_amdgcn_rcpf(t[i][1])}));
#pragma unroll
    for (int i = 0; i < N; ++i) { x[i] *= t[i]; asm volatile("" : "+v"(x[i])); }
  };
  {
    f32x2* red = (f32x2*)lds;
    f32x2 mine[7];
    if (kh1) {
#pragma unroll
      for (int j = 0; j < 7; ++j) { red[(w * 7 + j) * 64 + l] = (f32x2){acc[j][0], acc[j][1]}; mine[j] = (f32x2){acc[j][2], acc[j][3]}; }
    } else {
#pragma unroll
      for (int j = 0; j < 7; ++j) { red[(w * 7 + j) * 64 + l] = (f32x2){acc[j][2], acc[j][3]}; mine[j] = (f32x2){acc[j][0], acc[j][1]}; }
    }
    f32x2 csb[7];
    unsigned cso = (unsigned)(L_CONST + (112 + n16) * 8);
    asm volatile("" : "+v"(cso));
    consts_put();
    unsigned pso[3], pqo[3], oto = (unsigned)(((w ^ 4) * 7 * 64 + l) * 8);
#pragma unroll
    for (int e = 0; e < 3; ++e) {
      const int row = rt1 * 16 + (e < 2 ? 4 * h4 + 2 * kh1 + e : n16);
      pso[e] = (unsigned)(L_PST + row * 4); pqo[e] = (unsigned)(L_PST + 512 + row * 16 + (row & 3) * 4);
    }
    asm volatile("" : "+v"(pso[0]), "+v"(pso[2]), "+v"(pqo[0]), "+v"(pqo[1]), "+v"(pqo[2]), "+v"(oto));
    pso[1] = pso[0] + 4u;
    if (n16 == 12) *(f32x4*)(lds + L_PST + (w * 16 + 4 * h4) * 4) = acc[6];
    if (!kh1) {
#pragma unroll
      for (int k = 8; k < 16; ++k) LDW(k);
    }
    BAR();
    if (kh1) {
#pragma unroll
      for (int k = 8; k < 16; ++k) LDW(k);
    }
    __builtin_amdgcn_s_setprio(3);
    STAMPT(9);
    f32x2 oth[7];
    f32x2 pa[3], pb[3];
#pragma unroll
    for (int e = 0; e < 3; ++e) {
      pa[e] = (f32x2){*(const float*)(lds + pso[e]), *(const float*)(lds + pqo[e])};
      pb[e] = (f32x2){*(const float*)(lds + pso[e] + 256), *(const float*)(lds + pqo[e] + 1024)};
    }
#pragma unroll
    for (int j = 0; j < 7; ++j) { oth[j] = *(const f32x2*)(lds + oto + j * 512); csb[j] = *(const f32x2*)(lds + cso + 128 * j); }
    SCHED();
    float mu2[2], rs2[2];
#pragma unroll
    for (int e = 0; e < 3; ++e) {
      const float mean = (pa[e][0] + pb[e][0]) * (1.0f / D_);
      const float var = __builtin_fmaxf((pa[e][1] + pb[e][1]) * (1.0f / D_) - mean * mean, 0.f);
      const float rstd = __builtin_amdgcn_rsqf(var + 1e-5f);
      if (e < 2) { mu2[e] = mean; rs2[e] = rstd; } else { mu_n = mean; rs_n = rstd; }
    }
    f32x2 zz[7];
    const f32x2 nmu = {-mu2[0], -mu2[1]}, rsv = {rs2[0], rs2[1]};
#pragma unroll
    for (int j = 0; j < 7; ++j)
      zz[j] = __builtin_elementwise_fma(__builtin_elementwise_fma(nmu, (f32x2){csb[j][0], csb[j][0]}, mine[j] + oth[j]), rsv, (f32x2){csb[j][1], csb[j][1]});
    gelu_n(zz, IC<7>());
    if (n16 >= 12) zz[6] = (f32x2){0.f, 0.f};
    f32x2 s1 = ((zz[0] + zz[1]) + (zz[2] + zz[3])) + ((zz[4] + zz[5]) + zz[6]);
    f32x2 s2 = zz[0] * zz[0], s3 = zz[1] * zz[1];
#pragma unroll
    for (int j = 2; j < 7; ++j) { if (j & 1) s3 = __builtin_elementwise_fma(zz[j], zz[j], s3); else s2 = __builtin_elementwise_fma(zz[j], zz[j], s2); }
    s2 += s3;
    asm("" : "+v"(s1), "+v"(s2));
    float a0 = s1[0] + dpp_f<0xB1>(s1[0]), a1 = s1[1] + dpp_f<0xB1>(s1[1]), b0 = s2[0] + dpp_f<0xB1>(s2[0]);
    a0 = (l & 1) ? a1 : a0;
    const float b1 = s2[1] + dpp_f<0xB1>(s2[1]);
    b0 = (l & 1) ? b1 : b0;
    a0 += dpp_f<0x4E>(a0); b0 += dpp_f<0x4E>(b0);
    a0 += dpp_f<0x124>(a0); b0 += dpp_f<0x124>(b0);
    a0 += dpp_f<0x128>(a0); b0 += dpp_f<0x128>(b0);
    {
      const int src = ((l >> 1) & 3) * 64 + (l & 1) * 4;
      const unsigned pa0 = __builtin_amdgcn_ds_bpermute(src, __float_as_uint(a0)), pb0 = __builtin_amdgcn_ds_bpermute(src, __float_as_uint(b0));
      if (l < 8) st16(rs, WS_PART + (unsigned)((R * 8 + c) * 8 + w) * 128u + l16, (u32x4){tag, pa0, tag, pb0});
    }
    SCHED();
    __builtin_amdgcn_s_setprio(0);
    STAMP(10);
#pragma unroll
    for (int k = 0; k < 10; ++k) {
      if (k < 9) {
        int x = k * 8 + w;
        x = x < 70 ? x : 69;
        const int i = x + (x < 35 ? 35 : 70);
        dma1k(rs, lds + i * 1024, l16, WS_WINF + (unsigned)c * WBLK + (unsigned)i * 1024u);
      }
    }
    wo_put();
#pragma unroll
    for (int j = 0; j < 7; ++j)
      *(unsigned*)(lds + L_Z + (((rt1 >> 1) * 7 + j) * 64 + l) * 16 + (4 * (rt1 & 1) + 2 * kh1) * 2) = pack2(zz[j][0], zz[j][1]);
    SCHED();
    u32x4 v[8];
    const unsigned po = WS_PART + (unsigned)(R * 8) * 1024u + l16;
#pragma unroll
    for (int g = 0; g < 4; ++g) WSP_TR(g);
    if (w == 2) {
      __builtin_amdgcn_s_setprio(3);
      unsigned need = 0xffu;
      if (need != 0u) {
        _Pragma("unroll 1") for (unsigned spins = 0; spins < SPIN_CAP; ++spins) {
#pragma unroll
          for (int k = 0; k < 8; ++k)
            if (need & (1u << k)) v[k] = ld16(rs, po + (unsigned)k * 1024u);
#pragma unroll
          for (int k = 0; k < 8; ++k)
            if (__all(v[k][0] == tag && v[k][2] == tag)) need &= ~(1u << k);
          if (need == 0u) break;
          __builtin_amdgcn_s_sleep(1);
        }
      }
      float t1 = 0.f, t2 = 0.f;
#pragma unroll
      for (int k = 0; k < 8; ++k) { t1 += __uint_as_float(v[k][1]); t2 += __uint_as_float(v[k][3]); }
      const float mean = t1 * (1.0f / FH);
      const float var = __builtin_fmaxf(t2 * (1.0f / FH) - mean * mean, 0.f);
      const float rstd = __builtin_amdgcn_rsqf(var + 1e-5f);
      const int sg = 16 * ((l >> 3) & 3) + 4 * ((l >> 1) & 3) + 2 * (l >> 5) + (l & 1);
      *(f32x2*)(lds + L_ST2 + sg * 8) = (f32x2){rstd, -mean * rstd};
    }
    STAMPT(11);
    DRAIN();
    BAR();
    __builtin_amdgcn_s_setprio(3);
    STAMP(12);
    u32x4 zf[2];
#pragma unroll
    for (int m = 0; m < 2; ++m) zf[m] = *(const u32x4*)(lds + L_Z + ((m * 7 + (w < 7 ? w : 0)) * 64 + l) * 16);
    if (w < 7) {
#pragma unroll
      for (int m = 0; m < 2; ++m) {
        const f32x4* sp = (const f32x4*)(lds + L_ST2 + (32 * m + 4 * h4) * 8);
        u32x4 o;
#pragma unroll
        for (int e = 0; e < 4; ++e) {
          f32x4 st = sp[(e & 1) + 8 * (e >> 1)];
          o[e] = pack2(__builtin_fmaf(bflo(zf[m][e]), st[0], st[1]), __builtin_fmaf(bfhi(zf[m][e]), st[2], st[3]));
        }
        st16(rs, WS_Z2F + (unsigned)((c * 64 + 4 * (R >> 1) + h4) * 7 + w) * 1024u + (unsigned)((2 * (R & 1) + m) * 256 + n16 * 16), o);
      }
    }
    SCHED();
    __builtin_amdgcn_s_setprio(0);
#pragma unroll
    for (int k = 0; k < 4; ++k)
      if (w < 7) dma1k(rs, lds + (k * 7 + w) * 1024, l16, WS_WINF + (unsigned)c * WBLK + (unsigned)(k * 7 + w) * 1024u);
    SCHED();
    STAMPT(13);
  }
  unsigned bs_raw;
  u32x2 xp[3];
  constexpr int NST = 5, STG = 28672;
#define SLOT(t) (((t) % NST) * STG)
  const unsigned sZ = WS_Z2F + (unsigned)(c * 64) * 7168u + (unsigned)w * 1024u;
  auto issue = [&](int t, int k) { dma1k(rs, lds + SLOT(t) + k * 7168 + w * 1024, l16, sZ + (unsigned)(16 * k + ((t + rot) & 15)) * 7168u); };
  {
    f32x4 acc[7];
#pragma unroll
    for (int j = 0; j < 7; ++j) acc[j] = (f32x4){0.f, 0.f, 0.f, 0.f};
    u32x4 bf[7];
#pragma unroll
    for (int j = 0; j < 7; ++j) bf[j] = BFRAG(4, j);
#pragma unroll
    for (int qi = 0; qi < 10; ++qi) {
      const int q = (qi + 4) % 10, qn = (qi + 5) % 10;
      if (qi == 5) {
        asm volatile("s_waitcnt vmcnt(8)" ::: "memory");
        __builtin_amdgcn_s_barrier();
        asm volatile("" ::: "memory");
        if (w == 7) st_flag4(rs, WS_FZ + (unsigned)(c * 32 + R) * 128u, tag, l);
        STAMP(6);
#pragma unroll
        for (int k = 24; k < 32; ++k) LDW(k);
        bs_raw = p.b_sp[R * 64 + tau(2 * rp + (kq & 1), n16)];
#pragma unroll
        for (int j = 0; j < 3; ++j) {
          const int dj = (w >> 2) ? (3 + (j < 2 ? j : 1)) : j;
          const int d0 = 80 * c + 16 * dj + 4 * h4;
          xp[j] = ld8p(rs, WS_X1B + (unsigned)(R * 4 + (w & 3)) * 20480u + (unsigned)(d0 >> 2) * 128u + (unsigned)psi(n16) * 8u);
        }
      }
      SCHED();
#pragma unroll
      for (int j = 0; j < 7; ++j) {
        acc[j] = MFMA16(bf[j], h2[q], acc[j]);
        if (qi < 9) bf[j] = BFRAG(qn, j);
        if (qi >= 1 && qi < 5 && (j == 1 || j == 4)) LDW(16 + 2 * (qi - 1) + (j >> 2));
        SCHED();
      }
    }
    STAMPT(29);
    f32x4* red = (f32x4*)(lds + 100352);
    f32x4 m4[4];
    if (kh1 == 0) {
#pragma unroll
      for (int j = 0; j < 3; ++j) red[(w * 4 + j) * 64 + l] = acc[4 + j];
#pragma unroll
      for (int j = 0; j < 4; ++j) m4[j] = acc[j];
    } else {
#pragma unroll
      for (int j = 0; j < 4; ++j) red[(w * 4 + j) * 64 + l] = acc[j];
#pragma unroll
      for (int j = 0; j < 4; ++j) m4[j] = acc[4 + (j < 3 ? j : 2)];
    }
#pragma unroll
    for (int g = 4; g < 6; ++g) WSP_TR(g);
    if (w == 3) {
      const unsigned fo[1] = {l < 32 ? fz + (unsigned)(c * 32 + l) * 128u : ~0u};
      wait_lines<1, 1>(rs, fo, tag);
    }
    DRAIN();
    BAR();
    STAMP(14);
    auto issue_q = [&](int t) {
#pragma unroll
      for (int j = 0; j < 7; ++j)
        dma1k(rs, lds + SLOT(t) + (w & 3) * 7168 + j * 1024, l16, WS_Z2F + (unsigned)(c * 64 + 16 * (w & 3) + ((t + rot) & 15)) * 7168u + (unsigned)j * 1024u);
    };
    if (w >= 4) { issue_q(0); issue_q(1); }
    SCHED();
    f32x4 o4[4], c01[4], c23[4];
#pragma unroll
    for (int j = 0; j < 4; ++j) {
      const int jj = (kh1 && j == 3) ? 2 : j, jt = 4 * kh1 + jj;
      o4[j] = red[((w ^ 4) * 4 + jj) * 64 + l];
      c01[j] = *(const f32x4*)(lds + L_CONST + (16 * jt + 4 * h4) * 8);
      c23[j] = *(const f32x4*)(lds + L_CONST + (16 * jt + 4 * h4 + 2) * 8);
    }
    SCHED();
    f32x2 z[8];
#pragma unroll
    for (int i = 0; i < 8; ++i) {
      const int j = i >> 1, r = 2 * (i & 1);
      const f32x4 cc = r ? c23[j] : c01[j];
      z[i] = __builtin_elementwise_fma(__builtin_elementwise_fma((f32x2){-mu_n, -mu_n}, (f32x2){cc[0], cc[2]}, (f32x2){o4[j][r], o4[j][r + 1]} + (f32x2){m4[j][r], m4[j][r + 1]}),
                                       (f32x2){rs_n, rs_n}, (f32x2){cc[1], cc[3]});
    }
    SCHED();
    gelu_n(z, IC<6>());
    if (kh1 == 0) gelu_n(z + 6, IC<2>());
#pragma unroll
    for (int j = 0; j < 4; ++j)
      if (kh1 == 0 || j < 3) *(u32x2*)(lds + L_Z + (((kh1 ? 4 + j : j) * 4 + rt1) * 64 + l) * 8) = (u32x2){pack2(z[2 * j][0], z[2 * j][1]), pack2(z[2 * j + 1][0], z[2 * j + 1][1])};
    SCHED();
    if (w < 4) issue_q(2);
    STAMPT(30);
  }

  f32x4 acc2[2][7];
  {
    SCHED();
#pragma unroll
    for (int g = 6; g < 8; ++g) WSP_TR(g);
#pragma unroll
    for (int i = 0; i < 2; ++i)
#pragma unroll
      for (int j = 0; j < 7; ++j) acc2[i][j] = (f32x4){0.f, 0.f, 0.f, 0.f};
    const char* rd = lds + kq * 7168 + l16;
#define VMW(n) asm volatile("s_waitcnt vmcnt(%0)" ::"n"((n) * 4) : "memory")
#define RBAR() do { __builtin_amdgcn_s_barrier(); asm volatile("" ::: "memory"); } while (0)
    SCHED();
    if (w >= 4) DRAIN();
    BAR();
    if (w < 7) {
#pragma unroll
      for (int k = 0; k < 4; ++k) issue(3, k);
    }
    STAMP(20);
    u32x4 fr[7];
#pragma unroll
    for (int j = 0; j < 7; ++j) fr[j] = *(const u32x4*)(rd + j * 1024);
    u32x4 wo[18];
#define LDO(k) wo[k] = ld16p(rs, WS_WOUTF + (unsigned)c * WBLK + (unsigned)((k) < 17 || w < 4 ? w + 8 * (k) : w) * 1024u + l16)
#pragma unroll
    for (int t = 0; t < 15; ++t) {
      SCHED();
      if (t < 13) VMW(2);
      else if (t == 13) asm volatile("s_waitcnt vmcnt(10)" ::: "memory");
      else asm volatile("s_waitcnt vmcnt(12)" ::: "memory");
      RBAR();
      SCHED();
#pragma unroll
      for (int j = 0; j < 7; ++j) {
        acc2[0][j] = MFMA16(fr[j], wsp[0][t], acc2[0][j]);
        acc2[1][j] = MFMA16(fr[j], wsp[1][t], acc2[1][j]);
        fr[j] = *(const u32x4*)(rd + SLOT(t + 1) + j * 1024);
        if (t + 4 < 16 && j < 4 && w < 7) issue(t + 4, j);
        if (t >= 12 && j < 6) LDO(6 * (t - 12) + j);
        SCHED();
      }
    }
    f32x4* red = (f32x4*)(lds + STG) + rp * (42 * 64) + l;
    f32x4 own[3];
    f32x2 own3;
    auto last = [&](auto KQ_) {
      constexpr int KQ = decltype(KQ_)::value;
      constexpr int ord[7] = {KQ < 2 ? 4 : 0, KQ < 2 ? 5 : 1, KQ < 2 ? 6 : 2, 3, KQ < 2 ? 0 : 4, KQ < 2 ? 1 : 5, KQ < 2 ? 2 : 6};
#pragma unroll
      for (int x = 0; x < 9; ++x) {
        if (x < 7) {
          const int j = ord[x];
          acc2[1 - (KQ & 1)][j] = MFMA16(fr[j], wsp[1 - (KQ & 1)][15], acc2[1 - (KQ & 1)][j]);
          acc2[KQ & 1][j] = MFMA16(fr[j], wsp[KQ & 1][15], acc2[KQ & 1][j]);
        }
        if (x >= 2) {
          const int j = ord[x - 2];
#pragma unroll
          for (int i = 0; i < 2; ++i) {
            const int ko = i + 2 * (j > 3);
            if (j != 3) {
              if (KQ != ko) red[((i * 7 + j) * 3 + (KQ > ko ? KQ - 1 : KQ)) * 64] = acc2[i][j];
            } else if ((KQ & 1) != i) {
              red[((i * 7 + 3) * 3 + (KQ >> 1)) * 64] = acc2[i][3];
            } else {
              ((f32x2*)&red[((i * 7 + 3) * 3 + 2) * 64])[1 - (KQ >> 1)] = (f32x2){acc2[i][3][2 - 2 * (KQ >> 1)], acc2[i][3][3 - 2 * (KQ >> 1)]};
            }
          }
        }
        SCHED();
      }
#pragma unroll
      for (int x = 0; x < 3; ++x) own[x] = acc2[KQ & 1][4 * (KQ >> 1) + x];
      own3 = (f32x2){acc2[KQ & 1][3][2 * (KQ >> 1)], acc2[KQ & 1][3][2 * (KQ >> 1) + 1]};
    };
    if (kq == 0) last(IC<0>());
    else if (kq == 1) last(IC<1>());
    else if (kq == 2) last(IC<2>());
    else last(IC<3>());
    STAMP(15);
    const int io = kq & 1, jb = kq >> 1;
    const int rto = 2 * rp + io;
    u32x2 z[3];
#pragma unroll
    for (int x = 0; x < 3; ++x) z[x] = *(const u32x2*)(lds + L_Z + (((4 * jb + x) * 4 + rto) * 64 + l) * 8);
    const unsigned z3 = *(const unsigned*)(lds + L_Z + ((12 + rto) * 64 + l) * 8 + 4 * jb);
    u32x2 gbv[3];
#pragma unroll
    for (int j = 0; j < 3; ++j) gbv[j] = *(const u32x2*)(lds + L_MISC + (4 * ((w >> 2) ? 3 + (j < 2 ? j : 1) : j) + h4) * 8);
    BAR();
    {
      f32x4 r3[3][3];
      f32x2 r33[3];
#pragma unroll
      for (int k = 0; k < 3; ++k) {
#pragma unroll
        for (int x = 0; x < 3; ++x) r3[x][k] = red[((io * 7 + 4 * jb + x) * 3 + k) * 64];
        r33[k] = ((const f32x2*)&red[((io * 7 + 3) * 3 + k) * 64])[jb];
      }
      SCHED();
      const float bs = bflo(bs_raw);
      const unsigned ub = WS_UF + (unsigned)((R * 4 + rto) * 8 + c) * 4096u + (unsigned)jb * 2048u + l16;
      u32x4 o[2];
#pragma unroll
      for (int x = 0; x < 3; ++x) {
        const f32x4 g = (own[x] + r3[x][0]) + (r3[x][1] + r3[x][2]) + bs;
        o[x >> 1][2 * (x & 1)] = pack2(bflo(z[x][0]) * g[0], bfhi(z[x][0]) * g[1]);
        o[x >> 1][2 * (x & 1) + 1] = pack2(bflo(z[x][1]) * g[2], bfhi(z[x][1]) * g[3]);
      }
      st16(rs, ub, o[0]);
      if (jb == 1 && h4 == 3) { o[1][0] = 0u; o[1][1] = 0u; }
      const f32x2 g3 = (own3 + r33[0]) + (r33[1] + r33[2]) + bs;
      o[1][2] = pack2(bflo(z3) * g3[0], bfhi(z3) * g3[1]);
      o[1][3] = ((o[0][0] + o[0][1]) + (o[0][2] + o[0][3])) + ((o[1][0] + o[1][1]) + (o[1][2] + tag));
      st16(rs, ub + 1024u, o[1]);
    }
    SCHED();
    BAR();
    STAMPT(16);

    const int rt = w & 3, kh = w >> 2;
    u32x4 uf[4][4];
    const unsigned ub = WS_UF + (unsigned)((R * 4 + rt) * 8 + kh) * 4096u + l16;
    auto ld_u = [&](int cc) {
#pragma unroll
      for (int qq = 0; qq < 4; ++qq) uf[cc][qq] = ld16(rs, ub + (unsigned)cc * 8192u + (unsigned)qq * 1024u);
    };
    auto u_ok = [&](int cc) {
      unsigned d = 0u;
#pragma unroll
      for (int e = 0; e < 2; ++e) {
        const u32x4 a = uf[cc][2 * e], b = uf[cc][2 * e + 1];
        d |= (((a[0] + a[1]) + (a[2] + a[3])) + ((b[0] + b[1]) + (b[2] + tag))) ^ b[3];
      }
      return __all(d == 0u);
    };
#pragma unroll
    for (int k = 0; k < 18; ++k) {
      if (k == 17) {
        SCHED();
        ld_u(0);
        ld_u(1);
        SCHED();
      }
      if (k < 17 || w < 4) *(u32x4*)(lds + (w + 8 * k) * 1024 + l16) = wo[k];
    }
    BAR();
    STAMPT(17);
    f32x4 acc[5];
#pragma unroll
    for (int j = 0; j < 5; ++j) acc[j] = (f32x4){0.f, 0.f, 0.f, 0.f};
    unsigned wb0 = (unsigned)(kh * 17920) + l16, wb1 = wb0 + 71680u, wh0 = (unsigned)(kh * 17920 + 15360 + l * 8), wh1 = wh0 + 71680u;
    asm volatile("" : "+v"(wb0), "+v"(wb1), "+v"(wh0), "+v"(wh1));
    u32x4 af[2][15];
    u32x2 ah[2][5];
    auto g3ld = [&](int cc) {
#pragma unroll
      for (int i = 0; i < 15; ++i) af[cc & 1][i] = *(const u32x4*)(lds + (cc < 2 ? wb0 : wb1) + (cc & 1) * 35840 + i * 1024);
#pragma unroll
      for (int dj = 0; dj < 5; ++dj) ah[cc & 1][dj] = *(const u32x2*)(lds + (cc < 2 ? wh0 : wh1) + (cc & 1) * 35840 + dj * 512);
    };
    g3ld(0);
    SCHED();
    ld_u(2);
    SCHED();
    g3ld(1);
#pragma unroll
    for (int cc = 0; cc < 4; ++cc) {
      SCHED();
      if (!u_ok(cc)) {
        _Pragma("unroll 1") for (unsigned spins = 0; spins < SPIN_CAP; ++spins) {
          __builtin_amdgcn_s_sleep(1);
          ld_u(cc);
          if (u_ok(cc)) break;
        }
      }
      uf[cc][1][3] = uf[cc][3][2];
      const u32x2 uh = {uf[cc][3][0], uf[cc][3][1]};
      SCHED();
#pragma unroll
      for (int i = 0; i < 15; ++i) {
        acc[i % 5] = MFMA16(af[cc & 1][i], uf[cc][i / 5], acc[i % 5]);
        if (cc < 2) af[cc & 1][i] = *(const u32x4*)(lds + wb1 + (cc & 1) * 35840 + i * 1024);
        SCHED();
      }
#pragma unroll
      for (int dj = 0; dj < 5; ++dj) {
        acc[dj] = MFMA16K16(ah[cc & 1][dj], uh, acc[dj]);
        if (cc < 2) ah[cc & 1][dj] = *(const u32x2*)(lds + wh1 + (cc & 1) * 35840 + dj * 512);
        SCHED();
      }
      if (cc == 0) ld_u(3);
    }
    STAMPT(18);
    f32x4* X = (f32x4*)(lds + WBLK) + rt * (5 * 64) + l;
    f32x4 m4[3];
    if (kh == 0) {
      X[3 * 64] = acc[3]; X[4 * 64] = acc[4];
      m4[0] = acc[0]; m4[1] = acc[1]; m4[2] = acc[2];
    } else {
      X[0] = acc[0]; X[64] = acc[1]; X[128] = acc[2];
      m4[0] = acc[3]; m4[1] = acc[4]; m4[2] = acc[4];
    }
    BAR();
    if (bid == 0 && tid == 320) st4(rs, WS_EPOCH, tag);
    const int t = R * 64 + tau(rt, n16);
    f32x4 o4[3];
#pragma unroll
    for (int j = 0; j < 3; ++j) o4[j] = X[(kh ? 3 + (j < 2 ? j : 1) : j) * 64];
    SCHED();
#pragma unroll
    for (int j = 0; j < 3; ++j) {
      const f32x4 v = rbf4((f32x4){bflo(gbv[j][0]), bfhi(gbv[j][0]), bflo(gbv[j][1]), bfhi(gbv[j][1])} * rbf4(o4[j] + m4[j]));
      const u32x2 o = {pack2(bflo(xp[j][0]) + v[0], bfhi(xp[j][0]) + v[1]), pack2(bflo(xp[j][1]) + v[2], bfhi(xp[j][1]) + v[3])};
      if (j < 2 || kh == 0)
        __builtin_amdgcn_raw_buffer_store_b64(o, __builtin_amdgcn_make_buffer_rsrc((void*)p.out, 0, S_ * D_ * 2, 0x00020000),
                                              (unsigned)(t * D_ + 80 * c + 16 * (kh ? 3 + j : j) + 4 * h4) * 2u, 0, 0);
    }
  }
  STAMP(19);
  if (w == STW && bid == CYC_BID) {
    const unsigned dl = 5000u + (CYC_MODE ? (stv[CYC_B] - stv[CYC_A]) / 5u : (stv[CYC_B] - stv[CYC_A]) * 4u);
    while ((unsigned)__builtin_amdgcn_s_memrealtime() - t_start < dl) __builtin_amdgcn_s_sleep(1);
  }
  if (tid == STW * 64) {
#pragma unroll
    for (int i = 0; i < 32; ++i) *(unsigned long long*)(ws + WS_STAMP + (blockIdx.x * 32 + i) * 8) = stv[i];
  }
}

extern "C" __device__ __attribute__((used, noinline, visibility("hidden"))) void mega_tail() {}

extern "C" void kernel_launch(void* const* d_in, const int* in_sizes, int n_in, void* d_out, int out_size,
                              void* d_ws, size_t ws_size, hipStream_t stream) {
  Params p;
  p.q = (const u16*)d_in[0];
  p.k = (const u16*)d_in[1];
  p.v = (const u16*)d_in[2];
  p.shift_k = (const u16*)d_in[3];
  p.ssm_k = (const u16*)d_in[4];
  p.ssm_D = (const u16*)d_in[5];
  p.w_in = (const u16*)d_in[7];
  p.w_sp = (const u16*)d_in[8];
  p.b_sp = (const u16*)d_in[9];
  p.w_out = (const u16*)d_in[10];
  p.cvec = (const u16*)d_in[11];
  p.w_mod = (const u16*)d_in[12];
  p.out = (u16*)d_out;
  p.ws = (char*)d_ws;
  mega<<<256, 512, 0, stream>>>(p);
}
```

```cpp
#include <hip/hip_runtime.h>
#include <stdint.h>

typedef unsigned short u16;
typedef __attribute__((ext_vector_type(8))) short bf16x8;
typedef __attribute__((ext_vector_type(4))) short bf16x4;
typedef __attribute__((ext_vector_type(4))) float f32x4;
typedef __attribute__((ext_vector_type(2))) float f32x2;
typedef __attribute__((ext_vector_type(4))) unsigned int u32x4;
typedef __attribute__((ext_vector_type(2))) unsigned int u32x2;
#define LAS __attribute__((address_space(3)))
typedef __amdgpu_buffer_rsrc_t rsrc_t;
typedef __attribute__((ext_vector_type(4))) short s16x4;

constexpr int S_ = 2048, D_ = 640, F_ = 1728, FH = 864, CB = 108;

constexpr unsigned WS_EPOCH = 0;
constexpr unsigned WS_FWI = 4096;
constexpr unsigned WS_FZ = WS_FWI + 384 * 128;
constexpr unsigned WS_FWO = WS_FZ + 256 * 128;
constexpr unsigned F4S = 896 * 128;
constexpr unsigned WS_CS = WS_FZ + 256 * 128;
constexpr unsigned WS_MOD = 131072;
constexpr unsigned WS_MODP = WS_MOD + 81920;
constexpr unsigned WS_ST1 = 393216;
constexpr unsigned WS_PART = 524288;
constexpr unsigned WS_CSP = WS_PART + 262144;
constexpr unsigned WS_FX = WS_CSP + 655360;
constexpr unsigned WS_X1B = WS_FX + 16 * 4 * 160 * 128;
constexpr unsigned WS_QB = WS_X1B + 2621440;
constexpr unsigned WS_KV3 = WS_QB + 2621440;
constexpr unsigned WS_MODQ = WS_KV3 + 1310720;
constexpr unsigned WBLK = 143360;
constexpr unsigned WS_WINF = WS_MODQ + 245760;
constexpr unsigned WS_WOUTF = WS_WINF + 16 * WBLK;
constexpr unsigned WS_Z2F = WS_WOUTF + 8 * WBLK;
constexpr unsigned WS_UF = WS_Z2F + 8 * 64 * 7168;
constexpr unsigned WS_F4 = WS_UF + 32 * 4 * 8 * 5120;
constexpr unsigned WS_END = WS_F4 + 4 * F4S;
constexpr unsigned WS_STAMP = 200u << 20;

constexpr int G_COPY = 4160;
constexpr int L_U = WBLK;
constexpr int U_CH = (256 + S_) * 2 + 32;
constexpr int L_FLG = L_U + 4 * U_CH;
constexpr int L_RT = 16384;
constexpr int L_CONST = WBLK;
constexpr int L_PST = L_CONST + 2048;
constexpr int L_Z = L_CONST + 5120;
constexpr int L_ST2 = L_Z + 14336;
constexpr int L_MISC = L_ST2 + 512;
constexpr int LDS_TOTAL = 163840;

#define AUX_T 610
#define RT2_T 360
#define CB_T 380
#define W_T 730
#define CS_T 830
#define Y_T 930
#define K_T 460
#ifndef M_T
#define M_T 520
#endif
#define WA_T 580
constexpr int SC1 = 16, NT = 2;
constexpr unsigned SPIN_CAP = 1u << 16;

__device__ __forceinline__ float bflo(unsigned x) { return __uint_as_float(x << 16); }
__device__ __forceinline__ float bfhi(unsigned x) { return __uint_as_float(x & 0xffff0000u); }
__device__ __forceinline__ unsigned f2bf(float x) {
  __bf16 b = (__bf16)x;
  return (unsigned)__builtin_bit_cast(unsigned short, b);
}
__device__ __forceinline__ float rbf(float x) { return bflo(f2bf(x)); }
typedef __attribute__((ext_vector_type(2))) __bf16 bf2_t;
__device__ __forceinline__ unsigned pack2(float lo, float hi) {
  f32x2 v = {lo, hi};
  return __builtin_bit_cast(unsigned, __builtin_convertvector(v, bf2_t));
}
__device__ __forceinline__ f32x2 rbf2(f32x2 v) {
  const unsigned p = __builtin_bit_cast(unsigned, __builtin_convertvector(v, bf2_t));
  return (f32x2){__uint_as_float(p << 16), __uint_as_float(p & 0xffff0000u)};
}
__device__ __forceinline__ f32x4 rbf4(f32x4 v) {
  const f32x2 a = rbf2((f32x2){v[0], v[1]}), b = rbf2((f32x2){v[2], v[3]});
  return (f32x4){a[0], a[1], b[0], b[1]};
}
template <int CTRL>
__device__ __forceinline__ float dpp_f(float v) {
  return __int_as_float(__builtin_amdgcn_update_dpp(0, __float_as_int(v), CTRL, 0xf, 0xf, true));
}
__device__ __forceinline__ float sum8(float v) {
  v += dpp_f<0xB1>(v);
  v += dpp_f<0x4E>(v);
  v += dpp_f<0x141>(v);
  return v;
}
__device__ __forceinline__ float sum16(float v) {
  v = sum8(v);
  v += dpp_f<0x140>(v);
  return v;
}
__device__ __forceinline__ float sum_rows(float v) {
  auto a = __builtin_amdgcn_permlane16_swap(__float_as_uint(v), __float_as_uint(v), false, false);
  v = __uint_as_float(a[0]) + __uint_as_float(a[1]);
  auto b = __builtin_amdgcn_permlane32_swap(__float_as_uint(v), __float_as_uint(v), false, false);
  return __uint_as_float(b[0]) + __uint_as_float(b[1]);
}
#define MFMA16(a, b, c) __builtin_amdgcn_mfma_f32_16x16x32_bf16(__builtin_bit_cast(bf16x8, a), __builtin_bit_cast(bf16x8, b), c, 0, 0, 0)
#define MFMA16K16(a, b, c) __builtin_amdgcn_mfma_f32_16x16x16bf16_1k(__builtin_bit_cast(bf16x4, a), __builtin_bit_cast(bf16x4, b), c, 0, 0, 0)
__device__ __forceinline__ u32x2 trd(const char* p) {
  return __builtin_bit_cast(u32x2, __builtin_amdgcn_ds_read_tr16_b64_v4i16((LAS s16x4*)p));
}
__device__ __forceinline__ u32x4 trfrag(const char* p, int pitch) {
  u32x2 a = trd(p), b = trd(p + 16 * pitch);
  return (u32x4){a[0], a[1], b[0], b[1]};
}
#define BAR() asm volatile("s_waitcnt lgkmcnt(0)\n\ts_barrier" ::: "memory")
#define DRAIN() do { __builtin_amdgcn_s_waitcnt(0x0F70); asm volatile("" ::: "memory"); } while (0)
#define SCHED() __builtin_amdgcn_sched_barrier(0)
template <int N> struct IC { static constexpr int value = N; };
template <int I, int N, class F>
__device__ __forceinline__ void sfor(F&& f) {
  if constexpr (I < N) { f(IC<I>()); sfor<I + 1, N>(f); }
}

__device__ __forceinline__ u32x4 ld16(rsrc_t r, unsigned off) { return __builtin_amdgcn_raw_buffer_load_b128(r, off, 0, SC1); }
__device__ __forceinline__ u32x2 ld8(rsrc_t r, unsigned off) { return __builtin_amdgcn_raw_buffer_load_b64(r, off, 0, SC1); }
__device__ __forceinline__ unsigned ld4(rsrc_t r, unsigned off) { return __builtin_amdgcn_raw_buffer_load_b32(r, off, 0, SC1); }
#define ST_AUX 16
__device__ __forceinline__ void st16(rsrc_t r, unsigned off, u32x4 v) { __builtin_amdgcn_raw_buffer_store_b128(v, r, off, 0, ST_AUX); }
__device__ __forceinline__ void st8(rsrc_t r, unsigned off, u32x2 v) { __builtin_amdgcn_raw_buffer_store_b64(v, r, off, 0, SC1); }
__device__ __forceinline__ void st_flag4(rsrc_t r, unsigned off, unsigned tag, int l) {
  if (l < 32) st16(r, WS_F4 - WS_FWI + off + (unsigned)(l >> 3) * F4S + (unsigned)(l & 7) * 16u, (u32x4){tag, tag, tag, tag});
}
__device__ __forceinline__ void st4(rsrc_t r, unsigned off, unsigned v) { __builtin_amdgcn_raw_buffer_store_b32(v, r, off, 0, SC1); }
#define BULK_AUX 16
__device__ __forceinline__ u32x4 ld16p(rsrc_t r, unsigned off) { return __builtin_amdgcn_raw_buffer_load_b128(r, off, 0, BULK_AUX); }
__device__ __forceinline__ u32x2 ld8p(rsrc_t r, unsigned off) { return __builtin_amdgcn_raw_buffer_load_b64(r, off, 0, BULK_AUX); }
__device__ __forceinline__ void dma1k(rsrc_t r, char* lds_dst, unsigned l16, unsigned soff) {
  __builtin_amdgcn_raw_ptr_buffer_load_lds(r, (LAS void*)lds_dst, 16, l16, soff, 0, BULK_AUX);
}

#define STAMP(i)
#define STAMPF(i)
#define STAMPT(i) STAMP(i)

template <int N, int SLP>
__device__ __forceinline__ void wait_lines(rsrc_t r, const unsigned (&off)[N], unsigned tag) {
  bool need[N];
#pragma unroll
  for (int i = 0; i < N; ++i) need[i] = off[i] != ~0u;
  _Pragma("unroll 1") for (unsigned spins = 0; spins < SPIN_CAP; ++spins) {
    unsigned v[N];
#pragma unroll
    for (int i = 0; i < N; ++i) {
      v[i] = tag;
      if (need[i]) v[i] = ld4(r, off[i]);
    }
    bool any = false;
#pragma unroll
    for (int i = 0; i < N; ++i) { need[i] = v[i] != tag; any = any || need[i]; }
    if (!__any(any)) break;
    __builtin_amdgcn_s_sleep(SLP);
  }
  asm volatile("" ::: "memory");
}

__device__ __forceinline__ int tau(int rt, int n) { return 32 * (rt >> 1) + 8 * (n >> 2) + 4 * (rt & 1) + (n & 3); }
__device__ __forceinline__ int psi(int n) { return 2 * (n & 7) + (n >> 3); }
#define QX_CM(d, s0, s1, P) "v_cndmask_b32_dpp " d ", " s0 ", " s1 ", vcc quad_perm:" P " row_mask:0xf bank_mask:0xf\n\t"
#define QX_BODY(M, P) \
  asm("s_mov_b32 vcc_lo, " M "\n\ts_mov_b32 vcc_hi, " M "\n\t" \
      QX_CM("%0", "%12", "%8", P) "v_xor_b32 %4, %8, %0\n\t" QX_CM("%1", "%13", "%9", P) "v_xor_b32 %5, %9, %1\n\t" \
      QX_CM("%2", "%14", "%10", P) "v_xor_b32 %6, %10, %2\n\t" QX_CM("%3", "%15", "%11", P) "v_xor_b32 %7, %11, %3" \
      : "=&v"(t[0]), "=&v"(t[1]), "=&v"(t[2]), "=&v"(t[3]), "=&v"(u[0]), "=&v"(u[1]), "=&v"(u[2]), "=&v"(u[3]) \
      : "v"(a[0]), "v"(a[1]), "v"(a[2]), "v"(a[3]), "v"(b[0]), "v"(b[1]), "v"(b[2]), "v"(b[3]) : "vcc")
template <int BIT>
__device__ __forceinline__ void quad_xchg3(u32x4& a, u32x4& b) {
  unsigned t[4], u[4];
  if (BIT == 0) QX_BODY("0x55555555", "[1,0,3,2]");
  else QX_BODY("0x33333333", "[2,3,0,1]");
  a = (u32x4){t[0], t[1], t[2], t[3]};
#pragma unroll
  for (int d = 0; d < 4; ++d) b[d] ^= (unsigned)__builtin_amdgcn_update_dpp(0, (int)u[d], BIT == 0 ? 0xB1 : 0x4E, 0xf, 0xf, true);
}
__device__ __forceinline__ void quad_transpose(u32x4& x0, u32x4& x1, u32x4& x2, u32x4& x3) {
  quad_xchg3<0>(x0, x1);
  quad_xchg3<0>(x2, x3);
  quad_xchg3<1>(x0, x2);
  quad_xchg3<1>(x1, x3);
  asm volatile("" : "+v"(x0), "+v"(x1), "+v"(x2), "+v"(x3));
}

struct Params {
  const u16 *q, *k, *v, *shift_k, *ssm_k, *ssm_D, *w_in, *w_sp, *b_sp, *w_out, *cvec, *w_mod;
  u16* out;
  char* ws;
};

__global__ __launch_bounds__(512, 2) void mega(Params p) {
  __shared__ __attribute__((aligned(16))) char lds[LDS_TOTAL];
  {
    unsigned long long pc0;
    unsigned vt;
    asm volatile("v_readfirstlane_b32 vcc_lo, %3\n\ts_cmpk_lt_u32 %2, 192\n\ts_cbranch_scc1 4f\n\ts_cmpk_lt_u32 vcc_lo, 64\n\ts_cbranch_scc0 4f\n\t"
                 "s_lshr_b32 vcc_hi, %2, 3\n\ts_sub_u32 vcc_hi, vcc_hi, 24\n\ts_lshl_b32 vcc_hi, vcc_hi, 13\n\t"
                 "v_lshlrev_b32 %1, 7, %3\n\tv_add_u32 %1, vcc_hi, %1\n\ts_getpc_b64 %0\n.Lmega_here:\n\t"
                 "v_min_u32 %1, mega_tail-.Lmega_here-4, %1\n\ts_movk_i32 m0, 0x2000\n\ts_nop 1\n\tglobal_load_lds_dword %1, %0\n4:\n\t"
                 "s_cmpk_lt_u32 %2, 160\n\ts_cbranch_scc0 1f\n\ts_cmpk_lt_u32 vcc_lo, 256\n\ts_cbranch_scc1 3f\n\t"
                 "s_call_b64 vcc, .Ltouch1\n\ts_branch 3f\n1:\n\t"
                 "s_cmpk_lt_u32 vcc_lo, 256\n\ts_cbranch_scc1 2f\n\ts_cmpk_lt_u32 %2, 224\n\ts_cbranch_scc0 2f\n\ts_call_b64 vcc, .Ltouch3\n\ts_branch 3f\n"
                 "2:\n\ts_call_b64 vcc, .Ltouch2\n3:"
                 : "=&s"(pc0), "=&v"(vt) : "s"(blockIdx.x), "v"(threadIdx.x) : "vcc", "scc", "memory");
  }
  SCHED();
  const int tid = threadIdx.x, l = tid & 63;
  const int w = __builtin_amdgcn_readfirstlane(tid >> 6);
  const int bid = blockIdx.x, xcd = bid & 7, idx = bid >> 3;
#define TOUCH_AT(n) asm volatile("s_branch 1f\n.Ltouch" #n ":\n\ts_setpc_b64 vcc\n1:")
  char* const ws = p.ws;
  asm volatile("" ::"s"(p.q), "s"(p.k), "s"(p.v), "s"(p.shift_k), "s"(p.ssm_k), "s"(p.ssm_D), "s"(p.w_in), "s"(p.w_sp), "s"(p.b_sp),
               "s"(p.w_out), "s"(p.cvec), "s"(p.w_mod), "s"(p.out), "s"(p.ws));
  unsigned epoch, tag, t_start;
  unsigned long long t64;
  asm volatile("s_load_dword %0, %2, 0x0\n\ts_memrealtime %1" : "=&s"(epoch), "=&s"(t64) : "s"(ws));
#define TAG_READY() do { SCHED(); \
    asm volatile("s_waitcnt lgkmcnt(0)\n\ts_mul_i32 %0, %2, 0x9e3779b1\n\ts_add_i32 %0, %0, 0x9e3779b9\n\ts_mov_b64 %1, %3" \
                 : "=&s"(tag), "=&s"(t64r) : "s"(epoch), "s"(t64) : "scc"); t_start = (unsigned)t64r; SCHED(); } while (0)
  unsigned long long t64r;
  const rsrc_t rs = __builtin_amdgcn_make_buffer_rsrc(ws, 0, WS_END, 0x00020000);
  const unsigned l16 = (unsigned)l * 16u;
  STAMP(0);
  const bool is_conv = idx < 20;
  const int quad = xcd * 20 + idx;
  const int R = is_conv ? (xcd & 3) * 5 + (idx >> 2) : (xcd & 3) * 3 + 15 + (idx >> 2);
  const int c = (xcd >> 2) * 4 + (idx & 3);
  const unsigned fwi = WS_F4 + (unsigned)(xcd & 3) * F4S, fz = fwi + (WS_FZ - WS_FWI);
  const int n16 = l & 15, h4 = l >> 4;
  u32x4 wsp[4][8];
  const int rotg = (idx >> 4) & 1, rotb = (idx >> 2) & 3;
  const rsrc_t rsp = __builtin_amdgcn_make_buffer_rsrc((void*)p.w_sp, 0, S_ * S_ * 2, 0x00020000);
  const unsigned wsp_vo = (unsigned)(((l >> 2) & 3) * 8 * 4096 + h4 * 64 + ((l + rotb) & 3) * 16);
#define LDW(k) LDW_(k)
#define WSP_TR(g) WSP_TR_(g)
#define WROW R
#define LDW_(k) wsp[((k) >> 2) & 3][((k) >> 4) * 4 + ((k) & 3)] = __builtin_amdgcn_raw_buffer_load_b128(rsp, wsp_vo, \
    (unsigned)((WROW * 64 + 32 * (((k) >> 3) & 1) + 4 * (((k) >> 2) & 1) + ((k) & 3)) * 4096 + w * 512 + ((((k) >> 4) ^ rotg) * 256)), 0)
#define WSP_TR_(g) quad_transpose(wsp[(g) & 3][((g) >> 2) * 4], wsp[(g) & 3][((g) >> 2) * 4 + 1], wsp[(g) & 3][((g) >> 2) * 4 + 2], wsp[(g) & 3][((g) >> 2) * 4 + 3])
  const rsrc_t rwi = __builtin_amdgcn_make_buffer_rsrc((void*)p.w_in, 0, D_ * F_ * 2, 0x00020000);
  const rsrc_t rwo = __builtin_amdgcn_make_buffer_rsrc((void*)p.w_out, 0, FH * D_ * 2, 0x00020000);
  const rsrc_t rq = __builtin_amdgcn_make_buffer_rsrc((void*)p.q, 0, S_ * D_ * 2, 0x00020000);
  auto hold = [&](unsigned tt) { while ((unsigned)__builtin_amdgcn_s_memrealtime() - t_start < tt) __builtin_amdgcn_s_sleep(2); };
  volatile LAS unsigned* lf = (volatile LAS unsigned*)(lds + L_FLG);
  auto lf_set = [&](int i) { asm volatile("s_waitcnt lgkmcnt(0)" ::: "memory"); if (l == 0) lf[i] = tag; };
  auto lf_wait = [&](int i0, int n) {
    _Pragma("unroll 1") for (unsigned spins = 0; spins < (SPIN_CAP << 4); ++spins) {
      const unsigned v = lf[i0 + (l & (n - 1))];
      if (__all(v == tag)) break;
      __builtin_amdgcn_s_sleep(1);
    }
    asm volatile("" ::: "memory");
  };
  auto xflag = [&](int s) {
    st16(rs, WS_FX + (unsigned)(((4 * s + (w & 3)) * 4 + ((l >> 3) & 3)) * 160 + quad) * 128u + (unsigned)(l & 7) * 16u, (u32x4){tag, tag, tag, tag});
    SCHED();
  };
  auto load_b2 = [&]() {
#pragma unroll
    for (int k = 0; k < 35; ++k) {
      int i = k + (idx >> 2) * 4;
      i = (i >= 35 ? i - 35 : i) + 35 * w;
      dma1k(rs, lds + i * 1024, l16, WS_WINF + (unsigned)(8 + c) * WBLK + (unsigned)i * 1024u);
    }
  };
  char* const wo_st = lds + 122880;
  auto wo_ld = [&]() {
    const int U = R < 28 ? R : 0, u2 = U >> 1, pt = u2 / 6, f6 = u2 % 6;
    const int c0 = 4 * (U & 1) + 2 * (pt < 2 ? f6 / 3 : f6), fi = f6 % 3;
    const int b0 = pt < 2 ? CB * (c0 + (fi >> 1)) + 48 * pt + 2 * (fi & 1) : CB * c0 + 96, d1 = pt < 2 ? 16 : 2, d2 = pt < 2 ? 32 : CB;
    const int b3 = pt < 2 ? CB * (c0 + 1) + 48 * pt + 16 * fi + 2 : b0 + CB + 2;
#pragma unroll
    for (int r = 0; r < 5; ++r) {
      const int g = r * 64 + l, rho = g / 10;
      const int fb = (rho & 16) ? ((rho & 2) ? b3 : b0 + d2) : ((rho & 2) ? b0 + d1 : b0);
      __builtin_amdgcn_raw_ptr_buffer_load_lds(rwo, (LAS void*)(wo_st + r * 1024), 16, (unsigned)((fb + (rho & 13)) * (D_ * 2) + (g % 10) * 16 + c * 160), 0, 0, 0);
    }
  };
  auto rt_ld = [&](int i0, char* st) {
    const int cb = (i0 < 160 ? 8 : 0) + (i0 & 7), Q = (i0 < 160 ? i0 : i0 - 160) >> 3;
#pragma unroll
    for (int r = 0; r < 7; ++r) {
      const int g = r * 64 + l;
      __builtin_amdgcn_raw_ptr_buffer_load_lds(rwi, (LAS void*)(st + r * 1024), 16, (unsigned)((g / 14) * (F_ * 2) + (g % 14) * 16 + Q * 32 * F_ * 2 + cb * CB * 2),
                                               0, 0, NT);
    }
  };
  auto rt_job = [&](int i0, char* st) {
    const int cb = (i0 < 160 ? 8 : 0) + (i0 & 7), Q = (i0 < 160 ? i0 : i0 - 160) >> 3;
    const int col = 640 * (l >> 5) + 32 * Q + (l & 31);
    const unsigned po = WS_MODP + (unsigned)col * 8u;
    u32x2 pp8[8];
    hold(CB_T);
    _Pragma("unroll 1") for (unsigned spins = 0; spins < SPIN_CAP; ++spins) {
      unsigned dd = 0u;
#pragma unroll
      for (int i = 0; i < 8; ++i) pp8[i] = ld8(rs, po + (unsigned)i * 10240u);
#pragma unroll
      for (int i = 0; i < 8; ++i) dd |= pp8[i][0] ^ tag;
      if (__all(dd == 0u)) break;
      __builtin_amdgcn_s_sleep(4);
    }
    STAMPF(9);
    {
      float t = 0.f;
#pragma unroll
      for (int i = 0; i < 8; ++i) t += __uint_as_float(pp8[i][1]);
      if (l >= 32) *(float*)(st + 7168 + (l - 32) * 4) = rbf(1.0f + rbf(t));
      else *(u16*)(st + 7296 + l * 2) = (u16)f2bf(t);
    }
    DRAIN();
    asm volatile("s_waitcnt lgkmcnt(0)" ::: "memory");
    const f32x4 ca = *(const f32x4*)(st + 7168 + h4 * 16), cc = *(const f32x4*)(st + 7168 + 64 + h4 * 16);
    const char* bp = st + (4 * h4 + (n16 >> 2)) * 224 + (n16 & 3) * 8;
    u32x4 x[7], o[7];
#pragma unroll
    for (int j = 0; j < 7; ++j) {
      x[j] = trfrag(bp + j * 32, 224);
      o[j] = (u32x4){pack2(bflo(x[j][0]) * ca[0], bfhi(x[j][0]) * ca[1]), pack2(bflo(x[j][1]) * ca[2], bfhi(x[j][1]) * ca[3]),
                     pack2(bflo(x[j][2]) * cc[0], bfhi(x[j][2]) * cc[1]), pack2(bflo(x[j][3]) * cc[2], bfhi(x[j][3]) * cc[3])};
      if (j == 6 && n16 == 12) o[j] = (u32x4){0x3F803F80u, 0x3F803F80u, 0x3F803F80u, 0x3F803F80u};
      st16(rs, WS_WINF + (unsigned)cb * WBLK + (unsigned)(Q * 7 + j) * 1024u + l16, o[j]);
    }
    STAMPF(11);
    const u32x2 e0 = *(const u32x2*)(st + 7296 + h4 * 8), e1 = *(const u32x2*)(st + 7328 + h4 * 8);
    f32x4 d[2] = {{0.f, 0.f, 0.f, 0.f}, {0.f, 0.f, 0.f, 0.f}};
#pragma unroll
    for (int j = 0; j < 7; ++j) {
      const unsigned one = n16 == 4 * (j & 3) ? 0x3F803F80u : 0u;
      const bool sr = n16 == 4 * (j & 3) + 1;
      d[j >> 2] = MFMA16(((u32x4){one, one, one, one}), o[j], d[j >> 2]);
      d[j >> 2] = MFMA16(((u32x4){sr ? e0[0] : 0u, sr ? e0[1] : 0u, sr ? e1[0] : 0u, sr ? e1[1] : 0u}), x[j], d[j >> 2]);
    }
#pragma unroll
    for (int m = 0; m < 2; ++m)
      st16(rs, WS_CSP + (unsigned)((cb * 20 + Q) * 2 + m) * 1024u + l16, (u32x4){tag, __float_as_uint(d[m][0]), tag, __float_as_uint(d[m][1])});
    STAMPF(13);
    asm volatile("s_waitcnt vmcnt(2)" ::: "memory");
    st_flag4(rs, WS_FWI + (unsigned)(cb * 20 + Q) * 128u, tag, l);
  };

  auto red8 = [&](float v) { v += dpp_f<0x128>(v); return sum_rows(v); };
  const rsrc_t rwm = __builtin_amdgcn_make_buffer_rsrc((void*)p.w_mod, 0, D_ * 3840 * 2, 0x00020000);
  auto gemv_load = [&](u32x4* pre, int blk, int sl, int np) {
    pre[10][0] = *(const unsigned*)(p.cvec + 8 * np * sl + 2 * (l < 4 * np ? l : 0));
#pragma unroll
    for (int pp = 0; pp < np; ++pp)
      pre[pp] = __builtin_amdgcn_raw_buffer_load_b128(rwm, (unsigned)((l >> 3) * 7680 + (l & 7) * 16), (unsigned)((8 * np * sl + pp * 8) * 7680 + blk * 128), NT);
  };
  auto gemv_fin = [&](float* acc, const u32x4* pre, int np, char* scr) {
    const unsigned cv2 = pre[10][0];
    unsigned* svt = (unsigned*)scr + w * 96;
#pragma unroll
    for (int e = 0; e < 2; ++e) {
      const int row = 2 * l + e;
      const float cv = e ? bfhi(cv2) : bflo(cv2);
      if (l < 4 * np) svt[(row & 7) * 12 + (row >> 3)] = __float_as_uint(rbf(cv * __builtin_amdgcn_rcpf(1.0f + __expf(-cv))));
    }
    asm volatile("s_waitcnt lgkmcnt(0)" ::: "memory");
    u32x4 sv[3];
#pragma unroll
    for (int i = 0; i < (np + 3) / 4; ++i) sv[i] = *(const u32x4*)(svt + (l >> 3) * 12 + 4 * i);
    SCHED();
#pragma unroll
    for (int e = 0; e < 8; ++e) acc[e] = 0.f;
#pragma unroll
    for (int pp = 0; pp < np; ++pp) {
      const unsigned sh = sv[pp >> 2][pp & 3], sl2 = sh >> 16;
#pragma unroll
      for (int e = 0; e < 4; ++e) {
        const unsigned x = pre[pp][e];
        acc[2 * e] = __builtin_amdgcn_fdot2_f32_bf16(__builtin_bit_cast(bf2_t, x), __builtin_bit_cast(bf2_t, sl2), acc[2 * e], false);
        acc[2 * e + 1] = __builtin_amdgcn_fdot2_f32_bf16(__builtin_bit_cast(bf2_t, x), __builtin_bit_cast(bf2_t, sh), acc[2 * e + 1], false);
      }
    }
#pragma unroll
    for (int e = 0; e < 8; ++e) acc[e] = red8(acc[e]);
  };
  auto gemv_put = [&](const float* acc, unsigned off) {
    float v = acc[0];
#pragma unroll
    for (int e = 1; e < 8; ++e) { v = (l >> 3) == e ? acc[e] : v; asm("" : "+v"(v)); }
    st8(rs, off + (unsigned)l * 8u, (u32x2){tag, (unsigned)__builtin_amdgcn_ds_bpermute(((l & 7) * 8 + (l >> 3)) * 4, (int)__float_as_uint(v))});
  };
  typedef const __attribute__((address_space(4))) unsigned* cptr_t;

  if (__builtin_expect(is_conv, 1)) {
    if (w >= 4) {
      TOUCH_AT(1);
      const int pw = w - 4;
      const rsrc_t ra = __builtin_amdgcn_make_buffer_rsrc((void*)((idx & 2) ? p.v : p.k), 0, S_ * D_ * 2, 0x00020000);
      const rsrc_t rb = __builtin_amdgcn_make_buffer_rsrc((void*)((idx & 2) ? p.k : p.v), 0, S_ * D_ * 2, 0x00020000);
      const rsrc_t rk = __builtin_amdgcn_make_buffer_rsrc((void*)p.k, 0, S_ * D_ * 2, 0x00020000);
      const unsigned vo = (unsigned)((128 * pw + 2 * l) * (D_ * 2));
      const unsigned so0 = (unsigned)((idx & 1) * (D_ * 2) + quad * 8), so1 = (unsigned)((1 - (idx & 1)) * (D_ * 2) + quad * 8);
      u32x2 g[3][4], kh;
      auto gl = [&](int s, int j0, int j1) {
        __builtin_amdgcn_s_setprio(0);
#pragma unroll
        for (int j = j0; j < j1; ++j) {
          g[s][j] = __builtin_amdgcn_raw_buffer_load_b64(j < 2 ? ra : rb, vo, ((j & 1) ? so1 : so0) + (unsigned)(s * 512 * D_ * 2), 0);
          SCHED();
        }
        __builtin_amdgcn_s_setprio(3);
      };
      const cptr_t sp = (cptr_t)(p.shift_k + quad * 16);
      unsigned spv[8];
#pragma unroll
      for (int i = 0; i < 8; ++i) spv[i] = sp[i];
      __builtin_amdgcn_s_sleep(3);
      gl(0, 0, 4);
      {
        const int row = 512 * (l >> 2) + 128 * pw - 3 + (l & 3);
        kh = __builtin_amdgcn_raw_buffer_load_b64(rk, (l >= 12 || (l & 3) == 3 || row < 0) ? 0x80000000u : (unsigned)(row * D_ * 2 + quad * 8), 0, 0);
      }
      TAG_READY();
      f32x2 wt[2][4];
#pragma unroll
      for (int d = 0; d < 2; ++d) {
        wt[d][0] = (f32x2){bflo(spv[4 * d]), bflo(spv[4 * d + 2])}; wt[d][1] = (f32x2){bfhi(spv[4 * d]), bfhi(spv[4 * d + 2])};
        wt[d][2] = (f32x2){bflo(spv[4 * d + 1]), bflo(spv[4 * d + 3])}; wt[d][3] = (f32x2){bfhi(spv[4 * d + 1]), bfhi(spv[4 * d + 3])};
        asm volatile("" : "+s"(wt[d][0]), "+s"(wt[d][1]), "+s"(wt[d][2]), "+s"(wt[d][3]));
      }
      SCHED();
      {
        const int j = idx * 4 + pw;
        const unsigned o = (unsigned)((512 + 64 * (j >> 2) + l) * (D_ * 2) + (((160 * xcd) >> 7) + ((j >> 1) & 1)) * 128);
        __builtin_amdgcn_raw_ptr_buffer_load_lds((j & 1) ? ra : rb, (LAS void*)(lds + 98304), 4, j < 64 ? o : 0x80000000u, 0, 0, 0);
      }
      SCHED();
      gl(1, 0, 4);
      STAMP(21);
      char* up = lds + L_U + 512 + 256 * pw + (l >> 2) * 16 + (3 - (l & 3)) * 4;
      auto kv = [&](auto S__, auto N__) {
        constexpr int s = decltype(S__)::value;
        u32x2* x = g[s];
        SCHED();
        asm volatile("s_waitcnt vmcnt(%5)" : "+v"(x[0]), "+v"(x[1]), "+v"(x[2]), "+v"(x[3]), "+v"(kh) : "n"(decltype(N__)::value));
        if (s == 2) STAMP(27);
        if (idx & 1) { asm volatile(""); const u32x2 t = x[0], u = x[2]; x[0] = x[1]; x[1] = t; x[2] = x[3]; x[3] = u; }
        asm volatile("" : "+v"(x[0]), "+v"(x[1]), "+v"(x[2]), "+v"(x[3]));
        if (idx & 2) { asm volatile(""); const u32x2 t = x[0], u = x[1]; x[0] = x[2]; x[1] = x[3]; x[2] = t; x[3] = u; }
        asm volatile("" : "+v"(x[0]), "+v"(x[1]), "+v"(x[2]), "+v"(x[3]));
        f32x2 kf[2][5], o[2][2];
#pragma unroll
        for (int d = 0; d < 2; ++d) {
          unsigned kk[5];
          kk[3] = x[0][d]; kk[4] = x[1][d];
          kk[2] = __builtin_amdgcn_update_dpp((unsigned)__builtin_amdgcn_readlane(kh[d], 4 * s + 2), kk[4], 0x138, 0xf, 0xf, false);
          kk[1] = __builtin_amdgcn_update_dpp((unsigned)__builtin_amdgcn_readlane(kh[d], 4 * s + 1), kk[3], 0x138, 0xf, 0xf, false);
          kk[0] = __builtin_amdgcn_update_dpp((unsigned)__builtin_amdgcn_readlane(kh[d], 4 * s), kk[2], 0x138, 0xf, 0xf, false);
#pragma unroll
          for (int j = 0; j < 5; ++j) kf[d][j] = (f32x2){bflo(kk[j]), bfhi(kk[j])};
        }
#pragma unroll
        for (int t = 0; t < 5; ++t) {
#pragma unroll
          for (int i = 0; i < 4; ++i) {
            const int d = i >> 1, r = i & 1;
            if (t == 4) o[d][r] *= (f32x2){bflo(x[2 + r][d]), bfhi(x[2 + r][d])};
            else if (t == 0) o[d][r] = kf[d][r + 3] * wt[d][0];
            else o[d][r] = __builtin_elementwise_fma(kf[d][r + 3 - t], wt[d][t], o[d][r]);
          }
          asm volatile("" : "+v"(o[0][0]), "+v"(o[0][1]), "+v"(o[1][0]), "+v"(o[1][1]));
        }
#pragma unroll
        for (int i = 0; i < 4; ++i) *(unsigned*)(up + i * U_CH + s * 1024) = pack2(o[i >> 1][1][i & 1], o[i >> 1][0][i & 1]);
        lf_set(4 * s + pw);
        SCHED();
      };
      const char* yr = lds + L_U + (n16 >> 2) * U_CH + (128 * pw + 32 * h4 + 8 * (n16 & 1) + 4 * ((n16 >> 1) & 1)) * 2;
      const unsigned yo = (unsigned)(8 * pw + (l >> 3)) * 20480u + (unsigned)quad * 128u + (unsigned)(l & 7) * 16u;
      const unsigned so = WS_ST1 + (unsigned)xcd * 16384u + (unsigned)pw * 1024u + l16;
      const unsigned mo = WS_MODQ + (unsigned)((n16 < 12 ? n16 >> 2 : 0) * D_ + quad * 4 + (l & 3)) * 8u + (unsigned)h4 * 40960u;
      u32x4 sv[4], qv[4];
      u32x2 mv[2];
      auto ld_e = [&]() {
#pragma unroll
        for (int s = 0; s < 4; ++s) sv[s] = ld16(rs, so + (unsigned)s * 4096u);
        SCHED();
      };
      auto s_ok = [&]() {
        unsigned d = 0u;
#pragma unroll
        for (int s = 0; s < 4; ++s) d |= (sv[s][1] ^ tag) | (sv[s][3] ^ tag);
        return __all(d == 0u);
      };
      kv(IC<0>(), IC<5>());
      STAMP(23);
      gl(2, 0, 4);
      kv(IC<1>(), IC<4>());
      STAMPF(9);
      u32x4 k3[2];
      const unsigned k3o = WS_KV3 + (unsigned)(16 * pw + (l >> 2)) * 20480u + (unsigned)(((l >> 1) & 1) * 10240 + (2 * quad + (l & 1)) * 16);
      hold(K_T);
      k3[0] = ld16(rs, k3o); k3[1] = ld16(rs, k3o + 5120u);
      SCHED();
      ld_e();
      kv(IC<2>(), IC<6>());
      STAMPF(11);
      auto k_ok = [&]() { return __all(((k3[0][1] ^ tag) | (k3[0][3] ^ tag) | (k3[1][1] ^ tag) | (k3[1][3] ^ tag)) == 0u); };
      asm volatile("" : "+v"(k3[0]), "+v"(k3[1]));
      if (!k_ok()) {
        _Pragma("unroll 1") for (unsigned spins = 0; spins < SPIN_CAP; ++spins) {
          __builtin_amdgcn_s_sleep(4);
          k3[0] = ld16(rs, k3o); k3[1] = ld16(rs, k3o + 5120u);
          if (k_ok()) break;
        }
      }
      *(u32x4*)(lds + L_U + (2 * (l & 1) + ((l >> 1) & 1)) * U_CH + 512 + (1536 + 128 * pw + 8 * (l >> 2)) * 2) = (u32x4){k3[0][0], k3[0][2], k3[1][0], k3[1][2]};
      lf_set(12 + pw);
      __builtin_amdgcn_s_setprio(3);
      STAMPF(18);
      asm volatile("" : "+v"(sv[0]), "+v"(sv[1]), "+v"(sv[2]), "+v"(sv[3]));
      if (!s_ok()) {
        _Pragma("unroll 1") for (unsigned spins = 0; spins < SPIN_CAP; ++spins) {
          __builtin_amdgcn_s_sleep(4);
          ld_e();
          if (s_ok()) break;
        }
      }
      asm volatile("" ::: "memory");
      SCHED();
      STAMPF(17);
#pragma unroll
      for (int s = 0; s < 4; ++s) qv[s] = ld16p(rs, WS_QB + yo + (unsigned)s * (32u * 20480u));
      hold(M_T);
      mv[0] = ld8(rs, mo); mv[1] = ld8(rs, mo + 20480u);
      SCHED();
      STAMP(2);
      u32x2 yv[4][2];
      auto ld_y = [&](int s) {
#pragma unroll
        for (int e = 0; e < 2; ++e) yv[s][e] = trd(yr + 1024 * s + 32 * e);
      };
      lf_wait(20, 4);
      ld_y(0); ld_y(1);
      SCHED();
      asm volatile("" : "+v"(mv[0]), "+v"(mv[1]), "+v"(qv[0]), "+v"(qv[1]), "+v"(qv[2]), "+v"(qv[3]));
      if (!__all(((mv[0][0] ^ tag) | (mv[1][0] ^ tag)) == 0u)) {
        _Pragma("unroll 1") for (unsigned spins = 0; spins < SPIN_CAP; ++spins) {
          __builtin_amdgcn_s_sleep(4);
          mv[0] = ld8(rs, mo); mv[1] = ld8(rs, mo + 20480u);
          if (__all(((mv[0][0] ^ tag) | (mv[1][0] ^ tag)) == 0u)) break;
        }
      }
      STAMPF(13);
      f32x4 al, be;
      {
        const float mgf = rbf(sum_rows(__uint_as_float(mv[0][1]) + __uint_as_float(mv[1][1])));
        auto rl = [&](int i) { return __uint_as_float(__builtin_amdgcn_readlane(__float_as_uint(mgf), i)); };
        const f32x4 sa = {rl(0), rl(1), rl(2), rl(3)}, ca = {rl(4), rl(5), rl(6), rl(7)}, ga = {rl(8), rl(9), rl(10), rl(11)};
        al = rbf4(ca + 1.0f) * ga;
        be = sa * ga;
      }
      f32x4 qf[2][2], P[2][2];
      auto xpre = [&](int s) {
#pragma unroll
        for (int e = 0; e < 2; ++e) {
          const float r1 = __builtin_amdgcn_rcpf(bfhi(sv[s][2 * e])), m1 = -bflo(sv[s][2 * e]) * r1;
          const f32x4 qq = {bflo(qv[s][2 * e]), bfhi(qv[s][2 * e]), bflo(qv[s][2 * e + 1]), bfhi(qv[s][2 * e + 1])};
          qf[s & 1][e] = qq;
          P[s & 1][e] = __builtin_elementwise_fma(__builtin_elementwise_fma(qq, (f32x4){r1, r1, r1, r1}, (f32x4){m1, m1, m1, m1}), al, be);
        }
        asm volatile("" : "+v"(qf[s & 1][0]), "+v"(qf[s & 1][1]), "+v"(P[s & 1][0]), "+v"(P[s & 1][1]));
      };
      auto xfin = [&](int s) {
        u32x4 o;
#pragma unroll
        for (int e = 0; e < 2; ++e) {
          const u32x2 y = yv[s][e];
          const f32x4 yf = {bflo(y[0]), bfhi(y[0]), bflo(y[1]), bfhi(y[1])};
          const f32x4 x = __builtin_elementwise_fma(P[s & 1][e], yf, qf[s & 1][e]);
          o[2 * e] = pack2(x[0], x[1]);
          o[2 * e + 1] = pack2(x[2], x[3]);
        }
        st16(rs, WS_X1B + yo + (unsigned)s * (32u * 20480u), o);
      };
      xpre(0); xfin(0);
      xpre(1); xfin(1);
      SCHED();
      xpre(2); xpre(3);
      lf_wait(24, 4);
      __builtin_amdgcn_s_setprio(3);
      ld_y(2); ld_y(3);
      xfin(2); xfin(3);
      STAMP(28);
      asm volatile("s_waitcnt vmcnt(2)" ::: "memory");
      xflag(l >> 5);
      asm volatile("s_waitcnt vmcnt(1)" ::: "memory");
      xflag(2 + (l >> 5));
      STAMP(5);
    } else {
      const int ch = w;
      char* U = lds + L_U + ch * U_CH;
      char* G = lds + ch * (4 * G_COPY);
      {
        const rsrc_t rf = __builtin_amdgcn_make_buffer_rsrc((void*)(p.ssm_k + (size_t)(quad * 4 + ch) * S_), 0, S_ * 2, 0x00020000);
        u32x4 fa[4];
        u32x2 fb[4];
        __builtin_amdgcn_s_setprio(3);
#pragma unroll
        for (int k = 0; k < 4; ++k) {
          fa[k] = __builtin_amdgcn_raw_buffer_load_b128(rf, l16 + (unsigned)k * 1024u, 0, NT);
        }
        __builtin_amdgcn_s_setprio(1);
        TAG_READY();
#pragma unroll
        for (int k = 0; k < 4; ++k) {
#pragma unroll
          for (int d = 0; d < 2; ++d)
            fb[k][d] = __builtin_amdgcn_update_dpp(k < 3 ? (unsigned)__builtin_amdgcn_readfirstlane(fa[k < 3 ? k + 1 : k][d]) : 0u, fa[k][d], 0x130, 0xf, 0xf, false);
          const unsigned x[6] = {fa[k][0], fa[k][1], fa[k][2], fa[k][3], fb[k][0], fb[k][1]};
          unsigned y[5];
#pragma unroll
          for (int d = 0; d < 5; ++d) y[d] = __builtin_amdgcn_alignbit(x[d + 1], x[d], 16);
          char* gp = G + 32 + k * 1024 + l16;
          *(u32x4*)(gp) = fa[k];
          *(u32x4*)(gp + G_COPY) = (u32x4){y[0], y[1], y[2], y[3]};
          *(u32x4*)(gp + 2 * G_COPY) = (u32x4){x[1], x[2], x[3], x[4]};
          *(u32x4*)(gp + 3 * G_COPY) = (u32x4){y[1], y[2], y[3], y[4]};
          if (k == 0) {
            const unsigned f0 = __builtin_amdgcn_readfirstlane(x[0]), f1 = __builtin_amdgcn_readfirstlane(x[1]);
            const unsigned long long f64 = (unsigned long long)f0 | ((unsigned long long)f1 << 32);
            const unsigned long long hi = l ? (f64 << (64 - 16 * (l & 3))) : 0ull;
            if (l < 4) {
              *(u32x4*)(G + l * G_COPY) = (u32x4){0u, 0u, 0u, 0u};
              *(u32x4*)(G + l * G_COPY + 16) = (u32x4){0u, 0u, (unsigned)hi, (unsigned)(hi >> 32)};
            }
            if (l >= 32) *(u32x4*)(U + (l - 32) * 16) = (u32x4){0u, 0u, 0u, 0u};
          }
        }
        asm volatile("s_waitcnt lgkmcnt(0)" ::: "memory");
        STAMP(1);
      }
      const int a = n16, h = h4, jj = h >> 1, e2 = h & 1;
      const char* ap = G + ((a + 1) & 3) * G_COPY + 2 * (16 * jj + a - 8 * e2 + 9 - ((a + 1) & 3));
      const char* bp = U + 512 + 32 * (a - jj) + 16 * e2;
      auto lda = [&](u32x4* A, int r, int p0, int np) {
#pragma unroll
        for (int pp = 0; pp < np; ++pp) {
          u32x2 lo = *(const volatile LAS u32x2*)(ap + 512 * (p0 + pp) + 64 * r);
          u32x2 hi = *(const volatile LAS u32x2*)(ap + 512 * (p0 + pp) + 64 * r + 8);
          A[pp] = (u32x4){lo[0], lo[1], hi[0], hi[1]};
        }
      };
      u32x4 AD;
      {
        const unsigned dDr = (*(cptr_t)(p.ssm_D + quad * 4 + (ch & 2)) >> (16 * (ch & 1))) & 0xffffu;
        const unsigned dv = h == (a >> 3) ? ((a & 1) ? dDr : dDr << 16) : 0u;
        const int dw = (7 - (a & 7)) >> 1;
#pragma unroll
        for (int d = 0; d < 4; ++d) AD[d] = dw == d ? dv : 0u;
      }
      u32x4 A0[8][4];
      f32x4 acc[8];
#pragma unroll
      for (int n = 0; n < 8; ++n) acc[n] = (f32x4){0.f, 0.f, 0.f, 0.f};
#pragma unroll
      for (int r = 0; r < 8; ++r) lda(A0[r], r, 0, 4);
      auto yput = [&](int s) {
#pragma unroll
        for (int e = 0; e < 2; ++e)
          *(u32x2*)(U + 1024 * s + 512 * e + (16 * a + 4 * h) * 2) = (u32x2){pack2(acc[2 * s + e][0], acc[2 * s + e][1]), pack2(acc[2 * s + e][2], acc[2 * s + e][3])};
        lf_set(16 + 4 * s + ch);
      };
      auto stage = [&](auto S__) {
        constexpr int s = decltype(S__)::value, NH = s < 2 ? 4 - 2 * s : 0, NP = 8 - 2 * s, NB = s < 2 ? 2 : 8;
        u32x4 A1[3][NH > 0 ? NH : 1], B[NB][2];
        auto ldb = [&](int r) {
#pragma unroll
          for (int e = 0; e < 2; ++e) B[r % NB][e] = *(const u32x4*)(bp + 512 * (2 * s + e) - 64 * r);
        };
        if (NH) { lda(A1[0], 0, 4, NH); lda(A1[1], 1, 4, NH); }
        SCHED();
        if (s > 0) yput(s - 1);
        lf_wait(4 * s, 4);
        if (s == 0) STAMP(3);
#pragma unroll
        for (int r = 0; r < NB; ++r) ldb(r);
        SCHED();
#pragma unroll
        for (int e = 0; e < 2; ++e) acc[2 * s + e] = MFMA16(AD, B[0][e], acc[2 * s + e]);
#pragma unroll
        for (int r = 0; r < 8; ++r) {
          if (NH && r + 2 < 8) lda(A1[(r + 2) % 3], r + 2, 4, NH);
          SCHED();
#pragma unroll
          for (int e = 0; e < 2; ++e)
#pragma unroll
            for (int pp = 0; pp < NP - e; ++pp) acc[2 * s + e + pp] = MFMA16(pp < 4 ? A0[r][pp] : A1[r % 3][pp < 4 ? 0 : pp - 4], B[r % NB][e], acc[2 * s + e + pp]);
          SCHED();
          if (r + NB < 8) ldb(r + NB);
        }
      };
      stage(IC<0>());
      STAMP(4);
      stage(IC<1>());
      STAMPF(9);
      const unsigned fo[1] = {fwi + (unsigned)((8 + c) * 20 + 5 * w + (l < 5 ? l : 0)) * 128u};
      unsigned fv = ~tag;
      {
        u32x4 B[4][4];
        auto ldb = [&](int r) {
#pragma unroll
          for (int m = 0; m < 4; ++m) B[r & 3][m] = *(const u32x4*)(bp + 512 * (4 + m) - 64 * r);
        };
        yput(1);
        if (w == 3) wo_ld();
        __builtin_amdgcn_s_setprio(3);
        lf_wait(8, 8);
        STAMP(22);
#pragma unroll
        for (int r = 0; r < 4; ++r) ldb(r);
        SCHED();
#pragma unroll
        for (int m = 0; m < 4; ++m) acc[4 + m] = MFMA16(AD, B[0][m], acc[4 + m]);
#pragma unroll
        for (int r = 0; r < 8; ++r) {
#pragma unroll
          for (int m = 0; m < 4; ++m)
#pragma unroll
            for (int pp = 0; pp < 4 - m; ++pp) acc[4 + m + pp] = MFMA16(A0[r][pp], B[r & 3][m], acc[4 + m + pp]);
          SCHED();
          if (r + 4 < 8) ldb(r + 4);
          if (r == 1) fv = ld4(rs, fo[0]);
          SCHED();
        }
#pragma unroll
        for (int e = 0; e < 4; ++e)
          *(u32x2*)(U + 2048 + 512 * e + (16 * a + 4 * h) * 2) = (u32x2){pack2(acc[4 + e][0], acc[4 + e][1]), pack2(acc[4 + e][2], acc[4 + e][3])};
        lf_set(24 + ch);
      }
      STAMP(28);
      __builtin_amdgcn_s_setprio(0);
      if (!__all(fv == tag)) wait_lines<1, 4>(rs, fo, tag);
      lf_wait(20, 4);
    }
    __builtin_amdgcn_s_setprio(0);
  } else {
    const int at = idx - 20;
    char* const scr = lds + WBLK + 14848;
    auto gv_blk = [&](int j) { return j < 160 ? 30 + (j >> 3) : j < 400 ? (j >> 3) - 20 : j >> 3; };
    auto gv_fin = [&](const u32x4* pre, int j) {
      float acc[8];
      gemv_fin(acc, pre, 10, scr);
      gemv_put(acc, j < 160 ? WS_MODP + (unsigned)((j & 7) * 1280 + (j >> 3) * 64) * 8u : WS_MODQ + (unsigned)((j & 7) * 2560 + ((j >> 3) - 20) * 64) * 8u);
    };
    const int ja = xcd * 12 + at, ai = xcd * 8 + at, bi = xcd * 4 + at - 8, g = w >> 2, wj = w & 3;
    const int rj = w == 0 ? ja : w == 1 ? (at >= 4 ? 92 + ai : -1) : w == 4 ? 160 + ja : w == 5 && at < 8 ? 256 + ai : -1;
    char* const rst = lds + (g ? WBLK : 65536) + wj * 7424;
    u32x4 pre[11];
    if (at >= 8 || g == 0) {
      TOUCH_AT(2);
      const int T = at >= 8 ? 64 + 2 * bi + g : ai;
      unsigned* mds = (unsigned*)(lds + 2048) + 16 * g;
      char* qt = lds + L_RT + g * (16 * 1312);
      u32x4 ld[5];
      const bool e1 = at >= 8 && w == 3;
      if (e1) gemv_load(pre, gv_blk(128 + bi), bi & 7, 10);
      else __builtin_amdgcn_s_sleep(8);
#pragma unroll
      for (int j = 0; j < 5; ++j)
        ld[j] = __builtin_amdgcn_raw_buffer_load_b128(rq, (unsigned)(h4 * D_ * 2 + n16 * 16 + j * 256), (unsigned)((32 * (T >> 1) + 8 * wj + 4 * (T & 1)) * D_ * 2), NT);
      TAG_READY();
      {
        const bf2_t ONE2 = {(__bf16)1.0f, (__bf16)1.0f};
        char* dst = qt + (4 * wj + h4) * 1312 + n16 * 16;
        float sa = 0.f, va = 0.f;
#pragma unroll
        for (int j = 0; j < 5; ++j) {
          *(u32x4*)(dst + j * 256) = ld[j];
#pragma unroll
          for (int d = 0; d < 4; ++d) {
            const unsigned a0 = ld[j][d];
            const bf2_t xa = __builtin_bit_cast(bf2_t, a0);
            sa = __builtin_amdgcn_fdot2_f32_bf16(xa, ONE2, sa, false); va = __builtin_amdgcn_fdot2_f32_bf16(xa, xa, va, false);
          }
        }
        lf_set(8 + w);
        const float ma = sum16(sa) * (1.0f / D_);
        va = __builtin_fmaxf(sum16(va) * (1.0f / D_) - ma * ma, 0.f);
        if (n16 == 0) mds[4 * wj + h4] = pack2(ma, __builtin_amdgcn_sqrtf(rbf(rbf(va) + 1e-5f)));
      }
      if (e1) gv_fin(pre, 128 + bi);
      lf_wait(8 + 4 * g, 4);
      STAMP(23);
      {
        const char* src = qt + (l & 7) * 1312 + (40 * wj + (l >> 3)) * 8;
#pragma unroll
        for (int j = 0; j < 5; ++j) {
          const u32x2 x = *(const u32x2*)(src + j * 64), y = *(const u32x2*)(src + j * 64 + 8 * 1312);
          st16(rs, WS_QB + (unsigned)T * 20480u + (unsigned)(5 * wj + j) * 1024u + l16, (u32x4){x[0], x[1], y[0], y[1]});
        }
      }
      SCHED();
      const int g2 = at < 8 || w < 3 || w == 4 ? 400 : w == 3 ? 384 + bi : 256 + wj * 32 + bi;
      if (g2 < 400) {
        gemv_load(pre, gv_blk(g2), g2 & 7, 10);
        asm volatile("s_waitcnt vmcnt(10)" ::: "memory");
      } else if (w < 2 && rj >= 0) {
        rt_ld(rj, rst);
        asm volatile("s_waitcnt vmcnt(7)" ::: "memory");
      } else {
        DRAIN();
      }
      lf_set(16 + w);
      if (wj == 2) {
        lf_wait(16 + 4 * g, 4);
        st16(rs, WS_ST1 + (unsigned)(l >> 3) * 16384u + (unsigned)T * 128u + (unsigned)(l & 7) * 16u, (u32x4){mds[l & 7], tag, mds[8 + (l & 7)], tag});
        STAMP(22);
      }
      if (w >= 4 && w < 7) lf_set(w);
      if (g2 < 400) gv_fin(pre, g2);
      STAMP(25);
    } else {
      constexpr int KB = L_RT + 16 * 1312;
      TOUCH_AT(3);
      const int g1 = w < 6 ? 2 * ai + wj : 32 + wj * 64 + ai;
      if (w >= 6) {
        const rsrc_t rk = __builtin_amdgcn_make_buffer_rsrc((void*)(w == 6 ? p.k : p.v), 0, S_ * D_ * 2, 0x00020000);
        const unsigned ko = (unsigned)((1536 + 8 * ai) * (D_ * 2) - (w == 6 ? 4096 : 0));
        char* dst = lds + KB + (w == 6 ? 0 : 14336);
        __builtin_amdgcn_s_sleep(3);
#pragma unroll
        for (int n = 0; n < 10; ++n) __builtin_amdgcn_raw_ptr_buffer_load_lds(rk, (LAS void*)(dst + n * 1024), 16, l16, ko + (unsigned)n * 1024u, 0, NT);
        if (w == 6) {
#pragma unroll
          for (int n = 10; n < 14; ++n) __builtin_amdgcn_raw_ptr_buffer_load_lds(rk, (LAS void*)(dst + n * 1024), 16, l16, ko + (unsigned)n * 1024u, 0, NT);
        }
      }
      SCHED();
      u32x4 swt[2];
      swt[0] = *(const u32x4*)(p.shift_k + (tid - 256) * 8);
      swt[1] = *(const u32x4*)(p.shift_k + (256 + l) * 8);
      SCHED();
      if (w >= 6) __builtin_amdgcn_s_sleep(8);
      gemv_load(pre, gv_blk(g1), g1 & 7, 10);
      TAG_READY();
      asm volatile("s_waitcnt vmcnt(11)" : "+v"(swt[0]), "+v"(swt[1]) :: "memory");
      lf_set(8 + w);
      if (w < 6) gv_fin(pre, g1);
      STAMP(25);
      lf_wait(12, 4);
      STAMP(23);
#pragma unroll 1
      for (int n = 0; n < (w == 7 ? 2 : 1); ++n) {
        {
          const int pr = n ? 256 + l : tid - 256;
          const char* kp = lds + KB + 4096 + pr * 4;
          unsigned kk[11], vv[8];
#pragma unroll
          for (int i = 0; i < 11; ++i) kk[i] = *(const unsigned*)(kp + (i - 3) * 1280);
#pragma unroll
          for (int i = 0; i < 8; ++i) vv[i] = *(const unsigned*)(kp + 10240 + i * 1280);
          const u32x4 sw = n ? swt[1] : swt[0];
          const f32x2 wt[4] = {{bflo(sw[0]), bflo(sw[2])}, {bfhi(sw[0]), bfhi(sw[2])}, {bflo(sw[1]), bflo(sw[3])}, {bfhi(sw[1]), bfhi(sw[3])}};
          f32x2 o[8];
#pragma unroll
          for (int i = 0; i < 8; ++i) {
            f32x2 t = (f32x2){bflo(kk[i + 3]), bfhi(kk[i + 3])} * wt[0];
#pragma unroll
            for (int j = 1; j < 4; ++j) t = __builtin_elementwise_fma((f32x2){bflo(kk[i + 3 - j]), bfhi(kk[i + 3 - j])}, wt[j], t);
            o[i] = t * (f32x2){bflo(vv[i]), bfhi(vv[i])};
          }
#pragma unroll
          for (int e = 0; e < 4; ++e) {
            const int i = 7 - 4 * (e & 1);
            st16(rs, WS_KV3 + (unsigned)ai * 20480u + (unsigned)(e * 5120 + pr * 16),
                 (u32x4){pack2(o[i][e >> 1], o[i - 1][e >> 1]), tag, pack2(o[i - 2][e >> 1], o[i - 3][e >> 1]), tag});
          }
        }
      }
      STAMP(22);
      if (w < 7) lf_set(w);
      if (w >= 6) gv_fin(pre, g1);
      STAMPF(30);
    }
    if (w == 6 && ja < 80) {
      hold(AUX_T);
      gemv_load(pre, gv_blk(400 + ja), ja & 7, 10);
      gv_fin(pre, 400 + ja);
    }
    if (rj >= 0) {
      if (w >= 2) { hold(RT2_T); rt_ld(rj, rst); }
      rt_job(rj, rst);
    }
    STAMP(26);
    lf_set(w);
    if (w == 3) { hold(WA_T); wo_ld(); }
    STAMP(3);
    if (w < 4) {
      {
        const unsigned fo[1] = {l < 5 ? fwi + (unsigned)((8 + c) * 20 + 5 * w + l) * 128u : ~0u};
        hold(W_T);
        wait_lines<1, 8>(rs, fo, tag);
      }
      lf_wait(0, 8);
      STAMP(4);
    }
    if ((at == 8 || at == 9) && w == 5 && xcd * 2 + at - 8 < 10) {
      const unsigned o = WS_MODQ + (unsigned)(h4 * 5120 + 1920 + (xcd * 2 + at - 8) * 64 + n16 * 4) * 8u;
      u32x4 v[4];
      hold(CS_T);
      _Pragma("unroll 1") for (unsigned spins = 0; spins < SPIN_CAP; ++spins) {
        unsigned dd = 0u;
#pragma unroll
        for (int k = 0; k < 4; ++k) v[k] = ld16(rs, o + (unsigned)((k >> 1) * 20480 + (k & 1) * 16));
#pragma unroll
        for (int k = 0; k < 4; ++k) dd |= (v[k][0] ^ tag) | (v[k][2] ^ tag);
        if (__all(dd == 0u)) break;
        __builtin_amdgcn_s_sleep(16);
      }
      float t[4];
#pragma unroll
      for (int k = 0; k < 4; ++k) t[k] = sum_rows(__uint_as_float(v[k >> 1][1 + 2 * (k & 1)]) + __uint_as_float(v[2 + (k >> 1)][1 + 2 * (k & 1)]));
#pragma unroll
      for (int e = 0; e < 2; ++e)
        st16(rs, WS_MOD + (unsigned)(4 * e + h4) * 2560u + (unsigned)((xcd * 2 + at - 8) * 16 + n16) * 16u, (u32x4){tag, pack2(t[0], t[1]), tag, pack2(t[2], t[3])});
    }
    if (w == 4 || (w == 5 && at >= 10)) {
      const int cj = w == 4 ? ja : 86 + xcd * 2 + at, cb = cj / 7, j = cj % 7;
      const unsigned o = WS_CSP + (unsigned)((cb * 20 + 5 * h4) * 2 + (j >> 2)) * 1024u + (unsigned)((j & 3) * 16 + n16) * 16u;
      u32x4 v[5];
      hold(CS_T);
      __builtin_amdgcn_s_setprio(3);
      _Pragma("unroll 1") for (unsigned spins = 0; spins < SPIN_CAP; ++spins) {
        unsigned dd = 0u;
#pragma unroll
        for (int k = 0; k < 5; ++k) v[k] = ld16(rs, o + (unsigned)k * 2048u);
#pragma unroll
        for (int k = 0; k < 5; ++k) dd |= (v[k][0] ^ tag) | (v[k][2] ^ tag);
        if (__all(dd == 0u)) break;
        __builtin_amdgcn_s_sleep(8);
      }
      float t0 = 0.f, t1 = 0.f;
#pragma unroll
      for (int k = 0; k < 5; ++k) { t0 += __uint_as_float(v[k][1]); t1 += __uint_as_float(v[k][3]); }
      t0 = sum_rows(t0); t1 = sum_rows(t1);
      if (l < 16) st16(rs, WS_CS + (unsigned)(cb * 2048 + j * 256) + l16, (u32x4){tag, __float_as_uint(t0), tag, __float_as_uint(t1)});
      __builtin_amdgcn_s_setprio(0);
      STAMP(24);
    }
  }

  STAMPF(16);
  u32x4 wox[5];
  DRAIN();
  if (w == 3) {
    const char* bp = wo_st + (4 * h4 + (n16 >> 2)) * 160 + (n16 & 3) * 8;
#pragma unroll
    for (int dj = 0; dj < 5; ++dj) wox[dj] = trfrag(bp + dj * 32, 160);
    asm volatile("s_waitcnt lgkmcnt(0)" : "+v"(wox[0]), "+v"(wox[1]), "+v"(wox[2]), "+v"(wox[3]), "+v"(wox[4]) :: "memory");
  }
  auto wo_put = [&]() {
    if (w == 3 && R < 28) {
      const unsigned base = WS_WOUTF + (unsigned)c * WBLK + (unsigned)((R >> 1) * 10240 + (R & 1) * 1024);
#pragma unroll
      for (int dj = 0; dj < 5; ++dj) st16(rs, base + (unsigned)dj * 2048u + l16, wox[dj]);
    }
  };
  if (w < 4) load_b2();
  STAMPF(29);
  u32x4 k_c;
  unsigned kc_o;
  {
    const int i = tid < 224 ? tid : 0;
    const int col = i >= 112 ? (8 + c) * 128 + i - 112 : c * 128 + i;
    kc_o = w < 4 ? WS_CS + (unsigned)col * 16u : WS_MOD + (unsigned)xcd * 2560u + (unsigned)(20 * c + (tid < 276 ? tid - 256 : 0)) * 16u;
  }
  auto consts_put = [&]() {
    if (!__all(k_c[0] == tag && k_c[2] == tag)) {
      _Pragma("unroll 1") for (unsigned spins = 0; spins < SPIN_CAP; ++spins) {
        __builtin_amdgcn_s_sleep(8);
        k_c = ld16(rs, kc_o);
        if (__all(k_c[0] == tag && k_c[2] == tag)) break;
      }
    }
    if (tid < 224) *(u32x2*)(lds + L_CONST + tid * 8) = (u32x2){k_c[1], k_c[3]};
    if (tid >= 256 && tid < 276) *(u32x2*)(lds + L_MISC + (tid - 256) * 8) = (u32x2){k_c[1], k_c[3]};
  };
  const int rt1 = w & 3, kh1 = w >> 2;
  unsigned bo0 = (unsigned)(kh1 * 71680) + l16, bo1 = bo0 + 35840u;
  asm volatile("" : "+v"(bo0), "+v"(bo1));
#define BFRAG(q, j) (*(const u32x4*)(lds + ((q) < 5 ? bo0 : bo1) + ((q) % 5) * 7168 + (j) * 1024))
  u32x4 h2[10];
  f32x4 acc[7];
  float mu_n, rs_n;
  {
    if (w == 6) {
      const unsigned fo[1] = {l < 20 ? fwi + (unsigned)(c * 20 + l) * 128u : ~0u};
      hold(CS_T);
      wait_lines<1, 8>(rs, fo, tag);
    }
    if (w == 7) {
      const unsigned fb = WS_FX + (unsigned)(((R >> 1) * 4 + (R & 1) * 2 + (c >> 2)) * 160 + l) * 128u;
      const unsigned fo[3] = {fb, fb + 8192u, l < 32 ? fb + 16384u : ~0u};
      if (!is_conv) hold(Y_T);
      wait_lines<3, 2>(rs, fo, tag);
      if (l == 0) lf[33] = tag;
    }
    lf_wait(33, 1);
    STAMP(7);
    const unsigned xo = (unsigned)(R * 4 + rt1) * 20480u + (unsigned)(10 * kh1) * 1024u +
                        (unsigned)((4 * ((l >> 3) & 1) + h4) * 128 + (l & 7) * 16);
    SCHED();
#pragma unroll
    for (int q = 0; q < 3; ++q) h2[q] = ld16p(rs, WS_X1B + xo + (unsigned)q * 1024u);
    SCHED();
    __builtin_amdgcn_s_waitcnt(0x0F73);
    BAR();
    STAMP(8);
#pragma unroll
    for (int j = 0; j < 7; ++j) acc[j] = (f32x4){0.f, 0.f, 0.f, 0.f};
    f32x4 accg = {0.f, 0.f, 0.f, 0.f};
    u32x4 bf[7];
#pragma unroll
    for (int j = 0; j < 7; ++j) bf[j] = BFRAG(0, j);
    SCHED();
#pragma unroll
    for (int q = 3; q < 6; ++q) h2[q] = ld16p(rs, WS_X1B + xo + (unsigned)q * 1024u);
    SCHED();
#pragma unroll
    for (int q = 0; q < 10; ++q) {
      {
#pragma unroll
        for (int e = 0; e < 6; ++e) {
          const int d = e & 1;
          if ((e >> 1) == 1) h2[q][2 + d] ^= (unsigned)__builtin_amdgcn_update_dpp(0, (int)h2[q][d], 0x128, 0xf, 0x3, false);
          else h2[q][d] ^= (unsigned)__builtin_amdgcn_update_dpp(0, (int)h2[q][2 + d], 0x128, 0xf, 0xC, false);
        }
      }
      if (q == 5) { __builtin_amdgcn_s_barrier(); asm volatile("" ::: "memory"); }
      SCHED();
      accg = MFMA16(h2[q], h2[q], accg);
      SCHED();
#pragma unroll
      for (int j = 0; j < 7; ++j) {
        acc[j] = MFMA16(h2[q], bf[j], acc[j]);
        if (q == 9 && j == 1 && h4 == (n16 >> 2)) *(f32x4*)(lds + L_PST + 512 + (w * 16 + n16) * 16) = accg;
        if (q < 9) bf[j] = BFRAG(q + 1, j);
        if (q >= 2 && j == 2) LDW(q - 2);
        if ((j == 0 || j == 4) && 6 + 2 * q + (j >> 2) < 10) h2[6 + 2 * q + (j >> 2)] = ld16p(rs, WS_X1B + xo + (unsigned)(6 + 2 * q + (j >> 2)) * 1024u);
        if (q == 4 && j == 0) k_c = ld16(rs, kc_o);
        if (q >= 5 && j == 5) {
          const int i = (5 + q) * 7 + (w < 7 ? w : 0);
          dma1k(rs, lds + i * 1024, l16, WS_WINF + (unsigned)c * WBLK + (unsigned)i * 1024u);
          if (q == 9) { const int i2 = 28 + (w < 7 ? w : 0); dma1k(rs, lds + i2 * 1024, l16, WS_WINF + (unsigned)c * WBLK + (unsigned)i2 * 1024u); }
        }
        SCHED();
      }
    }
    STAMP(31);
  }
  auto gelu_n = [&](f32x2* x, auto N_) {
    constexpr int N = decltype(N_)::value;
    const float c0 = -2.0f * 0.7978845608028654f * 1.4426950408889634f, c1 = c0 * 0.044715f;
    f32x2 k0 = {c0, c0}, k1 = {c1, c1}, k2 = {1.0f, 1.0f};
    asm volatile("" : "+v"(k0), "+v"(k1), "+v"(k2));
    f32x2 t[N];
#define GST(e) do { _Pragma("unroll") for (int i = 0; i < N; ++i) { t[i] = (e); asm volatile("" : "+v"(t[i])); } } while (0)
    GST(x[i] * x[i]);
    GST(__builtin_elementwise_fma(t[i], k1, k0));
    GST(t[i] * x[i]);
    GST(((f32x2){__builtin_amdgcn_exp2f(t[i][0]), __builtin_amdgcn_exp2f(t[i][1])}));
    GST(t[i] + k2);
    GST(((f32x2){__builtin_amdgcn_rcpf(t[i][0]), __builtin_amdgcn_rcpf(t[i][1])}));
#pragma unroll
    for (int i = 0; i < N; ++i) { x[i] *= t[i]; asm volatile("" : "+v"(x[i])); }
  };
  {
    f32x2* red = (f32x2*)lds;
    f32x2 mine[7];
    if (kh1) {
#pragma unroll
      for (int j = 0; j < 7; ++j) { red[(w * 7 + j) * 64 + l] = (f32x2){acc[j][0], acc[j][1]}; mine[j] = (f32x2){acc[j][2], acc[j][3]}; }
    } else {
#pragma unroll
      for (int j = 0; j < 7; ++j) { red[(w * 7 + j) * 64 + l] = (f32x2){acc[j][2], acc[j][3]}; mine[j] = (f32x2){acc[j][0], acc[j][1]}; }
    }
    f32x2 csb[7];
    unsigned cso = (unsigned)(L_CONST + (112 + n16) * 8);
    asm volatile("" : "+v"(cso));
    consts_put();
    unsigned pso[3], pqo[3], oto = (unsigned)(((w ^ 4) * 7 * 64 + l) * 8);
#pragma unroll
    for (int e = 0; e < 3; ++e) {
      const int row = rt1 * 16 + (e < 2 ? 4 * h4 + 2 * kh1 + e : n16);
      pso[e] = (unsigned)(L_PST + row * 4); pqo[e] = (unsigned)(L_PST + 512 + row * 16 + (row & 3) * 4);
    }
    asm volatile("" : "+v"(pso[0]), "+v"(pso[2]), "+v"(pqo[0]), "+v"(pqo[1]), "+v"(pqo[2]), "+v"(oto));
    pso[1] = pso[0] + 4u;
    if (n16 == 12) *(f32x4*)(lds + L_PST + (w * 16 + 4 * h4) * 4) = acc[6];
    if (!kh1) {
#pragma unroll
      for (int k = 8; k < 16; ++k) LDW(k);
    }
    BAR();
    if (kh1) {
#pragma unroll
      for (int k = 8; k < 16; ++k) LDW(k);
    }
    __builtin_amdgcn_s_setprio(3);
    STAMPT(9);
    f32x2 oth[7];
    f32x2 pa[3], pb[3];
#pragma unroll
    for (int e = 0; e < 3; ++e) {
      pa[e] = (f32x2){*(const float*)(lds + pso[e]), *(const float*)(lds + pqo[e])};
      pb[e] = (f32x2){*(const float*)(lds + pso[e] + 256), *(const float*)(lds + pqo[e] + 1024)};
    }
#pragma unroll
    for (int j = 0; j < 7; ++j) { oth[j] = *(const f32x2*)(lds + oto + j * 512); csb[j] = *(const f32x2*)(lds + cso + 128 * j); }
    SCHED();
    float mu2[2], rs2[2];
#pragma unroll
    for (int e = 0; e < 3; ++e) {
      const float mean = (pa[e][0] + pb[e][0]) * (1.0f / D_);
      const float var = __builtin_fmaxf((pa[e][1] + pb[e][1]) * (1.0f / D_) - mean * mean, 0.f);
      const float rstd = __builtin_amdgcn_rsqf(var + 1e-5f);
      if (e < 2) { mu2[e] = mean; rs2[e] = rstd; } else { mu_n = mean; rs_n = rstd; }
    }
    f32x2 zz[7];
    const f32x2 nmu = {-mu2[0], -mu2[1]}, rsv = {rs2[0], rs2[1]};
#pragma unroll
    for (int j = 0; j < 7; ++j)
      zz[j] = __builtin_elementwise_fma(__builtin_elementwise_fma(nmu, (f32x2){csb[j][0], csb[j][0]}, mine[j] + oth[j]), rsv, (f32x2){csb[j][1], csb[j][1]});
    gelu_n(zz, IC<7>());
    if (n16 >= 12) zz[6] = (f32x2){0.f, 0.f};
    f32x2 s1 = ((zz[0] + zz[1]) + (zz[2] + zz[3])) + ((zz[4] + zz[5]) + zz[6]);
    f32x2 s2 = zz[0] * zz[0], s3 = zz[1] * zz[1];
#pragma unroll
    for (int j = 2; j < 7; ++j) { if (j & 1) s3 = __builtin_elementwise_fma(zz[j], zz[j], s3); else s2 = __builtin_elementwise_fma(zz[j], zz[j], s2); }
    s2 += s3;
    asm("" : "+v"(s1), "+v"(s2));
    float a0 = s1[0] + dpp_f<0xB1>(s1[0]), a1 = s1[1] + dpp_f<0xB1>(s1[1]), b0 = s2[0] + dpp_f<0xB1>(s2[0]);
    a0 = (l & 1) ? a1 : a0;
    const float b1 = s2[1] + dpp_f<0xB1>(s2[1]);
    b0 = (l & 1) ? b1 : b0;
    a0 += dpp_f<0x4E>(a0); b0 += dpp_f<0x4E>(b0);
    a0 += dpp_f<0x124>(a0); b0 += dpp_f<0x124>(b0);
    a0 += dpp_f<0x128>(a0); b0 += dpp_f<0x128>(b0);
    {
      const int src = ((l >> 1) & 3) * 64 + (l & 1) * 4;
      const unsigned pa0 = __builtin_amdgcn_ds_bpermute(src, __float_as_uint(a0)), pb0 = __builtin_amdgcn_ds_bpermute(src, __float_as_uint(b0));
      if (l < 8) st16(rs, WS_PART + (unsigned)((R * 8 + c) * 8 + w) * 128u + l16, (u32x4){tag, pa0, tag, pb0});
    }
    SCHED();
    __builtin_amdgcn_s_setprio(0);
    STAMP(10);
#pragma unroll
    for (int k = 0; k < 10; ++k) {
      if (k < 9) {
        int x = k * 8 + w;
        x = x < 70 ? x : 69;
        const int i = x + (x < 35 ? 35 : 70);
        dma1k(rs, lds + i * 1024, l16, WS_WINF + (unsigned)c * WBLK + (unsigned)i * 1024u);
      }
    }
    wo_put();
#pragma unroll
    for (int j = 0; j < 7; ++j)
      *(unsigned*)(lds + L_Z + (((rt1 >> 1) * 7 + j) * 64 + l) * 16 + (4 * (rt1 & 1) + 2 * kh1) * 2) = pack2(zz[j][0], zz[j][1]);
    SCHED();
    u32x4 v[8];
    const unsigned po = WS_PART + (unsigned)(R * 8) * 1024u + l16;
#pragma unroll
    for (int g = 0; g < 4; ++g) WSP_TR(g);
    if (w == 2) {
      __builtin_amdgcn_s_setprio(3);
      unsigned need = 0xffu;
      if (need != 0u) {
        _Pragma("unroll 1") for (unsigned spins = 0; spins < SPIN_CAP; ++spins) {
#pragma unroll
          for (int k = 0; k < 8; ++k)
            if (need & (1u << k)) v[k] = ld16(rs, po + (unsigned)k * 1024u);
#pragma unroll
          for (int k = 0; k < 8; ++k)
            if (__all(v[k][0] == tag && v[k][2] == tag)) need &= ~(1u << k);
          if (need == 0u) break;
          __builtin_amdgcn_s_sleep(1);
        }
      }
      float t1 = 0.f, t2 = 0.f;
#pragma unroll
      for (int k = 0; k < 8; ++k) { t1 += __uint_as_float(v[k][1]); t2 += __uint_as_float(v[k][3]); }
      const float mean = t1 * (1.0f / FH);
      const float var = __builtin_fmaxf(t2 * (1.0f / FH) - mean * mean, 0.f);
      const float rstd = __builtin_amdgcn_rsqf(var + 1e-5f);
      const int sg = 16 * ((l >> 3) & 3) + 4 * ((l >> 1) & 3) + 2 * (l >> 5) + (l & 1);
      *(f32x2*)(lds + L_ST2 + sg * 8) = (f32x2){rstd, -mean * rstd};
    }
    STAMPT(11);
    DRAIN();
    if (w == 3) st_flag4(rs, WS_FWO + (unsigned)(c * 32 + R) * 128u, tag, l);
    BAR();
    __builtin_amdgcn_s_setprio(3);
    STAMP(12);
    u32x4 zf[2];
#pragma unroll
    for (int m = 0; m < 2; ++m) zf[m] = *(const u32x4*)(lds + L_Z + ((m * 7 + (w < 7 ? w : 0)) * 64 + l) * 16);
    if (w < 7) {
#pragma unroll
      for (int m = 0; m < 2; ++m) {
        const f32x4* sp = (const f32x4*)(lds + L_ST2 + (32 * m + 4 * h4) * 8);
        u32x4 o;
#pragma unroll
        for (int e = 0; e < 4; ++e) {
          f32x4 st = sp[(e & 1) + 8 * (e >> 1)];
          o[e] = pack2(__builtin_fmaf(bflo(zf[m][e]), st[0], st[1]), __builtin_fmaf(bfhi(zf[m][e]), st[2], st[3]));
        }
        st16(rs, WS_Z2F + (unsigned)((c * 64 + 4 * (R >> 1) + h4) * 7 + w) * 1024u + (unsigned)((2 * (R & 1) + m) * 256 + n16 * 16), o);
      }
    }
    SCHED();
    __builtin_amdgcn_s_setprio(0);
#pragma unroll
    for (int k = 0; k < 4; ++k)
      if (w < 7) dma1k(rs, lds + (k * 7 + w) * 1024, l16, WS_WINF + (unsigned)c * WBLK + (unsigned)(k * 7 + w) * 1024u);
    SCHED();
    STAMPT(13);
  }
  unsigned bs_raw;
  unsigned z1d[7];
  constexpr int L_RED = 28672;
#define WOA(i) (((i) < 28 ? (i) : (i) < 108 ? (i) + 32 : (i) < 120 ? (i) + 37 : (i) - 92) * 1024)
#define RBAR() do { __builtin_amdgcn_s_barrier(); asm volatile("" ::: "memory"); } while (0)
#define ZAUX 16
#define NW 14
  auto wo_dma = [&](int k) {
    int i = k < 8 ? (k + (idx >> 2)) & 7 : k + (idx >> 2);
    i = (i >= 15 ? i - 7 : i) * 8 + w;
    const int a = i + ((27 - i) >> 31 & 32) + ((107 - i) >> 31 & 5);
    dma1k(rs, lds + a * 1024, l16, WS_WOUTF + (unsigned)c * WBLK + (unsigned)i * 1024u);
  };
  unsigned zs[8];
#pragma unroll
  for (int t = 0; t < 8; ++t) zs[t] = WS_Z2F + (unsigned)(c * 64 + 8 * w + 4 * ((t >> 2) ^ rotg) + ((t + rotb) & 3)) * 7168u;
  u32x4 zw[NW];
#define LDZ(f) do { if (((f) >> 3) == 4) { zs[(f) & 7] += 4096u; asm volatile("" : "+s"(zs[(f) & 7])); } \
    zw[(f) % NW] = __builtin_amdgcn_raw_buffer_load_b128(rs, l16 + (unsigned)(((f) >> 3) & 3) * 1024u, zs[(f) & 7], ZAUX); } while (0)
  {
    f32x4 acc[7];
#pragma unroll
    for (int j = 0; j < 7; ++j) acc[j] = (f32x4){0.f, 0.f, 0.f, 0.f};
    u32x4 bf[7];
#pragma unroll
    for (int j = 0; j < 7; ++j) bf[j] = BFRAG(4, j);
#pragma unroll
    for (int qi = 0; qi < 10; ++qi) {
      const int q = (qi + 4) % 10, qn = (qi + 5) % 10;
      if (qi == 5) {
        asm volatile("s_waitcnt vmcnt(8)" ::: "memory");
        __builtin_amdgcn_s_barrier();
        asm volatile("" ::: "memory");
        if (w == 7) st_flag4(rs, WS_FZ + (unsigned)(c * 32 + R) * 128u, tag, l);
        STAMP(6);
#pragma unroll
        for (int k = 24; k < 32; ++k) LDW(k);
        bs_raw = p.b_sp[R * 64 + tau(rt1, n16)];
      }
      SCHED();
#pragma unroll
      for (int j = 0; j < 7; ++j) {
        acc[j] = MFMA16(bf[j], h2[q], acc[j]);
        if (qi < 9) bf[j] = BFRAG(qn, j);
        if (qi >= 1 && qi < 5 && (j == 1 || j == 4)) LDW(16 + 2 * (qi - 1) + (j >> 2));
        SCHED();
      }
    }
    STAMPT(29);
    f32x2* red = (f32x2*)(lds + L_RED);
    f32x2 mine[7];
    if (kh1) {
#pragma unroll
      for (int j = 0; j < 7; ++j) { red[(w * 7 + j) * 64 + l] = (f32x2){acc[j][0], acc[j][1]}; mine[j] = (f32x2){acc[j][2], acc[j][3]}; }
    } else {
#pragma unroll
      for (int j = 0; j < 7; ++j) { red[(w * 7 + j) * 64 + l] = (f32x2){acc[j][2], acc[j][3]}; mine[j] = (f32x2){acc[j][0], acc[j][1]}; }
    }
#define WOE 15
#define TRA 6
#define POLLP 1
#pragma unroll
    for (int g = 4; g < TRA; ++g) WSP_TR(g);
    if (WOE > 0 && w == 3) {
      const unsigned fo[1] = {l < 28 ? fwi + (WS_FWO - WS_FWI) + (unsigned)(c * 32 + l) * 128u : ~0u};
      wait_lines<1, 1>(rs, fo, tag);
    }
    BAR();
    STAMP(14);
    f32x2 oth[7];
    f32x4 cc[7];
#pragma unroll
    for (int j = 0; j < 7; ++j) {
      oth[j] = red[((w ^ 4) * 7 + j) * 64 + l];
      cc[j] = *(const f32x4*)(lds + L_CONST + (16 * j + 4 * h4 + 2 * kh1) * 8);
    }
    SCHED();
#pragma unroll
    for (int k = 0; k < WOE; ++k) wo_dma(k);
    SCHED();
    f32x2 z[7];
#pragma unroll
    for (int j = 0; j < 7; ++j) {
      z[j] = __builtin_elementwise_fma(__builtin_elementwise_fma((f32x2){-mu_n, -mu_n}, (f32x2){cc[j][0], cc[j][2]}, oth[j] + mine[j]),
                                       (f32x2){rs_n, rs_n}, (f32x2){cc[j][1], cc[j][3]});
      asm volatile("" : "+v"(z[j]));
    }
    SCHED();
    auto z_start = [&]() {
      const unsigned fo[1] = {l < 4 ? fz + (unsigned)(c * 32 + 4 * w + l) * 128u : ~0u};
      wait_lines<1, 1>(rs, fo, tag);
      STAMPT(23);
#pragma unroll
      for (int f = 0; f < NW; ++f) { LDZ(f); SCHED(); }
    };
    auto z1_fin = [&]() {
      gelu_n(z, IC<7>());
#pragma unroll
      for (int j = 0; j < 7; ++j) z1d[j] = pack2(z[j][0], z[j][1]);
      if (h4 == 3) z1d[6] = 0u;
      asm volatile("" : "+v"(z1d[0]), "+v"(z1d[1]), "+v"(z1d[2]), "+v"(z1d[3]), "+v"(z1d[4]), "+v"(z1d[5]), "+v"(z1d[6]));
      SCHED();
      STAMPT(30);
    };
    if (POLLP == 1) { z_start(); z1_fin(); }
    else if (POLLP == 0) { z1_fin(); z_start(); }
    else if (kh1) { z_start(); z1_fin(); }
    else { z1_fin(); z_start(); }
  }

  {
    SCHED();
#pragma unroll
    for (int g = TRA; g < 8; ++g) WSP_TR(g);
    SCHED();
    STAMP(20);
    const int rt = rt1, kh = kh1;
    unsigned rw = (unsigned)(L_RED + w * 512 + l * 8), rr = (unsigned)(L_RED + w * 4096 + l * 8);
    asm volatile("" : "+v"(rw), "+v"(rr));
    const float bs = bflo(bs_raw);
    const unsigned uo = WS_UF + (unsigned)((R * 4 + rt) * 8 + c) * 5120u + (unsigned)kh * 2560u;
    const unsigned ui = WS_UF + (unsigned)((R * 4 + rt) * 8 + 4 * kh) * 5120u;
    unsigned l8;
    f32x4 acc2[2][4];
    float rv[8][2];
    u32x4 ud;
    u32x4 P[2][8];
    u32x2 P3[8], xp[3];
    auto ld_p = [&](int pt, int i) { P[pt][i] = __builtin_amdgcn_raw_buffer_load_b128(rs, l16 + (unsigned)pt * 1024u, ui + (unsigned)i * 2560u, SC1); };
    auto ld_p3 = [&](int i) { P3[i] = __builtin_amdgcn_raw_buffer_load_b64(rs, l8 + 2048u, ui + (unsigned)i * 2560u, SC1); };
    auto red_put = [&](int j, int r) {
      *(f32x2*)(lds + rw + r * 4096) = (f32x2){acc2[j & 1][r][0], acc2[j & 1][r][1]};
      *(f32x2*)(lds + rw + (r + 4) * 4096) = (f32x2){acc2[j & 1][r][2], acc2[j & 1][r][3]};
    };
    auto red_put1 = [&](int j, int i) { *(volatile LAS float*)(lds + rw + (i >> 2) * 4096 + ((i >> 1) & 1) * 16384 + (i & 1) * 4) = acc2[j & 1][i >> 2][i & 3]; };
    auto red_get = [&](int i) { const f32x2 x = *(const volatile LAS f32x2*)(lds + rr + i * 512); rv[i][0] = x[0]; rv[i][1] = x[1]; };
    float zz[2], gg[2];
    auto red_fin = [&](int j, int k) {
      const int e = k & 1, i = k >> 1;
#define VADD(d, x, y) asm volatile("v_add_f32 %0, %1, %2" : "=v"(d) : "v"(x), "v"(y))
      if (i < 4) VADD(rv[2 * i][e], rv[2 * i][e], rv[2 * i + 1][e]);
      if (i == 4 || i == 5) VADD(rv[4 * (i & 1)][e], rv[4 * (i & 1)][e], rv[4 * (i & 1) + 2][e]);
      if (i == 6) VADD(rv[0][e], rv[0][e], bs);
      if (i == 7) VADD(gg[e], rv[0][e], rv[4][e]);
      if (i == 8) { zz[e] = e ? bfhi(z1d[j]) : bflo(z1d[j]); asm volatile("" : "+v"(zz[e])); }
      if (i == 9) { zz[e] *= gg[e]; asm volatile("" : "+v"(zz[e])); }
      if (k == 20) { ud[j % 3] = pack2(zz[0], zz[1]); asm volatile("" : "+v"(ud[j % 3])); }
      if (k == 21) {
        if (j % 3 == 0) ud[3] = ud[0] + tag; else ud[3] += ud[j % 3];
        asm volatile("" : "+v"(ud[3]));
        if (j == 6) __builtin_amdgcn_raw_buffer_store_b64((u32x2){ud[0], ud[3]}, rs, l8 + 2048u, uo, SC1);
        else if (j % 3 == 2) __builtin_amdgcn_raw_buffer_store_b128(ud, rs, l16 + (unsigned)(j / 3) * 1024u, uo, SC1);
      }
    };
#define G2S 0
    struct Sch { int wide, lp, prio, lf, b1, b2, wp[16], rp[8], vp[22]; };
    constexpr Sch SC[4] = {
      {0, 2, 8, 1, 18, 29, {0, 1, 2, 3, 4, 5, 6, 7, 8, 9, 10, 11, 12, 13, 14, 15}, {19, 19, 20, 20, 21, 21, 22, 22}, {29, 29, 30, 30, 31, 31, 32, 33, 35, 36, 37, 38, 39, 40, 41, 42, 43, 44, 45, 46, 47, 48}},
      {1, 3, 4, 0, 18, 28, {3, 7, 11, 15, 99, 99, 99, 99, 99, 99, 99, 99, 99, 99, 99, 99}, {20, 20, 21, 21, 22, 22, 24, 24}, {28, 28, 29, 29, 30, 30, 31, 31, 32, 32, 33, 33, 34, 34, 35, 36, 36, 37, 37, 38, 38, 39}},
      {0, 1, 4, 1, 17, 29, {0, 1, 2, 3, 4, 5, 6, 7, 8, 9, 10, 11, 12, 13, 14, 15}, {17, 17, 18, 18, 19, 19, 20, 20}, {29, 29, 30, 30, 31, 31, 32, 34, 35, 36, 38, 39, 40, 42, 43, 44, 45, 46, 47, 48, 48, 49}},
      {0, 2, 0, 1, 17, 25, {0, 1, 2, 3, 4, 5, 6, 7, 8, 9, 10, 11, 12, 13, 14, 15}, {17, 17, 18, 18, 19, 19, 20, 20}, {25, 25, 26, 26, 27, 27, 28, 28, 29, 29, 30, 30, 31, 31, 32, 33, 23, 24, 34, 35, 36, 37}}};
    constexpr Sch S = SC[G2S];
    sfor<0, 224>([&](auto M_) __attribute__((always_inline)) {
      constexpr int mm = decltype(M_)::value, f = mm >> 2, r = mm & 3, j = f >> 3, t = f & 7, m = mm & 31;
      if (t == 0) asm volatile("v_mfma_f32_16x16x32_bf16 %0, %1, %2, 0" : "=&v"(acc2[j & 1][r]) : "v"(zw[f % NW]), "v"(wsp[r][t]), "v"(zw[(f | 1) % NW]));
      else asm volatile("v_mfma_f32_16x16x32_bf16 %0, %1, %2, %0" : "+v"(acc2[j & 1][r]) : "v"(zw[f % NW]), "v"(wsp[r][t]), "v"(zw[(f | 1) % NW]));
      SCHED();
      if (S.prio && (m + 31 - S.lp) % (S.prio ? S.prio : 1) == 0) __builtin_amdgcn_s_setprio(1);
      auto ld = [&]() {
        if (r == S.lp) {
          if (S.prio && (m + 32 - S.lp) % (S.prio ? S.prio : 1) == 0) __builtin_amdgcn_s_setprio(0);
          constexpr int g = S.lp == 3 ? f : f - 1;
          if (g >= 0 && g + NW < 56) LDZ(g + NW);
          constexpr int n1 = WOE < 8 ? 8 - WOE : 0, n2 = WOE < 8 ? 7 : 15 - WOE, per = n1 + n2 + 8 > 13 ? 2 : 1, y0 = (g - (per == 2 ? 56 - NW : 54 - n1 - n2 - 8)) * per;
          sfor<0, per>([&](auto E_) __attribute__((always_inline)) {
            constexpr int y = y0 + decltype(E_)::value;
            if (y >= 0 && y < n1) wo_dma(WOE + y);
            if (y >= n1 && y < n1 + 8) ld_p(0, (y - n1) & 7);
            if (y >= n1 + 8 && y < n1 + 8 + n2) wo_dma(15 - n2 + y - n1 - 8);
          });
          SCHED();
        }
      };
      if (!S.lf) ld();
      if ((m == S.b1 && j > 0) || m == S.b2) { asm volatile("" ::: "memory"); __builtin_amdgcn_s_waitcnt(0xC07F); RBAR(); SCHED(); }
      sfor<0, 16>([&](auto I_) __attribute__((always_inline)) {
        constexpr int i = decltype(I_)::value;
        if (j > 0 && S.wp[i] == m) { if (S.wide) red_put(j + 1, i & 3); else red_put1(j + 1, i); SCHED(); }
        if (j > 0 && i < 8 && S.rp[i & 7] == m) { red_get(i & 7); SCHED(); }
      });
      sfor<0, 22>([&](auto K_) __attribute__((always_inline)) {
        constexpr int k = decltype(K_)::value;
        if (j > 0 && S.vp[k] == m) { red_fin(j > 0 ? j - 1 : 0, k); SCHED(); }
        if (j > 1 && S.vp[k] == m + 32) { red_fin(j > 1 ? j - 2 : 0, k); SCHED(); }
      });
      if (S.lf) ld();
    });
    sfor<0, 22>([&](auto K_) __attribute__((always_inline)) { if (S.vp[decltype(K_)::value] >= 32) red_fin(5, decltype(K_)::value); });
    asm volatile("v_lshrrev_b32 %0, 1, %1\n\ts_nop 3" : "=v"(l8) : "v"(l16));
    const int lr = (int)(l8 >> 3), nr = lr & 15, hr = lr >> 4;
    SCHED();
    __builtin_amdgcn_s_setprio(3);
#pragma unroll
    for (int r = 0; r < 4; ++r) red_put(6, r);
    STAMP(15);
    asm volatile("s_waitcnt vmcnt(%0)" :: "n"(8 + (WOE < 8 ? 7 : 15 - WOE)) : "memory");
    BAR();
#pragma unroll
    for (int i = 0; i < 8; ++i) red_get(i);
    unsigned wb[3];
#pragma unroll
    for (int i = 0; i < 3; ++i) { wb[i] = (unsigned)(kh * 1024 + i * 65536) + l16; asm volatile("" : "+v"(wb[i])); }
    u32x4 af[30];
#define LDA(m) af[(m) % 30] = *(const u32x4*)(lds + wb[WOA(2 * (m)) >> 16] + (WOA(2 * (m)) & 65535))
    SCHED();
#pragma unroll
    for (int m = 0; m < 7; ++m) LDA(m);
    SCHED();
#pragma unroll
    for (int k = 0; k < 22; ++k) red_fin(6, k < 2 ? 16 + k : k < 18 ? k - 2 : k);
    SCHED();
    RBAR();
    __builtin_amdgcn_s_setprio(0);
    STAMPT(16);
#pragma unroll
    for (int k = 0; k < 3; ++k) {
      const int i = 120 + (k < 2 || w < 4 ? w + 8 * k : w);
      dma1k(rs, lds + (i - 92) * 1024, l16, WS_WOUTF + (unsigned)c * WBLK + (unsigned)i * 1024u);
    }
#pragma unroll
    for (int x = 0; x < 3; ++x)
      xp[x] = __builtin_amdgcn_raw_buffer_load_b64(rs, (unsigned)(hr * 128 + psi(nr) * 8), WS_X1B + (unsigned)(R * 4 + rt) * 20480u + (unsigned)(5 * c + (kh ? (3 + (x < 2 ? x : 1)) : x)) * 512u, BULK_AUX);
#pragma unroll
    for (int m = 7; m < 14; ++m) LDA(m);
    auto p_ok = [&](int pt) {
      unsigned d = 0u;
#pragma unroll
      for (int i = 0; i < 8; ++i) d |= ((P[pt][i][0] + P[pt][i][1]) + (P[pt][i][2] + tag)) ^ P[pt][i][3];
      return __all(d == 0u);
    };
    auto p_get = [&](int pt) {
      if (!p_ok(pt)) {
        _Pragma("unroll 1") for (unsigned spins = 0; spins < SPIN_CAP; ++spins) {
          __builtin_amdgcn_s_sleep(1);
#pragma unroll
          for (int i = 0; i < 8; ++i) ld_p(pt, i);
          if (p_ok(pt)) break;
        }
      }
#pragma unroll
      for (int i = 0; i < 6; ++i) P[pt][4 * (i / 3) + i % 3][3] = P[pt][4 * (i / 3) + 3][i % 3];
    };
    f32x4 acc[5];
#define P2M 8
#define P3M 40
    u32x2 gbv[3];
    u32x4 F3[2];
    auto g3 = [&](auto M0_, auto M1_) {
#pragma unroll
      for (int m = decltype(M0_)::value; m < decltype(M1_)::value; ++m) {
        if (m == 16 && WOE < 15) {
          asm volatile("s_waitcnt vmcnt(%0)" :: "n"(8 + (P2M < 16 ? (16 - P2M > 8 ? 8 : 16 - P2M) : 0)) : "memory");
          RBAR();
        }
        SCHED();
        const int u2 = m / 5, f6 = u2 % 6;
        acc[m % 5] = MFMA16(af[m % 30], u2 < 12 ? P[u2 < 12 ? u2 / 6 : 0][4 * (f6 / 3) + f6 % 3] : F3[u2 & 1], m < 5 ? ((f32x4){0.f, 0.f, 0.f, 0.f}) : acc[m % 5]);
        if (m + 14 < 70) LDA(m + 14);
        if (m >= P2M && m < P2M + 8) ld_p(1, m - P2M);
        if (m >= P3M && m < P3M + 8) ld_p3(m - P3M);
        if (m >= 56 && m < 59) gbv[m - 56] = *(const u32x2*)(lds + L_MISC + (4 * (kh ? 3 + (m < 58 ? m - 56 : 1) : m - 56) + hr) * 8);
        SCHED();
      }
    };
    p_get(0);
    g3(IC<0>(), IC<30>());
    STAMPT(17);
    p_get(1);
    RBAR();
    STAMPT(25);
    g3(IC<30>(), IC<60>());
    auto ok3 = [&]() {
      unsigned d = 0u;
#pragma unroll
      for (int i = 0; i < 8; ++i) d |= (P3[i][0] + tag) ^ P3[i][1];
      return __all(d == 0u);
    };
    if (!ok3()) {
      _Pragma("unroll 1") for (unsigned spins = 0; spins < SPIN_CAP; ++spins) {
        __builtin_amdgcn_s_sleep(1);
#pragma unroll
        for (int i = 0; i < 8; ++i) ld_p3(i);
        if (ok3()) break;
      }
    }
#pragma unroll
    for (int i = 0; i < 2; ++i) F3[i] = (u32x4){P3[4 * i][0], P3[4 * i + 1][0], P3[4 * i + 2][0], P3[4 * i + 3][0]};
    STAMPT(26);
    g3(IC<60>(), IC<70>());
    STAMPT(18);
    f32x4* X = (f32x4*)lds + rt * (5 * 64) + lr;
    f32x4 m4[3];
    if (kh == 0) {
      X[3 * 64] = acc[3]; X[4 * 64] = acc[4];
      m4[0] = acc[0]; m4[1] = acc[1]; m4[2] = acc[2];
    } else {
      X[0] = acc[0]; X[64] = acc[1]; X[128] = acc[2];
      m4[0] = acc[3]; m4[1] = acc[4]; m4[2] = acc[4];
    }
    BAR();
    if (bid == 0 && w == 5 && lr == 0) st4(rs, WS_EPOCH, tag);
    const int t = R * 64 + tau(rt, nr);
    f32x4 o4[3];
#pragma unroll
    for (int j = 0; j < 3; ++j) o4[j] = X[(kh ? 3 + (j < 2 ? j : 1) : j) * 64];
    SCHED();
#pragma unroll
    for (int j = 0; j < 3; ++j) {
      const f32x4 v = rbf4((f32x4){bflo(gbv[j][0]), bfhi(gbv[j][0]), bflo(gbv[j][1]), bfhi(gbv[j][1])} * rbf4(o4[j] + m4[j]));
      const u32x2 o = {pack2(bflo(xp[j][0]) + v[0], bfhi(xp[j][0]) + v[1]), pack2(bflo(xp[j][1]) + v[2], bfhi(xp[j][1]) + v[3])};
      if (j < 2 || kh == 0)
        __builtin_amdgcn_raw_buffer_store_b64(o, __builtin_amdgcn_make_buffer_rsrc((void*)p.out, 0, S_ * D_ * 2, 0x00020000),
                                              (unsigned)(t * D_ + 80 * c + 16 * (kh ? 3 + j : j) + 4 * hr) * 2u, 0, 0);
    }
  }
  STAMP(19);
}

extern "C" __device__ __attribute__((used, noinline, visibility("hidden"))) void mega_tail() {}

extern "C" void kernel_launch(void* const* d_in, const int* in_sizes, int n_in, void* d_out, int out_size,
                              void* d_ws, size_t ws_size, hipStream_t stream) {
  Params p;
  p.q = (const u16*)d_in[0];
  p.k = (const u16*)d_in[1];
  p.v = (const u16*)d_in[2];
  p.shift_k = (const u16*)d_in[3];
  p.ssm_k = (const u16*)d_in[4];
  p.ssm_D = (const u16*)d_in[5];
  p.w_in = (const u16*)d_in[7];
  p.w_sp = (const u16*)d_in[8];
  p.b_sp = (const u16*)d_in[9];
  p.w_out = (const u16*)d_in[10];
  p.cvec = (const u16*)d_in[11];
  p.w_mod = (const u16*)d_in[12];
  p.out = (u16*)d_out;
  p.ws = (char*)d_ws;
  mega<<<256, 512, 0, stream>>>(p);
}
```

```cpp
#include <hip/hip_runtime.h>
#include <stdint.h>

typedef unsigned short u16;
typedef __attribute__((ext_vector_type(8))) short bf16x8;
typedef __attribute__((ext_vector_type(4))) short bf16x4;
typedef __attribute__((ext_vector_type(4))) float f32x4;
typedef __attribute__((ext_vector_type(2))) float f32x2;
typedef __attribute__((ext_vector_type(4))) unsigned int u32x4;
typedef __attribute__((ext_vector_type(2))) unsigned int u32x2;
#define LAS __attribute__((address_space(3)))
typedef __amdgpu_buffer_rsrc_t rsrc_t;
typedef __attribute__((ext_vector_type(4))) short s16x4;

constexpr int S_ = 2048, D_ = 640, F_ = 1728, FH = 864, CB = 108;

constexpr unsigned WS_EPOCH = 0;
constexpr unsigned WS_FWI = 4096;
constexpr unsigned WS_FZ = WS_FWI + 384 * 128;
constexpr unsigned WS_FWO = WS_FZ + 256 * 128;
constexpr unsigned F4S = 896 * 128;
constexpr unsigned WS_CS = WS_FZ + 256 * 128;
constexpr unsigned WS_MOD = 131072;
constexpr unsigned WS_MODP = WS_MOD + 81920;
constexpr unsigned WS_ST1 = 393216;
constexpr unsigned WS_PART = 524288;
constexpr unsigned WS_CSP = WS_PART + 262144;
constexpr unsigned WS_FX = WS_CSP + 655360;
constexpr unsigned WS_X1B = WS_FX + 16 * 4 * 160 * 128;
constexpr unsigned WS_QB = WS_X1B + 2621440;
constexpr unsigned WS_KV3 = WS_QB + 2621440;
constexpr unsigned WS_MODQ = WS_KV3 + 1310720;
constexpr unsigned WBLK = 143360;
constexpr unsigned WS_WINF = WS_MODQ + 245760;
constexpr unsigned WS_WOUTF = WS_WINF + 16 * WBLK;
constexpr unsigned WS_Z2F = WS_WOUTF + 8 * WBLK;
constexpr unsigned WS_UF = WS_Z2F + 8 * 64 * 7168;
constexpr unsigned WS_F4 = WS_UF + 32 * 4 * 8 * 4096;
constexpr unsigned WS_END = WS_F4 + 4 * F4S;
constexpr unsigned WS_STAMP = 200u << 20;

constexpr int G_COPY = 4160;
constexpr int L_U = WBLK;
constexpr int U_CH = (256 + S_) * 2 + 32;
constexpr int L_FLG = L_U + 4 * U_CH;
constexpr int L_RT = 16384;
constexpr int L_CONST = WBLK;
constexpr int L_PST = L_CONST + 2048;
constexpr int L_Z = L_CONST + 5120;
constexpr int L_ST2 = L_Z + 14336;
constexpr int L_MISC = L_ST2 + 512;
constexpr int LDS_TOTAL = 163840;

#define AUX_T 610
#define RT2_T 360
#define CB_T 380
#define W_T 730
#define CS_T 830
#define Y_T 930
#define K_T 460
#ifndef M_T
#define M_T 520
#endif
#define WA_T 580
constexpr int SC1 = 16, NT = 2;
constexpr unsigned SPIN_CAP = 1u << 16;

__device__ __forceinline__ float bflo(unsigned x) { return __uint_as_float(x << 16); }
__device__ __forceinline__ float bfhi(unsigned x) { return __uint_as_float(x & 0xffff0000u); }
__device__ __forceinline__ unsigned f2bf(float x) {
  __bf16 b = (__bf16)x;
  return (unsigned)__builtin_bit_cast(unsigned short, b);
}
__device__ __forceinline__ float rbf(float x) { return bflo(f2bf(x)); }
typedef __attribute__((ext_vector_type(2))) __bf16 bf2_t;
__device__ __forceinline__ unsigned pack2(float lo, float hi) {
  f32x2 v = {lo, hi};
  return __builtin_bit_cast(unsigned, __builtin_convertvector(v, bf2_t));
}
__device__ __forceinline__ f32x2 rbf2(f32x2 v) {
  const unsigned p = __builtin_bit_cast(unsigned, __builtin_convertvector(v, bf2_t));
  return (f32x2){__uint_as_float(p << 16), __uint_as_float(p & 0xffff0000u)};
}
__device__ __forceinline__ f32x4 rbf4(f32x4 v) {
  const f32x2 a = rbf2((f32x2){v[0], v[1]}), b = rbf2((f32x2){v[2], v[3]});
  return (f32x4){a[0], a[1], b[0], b[1]};
}
template <int CTRL>
__device__ __forceinline__ float dpp_f(float v) {
  return __int_as_float(__builtin_amdgcn_update_dpp(0, __float_as_int(v), CTRL, 0xf, 0xf, true));
}
__device__ __forceinline__ float sum8(float v) {
  v += dpp_f<0xB1>(v);
  v += dpp_f<0x4E>(v);
  v += dpp_f<0x141>(v);
  return v;
}
__device__ __forceinline__ float sum16(float v) {
  v = sum8(v);
  v += dpp_f<0x140>(v);
  return v;
}
__device__ __forceinline__ float sum_rows(float v) {
  auto a = __builtin_amdgcn_permlane16_swap(__float_as_uint(v), __float_as_uint(v), false, false);
  v = __uint_as_float(a[0]) + __uint_as_float(a[1]);
  auto b = __builtin_amdgcn_permlane32_swap(__float_as_uint(v), __float_as_uint(v), false, false);
  return __uint_as_float(b[0]) + __uint_as_float(b[1]);
}
#define MFMA16(a, b, c) __builtin_amdgcn_mfma_f32_16x16x32_bf16(__builtin_bit_cast(bf16x8, a), __builtin_bit_cast(bf16x8, b), c, 0, 0, 0)
#define MFMA16K16(a, b, c) __builtin_amdgcn_mfma_f32_16x16x16bf16_1k(__builtin_bit_cast(bf16x4, a), __builtin_bit_cast(bf16x4, b), c, 0, 0, 0)
__device__ __forceinline__ u32x2 trd(const char* p) {
  return __builtin_bit_cast(u32x2, __builtin_amdgcn_ds_read_tr16_b64_v4i16((LAS s16x4*)p));
}
__device__ __forceinline__ u32x4 trfrag(const char* p, int pitch) {
  u32x2 a = trd(p), b = trd(p + 16 * pitch);
  return (u32x4){a[0], a[1], b[0], b[1]};
}
#define BAR() asm volatile("s_waitcnt lgkmcnt(0)\n\ts_barrier" ::: "memory")
#define DRAIN() do { __builtin_amdgcn_s_waitcnt(0x0F70); asm volatile("" ::: "memory"); } while (0)
#define SCHED() __builtin_amdgcn_sched_barrier(0)
template <int N> struct IC { static constexpr int value = N; };
template <int I, int N, class F>
__device__ __forceinline__ void sfor(F&& f) {
  if constexpr (I < N) { f(IC<I>()); sfor<I + 1, N>(f); }
}

__device__ __forceinline__ u32x4 ld16(rsrc_t r, unsigned off) { return __builtin_amdgcn_raw_buffer_load_b128(r, off, 0, SC1); }
__device__ __forceinline__ u32x2 ld8(rsrc_t r, unsigned off) { return __builtin_amdgcn_raw_buffer_load_b64(r, off, 0, SC1); }
__device__ __forceinline__ unsigned ld4(rsrc_t r, unsigned off) { return __builtin_amdgcn_raw_buffer_load_b32(r, off, 0, SC1); }
#define ST_AUX 16
__device__ __forceinline__ void st16(rsrc_t r, unsigned off, u32x4 v) { __builtin_amdgcn_raw_buffer_store_b128(v, r, off, 0, ST_AUX); }
__device__ __forceinline__ void st8(rsrc_t r, unsigned off, u32x2 v) { __builtin_amdgcn_raw_buffer_store_b64(v, r, off, 0, SC1); }
__device__ __forceinline__ void st_flag4(rsrc_t r, unsigned off, unsigned tag, int l) {
  if (l < 32) st16(r, WS_F4 - WS_FWI + off + (unsigned)(l >> 3) * F4S + (unsigned)(l & 7) * 16u, (u32x4){tag, tag, tag, tag});
}
__device__ __forceinline__ void st4(rsrc_t r, unsigned off, unsigned v) { __builtin_amdgcn_raw_buffer_store_b32(v, r, off, 0, SC1); }
#define BULK_AUX 16
__device__ __forceinline__ u32x4 ld16p(rsrc_t r, unsigned off) { return __builtin_amdgcn_raw_buffer_load_b128(r, off, 0, BULK_AUX); }
__device__ __forceinline__ u32x2 ld8p(rsrc_t r, unsigned off) { return __builtin_amdgcn_raw_buffer_load_b64(r, off, 0, BULK_AUX); }
__device__ __forceinline__ void dma1k(rsrc_t r, char* lds_dst, unsigned l16, unsigned soff) {
  __builtin_amdgcn_raw_ptr_buffer_load_lds(r, (LAS void*)lds_dst, 16, l16, soff, 0, BULK_AUX);
}

#define STAMP(i)
#define STAMPF(i)
#define STAMPT(i) STAMP(i)

template <int N, int SLP>
__device__ __forceinline__ void wait_lines(rsrc_t r, const unsigned (&off)[N], unsigned tag) {
  bool need[N];
#pragma unroll
  for (int i = 0; i < N; ++i) need[i] = off[i] != ~0u;
  _Pragma("unroll 1") for (unsigned spins = 0; spins < SPIN_CAP; ++spins) {
    unsigned v[N];
#pragma unroll
    for (int i = 0; i < N; ++i) {
      v[i] = tag;
      if (need[i]) v[i] = ld4(r, off[i]);
    }
    bool any = false;
#pragma unroll
    for (int i = 0; i < N; ++i) { need[i] = v[i] != tag; any = any || need[i]; }
    if (!__any(any)) break;
    __builtin_amdgcn_s_sleep(SLP);
  }
  asm volatile("" ::: "memory");
}

__device__ __forceinline__ int tau(int rt, int n) { return 32 * (rt >> 1) + 8 * (n >> 2) + 4 * (rt & 1) + (n & 3); }
__device__ __forceinline__ int psi(int n) { return 2 * (n & 7) + (n >> 3); }
#define QX_CM(d, s0, s1, P) "v_cndmask_b32_dpp " d ", " s0 ", " s1 ", vcc quad_perm:" P " row_mask:0xf bank_mask:0xf\n\t"
#define QX_BODY(M, P) \
  asm("s_mov_b32 vcc_lo, " M "\n\ts_mov_b32 vcc_hi, " M "\n\t" \
      QX_CM("%0", "%12", "%8", P) "v_xor_b32 %4, %8, %0\n\t" QX_CM("%1", "%13", "%9", P) "v_xor_b32 %5, %9, %1\n\t" \
      QX_CM("%2", "%14", "%10", P) "v_xor_b32 %6, %10, %2\n\t" QX_CM("%3", "%15", "%11", P) "v_xor_b32 %7, %11, %3" \
      : "=&v"(t[0]), "=&v"(t[1]), "=&v"(t[2]), "=&v"(t[3]), "=&v"(u[0]), "=&v"(u[1]), "=&v"(u[2]), "=&v"(u[3]) \
      : "v"(a[0]), "v"(a[1]), "v"(a[2]), "v"(a[3]), "v"(b[0]), "v"(b[1]), "v"(b[2]), "v"(b[3]) : "vcc")
template <int BIT>
__device__ __forceinline__ void quad_xchg3(u32x4& a, u32x4& b) {
  unsigned t[4], u[4];
  if (BIT == 0) QX_BODY("0x55555555", "[1,0,3,2]");
  else QX_BODY("0x33333333", "[2,3,0,1]");
  a = (u32x4){t[0], t[1], t[2], t[3]};
#pragma unroll
  for (int d = 0; d < 4; ++d) b[d] ^= (unsigned)__builtin_amdgcn_update_dpp(0, (int)u[d], BIT == 0 ? 0xB1 : 0x4E, 0xf, 0xf, true);
}
__device__ __forceinline__ void quad_transpose(u32x4& x0, u32x4& x1, u32x4& x2, u32x4& x3) {
  quad_xchg3<0>(x0, x1);
  quad_xchg3<0>(x2, x3);
  quad_xchg3<1>(x0, x2);
  quad_xchg3<1>(x1, x3);
  asm volatile("" : "+v"(x0), "+v"(x1), "+v"(x2), "+v"(x3));
}

struct Params {
  const u16 *q, *k, *v, *shift_k, *ssm_k, *ssm_D, *w_in, *w_sp, *b_sp, *w_out, *cvec, *w_mod;
  u16* out;
  char* ws;
};

__global__ __launch_bounds__(512, 2) void mega(Params p) {
  __shared__ __attribute__((aligned(16))) char lds[LDS_TOTAL];
  {
    unsigned long long pc0;
    unsigned vt;
    asm volatile("v_readfirstlane_b32 vcc_lo, %3\n\ts_cmpk_lt_u32 %2, 192\n\ts_cbranch_scc1 4f\n\ts_cmpk_lt_u32 vcc_lo, 64\n\ts_cbranch_scc0 4f\n\t"
                 "s_lshr_b32 vcc_hi, %2, 3\n\ts_sub_u32 vcc_hi, vcc_hi, 24\n\ts_lshl_b32 vcc_hi, vcc_hi, 13\n\t"
                 "v_lshlrev_b32 %1, 7, %3\n\tv_add_u32 %1, vcc_hi, %1\n\ts_getpc_b64 %0\n.Lmega_here:\n\t"
                 "v_min_u32 %1, mega_tail-.Lmega_here-4, %1\n\ts_movk_i32 m0, 0x2000\n\ts_nop 1\n\tglobal_load_lds_dword %1, %0\n4:\n\t"
                 "s_cmpk_lt_u32 %2, 160\n\ts_cbranch_scc0 1f\n\ts_cmpk_lt_u32 vcc_lo, 256\n\ts_cbranch_scc1 3f\n\t"
                 "s_call_b64 vcc, .Ltouch1\n\ts_branch 3f\n1:\n\t"
                 "s_cmpk_lt_u32 vcc_lo, 256\n\ts_cbranch_scc1 2f\n\ts_cmpk_lt_u32 %2, 224\n\ts_cbranch_scc0 2f\n\ts_call_b64 vcc, .Ltouch3\n\ts_branch 3f\n"
                 "2:\n\ts_call_b64 vcc, .Ltouch2\n3:"
                 : "=&s"(pc0), "=&v"(vt) : "s"(blockIdx.x), "v"(threadIdx.x) : "vcc", "scc", "memory");
  }
  SCHED();
  const int tid = threadIdx.x, l = tid & 63;
  const int w = __builtin_amdgcn_readfirstlane(tid >> 6);
  const int bid = blockIdx.x, xcd = bid & 7, idx = bid >> 3;
#define TOUCH_AT(n) asm volatile("s_branch 1f\n.Ltouch" #n ":\n\ts_setpc_b64 vcc\n1:")
  char* const ws = p.ws;
  asm volatile("" ::"s"(p.q), "s"(p.k), "s"(p.v), "s"(p.shift_k), "s"(p.ssm_k), "s"(p.ssm_D), "s"(p.w_in), "s"(p.w_sp), "s"(p.b_sp),
               "s"(p.w_out), "s"(p.cvec), "s"(p.w_mod), "s"(p.out), "s"(p.ws));
  unsigned epoch, tag, t_start;
  unsigned long long t64;
  asm volatile("s_load_dword %0, %2, 0x0\n\ts_memrealtime %1" : "=&s"(epoch), "=&s"(t64) : "s"(ws));
#define TAG_READY() do { SCHED(); \
    asm volatile("s_waitcnt lgkmcnt(0)\n\ts_mul_i32 %0, %2, 0x9e3779b1\n\ts_add_i32 %0, %0, 0x9e3779b9\n\ts_mov_b64 %1, %3" \
                 : "=&s"(tag), "=&s"(t64r) : "s"(epoch), "s"(t64) : "scc"); t_start = (unsigned)t64r; SCHED(); } while (0)
  unsigned long long t64r;
  const rsrc_t rs = __builtin_amdgcn_make_buffer_rsrc(ws, 0, WS_END, 0x00020000);
  const unsigned l16 = (unsigned)l * 16u;
  STAMP(0);
  const bool is_conv = idx < 20;
  const int quad = xcd * 20 + idx;
  const int R = is_conv ? (xcd & 3) * 5 + (idx >> 2) : (xcd & 3) * 3 + 15 + (idx >> 2);
  const int c = (xcd >> 2) * 4 + (idx & 3);
  const unsigned fwi = WS_F4 + (unsigned)(xcd & 3) * F4S, fz = fwi + (WS_FZ - WS_FWI);
  const int n16 = l & 15, h4 = l >> 4;
  u32x4 wsp[4][8];
#define ROTC 2
#define WAUX 0
  const int rotg = ((idx >> 4) ^ ((ROTC & 2) ? idx >> 1 : 0)) & 1, rotb = (idx >> 2) & 3, rotr = (ROTC & 1) ? (idx & 1) * 2 : 0;
  const rsrc_t rsp = __builtin_amdgcn_make_buffer_rsrc((void*)p.w_sp, 0, S_ * S_ * 2, 0x00020000);
  const unsigned wsp_vo = (unsigned)(((l >> 2) & 3) * 8 * 4096 + h4 * 64 + ((l + rotb) & 3) * 16);
#define LDW(k) LDW_(k)
#define WSP_TR(g) WSP_TR_(g)
#define WROW R
#define LDW_(k) wsp[((k) >> 2) & 3][((k) >> 4) * 4 + ((k) & 3)] = __builtin_amdgcn_raw_buffer_load_b128(rsp, wsp_vo, \
    (unsigned)((WROW * 64 + 16 * ((((k) >> 2) & 2) ^ rotr) + 4 * (((k) >> 2) & 1) + ((k) & 3)) * 4096 + w * 512 + ((((k) >> 4) ^ rotg) * 256)), WAUX)
#define WSP_TR_(g) quad_transpose(wsp[(g) & 3][((g) >> 2) * 4], wsp[(g) & 3][((g) >> 2) * 4 + 1], wsp[(g) & 3][((g) >> 2) * 4 + 2], wsp[(g) & 3][((g) >> 2) * 4 + 3])
  const rsrc_t rwi = __builtin_amdgcn_make_buffer_rsrc((void*)p.w_in, 0, D_ * F_ * 2, 0x00020000);
  const rsrc_t rwo = __builtin_amdgcn_make_buffer_rsrc((void*)p.w_out, 0, FH * D_ * 2, 0x00020000);
  const rsrc_t rq = __builtin_amdgcn_make_buffer_rsrc((void*)p.q, 0, S_ * D_ * 2, 0x00020000);
  auto hold = [&](unsigned tt) { while ((unsigned)__builtin_amdgcn_s_memrealtime() - t_start < tt) __builtin_amdgcn_s_sleep(2); };
  volatile LAS unsigned* lf = (volatile LAS unsigned*)(lds + L_FLG);
  auto lf_set = [&](int i) { asm volatile("s_waitcnt lgkmcnt(0)" ::: "memory"); if (l == 0) lf[i] = tag; };
  auto lf_wait = [&](int i0, int n) {
    _Pragma("unroll 1") for (unsigned spins = 0; spins < (SPIN_CAP << 4); ++spins) {
      const unsigned v = lf[i0 + (l & (n - 1))];
      if (__all(v == tag)) break;
      __builtin_amdgcn_s_sleep(1);
    }
    asm volatile("" ::: "memory");
  };
  auto xflag = [&](int s) {
    st16(rs, WS_FX + (unsigned)(((4 * s + (w & 3)) * 4 + ((l >> 3) & 3)) * 160 + quad) * 128u + (unsigned)(l & 7) * 16u, (u32x4){tag, tag, tag, tag});
    SCHED();
  };
  auto load_b2 = [&]() {
#pragma unroll
    for (int k = 0; k < 35; ++k) {
      int i = k + (idx >> 2) * 4;
      i = (i >= 35 ? i - 35 : i) + 35 * w;
      dma1k(rs, lds + i * 1024, l16, WS_WINF + (unsigned)(8 + c) * WBLK + (unsigned)i * 1024u);
    }
  };
  char* const wo_st = lds + 122880;
  auto wo_ld = [&]() {
    const int U = R < 28 ? R : 0, u2 = U >> 1, fi = (u2 + 1) % 3;
    const int c0 = 4 * (U & 1) + (u2 < 8 ? u2 >> 1 : u2 < 11 ? 0 : 2);
    const int b0 = u2 < 8 ? CB * c0 + 2 * (u2 & 1) + 96 : CB * (c0 + (fi >> 1)) + 48 + 2 * (fi & 1), d1 = u2 < 8 ? -96 : 16, d2 = d1 + 16;
    const int b3 = u2 < 8 ? b0 - 64 : CB * (c0 + 1) + 50 + 16 * fi;
#pragma unroll
    for (int r = 0; r < 5; ++r) {
      const int g = r * 64 + l, rho = g / 10;
      const int fb = (rho & 16) ? ((rho & 2) ? b3 : b0 + d2) : ((rho & 2) ? b0 + d1 : b0);
      __builtin_amdgcn_raw_ptr_buffer_load_lds(rwo, (LAS void*)(wo_st + r * 1024), 16, (unsigned)((fb + (rho & 13)) * (D_ * 2) + (g % 10) * 16 + c * 160), 0, 0, 0);
    }
  };
  auto rt_ld = [&](int i0, char* st) {
    const int cb = (i0 < 160 ? 8 : 0) + (i0 & 7), Q = (i0 < 160 ? i0 : i0 - 160) >> 3;
#pragma unroll
    for (int r = 0; r < 7; ++r) {
      const int g = r * 64 + l;
      __builtin_amdgcn_raw_ptr_buffer_load_lds(rwi, (LAS void*)(st + r * 1024), 16, (unsigned)((g / 14) * (F_ * 2) + (g % 14) * 16 + Q * 32 * F_ * 2 + cb * CB * 2),
                                               0, 0, NT);
    }
  };
  auto rt_job = [&](int i0, char* st) {
    const int cb = (i0 < 160 ? 8 : 0) + (i0 & 7), Q = (i0 < 160 ? i0 : i0 - 160) >> 3;
    const int col = 640 * (l >> 5) + 32 * Q + (l & 31);
    const unsigned po = WS_MODP + (unsigned)col * 8u;
    u32x2 pp8[8];
    hold(CB_T);
    _Pragma("unroll 1") for (unsigned spins = 0; spins < SPIN_CAP; ++spins) {
      unsigned dd = 0u;
#pragma unroll
      for (int i = 0; i < 8; ++i) pp8[i] = ld8(rs, po + (unsigned)i * 10240u);
#pragma unroll
      for (int i = 0; i < 8; ++i) dd |= pp8[i][0] ^ tag;
      if (__all(dd == 0u)) break;
      __builtin_amdgcn_s_sleep(4);
    }
    STAMPF(9);
    {
      float t = 0.f;
#pragma unroll
      for (int i = 0; i < 8; ++i) t += __uint_as_float(pp8[i][1]);
      if (l >= 32) *(float*)(st + 7168 + (l - 32) * 4) = rbf(1.0f + rbf(t));
      else *(u16*)(st + 7296 + l * 2) = (u16)f2bf(t);
    }
    DRAIN();
    asm volatile("s_waitcnt lgkmcnt(0)" ::: "memory");
    const f32x4 ca = *(const f32x4*)(st + 7168 + h4 * 16), cc = *(const f32x4*)(st + 7168 + 64 + h4 * 16);
    const char* bp = st + (4 * h4 + (n16 >> 2)) * 224 + (n16 & 3) * 8;
    u32x4 x[7], o[7];
#pragma unroll
    for (int j = 0; j < 7; ++j) {
      x[j] = trfrag(bp + j * 32, 224);
      o[j] = (u32x4){pack2(bflo(x[j][0]) * ca[0], bfhi(x[j][0]) * ca[1]), pack2(bflo(x[j][1]) * ca[2], bfhi(x[j][1]) * ca[3]),
                     pack2(bflo(x[j][2]) * cc[0], bfhi(x[j][2]) * cc[1]), pack2(bflo(x[j][3]) * cc[2], bfhi(x[j][3]) * cc[3])};
      if (j == 6 && n16 == 12) o[j] = (u32x4){0x3F803F80u, 0x3F803F80u, 0x3F803F80u, 0x3F803F80u};
      st16(rs, WS_WINF + (unsigned)cb * WBLK + (unsigned)(Q * 7 + j) * 1024u + l16, o[j]);
    }
    STAMPF(11);
    const u32x2 e0 = *(const u32x2*)(st + 7296 + h4 * 8), e1 = *(const u32x2*)(st + 7328 + h4 * 8);
    f32x4 d[2] = {{0.f, 0.f, 0.f, 0.f}, {0.f, 0.f, 0.f, 0.f}};
#pragma unroll
    for (int j = 0; j < 7; ++j) {
      const unsigned one = n16 == 4 * (j & 3) ? 0x3F803F80u : 0u;
      const bool sr = n16 == 4 * (j & 3) + 1;
      d[j >> 2] = MFMA16(((u32x4){one, one, one, one}), o[j], d[j >> 2]);
      d[j >> 2] = MFMA16(((u32x4){sr ? e0[0] : 0u, sr ? e0[1] : 0u, sr ? e1[0] : 0u, sr ? e1[1] : 0u}), x[j], d[j >> 2]);
    }
#pragma unroll
    for (int m = 0; m < 2; ++m)
      st16(rs, WS_CSP + (unsigned)((cb * 20 + Q) * 2 + m) * 1024u + l16, (u32x4){tag, __float_as_uint(d[m][0]), tag, __float_as_uint(d[m][1])});
    STAMPF(13);
    asm volatile("s_waitcnt vmcnt(2)" ::: "memory");
    st_flag4(rs, WS_FWI + (unsigned)(cb * 20 + Q) * 128u, tag, l);
  };

  auto red8 = [&](float v) { v += dpp_f<0x128>(v); return sum_rows(v); };
  const rsrc_t rwm = __builtin_amdgcn_make_buffer_rsrc((void*)p.w_mod, 0, D_ * 3840 * 2, 0x00020000);
  auto gemv_load = [&](u32x4* pre, int blk, int sl, int np) {
    pre[10][0] = *(const unsigned*)(p.cvec + 8 * np * sl + 2 * (l < 4 * np ? l : 0));
#pragma unroll
    for (int pp = 0; pp < np; ++pp)
      pre[pp] = __builtin_amdgcn_raw_buffer_load_b128(rwm, (unsigned)((l >> 3) * 7680 + (l & 7) * 16), (unsigned)((8 * np * sl + pp * 8) * 7680 + blk * 128), NT);
  };
  auto gemv_fin = [&](float* acc, const u32x4* pre, int np, char* scr) {
    const unsigned cv2 = pre[10][0];
    unsigned* svt = (unsigned*)scr + w * 96;
#pragma unroll
    for (int e = 0; e < 2; ++e) {
      const int row = 2 * l + e;
      const float cv = e ? bfhi(cv2) : bflo(cv2);
      if (l < 4 * np) svt[(row & 7) * 12 + (row >> 3)] = __float_as_uint(rbf(cv * __builtin_amdgcn_rcpf(1.0f + __expf(-cv))));
    }
    asm volatile("s_waitcnt lgkmcnt(0)" ::: "memory");
    u32x4 sv[3];
#pragma unroll
    for (int i = 0; i < (np + 3) / 4; ++i) sv[i] = *(const u32x4*)(svt + (l >> 3) * 12 + 4 * i);
    SCHED();
#pragma unroll
    for (int e = 0; e < 8; ++e) acc[e] = 0.f;
#pragma unroll
    for (int pp = 0; pp < np; ++pp) {
      const unsigned sh = sv[pp >> 2][pp & 3], sl2 = sh >> 16;
#pragma unroll
      for (int e = 0; e < 4; ++e) {
        const unsigned x = pre[pp][e];
        acc[2 * e] = __builtin_amdgcn_fdot2_f32_bf16(__builtin_bit_cast(bf2_t, x), __builtin_bit_cast(bf2_t, sl2), acc[2 * e], false);
        acc[2 * e + 1] = __builtin_amdgcn_fdot2_f32_bf16(__builtin_bit_cast(bf2_t, x), __builtin_bit_cast(bf2_t, sh), acc[2 * e + 1], false);
      }
    }
#pragma unroll
    for (int e = 0; e < 8; ++e) acc[e] = red8(acc[e]);
  };
  auto gemv_put = [&](const float* acc, unsigned off) {
    float v = acc[0];
#pragma unroll
    for (int e = 1; e < 8; ++e) { v = (l >> 3) == e ? acc[e] : v; asm("" : "+v"(v)); }
    st8(rs, off + (unsigned)l * 8u, (u32x2){tag, (unsigned)__builtin_amdgcn_ds_bpermute(((l & 7) * 8 + (l >> 3)) * 4, (int)__float_as_uint(v))});
  };
  typedef const __attribute__((address_space(4))) unsigned* cptr_t;

  if (__builtin_expect(is_conv, 1)) {
    if (w >= 4) {
      TOUCH_AT(1);
      const int pw = w - 4;
      const rsrc_t ra = __builtin_amdgcn_make_buffer_rsrc((void*)((idx & 2) ? p.v : p.k), 0, S_ * D_ * 2, 0x00020000);
      const rsrc_t rb = __builtin_amdgcn_make_buffer_rsrc((void*)((idx & 2) ? p.k : p.v), 0, S_ * D_ * 2, 0x00020000);
      const rsrc_t rk = __builtin_amdgcn_make_buffer_rsrc((void*)p.k, 0, S_ * D_ * 2, 0x00020000);
      const unsigned vo = (unsigned)((128 * pw + 2 * l) * (D_ * 2));
      const unsigned so0 = (unsigned)((idx & 1) * (D_ * 2) + quad * 8), so1 = (unsigned)((1 - (idx & 1)) * (D_ * 2) + quad * 8);
      u32x2 g[3][4], kh;
      auto gl = [&](int s, int j0, int j1) {
        __builtin_amdgcn_s_setprio(0);
#pragma unroll
        for (int j = j0; j < j1; ++j) {
          g[s][j] = __builtin_amdgcn_raw_buffer_load_b64(j < 2 ? ra : rb, vo, ((j & 1) ? so1 : so0) + (unsigned)(s * 512 * D_ * 2), 0);
          SCHED();
        }
        __builtin_amdgcn_s_setprio(3);
      };
      const cptr_t sp = (cptr_t)(p.shift_k + quad * 16);
      unsigned spv[8];
#pragma unroll
      for (int i = 0; i < 8; ++i) spv[i] = sp[i];
      __builtin_amdgcn_s_sleep(3);
      gl(0, 0, 4);
      {
        const int row = 512 * (l >> 2) + 128 * pw - 3 + (l & 3);
        kh = __builtin_amdgcn_raw_buffer_load_b64(rk, (l >= 12 || (l & 3) == 3 || row < 0) ? 0x80000000u : (unsigned)(row * D_ * 2 + quad * 8), 0, 0);
      }
      TAG_READY();
      f32x2 wt[2][4];
#pragma unroll
      for (int d = 0; d < 2; ++d) {
        wt[d][0] = (f32x2){bflo(spv[4 * d]), bflo(spv[4 * d + 2])}; wt[d][1] = (f32x2){bfhi(spv[4 * d]), bfhi(spv[4 * d + 2])};
        wt[d][2] = (f32x2){bflo(spv[4 * d + 1]), bflo(spv[4 * d + 3])}; wt[d][3] = (f32x2){bfhi(spv[4 * d + 1]), bfhi(spv[4 * d + 3])};
        asm volatile("" : "+s"(wt[d][0]), "+s"(wt[d][1]), "+s"(wt[d][2]), "+s"(wt[d][3]));
      }
      SCHED();
      {
        const int j = idx * 4 + pw;
        const unsigned o = (unsigned)((512 + 64 * (j >> 2) + l) * (D_ * 2) + (((160 * xcd) >> 7) + ((j >> 1) & 1)) * 128);
        __builtin_amdgcn_raw_ptr_buffer_load_lds((j & 1) ? ra : rb, (LAS void*)(lds + 98304), 4, j < 64 ? o : 0x80000000u, 0, 0, 0);
      }
      SCHED();
      gl(1, 0, 4);
      STAMP(21);
      char* up = lds + L_U + 512 + 256 * pw + (l >> 2) * 16 + (3 - (l & 3)) * 4;
      auto kv = [&](auto S__, auto N__) {
        constexpr int s = decltype(S__)::value;
        u32x2* x = g[s];
        SCHED();
        asm volatile("s_waitcnt vmcnt(%5)" : "+v"(x[0]), "+v"(x[1]), "+v"(x[2]), "+v"(x[3]), "+v"(kh) : "n"(decltype(N__)::value));
        if (s == 2) STAMP(27);
        if (idx & 1) { asm volatile(""); const u32x2 t = x[0], u = x[2]; x[0] = x[1]; x[1] = t; x[2] = x[3]; x[3] = u; }
        asm volatile("" : "+v"(x[0]), "+v"(x[1]), "+v"(x[2]), "+v"(x[3]));
        if (idx & 2) { asm volatile(""); const u32x2 t = x[0], u = x[1]; x[0] = x[2]; x[1] = x[3]; x[2] = t; x[3] = u; }
        asm volatile("" : "+v"(x[0]), "+v"(x[1]), "+v"(x[2]), "+v"(x[3]));
        f32x2 kf[2][5], o[2][2];
#pragma unroll
        for (int d = 0; d < 2; ++d) {
          unsigned kk[5];
          kk[3] = x[0][d]; kk[4] = x[1][d];
          kk[2] = __builtin_amdgcn_update_dpp((unsigned)__builtin_amdgcn_readlane(kh[d], 4 * s + 2), kk[4], 0x138, 0xf, 0xf, false);
          kk[1] = __builtin_amdgcn_update_dpp((unsigned)__builtin_amdgcn_readlane(kh[d], 4 * s + 1), kk[3], 0x138, 0xf, 0xf, false);
          kk[0] = __builtin_amdgcn_update_dpp((unsigned)__builtin_amdgcn_readlane(kh[d], 4 * s), kk[2], 0x138, 0xf, 0xf, false);
#pragma unroll
          for (int j = 0; j < 5; ++j) kf[d][j] = (f32x2){bflo(kk[j]), bfhi(kk[j])};
        }
#pragma unroll
        for (int t = 0; t < 5; ++t) {
#pragma unroll
          for (int i = 0; i < 4; ++i) {
            const int d = i >> 1, r = i & 1;
            if (t == 4) o[d][r] *= (f32x2){bflo(x[2 + r][d]), bfhi(x[2 + r][d])};
            else if (t == 0) o[d][r] = kf[d][r + 3] * wt[d][0];
            else o[d][r] = __builtin_elementwise_fma(kf[d][r + 3 - t], wt[d][t], o[d][r]);
          }
          asm volatile("" : "+v"(o[0][0]), "+v"(o[0][1]), "+v"(o[1][0]), "+v"(o[1][1]));
        }
#pragma unroll
        for (int i = 0; i < 4; ++i) *(unsigned*)(up + i * U_CH + s * 1024) = pack2(o[i >> 1][1][i & 1], o[i >> 1][0][i & 1]);
        asm volatile("" ::: "memory");
        if (l == 0) lf[4 * s + pw] = tag;
        SCHED();
      };
      const char* yr = lds + L_U + (n16 >> 2) * U_CH + (128 * pw + 32 * h4 + 8 * (n16 & 1) + 4 * ((n16 >> 1) & 1)) * 2;
      const unsigned yo = (unsigned)(8 * pw + (l >> 3)) * 20480u + (unsigned)quad * 128u + (unsigned)(l & 7) * 16u;
      const unsigned so = WS_ST1 + (unsigned)xcd * 16384u + (unsigned)pw * 1024u + l16;
      const unsigned mo = WS_MODQ + (unsigned)((n16 < 12 ? n16 >> 2 : 0) * D_ + quad * 4 + (l & 3)) * 8u + (unsigned)h4 * 40960u;
      u32x4 sv[4], qv[4];
      u32x2 mv[2];
      auto ld_e = [&]() {
#pragma unroll
        for (int s = 0; s < 4; ++s) sv[s] = ld16(rs, so + (unsigned)s * 4096u);
        SCHED();
      };
      auto s_ok = [&]() {
        unsigned d = 0u;
#pragma unroll
        for (int s = 0; s < 4; ++s) d |= (sv[s][1] ^ tag) | (sv[s][3] ^ tag);
        return __all(d == 0u);
      };
      kv(IC<0>(), IC<5>());
      STAMP(23);
      gl(2, 0, 4);
      kv(IC<1>(), IC<4>());
      STAMPF(9);
      u32x4 k3[2];
      const unsigned k3o = WS_KV3 + (unsigned)(16 * pw + (l >> 2)) * 20480u + (unsigned)(((l >> 1) & 1) * 10240 + (2 * quad + (l & 1)) * 16);
      hold(K_T);
      k3[0] = ld16(rs, k3o); k3[1] = ld16(rs, k3o + 5120u);
      SCHED();
      ld_e();
      kv(IC<2>(), IC<6>());
      STAMPF(11);
      auto k_ok = [&]() { return __all(((k3[0][1] ^ tag) | (k3[0][3] ^ tag) | (k3[1][1] ^ tag) | (k3[1][3] ^ tag)) == 0u); };
      asm volatile("" : "+v"(k3[0]), "+v"(k3[1]));
      if (!k_ok()) {
        _Pragma("unroll 1") for (unsigned spins = 0; spins < SPIN_CAP; ++spins) {
          __builtin_amdgcn_s_sleep(4);
          k3[0] = ld16(rs, k3o); k3[1] = ld16(rs, k3o + 5120u);
          if (k_ok()) break;
        }
      }
      *(u32x4*)(lds + L_U + (2 * (l & 1) + ((l >> 1) & 1)) * U_CH + 512 + (1536 + 128 * pw + 8 * (l >> 2)) * 2) = (u32x4){k3[0][0], k3[0][2], k3[1][0], k3[1][2]};
      asm volatile("" ::: "memory");
      if (l == 0) lf[12 + pw] = tag;
      __builtin_amdgcn_s_setprio(3);
      STAMPF(18);
      asm volatile("" : "+v"(sv[0]), "+v"(sv[1]), "+v"(sv[2]), "+v"(sv[3]));
      if (!s_ok()) {
        _Pragma("unroll 1") for (unsigned spins = 0; spins < SPIN_CAP; ++spins) {
          __builtin_amdgcn_s_sleep(4);
          ld_e();
          if (s_ok()) break;
        }
      }
      asm volatile("" ::: "memory");
      SCHED();
      STAMPF(17);
#pragma unroll
      for (int s = 0; s < 4; ++s) qv[s] = ld16p(rs, WS_QB + yo + (unsigned)s * (32u * 20480u));
      hold(M_T);
      mv[0] = ld8(rs, mo); mv[1] = ld8(rs, mo + 20480u);
      SCHED();
      STAMP(2);
      u32x2 yv[4][2];
      auto ld_y = [&](int s) {
#pragma unroll
        for (int e = 0; e < 2; ++e) yv[s][e] = trd(yr + 1024 * s + 32 * e);
      };
      lf_wait(20, 4);
      ld_y(0); ld_y(1);
      SCHED();
      asm volatile("" : "+v"(mv[0]), "+v"(mv[1]), "+v"(qv[0]), "+v"(qv[1]), "+v"(qv[2]), "+v"(qv[3]));
      if (!__all(((mv[0][0] ^ tag) | (mv[1][0] ^ tag)) == 0u)) {
        _Pragma("unroll 1") for (unsigned spins = 0; spins < SPIN_CAP; ++spins) {
          __builtin_amdgcn_s_sleep(4);
          mv[0] = ld8(rs, mo); mv[1] = ld8(rs, mo + 20480u);
          if (__all(((mv[0][0] ^ tag) | (mv[1][0] ^ tag)) == 0u)) break;
        }
      }
      STAMPF(13);
      f32x4 al, be;
      {
        const float mgf = rbf(sum_rows(__uint_as_float(mv[0][1]) + __uint_as_float(mv[1][1])));
        auto rl = [&](int i) { return __uint_as_float(__builtin_amdgcn_readlane(__float_as_uint(mgf), i)); };
        const f32x4 sa = {rl(0), rl(1), rl(2), rl(3)}, ca = {rl(4), rl(5), rl(6), rl(7)}, ga = {rl(8), rl(9), rl(10), rl(11)};
        al = rbf4(ca + 1.0f) * ga;
        be = sa * ga;
      }
      f32x4 qf[2][2], P[2][2];
      auto xpre = [&](int s) {
#pragma unroll
        for (int e = 0; e < 2; ++e) {
          const float r1 = __builtin_amdgcn_rcpf(bfhi(sv[s][2 * e])), m1 = -bflo(sv[s][2 * e]) * r1;
          const f32x4 qq = {bflo(qv[s][2 * e]), bfhi(qv[s][2 * e]), bflo(qv[s][2 * e + 1]), bfhi(qv[s][2 * e + 1])};
          qf[s & 1][e] = qq;
          P[s & 1][e] = __builtin_elementwise_fma(__builtin_elementwise_fma(qq, (f32x4){r1, r1, r1, r1}, (f32x4){m1, m1, m1, m1}), al, be);
        }
        asm volatile("" : "+v"(qf[s & 1][0]), "+v"(qf[s & 1][1]), "+v"(P[s & 1][0]), "+v"(P[s & 1][1]));
      };
      auto xfin = [&](int s) {
        u32x4 o;
#pragma unroll
        for (int e = 0; e < 2; ++e) {
          const u32x2 y = yv[s][e];
          const f32x4 yf = {bflo(y[0]), bfhi(y[0]), bflo(y[1]), bfhi(y[1])};
          const f32x4 x = __builtin_elementwise_fma(P[s & 1][e], yf, qf[s & 1][e]);
          o[2 * e] = pack2(x[0], x[1]);
          o[2 * e + 1] = pack2(x[2], x[3]);
        }
        st16(rs, WS_X1B + yo + (unsigned)s * (32u * 20480u), o);
      };
      xpre(0); xfin(0);
      xpre(1); xfin(1);
      SCHED();
      xpre(2); xpre(3);
      lf_wait(24, 4);
      __builtin_amdgcn_s_setprio(3);
      ld_y(2); ld_y(3);
      xfin(2); xfin(3);
      STAMP(28);
      asm volatile("s_waitcnt vmcnt(2)" ::: "memory");
      xflag(l >> 5);
      asm volatile("s_waitcnt vmcnt(1)" ::: "memory");
      xflag(2 + (l >> 5));
      STAMP(5);
    } else {
      const int ch = w;
      char* U = lds + L_U + ch * U_CH;
      char* G = lds + ch * (4 * G_COPY);
      {
        const rsrc_t rf = __builtin_amdgcn_make_buffer_rsrc((void*)(p.ssm_k + (size_t)(quad * 4 + ch) * S_), 0, S_ * 2, 0x00020000);
        u32x4 fa[4];
        u32x2 fb[4];
        __builtin_amdgcn_s_setprio(3);
#pragma unroll
        for (int k = 0; k < 4; ++k) {
          fa[k] = __builtin_amdgcn_raw_buffer_load_b128(rf, l16 + (unsigned)k * 1024u, 0, NT);
        }
        __builtin_amdgcn_s_setprio(1);
        TAG_READY();
#pragma unroll
        for (int k = 0; k < 4; ++k) {
#pragma unroll
          for (int d = 0; d < 2; ++d)
            fb[k][d] = __builtin_amdgcn_update_dpp(k < 3 ? (unsigned)__builtin_amdgcn_readfirstlane(fa[k < 3 ? k + 1 : k][d]) : 0u, fa[k][d], 0x130, 0xf, 0xf, false);
          const unsigned x[6] = {fa[k][0], fa[k][1], fa[k][2], fa[k][3], fb[k][0], fb[k][1]};
          unsigned y[5];
#pragma unroll
          for (int d = 0; d < 5; ++d) y[d] = __builtin_amdgcn_alignbit(x[d + 1], x[d], 16);
          char* gp = G + 32 + k * 1024 + l16;
          *(u32x4*)(gp) = fa[k];
          *(u32x4*)(gp + G_COPY) = (u32x4){y[0], y[1], y[2], y[3]};
          *(u32x4*)(gp + 2 * G_COPY) = (u32x4){x[1], x[2], x[3], x[4]};
          *(u32x4*)(gp + 3 * G_COPY) = (u32x4){y[1], y[2], y[3], y[4]};
          if (k == 0) {
            const unsigned f0 = __builtin_amdgcn_readfirstlane(x[0]), f1 = __builtin_amdgcn_readfirstlane(x[1]);
            const unsigned long long f64 = (unsigned long long)f0 | ((unsigned long long)f1 << 32);
            const unsigned long long hi = l ? (f64 << (64 - 16 * (l & 3))) : 0ull;
            if (l < 4) {
              *(u32x4*)(G + l * G_COPY) = (u32x4){0u, 0u, 0u, 0u};
              *(u32x4*)(G + l * G_COPY + 16) = (u32x4){0u, 0u, (unsigned)hi, (unsigned)(hi >> 32)};
            }
            if (l >= 32) *(u32x4*)(U + (l - 32) * 16) = (u32x4){0u, 0u, 0u, 0u};
          }
        }
        asm volatile("s_waitcnt lgkmcnt(0)" ::: "memory");
        STAMP(1);
      }
      const int a = n16, h = h4, jj = h >> 1, e2 = h & 1;
      const char* ap = G + ((a + 1) & 3) * G_COPY + 2 * (16 * jj + a - 8 * e2 + 9 - ((a + 1) & 3));
      const char* bp = U + 512 + 32 * (a - jj) + 16 * e2;
      auto lda = [&](u32x4* A, int r, int p0, int np) {
#pragma unroll
        for (int pp = 0; pp < np; ++pp) {
          u32x2 lo = *(const volatile LAS u32x2*)(ap + 512 * (p0 + pp) + 64 * r);
          u32x2 hi = *(const volatile LAS u32x2*)(ap + 512 * (p0 + pp) + 64 * r + 8);
          A[pp] = (u32x4){lo[0], lo[1], hi[0], hi[1]};
        }
      };
      u32x4 AD;
      {
        const unsigned dDr = (*(cptr_t)(p.ssm_D + quad * 4 + (ch & 2)) >> (16 * (ch & 1))) & 0xffffu;
        const unsigned dv = h == (a >> 3) ? ((a & 1) ? dDr : dDr << 16) : 0u;
        const int dw = (7 - (a & 7)) >> 1;
#pragma unroll
        for (int d = 0; d < 4; ++d) AD[d] = dw == d ? dv : 0u;
      }
      u32x4 A0[8][4];
      f32x4 acc[8];
#pragma unroll
      for (int n = 0; n < 8; ++n) acc[n] = (f32x4){0.f, 0.f, 0.f, 0.f};
#pragma unroll
      for (int r = 0; r < 8; ++r) lda(A0[r], r, 0, 4);
      auto yput = [&](int s) {
#pragma unroll
        for (int e = 0; e < 2; ++e)
          *(u32x2*)(U + 1024 * s + 512 * e + (16 * a + 4 * h) * 2) = (u32x2){pack2(acc[2 * s + e][0], acc[2 * s + e][1]), pack2(acc[2 * s + e][2], acc[2 * s + e][3])};
        lf_set(16 + 4 * s + ch);
      };
      auto stage = [&](auto S__) {
        constexpr int s = decltype(S__)::value, NH = s < 2 ? 4 - 2 * s : 0, NP = 8 - 2 * s, NB = s < 2 ? 2 : 8;
        u32x4 A1[3][NH > 0 ? NH : 1], B[NB][2];
        auto ldb = [&](int r) {
#pragma unroll
          for (int e = 0; e < 2; ++e) B[r % NB][e] = *(const u32x4*)(bp + 512 * (2 * s + e) - 64 * r);
        };
        if (NH) { lda(A1[0], 0, 4, NH); lda(A1[1], 1, 4, NH); }
        SCHED();
        if (s > 0) yput(s - 1);
        lf_wait(4 * s, 4);
        if (s == 0) STAMP(3);
#pragma unroll
        for (int r = 0; r < NB; ++r) ldb(r);
        SCHED();
#pragma unroll
        for (int e = 0; e < 2; ++e) acc[2 * s + e] = MFMA16(AD, B[0][e], acc[2 * s + e]);
#pragma unroll
        for (int r = 0; r < 8; ++r) {
          if (NH && r + 2 < 8) lda(A1[(r + 2) % 3], r + 2, 4, NH);
          SCHED();
#pragma unroll
          for (int e = 0; e < 2; ++e)
#pragma unroll
            for (int pp = 0; pp < NP - e; ++pp) acc[2 * s + e + pp] = MFMA16(pp < 4 ? A0[r][pp] : A1[r % 3][pp < 4 ? 0 : pp - 4], B[r % NB][e], acc[2 * s + e + pp]);
          SCHED();
          if (r + NB < 8) ldb(r + NB);
        }
      };
      stage(IC<0>());
      STAMP(4);
      stage(IC<1>());
      STAMPF(9);
      const unsigned fo[1] = {fwi + (unsigned)((8 + c) * 20 + 5 * w + (l < 5 ? l : 0)) * 128u};
      unsigned fv = ~tag;
      {
        u32x4 B[4][4];
        auto ldb = [&](int r) {
#pragma unroll
          for (int m = 0; m < 4; ++m) B[r & 3][m] = *(const u32x4*)(bp + 512 * (4 + m) - 64 * r);
        };
        yput(1);
        if (w == 3) wo_ld();
        __builtin_amdgcn_s_setprio(3);
        lf_wait(8, 8);
        STAMP(22);
#pragma unroll
        for (int r = 0; r < 4; ++r) ldb(r);
        SCHED();
#pragma unroll
        for (int m = 0; m < 4; ++m) acc[4 + m] = MFMA16(AD, B[0][m], acc[4 + m]);
#pragma unroll
        for (int r = 0; r < 8; ++r) {
#pragma unroll
          for (int m = 0; m < 4; ++m)
#pragma unroll
            for (int pp = 0; pp < 4 - m; ++pp) acc[4 + m + pp] = MFMA16(A0[r][pp], B[r & 3][m], acc[4 + m + pp]);
          SCHED();
          if (r + 4 < 8) ldb(r + 4);
          if (r == 1) fv = ld4(rs, fo[0]);
          SCHED();
        }
#pragma unroll
        for (int e = 0; e < 4; ++e)
          *(u32x2*)(U + 2048 + 512 * e + (16 * a + 4 * h) * 2) = (u32x2){pack2(acc[4 + e][0], acc[4 + e][1]), pack2(acc[4 + e][2], acc[4 + e][3])};
        lf_set(24 + ch);
      }
      STAMP(28);
      __builtin_amdgcn_s_setprio(0);
      if (!__all(fv == tag)) wait_lines<1, 4>(rs, fo, tag);
      lf_wait(20, 4);
    }
    __builtin_amdgcn_s_setprio(0);
  } else {
    const int at = idx - 20;
    char* const scr = lds + WBLK + 14848;
    auto gv_blk = [&](int j) { return j < 160 ? 30 + (j >> 3) : j < 400 ? (j >> 3) - 20 : j >> 3; };
    auto gv_fin = [&](const u32x4* pre, int j) {
      float acc[8];
      gemv_fin(acc, pre, 10, scr);
      gemv_put(acc, j < 160 ? WS_MODP + (unsigned)((j & 7) * 1280 + (j >> 3) * 64) * 8u : WS_MODQ + (unsigned)((j & 7) * 2560 + ((j >> 3) - 20) * 64) * 8u);
    };
    const int ja = xcd * 12 + at, ai = xcd * 8 + at, bi = xcd * 4 + at - 8, g = w >> 2, wj = w & 3;
    const int rj = w == 0 ? ja : w == 1 ? (at >= 4 ? 92 + ai : -1) : w == 4 ? 160 + ja : w == 5 && at < 8 ? 256 + ai : -1;
    char* const rst = lds + (g ? WBLK : 65536) + wj * 7424;
    u32x4 pre[11];
    if (at >= 8 || g == 0) {
      TOUCH_AT(2);
      const int T = at >= 8 ? 64 + 2 * bi + g : ai;
      unsigned* mds = (unsigned*)(lds + 2048) + 16 * g;
      char* qt = lds + L_RT + g * (16 * 1312);
      u32x4 ld[5];
      const bool e1 = at >= 8 && w == 3;
      if (e1) gemv_load(pre, gv_blk(128 + bi), bi & 7, 10);
      else __builtin_amdgcn_s_sleep(8);
#pragma unroll
      for (int j = 0; j < 5; ++j)
        ld[j] = __builtin_amdgcn_raw_buffer_load_b128(rq, (unsigned)(h4 * D_ * 2 + n16 * 16 + j * 256), (unsigned)((32 * (T >> 1) + 8 * wj + 4 * (T & 1)) * D_ * 2), NT);
      TAG_READY();
      {
        const bf2_t ONE2 = {(__bf16)1.0f, (__bf16)1.0f};
        char* dst = qt + (4 * wj + h4) * 1312 + n16 * 16;
        float sa = 0.f, va = 0.f;
#pragma unroll
        for (int j = 0; j < 5; ++j) {
          *(u32x4*)(dst + j * 256) = ld[j];
#pragma unroll
          for (int d = 0; d < 4; ++d) {
            const unsigned a0 = ld[j][d];
            const bf2_t xa = __builtin_bit_cast(bf2_t, a0);
            sa = __builtin_amdgcn_fdot2_f32_bf16(xa, ONE2, sa, false); va = __builtin_amdgcn_fdot2_f32_bf16(xa, xa, va, false);
          }
        }
        lf_set(8 + w);
        const float ma = sum16(sa) * (1.0f / D_);
        va = __builtin_fmaxf(sum16(va) * (1.0f / D_) - ma * ma, 0.f);
        if (n16 == 0) mds[4 * wj + h4] = pack2(ma, __builtin_amdgcn_sqrtf(rbf(rbf(va) + 1e-5f)));
      }
      if (e1) gv_fin(pre, 128 + bi);
      lf_wait(8 + 4 * g, 4);
      STAMP(23);
      {
        const char* src = qt + (l & 7) * 1312 + (40 * wj + (l >> 3)) * 8;
#pragma unroll
        for (int j = 0; j < 5; ++j) {
          const u32x2 x = *(const u32x2*)(src + j * 64), y = *(const u32x2*)(src + j * 64 + 8 * 1312);
          st16(rs, WS_QB + (unsigned)T * 20480u + (unsigned)(5 * wj + j) * 1024u + l16, (u32x4){x[0], x[1], y[0], y[1]});
        }
      }
      SCHED();
      const int g2 = at < 8 || w < 3 || w == 4 ? 400 : w == 3 ? 384 + bi : 256 + wj * 32 + bi;
      if (g2 < 400) {
        gemv_load(pre, gv_blk(g2), g2 & 7, 10);
        asm volatile("s_waitcnt vmcnt(10)" ::: "memory");
      } else if (w < 2 && rj >= 0) {
        rt_ld(rj, rst);
        asm volatile("s_waitcnt vmcnt(7)" ::: "memory");
      } else {
        DRAIN();
      }
      lf_set(16 + w);
      if (wj == 2) {
        lf_wait(16 + 4 * g, 4);
        st16(rs, WS_ST1 + (unsigned)(l >> 3) * 16384u + (unsigned)T * 128u + (unsigned)(l & 7) * 16u, (u32x4){mds[l & 7], tag, mds[8 + (l & 7)], tag});
        STAMP(22);
      }
      if (w >= 4 && w < 7) lf_set(w);
      if (g2 < 400) gv_fin(pre, g2);
      STAMP(25);
    } else {
      constexpr int KB = L_RT + 16 * 1312;
      TOUCH_AT(3);
      const int g1 = w < 6 ? 2 * ai + wj : 32 + wj * 64 + ai;
      if (w >= 6) {
        const rsrc_t rk = __builtin_amdgcn_make_buffer_rsrc((void*)(w == 6 ? p.k : p.v), 0, S_ * D_ * 2, 0x00020000);
        const unsigned ko = (unsigned)((1536 + 8 * ai) * (D_ * 2) - (w == 6 ? 4096 : 0));
        char* dst = lds + KB + (w == 6 ? 0 : 14336);
        __builtin_amdgcn_s_sleep(3);
#pragma unroll
        for (int n = 0; n < 10; ++n) __builtin_amdgcn_raw_ptr_buffer_load_lds(rk, (LAS void*)(dst + n * 1024), 16, l16, ko + (unsigned)n * 1024u, 0, NT);
        if (w == 6) {
#pragma unroll
          for (int n = 10; n < 14; ++n) __builtin_amdgcn_raw_ptr_buffer_load_lds(rk, (LAS void*)(dst + n * 1024), 16, l16, ko + (unsigned)n * 1024u, 0, NT);
        }
      }
      SCHED();
      u32x4 swt[2];
      swt[0] = *(const u32x4*)(p.shift_k + (tid - 256) * 8);
      swt[1] = *(const u32x4*)(p.shift_k + (256 + l) * 8);
      SCHED();
      if (w >= 6) __builtin_amdgcn_s_sleep(8);
      gemv_load(pre, gv_blk(g1), g1 & 7, 10);
      TAG_READY();
      asm volatile("s_waitcnt vmcnt(11)" : "+v"(swt[0]), "+v"(swt[1]) :: "memory");
      lf_set(8 + w);
      if (w < 6) gv_fin(pre, g1);
      STAMP(25);
      lf_wait(12, 4);
      STAMP(23);
#pragma unroll 1
      for (int n = 0; n < (w == 7 ? 2 : 1); ++n) {
        {
          const int pr = n ? 256 + l : tid - 256;
          const char* kp = lds + KB + 4096 + pr * 4;
          unsigned kk[11], vv[8];
#pragma unroll
          for (int i = 0; i < 11; ++i) kk[i] = *(const unsigned*)(kp + (i - 3) * 1280);
#pragma unroll
          for (int i = 0; i < 8; ++i) vv[i] = *(const unsigned*)(kp + 10240 + i * 1280);
          const u32x4 sw = n ? swt[1] : swt[0];
          const f32x2 wt[4] = {{bflo(sw[0]), bflo(sw[2])}, {bfhi(sw[0]), bfhi(sw[2])}, {bflo(sw[1]), bflo(sw[3])}, {bfhi(sw[1]), bfhi(sw[3])}};
          f32x2 o[8];
#pragma unroll
          for (int i = 0; i < 8; ++i) {
            f32x2 t = (f32x2){bflo(kk[i + 3]), bfhi(kk[i + 3])} * wt[0];
#pragma unroll
            for (int j = 1; j < 4; ++j) t = __builtin_elementwise_fma((f32x2){bflo(kk[i + 3 - j]), bfhi(kk[i + 3 - j])}, wt[j], t);
            o[i] = t * (f32x2){bflo(vv[i]), bfhi(vv[i])};
          }
#pragma unroll
          for (int e = 0; e < 4; ++e) {
            const int i = 7 - 4 * (e & 1);
            st16(rs, WS_KV3 + (unsigned)ai * 20480u + (unsigned)(e * 5120 + pr * 16),
                 (u32x4){pack2(o[i][e >> 1], o[i - 1][e >> 1]), tag, pack2(o[i - 2][e >> 1], o[i - 3][e >> 1]), tag});
          }
        }
      }
      STAMP(22);
      if (w < 7) lf_set(w);
      if (w >= 6) gv_fin(pre, g1);
      STAMPF(30);
    }
    if (w == 6 && ja < 80) {
      hold(AUX_T);
      gemv_load(pre, gv_blk(400 + ja), ja & 7, 10);
      gv_fin(pre, 400 + ja);
    }
    if (rj >= 0) {
      if (w >= 2) { hold(RT2_T); rt_ld(rj, rst); }
      rt_job(rj, rst);
    }
    STAMP(26);
    lf_set(w);
    if (w == 3) { hold(WA_T); wo_ld(); }
    STAMP(3);
    if (w < 4) {
      {
        const unsigned fo[1] = {l < 5 ? fwi + (unsigned)((8 + c) * 20 + 5 * w + l) * 128u : ~0u};
        hold(W_T);
        wait_lines<1, 8>(rs, fo, tag);
      }
      lf_wait(0, 8);
      STAMP(4);
    }
    if ((at == 8 || at == 9) && w == 5 && xcd * 2 + at - 8 < 10) {
      const unsigned o = WS_MODQ + (unsigned)(h4 * 5120 + 1920 + (xcd * 2 + at - 8) * 64 + n16 * 4) * 8u;
      u32x4 v[4];
      hold(CS_T);
      _Pragma("unroll 1") for (unsigned spins = 0; spins < SPIN_CAP; ++spins) {
        unsigned dd = 0u;
#pragma unroll
        for (int k = 0; k < 4; ++k) v[k] = ld16(rs, o + (unsigned)((k >> 1) * 20480 + (k & 1) * 16));
#pragma unroll
        for (int k = 0; k < 4; ++k) dd |= (v[k][0] ^ tag) | (v[k][2] ^ tag);
        if (__all(dd == 0u)) break;
        __builtin_amdgcn_s_sleep(16);
      }
      float t[4];
#pragma unroll
      for (int k = 0; k < 4; ++k) t[k] = sum_rows(__uint_as_float(v[k >> 1][1 + 2 * (k & 1)]) + __uint_as_float(v[2 + (k >> 1)][1 + 2 * (k & 1)]));
#pragma unroll
      for (int e = 0; e < 2; ++e)
        st16(rs, WS_MOD + (unsigned)(4 * e + h4) * 2560u + (unsigned)((xcd * 2 + at - 8) * 16 + n16) * 16u, (u32x4){tag, pack2(t[0], t[1]), tag, pack2(t[2], t[3])});
    }
    if (w == 4 || (w == 5 && at >= 10)) {
      const int cj = w == 4 ? ja : 86 + xcd * 2 + at, cb = cj / 7, j = cj % 7;
      const unsigned o = WS_CSP + (unsigned)((cb * 20 + 5 * h4) * 2 + (j >> 2)) * 1024u + (unsigned)((j & 3) * 16 + n16) * 16u;
      u32x4 v[5];
      hold(CS_T);
      __builtin_amdgcn_s_setprio(3);
      _Pragma("unroll 1") for (unsigned spins = 0; spins < SPIN_CAP; ++spins) {
        unsigned dd = 0u;
#pragma unroll
        for (int k = 0; k < 5; ++k) v[k] = ld16(rs, o + (unsigned)k * 2048u);
#pragma unroll
        for (int k = 0; k < 5; ++k) dd |= (v[k][0] ^ tag) | (v[k][2] ^ tag);
        if (__all(dd == 0u)) break;
        __builtin_amdgcn_s_sleep(8);
      }
      float t0 = 0.f, t1 = 0.f;
#pragma unroll
      for (int k = 0; k < 5; ++k) { t0 += __uint_as_float(v[k][1]); t1 += __uint_as_float(v[k][3]); }
      t0 = sum_rows(t0); t1 = sum_rows(t1);
      if (l < 16) st16(rs, WS_CS + (unsigned)(cb * 2048 + j * 256) + l16, (u32x4){tag, __float_as_uint(t0), tag, __float_as_uint(t1)});
      __builtin_amdgcn_s_setprio(0);
      STAMP(24);
    }
  }

  STAMPF(16);
  u32x4 wox[5];
  DRAIN();
  if (w == 3) {
    const char* bp = wo_st + (4 * h4 + (n16 >> 2)) * 160 + (n16 & 3) * 8;
#pragma unroll
    for (int dj = 0; dj < 5; ++dj) wox[dj] = trfrag(bp + dj * 32, 160);
    asm volatile("s_waitcnt lgkmcnt(0)" : "+v"(wox[0]), "+v"(wox[1]), "+v"(wox[2]), "+v"(wox[3]), "+v"(wox[4]) :: "memory");
  }
  auto wo_put = [&]() {
    if (w == 3 && R < 28) {
      const unsigned base = WS_WOUTF + (unsigned)c * WBLK + (unsigned)((R >> 1) * 10240 + (R & 1) * 1024);
#pragma unroll
      for (int dj = 0; dj < 5; ++dj) st16(rs, base + (unsigned)((R & 1) ? dj : (dj + 2) % 5) * 2048u + l16, wox[dj]);
    }
  };
  if (w < 4) load_b2();
  STAMPF(29);
  u32x4 k_c;
  unsigned kc_o;
  {
    const int i = tid < 224 ? tid : 0;
    const int col = i >= 112 ? (8 + c) * 128 + i - 112 : c * 128 + i;
    kc_o = w < 4 ? WS_CS + (unsigned)col * 16u : WS_MOD + (unsigned)xcd * 2560u + (unsigned)(20 * c + (tid < 276 ? tid - 256 : 0)) * 16u;
  }
  auto consts_put = [&]() {
    if (!__all(k_c[0] == tag && k_c[2] == tag)) {
      _Pragma("unroll 1") for (unsigned spins = 0; spins < SPIN_CAP; ++spins) {
        __builtin_amdgcn_s_sleep(8);
        k_c = ld16(rs, kc_o);
        if (__all(k_c[0] == tag && k_c[2] == tag)) break;
      }
    }
    if (tid < 224) *(u32x2*)(lds + L_CONST + tid * 8) = (u32x2){k_c[1], k_c[3]};
    if (tid >= 256 && tid < 276) *(f32x4*)(lds + L_MISC + (tid - 256) * 16) = (f32x4){bflo(k_c[1]), bfhi(k_c[1]), bflo(k_c[3]), bfhi(k_c[3])};
  };
  const int wq = w ^ rotr, rt1 = wq & 3, kh1 = w >> 2;
  unsigned bo0 = (unsigned)(kh1 * 71680) + l16, bo1 = bo0 + 35840u;
  asm volatile("" : "+v"(bo0), "+v"(bo1));
#define BFRAG(q, j) (*(const u32x4*)(lds + ((q) < 5 ? bo0 : bo1) + ((q) % 5) * 7168 + (j) * 1024))
  u32x4 h2[10];
  f32x4 acc[7];
  float mu_n, rs_n;
  {
    if (w == 6) {
      const unsigned fo[1] = {l < 20 ? fwi + (unsigned)(c * 20 + l) * 128u : ~0u};
      hold(CS_T);
      wait_lines<1, 8>(rs, fo, tag);
    }
    if (w == 7) {
      const unsigned fb = WS_FX + (unsigned)(((R >> 1) * 4 + (R & 1) * 2 + (c >> 2)) * 160 + l) * 128u;
      const unsigned fo[3] = {fb, fb + 8192u, l < 32 ? fb + 16384u : ~0u};
      if (!is_conv) hold(Y_T);
      wait_lines<3, 2>(rs, fo, tag);
      if (l == 0) lf[33] = tag;
    }
    lf_wait(33, 1);
    STAMP(7);
    const unsigned xo = (unsigned)(R * 4 + rt1) * 20480u + (unsigned)(10 * kh1) * 1024u +
                        (unsigned)((4 * ((l >> 3) & 1) + h4) * 128 + (l & 7) * 16);
    SCHED();
#pragma unroll
    for (int q = 0; q < 3; ++q) h2[q] = ld16p(rs, WS_X1B + xo + (unsigned)q * 1024u);
    SCHED();
    __builtin_amdgcn_s_waitcnt(0x0F73);
    BAR();
    STAMP(8);
#pragma unroll
    for (int j = 0; j < 7; ++j) acc[j] = (f32x4){0.f, 0.f, 0.f, 0.f};
    f32x4 accg = {0.f, 0.f, 0.f, 0.f};
    u32x4 bf[7];
#pragma unroll
    for (int j = 0; j < 7; ++j) bf[j] = BFRAG(0, j);
    SCHED();
#pragma unroll
    for (int q = 3; q < 6; ++q) h2[q] = ld16p(rs, WS_X1B + xo + (unsigned)q * 1024u);
    SCHED();
#pragma unroll
    for (int q = 0; q < 10; ++q) {
      {
#pragma unroll
        for (int e = 0; e < 6; ++e) {
          const int d = e & 1;
          if ((e >> 1) == 1) h2[q][2 + d] ^= (unsigned)__builtin_amdgcn_update_dpp(0, (int)h2[q][d], 0x128, 0xf, 0x3, false);
          else h2[q][d] ^= (unsigned)__builtin_amdgcn_update_dpp(0, (int)h2[q][2 + d], 0x128, 0xf, 0xC, false);
        }
      }
      if (q == 5) { __builtin_amdgcn_s_barrier(); asm volatile("" ::: "memory"); }
      SCHED();
      accg = MFMA16(h2[q], h2[q], accg);
      SCHED();
#pragma unroll
      for (int j = 0; j < 7; ++j) {
        acc[j] = MFMA16(h2[q], bf[j], acc[j]);
        if (q == 9 && j == 1 && h4 == (n16 >> 2)) *(f32x4*)(lds + L_PST + 512 + (wq * 16 + n16) * 16) = accg;
        if (q < 9) bf[j] = BFRAG(q + 1, j);
#define WQ0 2
        if (WQ0 == 2 && q >= 2 && j == 2) LDW(q - 2);
        if (WQ0 == 6 && q >= 6 && (j == 2 || j == 4)) LDW(2 * (q - 6) + (j >> 2));
        if ((j == 0 || j == 4) && 6 + 2 * q + (j >> 2) < 10) h2[6 + 2 * q + (j >> 2)] = ld16p(rs, WS_X1B + xo + (unsigned)(6 + 2 * q + (j >> 2)) * 1024u);
        if (q == 4 && j == 0) k_c = ld16(rs, kc_o);
        if (q >= 5 && j == 5) {
          const int i = (5 + q) * 7 + (w < 7 ? w : 0);
          dma1k(rs, lds + i * 1024, l16, WS_WINF + (unsigned)c * WBLK + (unsigned)i * 1024u);
          if (q == 9) { const int i2 = 28 + (w < 7 ? w : 0); dma1k(rs, lds + i2 * 1024, l16, WS_WINF + (unsigned)c * WBLK + (unsigned)i2 * 1024u); }
        }
        SCHED();
      }
    }
    STAMP(31);
  }
  auto gelu_n = [&](f32x2* x, auto N_) {
    constexpr int N = decltype(N_)::value;
    const float c0 = -2.0f * 0.7978845608028654f * 1.4426950408889634f, c1 = c0 * 0.044715f;
    f32x2 k0 = {c0, c0}, k1 = {c1, c1}, k2 = {1.0f, 1.0f};
    asm volatile("" : "+v"(k0), "+v"(k1), "+v"(k2));
    f32x2 t[N];
#define GST(e) do { _Pragma("unroll") for (int i = 0; i < N; ++i) { t[i] = (e); asm volatile("" : "+v"(t[i])); } } while (0)
    GST(x[i] * x[i]);
    GST(__builtin_elementwise_fma(t[i], k1, k0));
    GST(t[i] * x[i]);
    GST(((f32x2){__builtin_amdgcn_exp2f(t[i][0]), __builtin_amdgcn_exp2f(t[i][1])}));
    GST(t[i] + k2);
    GST(((f32x2){__builtin_amdgcn_rcpf(t[i][0]), __builtin_amdgcn_rcpf(t[i][1])}));
#pragma unroll
    for (int i = 0; i < N; ++i) { x[i] *= t[i]; asm volatile("" : "+v"(x[i])); }
  };
  {
    f32x2* red = (f32x2*)lds;
    f32x2 mine[7];
    if (kh1) {
#pragma unroll
      for (int j = 0; j < 7; ++j) { red[(w * 7 + j) * 64 + l] = (f32x2){acc[j][0], acc[j][1]}; mine[j] = (f32x2){acc[j][2], acc[j][3]}; }
    } else {
#pragma unroll
      for (int j = 0; j < 7; ++j) { red[(w * 7 + j) * 64 + l] = (f32x2){acc[j][2], acc[j][3]}; mine[j] = (f32x2){acc[j][0], acc[j][1]}; }
    }
    f32x2 csb[7];
    unsigned cso = (unsigned)(L_CONST + (112 + n16) * 8);
    asm volatile("" : "+v"(cso));
    consts_put();
    unsigned pso[3], pqo[3], oto = (unsigned)(((w ^ 4) * 7 * 64 + l) * 8);
#pragma unroll
    for (int e = 0; e < 3; ++e) {
      const int row = rt1 * 16 + (e < 2 ? 4 * h4 + 2 * kh1 + e : n16);
      pso[e] = (unsigned)(L_PST + row * 4); pqo[e] = (unsigned)(L_PST + 512 + row * 16 + (row & 3) * 4);
    }
    asm volatile("" : "+v"(pso[0]), "+v"(pso[2]), "+v"(pqo[0]), "+v"(pqo[1]), "+v"(pqo[2]), "+v"(oto));
    pso[1] = pso[0] + 4u;
    if (n16 == 12) *(f32x4*)(lds + L_PST + (wq * 16 + 4 * h4) * 4) = acc[6];
    if (!kh1) {
#pragma unroll
      for (int k = 8; k < 16; ++k) LDW(k);
    }
    BAR();
    if (kh1) {
#pragma unroll
      for (int k = 8; k < 16; ++k) LDW(k);
    }
    __builtin_amdgcn_s_setprio(3);
    STAMPT(9);
    f32x2 oth[7];
    f32x2 pa[3], pb[3];
#pragma unroll
    for (int e = 0; e < 3; ++e) {
      pa[e] = (f32x2){*(const float*)(lds + pso[e]), *(const float*)(lds + pqo[e])};
      pb[e] = (f32x2){*(const float*)(lds + pso[e] + 256), *(const float*)(lds + pqo[e] + 1024)};
    }
#pragma unroll
    for (int j = 0; j < 7; ++j) { oth[j] = *(const f32x2*)(lds + oto + j * 512); csb[j] = *(const f32x2*)(lds + cso + 128 * j); }
    SCHED();
    float mu2[2], rs2[2];
#pragma unroll
    for (int e = 0; e < 3; ++e) {
      const float mean = (pa[e][0] + pb[e][0]) * (1.0f / D_);
      const float var = __builtin_fmaxf((pa[e][1] + pb[e][1]) * (1.0f / D_) - mean * mean, 0.f);
      const float rstd = __builtin_amdgcn_rsqf(var + 1e-5f);
      if (e < 2) { mu2[e] = mean; rs2[e] = rstd; } else { mu_n = mean; rs_n = rstd; }
    }
    f32x2 zz[7];
    const f32x2 nmu = {-mu2[0], -mu2[1]}, rsv = {rs2[0], rs2[1]};
#pragma unroll
    for (int j = 0; j < 7; ++j)
      zz[j] = __builtin_elementwise_fma(__builtin_elementwise_fma(nmu, (f32x2){csb[j][0], csb[j][0]}, mine[j] + oth[j]), rsv, (f32x2){csb[j][1], csb[j][1]});
    gelu_n(zz, IC<7>());
    if (n16 >= 12) zz[6] = (f32x2){0.f, 0.f};
    f32x2 s1 = ((zz[0] + zz[1]) + (zz[2] + zz[3])) + ((zz[4] + zz[5]) + zz[6]);
    f32x2 s2 = zz[0] * zz[0], s3 = zz[1] * zz[1];
#pragma unroll
    for (int j = 2; j < 7; ++j) { if (j & 1) s3 = __builtin_elementwise_fma(zz[j], zz[j], s3); else s2 = __builtin_elementwise_fma(zz[j], zz[j], s2); }
    s2 += s3;
    asm("" : "+v"(s1), "+v"(s2));
    float a0 = s1[0] + dpp_f<0xB1>(s1[0]), a1 = s1[1] + dpp_f<0xB1>(s1[1]), b0 = s2[0] + dpp_f<0xB1>(s2[0]);
    a0 = (l & 1) ? a1 : a0;
    const float b1 = s2[1] + dpp_f<0xB1>(s2[1]);
    b0 = (l & 1) ? b1 : b0;
    a0 += dpp_f<0x4E>(a0); b0 += dpp_f<0x4E>(b0);
    a0 += dpp_f<0x124>(a0); b0 += dpp_f<0x124>(b0);
    a0 += dpp_f<0x128>(a0); b0 += dpp_f<0x128>(b0);
    {
      const int src = ((l >> 1) & 3) * 64 + (l & 1) * 4;
      const unsigned pa0 = __builtin_amdgcn_ds_bpermute(src, __float_as_uint(a0)), pb0 = __builtin_amdgcn_ds_bpermute(src, __float_as_uint(b0));
      if (l < 8) st16(rs, WS_PART + (unsigned)((R * 8 + c) * 8 + wq) * 128u + l16, (u32x4){tag, pa0, tag, pb0});
    }
    SCHED();
    __builtin_amdgcn_s_setprio(0);
    STAMP(10);
#pragma unroll
    for (int k = 0; k < 10; ++k) {
      if (k < 9) {
        int x = k * 8 + w;
        x = x < 70 ? x : 69;
        const int i = x + (x < 35 ? 35 : 70);
        dma1k(rs, lds + i * 1024, l16, WS_WINF + (unsigned)c * WBLK + (unsigned)i * 1024u);
      }
    }
    wo_put();
#pragma unroll
    for (int j = 0; j < 7; ++j)
      *(unsigned*)(lds + L_Z + (((rt1 >> 1) * 7 + j) * 64 + l) * 16 + (4 * (rt1 & 1) + 2 * kh1) * 2) = pack2(zz[j][0], zz[j][1]);
    SCHED();
    u32x4 v[8];
    const unsigned po = WS_PART + (unsigned)(R * 8) * 1024u + l16;
#pragma unroll
    for (int g = 0; g < 4; ++g) WSP_TR(g);
    if (w == 2) {
      __builtin_amdgcn_s_setprio(3);
      unsigned need = 0xffu;
      if (need != 0u) {
        _Pragma("unroll 1") for (unsigned spins = 0; spins < SPIN_CAP; ++spins) {
#pragma unroll
          for (int k = 0; k < 8; ++k)
            if (need & (1u << k)) v[k] = ld16(rs, po + (unsigned)k * 1024u);
#pragma unroll
          for (int k = 0; k < 8; ++k)
            if (__all(v[k][0] == tag && v[k][2] == tag)) need &= ~(1u << k);
          if (need == 0u) break;
          __builtin_amdgcn_s_sleep(1);
        }
      }
      float t1 = 0.f, t2 = 0.f;
#pragma unroll
      for (int k = 0; k < 8; ++k) { t1 += __uint_as_float(v[k][1]); t2 += __uint_as_float(v[k][3]); }
      const float mean = t1 * (1.0f / FH);
      const float var = __builtin_fmaxf(t2 * (1.0f / FH) - mean * mean, 0.f);
      const float rstd = __builtin_amdgcn_rsqf(var + 1e-5f);
      const int sg = 16 * ((l >> 3) & 3) + 4 * ((l >> 1) & 3) + 2 * (l >> 5) + (l & 1);
      *(f32x2*)(lds + L_ST2 + sg * 8) = (f32x2){rstd, -mean * rstd};
    }
    STAMPT(11);
    DRAIN();
    if (w == 3) st_flag4(rs, WS_FWO + (unsigned)(c * 32 + R) * 128u, tag, l);
    BAR();
    __builtin_amdgcn_s_setprio(3);
    STAMP(12);
    u32x4 zf[2];
#pragma unroll
    for (int m = 0; m < 2; ++m) zf[m] = *(const u32x4*)(lds + L_Z + ((m * 7 + (w < 7 ? w : 0)) * 64 + l) * 16);
    if (w < 7) {
#pragma unroll
      for (int m = 0; m < 2; ++m) {
        const f32x4* sp = (const f32x4*)(lds + L_ST2 + (32 * m + 4 * h4) * 8);
        u32x4 o;
#pragma unroll
        for (int e = 0; e < 4; ++e) {
          f32x4 st = sp[(e & 1) + 8 * (e >> 1)];
          o[e] = pack2(__builtin_fmaf(bflo(zf[m][e]), st[0], st[1]), __builtin_fmaf(bfhi(zf[m][e]), st[2], st[3]));
        }
        st16(rs, WS_Z2F + (unsigned)((c * 64 + 4 * (R >> 1) + h4) * 7 + (w < 6 ? w + 1 : 0)) * 1024u + (unsigned)((2 * (R & 1) + m) * 256 + n16 * 16), o);
      }
    }
    SCHED();
    __builtin_amdgcn_s_setprio(0);
#pragma unroll
    for (int k = 0; k < 4; ++k)
      if (w < 7) dma1k(rs, lds + (k * 7 + w) * 1024, l16, WS_WINF + (unsigned)c * WBLK + (unsigned)(k * 7 + w) * 1024u);
    SCHED();
    STAMPT(13);
  }
  unsigned bs_raw;
  unsigned z1d[7];
  constexpr int L_RED = 28672;
#define WOA(i) (((i) < 28 ? (i) : (i) < 108 ? (i) + 32 : (i) < 120 ? (i) + 37 : (i) - 92) * 1024)
#define RBAR() do { __builtin_amdgcn_s_barrier(); asm volatile("" ::: "memory"); } while (0)
#define ZAUX 16
#define NW 14
  auto wo_dma = [&](int k) {
    int i = k < 8 ? (k + (idx >> 2)) & 7 : k + (idx >> 2);
    i = (i >= 15 ? i - 7 : i) * 8 + w;
    const int a = i + ((27 - i) >> 31 & 32) + ((107 - i) >> 31 & 5);
    dma1k(rs, lds + a * 1024, l16, WS_WOUTF + (unsigned)c * WBLK + (unsigned)i * 1024u);
  };
  unsigned zs[8];
#pragma unroll
  for (int t = 0; t < 8; ++t) zs[t] = WS_Z2F + (unsigned)(c * 64 + 8 * w + 4 * ((t >> 2) ^ rotg) + ((t + rotb) & 3)) * 7168u;
  u32x4 zw[NW];
#define LDZ(f) do { if (((f) >> 3) == 4) { zs[(f) & 7] += 4096u; asm volatile("" : "+s"(zs[(f) & 7])); } \
    zw[(f) % NW] = __builtin_amdgcn_raw_buffer_load_b128(rs, l16 + (unsigned)(((f) >> 3) & 3) * 1024u, zs[(f) & 7], ZAUX); } while (0)
  {
    f32x4 acc[7];
#pragma unroll
    for (int j = 0; j < 7; ++j) acc[j] = (f32x4){0.f, 0.f, 0.f, 0.f};
    u32x4 bf[7];
#pragma unroll
    for (int j = 0; j < 7; ++j) bf[j] = BFRAG(4, j);
#pragma unroll
    for (int qi = 0; qi < 10; ++qi) {
      const int q = (qi + 4) % 10, qn = (qi + 5) % 10;
      if (qi == 5) {
        asm volatile("s_waitcnt vmcnt(8)" ::: "memory");
        __builtin_amdgcn_s_barrier();
        asm volatile("" ::: "memory");
        if (w == 7) st_flag4(rs, WS_FZ + (unsigned)(c * 32 + R) * 128u, tag, l);
        STAMP(6);
#pragma unroll
        for (int k = 24; k < 32; ++k) LDW(k);
        bs_raw = p.b_sp[R * 64 + tau(rt1, n16)];
      }
      SCHED();
#pragma unroll
      for (int j = 0; j < 7; ++j) {
        acc[j] = MFMA16(bf[j], h2[q], acc[j]);
        if (qi < 9) bf[j] = BFRAG(qn, j);
        if (qi >= 1 && qi < 5 && (j == 1 || j == 4)) LDW(16 + 2 * (qi - 1) + (j >> 2));
        SCHED();
      }
    }
    STAMPT(29);
    f32x2* red = (f32x2*)(lds + L_RED);
    f32x2 mine[7];
    if (kh1) {
#pragma unroll
      for (int j = 0; j < 7; ++j) { red[(w * 7 + j) * 64 + l] = (f32x2){acc[j][0], acc[j][1]}; mine[j] = (f32x2){acc[j][2], acc[j][3]}; }
    } else {
#pragma unroll
      for (int j = 0; j < 7; ++j) { red[(w * 7 + j) * 64 + l] = (f32x2){acc[j][2], acc[j][3]}; mine[j] = (f32x2){acc[j][0], acc[j][1]}; }
    }
#define WOE 15
#define TRA 6
#define POLLP 1
#pragma unroll
    for (int g = 4; g < TRA; ++g) WSP_TR(g);
    if (WOE > 0 && w == 3) {
      const unsigned fo[1] = {l < 28 ? fwi + (WS_FWO - WS_FWI) + (unsigned)(c * 32 + l) * 128u : ~0u};
      wait_lines<1, 1>(rs, fo, tag);
    }
    BAR();
    STAMP(14);
    f32x2 oth[7];
    f32x4 cc[7];
#pragma unroll
    for (int j = 0; j < 7; ++j) {
      oth[j] = red[((w ^ 4) * 7 + j) * 64 + l];
      cc[j] = *(const f32x4*)(lds + L_CONST + (16 * j + 4 * h4 + 2 * kh1) * 8);
    }
    SCHED();
#pragma unroll
    for (int k = 0; k < WOE; ++k) wo_dma(k);
    SCHED();
    f32x2 z[7];
#pragma unroll
    for (int j = 0; j < 7; ++j) {
      z[j] = __builtin_elementwise_fma(__builtin_elementwise_fma((f32x2){-mu_n, -mu_n}, (f32x2){cc[j][0], cc[j][2]}, oth[j] + mine[j]),
                                       (f32x2){rs_n, rs_n}, (f32x2){cc[j][1], cc[j][3]});
      asm volatile("" : "+v"(z[j]));
    }
    SCHED();
    auto z_start = [&]() {
      const unsigned fo[1] = {l < 4 ? fz + (unsigned)(c * 32 + 4 * w + l) * 128u : ~0u};
      wait_lines<1, 1>(rs, fo, tag);
      STAMPT(23);
#pragma unroll
      for (int f = 0; f < NW; ++f) { LDZ(f); SCHED(); }
    };
    auto z1_fin = [&]() {
      gelu_n(z, IC<7>());
#pragma unroll
      for (int j = 0; j < 7; ++j) z1d[j] = pack2(z[j][0], z[j][1]);
      asm volatile("" : "+v"(z1d[0]), "+v"(z1d[1]), "+v"(z1d[2]), "+v"(z1d[3]), "+v"(z1d[4]), "+v"(z1d[5]), "+v"(z1d[6]));
      SCHED();
      STAMPT(30);
    };
    if (POLLP == 1) { z_start(); z1_fin(); }
    else if (POLLP == 0) { z1_fin(); z_start(); }
    else if (kh1) { z_start(); z1_fin(); }
    else { z1_fin(); z_start(); }
  }

#define WO3 (-92)
#define WO2 WOE
#define TRC TRA
#define VMW(n) asm volatile("s_waitcnt vmcnt(%0)" ::"n"(n) : "memory")
  {
    SCHED();
#pragma unroll
    for (int g = TRC; g < 8; ++g) WSP_TR(g);
    SCHED();
    STAMP(20);
    const int rt = rt1, kh = kh1;
    unsigned rw = (unsigned)(L_RED + w * 512 + l * 8), rr = (unsigned)(L_RED + w * 4096 + l * 8);
    asm volatile("" : "+v"(rw), "+v"(rr));
    const float bs = bflo(bs_raw);
    const unsigned uo = WS_UF + (unsigned)((R * 4 + rt) * 8 + c) * 4096u + (unsigned)kh * 2048u;
    const unsigned ui = WS_UF + (unsigned)((R * 4 + rt) * 8 + 4 * kh) * 4096u;
    unsigned long long hm = 0xffff000000000000ull;
    asm volatile("" : "+s"(hm));
    unsigned l8;
    f32x4 acc2[2][4];
    float rv[8][2];
    u32x4 ud;
    u32x4 P[2][8];
    u32x2 xp[3];
    auto ld_p = [&](int pt, int i) { P[pt][i] = __builtin_amdgcn_raw_buffer_load_b128(rs, l16 + (unsigned)pt * 1024u, ui + (unsigned)i * 2048u, SC1); };
    auto red_put = [&](int j, int r) {
      *(f32x2*)(lds + rw + r * 4096) = (f32x2){acc2[j & 1][r][0], acc2[j & 1][r][1]};
      *(f32x2*)(lds + rw + (r + 4) * 4096) = (f32x2){acc2[j & 1][r][2], acc2[j & 1][r][3]};
    };
    auto red_put1 = [&](int j, int i) { *(volatile LAS float*)(lds + rw + (i >> 2) * 4096 + ((i >> 1) & 1) * 16384 + (i & 1) * 4) = acc2[j & 1][i >> 2][i & 3]; };
    auto red_get = [&](int i) { const f32x2 x = *(const volatile LAS f32x2*)(lds + rr + i * 512); rv[i][0] = x[0]; rv[i][1] = x[1]; };
    float zz[2], gg[2];
    unsigned cs;
    auto sum_rows_u = [&](unsigned v) {
      auto a = __builtin_amdgcn_permlane16_swap(v, v, false, false);
      v = a[0] + a[1];
      auto b = __builtin_amdgcn_permlane32_swap(v, v, false, false);
      return b[0] + b[1];
    };
    auto red_fin = [&](int j, int k) {
      const int e = k & 1, i = k >> 1;
#define VADD(d, x, y) asm volatile("v_add_f32 %0, %1, %2" : "=v"(d) : "v"(x), "v"(y))
      if (i < 4) VADD(rv[2 * i][e], rv[2 * i][e], rv[2 * i + 1][e]);
      if (i == 4 || i == 5) VADD(rv[4 * (i & 1)][e], rv[4 * (i & 1)][e], rv[4 * (i & 1) + 2][e]);
      if (i == 6) VADD(rv[0][e], rv[0][e], bs);
      if (i == 7) VADD(gg[e], rv[0][e], rv[4][e]);
      if (i == 8) { zz[e] = e ? bfhi(z1d[(j + 6) % 7]) : bflo(z1d[(j + 6) % 7]); asm volatile("" : "+v"(zz[e])); }
      if (i == 9) { zz[e] *= gg[e]; asm volatile("" : "+v"(zz[e])); }
      if (k == 20) { ud[j & 3] = pack2(zz[0], zz[1]); asm volatile("" : "+v"(ud[j & 3])); }
      if (k == 21) {
        if (j == 0) asm volatile("v_cndmask_b32 %0, %0, 0, %1" : "+v"(ud[0]) : "s"(hm));
        if (j == 2) asm volatile("v_add3_u32 %0, %1, %2, %3" : "=v"(cs) : "v"(ud[0]), "v"(ud[1]), "v"(ud[2]));
        if (j == 3) {
          cs = tag - sum_rows_u(cs + ud[3]);
          asm volatile("v_cndmask_b32 %0, %0, %1, %2" : "+v"(ud[0]) : "v"(cs), "s"(hm));
        }
        if (j >= 4) { ud[3] = (j == 4 ? tag : ud[3]) - ud[j & 3]; asm volatile("" : "+v"(ud[3])); }
        if (j % 3 == 0 && j) __builtin_amdgcn_raw_buffer_store_b128(ud, rs, l16 + (unsigned)(j / 6) * 1024u, uo, SC1);
      }
    };
#define P1M 4
#define G2S 0
    struct Sch { int wide, lp, prio, lf, b1, b2, wp[16], rp[8], vp[22]; };
    constexpr Sch SC[4] = {
      {0, 2, 8, 1, 18, 29, {0, 1, 2, 3, 4, 5, 6, 7, 8, 9, 10, 11, 12, 13, 14, 15}, {19, 19, 20, 20, 21, 21, 22, 22}, {29, 29, 30, 30, 31, 31, 32, 33, 35, 36, 37, 38, 39, 40, 41, 42, 43, 44, 45, 46, 47, 48}},
      {1, 3, 4, 0, 18, 28, {3, 7, 11, 15, 99, 99, 99, 99, 99, 99, 99, 99, 99, 99, 99, 99}, {20, 20, 21, 21, 22, 22, 24, 24}, {28, 28, 29, 29, 30, 30, 31, 31, 32, 32, 33, 33, 34, 34, 35, 36, 36, 37, 37, 38, 38, 39}},
      {0, 1, 4, 1, 17, 29, {0, 1, 2, 3, 4, 5, 6, 7, 8, 9, 10, 11, 12, 13, 14, 15}, {17, 17, 18, 18, 19, 19, 20, 20}, {29, 29, 30, 30, 31, 31, 32, 34, 35, 36, 38, 39, 40, 42, 43, 44, 45, 46, 47, 48, 48, 49}},
      {0, 2, 0, 1, 17, 25, {0, 1, 2, 3, 4, 5, 6, 7, 8, 9, 10, 11, 12, 13, 14, 15}, {17, 17, 18, 18, 19, 19, 20, 20}, {25, 25, 26, 26, 27, 27, 28, 28, 29, 29, 30, 30, 31, 31, 32, 33, 23, 24, 34, 35, 36, 37}}};
#define JF 4
#define PC0 24
#define P1D 2
#define VL 2
    constexpr Sch S = SC[G2S];
    constexpr Sch S5 = {0, S.lp, S.prio, S.lf, 17, 24, {0, 1, 2, 3, 4, 5, 6, 7, 8, 9, 10, 11, 12, 13, 14, 15}, {17, 17, 18, 18, 19, 19, 20, 20}, {24, 24, 25, 25, 26, 26, 27, 27, 28, 28, 29, 29, 30, 30, 31, 31, 22, 23, 32, 32, 33, 33}};
    constexpr Sch S6 = {1, S.lp, S.prio, S.lf, 5, 12, {99, 99, 99, 3, 99, 99, 99, 99, 99, 99, 99, 99, 99, 99, 99, 99}, {5, 5, 6, 6, 7, 7, 8, 8},
                        {12, 12 + 1 / VL, 12 + 2 / VL, 12 + 3 / VL, 12 + 4 / VL, 12 + 5 / VL, 12 + 6 / VL, 12 + 7 / VL, 12 + 8 / VL, 12 + 9 / VL, 12 + 10 / VL, 12 + 11 / VL, 12 + 12 / VL, 12 + 13 / VL, 12 + 14 / VL, 12 + 15 / VL, 10, 11,
                         12 + 16 / VL, 12 + 17 / VL, 12 + 18 / VL, 12 + 19 / VL}};
    unsigned wb[3], xo;
#define XPM 19
    unsigned xs = WS_X1B + (unsigned)(R * 4 + rt) * 20480u + (unsigned)(5 * c + 2 * kh) * 512u, wl[3], wg = WS_WOUTF + (unsigned)c * WBLK - (unsigned)(WO3 * 1024);
#pragma unroll
    for (int k = 0; k < 3; ++k) wl[k] = (unsigned)(120 + WO3 + (k < 2 || w < 4 ? w + 8 * k : w)) * 1024u;
    asm volatile("" : "+s"(xs), "+s"(wg), "+s"(wl[0]), "+s"(wl[2]));
#define NAF 32
#define NA0 18
    u32x4 af[NAF];
#define LDA(m) af[(m) % NAF] = *(const u32x4*)(lds + wb[WOA(2 * (m)) >> 16] + (WOA(2 * (m)) & 65535))
    unsigned ck[2];
    const unsigned tag4 = tag * 4u;
    auto p_chk = [&](int pt, int pr, int k) {
      u32x4* q = P[pt] + 4 * pr;
      if (k < 2) ck[k & 1] = q[k & 1][0] + q[k & 1][1];
      else if (k < 8) ck[k & 1] = ck[k & 1] + q[(k & 1) + 2 * ((k >> 2) & 1)][k & 2] + q[(k & 1) + 2 * ((k >> 2) & 1)][(k & 2) + 1];
      else if (k == 8) ck[0] = pt ? ck[0] + ck[1] : sum_rows_u(ck[0] + ck[1]);
      if (k < 9) asm volatile("" : "+v"(ck[k & 1]));
      if (k == 9) {
        if (__builtin_expect(__any(ck[0] != tag4), 0)) {
          _Pragma("unroll 1") for (unsigned spins = 0; spins < SPIN_CAP; ++spins) {
            unsigned d = 0u;
#pragma unroll
            for (int i = 0; i < 4; ++i) ld_p(pt, 4 * pr + i);
#pragma unroll
            for (int i = 0; i < 4; ++i) d += q[i][0] + q[i][1] + q[i][2] + q[i][3];
            if (!pt) d = sum_rows_u(d);
            if (!__any(d != tag4)) break;
            __builtin_amdgcn_s_sleep(1);
          }
        }
      }
      if (k >= 10 && !pt) { unsigned x = q[(k - 10) & 3][0]; asm volatile("v_cndmask_b32 %0, %0, 0, %1" : "+v"(x) : "s"(hm)); q[(k - 10) & 3][0] = x; }
      if (k >= 10 && k < 13 && pt) { q[(k - 10) % 3][3] = q[3][(k - 10) % 3]; asm volatile("" : "+v"(q[(k - 10) % 3])); }
    };
    sfor<0, 224>([&](auto M_) __attribute__((always_inline)) {
      constexpr int mm = decltype(M_)::value, j = mm >> 5, m = mm & 31;
      constexpr bool ro = j >= 5 && m >= 16;
      constexpr int t = ro ? 4 + (m & 3) : m >> 2, r = ro ? (m >> 2) & 3 : m & 3, f = 8 * j + t;
      constexpr Sch C = j < JF ? S : j < 6 ? S5 : S6, B = j <= JF ? S : S5;
      if (t == 0) asm volatile("v_mfma_f32_16x16x32_bf16 %0, %1, %2, 0" : "=&v"(acc2[j & 1][r]) : "v"(zw[f % NW]), "v"(wsp[r][t]), "v"(zw[(f | 1) % NW]));
      else asm volatile("v_mfma_f32_16x16x32_bf16 %0, %1, %2, %0" : "+v"(acc2[j & 1][r]) : "v"(zw[f % NW]), "v"(wsp[r][t]), "v"(zw[(f | 1) % NW]));
      SCHED();
      if (S.prio && (m + 31 - S.lp) % (S.prio ? S.prio : 1) == 0) __builtin_amdgcn_s_setprio(1);
      auto ld = [&]() {
        if ((m & 3) == S.lp) {
          if (S.prio && (m + 32 - S.lp) % (S.prio ? S.prio : 1) == 0) __builtin_amdgcn_s_setprio(0);
          constexpr int g = S.lp == 3 ? mm >> 2 : (mm >> 2) - 1;
          if (g >= 0 && g + NW < 56) LDZ(g + NW);
          constexpr int y = g + NW - 56;
          if (y >= 0 && WO2 + y < 15) wo_dma(WO2 + y);
          SCHED();
        }
        if (j == 6 && m >= P1M && m < P1M + 8 * P1D && (m - P1M) % P1D == 0) { ld_p(0, ((m - P1M) / P1D) & 7); SCHED(); }
      };
      if (!S.lf) ld();
      if ((m == C.b1 && j > 0) || m == C.b2) {
        if (j == 6 && m == C.b2 && WO2 < 15) VMW(m > P1M ? (m - P1M + P1D - 1) / P1D : 0);
        asm volatile("" ::: "memory"); __builtin_amdgcn_s_waitcnt(0xC07F); RBAR(); SCHED();
      }
      sfor<0, 16>([&](auto I_) __attribute__((always_inline)) {
        constexpr int i = decltype(I_)::value;
        if (j > 0 && C.wp[i] == m) { if (C.wide) red_put(j + 1, i & 3); else red_put1(j + 1, i); SCHED(); }
        if (j > 0 && i < 8 && C.rp[i & 7] == m) { red_get(i & 7); SCHED(); }
      });
      if (j == 5 && m >= 27 && (m & 1)) { red_put(j, ((m - 27) >> 1) & 3); SCHED(); }
      if (j == 6 && m >= 23 && (m & 3) == 3) { red_put(j, ((m - 23) >> 2) & 3); SCHED(); }
      if (j == 6 && m == XPM) {
        asm volatile("v_lshrrev_b32 %0, 1, %1" : "=v"(l8) : "v"(l16));
        xo = (l8 & 0x180u) + (unsigned)psi((l8 >> 3) & 15) * 8u;
        asm volatile("" : "+v"(xo));
      }
      if (j == 6 && m > XPM && m < XPM + 4) { xp[(m - XPM - 1) % 3] = __builtin_amdgcn_raw_buffer_load_b64(rs, xo + (unsigned)(m - XPM - 1) * 512u, xs, BULK_AUX); SCHED(); }
      if (j == 6 && m == 4) {
#pragma unroll
        for (int i = 0; i < 3; ++i) { wb[i] = (unsigned)(kh * 1024 + i * 65536) + l16; asm volatile("" : "+v"(wb[i])); }
      }
      if (j == 6 && m != 23 && m != 27 && m != 31) {
        constexpr int n = NA0 - 1 - (30 - m) + (m < 27) + (m < 23);
        if (n >= 0) { LDA(n >= 0 ? n : 0); SCHED(); }
      }
      if (j == 6 && m >= PC0 && m < PC0 + 7) { p_chk(0, 0, 2 * (m - PC0)); SCHED(); p_chk(0, 0, 2 * (m - PC0) + 1); SCHED(); }
      if (j == 6 && m == PC0 - 1) { red_fin(6, 16); red_fin(6, 17); SCHED(); }
      sfor<0, 22>([&](auto K_) __attribute__((always_inline)) {
        constexpr int k = decltype(K_)::value;
        if (j > 1 && B.vp[k] == m + 32) { red_fin(j > 1 ? j - 2 : 0, k); SCHED(); }
        if (j > 0 && C.vp[k] == m) { red_fin(j > 0 ? j - 1 : 0, k); SCHED(); }
      });
      if (S.lf) ld();
    });
    STAMP(15);
#define NM0 2
#define GM 19
#define MB 30
    f32x4 acc[5];
    f32x4 xf[3], gf[3];
    unsigned go, oo, eo, ho, tw, tr, o1, ta, tb;
    unsigned s1 = (unsigned)((w ^ 4) * 3072 + 1024), s2 = (unsigned)(L_MISC + 128 * kh), s3 = (unsigned)(w * 3072 + 1024), s4 = s1 - (unsigned)kh * 1024u;
    unsigned os = (unsigned)((R * 64 + tau(rt, 0)) * D_ + 80 * c + 32 * kh) * 2u;
    unsigned long long om = kh ? ~0ull : 0xccccccccccccccccull;
    asm volatile("" : "+s"(s1), "+s"(s2), "+s"(s3), "+s"(s4), "+s"(os), "+s"(om));
#define PIN2(a, b) asm volatile("" : "+v"(a), "+v"(b))
    auto glue = [&](int k) {
      if (k == 0) { ta = l16 >> 6; tb = l16 & 48u; PIN2(ta, tb); }
      if (k == 1) { tr = ta * 112u + tb; oo = ta & 12u; PIN2(tr, oo); }
      if (k == 2) { tr += s1; oo += ta; PIN2(tr, oo); }
      if (k == 3) { oo = oo * 1280u + tb; ta = l16 >> 5; PIN2(oo, ta); }
      if (k == 4) { ta &= 24u; tb = l16 & 0xf0u; PIN2(ta, tb); }
      if (k == 5) { tw = tb * 7u + ta; go = (ta << 1) + s2; PIN2(tw, go); }
      if (k == 6) { tw += s1; asm volatile("v_cndmask_b32 %0, %1, -2.0, %2" : "=v"(o1) : "v"(oo), "s"(om)); asm volatile("" : "+v"(tw)); }
      if (k == 7) { eo = l16 + s3; ho = l16 + s4; PIN2(eo, ho); }
      if (k >= 8 && k < 14) {
        const unsigned x = xp[((k - 8) >> 1) % 3][k & 1];
        float lo = bflo(x), hi = bfhi(x);
        PIN2(lo, hi);
        xf[((k - 8) >> 1) % 3][2 * (k & 1)] = lo; xf[((k - 8) >> 1) % 3][2 * (k & 1) + 1] = hi;
      }
    };
    auto g3 = [&](auto M0_, auto M1_) __attribute__((always_inline)) {
      sfor<decltype(M0_)::value, decltype(M1_)::value>([&](auto M_) __attribute__((always_inline)) {
        constexpr int mi = decltype(M_)::value;
        constexpr int m = mi < 55 ? mi : mi < 64 ? 55 + 5 * ((mi - 55) / 3) + (mi - 55) % 3 : 58 + 5 * ((mi - 64) / 2) + (mi & 1), u2 = m / 5;
        const u32x4 fb = u2 < 8 ? P[0][u2 & 7] : P[1][u2 < 11 ? (u2 - 8) & 3 : (u2 - 7) & 7];
        if (m < 5) asm volatile("v_mfma_f32_16x16x32_bf16 %0, %1, %2, 0" : "=&v"(acc[m % 5]) : "v"(af[m % NAF]), "v"(fb));
        else asm volatile("v_mfma_f32_16x16x32_bf16 %0, %1, %2, %0" : "+v"(acc[m % 5]) : "v"(af[m % NAF]), "v"(fb));
        SCHED();
        if (mi == NM0) RBAR();
        if (mi >= NM0 && mi < NM0 + 3) dma1k(rs, lds + wl[(mi - NM0) % 3], l16, wg + wl[(mi - NM0) % 3]);
        if (mi >= 5 && mi < 19) p_chk(0, 1, mi - 5);
        if (mi >= GM && mi < GM + 14) glue(mi - GM);
        if (mi >= 47 && mi < 54) { p_chk(1, 1, 2 * (mi - 47)); SCHED(); p_chk(1, 1, 2 * (mi - 47) + 1); }
        if (mi >= NM0 && mi < 40 && NA0 + mi - NM0 < 60) LDA(NA0 + mi - NM0);
        if (mi >= 66 && mi < 69) *(f32x4*)(lds + eo + (mi - 66) * 1024) = acc[(mi - 66) % 3];
        SCHED();
      });
    };
    __builtin_amdgcn_s_setprio(3);
    asm volatile("s_nop 7");
    red_put(6, 3);
    SCHED();
    g3(IC<0>(), IC<NM0 / 2>());
    BAR();
#pragma unroll
    for (int i = 0; i < 8; ++i) red_get(i);
    SCHED();
    g3(IC<NM0 / 2>(), IC<NM0>());
#pragma unroll
    for (int k = 0; k < 20; ++k) red_fin(6, k < 16 ? k : k + 2);
    SCHED();
    __builtin_amdgcn_s_setprio(0);
    STAMPT(16);
    g3(IC<NM0>(), IC<MB>());
    STAMPT(22);
    DRAIN();
    RBAR();
    STAMPT(27);
#pragma unroll
    for (int i = 0; i < 8; ++i) { ld_p(1, i); SCHED(); }
    STAMPT(26);
    g3(IC<MB>(), IC<40>());
    STAMPT(24);
#pragma unroll
    for (int m = NA0 + 40 - NM0; m < 70; ++m) LDA(m);
#pragma unroll
    for (int x = 0; x < 3; ++x) gf[x] = *(const f32x4*)(lds + go + x * 64);
    SCHED();
    const rsrc_t ro = __builtin_amdgcn_make_buffer_rsrc((void*)p.out, 0, S_ * D_ * 2, 0x00020000);
    STAMPT(17);
#pragma unroll
    for (int k = 0; k < 14; ++k) p_chk(1, 0, k);
    asm volatile("s_nop 1");
    STAMPT(25);
    g3(IC<40>(), IC<70>());
    STAMPT(18);
    BAR();
    STAMPT(28);
    f32x4 o4[3];
    u32x4 ov[2];
#pragma unroll
    for (int j = 2; j >= 0; --j) o4[j] = *(const volatile LAS f32x4*)(lds + ho + j * 1024);
    asm volatile("" : "+v"(acc[2]), "+v"(acc[3]), "+v"(acc[4]));
    SCHED();
#pragma unroll
    for (int j = 2; j >= 0; --j) {
      const f32x4 v = __builtin_elementwise_fma(gf[j], o4[j] + acc[2 + j], xf[j]);
      *(volatile LAS u32x2*)(lds + tw + j * 32) = (u32x2){pack2(v[0], v[1]), pack2(v[2], v[3])};
      if (j < 2) ov[j] = *(const volatile LAS u32x4*)(lds + tr + j * 32);
    }
    __builtin_amdgcn_raw_buffer_store_b128(ov[1], ro, oo + 32u, os, 0);
    __builtin_amdgcn_raw_buffer_store_b128(ov[0], ro, o1, os, 0);
    if (bid == 0 && w == 5 && l8 == 0u) st4(rs, WS_EPOCH, tag);
  }
  STAMP(19);
}

extern "C" __device__ __attribute__((used, noinline, visibility("hidden"))) void mega_tail() {}

extern "C" void kernel_launch(void* const* d_in, const int* in_sizes, int n_in, void* d_out, int out_size,
                              void* d_ws, size_t ws_size, hipStream_t stream) {
  Params p;
  p.q = (const u16*)d_in[0];
  p.k = (const u16*)d_in[1];
  p.v = (const u16*)d_in[2];
  p.shift_k = (const u16*)d_in[3];
  p.ssm_k = (const u16*)d_in[4];
  p.ssm_D = (const u16*)d_in[5];
  p.w_in = (const u16*)d_in[7];
  p.w_sp = (const u16*)d_in[8];
  p.b_sp = (const u16*)d_in[9];
  p.w_out = (const u16*)d_in[10];
  p.cvec = (const u16*)d_in[11];
  p.w_mod = (const u16*)d_in[12];
  p.out = (u16*)d_out;
  p.ws = (char*)d_ws;
  mega<<<256, 512, 0, stream>>>(p);
}
```

```cpp
#include <hip/hip_runtime.h>
#include <stdint.h>

typedef unsigned short u16;
typedef __attribute__((ext_vector_type(8))) short bf16x8;
typedef __attribute__((ext_vector_type(4))) short bf16x4;
typedef __attribute__((ext_vector_type(4))) float f32x4;
typedef __attribute__((ext_vector_type(2))) float f32x2;
typedef __attribute__((ext_vector_type(4))) unsigned int u32x4;
typedef __attribute__((ext_vector_type(2))) unsigned int u32x2;
#define LAS __attribute__((address_space(3)))
typedef __amdgpu_buffer_rsrc_t rsrc_t;
typedef __attribute__((ext_vector_type(4))) short s16x4;

constexpr int S_ = 2048, D_ = 640, F_ = 1728, FH = 864, CB = 108;

constexpr unsigned WS_EPOCH = 0;
constexpr unsigned WS_FWI = 4096;
constexpr unsigned WS_FZ = WS_FWI + 384 * 128;
constexpr unsigned WS_FWO = WS_FZ + 256 * 128;
constexpr unsigned F4S = 896 * 128;
constexpr unsigned WS_CS = WS_FZ + 256 * 128;
constexpr unsigned WS_MOD = 131072;
constexpr unsigned WS_MODP = WS_MOD + 81920;
constexpr unsigned WS_ST1 = 393216;
constexpr unsigned WS_PART = 524288;
constexpr unsigned WS_CSP = WS_PART + 262144;
constexpr unsigned WS_FX = WS_CSP + 655360;
constexpr unsigned WS_X1B = WS_FX + 16 * 4 * 160 * 128;
constexpr unsigned WS_QB = WS_X1B + 2621440;
constexpr unsigned WS_KV3 = WS_QB + 2621440;
constexpr unsigned WS_MODQ = WS_KV3 + 1310720;
constexpr unsigned WBLK = 143360;
constexpr unsigned WS_WINF = WS_MODQ + 245760;
constexpr unsigned WS_WOUTF = WS_WINF + 16 * WBLK;
constexpr unsigned WS_Z2F = WS_WOUTF + 8 * WBLK;
constexpr unsigned WS_UF = WS_Z2F + 8 * 64 * 7168;
constexpr unsigned WS_F4 = WS_UF + 32 * 4 * 8 * 4096;
constexpr unsigned WS_END = WS_F4 + 4 * F4S;
constexpr unsigned WS_STAMP = 200u << 20;

constexpr int G_COPY = 4160;
constexpr int L_U = WBLK;
constexpr int U_CH = (256 + S_) * 2 + 32;
constexpr int L_FLG = L_U + 4 * U_CH;
constexpr int L_RT = 16384;
constexpr int L_CONST = WBLK;
constexpr int L_PST = L_CONST + 2048;
constexpr int L_Z = L_CONST + 5120;
constexpr int L_ST2 = L_Z + 14336;
constexpr int L_MISC = L_ST2 + 512;
constexpr int LDS_TOTAL = 163840;

#define AUX_T 610
#define RT2_T 360
#define CB_T 380
#define W_T 730
#define CS_T 830
#define Y_T 930
#define K_T 460
#ifndef M_T
#define M_T 520
#endif
#define WA_T 580
constexpr int SC1 = 16, NT = 2;
constexpr unsigned SPIN_CAP = 1u << 16;

__device__ __forceinline__ float bflo(unsigned x) { return __uint_as_float(x << 16); }
__device__ __forceinline__ float bfhi(unsigned x) { return __uint_as_float(x & 0xffff0000u); }
__device__ __forceinline__ unsigned f2bf(float x) {
  __bf16 b = (__bf16)x;
  return (unsigned)__builtin_bit_cast(unsigned short, b);
}
__device__ __forceinline__ float rbf(float x) { return bflo(f2bf(x)); }
typedef __attribute__((ext_vector_type(2))) __bf16 bf2_t;
__device__ __forceinline__ unsigned pack2(float lo, float hi) {
  f32x2 v = {lo, hi};
  return __builtin_bit_cast(unsigned, __builtin_convertvector(v, bf2_t));
}
__device__ __forceinline__ f32x2 rbf2(f32x2 v) {
  const unsigned p = __builtin_bit_cast(unsigned, __builtin_convertvector(v, bf2_t));
  return (f32x2){__uint_as_float(p << 16), __uint_as_float(p & 0xffff0000u)};
}
__device__ __forceinline__ f32x4 rbf4(f32x4 v) {
  const f32x2 a = rbf2((f32x2){v[0], v[1]}), b = rbf2((f32x2){v[2], v[3]});
  return (f32x4){a[0], a[1], b[0], b[1]};
}
template <int CTRL>
__device__ __forceinline__ float dpp_f(float v) {
  return __int_as_float(__builtin_amdgcn_update_dpp(0, __float_as_int(v), CTRL, 0xf, 0xf, true));
}
__device__ __forceinline__ float sum8(float v) {
  v += dpp_f<0xB1>(v);
  v += dpp_f<0x4E>(v);
  v += dpp_f<0x141>(v);
  return v;
}
__device__ __forceinline__ float sum16(float v) {
  v = sum8(v);
  v += dpp_f<0x140>(v);
  return v;
}
__device__ __forceinline__ float sum_rows(float v) {
  auto a = __builtin_amdgcn_permlane16_swap(__float_as_uint(v), __float_as_uint(v), false, false);
  v = __uint_as_float(a[0]) + __uint_as_float(a[1]);
  auto b = __builtin_amdgcn_permlane32_swap(__float_as_uint(v), __float_as_uint(v), false, false);
  return __uint_as_float(b[0]) + __uint_as_float(b[1]);
}
#define MFMA16(a, b, c) __builtin_amdgcn_mfma_f32_16x16x32_bf16(__builtin_bit_cast(bf16x8, a), __builtin_bit_cast(bf16x8, b), c, 0, 0, 0)
#define MFMA16K16(a, b, c) __builtin_amdgcn_mfma_f32_16x16x16bf16_1k(__builtin_bit_cast(bf16x4, a), __builtin_bit_cast(bf16x4, b), c, 0, 0, 0)
__device__ __forceinline__ u32x2 trd(const char* p) {
  return __builtin_bit_cast(u32x2, __builtin_amdgcn_ds_read_tr16_b64_v4i16((LAS s16x4*)p));
}
__device__ __forceinline__ u32x4 trfrag(const char* p, int pitch) {
  u32x2 a = trd(p), b = trd(p + 16 * pitch);
  return (u32x4){a[0], a[1], b[0], b[1]};
}
#define BAR() asm volatile("s_waitcnt lgkmcnt(0)\n\ts_barrier" ::: "memory")
#define DRAIN() do { __builtin_amdgcn_s_waitcnt(0x0F70); asm volatile("" ::: "memory"); } while (0)
#define SCHED() __builtin_amdgcn_sched_barrier(0)
template <int N> struct IC { static constexpr int value = N; };
template <int I, int N, class F>
__device__ __forceinline__ void sfor(F&& f) {
  if constexpr (I < N) { f(IC<I>()); sfor<I + 1, N>(f); }
}

__device__ __forceinline__ u32x4 ld16(rsrc_t r, unsigned off) { return __builtin_amdgcn_raw_buffer_load_b128(r, off, 0, SC1); }
__device__ __forceinline__ u32x2 ld8(rsrc_t r, unsigned off) { return __builtin_amdgcn_raw_buffer_load_b64(r, off, 0, SC1); }
__device__ __forceinline__ unsigned ld4(rsrc_t r, unsigned off) { return __builtin_amdgcn_raw_buffer_load_b32(r, off, 0, SC1); }
#define ST_AUX 16
__device__ __forceinline__ void st16(rsrc_t r, unsigned off, u32x4 v) { __builtin_amdgcn_raw_buffer_store_b128(v, r, off, 0, ST_AUX); }
__device__ __forceinline__ void st8(rsrc_t r, unsigned off, u32x2 v) { __builtin_amdgcn_raw_buffer_store_b64(v, r, off, 0, SC1); }
__device__ __forceinline__ void st_flag4(rsrc_t r, unsigned off, unsigned tag, int l) {
  if (l < 32) st16(r, WS_F4 - WS_FWI + off + (unsigned)(l >> 3) * F4S + (unsigned)(l & 7) * 16u, (u32x4){tag, tag, tag, tag});
}
__device__ __forceinline__ void st4(rsrc_t r, unsigned off, unsigned v) { __builtin_amdgcn_raw_buffer_store_b32(v, r, off, 0, SC1); }
#define BULK_AUX 16
__device__ __forceinline__ u32x4 ld16p(rsrc_t r, unsigned off) { return __builtin_amdgcn_raw_buffer_load_b128(r, off, 0, BULK_AUX); }
__device__ __forceinline__ u32x2 ld8p(rsrc_t r, unsigned off) { return __builtin_amdgcn_raw_buffer_load_b64(r, off, 0, BULK_AUX); }
__device__ __forceinline__ void dma1k(rsrc_t r, char* lds_dst, unsigned l16, unsigned soff) {
  __builtin_amdgcn_raw_ptr_buffer_load_lds(r, (LAS void*)lds_dst, 16, l16, soff, 0, BULK_AUX);
}

#define STAMP(i)
#define STAMPF(i)
#define STAMPT(i) STAMP(i)

template <int N, int SLP>
__device__ __forceinline__ void wait_lines(rsrc_t r, const unsigned (&off)[N], unsigned tag) {
  bool need[N];
#pragma unroll
  for (int i = 0; i < N; ++i) need[i] = off[i] != ~0u;
  _Pragma("unroll 1") for (unsigned spins = 0; spins < SPIN_CAP; ++spins) {
    unsigned v[N];
#pragma unroll
    for (int i = 0; i < N; ++i) {
      v[i] = tag;
      if (need[i]) v[i] = ld4(r, off[i]);
    }
    bool any = false;
#pragma unroll
    for (int i = 0; i < N; ++i) { need[i] = v[i] != tag; any = any || need[i]; }
    if (!__any(any)) break;
    __builtin_amdgcn_s_sleep(SLP);
  }
  asm volatile("" ::: "memory");
}

__device__ __forceinline__ int tau(int rt, int n) { return 32 * (rt >> 1) + 8 * (n >> 2) + 4 * (rt & 1) + (n & 3); }
__device__ __forceinline__ int psi(int n) { return 2 * (n & 7) + (n >> 3); }
#define QX_CM(d, s0, s1, P) "v_cndmask_b32_dpp " d ", " s0 ", " s1 ", vcc quad_perm:" P " row_mask:0xf bank_mask:0xf\n\t"
#define QX_BODY(M, P) \
  asm("s_mov_b32 vcc_lo, " M "\n\ts_mov_b32 vcc_hi, " M "\n\t" \
      QX_CM("%0", "%12", "%8", P) "v_xor_b32 %4, %8, %0\n\t" QX_CM("%1", "%13", "%9", P) "v_xor_b32 %5, %9, %1\n\t" \
      QX_CM("%2", "%14", "%10", P) "v_xor_b32 %6, %10, %2\n\t" QX_CM("%3", "%15", "%11", P) "v_xor_b32 %7, %11, %3" \
      : "=&v"(t[0]), "=&v"(t[1]), "=&v"(t[2]), "=&v"(t[3]), "=&v"(u[0]), "=&v"(u[1]), "=&v"(u[2]), "=&v"(u[3]) \
      : "v"(a[0]), "v"(a[1]), "v"(a[2]), "v"(a[3]), "v"(b[0]), "v"(b[1]), "v"(b[2]), "v"(b[3]) : "vcc")
template <int BIT>
__device__ __forceinline__ void quad_xchg3(u32x4& a, u32x4& b) {
  unsigned t[4], u[4];
  if (BIT == 0) QX_BODY("0x55555555", "[1,0,3,2]");
  else QX_BODY("0x33333333", "[2,3,0,1]");
  a = (u32x4){t[0], t[1], t[2], t[3]};
#pragma unroll
  for (int d = 0; d < 4; ++d) b[d] ^= (unsigned)__builtin_amdgcn_update_dpp(0, (int)u[d], BIT == 0 ? 0xB1 : 0x4E, 0xf, 0xf, true);
}
__device__ __forceinline__ void quad_transpose(u32x4& x0, u32x4& x1, u32x4& x2, u32x4& x3) {
  quad_xchg3<0>(x0, x1);
  quad_xchg3<0>(x2, x3);
  quad_xchg3<1>(x0, x2);
  quad_xchg3<1>(x1, x3);
  asm volatile("" : "+v"(x0), "+v"(x1), "+v"(x2), "+v"(x3));
}

struct Params {
  const u16 *q, *k, *v, *shift_k, *ssm_k, *ssm_D, *w_in, *w_sp, *b_sp, *w_out, *cvec, *w_mod;
  u16* out;
  char* ws;
};

__global__ __launch_bounds__(512, 2) void mega(Params p) {
  __shared__ __attribute__((aligned(16))) char lds[LDS_TOTAL];
  {
    unsigned long long pc0;
    unsigned vt;
    asm volatile("v_readfirstlane_b32 vcc_lo, %3\n\ts_cmpk_lt_u32 %2, 192\n\ts_cbranch_scc1 4f\n\ts_cmpk_lt_u32 vcc_lo, 64\n\ts_cbranch_scc0 4f\n\t"
                 "s_lshr_b32 vcc_hi, %2, 3\n\ts_sub_u32 vcc_hi, vcc_hi, 24\n\ts_lshl_b32 vcc_hi, vcc_hi, 13\n\t"
                 "v_lshlrev_b32 %1, 7, %3\n\tv_add_u32 %1, vcc_hi, %1\n\ts_getpc_b64 %0\n.Lmega_here:\n\t"
                 "v_min_u32 %1, mega_tail-.Lmega_here-4, %1\n\ts_movk_i32 m0, 0x2000\n\ts_nop 1\n\tglobal_load_lds_dword %1, %0\n4:\n\t"
                 "s_cmpk_lt_u32 %2, 160\n\ts_cbranch_scc0 1f\n\ts_cmpk_lt_u32 vcc_lo, 256\n\ts_cbranch_scc1 3f\n\t"
                 "s_call_b64 vcc, .Ltouch1\n\ts_branch 3f\n1:\n\t"
                 "s_cmpk_lt_u32 vcc_lo, 256\n\ts_cbranch_scc1 2f\n\ts_cmpk_lt_u32 %2, 224\n\ts_cbranch_scc0 2f\n\ts_call_b64 vcc, .Ltouch3\n\ts_branch 3f\n"
                 "2:\n\ts_call_b64 vcc, .Ltouch2\n3:"
                 : "=&s"(pc0), "=&v"(vt) : "s"(blockIdx.x), "v"(threadIdx.x) : "vcc", "scc", "memory");
  }
  SCHED();
  const int tid = threadIdx.x, l = tid & 63;
  const int w = __builtin_amdgcn_readfirstlane(tid >> 6);
  const int bid = blockIdx.x, xcd = bid & 7, idx = bid >> 3;
#define TOUCH_AT(n) asm volatile("s_branch 1f\n.Ltouch" #n ":\n\ts_setpc_b64 vcc\n1:")
  char* const ws = p.ws;
  asm volatile("" ::"s"(p.q), "s"(p.k), "s"(p.v), "s"(p.shift_k), "s"(p.ssm_k), "s"(p.ssm_D), "s"(p.w_in), "s"(p.w_sp), "s"(p.b_sp),
               "s"(p.w_out), "s"(p.cvec), "s"(p.w_mod), "s"(p.out), "s"(p.ws));
  unsigned epoch, tag, t_start;
  unsigned long long t64;
  asm volatile("s_load_dword %0, %2, 0x0\n\ts_memrealtime %1" : "=&s"(epoch), "=&s"(t64) : "s"(ws));
#define TAG_READY() do { SCHED(); \
    asm volatile("s_waitcnt lgkmcnt(0)\n\ts_mul_i32 %0, %2, 0x9e3779b1\n\ts_add_i32 %0, %0, 0x9e3779b9\n\ts_mov_b64 %1, %3" \
                 : "=&s"(tag), "=&s"(t64r) : "s"(epoch), "s"(t64) : "scc"); t_start = (unsigned)t64r; SCHED(); } while (0)
  unsigned long long t64r;
  const rsrc_t rs = __builtin_amdgcn_make_buffer_rsrc(ws, 0, WS_END, 0x00020000);
  const unsigned l16 = (unsigned)l * 16u;
  STAMP(0);
  const bool is_conv = idx < 20;
  const int quad = xcd * 20 + idx;
  const int R = is_conv ? (xcd & 3) * 5 + (idx >> 2) : (xcd & 3) * 3 + 15 + (idx >> 2);
  const int c = (xcd >> 2) * 4 + (idx & 3);
  const unsigned fwi = WS_F4 + (unsigned)(xcd & 3) * F4S, fz = fwi + (WS_FZ - WS_FWI);
  const int n16 = l & 15, h4 = l >> 4;
  u32x4 wsp[4][8];
#define ROTC 2
#define WAUX 0
  const int rotg = ((idx >> 4) ^ ((ROTC & 2) ? idx >> 1 : 0)) & 1, rotb = (idx >> 2) & 3, rotr = (ROTC & 1) ? (idx & 1) * 2 : 0;
  const rsrc_t rsp = __builtin_amdgcn_make_buffer_rsrc((void*)p.w_sp, 0, S_ * S_ * 2, 0x00020000);
  const unsigned wsp_vo = (unsigned)(((l >> 2) & 3) * 8 * 4096 + h4 * 64 + ((l + rotb) & 3) * 16);
#define LDW(k) LDW_(k)
#define WSP_TR(g) WSP_TR_(g)
#define WROW R
#define LDW_(k) wsp[((k) >> 2) & 3][((k) >> 4) * 4 + ((k) & 3)] = __builtin_amdgcn_raw_buffer_load_b128(rsp, wsp_vo, \
    (unsigned)((WROW * 64 + 16 * ((((k) >> 2) & 2) ^ rotr) + 4 * (((k) >> 2) & 1) + ((k) & 3)) * 4096 + w * 512 + ((((k) >> 4) ^ rotg) * 256)), WAUX)
#define WSP_TR_(g) quad_transpose(wsp[(g) & 3][((g) >> 2) * 4], wsp[(g) & 3][((g) >> 2) * 4 + 1], wsp[(g) & 3][((g) >> 2) * 4 + 2], wsp[(g) & 3][((g) >> 2) * 4 + 3])
  const rsrc_t rwi = __builtin_amdgcn_make_buffer_rsrc((void*)p.w_in, 0, D_ * F_ * 2, 0x00020000);
  const rsrc_t rwo = __builtin_amdgcn_make_buffer_rsrc((void*)p.w_out, 0, FH * D_ * 2, 0x00020000);
  const rsrc_t rq = __builtin_amdgcn_make_buffer_rsrc((void*)p.q, 0, S_ * D_ * 2, 0x00020000);
  auto hold = [&](unsigned tt) { while ((unsigned)__builtin_amdgcn_s_memrealtime() - t_start < tt) __builtin_amdgcn_s_sleep(2); };
  volatile LAS unsigned* lf = (volatile LAS unsigned*)(lds + L_FLG);
  auto lf_set = [&](int i) { asm volatile("s_waitcnt lgkmcnt(0)" ::: "memory"); if (l == 0) lf[i] = tag; };
  auto lf_wait = [&](int i0, int n) {
    _Pragma("unroll 1") for (unsigned spins = 0; spins < (SPIN_CAP << 4); ++spins) {
      const unsigned v = lf[i0 + (l & (n - 1))];
      if (__all(v == tag)) break;
      __builtin_amdgcn_s_sleep(1);
    }
    asm volatile("" ::: "memory");
  };
  auto xflag = [&](int s) {
    st16(rs, WS_FX + (unsigned)(((4 * s + (w & 3)) * 4 + ((l >> 3) & 3)) * 160 + quad) * 128u + (unsigned)(l & 7) * 16u, (u32x4){tag, tag, tag, tag});
    SCHED();
  };
  auto load_b2 = [&]() {
#pragma unroll
    for (int k = 0; k < 35; ++k) {
      int i = k + (idx >> 2) * 4;
      i = (i >= 35 ? i - 35 : i) + 35 * w;
      dma1k(rs, lds + i * 1024, l16, WS_WINF + (unsigned)(8 + c) * WBLK + (unsigned)i * 1024u);
    }
  };
  char* const wo_st = lds + 122880;
  auto wo_ld = [&]() {
    const int U = R < 28 ? R : 0, u2 = U >> 1, fi = (u2 + 1) % 3;
    const int c0 = 4 * (U & 1) + (u2 < 8 ? u2 >> 1 : u2 < 11 ? 0 : 2);
    const int b0 = u2 < 8 ? CB * c0 + 2 * (u2 & 1) + 96 : CB * (c0 + (fi >> 1)) + 48 + 2 * (fi & 1), d1 = u2 < 8 ? -96 : 16, d2 = d1 + 16;
    const int b3 = u2 < 8 ? b0 - 64 : CB * (c0 + 1) + 50 + 16 * fi;
#pragma unroll
    for (int r = 0; r < 5; ++r) {
      const int g = r * 64 + l, rho = g / 10;
      const int fb = (rho & 16) ? ((rho & 2) ? b3 : b0 + d2) : ((rho & 2) ? b0 + d1 : b0);
      __builtin_amdgcn_raw_ptr_buffer_load_lds(rwo, (LAS void*)(wo_st + r * 1024), 16, (unsigned)((fb + (rho & 13)) * (D_ * 2) + (g % 10) * 16 + c * 160), 0, 0, 0);
    }
  };
  auto rt_ld = [&](int i0, char* st) {
    const int cb = (i0 < 160 ? 8 : 0) + (i0 & 7), Q = (i0 < 160 ? i0 : i0 - 160) >> 3;
#pragma unroll
    for (int r = 0; r < 7; ++r) {
      const int g = r * 64 + l;
      __builtin_amdgcn_raw_ptr_buffer_load_lds(rwi, (LAS void*)(st + r * 1024), 16, (unsigned)((g / 14) * (F_ * 2) + (g % 14) * 16 + Q * 32 * F_ * 2 + cb * CB * 2),
                                               0, 0, NT);
    }
  };
  auto rt_job = [&](int i0, char* st) {
    const int cb = (i0 < 160 ? 8 : 0) + (i0 & 7), Q = (i0 < 160 ? i0 : i0 - 160) >> 3;
    const int col = 640 * (l >> 5) + 32 * Q + (l & 31);
    const unsigned po = WS_MODP + (unsigned)col * 8u;
    u32x2 pp8[8];
    hold(CB_T);
    _Pragma("unroll 1") for (unsigned spins = 0; spins < SPIN_CAP; ++spins) {
      unsigned dd = 0u;
#pragma unroll
      for (int i = 0; i < 8; ++i) pp8[i] = ld8(rs, po + (unsigned)i * 10240u);
#pragma unroll
      for (int i = 0; i < 8; ++i) dd |= pp8[i][0] ^ tag;
      if (__all(dd == 0u)) break;
      __builtin_amdgcn_s_sleep(4);
    }
    STAMPF(9);
    {
      float t = 0.f;
#pragma unroll
      for (int i = 0; i < 8; ++i) t += __uint_as_float(pp8[i][1]);
      if (l >= 32) *(float*)(st + 7168 + (l - 32) * 4) = rbf(1.0f + rbf(t));
      else *(u16*)(st + 7296 + l * 2) = (u16)f2bf(t);
    }
    DRAIN();
    asm volatile("s_waitcnt lgkmcnt(0)" ::: "memory");
    const f32x4 ca = *(const f32x4*)(st + 7168 + h4 * 16), cc = *(const f32x4*)(st + 7168 + 64 + h4 * 16);
    const char* bp = st + (4 * h4 + (n16 >> 2)) * 224 + (n16 & 3) * 8;
    u32x4 x[7], o[7];
#pragma unroll
    for (int j = 0; j < 7; ++j) {
      x[j] = trfrag(bp + j * 32, 224);
      o[j] = (u32x4){pack2(bflo(x[j][0]) * ca[0], bfhi(x[j][0]) * ca[1]), pack2(bflo(x[j][1]) * ca[2], bfhi(x[j][1]) * ca[3]),
                     pack2(bflo(x[j][2]) * cc[0], bfhi(x[j][2]) * cc[1]), pack2(bflo(x[j][3]) * cc[2], bfhi(x[j][3]) * cc[3])};
      if (j == 6 && n16 == 12) o[j] = (u32x4){0x3F803F80u, 0x3F803F80u, 0x3F803F80u, 0x3F803F80u};
      st16(rs, WS_WINF + (unsigned)cb * WBLK + (unsigned)(Q * 7 + j) * 1024u + l16, o[j]);
    }
    STAMPF(11);
    const u32x2 e0 = *(const u32x2*)(st + 7296 + h4 * 8), e1 = *(const u32x2*)(st + 7328 + h4 * 8);
    f32x4 d[2] = {{0.f, 0.f, 0.f, 0.f}, {0.f, 0.f, 0.f, 0.f}};
#pragma unroll
    for (int j = 0; j < 7; ++j) {
      const unsigned one = n16 == 4 * (j & 3) ? 0x3F803F80u : 0u;
      const bool sr = n16 == 4 * (j & 3) + 1;
      d[j >> 2] = MFMA16(((u32x4){one, one, one, one}), o[j], d[j >> 2]);
      d[j >> 2] = MFMA16(((u32x4){sr ? e0[0] : 0u, sr ? e0[1] : 0u, sr ? e1[0] : 0u, sr ? e1[1] : 0u}), x[j], d[j >> 2]);
    }
#pragma unroll
    for (int m = 0; m < 2; ++m)
      st16(rs, WS_CSP + (unsigned)((cb * 20 + Q) * 2 + m) * 1024u + l16, (u32x4){tag, __float_as_uint(d[m][0]), tag, __float_as_uint(d[m][1])});
    STAMPF(13);
    asm volatile("s_waitcnt vmcnt(2)" ::: "memory");
    st_flag4(rs, WS_FWI + (unsigned)(cb * 20 + Q) * 128u, tag, l);
  };

  auto red8 = [&](float v) { v += dpp_f<0x128>(v); return sum_rows(v); };
  const rsrc_t rwm = __builtin_amdgcn_make_buffer_rsrc((void*)p.w_mod, 0, D_ * 3840 * 2, 0x00020000);
  auto gemv_load = [&](u32x4* pre, int blk, int sl, int np) {
    pre[10][0] = *(const unsigned*)(p.cvec + 8 * np * sl + 2 * (l < 4 * np ? l : 0));
#pragma unroll
    for (int pp = 0; pp < np; ++pp)
      pre[pp] = __builtin_amdgcn_raw_buffer_load_b128(rwm, (unsigned)((l >> 3) * 7680 + (l & 7) * 16), (unsigned)((8 * np * sl + pp * 8) * 7680 + blk * 128), NT);
  };
  auto gemv_fin = [&](float* acc, const u32x4* pre, int np, char* scr) {
    const unsigned cv2 = pre[10][0];
    unsigned* svt = (unsigned*)scr + w * 96;
#pragma unroll
    for (int e = 0; e < 2; ++e) {
      const int row = 2 * l + e;
      const float cv = e ? bfhi(cv2) : bflo(cv2);
      if (l < 4 * np) svt[(row & 7) * 12 + (row >> 3)] = __float_as_uint(rbf(cv * __builtin_amdgcn_rcpf(1.0f + __expf(-cv))));
    }
    asm volatile("s_waitcnt lgkmcnt(0)" ::: "memory");
    u32x4 sv[3];
#pragma unroll
    for (int i = 0; i < (np + 3) / 4; ++i) sv[i] = *(const u32x4*)(svt + (l >> 3) * 12 + 4 * i);
    SCHED();
#pragma unroll
    for (int e = 0; e < 8; ++e) acc[e] = 0.f;
#pragma unroll
    for (int pp = 0; pp < np; ++pp) {
      const unsigned sh = sv[pp >> 2][pp & 3], sl2 = sh >> 16;
#pragma unroll
      for (int e = 0; e < 4; ++e) {
        const unsigned x = pre[pp][e];
        acc[2 * e] = __builtin_amdgcn_fdot2_f32_bf16(__builtin_bit_cast(bf2_t, x), __builtin_bit_cast(bf2_t, sl2), acc[2 * e], false);
        acc[2 * e + 1] = __builtin_amdgcn_fdot2_f32_bf16(__builtin_bit_cast(bf2_t, x), __builtin_bit_cast(bf2_t, sh), acc[2 * e + 1], false);
      }
    }
#pragma unroll
    for (int e = 0; e < 8; ++e) acc[e] = red8(acc[e]);
  };
  auto gemv_put = [&](const float* acc, unsigned off) {
    float v = acc[0];
#pragma unroll
    for (int e = 1; e < 8; ++e) { v = (l >> 3) == e ? acc[e] : v; asm("" : "+v"(v)); }
    st8(rs, off + (unsigned)l * 8u, (u32x2){tag, (unsigned)__builtin_amdgcn_ds_bpermute(((l & 7) * 8 + (l >> 3)) * 4, (int)__float_as_uint(v))});
  };
  typedef const __attribute__((address_space(4))) unsigned* cptr_t;

  if (__builtin_expect(is_conv, 1)) {
    if (w >= 4) {
      TOUCH_AT(1);
      const int pw = w - 4;
      const rsrc_t ra = __builtin_amdgcn_make_buffer_rsrc((void*)((idx & 2) ? p.v : p.k), 0, S_ * D_ * 2, 0x00020000);
      const rsrc_t rb = __builtin_amdgcn_make_buffer_rsrc((void*)((idx & 2) ? p.k : p.v), 0, S_ * D_ * 2, 0x00020000);
      const rsrc_t rk = __builtin_amdgcn_make_buffer_rsrc((void*)p.k, 0, S_ * D_ * 2, 0x00020000);
      const unsigned vo = (unsigned)((128 * pw + 2 * l) * (D_ * 2));
      const unsigned so0 = (unsigned)((idx & 1) * (D_ * 2) + quad * 8), so1 = (unsigned)((1 - (idx & 1)) * (D_ * 2) + quad * 8);
      u32x2 g[3][4], kh;
      auto gl = [&](int s, int j0, int j1) {
        __builtin_amdgcn_s_setprio(0);
#pragma unroll
        for (int j = j0; j < j1; ++j) {
          g[s][j] = __builtin_amdgcn_raw_buffer_load_b64(j < 2 ? ra : rb, vo, ((j & 1) ? so1 : so0) + (unsigned)(s * 512 * D_ * 2), 0);
          SCHED();
        }
        __builtin_amdgcn_s_setprio(3);
      };
      const cptr_t sp = (cptr_t)(p.shift_k + quad * 16);
      unsigned spv[8];
#pragma unroll
      for (int i = 0; i < 8; ++i) spv[i] = sp[i];
      __builtin_amdgcn_s_sleep(3);
      gl(0, 0, 4);
      {
        const int row = 512 * (l >> 2) + 128 * pw - 3 + (l & 3);
        kh = __builtin_amdgcn_raw_buffer_load_b64(rk, (l >= 12 || (l & 3) == 3 || row < 0) ? 0x80000000u : (unsigned)(row * D_ * 2 + quad * 8), 0, 0);
      }
      TAG_READY();
      f32x2 wt[2][4];
#pragma unroll
      for (int d = 0; d < 2; ++d) {
        wt[d][0] = (f32x2){bflo(spv[4 * d]), bflo(spv[4 * d + 2])}; wt[d][1] = (f32x2){bfhi(spv[4 * d]), bfhi(spv[4 * d + 2])};
        wt[d][2] = (f32x2){bflo(spv[4 * d + 1]), bflo(spv[4 * d + 3])}; wt[d][3] = (f32x2){bfhi(spv[4 * d + 1]), bfhi(spv[4 * d + 3])};
        asm volatile("" : "+s"(wt[d][0]), "+s"(wt[d][1]), "+s"(wt[d][2]), "+s"(wt[d][3]));
      }
      SCHED();
      {
        const int j = idx * 4 + pw;
        const unsigned o = (unsigned)((512 + 64 * (j >> 2) + l) * (D_ * 2) + (((160 * xcd) >> 7) + ((j >> 1) & 1)) * 128);
        __builtin_amdgcn_raw_ptr_buffer_load_lds((j & 1) ? ra : rb, (LAS void*)(lds + 98304), 4, j < 64 ? o : 0x80000000u, 0, 0, 0);
      }
      SCHED();
      gl(1, 0, 4);
      STAMP(21);
      char* up = lds + L_U + 512 + 256 * pw + (l >> 2) * 16 + (3 - (l & 3)) * 4;
      auto kv = [&](auto S__, auto N__) {
        constexpr int s = decltype(S__)::value;
        u32x2* x = g[s];
        SCHED();
        asm volatile("s_waitcnt vmcnt(%5)" : "+v"(x[0]), "+v"(x[1]), "+v"(x[2]), "+v"(x[3]), "+v"(kh) : "n"(decltype(N__)::value));
        if (s == 2) STAMP(27);
        if (idx & 1) { asm volatile(""); const u32x2 t = x[0], u = x[2]; x[0] = x[1]; x[1] = t; x[2] = x[3]; x[3] = u; }
        asm volatile("" : "+v"(x[0]), "+v"(x[1]), "+v"(x[2]), "+v"(x[3]));
        if (idx & 2) { asm volatile(""); const u32x2 t = x[0], u = x[1]; x[0] = x[2]; x[1] = x[3]; x[2] = t; x[3] = u; }
        asm volatile("" : "+v"(x[0]), "+v"(x[1]), "+v"(x[2]), "+v"(x[3]));
        f32x2 kf[2][5], o[2][2];
#pragma unroll
        for (int d = 0; d < 2; ++d) {
          unsigned kk[5];
          kk[3] = x[0][d]; kk[4] = x[1][d];
          kk[2] = __builtin_amdgcn_update_dpp((unsigned)__builtin_amdgcn_readlane(kh[d], 4 * s + 2), kk[4], 0x138, 0xf, 0xf, false);
          kk[1] = __builtin_amdgcn_update_dpp((unsigned)__builtin_amdgcn_readlane(kh[d], 4 * s + 1), kk[3], 0x138, 0xf, 0xf, false);
          kk[0] = __builtin_amdgcn_update_dpp((unsigned)__builtin_amdgcn_readlane(kh[d], 4 * s), kk[2], 0x138, 0xf, 0xf, false);
#pragma unroll
          for (int j = 0; j < 5; ++j) kf[d][j] = (f32x2){bflo(kk[j]), bfhi(kk[j])};
        }
#pragma unroll
        for (int t = 0; t < 5; ++t) {
#pragma unroll
          for (int i = 0; i < 4; ++i) {
            const int d = i >> 1, r = i & 1;
            if (t == 4) o[d][r] *= (f32x2){bflo(x[2 + r][d]), bfhi(x[2 + r][d])};
            else if (t == 0) o[d][r] = kf[d][r + 3] * wt[d][0];
            else o[d][r] = __builtin_elementwise_fma(kf[d][r + 3 - t], wt[d][t], o[d][r]);
          }
          asm volatile("" : "+v"(o[0][0]), "+v"(o[0][1]), "+v"(o[1][0]), "+v"(o[1][1]));
        }
#pragma unroll
        for (int i = 0; i < 4; ++i) *(unsigned*)(up + i * U_CH + s * 1024) = pack2(o[i >> 1][1][i & 1], o[i >> 1][0][i & 1]);
        asm volatile("" ::: "memory");
        if (l == 0) lf[4 * s + pw] = tag;
        SCHED();
      };
      const char* yr = lds + L_U + (n16 >> 2) * U_CH + (128 * pw + 32 * h4 + 8 * (n16 & 1) + 4 * ((n16 >> 1) & 1)) * 2;
      const unsigned yo = (unsigned)(8 * pw + (l >> 3)) * 20480u + (unsigned)quad * 128u + (unsigned)(l & 7) * 16u;
      const unsigned so = WS_ST1 + (unsigned)xcd * 16384u + (unsigned)pw * 1024u + l16;
      const unsigned mo = WS_MODQ + (unsigned)((n16 < 12 ? n16 >> 2 : 0) * D_ + quad * 4 + (l & 3)) * 8u + (unsigned)h4 * 40960u;
      u32x4 sv[4], qv[4];
      u32x2 mv[2];
      auto ld_e = [&]() {
#pragma unroll
        for (int s = 0; s < 4; ++s) sv[s] = ld16(rs, so + (unsigned)s * 4096u);
        SCHED();
      };
      auto s_ok = [&]() {
        unsigned d = 0u;
#pragma unroll
        for (int s = 0; s < 4; ++s) d |= (sv[s][1] ^ tag) | (sv[s][3] ^ tag);
        return __all(d == 0u);
      };
      kv(IC<0>(), IC<5>());
      STAMP(23);
#define G2A 4
#define EPOS 0
      gl(2, 0, G2A);
      kv(IC<1>(), IC<G2A>());
      gl(2, G2A, 4);
      STAMPF(9);
      u32x4 k3[2];
      const unsigned k3o = WS_KV3 + (unsigned)(16 * pw + (l >> 2)) * 20480u + (unsigned)(((l >> 1) & 1) * 10240 + (2 * quad + (l & 1)) * 16);
      if (EPOS == 2) kv(IC<2>(), IC<0>());
      hold(K_T);
      k3[0] = ld16(rs, k3o); k3[1] = ld16(rs, k3o + 5120u);
      SCHED();
      if (EPOS != 1) ld_e();
      if (EPOS != 2) kv(IC<2>(), IC<EPOS ? 2 : 6>());
      if (EPOS == 1) ld_e();
      STAMPF(11);
      auto k_ok = [&]() { return __all(((k3[0][1] ^ tag) | (k3[0][3] ^ tag) | (k3[1][1] ^ tag) | (k3[1][3] ^ tag)) == 0u); };
      asm volatile("" : "+v"(k3[0]), "+v"(k3[1]));
      if (!k_ok()) {
        _Pragma("unroll 1") for (unsigned spins = 0; spins < SPIN_CAP; ++spins) {
          __builtin_amdgcn_s_sleep(4);
          k3[0] = ld16(rs, k3o); k3[1] = ld16(rs, k3o + 5120u);
          if (k_ok()) break;
        }
      }
      *(u32x4*)(lds + L_U + (2 * (l & 1) + ((l >> 1) & 1)) * U_CH + 512 + (1536 + 128 * pw + 8 * (l >> 2)) * 2) = (u32x4){k3[0][0], k3[0][2], k3[1][0], k3[1][2]};
      asm volatile("" ::: "memory");
      if (l == 0) lf[12 + pw] = tag;
      __builtin_amdgcn_s_setprio(3);
      STAMPF(18);
      asm volatile("" : "+v"(sv[0]), "+v"(sv[1]), "+v"(sv[2]), "+v"(sv[3]));
      if (!s_ok()) {
        _Pragma("unroll 1") for (unsigned spins = 0; spins < SPIN_CAP; ++spins) {
          __builtin_amdgcn_s_sleep(4);
          ld_e();
          if (s_ok()) break;
        }
      }
      asm volatile("" ::: "memory");
      SCHED();
      STAMPF(17);
#pragma unroll
      for (int s = 0; s < 4; ++s) qv[s] = ld16p(rs, WS_QB + yo + (unsigned)s * (32u * 20480u));
      hold(M_T);
      mv[0] = ld8(rs, mo); mv[1] = ld8(rs, mo + 20480u);
      SCHED();
      STAMP(2);
      u32x2 yv[4][2];
      auto ld_y = [&](int s) {
#pragma unroll
        for (int e = 0; e < 2; ++e) yv[s][e] = trd(yr + 1024 * s + 32 * e);
      };
      lf_wait(20, 4);
      ld_y(0); ld_y(1);
      SCHED();
      asm volatile("" : "+v"(mv[0]), "+v"(mv[1]), "+v"(qv[0]), "+v"(qv[1]), "+v"(qv[2]), "+v"(qv[3]));
      if (!__all(((mv[0][0] ^ tag) | (mv[1][0] ^ tag)) == 0u)) {
        _Pragma("unroll 1") for (unsigned spins = 0; spins < SPIN_CAP; ++spins) {
          __builtin_amdgcn_s_sleep(4);
          mv[0] = ld8(rs, mo); mv[1] = ld8(rs, mo + 20480u);
          if (__all(((mv[0][0] ^ tag) | (mv[1][0] ^ tag)) == 0u)) break;
        }
      }
      STAMPF(13);
      f32x4 al, be;
      {
        const float mgf = rbf(sum_rows(__uint_as_float(mv[0][1]) + __uint_as_float(mv[1][1])));
        auto rl = [&](int i) { return __uint_as_float(__builtin_amdgcn_readlane(__float_as_uint(mgf), i)); };
        const f32x4 sa = {rl(0), rl(1), rl(2), rl(3)}, ca = {rl(4), rl(5), rl(6), rl(7)}, ga = {rl(8), rl(9), rl(10), rl(11)};
        al = rbf4(ca + 1.0f) * ga;
        be = sa * ga;
      }
      f32x4 qf[2][2], P[2][2];
      auto xpre = [&](int s) {
#pragma unroll
        for (int e = 0; e < 2; ++e) {
          const float r1 = __builtin_amdgcn_rcpf(bfhi(sv[s][2 * e])), m1 = -bflo(sv[s][2 * e]) * r1;
          const f32x4 qq = {bflo(qv[s][2 * e]), bfhi(qv[s][2 * e]), bflo(qv[s][2 * e + 1]), bfhi(qv[s][2 * e + 1])};
          qf[s & 1][e] = qq;
          P[s & 1][e] = __builtin_elementwise_fma(__builtin_elementwise_fma(qq, (f32x4){r1, r1, r1, r1}, (f32x4){m1, m1, m1, m1}), al, be);
        }
        asm volatile("" : "+v"(qf[s & 1][0]), "+v"(qf[s & 1][1]), "+v"(P[s & 1][0]), "+v"(P[s & 1][1]));
      };
      auto xfin = [&](int s) {
        u32x4 o;
#pragma unroll
        for (int e = 0; e < 2; ++e) {
          const u32x2 y = yv[s][e];
          const f32x4 yf = {bflo(y[0]), bfhi(y[0]), bflo(y[1]), bfhi(y[1])};
          const f32x4 x = __builtin_elementwise_fma(P[s & 1][e], yf, qf[s & 1][e]);
          o[2 * e] = pack2(x[0], x[1]);
          o[2 * e + 1] = pack2(x[2], x[3]);
        }
        st16(rs, WS_X1B + yo + (unsigned)s * (32u * 20480u), o);
      };
      xpre(0); xfin(0);
      xpre(1); xfin(1);
      SCHED();
      xpre(2); xpre(3);
      lf_wait(24, 4);
      __builtin_amdgcn_s_setprio(3);
      ld_y(2); ld_y(3);
      xfin(2); xfin(3);
      STAMP(28);
      asm volatile("s_waitcnt vmcnt(2)" ::: "memory");
      xflag(l >> 5);
      asm volatile("s_waitcnt vmcnt(1)" ::: "memory");
      xflag(2 + (l >> 5));
      STAMP(5);
    } else {
      const int ch = w;
      char* U = lds + L_U + ch * U_CH;
      char* G = lds + ch * (4 * G_COPY);
      {
        const rsrc_t rf = __builtin_amdgcn_make_buffer_rsrc((void*)(p.ssm_k + (size_t)(quad * 4 + ch) * S_), 0, S_ * 2, 0x00020000);
        u32x4 fa[4];
        u32x2 fb[4];
        __builtin_amdgcn_s_setprio(3);
#pragma unroll
        for (int k = 0; k < 4; ++k) {
          fa[k] = __builtin_amdgcn_raw_buffer_load_b128(rf, l16 + (unsigned)k * 1024u, 0, NT);
        }
        __builtin_amdgcn_s_setprio(1);
        TAG_READY();
#pragma unroll
        for (int k = 0; k < 4; ++k) {
#pragma unroll
          for (int d = 0; d < 2; ++d)
            fb[k][d] = __builtin_amdgcn_update_dpp(k < 3 ? (unsigned)__builtin_amdgcn_readfirstlane(fa[k < 3 ? k + 1 : k][d]) : 0u, fa[k][d], 0x130, 0xf, 0xf, false);
          const unsigned x[6] = {fa[k][0], fa[k][1], fa[k][2], fa[k][3], fb[k][0], fb[k][1]};
          unsigned y[5];
#pragma unroll
          for (int d = 0; d < 5; ++d) y[d] = __builtin_amdgcn_alignbit(x[d + 1], x[d], 16);
          char* gp = G + 32 + k * 1024 + l16;
          *(u32x4*)(gp) = fa[k];
          *(u32x4*)(gp + G_COPY) = (u32x4){y[0], y[1], y[2], y[3]};
          *(u32x4*)(gp + 2 * G_COPY) = (u32x4){x[1], x[2], x[3], x[4]};
          *(u32x4*)(gp + 3 * G_COPY) = (u32x4){y[1], y[2], y[3], y[4]};
          if (k == 0) {
            const unsigned f0 = __builtin_amdgcn_readfirstlane(x[0]), f1 = __builtin_amdgcn_readfirstlane(x[1]);
            const unsigned long long f64 = (unsigned long long)f0 | ((unsigned long long)f1 << 32);
            const unsigned long long hi = l ? (f64 << (64 - 16 * (l & 3))) : 0ull;
            if (l < 4) {
              *(u32x4*)(G + l * G_COPY) = (u32x4){0u, 0u, 0u, 0u};
              *(u32x4*)(G + l * G_COPY + 16) = (u32x4){0u, 0u, (unsigned)hi, (unsigned)(hi >> 32)};
            }
            if (l >= 32) *(u32x4*)(U + (l - 32) * 16) = (u32x4){0u, 0u, 0u, 0u};
          }
        }
        asm volatile("s_waitcnt lgkmcnt(0)" ::: "memory");
        STAMP(1);
      }
      const int a = n16, h = h4, jj = h >> 1, e2 = h & 1;
      const char* ap = G + ((a + 1) & 3) * G_COPY + 2 * (16 * jj + a - 8 * e2 + 9 - ((a + 1) & 3));
      const char* bp = U + 512 + 32 * (a - jj) + 16 * e2;
      auto lda = [&](u32x4* A, int r, int p0, int np) {
#pragma unroll
        for (int pp = 0; pp < np; ++pp) {
          u32x2 lo = *(const volatile LAS u32x2*)(ap + 512 * (p0 + pp) + 64 * r);
          u32x2 hi = *(const volatile LAS u32x2*)(ap + 512 * (p0 + pp) + 64 * r + 8);
          A[pp] = (u32x4){lo[0], lo[1], hi[0], hi[1]};
        }
      };
      u32x4 AD;
      {
        const unsigned dDr = (*(cptr_t)(p.ssm_D + quad * 4 + (ch & 2)) >> (16 * (ch & 1))) & 0xffffu;
        const unsigned dv = h == (a >> 3) ? ((a & 1) ? dDr : dDr << 16) : 0u;
        const int dw = (7 - (a & 7)) >> 1;
#pragma unroll
        for (int d = 0; d < 4; ++d) AD[d] = dw == d ? dv : 0u;
      }
      u32x4 A0[8][4];
      f32x4 acc[8];
#pragma unroll
      for (int n = 0; n < 8; ++n) acc[n] = (f32x4){0.f, 0.f, 0.f, 0.f};
#pragma unroll
      for (int r = 0; r < 8; ++r) lda(A0[r], r, 0, 4);
      auto yput = [&](int s) {
#pragma unroll
        for (int e = 0; e < 2; ++e)
          *(u32x2*)(U + 1024 * s + 512 * e + (16 * a + 4 * h) * 2) = (u32x2){pack2(acc[2 * s + e][0], acc[2 * s + e][1]), pack2(acc[2 * s + e][2], acc[2 * s + e][3])};
        lf_set(16 + 4 * s + ch);
      };
      auto stage = [&](auto S__) {
        constexpr int s = decltype(S__)::value, NH = s < 2 ? 4 - 2 * s : 0, NP = 8 - 2 * s, NB = s < 2 ? 2 : 8;
        u32x4 A1[3][NH > 0 ? NH : 1], B[NB][2];
        auto ldb = [&](int r) {
#pragma unroll
          for (int e = 0; e < 2; ++e) B[r % NB][e] = *(const u32x4*)(bp + 512 * (2 * s + e) - 64 * r);
        };
        if (NH) { lda(A1[0], 0, 4, NH); lda(A1[1], 1, 4, NH); }
        SCHED();
        if (s > 0) yput(s - 1);
        lf_wait(4 * s, 4);
        if (s == 0) STAMP(3);
#pragma unroll
        for (int r = 0; r < NB; ++r) ldb(r);
        SCHED();
#pragma unroll
        for (int e = 0; e < 2; ++e) acc[2 * s + e] = MFMA16(AD, B[0][e], acc[2 * s + e]);
#pragma unroll
        for (int r = 0; r < 8; ++r) {
          if (NH && r + 2 < 8) lda(A1[(r + 2) % 3], r + 2, 4, NH);
          SCHED();
#pragma unroll
          for (int e = 0; e < 2; ++e)
#pragma unroll
            for (int pp = 0; pp < NP - e; ++pp) acc[2 * s + e + pp] = MFMA16(pp < 4 ? A0[r][pp] : A1[r % 3][pp < 4 ? 0 : pp - 4], B[r % NB][e], acc[2 * s + e + pp]);
          SCHED();
          if (r + NB < 8) ldb(r + NB);
        }
      };
      stage(IC<0>());
      STAMP(4);
      stage(IC<1>());
      STAMPF(9);
      const unsigned fo[1] = {fwi + (unsigned)((8 + c) * 20 + 5 * w + (l < 5 ? l : 0)) * 128u};
      unsigned fv = ~tag;
      {
        u32x4 B[4][4];
        auto ldb = [&](int r) {
#pragma unroll
          for (int m = 0; m < 4; ++m) B[r & 3][m] = *(const u32x4*)(bp + 512 * (4 + m) - 64 * r);
        };
        yput(1);
        if (w == 3) wo_ld();
        __builtin_amdgcn_s_setprio(3);
        lf_wait(8, 8);
        STAMP(22);
#pragma unroll
        for (int r = 0; r < 4; ++r) ldb(r);
        SCHED();
#pragma unroll
        for (int m = 0; m < 4; ++m) acc[4 + m] = MFMA16(AD, B[0][m], acc[4 + m]);
#pragma unroll
        for (int r = 0; r < 8; ++r) {
#pragma unroll
          for (int m = 0; m < 4; ++m)
#pragma unroll
            for (int pp = 0; pp < 4 - m; ++pp) acc[4 + m + pp] = MFMA16(A0[r][pp], B[r & 3][m], acc[4 + m + pp]);
          SCHED();
          if (r + 4 < 8) ldb(r + 4);
          if (r == 1) fv = ld4(rs, fo[0]);
          SCHED();
        }
#pragma unroll
        for (int e = 0; e < 4; ++e)
          *(u32x2*)(U + 2048 + 512 * e + (16 * a + 4 * h) * 2) = (u32x2){pack2(acc[4 + e][0], acc[4 + e][1]), pack2(acc[4 + e][2], acc[4 + e][3])};
        lf_set(24 + ch);
      }
      STAMP(28);
      __builtin_amdgcn_s_setprio(0);
      if (!__all(fv == tag)) wait_lines<1, 4>(rs, fo, tag);
      lf_wait(20, 4);
    }
    __builtin_amdgcn_s_setprio(0);
  } else {
    const int at = idx - 20;
    char* const scr = lds + WBLK + 14848;
    auto gv_blk = [&](int j) { return j < 160 ? 30 + (j >> 3) : j < 400 ? (j >> 3) - 20 : j >> 3; };
    auto gv_fin = [&](const u32x4* pre, int j) {
      float acc[8];
      gemv_fin(acc, pre, 10, scr);
      gemv_put(acc, j < 160 ? WS_MODP + (unsigned)((j & 7) * 1280 + (j >> 3) * 64) * 8u : WS_MODQ + (unsigned)((j & 7) * 2560 + ((j >> 3) - 20) * 64) * 8u);
    };
    const int ja = xcd * 12 + at, ai = xcd * 8 + at, bi = xcd * 4 + at - 8, g = w >> 2, wj = w & 3;
    const int rj = w == 0 ? ja : w == 1 ? (at >= 4 ? 92 + ai : -1) : w == 4 ? 160 + ja : w == 5 && at < 8 ? 256 + ai : -1;
    char* const rst = lds + (g ? WBLK : 65536) + wj * 7424;
    u32x4 pre[11];
    if (at >= 8 || g == 0) {
      TOUCH_AT(2);
      const int T = at >= 8 ? 64 + 2 * bi + g : ai;
      unsigned* mds = (unsigned*)(lds + 2048) + 16 * g;
      char* qt = lds + L_RT + g * (16 * 1312);
      u32x4 ld[5];
      const bool e1 = at >= 8 && w == 3;
      if (e1) gemv_load(pre, gv_blk(128 + bi), bi & 7, 10);
      else __builtin_amdgcn_s_sleep(8);
#pragma unroll
      for (int j = 0; j < 5; ++j)
        ld[j] = __builtin_amdgcn_raw_buffer_load_b128(rq, (unsigned)(h4 * D_ * 2 + n16 * 16 + j * 256), (unsigned)((32 * (T >> 1) + 8 * wj + 4 * (T & 1)) * D_ * 2), NT);
      TAG_READY();
      {
        const bf2_t ONE2 = {(__bf16)1.0f, (__bf16)1.0f};
        char* dst = qt + (4 * wj + h4) * 1312 + n16 * 16;
        float sa = 0.f, va = 0.f;
#pragma unroll
        for (int j = 0; j < 5; ++j) {
          *(u32x4*)(dst + j * 256) = ld[j];
#pragma unroll
          for (int d = 0; d < 4; ++d) {
            const unsigned a0 = ld[j][d];
            const bf2_t xa = __builtin_bit_cast(bf2_t, a0);
            sa = __builtin_amdgcn_fdot2_f32_bf16(xa, ONE2, sa, false); va = __builtin_amdgcn_fdot2_f32_bf16(xa, xa, va, false);
          }
        }
        lf_set(8 + w);
        const float ma = sum16(sa) * (1.0f / D_);
        va = __builtin_fmaxf(sum16(va) * (1.0f / D_) - ma * ma, 0.f);
        if (n16 == 0) mds[4 * wj + h4] = pack2(ma, __builtin_amdgcn_sqrtf(rbf(rbf(va) + 1e-5f)));
      }
      if (e1) gv_fin(pre, 128 + bi);
      lf_wait(8 + 4 * g, 4);
      STAMP(23);
      {
        const char* src = qt + (l & 7) * 1312 + (40 * wj + (l >> 3)) * 8;
#pragma unroll
        for (int j = 0; j < 5; ++j) {
          const u32x2 x = *(const u32x2*)(src + j * 64), y = *(const u32x2*)(src + j * 64 + 8 * 1312);
          st16(rs, WS_QB + (unsigned)T * 20480u + (unsigned)(5 * wj + j) * 1024u + l16, (u32x4){x[0], x[1], y[0], y[1]});
        }
      }
      SCHED();
      const int g2 = at < 8 || w < 3 || w == 4 ? 400 : w == 3 ? 384 + bi : 256 + wj * 32 + bi;
      if (g2 < 400) {
        gemv_load(pre, gv_blk(g2), g2 & 7, 10);
        asm volatile("s_waitcnt vmcnt(10)" ::: "memory");
      } else if (w < 2 && rj >= 0) {
        rt_ld(rj, rst);
        asm volatile("s_waitcnt vmcnt(7)" ::: "memory");
      } else {
        DRAIN();
      }
      lf_set(16 + w);
      if (wj == 2) {
        lf_wait(16 + 4 * g, 4);
        st16(rs, WS_ST1 + (unsigned)(l >> 3) * 16384u + (unsigned)T * 128u + (unsigned)(l & 7) * 16u, (u32x4){mds[l & 7], tag, mds[8 + (l & 7)], tag});
        STAMP(22);
      }
      if (w >= 4 && w < 7) lf_set(w);
      if (g2 < 400) gv_fin(pre, g2);
      STAMP(25);
    } else {
      constexpr int KB = L_RT + 16 * 1312;
      TOUCH_AT(3);
      const int g1 = w < 6 ? 2 * ai + wj : 32 + wj * 64 + ai;
      if (w >= 6) {
        const rsrc_t rk = __builtin_amdgcn_make_buffer_rsrc((void*)(w == 6 ? p.k : p.v), 0, S_ * D_ * 2, 0x00020000);
        const unsigned ko = (unsigned)((1536 + 8 * ai) * (D_ * 2) - (w == 6 ? 4096 : 0));
        char* dst = lds + KB + (w == 6 ? 0 : 14336);
        __builtin_amdgcn_s_sleep(3);
#pragma unroll
        for (int n = 0; n < 10; ++n) __builtin_amdgcn_raw_ptr_buffer_load_lds(rk, (LAS void*)(dst + n * 1024), 16, l16, ko + (unsigned)n * 1024u, 0, NT);
        if (w == 6) {
#pragma unroll
          for (int n = 10; n < 14; ++n) __builtin_amdgcn_raw_ptr_buffer_load_lds(rk, (LAS void*)(dst + n * 1024), 16, l16, ko + (unsigned)n * 1024u, 0, NT);
        }
      }
      SCHED();
      u32x4 swt[2];
      swt[0] = *(const u32x4*)(p.shift_k + (tid - 256) * 8);
      swt[1] = *(const u32x4*)(p.shift_k + (256 + l) * 8);
      SCHED();
      if (w >= 6) __builtin_amdgcn_s_sleep(8);
      gemv_load(pre, gv_blk(g1), g1 & 7, 10);
      TAG_READY();
      asm volatile("s_waitcnt vmcnt(11)" : "+v"(swt[0]), "+v"(swt[1]) :: "memory");
      lf_set(8 + w);
      if (w < 6) gv_fin(pre, g1);
      STAMP(25);
      lf_wait(12, 4);
      STAMP(23);
#pragma unroll 1
      for (int n = 0; n < (w == 7 ? 2 : 1); ++n) {
        {
          const int pr = n ? 256 + l : tid - 256;
          const char* kp = lds + KB + 4096 + pr * 4;
          unsigned kk[11], vv[8];
#pragma unroll
          for (int i = 0; i < 11; ++i) kk[i] = *(const unsigned*)(kp + (i - 3) * 1280);
#pragma unroll
          for (int i = 0; i < 8; ++i) vv[i] = *(const unsigned*)(kp + 10240 + i * 1280);
          const u32x4 sw = n ? swt[1] : swt[0];
          const f32x2 wt[4] = {{bflo(sw[0]), bflo(sw[2])}, {bfhi(sw[0]), bfhi(sw[2])}, {bflo(sw[1]), bflo(sw[3])}, {bfhi(sw[1]), bfhi(sw[3])}};
          f32x2 o[8];
#pragma unroll
          for (int i = 0; i < 8; ++i) {
            f32x2 t = (f32x2){bflo(kk[i + 3]), bfhi(kk[i + 3])} * wt[0];
#pragma unroll
            for (int j = 1; j < 4; ++j) t = __builtin_elementwise_fma((f32x2){bflo(kk[i + 3 - j]), bfhi(kk[i + 3 - j])}, wt[j], t);
            o[i] = t * (f32x2){bflo(vv[i]), bfhi(vv[i])};
          }
#pragma unroll
          for (int e = 0; e < 4; ++e) {
            const int i = 7 - 4 * (e & 1);
            st16(rs, WS_KV3 + (unsigned)ai * 20480u + (unsigned)(e * 5120 + pr * 16),
                 (u32x4){pack2(o[i][e >> 1], o[i - 1][e >> 1]), tag, pack2(o[i - 2][e >> 1], o[i - 3][e >> 1]), tag});
          }
        }
      }
      STAMP(22);
      if (w < 7) lf_set(w);
      if (w >= 6) gv_fin(pre, g1);
      STAMPF(30);
    }
    if (w == 6 && ja < 80) {
      hold(AUX_T);
      gemv_load(pre, gv_blk(400 + ja), ja & 7, 10);
      gv_fin(pre, 400 + ja);
    }
    if (rj >= 0) {
      if (w >= 2) { hold(RT2_T); rt_ld(rj, rst); }
      rt_job(rj, rst);
    }
    STAMP(26);
    lf_set(w);
    if (w == 3) { hold(WA_T); wo_ld(); }
    STAMP(3);
    if (w < 4) {
      {
        const unsigned fo[1] = {l < 5 ? fwi + (unsigned)((8 + c) * 20 + 5 * w + l) * 128u : ~0u};
        hold(W_T);
        wait_lines<1, 8>(rs, fo, tag);
      }
      lf_wait(0, 8);
      STAMP(4);
    }
    if ((at == 8 || at == 9) && w == 5 && xcd * 2 + at - 8 < 10) {
      const unsigned o = WS_MODQ + (unsigned)(h4 * 5120 + 1920 + (xcd * 2 + at - 8) * 64 + n16 * 4) * 8u;
      u32x4 v[4];
      hold(CS_T);
      _Pragma("unroll 1") for (unsigned spins = 0; spins < SPIN_CAP; ++spins) {
        unsigned dd = 0u;
#pragma unroll
        for (int k = 0; k < 4; ++k) v[k] = ld16(rs, o + (unsigned)((k >> 1) * 20480 + (k & 1) * 16));
#pragma unroll
        for (int k = 0; k < 4; ++k) dd |= (v[k][0] ^ tag) | (v[k][2] ^ tag);
        if (__all(dd == 0u)) break;
        __builtin_amdgcn_s_sleep(16);
      }
      float t[4];
#pragma unroll
      for (int k = 0; k < 4; ++k) t[k] = sum_rows(__uint_as_float(v[k >> 1][1 + 2 * (k & 1)]) + __uint_as_float(v[2 + (k >> 1)][1 + 2 * (k & 1)]));
#pragma unroll
      for (int e = 0; e < 2; ++e)
        st16(rs, WS_MOD + (unsigned)(4 * e + h4) * 2560u + (unsigned)((xcd * 2 + at - 8) * 16 + n16) * 16u, (u32x4){tag, pack2(t[0], t[1]), tag, pack2(t[2], t[3])});
    }
    if (w == 4 || (w == 5 && at >= 10)) {
      const int cj = w == 4 ? ja : 86 + xcd * 2 + at, cb = cj / 7, j = cj % 7;
      const unsigned o = WS_CSP + (unsigned)((cb * 20 + 5 * h4) * 2 + (j >> 2)) * 1024u + (unsigned)((j & 3) * 16 + n16) * 16u;
      u32x4 v[5];
      hold(CS_T);
      __builtin_amdgcn_s_setprio(3);
      _Pragma("unroll 1") for (unsigned spins = 0; spins < SPIN_CAP; ++spins) {
        unsigned dd = 0u;
#pragma unroll
        for (int k = 0; k < 5; ++k) v[k] = ld16(rs, o + (unsigned)k * 2048u);
#pragma unroll
        for (int k = 0; k < 5; ++k) dd |= (v[k][0] ^ tag) | (v[k][2] ^ tag);
        if (__all(dd == 0u)) break;
        __builtin_amdgcn_s_sleep(8);
      }
      float t0 = 0.f, t1 = 0.f;
#pragma unroll
      for (int k = 0; k < 5; ++k) { t0 += __uint_as_float(v[k][1]); t1 += __uint_as_float(v[k][3]); }
      t0 = sum_rows(t0); t1 = sum_rows(t1);
      if (l < 16) st16(rs, WS_CS + (unsigned)(cb * 2048 + j * 256) + l16, (u32x4){tag, __float_as_uint(t0), tag, __float_as_uint(t1)});
      __builtin_amdgcn_s_setprio(0);
      STAMP(24);
    }
  }

  STAMPF(16);
  u32x4 wox[5];
  DRAIN();
  if (w == 3) {
    const char* bp = wo_st + (4 * h4 + (n16 >> 2)) * 160 + (n16 & 3) * 8;
#pragma unroll
    for (int dj = 0; dj < 5; ++dj) wox[dj] = trfrag(bp + dj * 32, 160);
    asm volatile("s_waitcnt lgkmcnt(0)" : "+v"(wox[0]), "+v"(wox[1]), "+v"(wox[2]), "+v"(wox[3]), "+v"(wox[4]) :: "memory");
  }
  auto wo_put = [&]() {
    if (w == 3 && R < 28) {
      const unsigned base = WS_WOUTF + (unsigned)c * WBLK + (unsigned)((R >> 1) * 10240 + (R & 1) * 1024);
#pragma unroll
      for (int dj = 0; dj < 5; ++dj) st16(rs, base + (unsigned)((R & 1) ? dj : (dj + 2) % 5) * 2048u + l16, wox[dj]);
    }
  };
  if (w < 4) load_b2();
  wo_put();
  STAMPF(29);
  u32x4 k_c;
  unsigned kc_o;
  {
    const int i = tid < 224 ? tid : 0;
    const int col = i >= 112 ? (8 + c) * 128 + i - 112 : c * 128 + i;
    kc_o = w < 4 ? WS_CS + (unsigned)col * 16u : WS_MOD + (unsigned)xcd * 2560u + (unsigned)(20 * c + (tid < 276 ? tid - 256 : 0)) * 16u;
  }
  auto consts_put = [&]() {
    if (!__all(k_c[0] == tag && k_c[2] == tag)) {
      _Pragma("unroll 1") for (unsigned spins = 0; spins < SPIN_CAP; ++spins) {
        __builtin_amdgcn_s_sleep(8);
        k_c = ld16(rs, kc_o);
        if (__all(k_c[0] == tag && k_c[2] == tag)) break;
      }
    }
    if (tid < 224) *(u32x2*)(lds + L_CONST + tid * 8) = (u32x2){k_c[1], k_c[3]};
    if (tid >= 256 && tid < 276) *(f32x4*)(lds + L_MISC + (tid - 256) * 16) = (f32x4){bflo(k_c[1]), bfhi(k_c[1]), bflo(k_c[3]), bfhi(k_c[3])};
  };
  const int wq = w ^ rotr, rt1 = wq & 3, kh1 = w >> 2;
  unsigned bo0 = (unsigned)(kh1 * 71680) + l16, bo1 = bo0 + 35840u;
  asm volatile("" : "+v"(bo0), "+v"(bo1));
#define BFRAG(q, j) (*(const u32x4*)(lds + ((q) < 5 ? bo0 : bo1) + ((q) % 5) * 7168 + (j) * 1024))
  u32x4 h2[10];
  f32x4 acc[7];
  float mu_n, rs_n;
  {
    if (w == 6) {
      const unsigned fo[1] = {l < 20 ? fwi + (unsigned)(c * 20 + l) * 128u : ~0u};
      hold(CS_T);
      wait_lines<1, 8>(rs, fo, tag);
    }
    if (w == 7) {
      const unsigned fb = WS_FX + (unsigned)(((R >> 1) * 4 + (R & 1) * 2 + (c >> 2)) * 160 + l) * 128u;
      const unsigned fo[3] = {fb, fb + 8192u, l < 32 ? fb + 16384u : ~0u};
      if (!is_conv) hold(Y_T);
      wait_lines<3, 2>(rs, fo, tag);
      if (l == 0) lf[33] = tag;
    }
    lf_wait(33, 1);
    STAMP(7);
    const unsigned xo = (unsigned)(R * 4 + rt1) * 20480u + (unsigned)(10 * kh1) * 1024u +
                        (unsigned)((4 * ((l >> 3) & 1) + h4) * 128 + (l & 7) * 16);
    SCHED();
#pragma unroll
    for (int q = 0; q < 3; ++q) h2[q] = ld16p(rs, WS_X1B + xo + (unsigned)q * 1024u);
    SCHED();
    __builtin_amdgcn_s_waitcnt(0x0F73);
    BAR();
    if (w == 3) st_flag4(rs, WS_FWO + (unsigned)(c * 32 + R) * 128u, tag, l);
    STAMP(8);
#pragma unroll
    for (int j = 0; j < 7; ++j) acc[j] = (f32x4){0.f, 0.f, 0.f, 0.f};
    f32x4 accg = {0.f, 0.f, 0.f, 0.f};
    u32x4 bf[7];
#pragma unroll
    for (int j = 0; j < 7; ++j) bf[j] = BFRAG(0, j);
    SCHED();
#pragma unroll
    for (int q = 3; q < 6; ++q) h2[q] = ld16p(rs, WS_X1B + xo + (unsigned)q * 1024u);
    SCHED();
#pragma unroll
    for (int q = 0; q < 10; ++q) {
      {
#pragma unroll
        for (int e = 0; e < 6; ++e) {
          const int d = e & 1;
          if ((e >> 1) == 1) h2[q][2 + d] ^= (unsigned)__builtin_amdgcn_update_dpp(0, (int)h2[q][d], 0x128, 0xf, 0x3, false);
          else h2[q][d] ^= (unsigned)__builtin_amdgcn_update_dpp(0, (int)h2[q][2 + d], 0x128, 0xf, 0xC, false);
        }
      }
      if (q == 5) { __builtin_amdgcn_s_barrier(); asm volatile("" ::: "memory"); }
      SCHED();
      accg = MFMA16(h2[q], h2[q], accg);
      SCHED();
#pragma unroll
      for (int j = 0; j < 7; ++j) {
        acc[j] = MFMA16(h2[q], bf[j], acc[j]);
        if (q == 9 && j == 1 && h4 == (n16 >> 2)) *(f32x4*)(lds + L_PST + 512 + (wq * 16 + n16) * 16) = accg;
        if (q < 9) bf[j] = BFRAG(q + 1, j);
#define WQ0 2
        if (WQ0 == 2 && q >= 2 && j == 2) LDW(q - 2);
        if (WQ0 == 6 && q >= 6 && (j == 2 || j == 4)) LDW(2 * (q - 6) + (j >> 2));
        if ((j == 0 || j == 4) && 6 + 2 * q + (j >> 2) < 10) h2[6 + 2 * q + (j >> 2)] = ld16p(rs, WS_X1B + xo + (unsigned)(6 + 2 * q + (j >> 2)) * 1024u);
        if (q == 4 && j == 0) k_c = ld16(rs, kc_o);
        if (q >= 5 && j == 5) {
          const int i = (5 + q) * 7 + (w < 7 ? w : 0);
          dma1k(rs, lds + i * 1024, l16, WS_WINF + (unsigned)c * WBLK + (unsigned)i * 1024u);
          if (q == 9) { const int i2 = 28 + (w < 7 ? w : 0); dma1k(rs, lds + i2 * 1024, l16, WS_WINF + (unsigned)c * WBLK + (unsigned)i2 * 1024u); }
        }
        SCHED();
      }
    }
    STAMP(31);
  }
  auto gelu_n = [&](f32x2* x, auto N_) {
    constexpr int N = decltype(N_)::value;
    const float c0 = -2.0f * 0.7978845608028654f * 1.4426950408889634f, c1 = c0 * 0.044715f;
    f32x2 k0 = {c0, c0}, k1 = {c1, c1}, k2 = {1.0f, 1.0f};
    asm volatile("" : "+v"(k0), "+v"(k1), "+v"(k2));
    f32x2 t[N];
#define GST(e) do { _Pragma("unroll") for (int i = 0; i < N; ++i) { t[i] = (e); asm volatile("" : "+v"(t[i])); } } while (0)
    GST(x[i] * x[i]);
    GST(__builtin_elementwise_fma(t[i], k1, k0));
    GST(t[i] * x[i]);
    GST(((f32x2){__builtin_amdgcn_exp2f(t[i][0]), __builtin_amdgcn_exp2f(t[i][1])}));
    GST(t[i] + k2);
    GST(((f32x2){__builtin_amdgcn_rcpf(t[i][0]), __builtin_amdgcn_rcpf(t[i][1])}));
#pragma unroll
    for (int i = 0; i < N; ++i) { x[i] *= t[i]; asm volatile("" : "+v"(x[i])); }
  };
  {
    f32x2* red = (f32x2*)lds;
    f32x2 mine[7];
    if (kh1) {
#pragma unroll
      for (int j = 0; j < 7; ++j) { red[(w * 7 + j) * 64 + l] = (f32x2){acc[j][0], acc[j][1]}; mine[j] = (f32x2){acc[j][2], acc[j][3]}; }
    } else {
#pragma unroll
      for (int j = 0; j < 7; ++j) { red[(w * 7 + j) * 64 + l] = (f32x2){acc[j][2], acc[j][3]}; mine[j] = (f32x2){acc[j][0], acc[j][1]}; }
    }
    f32x2 csb[7];
    unsigned cso = (unsigned)(L_CONST + (112 + n16) * 8);
    asm volatile("" : "+v"(cso));
    consts_put();
    unsigned pso[3], pqo[3], oto = (unsigned)(((w ^ 4) * 7 * 64 + l) * 8);
#pragma unroll
    for (int e = 0; e < 3; ++e) {
      const int row = rt1 * 16 + (e < 2 ? 4 * h4 + 2 * kh1 + e : n16);
      pso[e] = (unsigned)(L_PST + row * 4); pqo[e] = (unsigned)(L_PST + 512 + row * 16 + (row & 3) * 4);
    }
    asm volatile("" : "+v"(pso[0]), "+v"(pso[2]), "+v"(pqo[0]), "+v"(pqo[1]), "+v"(pqo[2]), "+v"(oto));
    pso[1] = pso[0] + 4u;
    if (n16 == 12) *(f32x4*)(lds + L_PST + (wq * 16 + 4 * h4) * 4) = acc[6];
    if (!kh1) {
#pragma unroll
      for (int k = 8; k < 16; ++k) LDW(k);
    }
    BAR();
    if (kh1) {
#pragma unroll
      for (int k = 8; k < 16; ++k) LDW(k);
    }
    __builtin_amdgcn_s_setprio(3);
    STAMPT(9);
    f32x2 oth[7];
    f32x2 pa[3], pb[3];
#pragma unroll
    for (int e = 0; e < 3; ++e) {
      pa[e] = (f32x2){*(const float*)(lds + pso[e]), *(const float*)(lds + pqo[e])};
      pb[e] = (f32x2){*(const float*)(lds + pso[e] + 256), *(const float*)(lds + pqo[e] + 1024)};
    }
#pragma unroll
    for (int j = 0; j < 7; ++j) { oth[j] = *(const f32x2*)(lds + oto + j * 512); csb[j] = *(const f32x2*)(lds + cso + 128 * j); }
    SCHED();
    float mu2[2], rs2[2];
#pragma unroll
    for (int e = 0; e < 3; ++e) {
      const float mean = (pa[e][0] + pb[e][0]) * (1.0f / D_);
      const float var = __builtin_fmaxf((pa[e][1] + pb[e][1]) * (1.0f / D_) - mean * mean, 0.f);
      const float rstd = __builtin_amdgcn_rsqf(var + 1e-5f);
      if (e < 2) { mu2[e] = mean; rs2[e] = rstd; } else { mu_n = mean; rs_n = rstd; }
    }
    f32x2 zz[7];
    const f32x2 nmu = {-mu2[0], -mu2[1]}, rsv = {rs2[0], rs2[1]};
#pragma unroll
    for (int j = 0; j < 7; ++j)
      zz[j] = __builtin_elementwise_fma(__builtin_elementwise_fma(nmu, (f32x2){csb[j][0], csb[j][0]}, mine[j] + oth[j]), rsv, (f32x2){csb[j][1], csb[j][1]});
    gelu_n(zz, IC<7>());
    if (n16 >= 12) zz[6] = (f32x2){0.f, 0.f};
    f32x2 s1 = ((zz[0] + zz[1]) + (zz[2] + zz[3])) + ((zz[4] + zz[5]) + zz[6]);
    f32x2 s2 = zz[0] * zz[0], s3 = zz[1] * zz[1];
#pragma unroll
    for (int j = 2; j < 7; ++j) { if (j & 1) s3 = __builtin_elementwise_fma(zz[j], zz[j], s3); else s2 = __builtin_elementwise_fma(zz[j], zz[j], s2); }
    s2 += s3;
    asm("" : "+v"(s1), "+v"(s2));
    float a0 = s1[0] + dpp_f<0xB1>(s1[0]), a1 = s1[1] + dpp_f<0xB1>(s1[1]), b0 = s2[0] + dpp_f<0xB1>(s2[0]);
    a0 = (l & 1) ? a1 : a0;
    const float b1 = s2[1] + dpp_f<0xB1>(s2[1]);
    b0 = (l & 1) ? b1 : b0;
    a0 += dpp_f<0x4E>(a0); b0 += dpp_f<0x4E>(b0);
    a0 += dpp_f<0x124>(a0); b0 += dpp_f<0x124>(b0);
    a0 += dpp_f<0x128>(a0); b0 += dpp_f<0x128>(b0);
    {
      const int src = ((l >> 1) & 3) * 64 + (l & 1) * 4;
      const unsigned pa0 = __builtin_amdgcn_ds_bpermute(src, __float_as_uint(a0)), pb0 = __builtin_amdgcn_ds_bpermute(src, __float_as_uint(b0));
      if (l < 8) st16(rs, WS_PART + (unsigned)((R * 8 + c) * 8 + wq) * 128u + l16, (u32x4){tag, pa0, tag, pb0});
    }
    SCHED();
    __builtin_amdgcn_s_setprio(0);
    STAMP(10);
#pragma unroll
    for (int k = 0; k < 10; ++k) {
      if (k < 9) {
        int x = k * 8 + w;
        x = x < 70 ? x : 69;
        const int i = x + (x < 35 ? 35 : 70);
        dma1k(rs, lds + i * 1024, l16, WS_WINF + (unsigned)c * WBLK + (unsigned)i * 1024u);
      }
    }
#pragma unroll
    for (int j = 0; j < 7; ++j)
      *(unsigned*)(lds + L_Z + (((rt1 >> 1) * 7 + j) * 64 + l) * 16 + (4 * (rt1 & 1) + 2 * kh1) * 2) = pack2(zz[j][0], zz[j][1]);
    SCHED();
#define TR0 2
#define TR1 TR0
#define TR2 TR1
#define TR3 5
#pragma unroll
    for (int g = 0; g < TR0; ++g) WSP_TR(g);
    if (w == 1) {
      const unsigned fo[1] = {l < 28 ? fwi + (WS_FWO - WS_FWI) + (unsigned)(c * 32 + l) * 128u : ~0u};
      wait_lines<1, 1>(rs, fo, tag);
    }
    DRAIN();
    BAR();
    STAMPT(11);
  }
  unsigned bs_raw;
  unsigned z1d[7];
  constexpr int L_RED = 28672;
#define WOA(i) (((i) < 28 ? (i) : (i) < 108 ? (i) + 32 : (i) < 120 ? (i) + 37 : (i) - 92) * 1024)
#define RBAR() do { __builtin_amdgcn_s_barrier(); asm volatile("" ::: "memory"); } while (0)
#define ZAUX 16
#define NW 14
  unsigned wx8 = (unsigned)((w + (idx >> 2)) & 7) * 1024u, wx4 = wx8 & 3072u, wgb = WS_WOUTF + (unsigned)c * WBLK;
  asm volatile("" : "+s"(wx8), "+s"(wx4), "+s"(wgb));
#define WOD(i0, x) dma1k(rs, lds + WOA(i0) + (x), l16, wgb + (unsigned)(i0) * 1024u + (x))
  auto wo_dma = [&](int k) {};
  unsigned zs[8];
#pragma unroll
  for (int t = 0; t < 8; ++t) zs[t] = WS_Z2F + (unsigned)(c * 64 + 8 * w + 4 * ((t >> 2) ^ rotg) + ((t + rotb) & 3)) * 7168u;
  u32x4 zw[NW];
#define LDZ(f) do { if (((f) >> 3) == 4) { zs[(f) & 7] += 4096u; asm volatile("" : "+s"(zs[(f) & 7])); } \
    zw[(f) % NW] = __builtin_amdgcn_raw_buffer_load_b128(rs, l16 + (unsigned)(((f) >> 3) & 3) * 1024u, zs[(f) & 7], ZAUX); } while (0)
#define WD 24
#define WE 32
#define QS 5
#define PQ 3
#define NW0 NW
#define PP 1
#define WZ 4
#define WY 11
  {
    f32x4 acc[7];
#pragma unroll
    for (int j = 0; j < 7; ++j) acc[j] = (f32x4){0.f, 0.f, 0.f, 0.f};
    u32x4 bf[7];
#pragma unroll
    for (int j = 0; j < 7; ++j) bf[j] = BFRAG(4, j);
    const unsigned po = WS_PART + (unsigned)((R * 8 + (l >> 3)) * 8 + wq) * 128u + (unsigned)(l & 7) * 16u;
    u32x4 pv;
    auto z1 = [&](auto Q0_, auto Q1_) __attribute__((always_inline)) {
#pragma unroll
      for (int qi = decltype(Q0_)::value; qi < decltype(Q1_)::value; ++qi) {
        const int q = (qi + 4) % 10, qn = (qi + 5) % 10;
        SCHED();
#pragma unroll
        for (int j = 0; j < 7; ++j) {
          acc[j] = MFMA16(bf[j], h2[q], acc[j]);
          if (qi < 9 && qi != QS - 1) bf[j] = BFRAG(qn, j);
          if (qi < 2 && (j == 1 || j == 4)) { const int i = (2 * qi + (j >> 2)) * 7 + (w < 7 ? w : 0); dma1k(rs, lds + i * 1024, l16, WS_WINF + (unsigned)c * WBLK + (unsigned)i * 1024u); }
          if (qi == PQ && j == 0) pv = ld16(rs, po);
          if (!PP && qi >= QS && (j & 1) && WD + 3 * (qi - QS) + (j >> 1) < WE) LDW((WD + 3 * (qi - QS) + (j >> 1)) & 31);
          SCHED();
        }
      }
    };
    auto req1 = [&]() {
#pragma unroll
      for (int k = 16; k < WD; ++k) { LDW(k); SCHED(); }
    };
    auto req2 = [&]() {
#pragma unroll
      for (int k = WD; k < (PP ? WE : WD); ++k) { LDW(k); SCHED(); }
      sfor<0, WZ>([&](auto G_) __attribute__((always_inline)) { WOD(68 + 8 * decltype(G_)::value, wx8); SCHED(); });
    };
    if (kh1) req1();
    z1(IC<0>(), IC<QS>());
    if (!kh1) req1();
    STAMPT(29);
#pragma unroll
    for (int g = TR0; g < TR1; ++g) WSP_TR(g);
    __builtin_amdgcn_s_setprio(3);
    if (!__all(((pv[0] ^ tag) | (pv[2] ^ tag)) == 0u)) {
      _Pragma("unroll 1") for (unsigned spins = 0; spins < SPIN_CAP; ++spins) {
        pv = ld16(rs, po);
        if (__all(((pv[0] ^ tag) | (pv[2] ^ tag)) == 0u)) break;
        __builtin_amdgcn_s_sleep(1);
      }
    }
    {
      const float mean = red8(__uint_as_float(pv[1])) * (1.0f / FH);
      const float var = __builtin_fmaxf(red8(__uint_as_float(pv[3])) * (1.0f / FH) - mean * mean, 0.f);
      const float rstd = __builtin_amdgcn_rsqf(var + 1e-5f);
      if (l < 8) *(f32x2*)(lds + L_ST2 + (16 * rt1 + 4 * (l >> 1) + 2 * kh1 + (l & 1)) * 8) = (f32x2){rstd, -mean * rstd};
    }
    BAR();
    STAMP(12);
    u32x4 zf[2];
#pragma unroll
    for (int m = 0; m < 2; ++m) zf[m] = *(const u32x4*)(lds + L_Z + ((m * 7 + (w < 7 ? w : 0)) * 64 + l) * 16);
    if (w < 7) {
#pragma unroll
      for (int m = 0; m < 2; ++m) {
        const f32x4* sp = (const f32x4*)(lds + L_ST2 + (32 * m + 4 * h4) * 8);
        u32x4 o;
#pragma unroll
        for (int e = 0; e < 4; ++e) {
          f32x4 st = sp[(e & 1) + 8 * (e >> 1)];
          o[e] = pack2(__builtin_fmaf(bflo(zf[m][e]), st[0], st[1]), __builtin_fmaf(bfhi(zf[m][e]), st[2], st[3]));
        }
        st16(rs, WS_Z2F + (unsigned)((c * 64 + 4 * (R >> 1) + h4) * 7 + (w < 6 ? w + 1 : 0)) * 1024u + (unsigned)((2 * (R & 1) + m) * 256 + n16 * 16), o);
      }
    }
    SCHED();
    __builtin_amdgcn_s_setprio(0);
    STAMPT(13);
#pragma unroll
    for (int j = 0; j < 7; ++j) bf[j] = BFRAG((QS + 4) % 10, j);
    if (kh1) req2();
    z1(IC<QS>(), IC<10>());
    f32x2* red = (f32x2*)(lds + L_RED);
    f32x2 mine[7];
    if (kh1) {
#pragma unroll
      for (int j = 0; j < 7; ++j) { red[(w * 7 + j) * 64 + l] = (f32x2){acc[j][0], acc[j][1]}; mine[j] = (f32x2){acc[j][2], acc[j][3]}; }
    } else {
#pragma unroll
      for (int j = 0; j < 7; ++j) { red[(w * 7 + j) * 64 + l] = (f32x2){acc[j][2], acc[j][3]}; mine[j] = (f32x2){acc[j][0], acc[j][1]}; }
    }
    if (!kh1) req2();
#pragma unroll
    for (int g = TR1; g < TR2; ++g) WSP_TR(g);
    asm volatile("s_waitcnt vmcnt(%0)" :: "n"(WE - WD + WZ) : "memory");
    BAR();
    if (w == 3) st_flag4(rs, WS_FZ + (unsigned)(c * 32 + R) * 128u, tag, l);
    STAMP(6);
    auto req = [&](auto G0_, auto G1_) __attribute__((always_inline)) {
#pragma unroll
      for (int k = WE; k < 32; ++k) LDW(k);
      bs_raw = p.b_sp[R * 64 + tau(rt1, n16)];
      SCHED();
      sfor<decltype(G0_)::value, decltype(G1_)::value>([&](auto G_) __attribute__((always_inline)) {
        constexpr int i0 = 4 * decltype(G_)::value;
        WOD(i0 < 68 ? i0 : i0 + 8 * WZ, wx4); SCHED();
      });
    };
    if (kh1) req(IC<0>(), IC<WY>());
    f32x2 z[7];
    {
      f32x2 oth[7];
      f32x4 cc[7];
#pragma unroll
      for (int j = 0; j < 7; ++j) {
        oth[j] = red[((w ^ 4) * 7 + j) * 64 + l];
        cc[j] = *(const f32x4*)(lds + L_CONST + (16 * j + 4 * h4 + 2 * kh1) * 8);
      }
      SCHED();
#pragma unroll
      for (int j = 0; j < 7; ++j)
        z[j] = __builtin_elementwise_fma(__builtin_elementwise_fma((f32x2){-mu_n, -mu_n}, (f32x2){cc[j][0], cc[j][2]}, oth[j] + mine[j]),
                                         (f32x2){rs_n, rs_n}, (f32x2){cc[j][1], cc[j][3]});
    }
    gelu_n(z, IC<7>());
#pragma unroll
    for (int j = 0; j < 7; ++j) z1d[j] = pack2(z[j][0], z[j][1]);
    asm volatile("" : "+v"(z1d[0]), "+v"(z1d[1]), "+v"(z1d[2]), "+v"(z1d[3]), "+v"(z1d[4]), "+v"(z1d[5]), "+v"(z1d[6]));
    SCHED();
    STAMPT(30);
    if (!kh1) req(IC<WY>(), IC<30 - 2 * WZ>());
    const unsigned fo[1] = {l < 4 ? fz + (unsigned)(c * 32 + 4 * w + l) * 128u : ~0u};
    const unsigned fv = ld4(rs, fo[0]);
    SCHED();
#pragma unroll
    for (int g = TR2; g < TR3; ++g) WSP_TR(g);
    SCHED();
    STAMP(14);
    if (__builtin_amdgcn_ballot_w64(fv == tag) != 15ull) wait_lines<1, 1>(rs, fo, tag);
    STAMPT(23);
#pragma unroll
    for (int f = 0; f < NW; ++f) { LDZ(f); SCHED(); }
  }

#define WO3 (-92)
#define WO2 15
#define TRC TR3
#define VMW(n) asm volatile("s_waitcnt vmcnt(%0)" ::"n"(n) : "memory")
  {
    SCHED();
#pragma unroll
    for (int g = TRC; g < 8; ++g) WSP_TR(g);
    SCHED();
    STAMP(20);
    const int rt = rt1, kh = kh1;
    unsigned rw = (unsigned)(L_RED + w * 512 + l * 8), rr = (unsigned)(L_RED + w * 4096 + l * 8);
    asm volatile("" : "+v"(rw), "+v"(rr));
    const float bs = bflo(bs_raw);
    const unsigned uo = WS_UF + (unsigned)((R * 4 + rt) * 8 + c) * 4096u + (unsigned)kh * 2048u;
    const unsigned ui = WS_UF + (unsigned)((R * 4 + rt) * 8 + 4 * kh) * 4096u;
    unsigned long long hm = 0xffff000000000000ull;
    asm volatile("" : "+s"(hm));
    unsigned l8;
    f32x4 acc2[2][4];
    float rv[8][2];
    u32x4 ud;
    u32x4 P[2][8];
    u32x2 xp[3];
    auto ld_p = [&](int pt, int i) { P[pt][i] = __builtin_amdgcn_raw_buffer_load_b128(rs, l16 + (unsigned)pt * 1024u, ui + (unsigned)i * 2048u, SC1); };
    auto red_put = [&](int j, int r) {
      *(f32x2*)(lds + rw + r * 4096) = (f32x2){acc2[j & 1][r][0], acc2[j & 1][r][1]};
      *(f32x2*)(lds + rw + (r + 4) * 4096) = (f32x2){acc2[j & 1][r][2], acc2[j & 1][r][3]};
    };
    auto red_put1 = [&](int j, int i) { *(volatile LAS float*)(lds + rw + (i >> 2) * 4096 + ((i >> 1) & 1) * 16384 + (i & 1) * 4) = acc2[j & 1][i >> 2][i & 3]; };
    auto red_get = [&](int i) { const f32x2 x = *(const volatile LAS f32x2*)(lds + rr + i * 512); rv[i][0] = x[0]; rv[i][1] = x[1]; };
    float zz[2], gg[2];
    unsigned cs;
    auto sum_rows_u = [&](unsigned v) {
      auto a = __builtin_amdgcn_permlane16_swap(v, v, false, false);
      v = a[0] + a[1];
      auto b = __builtin_amdgcn_permlane32_swap(v, v, false, false);
      return b[0] + b[1];
    };
    auto red_fin = [&](int j, int k) {
      const int e = k & 1, i = k >> 1;
#define VADD(d, x, y) asm volatile("v_add_f32 %0, %1, %2" : "=v"(d) : "v"(x), "v"(y))
      if (i < 4) VADD(rv[2 * i][e], rv[2 * i][e], rv[2 * i + 1][e]);
      if (i == 4 || i == 5) VADD(rv[4 * (i & 1)][e], rv[4 * (i & 1)][e], rv[4 * (i & 1) + 2][e]);
      if (i == 6) VADD(rv[0][e], rv[0][e], bs);
      if (i == 7) VADD(gg[e], rv[0][e], rv[4][e]);
      if (i == 8) { zz[e] = e ? bfhi(z1d[(j + 6) % 7]) : bflo(z1d[(j + 6) % 7]); asm volatile("" : "+v"(zz[e])); }
      if (i == 9) { zz[e] *= gg[e]; asm volatile("" : "+v"(zz[e])); }
      if (k == 20) { ud[j & 3] = pack2(zz[0], zz[1]); asm volatile("" : "+v"(ud[j & 3])); }
      if (k == 21) {
        if (j == 0) asm volatile("v_cndmask_b32 %0, %0, 0, %1" : "+v"(ud[0]) : "s"(hm));
        if (j == 2) asm volatile("v_add3_u32 %0, %1, %2, %3" : "=v"(cs) : "v"(ud[0]), "v"(ud[1]), "v"(ud[2]));
        if (j == 3) {
          cs = tag - sum_rows_u(cs + ud[3]);
          asm volatile("v_cndmask_b32 %0, %0, %1, %2" : "+v"(ud[0]) : "v"(cs), "s"(hm));
        }
        if (j >= 4) { ud[3] = (j == 4 ? tag : ud[3]) - ud[j & 3]; asm volatile("" : "+v"(ud[3])); }
        if (j % 3 == 0 && j) __builtin_amdgcn_raw_buffer_store_b128(ud, rs, l16 + (unsigned)(j / 6) * 1024u, uo, SC1);
      }
    };
#define P1M 4
#define G2S 0
    struct Sch { int wide, lp, prio, lf, b1, b2, wp[16], rp[8], vp[22]; };
    constexpr Sch SC[4] = {
      {0, 2, 8, 1, 18, 29, {0, 1, 2, 3, 4, 5, 6, 7, 8, 9, 10, 11, 12, 13, 14, 15}, {19, 19, 20, 20, 21, 21, 22, 22}, {29, 29, 30, 30, 31, 31, 32, 33, 35, 36, 37, 38, 39, 40, 41, 42, 43, 44, 45, 46, 47, 48}},
      {1, 3, 4, 0, 18, 28, {3, 7, 11, 15, 99, 99, 99, 99, 99, 99, 99, 99, 99, 99, 99, 99}, {20, 20, 21, 21, 22, 22, 24, 24}, {28, 28, 29, 29, 30, 30, 31, 31, 32, 32, 33, 33, 34, 34, 35, 36, 36, 37, 37, 38, 38, 39}},
      {0, 1, 4, 1, 17, 29, {0, 1, 2, 3, 4, 5, 6, 7, 8, 9, 10, 11, 12, 13, 14, 15}, {17, 17, 18, 18, 19, 19, 20, 20}, {29, 29, 30, 30, 31, 31, 32, 34, 35, 36, 38, 39, 40, 42, 43, 44, 45, 46, 47, 48, 48, 49}},
      {0, 2, 0, 1, 17, 25, {0, 1, 2, 3, 4, 5, 6, 7, 8, 9, 10, 11, 12, 13, 14, 15}, {17, 17, 18, 18, 19, 19, 20, 20}, {25, 25, 26, 26, 27, 27, 28, 28, 29, 29, 30, 30, 31, 31, 32, 33, 23, 24, 34, 35, 36, 37}}};
#define JF 4
#define PC0 24
#define P1D 2
#define VL 2
    constexpr Sch S = SC[G2S];
    constexpr Sch S5 = {0, S.lp, S.prio, S.lf, 17, 24, {0, 1, 2, 3, 4, 5, 6, 7, 8, 9, 10, 11, 12, 13, 14, 15}, {17, 17, 18, 18, 19, 19, 20, 20}, {24, 24, 25, 25, 26, 26, 27, 27, 28, 28, 29, 29, 30, 30, 31, 31, 22, 23, 32, 32, 33, 33}};
    constexpr Sch S6 = {1, S.lp, S.prio, S.lf, 5, 12, {99, 99, 99, 3, 99, 99, 99, 99, 99, 99, 99, 99, 99, 99, 99, 99}, {5, 5, 6, 6, 7, 7, 8, 8},
                        {12, 12 + 1 / VL, 12 + 2 / VL, 12 + 3 / VL, 12 + 4 / VL, 12 + 5 / VL, 12 + 6 / VL, 12 + 7 / VL, 12 + 8 / VL, 12 + 9 / VL, 12 + 10 / VL, 12 + 11 / VL, 12 + 12 / VL, 12 + 13 / VL, 12 + 14 / VL, 12 + 15 / VL, 10, 11,
                         12 + 16 / VL, 12 + 17 / VL, 12 + 18 / VL, 12 + 19 / VL}};
    unsigned wb[3], xo;
#define XPM 19
    unsigned xs = WS_X1B + (unsigned)(R * 4 + rt) * 20480u + (unsigned)(5 * c + 2 * kh) * 512u, wl[3], wg = WS_WOUTF + (unsigned)c * WBLK - (unsigned)(WO3 * 1024);
#pragma unroll
    for (int k = 0; k < 3; ++k) wl[k] = (unsigned)(120 + WO3 + (k < 2 || w < 4 ? w + 8 * k : w)) * 1024u;
    asm volatile("" : "+s"(xs), "+s"(wg), "+s"(wl[0]), "+s"(wl[2]));
#define NAF 32
#define NA0 18
    u32x4 af[NAF];
#define LDA(m) af[(m) % NAF] = *(const u32x4*)(lds + wb[WOA(2 * (m)) >> 16] + (WOA(2 * (m)) & 65535))
    unsigned ck[2];
    const unsigned tag4 = tag * 4u;
    auto p_chk = [&](int pt, int pr, int k) {
      u32x4* q = P[pt] + 4 * pr;
      if (k < 2) ck[k & 1] = q[k & 1][0] + q[k & 1][1];
      else if (k < 8) ck[k & 1] = ck[k & 1] + q[(k & 1) + 2 * ((k >> 2) & 1)][k & 2] + q[(k & 1) + 2 * ((k >> 2) & 1)][(k & 2) + 1];
      else if (k == 8) ck[0] = pt ? ck[0] + ck[1] : sum_rows_u(ck[0] + ck[1]);
      if (k < 9) asm volatile("" : "+v"(ck[k & 1]));
      if (k == 9) {
        if (__builtin_expect(__any(ck[0] != tag4), 0)) {
          _Pragma("unroll 1") for (unsigned spins = 0; spins < SPIN_CAP; ++spins) {
            unsigned d = 0u;
#pragma unroll
            for (int i = 0; i < 4; ++i) ld_p(pt, 4 * pr + i);
#pragma unroll
            for (int i = 0; i < 4; ++i) d += q[i][0] + q[i][1] + q[i][2] + q[i][3];
            if (!pt) d = sum_rows_u(d);
            if (!__any(d != tag4)) break;
            __builtin_amdgcn_s_sleep(1);
          }
        }
      }
      if (k >= 10 && !pt) { unsigned x = q[(k - 10) & 3][0]; asm volatile("v_cndmask_b32 %0, %0, 0, %1" : "+v"(x) : "s"(hm)); q[(k - 10) & 3][0] = x; }
      if (k >= 10 && k < 13 && pt) { q[(k - 10) % 3][3] = q[3][(k - 10) % 3]; asm volatile("" : "+v"(q[(k - 10) % 3])); }
    };
    sfor<0, 224>([&](auto M_) __attribute__((always_inline)) {
      constexpr int mm = decltype(M_)::value, j = mm >> 5, m = mm & 31;
      constexpr bool ro = j >= 5 && m >= 16;
      constexpr int t = ro ? 4 + (m & 3) : m >> 2, r = ro ? (m >> 2) & 3 : m & 3, f = 8 * j + t;
      constexpr Sch C = j < JF ? S : j < 6 ? S5 : S6, B = j <= JF ? S : S5;
      if (t == 0) asm volatile("v_mfma_f32_16x16x32_bf16 %0, %1, %2, 0" : "=&v"(acc2[j & 1][r]) : "v"(zw[f % NW]), "v"(wsp[r][t]), "v"(zw[(f | 1) % NW]));
      else asm volatile("v_mfma_f32_16x16x32_bf16 %0, %1, %2, %0" : "+v"(acc2[j & 1][r]) : "v"(zw[f % NW]), "v"(wsp[r][t]), "v"(zw[(f | 1) % NW]));
      SCHED();
      if (S.prio && (m + 31 - S.lp) % (S.prio ? S.prio : 1) == 0) __builtin_amdgcn_s_setprio(1);
      auto ld = [&]() {
        if ((m & 3) == S.lp) {
          if (S.prio && (m + 32 - S.lp) % (S.prio ? S.prio : 1) == 0) __builtin_amdgcn_s_setprio(0);
          constexpr int g = S.lp == 3 ? mm >> 2 : (mm >> 2) - 1;
          if (g >= 0 && g + NW < 56) LDZ(g + NW);
          constexpr int y = g + NW - 56;
          if (y >= 0 && WO2 + y < 15) wo_dma(WO2 + y);
          SCHED();
        }
        if (j == 6 && m >= P1M && m < P1M + 8 * P1D && (m - P1M) % P1D == 0) { ld_p(0, ((m - P1M) / P1D) & 7); SCHED(); }
      };
      if (!S.lf) ld();
      if ((m == C.b1 && j > 0) || m == C.b2) {
        if (j == 6 && m == C.b2 && WO2 < 15) VMW(m > P1M ? (m - P1M + P1D - 1) / P1D : 0);
        asm volatile("" ::: "memory"); __builtin_amdgcn_s_waitcnt(0xC07F); RBAR(); SCHED();
      }
      sfor<0, 16>([&](auto I_) __attribute__((always_inline)) {
        constexpr int i = decltype(I_)::value;
        if (j > 0 && C.wp[i] == m) { if (C.wide) red_put(j + 1, i & 3); else red_put1(j + 1, i); SCHED(); }
        if (j > 0 && i < 8 && C.rp[i & 7] == m) { red_get(i & 7); SCHED(); }
      });
      if (j == 5 && m >= 27 && (m & 1)) { red_put(j, ((m - 27) >> 1) & 3); SCHED(); }
      if (j == 6 && m >= 23 && (m & 3) == 3) { red_put(j, ((m - 23) >> 2) & 3); SCHED(); }
      if (j == 6 && m == XPM) {
        asm volatile("v_lshrrev_b32 %0, 1, %1" : "=v"(l8) : "v"(l16));
        xo = (l8 & 0x180u) + (unsigned)psi((l8 >> 3) & 15) * 8u;
        asm volatile("" : "+v"(xo));
      }
      if (j == 6 && m > XPM && m < XPM + 4) { xp[(m - XPM - 1) % 3] = __builtin_amdgcn_raw_buffer_load_b64(rs, xo + (unsigned)(m - XPM - 1) * 512u, xs, BULK_AUX); SCHED(); }
      if (j == 6 && m == 4) {
#pragma unroll
        for (int i = 0; i < 3; ++i) { wb[i] = (unsigned)(kh * 1024 + i * 65536) + l16; asm volatile("" : "+v"(wb[i])); }
      }
      if (j == 6 && m != 23 && m != 27 && m != 31) {
        constexpr int n = NA0 - 1 - (30 - m) + (m < 27) + (m < 23);
        if (n >= 0) { LDA(n >= 0 ? n : 0); SCHED(); }
      }
      if (j == 6 && m >= PC0 && m < PC0 + 7) { p_chk(0, 0, 2 * (m - PC0)); SCHED(); p_chk(0, 0, 2 * (m - PC0) + 1); SCHED(); }
      if (j == 6 && m == PC0 - 1) { red_fin(6, 16); red_fin(6, 17); SCHED(); }
      sfor<0, 22>([&](auto K_) __attribute__((always_inline)) {
        constexpr int k = decltype(K_)::value;
        if (j > 1 && B.vp[k] == m + 32) { red_fin(j > 1 ? j - 2 : 0, k); SCHED(); }
        if (j > 0 && C.vp[k] == m) { red_fin(j > 0 ? j - 1 : 0, k); SCHED(); }
      });
      if (S.lf) ld();
    });
    STAMP(15);
#define NM0 2
#define GM 19
#define MB 30
    f32x4 acc[5];
    f32x4 xf[3], gf[3];
    unsigned go, oo, eo, ho, tw, tr, o1, ta, tb;
    unsigned s1 = (unsigned)((w ^ 4) * 3072 + 1024), s2 = (unsigned)(L_MISC + 128 * kh), s3 = (unsigned)(w * 3072 + 1024), s4 = s1 - (unsigned)kh * 1024u;
    unsigned os = (unsigned)((R * 64 + tau(rt, 0)) * D_ + 80 * c + 32 * kh) * 2u;
    unsigned long long om = kh ? ~0ull : 0xccccccccccccccccull;
    asm volatile("" : "+s"(s1), "+s"(s2), "+s"(s3), "+s"(s4), "+s"(os), "+s"(om));
#define PIN2(a, b) asm volatile("" : "+v"(a), "+v"(b))
    auto glue = [&](int k) {
      if (k == 0) { ta = l16 >> 6; tb = l16 & 48u; PIN2(ta, tb); }
      if (k == 1) { tr = ta * 112u + tb; oo = ta & 12u; PIN2(tr, oo); }
      if (k == 2) { tr += s1; oo += ta; PIN2(tr, oo); }
      if (k == 3) { oo = oo * 1280u + tb; ta = l16 >> 5; PIN2(oo, ta); }
      if (k == 4) { ta &= 24u; tb = l16 & 0xf0u; PIN2(ta, tb); }
      if (k == 5) { tw = tb * 7u + ta; go = (ta << 1) + s2; PIN2(tw, go); }
      if (k == 6) { tw += s1; asm volatile("v_cndmask_b32 %0, %1, -2.0, %2" : "=v"(o1) : "v"(oo), "s"(om)); asm volatile("" : "+v"(tw)); }
      if (k == 7) { eo = l16 + s3; ho = l16 + s4; PIN2(eo, ho); }
      if (k >= 8 && k < 14) {
        const unsigned x = xp[((k - 8) >> 1) % 3][k & 1];
        float lo = bflo(x), hi = bfhi(x);
        PIN2(lo, hi);
        xf[((k - 8) >> 1) % 3][2 * (k & 1)] = lo; xf[((k - 8) >> 1) % 3][2 * (k & 1) + 1] = hi;
      }
    };
    auto g3 = [&](auto M0_, auto M1_) __attribute__((always_inline)) {
      sfor<decltype(M0_)::value, decltype(M1_)::value>([&](auto M_) __attribute__((always_inline)) {
        constexpr int mi = decltype(M_)::value;
        constexpr int m = mi < 55 ? mi : mi < 64 ? 55 + 5 * ((mi - 55) / 3) + (mi - 55) % 3 : 58 + 5 * ((mi - 64) / 2) + (mi & 1), u2 = m / 5;
        const u32x4 fb = u2 < 8 ? P[0][u2 & 7] : P[1][u2 < 11 ? (u2 - 8) & 3 : (u2 - 7) & 7];
        if (m < 5) asm volatile("v_mfma_f32_16x16x32_bf16 %0, %1, %2, 0" : "=&v"(acc[m % 5]) : "v"(af[m % NAF]), "v"(fb));
        else asm volatile("v_mfma_f32_16x16x32_bf16 %0, %1, %2, %0" : "+v"(acc[m % 5]) : "v"(af[m % NAF]), "v"(fb));
        SCHED();
        if (mi == NM0) RBAR();
        if (mi >= NM0 && mi < NM0 + 3) dma1k(rs, lds + wl[(mi - NM0) % 3], l16, wg + wl[(mi - NM0) % 3]);
        if (mi >= 5 && mi < 19) p_chk(0, 1, mi - 5);
        if (mi >= GM && mi < GM + 14) glue(mi - GM);
        if (mi >= 47 && mi < 54) { p_chk(1, 1, 2 * (mi - 47)); SCHED(); p_chk(1, 1, 2 * (mi - 47) + 1); }
        if (mi >= NM0 && mi < 40 && NA0 + mi - NM0 < 60) LDA(NA0 + mi - NM0);
        if (mi >= 66 && mi < 69) *(f32x4*)(lds + eo + (mi - 66) * 1024) = acc[(mi - 66) % 3];
        SCHED();
      });
    };
    __builtin_amdgcn_s_setprio(3);
    asm volatile("s_nop 7");
    red_put(6, 3);
    SCHED();
    g3(IC<0>(), IC<NM0 / 2>());
    BAR();
#pragma unroll
    for (int i = 0; i < 8; ++i) red_get(i);
    SCHED();
    g3(IC<NM0 / 2>(), IC<NM0>());
#pragma unroll
    for (int k = 0; k < 20; ++k) red_fin(6, k < 16 ? k : k + 2);
    SCHED();
    __builtin_amdgcn_s_setprio(0);
    STAMPT(16);
    g3(IC<NM0>(), IC<MB>());
    STAMPT(22);
    DRAIN();
    RBAR();
    STAMPT(27);
#pragma unroll
    for (int i = 0; i < 8; ++i) { ld_p(1, i); SCHED(); }
    STAMPT(26);
    g3(IC<MB>(), IC<40>());
    STAMPT(24);
#pragma unroll
    for (int m = NA0 + 40 - NM0; m < 70; ++m) LDA(m);
#pragma unroll
    for (int x = 0; x < 3; ++x) gf[x] = *(const f32x4*)(lds + go + x * 64);
    SCHED();
    const rsrc_t ro = __builtin_amdgcn_make_buffer_rsrc((void*)p.out, 0, S_ * D_ * 2, 0x00020000);
    STAMPT(17);
#pragma unroll
    for (int k = 0; k < 14; ++k) p_chk(1, 0, k);
    asm volatile("s_nop 1");
    STAMPT(25);
    g3(IC<40>(), IC<70>());
    STAMPT(18);
    BAR();
    STAMPT(28);
    f32x4 o4[3];
    u32x4 ov[2];
#pragma unroll
    for (int j = 2; j >= 0; --j) o4[j] = *(const volatile LAS f32x4*)(lds + ho + j * 1024);
    asm volatile("" : "+v"(acc[2]), "+v"(acc[3]), "+v"(acc[4]));
    SCHED();
#pragma unroll
    for (int j = 2; j >= 0; --j) {
      const f32x4 v = __builtin_elementwise_fma(gf[j], o4[j] + acc[2 + j], xf[j]);
      *(volatile LAS u32x2*)(lds + tw + j * 32) = (u32x2){pack2(v[0], v[1]), pack2(v[2], v[3])};
      if (j < 2) ov[j] = *(const volatile LAS u32x4*)(lds + tr + j * 32);
    }
    __builtin_amdgcn_raw_buffer_store_b128(ov[1], ro, oo + 32u, os, 0);
    __builtin_amdgcn_raw_buffer_store_b128(ov[0], ro, o1, os, 0);
    if (bid == 0 && w == 5 && l8 == 0u) st4(rs, WS_EPOCH, tag);
  }
  STAMP(19);
}

extern "C" __device__ __attribute__((used, noinline, visibility("hidden"))) void mega_tail() {}

extern "C" void kernel_launch(void* const* d_in, const int* in_sizes, int n_in, void* d_out, int out_size,
                              void* d_ws, size_t ws_size, hipStream_t stream) {
  Params p;
  p.q = (const u16*)d_in[0];
  p.k = (const u16*)d_in[1];
  p.v = (const u16*)d_in[2];
  p.shift_k = (const u16*)d_in[3];
  p.ssm_k = (const u16*)d_in[4];
  p.ssm_D = (const u16*)d_in[5];
  p.w_in = (const u16*)d_in[7];
  p.w_sp = (const u16*)d_in[8];
  p.b_sp = (const u16*)d_in[9];
  p.w_out = (const u16*)d_in[10];
  p.cvec = (const u16*)d_in[11];
  p.w_mod = (const u16*)d_in[12];
  p.out = (u16*)d_out;
  p.ws = (char*)d_ws;
  mega<<<256, 512, 0, stream>>>(p);
}
```

```cpp
#define G2A 2
#include <hip/hip_runtime.h>
#include <stdint.h>

typedef unsigned short u16;
typedef __attribute__((ext_vector_type(8))) short bf16x8;
typedef __attribute__((ext_vector_type(4))) short bf16x4;
typedef __attribute__((ext_vector_type(4))) float f32x4;
typedef __attribute__((ext_vector_type(2))) float f32x2;
typedef __attribute__((ext_vector_type(4))) unsigned int u32x4;
typedef __attribute__((ext_vector_type(2))) unsigned int u32x2;
#define LAS __attribute__((address_space(3)))
typedef __amdgpu_buffer_rsrc_t rsrc_t;
typedef __attribute__((ext_vector_type(4))) short s16x4;

constexpr int S_ = 2048, D_ = 640, F_ = 1728, FH = 864, CB = 108;

constexpr unsigned WS_EPOCH = 0;
constexpr unsigned WS_FWI = 4096;
constexpr unsigned WS_FZ = WS_FWI + 384 * 128;
constexpr unsigned WS_FWO = WS_FZ + 256 * 128;
constexpr unsigned F4S = 896 * 128;
constexpr unsigned WS_CS = WS_FZ + 256 * 128;
constexpr unsigned WS_MOD = 131072;
constexpr unsigned WS_MODP = WS_MOD + 81920;
constexpr unsigned WS_ST1 = 393216;
constexpr unsigned WS_PART = 524288;
constexpr unsigned WS_CSP = WS_PART + 262144;
constexpr unsigned WS_FX = WS_CSP + 655360;
constexpr unsigned WS_X1B = WS_FX + 16 * 4 * 160 * 128;
constexpr unsigned WS_QB = WS_X1B + 2621440;
constexpr unsigned WS_KV3 = WS_QB + 2621440;
constexpr unsigned WS_MODQ = WS_KV3 + 1310720;
constexpr unsigned WBLK = 143360;
constexpr unsigned WS_WINF = WS_MODQ + 245760;
constexpr unsigned WS_WOUTF = WS_WINF + 16 * WBLK;
constexpr unsigned WS_Z2F = WS_WOUTF + 8 * WBLK;
constexpr unsigned WS_UF = WS_Z2F + 8 * 64 * 7168;
constexpr unsigned WS_F4 = WS_UF + 32 * 4 * 8 * 4096;
constexpr unsigned WS_END = WS_F4 + 4 * F4S;
constexpr unsigned WS_STAMP = 200u << 20;

constexpr int G_COPY = 4160;
constexpr int L_U = WBLK;
constexpr int U_CH = (256 + S_) * 2 + 32;
constexpr int L_FLG = L_U + 4 * U_CH;
constexpr int L_RT = 16384;
constexpr int L_CONST = WBLK;
constexpr int L_PST = L_CONST + 2048;
constexpr int L_Z = L_CONST + 5120;
constexpr int L_ST2 = L_Z + 14336;
constexpr int L_MISC = L_ST2 + 512;
constexpr int LDS_TOTAL = 163840;

#define AUX_T 610
#define RT2_T 360
#define CB_T 380
#define W_T 730
#define CS_T 830
#define Y_T 930
#define K_T 460
#ifndef M_T
#define M_T 520
#endif
#define WA_T 580
constexpr int SC1 = 16, NT = 2;
constexpr unsigned SPIN_CAP = 1u << 16;

__device__ __forceinline__ float bflo(unsigned x) { return __uint_as_float(x << 16); }
__device__ __forceinline__ float bfhi(unsigned x) { return __uint_as_float(x & 0xffff0000u); }
__device__ __forceinline__ unsigned f2bf(float x) {
  __bf16 b = (__bf16)x;
  return (unsigned)__builtin_bit_cast(unsigned short, b);
}
__device__ __forceinline__ float rbf(float x) { return bflo(f2bf(x)); }
typedef __attribute__((ext_vector_type(2))) __bf16 bf2_t;
__device__ __forceinline__ unsigned pack2(float lo, float hi) {
  f32x2 v = {lo, hi};
  return __builtin_bit_cast(unsigned, __builtin_convertvector(v, bf2_t));
}
__device__ __forceinline__ f32x2 rbf2(f32x2 v) {
  const unsigned p = __builtin_bit_cast(unsigned, __builtin_convertvector(v, bf2_t));
  return (f32x2){__uint_as_float(p << 16), __uint_as_float(p & 0xffff0000u)};
}
__device__ __forceinline__ f32x4 rbf4(f32x4 v) {
  const f32x2 a = rbf2((f32x2){v[0], v[1]}), b = rbf2((f32x2){v[2], v[3]});
  return (f32x4){a[0], a[1], b[0], b[1]};
}
template <int CTRL>
__device__ __forceinline__ float dpp_f(float v) {
  return __int_as_float(__builtin_amdgcn_update_dpp(0, __float_as_int(v), CTRL, 0xf, 0xf, true));
}
__device__ __forceinline__ float sum8(float v) {
  v += dpp_f<0xB1>(v);
  v += dpp_f<0x4E>(v);
  v += dpp_f<0x141>(v);
  return v;
}
__device__ __forceinline__ float sum16(float v) {
  v = sum8(v);
  v += dpp_f<0x140>(v);
  return v;
}
__device__ __forceinline__ float sum_rows(float v) {
  auto a = __builtin_amdgcn_permlane16_swap(__float_as_uint(v), __float_as_uint(v), false, false);
  v = __uint_as_float(a[0]) + __uint_as_float(a[1]);
  auto b = __builtin_amdgcn_permlane32_swap(__float_as_uint(v), __float_as_uint(v), false, false);
  return __uint_as_float(b[0]) + __uint_as_float(b[1]);
}
#define MFMA16(a, b, c) __builtin_amdgcn_mfma_f32_16x16x32_bf16(__builtin_bit_cast(bf16x8, a), __builtin_bit_cast(bf16x8, b), c, 0, 0, 0)
#define MFMA16K16(a, b, c) __builtin_amdgcn_mfma_f32_16x16x16bf16_1k(__builtin_bit_cast(bf16x4, a), __builtin_bit_cast(bf16x4, b), c, 0, 0, 0)
__device__ __forceinline__ u32x2 trd(const char* p) {
  return __builtin_bit_cast(u32x2, __builtin_amdgcn_ds_read_tr16_b64_v4i16((LAS s16x4*)p));
}
__device__ __forceinline__ u32x4 trfrag(const char* p, int pitch) {
  u32x2 a = trd(p), b = trd(p + 16 * pitch);
  return (u32x4){a[0], a[1], b[0], b[1]};
}
#define BAR() asm volatile("s_waitcnt lgkmcnt(0)\n\ts_barrier" ::: "memory")
#define DRAIN() do { __builtin_amdgcn_s_waitcnt(0x0F70); asm volatile("" ::: "memory"); } while (0)
#define SCHED() __builtin_amdgcn_sched_barrier(0)
template <int N> struct IC { static constexpr int value = N; };
template <int I, int N, class F>
__device__ __forceinline__ void sfor(F&& f) {
  if constexpr (I < N) { f(IC<I>()); sfor<I + 1, N>(f); }
}

__device__ __forceinline__ u32x4 ld16(rsrc_t r, unsigned off) { return __builtin_amdgcn_raw_buffer_load_b128(r, off, 0, SC1); }
__device__ __forceinline__ u32x2 ld8(rsrc_t r, unsigned off) { return __builtin_amdgcn_raw_buffer_load_b64(r, off, 0, SC1); }
__device__ __forceinline__ unsigned ld4(rsrc_t r, unsigned off) { return __builtin_amdgcn_raw_buffer_load_b32(r, off, 0, SC1); }
#define ST_AUX 16
__device__ __forceinline__ void st16(rsrc_t r, unsigned off, u32x4 v) { __builtin_amdgcn_raw_buffer_store_b128(v, r, off, 0, ST_AUX); }
__device__ __forceinline__ void st8(rsrc_t r, unsigned off, u32x2 v) { __builtin_amdgcn_raw_buffer_store_b64(v, r, off, 0, SC1); }
__device__ __forceinline__ void st_flag4(rsrc_t r, unsigned off, unsigned tag, int l) {
  if (l < 32) st16(r, WS_F4 - WS_FWI + off + (unsigned)(l >> 3) * F4S + (unsigned)(l & 7) * 16u, (u32x4){tag, tag, tag, tag});
}
__device__ __forceinline__ void st4(rsrc_t r, unsigned off, unsigned v) { __builtin_amdgcn_raw_buffer_store_b32(v, r, off, 0, SC1); }
#define BULK_AUX 16
__device__ __forceinline__ u32x4 ld16p(rsrc_t r, unsigned off) { return __builtin_amdgcn_raw_buffer_load_b128(r, off, 0, BULK_AUX); }
__device__ __forceinline__ u32x2 ld8p(rsrc_t r, unsigned off) { return __builtin_amdgcn_raw_buffer_load_b64(r, off, 0, BULK_AUX); }
__device__ __forceinline__ void dma1k(rsrc_t r, char* lds_dst, unsigned l16, unsigned soff) {
  __builtin_amdgcn_raw_ptr_buffer_load_lds(r, (LAS void*)lds_dst, 16, l16, soff, 0, BULK_AUX);
}

#define STAMP(i)
#define STAMPF(i)
#define STAMPT(i) STAMP(i)

template <int N, int SLP>
__device__ __forceinline__ void wait_lines(rsrc_t r, const unsigned (&off)[N], unsigned tag) {
  bool need[N];
#pragma unroll
  for (int i = 0; i < N; ++i) need[i] = off[i] != ~0u;
  _Pragma("unroll 1") for (unsigned spins = 0; spins < SPIN_CAP; ++spins) {
    unsigned v[N];
#pragma unroll
    for (int i = 0; i < N; ++i) {
      v[i] = tag;
      if (need[i]) v[i] = ld4(r, off[i]);
    }
    bool any = false;
#pragma unroll
    for (int i = 0; i < N; ++i) { need[i] = v[i] != tag; any = any || need[i]; }
    if (!__any(any)) break;
    __builtin_amdgcn_s_sleep(SLP);
  }
  asm volatile("" ::: "memory");
}

__device__ __forceinline__ int tau(int rt, int n) { return 32 * (rt >> 1) + 8 * (n >> 2) + 4 * (rt & 1) + (n & 3); }
__device__ __forceinline__ int psi(int n) { return 2 * (n & 7) + (n >> 3); }
#define QX_CM(d, s0, s1, P) "v_cndmask_b32_dpp " d ", " s0 ", " s1 ", vcc quad_perm:" P " row_mask:0xf bank_mask:0xf\n\t"
#define QX_BODY(M, P) \
  asm("s_mov_b32 vcc_lo, " M "\n\ts_mov_b32 vcc_hi, " M "\n\t" \
      QX_CM("%0", "%12", "%8", P) "v_xor_b32 %4, %8, %0\n\t" QX_CM("%1", "%13", "%9", P) "v_xor_b32 %5, %9, %1\n\t" \
      QX_CM("%2", "%14", "%10", P) "v_xor_b32 %6, %10, %2\n\t" QX_CM("%3", "%15", "%11", P) "v_xor_b32 %7, %11, %3" \
      : "=&v"(t[0]), "=&v"(t[1]), "=&v"(t[2]), "=&v"(t[3]), "=&v"(u[0]), "=&v"(u[1]), "=&v"(u[2]), "=&v"(u[3]) \
      : "v"(a[0]), "v"(a[1]), "v"(a[2]), "v"(a[3]), "v"(b[0]), "v"(b[1]), "v"(b[2]), "v"(b[3]) : "vcc")
template <int BIT>
__device__ __forceinline__ void quad_xchg3(u32x4& a, u32x4& b) {
  unsigned t[4], u[4];
  if (BIT == 0) QX_BODY("0x55555555", "[1,0,3,2]");
  else QX_BODY("0x33333333", "[2,3,0,1]");
  a = (u32x4){t[0], t[1], t[2], t[3]};
#pragma unroll
  for (int d = 0; d < 4; ++d) b[d] ^= (unsigned)__builtin_amdgcn_update_dpp(0, (int)u[d], BIT == 0 ? 0xB1 : 0x4E, 0xf, 0xf, true);
}
__device__ __forceinline__ void quad_transpose(u32x4& x0, u32x4& x1, u32x4& x2, u32x4& x3) {
  quad_xchg3<0>(x0, x1);
  quad_xchg3<0>(x2, x3);
  quad_xchg3<1>(x0, x2);
  quad_xchg3<1>(x1, x3);
  asm volatile("" : "+v"(x0), "+v"(x1), "+v"(x2), "+v"(x3));
}

struct Params {
  const u16 *q, *k, *v, *shift_k, *ssm_k, *ssm_D, *w_in, *w_sp, *b_sp, *w_out, *cvec, *w_mod;
  u16* out;
  char* ws;
};

__global__ __launch_bounds__(512, 2) void mega(Params p) {
  __shared__ __attribute__((aligned(16))) char lds[LDS_TOTAL];
  {
    unsigned long long pc0;
    unsigned vt;
    asm volatile("v_readfirstlane_b32 vcc_lo, %3\n\ts_cmpk_lt_u32 %2, 192\n\ts_cbranch_scc1 4f\n\ts_cmpk_lt_u32 vcc_lo, 64\n\ts_cbranch_scc0 4f\n\t"
                 "s_lshr_b32 vcc_hi, %2, 3\n\ts_sub_u32 vcc_hi, vcc_hi, 24\n\ts_lshl_b32 vcc_hi, vcc_hi, 13\n\t"
                 "v_lshlrev_b32 %1, 7, %3\n\tv_add_u32 %1, vcc_hi, %1\n\ts_getpc_b64 %0\n.Lmega_here:\n\t"
                 "v_min_u32 %1, mega_tail-.Lmega_here-4, %1\n\ts_movk_i32 m0, 0x2000\n\ts_nop 1\n\tglobal_load_lds_dword %1, %0\n4:\n\t"
                 "s_cmpk_lt_u32 %2, 160\n\ts_cbranch_scc0 1f\n\ts_cmpk_lt_u32 vcc_lo, 256\n\ts_cbranch_scc1 3f\n\t"
                 "s_call_b64 vcc, .Ltouch1\n\ts_branch 3f\n1:\n\t"
                 "s_cmpk_lt_u32 vcc_lo, 256\n\ts_cbranch_scc1 2f\n\ts_cmpk_lt_u32 %2, 224\n\ts_cbranch_scc0 2f\n\ts_call_b64 vcc, .Ltouch3\n\ts_branch 3f\n"
                 "2:\n\ts_call_b64 vcc, .Ltouch2\n3:"
                 : "=&s"(pc0), "=&v"(vt) : "s"(blockIdx.x), "v"(threadIdx.x) : "vcc", "scc", "memory");
  }
  SCHED();
  const int tid = threadIdx.x, l = tid & 63;
  const int w = __builtin_amdgcn_readfirstlane(tid >> 6);
  const int bid = blockIdx.x, xcd = bid & 7, idx = bid >> 3;
#define TOUCH_AT(n) asm volatile("s_branch 1f\n.Ltouch" #n ":\n\ts_setpc_b64 vcc\n1:")
  char* const ws = p.ws;
  asm volatile("" ::"s"(p.q), "s"(p.k), "s"(p.v), "s"(p.shift_k), "s"(p.ssm_k), "s"(p.ssm_D), "s"(p.w_in), "s"(p.w_sp), "s"(p.b_sp),
               "s"(p.w_out), "s"(p.cvec), "s"(p.w_mod), "s"(p.out), "s"(p.ws));
  unsigned epoch, tag, t_start;
  unsigned long long t64;
  asm volatile("s_load_dword %0, %2, 0x0\n\ts_memrealtime %1" : "=&s"(epoch), "=&s"(t64) : "s"(ws));
#define TAG_READY() do { SCHED(); \
    asm volatile("s_waitcnt lgkmcnt(0)\n\ts_mul_i32 %0, %2, 0x9e3779b1\n\ts_add_i32 %0, %0, 0x9e3779b9\n\ts_mov_b64 %1, %3" \
                 : "=&s"(tag), "=&s"(t64r) : "s"(epoch), "s"(t64) : "scc"); t_start = (unsigned)t64r; SCHED(); } while (0)
  unsigned long long t64r;
  const rsrc_t rs = __builtin_amdgcn_make_buffer_rsrc(ws, 0, WS_END, 0x00020000);
  const unsigned l16 = (unsigned)l * 16u;
  STAMP(0);
  const bool is_conv = idx < 20;
  const int quad = xcd * 20 + idx;
  const int R = is_conv ? (xcd & 3) * 5 + (idx >> 2) : (xcd & 3) * 3 + 15 + (idx >> 2);
  const int c = (xcd >> 2) * 4 + (idx & 3);
  const unsigned fwi = WS_F4 + (unsigned)(xcd & 3) * F4S, fz = fwi + (WS_FZ - WS_FWI);
  const int n16 = l & 15, h4 = l >> 4;
  u32x4 wsp[4][8];
#define ROTC 2
#define WAUX 0
  const int rotg = ((idx >> 4) ^ ((ROTC & 2) ? idx >> 1 : 0)) & 1, rotb = (idx >> 2) & 3, rotr = (ROTC & 1) ? (idx & 1) * 2 : 0;
  const rsrc_t rsp = __builtin_amdgcn_make_buffer_rsrc((void*)p.w_sp, 0, S_ * S_ * 2, 0x00020000);
  const unsigned wsp_vo = (unsigned)(((l >> 2) & 3) * 8 * 4096 + h4 * 64 + ((l + rotb) & 3) * 16);
#define LDW(k) LDW_(k)
#define WSP_TR(g) WSP_TR_(g)
#define WROW R
#define LDW_(k) wsp[((k) >> 2) & 3][((k) >> 4) * 4 + ((k) & 3)] = __builtin_amdgcn_raw_buffer_load_b128(rsp, wsp_vo, \
    (unsigned)((WROW * 64 + 16 * ((((k) >> 2) & 2) ^ rotr) + 4 * (((k) >> 2) & 1) + ((k) & 3)) * 4096 + w * 512 + ((((k) >> 4) ^ rotg) * 256)), WAUX)
#define WSP_TR_(g) quad_transpose(wsp[(g) & 3][((g) >> 2) * 4], wsp[(g) & 3][((g) >> 2) * 4 + 1], wsp[(g) & 3][((g) >> 2) * 4 + 2], wsp[(g) & 3][((g) >> 2) * 4 + 3])
  const rsrc_t rwi = __builtin_amdgcn_make_buffer_rsrc((void*)p.w_in, 0, D_ * F_ * 2, 0x00020000);
  const rsrc_t rwo = __builtin_amdgcn_make_buffer_rsrc((void*)p.w_out, 0, FH * D_ * 2, 0x00020000);
  const rsrc_t rq = __builtin_amdgcn_make_buffer_rsrc((void*)p.q, 0, S_ * D_ * 2, 0x00020000);
  auto hold = [&](unsigned tt) { while ((unsigned)__builtin_amdgcn_s_memrealtime() - t_start < tt) __builtin_amdgcn_s_sleep(2); };
  volatile LAS unsigned* lf = (volatile LAS unsigned*)(lds + L_FLG);
  auto lf_set = [&](int i) { asm volatile("s_waitcnt lgkmcnt(0)" ::: "memory"); if (l == 0) lf[i] = tag; };
  auto lf_wait = [&](int i0, int n) {
    _Pragma("unroll 1") for (unsigned spins = 0; spins < (SPIN_CAP << 4); ++spins) {
      const unsigned v = lf[i0 + (l & (n - 1))];
      if (__all(v == tag)) break;
      __builtin_amdgcn_s_sleep(1);
    }
    asm volatile("" ::: "memory");
  };
  auto xflag = [&](int s) {
    st16(rs, WS_FX + (unsigned)(((4 * s + (w & 3)) * 4 + ((l >> 3) & 3)) * 160 + quad) * 128u + (unsigned)(l & 7) * 16u, (u32x4){tag, tag, tag, tag});
    SCHED();
  };
  auto load_b2 = [&]() {
#pragma unroll
    for (int k = 0; k < 35; ++k) {
      int i = k + (idx >> 2) * 4;
      i = (i >= 35 ? i - 35 : i) + 35 * w;
      dma1k(rs, lds + i * 1024, l16, WS_WINF + (unsigned)(8 + c) * WBLK + (unsigned)i * 1024u);
    }
  };
  char* const wo_st = lds + 122880;
  auto wo_ld = [&]() {
    const int U = R < 28 ? R : 0, u2 = U >> 1, fi = (u2 + 1) % 3;
    const int c0 = 4 * (U & 1) + (u2 < 8 ? u2 >> 1 : u2 < 11 ? 0 : 2);
    const int b0 = u2 < 8 ? CB * c0 + 2 * (u2 & 1) + 96 : CB * (c0 + (fi >> 1)) + 48 + 2 * (fi & 1), d1 = u2 < 8 ? -96 : 16, d2 = d1 + 16;
    const int b3 = u2 < 8 ? b0 - 64 : CB * (c0 + 1) + 50 + 16 * fi;
#pragma unroll
    for (int r = 0; r < 5; ++r) {
      const int g = r * 64 + l, rho = g / 10;
      const int fb = (rho & 16) ? ((rho & 2) ? b3 : b0 + d2) : ((rho & 2) ? b0 + d1 : b0);
      __builtin_amdgcn_raw_ptr_buffer_load_lds(rwo, (LAS void*)(wo_st + r * 1024), 16, (unsigned)((fb + (rho & 13)) * (D_ * 2) + (g % 10) * 16 + c * 160), 0, 0, 0);
    }
  };
  auto rt_ld = [&](int i0, char* st) {
    const int cb = (i0 < 160 ? 8 : 0) + (i0 & 7), Q = (i0 < 160 ? i0 : i0 - 160) >> 3;
#pragma unroll
    for (int r = 0; r < 7; ++r) {
      const int g = r * 64 + l;
      __builtin_amdgcn_raw_ptr_buffer_load_lds(rwi, (LAS void*)(st + r * 1024), 16, (unsigned)((g / 14) * (F_ * 2) + (g % 14) * 16 + Q * 32 * F_ * 2 + cb * CB * 2),
                                               0, 0, NT);
    }
  };
  auto rt_job = [&](int i0, char* st) {
    const int cb = (i0 < 160 ? 8 : 0) + (i0 & 7), Q = (i0 < 160 ? i0 : i0 - 160) >> 3;
    const int col = 640 * (l >> 5) + 32 * Q + (l & 31);
    const unsigned po = WS_MODP + (unsigned)col * 8u;
    u32x2 pp8[8];
    hold(CB_T);
    _Pragma("unroll 1") for (unsigned spins = 0; spins < SPIN_CAP; ++spins) {
      unsigned dd = 0u;
#pragma unroll
      for (int i = 0; i < 8; ++i) pp8[i] = ld8(rs, po + (unsigned)i * 10240u);
#pragma unroll
      for (int i = 0; i < 8; ++i) dd |= pp8[i][0] ^ tag;
      if (__all(dd == 0u)) break;
      __builtin_amdgcn_s_sleep(4);
    }
    STAMPF(9);
    {
      float t = 0.f;
#pragma unroll
      for (int i = 0; i < 8; ++i) t += __uint_as_float(pp8[i][1]);
      if (l >= 32) *(float*)(st + 7168 + (l - 32) * 4) = rbf(1.0f + rbf(t));
      else *(u16*)(st + 7296 + l * 2) = (u16)f2bf(t);
    }
    DRAIN();
    asm volatile("s_waitcnt lgkmcnt(0)" ::: "memory");
    const f32x4 ca = *(const f32x4*)(st + 7168 + h4 * 16), cc = *(const f32x4*)(st + 7168 + 64 + h4 * 16);
    const char* bp = st + (4 * h4 + (n16 >> 2)) * 224 + (n16 & 3) * 8;
    u32x4 x[7], o[7];
#pragma unroll
    for (int j = 0; j < 7; ++j) {
      x[j] = trfrag(bp + j * 32, 224);
      o[j] = (u32x4){pack2(bflo(x[j][0]) * ca[0], bfhi(x[j][0]) * ca[1]), pack2(bflo(x[j][1]) * ca[2], bfhi(x[j][1]) * ca[3]),
                     pack2(bflo(x[j][2]) * cc[0], bfhi(x[j][2]) * cc[1]), pack2(bflo(x[j][3]) * cc[2], bfhi(x[j][3]) * cc[3])};
      if (j == 6 && n16 == 12) o[j] = (u32x4){0x3F803F80u, 0x3F803F80u, 0x3F803F80u, 0x3F803F80u};
      st16(rs, WS_WINF + (unsigned)cb * WBLK + (unsigned)(Q * 7 + j) * 1024u + l16, o[j]);
    }
    STAMPF(11);
    const u32x2 e0 = *(const u32x2*)(st + 7296 + h4 * 8), e1 = *(const u32x2*)(st + 7328 + h4 * 8);
    f32x4 d[2] = {{0.f, 0.f, 0.f, 0.f}, {0.f, 0.f, 0.f, 0.f}};
#pragma unroll
    for (int j = 0; j < 7; ++j) {
      const unsigned one = n16 == 4 * (j & 3) ? 0x3F803F80u : 0u;
      const bool sr = n16 == 4 * (j & 3) + 1;
      d[j >> 2] = MFMA16(((u32x4){one, one, one, one}), o[j], d[j >> 2]);
      d[j >> 2] = MFMA16(((u32x4){sr ? e0[0] : 0u, sr ? e0[1] : 0u, sr ? e1[0] : 0u, sr ? e1[1] : 0u}), x[j], d[j >> 2]);
    }
#pragma unroll
    for (int m = 0; m < 2; ++m)
      st16(rs, WS_CSP + (unsigned)((cb * 20 + Q) * 2 + m) * 1024u + l16, (u32x4){tag, __float_as_uint(d[m][0]), tag, __float_as_uint(d[m][1])});
    STAMPF(13);
    asm volatile("s_waitcnt vmcnt(2)" ::: "memory");
    st_flag4(rs, WS_FWI + (unsigned)(cb * 20 + Q) * 128u, tag, l);
  };

  auto red8 = [&](float v) { v += dpp_f<0x128>(v); return sum_rows(v); };
  const rsrc_t rwm = __builtin_amdgcn_make_buffer_rsrc((void*)p.w_mod, 0, D_ * 3840 * 2, 0x00020000);
  auto gemv_load = [&](u32x4* pre, int blk, int sl, int np) {
    pre[10][0] = *(const unsigned*)(p.cvec + 8 * np * sl + 2 * (l < 4 * np ? l : 0));
#pragma unroll
    for (int pp = 0; pp < np; ++pp)
      pre[pp] = __builtin_amdgcn_raw_buffer_load_b128(rwm, (unsigned)((l >> 3) * 7680 + (l & 7) * 16), (unsigned)((8 * np * sl + pp * 8) * 7680 + blk * 128), NT);
  };
  auto gemv_fin = [&](float* acc, const u32x4* pre, int np, char* scr) {
    const unsigned cv2 = pre[10][0];
    unsigned* svt = (unsigned*)scr + w * 96;
#pragma unroll
    for (int e = 0; e < 2; ++e) {
      const int row = 2 * l + e;
      const float cv = e ? bfhi(cv2) : bflo(cv2);
      if (l < 4 * np) svt[(row & 7) * 12 + (row >> 3)] = __float_as_uint(rbf(cv * __builtin_amdgcn_rcpf(1.0f + __expf(-cv))));
    }
    asm volatile("s_waitcnt lgkmcnt(0)" ::: "memory");
    u32x4 sv[3];
#pragma unroll
    for (int i = 0; i < (np + 3) / 4; ++i) sv[i] = *(const u32x4*)(svt + (l >> 3) * 12 + 4 * i);
    SCHED();
#pragma unroll
    for (int e = 0; e < 8; ++e) acc[e] = 0.f;
#pragma unroll
    for (int pp = 0; pp < np; ++pp) {
      const unsigned sh = sv[pp >> 2][pp & 3], sl2 = sh >> 16;
#pragma unroll
      for (int e = 0; e < 4; ++e) {
        const unsigned x = pre[pp][e];
        acc[2 * e] = __builtin_amdgcn_fdot2_f32_bf16(__builtin_bit_cast(bf2_t, x), __builtin_bit_cast(bf2_t, sl2), acc[2 * e], false);
        acc[2 * e + 1] = __builtin_amdgcn_fdot2_f32_bf16(__builtin_bit_cast(bf2_t, x), __builtin_bit_cast(bf2_t, sh), acc[2 * e + 1], false);
      }
    }
#pragma unroll
    for (int e = 0; e < 8; ++e) acc[e] = red8(acc[e]);
  };
  auto gemv_put = [&](const float* acc, unsigned off) {
    float v = acc[0];
#pragma unroll
    for (int e = 1; e < 8; ++e) { v = (l >> 3) == e ? acc[e] : v; asm("" : "+v"(v)); }
    st8(rs, off + (unsigned)l * 8u, (u32x2){tag, (unsigned)__builtin_amdgcn_ds_bpermute(((l & 7) * 8 + (l >> 3)) * 4, (int)__float_as_uint(v))});
  };
  typedef const __attribute__((address_space(4))) unsigned* cptr_t;

  if (__builtin_expect(is_conv, 1)) {
    if (w >= 4) {
      TOUCH_AT(1);
      const int pw = w - 4;
      const rsrc_t ra = __builtin_amdgcn_make_buffer_rsrc((void*)((idx & 2) ? p.v : p.k), 0, S_ * D_ * 2, 0x00020000);
      const rsrc_t rb = __builtin_amdgcn_make_buffer_rsrc((void*)((idx & 2) ? p.k : p.v), 0, S_ * D_ * 2, 0x00020000);
      const rsrc_t rk = __builtin_amdgcn_make_buffer_rsrc((void*)p.k, 0, S_ * D_ * 2, 0x00020000);
      const unsigned vo = (unsigned)((128 * pw + 2 * l) * (D_ * 2));
      const unsigned so0 = (unsigned)((idx & 1) * (D_ * 2) + quad * 8), so1 = (unsigned)((1 - (idx & 1)) * (D_ * 2) + quad * 8);
      u32x2 g[3][4], kh;
      auto gl = [&](int s, int j0, int j1) {
        __builtin_amdgcn_s_setprio(0);
#pragma unroll
        for (int j = j0; j < j1; ++j) {
          g[s][j] = __builtin_amdgcn_raw_buffer_load_b64(j < 2 ? ra : rb, vo, ((j & 1) ? so1 : so0) + (unsigned)(s * 512 * D_ * 2), 0);
          SCHED();
        }
        __builtin_amdgcn_s_setprio(3);
      };
      const cptr_t sp = (cptr_t)(p.shift_k + quad * 16);
      unsigned spv[8];
#pragma unroll
      for (int i = 0; i < 8; ++i) spv[i] = sp[i];
      __builtin_amdgcn_s_sleep(3);
      gl(0, 0, 4);
      {
        const int row = 512 * (l >> 2) + 128 * pw - 3 + (l & 3);
        kh = __builtin_amdgcn_raw_buffer_load_b64(rk, (l >= 12 || (l & 3) == 3 || row < 0) ? 0x80000000u : (unsigned)(row * D_ * 2 + quad * 8), 0, 0);
      }
      TAG_READY();
      f32x2 wt[2][4];
#pragma unroll
      for (int d = 0; d < 2; ++d) {
        wt[d][0] = (f32x2){bflo(spv[4 * d]), bflo(spv[4 * d + 2])}; wt[d][1] = (f32x2){bfhi(spv[4 * d]), bfhi(spv[4 * d + 2])};
        wt[d][2] = (f32x2){bflo(spv[4 * d + 1]), bflo(spv[4 * d + 3])}; wt[d][3] = (f32x2){bfhi(spv[4 * d + 1]), bfhi(spv[4 * d + 3])};
        asm volatile("" : "+s"(wt[d][0]), "+s"(wt[d][1]), "+s"(wt[d][2]), "+s"(wt[d][3]));
      }
      SCHED();
      {
        const int j = idx * 4 + pw;
        const unsigned o = (unsigned)((512 + 64 * (j >> 2) + l) * (D_ * 2) + (((160 * xcd) >> 7) + ((j >> 1) & 1)) * 128);
        __builtin_amdgcn_raw_ptr_buffer_load_lds((j & 1) ? ra : rb, (LAS void*)(lds + 98304), 4, j < 64 ? o : 0x80000000u, 0, 0, 0);
      }
      SCHED();
      gl(1, 0, 4);
      STAMP(21);
      char* up = lds + L_U + 512 + 256 * pw + (l >> 2) * 16 + (3 - (l & 3)) * 4;
      auto kv = [&](auto S__, auto N__) {
        constexpr int s = decltype(S__)::value;
        u32x2* x = g[s];
        SCHED();
        asm volatile("s_waitcnt vmcnt(%5)" : "+v"(x[0]), "+v"(x[1]), "+v"(x[2]), "+v"(x[3]), "+v"(kh) : "n"(decltype(N__)::value));
        if (s == 2) STAMP(27);
        if (idx & 1) { asm volatile(""); const u32x2 t = x[0], u = x[2]; x[0] = x[1]; x[1] = t; x[2] = x[3]; x[3] = u; }
        asm volatile("" : "+v"(x[0]), "+v"(x[1]), "+v"(x[2]), "+v"(x[3]));
        if (idx & 2) { asm volatile(""); const u32x2 t = x[0], u = x[1]; x[0] = x[2]; x[1] = x[3]; x[2] = t; x[3] = u; }
        asm volatile("" : "+v"(x[0]), "+v"(x[1]), "+v"(x[2]), "+v"(x[3]));
        f32x2 kf[2][5], o[2][2];
#pragma unroll
        for (int d = 0; d < 2; ++d) {
          unsigned kk[5];
          kk[3] = x[0][d]; kk[4] = x[1][d];
          kk[2] = __builtin_amdgcn_update_dpp((unsigned)__builtin_amdgcn_readlane(kh[d], 4 * s + 2), kk[4], 0x138, 0xf, 0xf, false);
          kk[1] = __builtin_amdgcn_update_dpp((unsigned)__builtin_amdgcn_readlane(kh[d], 4 * s + 1), kk[3], 0x138, 0xf, 0xf, false);
          kk[0] = __builtin_amdgcn_update_dpp((unsigned)__builtin_amdgcn_readlane(kh[d], 4 * s), kk[2], 0x138, 0xf, 0xf, false);
#pragma unroll
          for (int j = 0; j < 5; ++j) kf[d][j] = (f32x2){bflo(kk[j]), bfhi(kk[j])};
        }
#pragma unroll
        for (int t = 0; t < 5; ++t) {
#pragma unroll
          for (int i = 0; i < 4; ++i) {
            const int d = i >> 1, r = i & 1;
            if (t == 4) o[d][r] *= (f32x2){bflo(x[2 + r][d]), bfhi(x[2 + r][d])};
            else if (t == 0) o[d][r] = kf[d][r + 3] * wt[d][0];
            else o[d][r] = __builtin_elementwise_fma(kf[d][r + 3 - t], wt[d][t], o[d][r]);
          }
          asm volatile("" : "+v"(o[0][0]), "+v"(o[0][1]), "+v"(o[1][0]), "+v"(o[1][1]));
        }
#pragma unroll
        for (int i = 0; i < 4; ++i) *(unsigned*)(up + i * U_CH + s * 1024) = pack2(o[i >> 1][1][i & 1], o[i >> 1][0][i & 1]);
        asm volatile("" ::: "memory");
        if (l == 0) lf[4 * s + pw] = tag;
        SCHED();
      };
      const char* yr = lds + L_U + (n16 >> 2) * U_CH + (128 * pw + 32 * h4 + 8 * (n16 & 1) + 4 * ((n16 >> 1) & 1)) * 2;
      const unsigned yo = (unsigned)(8 * pw + (l >> 3)) * 20480u + (unsigned)quad * 128u + (unsigned)(l & 7) * 16u;
      const unsigned so = WS_ST1 + (unsigned)xcd * 16384u + (unsigned)pw * 1024u + l16;
      const unsigned mo = WS_MODQ + (unsigned)((n16 < 12 ? n16 >> 2 : 0) * D_ + quad * 4 + (l & 3)) * 8u + (unsigned)h4 * 40960u;
      u32x4 sv[4], qv[4];
      u32x2 mv[2];
      auto ld_e = [&]() {
#pragma unroll
        for (int s = 0; s < 4; ++s) sv[s] = ld16(rs, so + (unsigned)s * 4096u);
        SCHED();
      };
      auto s_ok = [&]() {
        unsigned d = 0u;
#pragma unroll
        for (int s = 0; s < 4; ++s) d |= (sv[s][1] ^ tag) | (sv[s][3] ^ tag);
        return __all(d == 0u);
      };
      kv(IC<0>(), IC<5>());
      STAMP(23);
#define EPOS 0
      gl(2, 0, G2A);
      kv(IC<1>(), IC<G2A>());
      gl(2, G2A, 4);
      STAMPF(9);
      u32x4 k3[2];
      const unsigned k3o = WS_KV3 + (unsigned)(16 * pw + (l >> 2)) * 20480u + (unsigned)(((l >> 1) & 1) * 10240 + (2 * quad + (l & 1)) * 16);
      if (EPOS == 2) kv(IC<2>(), IC<0>());
      hold(K_T);
      k3[0] = ld16(rs, k3o); k3[1] = ld16(rs, k3o + 5120u);
      SCHED();
      if (EPOS != 1) ld_e();
      if (EPOS != 2) kv(IC<2>(), IC<EPOS ? 2 : 6>());
      if (EPOS == 1) ld_e();
      STAMPF(11);
      auto k_ok = [&]() { return __all(((k3[0][1] ^ tag) | (k3[0][3] ^ tag) | (k3[1][1] ^ tag) | (k3[1][3] ^ tag)) == 0u); };
      asm volatile("" : "+v"(k3[0]), "+v"(k3[1]));
      if (!k_ok()) {
        _Pragma("unroll 1") for (unsigned spins = 0; spins < SPIN_CAP; ++spins) {
          __builtin_amdgcn_s_sleep(4);
          k3[0] = ld16(rs, k3o); k3[1] = ld16(rs, k3o + 5120u);
          if (k_ok()) break;
        }
      }
      *(u32x4*)(lds + L_U + (2 * (l & 1) + ((l >> 1) & 1)) * U_CH + 512 + (1536 + 128 * pw + 8 * (l >> 2)) * 2) = (u32x4){k3[0][0], k3[0][2], k3[1][0], k3[1][2]};
      asm volatile("" ::: "memory");
      if (l == 0) lf[12 + pw] = tag;
      __builtin_amdgcn_s_setprio(3);
      STAMPF(18);
      asm volatile("" : "+v"(sv[0]), "+v"(sv[1]), "+v"(sv[2]), "+v"(sv[3]));
      if (!s_ok()) {
        _Pragma("unroll 1") for (unsigned spins = 0; spins < SPIN_CAP; ++spins) {
          __builtin_amdgcn_s_sleep(4);
          ld_e();
          if (s_ok()) break;
        }
      }
      asm volatile("" ::: "memory");
      SCHED();
      STAMPF(17);
#pragma unroll
      for (int s = 0; s < 4; ++s) qv[s] = ld16p(rs, WS_QB + yo + (unsigned)s * (32u * 20480u));
      hold(M_T);
      mv[0] = ld8(rs, mo); mv[1] = ld8(rs, mo + 20480u);
      SCHED();
      STAMP(2);
      u32x2 yv[4][2];
      auto ld_y = [&](int s) {
#pragma unroll
        for (int e = 0; e < 2; ++e) yv[s][e] = trd(yr + 1024 * s + 32 * e);
      };
      lf_wait(20, 4);
      ld_y(0); ld_y(1);
      SCHED();
      asm volatile("" : "+v"(mv[0]), "+v"(mv[1]), "+v"(qv[0]), "+v"(qv[1]), "+v"(qv[2]), "+v"(qv[3]));
      if (!__all(((mv[0][0] ^ tag) | (mv[1][0] ^ tag)) == 0u)) {
        _Pragma("unroll 1") for (unsigned spins = 0; spins < SPIN_CAP; ++spins) {
          __builtin_amdgcn_s_sleep(4);
          mv[0] = ld8(rs, mo); mv[1] = ld8(rs, mo + 20480u);
          if (__all(((mv[0][0] ^ tag) | (mv[1][0] ^ tag)) == 0u)) break;
        }
      }
      STAMPF(13);
      f32x4 al, be;
      {
        const float mgf = rbf(sum_rows(__uint_as_float(mv[0][1]) + __uint_as_float(mv[1][1])));
        auto rl = [&](int i) { return __uint_as_float(__builtin_amdgcn_readlane(__float_as_uint(mgf), i)); };
        const f32x4 sa = {rl(0), rl(1), rl(2), rl(3)}, ca = {rl(4), rl(5), rl(6), rl(7)}, ga = {rl(8), rl(9), rl(10), rl(11)};
        al = rbf4(ca + 1.0f) * ga;
        be = sa * ga;
      }
      f32x4 qf[2][2], P[2][2];
      auto xpre = [&](int s) {
#pragma unroll
        for (int e = 0; e < 2; ++e) {
          const float r1 = __builtin_amdgcn_rcpf(bfhi(sv[s][2 * e])), m1 = -bflo(sv[s][2 * e]) * r1;
          const f32x4 qq = {bflo(qv[s][2 * e]), bfhi(qv[s][2 * e]), bflo(qv[s][2 * e + 1]), bfhi(qv[s][2 * e + 1])};
          qf[s & 1][e] = qq;
          P[s & 1][e] = __builtin_elementwise_fma(__builtin_elementwise_fma(qq, (f32x4){r1, r1, r1, r1}, (f32x4){m1, m1, m1, m1}), al, be);
        }
        asm volatile("" : "+v"(qf[s & 1][0]), "+v"(qf[s & 1][1]), "+v"(P[s & 1][0]), "+v"(P[s & 1][1]));
      };
      auto xfin = [&](int s) {
        u32x4 o;
#pragma unroll
        for (int e = 0; e < 2; ++e) {
          const u32x2 y = yv[s][e];
          const f32x4 yf = {bflo(y[0]), bfhi(y[0]), bflo(y[1]), bfhi(y[1])};
          const f32x4 x = __builtin_elementwise_fma(P[s & 1][e], yf, qf[s & 1][e]);
          o[2 * e] = pack2(x[0], x[1]);
          o[2 * e + 1] = pack2(x[2], x[3]);
        }
        st16(rs, WS_X1B + yo + (unsigned)s * (32u * 20480u), o);
      };
      xpre(0); xfin(0);
      xpre(1); xfin(1);
      SCHED();
      xpre(2); xpre(3);
      lf_wait(24, 4);
      __builtin_amdgcn_s_setprio(3);
      ld_y(2); ld_y(3);
      xfin(2); xfin(3);
      STAMP(28);
      asm volatile("s_waitcnt vmcnt(2)" ::: "memory");
      xflag(l >> 5);
      asm volatile("s_waitcnt vmcnt(1)" ::: "memory");
      xflag(2 + (l >> 5));
      STAMP(5);
    } else {
      const int ch = w;
      char* U = lds + L_U + ch * U_CH;
      char* G = lds + ch * (4 * G_COPY);
      {
        const rsrc_t rf = __builtin_amdgcn_make_buffer_rsrc((void*)(p.ssm_k + (size_t)(quad * 4 + ch) * S_), 0, S_ * 2, 0x00020000);
        u32x4 fa[4];
        u32x2 fb[4];
        __builtin_amdgcn_s_setprio(3);
#pragma unroll
        for (int k = 0; k < 4; ++k) {
          fa[k] = __builtin_amdgcn_raw_buffer_load_b128(rf, l16 + (unsigned)k * 1024u, 0, NT);
        }
        __builtin_amdgcn_s_setprio(1);
        TAG_READY();
#pragma unroll
        for (int k = 0; k < 4; ++k) {
#pragma unroll
          for (int d = 0; d < 2; ++d)
            fb[k][d] = __builtin_amdgcn_update_dpp(k < 3 ? (unsigned)__builtin_amdgcn_readfirstlane(fa[k < 3 ? k + 1 : k][d]) : 0u, fa[k][d], 0x130, 0xf, 0xf, false);
          const unsigned x[6] = {fa[k][0], fa[k][1], fa[k][2], fa[k][3], fb[k][0], fb[k][1]};
          unsigned y[5];
#pragma unroll
          for (int d = 0; d < 5; ++d) y[d] = __builtin_amdgcn_alignbit(x[d + 1], x[d], 16);
          char* gp = G + 32 + k * 1024 + l16;
          *(u32x4*)(gp) = fa[k];
          *(u32x4*)(gp + G_COPY) = (u32x4){y[0], y[1], y[2], y[3]};
          *(u32x4*)(gp + 2 * G_COPY) = (u32x4){x[1], x[2], x[3], x[4]};
          *(u32x4*)(gp + 3 * G_COPY) = (u32x4){y[1], y[2], y[3], y[4]};
          if (k == 0) {
            const unsigned f0 = __builtin_amdgcn_readfirstlane(x[0]), f1 = __builtin_amdgcn_readfirstlane(x[1]);
            const unsigned long long f64 = (unsigned long long)f0 | ((unsigned long long)f1 << 32);
            const unsigned long long hi = l ? (f64 << (64 - 16 * (l & 3))) : 0ull;
            if (l < 4) {
              *(u32x4*)(G + l * G_COPY) = (u32x4){0u, 0u, 0u, 0u};
              *(u32x4*)(G + l * G_COPY + 16) = (u32x4){0u, 0u, (unsigned)hi, (unsigned)(hi >> 32)};
            }
            if (l >= 32) *(u32x4*)(U + (l - 32) * 16) = (u32x4){0u, 0u, 0u, 0u};
          }
        }
        asm volatile("s_waitcnt lgkmcnt(0)" ::: "memory");
        STAMP(1);
      }
      const int a = n16, h = h4, jj = h >> 1, e2 = h & 1;
      const char* ap = G + ((a + 1) & 3) * G_COPY + 2 * (16 * jj + a - 8 * e2 + 9 - ((a + 1) & 3));
      const char* bp = U + 512 + 32 * (a - jj) + 16 * e2;
      auto lda = [&](u32x4* A, int r, int p0, int np) {
#pragma unroll
        for (int pp = 0; pp < np; ++pp) {
          u32x2 lo = *(const volatile LAS u32x2*)(ap + 512 * (p0 + pp) + 64 * r);
          u32x2 hi = *(const volatile LAS u32x2*)(ap + 512 * (p0 + pp) + 64 * r + 8);
          A[pp] = (u32x4){lo[0], lo[1], hi[0], hi[1]};
        }
      };
      u32x4 AD;
      {
        const unsigned dDr = (*(cptr_t)(p.ssm_D + quad * 4 + (ch & 2)) >> (16 * (ch & 1))) & 0xffffu;
        const unsigned dv = h == (a >> 3) ? ((a & 1) ? dDr : dDr << 16) : 0u;
        const int dw = (7 - (a & 7)) >> 1;
#pragma unroll
        for (int d = 0; d < 4; ++d) AD[d] = dw == d ? dv : 0u;
      }
      u32x4 A0[8][4];
      f32x4 acc[8];
#pragma unroll
      for (int n = 0; n < 8; ++n) acc[n] = (f32x4){0.f, 0.f, 0.f, 0.f};
#pragma unroll
      for (int r = 0; r < 8; ++r) lda(A0[r], r, 0, 4);
      auto yput = [&](int s) {
#pragma unroll
        for (int e = 0; e < 2; ++e)
          *(u32x2*)(U + 1024 * s + 512 * e + (16 * a + 4 * h) * 2) = (u32x2){pack2(acc[2 * s + e][0], acc[2 * s + e][1]), pack2(acc[2 * s + e][2], acc[2 * s + e][3])};
        lf_set(16 + 4 * s + ch);
      };
      auto stage = [&](auto S__) {
        constexpr int s = decltype(S__)::value, NH = s < 2 ? 4 - 2 * s : 0, NP = 8 - 2 * s, NB = s < 2 ? 2 : 8;
        u32x4 A1[3][NH > 0 ? NH : 1], B[NB][2];
        auto ldb = [&](int r) {
#pragma unroll
          for (int e = 0; e < 2; ++e) B[r % NB][e] = *(const u32x4*)(bp + 512 * (2 * s + e) - 64 * r);
        };
        if (NH) { lda(A1[0], 0, 4, NH); lda(A1[1], 1, 4, NH); }
        SCHED();
        if (s > 0) yput(s - 1);
        lf_wait(4 * s, 4);
        if (s == 0) STAMP(3);
#pragma unroll
        for (int r = 0; r < NB; ++r) ldb(r);
        SCHED();
#pragma unroll
        for (int e = 0; e < 2; ++e) acc[2 * s + e] = MFMA16(AD, B[0][e], acc[2 * s + e]);
#pragma unroll
        for (int r = 0; r < 8; ++r) {
          if (NH && r + 2 < 8) lda(A1[(r + 2) % 3], r + 2, 4, NH);
          SCHED();
#pragma unroll
          for (int e = 0; e < 2; ++e)
#pragma unroll
            for (int pp = 0; pp < NP - e; ++pp) acc[2 * s + e + pp] = MFMA16(pp < 4 ? A0[r][pp] : A1[r % 3][pp < 4 ? 0 : pp - 4], B[r % NB][e], acc[2 * s + e + pp]);
          SCHED();
          if (r + NB < 8) ldb(r + NB);
        }
      };
      stage(IC<0>());
      STAMP(4);
      stage(IC<1>());
      STAMPF(9);
      const unsigned fo[1] = {fwi + (unsigned)((8 + c) * 20 + 5 * w + (l < 5 ? l : 0)) * 128u};
      unsigned fv = ~tag;
      {
        u32x4 B[4][4];
        auto ldb = [&](int r) {
#pragma unroll
          for (int m = 0; m < 4; ++m) B[r & 3][m] = *(const u32x4*)(bp + 512 * (4 + m) - 64 * r);
        };
        yput(1);
        if (w == 3) wo_ld();
        __builtin_amdgcn_s_setprio(3);
        lf_wait(8, 8);
        STAMP(22);
#pragma unroll
        for (int r = 0; r < 4; ++r) ldb(r);
        SCHED();
#pragma unroll
        for (int m = 0; m < 4; ++m) acc[4 + m] = MFMA16(AD, B[0][m], acc[4 + m]);
#pragma unroll
        for (int r = 0; r < 8; ++r) {
#pragma unroll
          for (int m = 0; m < 4; ++m)
#pragma unroll
            for (int pp = 0; pp < 4 - m; ++pp) acc[4 + m + pp] = MFMA16(A0[r][pp], B[r & 3][m], acc[4 + m + pp]);
          SCHED();
          if (r + 4 < 8) ldb(r + 4);
          if (r == 1) fv = ld4(rs, fo[0]);
          SCHED();
        }
#pragma unroll
        for (int e = 0; e < 4; ++e)
          *(u32x2*)(U + 2048 + 512 * e + (16 * a + 4 * h) * 2) = (u32x2){pack2(acc[4 + e][0], acc[4 + e][1]), pack2(acc[4 + e][2], acc[4 + e][3])};
        lf_set(24 + ch);
      }
      STAMP(28);
      __builtin_amdgcn_s_setprio(0);
      if (!__all(fv == tag)) wait_lines<1, 4>(rs, fo, tag);
      lf_wait(20, 4);
    }
    __builtin_amdgcn_s_setprio(0);
  } else {
    const int at = idx - 20;
    char* const scr = lds + WBLK + 14848;
    auto gv_blk = [&](int j) { return j < 160 ? 30 + (j >> 3) : j < 400 ? (j >> 3) - 20 : j >> 3; };
    auto gv_fin = [&](const u32x4* pre, int j) {
      float acc[8];
      gemv_fin(acc, pre, 10, scr);
      gemv_put(acc, j < 160 ? WS_MODP + (unsigned)((j & 7) * 1280 + (j >> 3) * 64) * 8u : WS_MODQ + (unsigned)((j & 7) * 2560 + ((j >> 3) - 20) * 64) * 8u);
    };
    const int ja = xcd * 12 + at, ai = xcd * 8 + at, bi = xcd * 4 + at - 8, g = w >> 2, wj = w & 3;
    const int rj = w == 0 ? ja : w == 1 ? (at >= 4 ? 92 + ai : -1) : w == 4 ? 160 + ja : w == 5 && at < 8 ? 256 + ai : -1;
    char* const rst = lds + (g ? WBLK : 65536) + wj * 7424;
    u32x4 pre[11];
    if (at >= 8 || g == 0) {
      TOUCH_AT(2);
      const int T = at >= 8 ? 64 + 2 * bi + g : ai;
      unsigned* mds = (unsigned*)(lds + 2048) + 16 * g;
      char* qt = lds + L_RT + g * (16 * 1312);
      u32x4 ld[5];
      const bool e1 = at >= 8 && w == 3;
      if (e1) gemv_load(pre, gv_blk(128 + bi), bi & 7, 10);
      else __builtin_amdgcn_s_sleep(8);
#pragma unroll
      for (int j = 0; j < 5; ++j)
        ld[j] = __builtin_amdgcn_raw_buffer_load_b128(rq, (unsigned)(h4 * D_ * 2 + n16 * 16 + j * 256), (unsigned)((32 * (T >> 1) + 8 * wj + 4 * (T & 1)) * D_ * 2), NT);
      TAG_READY();
      {
        const bf2_t ONE2 = {(__bf16)1.0f, (__bf16)1.0f};
        char* dst = qt + (4 * wj + h4) * 1312 + n16 * 16;
        float sa = 0.f, va = 0.f;
#pragma unroll
        for (int j = 0; j < 5; ++j) {
          *(u32x4*)(dst + j * 256) = ld[j];
#pragma unroll
          for (int d = 0; d < 4; ++d) {
            const unsigned a0 = ld[j][d];
            const bf2_t xa = __builtin_bit_cast(bf2_t, a0);
            sa = __builtin_amdgcn_fdot2_f32_bf16(xa, ONE2, sa, false); va = __builtin_amdgcn_fdot2_f32_bf16(xa, xa, va, false);
          }
        }
        lf_set(8 + w);
        const float ma = sum16(sa) * (1.0f / D_);
        va = __builtin_fmaxf(sum16(va) * (1.0f / D_) - ma * ma, 0.f);
        if (n16 == 0) mds[4 * wj + h4] = pack2(ma, __builtin_amdgcn_sqrtf(rbf(rbf(va) + 1e-5f)));
      }
      if (e1) gv_fin(pre, 128 + bi);
      lf_wait(8 + 4 * g, 4);
      STAMP(23);
      {
        const char* src = qt + (l & 7) * 1312 + (40 * wj + (l >> 3)) * 8;
#pragma unroll
        for (int j = 0; j < 5; ++j) {
          const u32x2 x = *(const u32x2*)(src + j * 64), y = *(const u32x2*)(src + j * 64 + 8 * 1312);
          st16(rs, WS_QB + (unsigned)T * 20480u + (unsigned)(5 * wj + j) * 1024u + l16, (u32x4){x[0], x[1], y[0], y[1]});
        }
      }
      SCHED();
      const int g2 = at < 8 || w < 3 || w == 4 ? 400 : w == 3 ? 384 + bi : 256 + wj * 32 + bi;
      if (g2 < 400) {
        gemv_load(pre, gv_blk(g2), g2 & 7, 10);
        asm volatile("s_waitcnt vmcnt(10)" ::: "memory");
      } else if (w < 2 && rj >= 0) {
        rt_ld(rj, rst);
        asm volatile("s_waitcnt vmcnt(7)" ::: "memory");
      } else {
        DRAIN();
      }
      lf_set(16 + w);
      if (wj == 2) {
        lf_wait(16 + 4 * g, 4);
        st16(rs, WS_ST1 + (unsigned)(l >> 3) * 16384u + (unsigned)T * 128u + (unsigned)(l & 7) * 16u, (u32x4){mds[l & 7], tag, mds[8 + (l & 7)], tag});
        STAMP(22);
      }
      if (w >= 4 && w < 7) lf_set(w);
      if (g2 < 400) gv_fin(pre, g2);
      STAMP(25);
    } else {
      constexpr int KB = L_RT + 16 * 1312;
      TOUCH_AT(3);
      const int g1 = w < 6 ? 2 * ai + wj : 32 + wj * 64 + ai;
      if (w >= 6) {
        const rsrc_t rk = __builtin_amdgcn_make_buffer_rsrc((void*)(w == 6 ? p.k : p.v), 0, S_ * D_ * 2, 0x00020000);
        const unsigned ko = (unsigned)((1536 + 8 * ai) * (D_ * 2) - (w == 6 ? 4096 : 0));
        char* dst = lds + KB + (w == 6 ? 0 : 14336);
        __builtin_amdgcn_s_sleep(3);
#pragma unroll
        for (int n = 0; n < 10; ++n) __builtin_amdgcn_raw_ptr_buffer_load_lds(rk, (LAS void*)(dst + n * 1024), 16, l16, ko + (unsigned)n * 1024u, 0, NT);
        if (w == 6) {
#pragma unroll
          for (int n = 10; n < 14; ++n) __builtin_amdgcn_raw_ptr_buffer_load_lds(rk, (LAS void*)(dst + n * 1024), 16, l16, ko + (unsigned)n * 1024u, 0, NT);
        }
      }
      SCHED();
      u32x4 swt[2];
      swt[0] = *(const u32x4*)(p.shift_k + (tid - 256) * 8);
      swt[1] = *(const u32x4*)(p.shift_k + (256 + l) * 8);
      SCHED();
      if (w >= 6) __builtin_amdgcn_s_sleep(8);
      gemv_load(pre, gv_blk(g1), g1 & 7, 10);
      TAG_READY();
      asm volatile("s_waitcnt vmcnt(11)" : "+v"(swt[0]), "+v"(swt[1]) :: "memory");
      lf_set(8 + w);
      if (w < 6) gv_fin(pre, g1);
      STAMP(25);
      lf_wait(12, 4);
      STAMP(23);
#pragma unroll 1
      for (int n = 0; n < (w == 7 ? 2 : 1); ++n) {
        {
          const int pr = n ? 256 + l : tid - 256;
          const char* kp = lds + KB + 4096 + pr * 4;
          unsigned kk[11], vv[8];
#pragma unroll
          for (int i = 0; i < 11; ++i) kk[i] = *(const unsigned*)(kp + (i - 3) * 1280);
#pragma unroll
          for (int i = 0; i < 8; ++i) vv[i] = *(const unsigned*)(kp + 10240 + i * 1280);
          const u32x4 sw = n ? swt[1] : swt[0];
          const f32x2 wt[4] = {{bflo(sw[0]), bflo(sw[2])}, {bfhi(sw[0]), bfhi(sw[2])}, {bflo(sw[1]), bflo(sw[3])}, {bfhi(sw[1]), bfhi(sw[3])}};
          f32x2 o[8];
#pragma unroll
          for (int i = 0; i < 8; ++i) {
            f32x2 t = (f32x2){bflo(kk[i + 3]), bfhi(kk[i + 3])} * wt[0];
#pragma unroll
            for (int j = 1; j < 4; ++j) t = __builtin_elementwise_fma((f32x2){bflo(kk[i + 3 - j]), bfhi(kk[i + 3 - j])}, wt[j], t);
            o[i] = t * (f32x2){bflo(vv[i]), bfhi(vv[i])};
          }
#pragma unroll
          for (int e = 0; e < 4; ++e) {
            const int i = 7 - 4 * (e & 1);
            st16(rs, WS_KV3 + (unsigned)ai * 20480u + (unsigned)(e * 5120 + pr * 16),
                 (u32x4){pack2(o[i][e >> 1], o[i - 1][e >> 1]), tag, pack2(o[i - 2][e >> 1], o[i - 3][e >> 1]), tag});
          }
        }
      }
      STAMP(22);
      if (w < 7) lf_set(w);
      if (w >= 6) gv_fin(pre, g1);
      STAMPF(30);
    }
    if (w == 6 && ja < 80) {
      hold(AUX_T);
      gemv_load(pre, gv_blk(400 + ja), ja & 7, 10);
      gv_fin(pre, 400 + ja);
    }
    if (rj >= 0) {
      if (w >= 2) { hold(RT2_T); rt_ld(rj, rst); }
      rt_job(rj, rst);
    }
    STAMP(26);
    lf_set(w);
    if (w == 3) { hold(WA_T); wo_ld(); }
    STAMP(3);
    if (w < 4) {
      {
        const unsigned fo[1] = {l < 5 ? fwi + (unsigned)((8 + c) * 20 + 5 * w + l) * 128u : ~0u};
        hold(W_T);
        wait_lines<1, 8>(rs, fo, tag);
      }
      lf_wait(0, 8);
      STAMP(4);
    }
    if ((at == 8 || at == 9) && w == 5 && xcd * 2 + at - 8 < 10) {
      const unsigned o = WS_MODQ + (unsigned)(h4 * 5120 + 1920 + (xcd * 2 + at - 8) * 64 + n16 * 4) * 8u;
      u32x4 v[4];
      hold(CS_T);
      _Pragma("unroll 1") for (unsigned spins = 0; spins < SPIN_CAP; ++spins) {
        unsigned dd = 0u;
#pragma unroll
        for (int k = 0; k < 4; ++k) v[k] = ld16(rs, o + (unsigned)((k >> 1) * 20480 + (k & 1) * 16));
#pragma unroll
        for (int k = 0; k < 4; ++k) dd |= (v[k][0] ^ tag) | (v[k][2] ^ tag);
        if (__all(dd == 0u)) break;
        __builtin_amdgcn_s_sleep(16);
      }
      float t[4];
#pragma unroll
      for (int k = 0; k < 4; ++k) t[k] = sum_rows(__uint_as_float(v[k >> 1][1 + 2 * (k & 1)]) + __uint_as_float(v[2 + (k >> 1)][1 + 2 * (k & 1)]));
#pragma unroll
      for (int e = 0; e < 2; ++e)
        st16(rs, WS_MOD + (unsigned)(4 * e + h4) * 2560u + (unsigned)((xcd * 2 + at - 8) * 16 + n16) * 16u, (u32x4){tag, pack2(t[0], t[1]), tag, pack2(t[2], t[3])});
    }
    if (w == 4 || (w == 5 && at >= 10)) {
      const int cj = w == 4 ? ja : 86 + xcd * 2 + at, cb = cj / 7, j = cj % 7;
      const unsigned o = WS_CSP + (unsigned)((cb * 20 + 5 * h4) * 2 + (j >> 2)) * 1024u + (unsigned)((j & 3) * 16 + n16) * 16u;
      u32x4 v[5];
      hold(CS_T);
      __builtin_amdgcn_s_setprio(3);
      _Pragma("unroll 1") for (unsigned spins = 0; spins < SPIN_CAP; ++spins) {
        unsigned dd = 0u;
#pragma unroll
        for (int k = 0; k < 5; ++k) v[k] = ld16(rs, o + (unsigned)k * 2048u);
#pragma unroll
        for (int k = 0; k < 5; ++k) dd |= (v[k][0] ^ tag) | (v[k][2] ^ tag);
        if (__all(dd == 0u)) break;
        __builtin_amdgcn_s_sleep(8);
      }
      float t0 = 0.f, t1 = 0.f;
#pragma unroll
      for (int k = 0; k < 5; ++k) { t0 += __uint_as_float(v[k][1]); t1 += __uint_as_float(v[k][3]); }
      t0 = sum_rows(t0); t1 = sum_rows(t1);
      if (l < 16) st16(rs, WS_CS + (unsigned)(cb * 2048 + j * 256) + l16, (u32x4){tag, __float_as_uint(t0), tag, __float_as_uint(t1)});
      __builtin_amdgcn_s_setprio(0);
      STAMP(24);
    }
  }

  STAMPF(16);
  u32x4 wox[5];
  DRAIN();
  if (w == 3) {
    const char* bp = wo_st + (4 * h4 + (n16 >> 2)) * 160 + (n16 & 3) * 8;
#pragma unroll
    for (int dj = 0; dj < 5; ++dj) wox[dj] = trfrag(bp + dj * 32, 160);
    asm volatile("s_waitcnt lgkmcnt(0)" : "+v"(wox[0]), "+v"(wox[1]), "+v"(wox[2]), "+v"(wox[3]), "+v"(wox[4]) :: "memory");
  }
  auto wo_put = [&]() {
    if (w == 3 && R < 28) {
      const unsigned base = WS_WOUTF + (unsigned)c * WBLK + (unsigned)((R >> 1) * 10240 + (R & 1) * 1024);
#pragma unroll
      for (int dj = 0; dj < 5; ++dj) st16(rs, base + (unsigned)((R & 1) ? dj : (dj + 2) % 5) * 2048u + l16, wox[dj]);
    }
  };
  if (w < 4) load_b2();
  wo_put();
  STAMPF(29);
  u32x4 k_c;
  unsigned kc_o;
  {
    const int i = tid < 224 ? tid : 0;
    const int col = i >= 112 ? (8 + c) * 128 + i - 112 : c * 128 + i;
    kc_o = w < 4 ? WS_CS + (unsigned)col * 16u : WS_MOD + (unsigned)xcd * 2560u + (unsigned)(20 * c + (tid < 276 ? tid - 256 : 0)) * 16u;
  }
  auto consts_put = [&]() {
    if (!__all(k_c[0] == tag && k_c[2] == tag)) {
      _Pragma("unroll 1") for (unsigned spins = 0; spins < SPIN_CAP; ++spins) {
        __builtin_amdgcn_s_sleep(8);
        k_c = ld16(rs, kc_o);
        if (__all(k_c[0] == tag && k_c[2] == tag)) break;
      }
    }
    if (tid < 224) *(u32x2*)(lds + L_CONST + tid * 8) = (u32x2){k_c[1], k_c[3]};
    if (tid >= 256 && tid < 276) *(f32x4*)(lds + L_MISC + (tid - 256) * 16) = (f32x4){bflo(k_c[1]), bfhi(k_c[1]), bflo(k_c[3]), bfhi(k_c[3])};
  };
  const int wq = w ^ rotr, rt1 = wq & 3, kh1 = w >> 2;
  unsigned bo0 = (unsigned)(kh1 * 71680) + l16, bo1 = bo0 + 35840u;
  asm volatile("" : "+v"(bo0), "+v"(bo1));
#define BFRAG(q, j) (*(const u32x4*)(lds + ((q) < 5 ? bo0 : bo1) + ((q) % 5) * 7168 + (j) * 1024))
  u32x4 h2[10];
  f32x4 acc[7];
  float mu_n, rs_n;
  {
    if (w == 6) {
      const unsigned fo[1] = {l < 20 ? fwi + (unsigned)(c * 20 + l) * 128u : ~0u};
      hold(CS_T);
      wait_lines<1, 8>(rs, fo, tag);
    }
    if (w == 7) {
      const unsigned fb = WS_FX + (unsigned)(((R >> 1) * 4 + (R & 1) * 2 + (c >> 2)) * 160 + l) * 128u;
      const unsigned fo[3] = {fb, fb + 8192u, l < 32 ? fb + 16384u : ~0u};
      if (!is_conv) hold(Y_T);
      wait_lines<3, 2>(rs, fo, tag);
      if (l == 0) lf[33] = tag;
    }
    lf_wait(33, 1);
    STAMP(7);
    const unsigned xo = (unsigned)(R * 4 + rt1) * 20480u + (unsigned)(10 * kh1) * 1024u +
                        (unsigned)((4 * ((l >> 3) & 1) + h4) * 128 + (l & 7) * 16);
    SCHED();
#pragma unroll
    for (int q = 0; q < 3; ++q) h2[q] = ld16p(rs, WS_X1B + xo + (unsigned)q * 1024u);
    SCHED();
    __builtin_amdgcn_s_waitcnt(0x0F73);
    BAR();
    if (w == 3) st_flag4(rs, WS_FWO + (unsigned)(c * 32 + R) * 128u, tag, l);
    STAMP(8);
#pragma unroll
    for (int j = 0; j < 7; ++j) acc[j] = (f32x4){0.f, 0.f, 0.f, 0.f};
    f32x4 accg = {0.f, 0.f, 0.f, 0.f};
    u32x4 bf[7];
#pragma unroll
    for (int j = 0; j < 7; ++j) bf[j] = BFRAG(0, j);
    SCHED();
#pragma unroll
    for (int q = 3; q < 6; ++q) h2[q] = ld16p(rs, WS_X1B + xo + (unsigned)q * 1024u);
    SCHED();
#pragma unroll
    for (int q = 0; q < 10; ++q) {
      {
#pragma unroll
        for (int e = 0; e < 6; ++e) {
          const int d = e & 1;
          if ((e >> 1) == 1) h2[q][2 + d] ^= (unsigned)__builtin_amdgcn_update_dpp(0, (int)h2[q][d], 0x128, 0xf, 0x3, false);
          else h2[q][d] ^= (unsigned)__builtin_amdgcn_update_dpp(0, (int)h2[q][2 + d], 0x128, 0xf, 0xC, false);
        }
      }
      if (q == 5) { __builtin_amdgcn_s_barrier(); asm volatile("" ::: "memory"); }
      SCHED();
      accg = MFMA16(h2[q], h2[q], accg);
      SCHED();
#pragma unroll
      for (int j = 0; j < 7; ++j) {
        acc[j] = MFMA16(h2[q], bf[j], acc[j]);
        if (q == 9 && j == 1 && h4 == (n16 >> 2)) *(f32x4*)(lds + L_PST + 512 + (wq * 16 + n16) * 16) = accg;
        if (q < 9) bf[j] = BFRAG(q + 1, j);
#define KCQ 4
#define WA 8
#define WB 12
#define WQ0 2
        if (WQ0 == 2 && q >= 2 && j == 2) LDW(q - 2);
        if (WQ0 == 6 && q >= 6 && (j == 2 || j == 4)) LDW(2 * (q - 6) + (j >> 2));
        if (WQ0 == 3 && q >= 2 && q < 5 && (j & 1) && 3 * (q - 2) + (j >> 1) < WA) LDW(3 * (q - 2) + (j >> 1));
        if (WQ0 == 4 && q >= 2 && !(q & 1) && j == 2) LDW((q - 2) >> 1);
        if ((j == 0 || j == 4) && 6 + 2 * q + (j >> 2) < 10) h2[6 + 2 * q + (j >> 2)] = ld16p(rs, WS_X1B + xo + (unsigned)(6 + 2 * q + (j >> 2)) * 1024u);
        if (q == KCQ && j == 0) k_c = ld16(rs, kc_o);
        if (q >= 5 && j == 5) {
          const int i = (5 + q) * 7 + (w < 7 ? w : 0);
          dma1k(rs, lds + i * 1024, l16, WS_WINF + (unsigned)c * WBLK + (unsigned)i * 1024u);
          if (q == 9) { const int i2 = 28 + (w < 7 ? w : 0); dma1k(rs, lds + i2 * 1024, l16, WS_WINF + (unsigned)c * WBLK + (unsigned)i2 * 1024u); }
        }
        SCHED();
      }
    }
    STAMP(31);
  }
  auto gelu_n = [&](f32x2* x, auto N_) {
    constexpr int N = decltype(N_)::value;
    const float c0 = -2.0f * 0.7978845608028654f * 1.4426950408889634f, c1 = c0 * 0.044715f;
    f32x2 k0 = {c0, c0}, k1 = {c1, c1}, k2 = {1.0f, 1.0f};
    asm volatile("" : "+v"(k0), "+v"(k1), "+v"(k2));
    f32x2 t[N];
#define GST(e) do { _Pragma("unroll") for (int i = 0; i < N; ++i) { t[i] = (e); asm volatile("" : "+v"(t[i])); } } while (0)
    GST(x[i] * x[i]);
    GST(__builtin_elementwise_fma(t[i], k1, k0));
    GST(t[i] * x[i]);
    GST(((f32x2){__builtin_amdgcn_exp2f(t[i][0]), __builtin_amdgcn_exp2f(t[i][1])}));
    GST(t[i] + k2);
    GST(((f32x2){__builtin_amdgcn_rcpf(t[i][0]), __builtin_amdgcn_rcpf(t[i][1])}));
#pragma unroll
    for (int i = 0; i < N; ++i) { x[i] *= t[i]; asm volatile("" : "+v"(x[i])); }
  };
  {
    f32x2* red = (f32x2*)lds;
    f32x2 mine[7];
    if (kh1) {
#pragma unroll
      for (int j = 0; j < 7; ++j) { red[(w * 7 + j) * 64 + l] = (f32x2){acc[j][0], acc[j][1]}; mine[j] = (f32x2){acc[j][2], acc[j][3]}; }
    } else {
#pragma unroll
      for (int j = 0; j < 7; ++j) { red[(w * 7 + j) * 64 + l] = (f32x2){acc[j][2], acc[j][3]}; mine[j] = (f32x2){acc[j][0], acc[j][1]}; }
    }
    f32x2 csb[7];
    unsigned cso = (unsigned)(L_CONST + (112 + n16) * 8);
    asm volatile("" : "+v"(cso));
    consts_put();
    unsigned pso[3], pqo[3], oto = (unsigned)(((w ^ 4) * 7 * 64 + l) * 8);
#pragma unroll
    for (int e = 0; e < 3; ++e) {
      const int row = rt1 * 16 + (e < 2 ? 4 * h4 + 2 * kh1 + e : n16);
      pso[e] = (unsigned)(L_PST + row * 4); pqo[e] = (unsigned)(L_PST + 512 + row * 16 + (row & 3) * 4);
    }
    asm volatile("" : "+v"(pso[0]), "+v"(pso[2]), "+v"(pqo[0]), "+v"(pqo[1]), "+v"(pqo[2]), "+v"(oto));
    pso[1] = pso[0] + 4u;
    if (n16 == 12) *(f32x4*)(lds + L_PST + (wq * 16 + 4 * h4) * 4) = acc[6];
    if (!kh1) {
#pragma unroll
      for (int k = WA; k < WB; ++k) LDW(k);
    }
    BAR();
    if (kh1) {
#pragma unroll
      for (int k = WA; k < WB; ++k) LDW(k);
    }
    __builtin_amdgcn_s_setprio(3);
    STAMPT(9);
    f32x2 oth[7];
    f32x2 pa[3], pb[3];
#pragma unroll
    for (int e = 0; e < 3; ++e) {
      pa[e] = (f32x2){*(const float*)(lds + pso[e]), *(const float*)(lds + pqo[e])};
      pb[e] = (f32x2){*(const float*)(lds + pso[e] + 256), *(const float*)(lds + pqo[e] + 1024)};
    }
#pragma unroll
    for (int j = 0; j < 7; ++j) { oth[j] = *(const f32x2*)(lds + oto + j * 512); csb[j] = *(const f32x2*)(lds + cso + 128 * j); }
    SCHED();
    float mu2[2], rs2[2];
#pragma unroll
    for (int e = 0; e < 3; ++e) {
      const float mean = (pa[e][0] + pb[e][0]) * (1.0f / D_);
      const float var = __builtin_fmaxf((pa[e][1] + pb[e][1]) * (1.0f / D_) - mean * mean, 0.f);
      const float rstd = __builtin_amdgcn_rsqf(var + 1e-5f);
      if (e < 2) { mu2[e] = mean; rs2[e] = rstd; } else { mu_n = mean; rs_n = rstd; }
    }
    f32x2 zz[7];
    const f32x2 nmu = {-mu2[0], -mu2[1]}, rsv = {rs2[0], rs2[1]};
#pragma unroll
    for (int j = 0; j < 7; ++j)
      zz[j] = __builtin_elementwise_fma(__builtin_elementwise_fma(nmu, (f32x2){csb[j][0], csb[j][0]}, mine[j] + oth[j]), rsv, (f32x2){csb[j][1], csb[j][1]});
    gelu_n(zz, IC<7>());
    if (n16 >= 12) zz[6] = (f32x2){0.f, 0.f};
    f32x2 s1 = ((zz[0] + zz[1]) + (zz[2] + zz[3])) + ((zz[4] + zz[5]) + zz[6]);
    f32x2 s2 = zz[0] * zz[0], s3 = zz[1] * zz[1];
#pragma unroll
    for (int j = 2; j < 7; ++j) { if (j & 1) s3 = __builtin_elementwise_fma(zz[j], zz[j], s3); else s2 = __builtin_elementwise_fma(zz[j], zz[j], s2); }
    s2 += s3;
    asm("" : "+v"(s1), "+v"(s2));
    float a0 = s1[0] + dpp_f<0xB1>(s1[0]), a1 = s1[1] + dpp_f<0xB1>(s1[1]), b0 = s2[0] + dpp_f<0xB1>(s2[0]);
    a0 = (l & 1) ? a1 : a0;
    const float b1 = s2[1] + dpp_f<0xB1>(s2[1]);
    b0 = (l & 1) ? b1 : b0;
    a0 += dpp_f<0x4E>(a0); b0 += dpp_f<0x4E>(b0);
    a0 += dpp_f<0x124>(a0); b0 += dpp_f<0x124>(b0);
    a0 += dpp_f<0x128>(a0); b0 += dpp_f<0x128>(b0);
    {
      const int src = ((l >> 1) & 3) * 64 + (l & 1) * 4;
      const unsigned pa0 = __builtin_amdgcn_ds_bpermute(src, __float_as_uint(a0)), pb0 = __builtin_amdgcn_ds_bpermute(src, __float_as_uint(b0));
      if (l < 8) st16(rs, WS_PART + (unsigned)((R * 8 + c) * 8 + wq) * 128u + l16, (u32x4){tag, pa0, tag, pb0});
    }
    SCHED();
    __builtin_amdgcn_s_setprio(0);
    STAMP(10);
#pragma unroll
    for (int k = 0; k < 10; ++k) {
      if (k < 9) {
        int x = k * 8 + w;
        x = x < 70 ? x : 69;
        const int i = x + (x < 35 ? 35 : 70);
        dma1k(rs, lds + i * 1024, l16, WS_WINF + (unsigned)c * WBLK + (unsigned)i * 1024u);
      }
    }
#pragma unroll
    for (int k = WB; k < 16; ++k) LDW(k);
#pragma unroll
    for (int j = 0; j < 7; ++j)
      *(unsigned*)(lds + L_Z + (((rt1 >> 1) * 7 + j) * 64 + l) * 16 + (4 * (rt1 & 1) + 2 * kh1) * 2) = pack2(zz[j][0], zz[j][1]);
    SCHED();
#define TR0 2
#define TR1 TR0
#define TR2 TR1
#define TR3 5
#pragma unroll
    for (int g = 0; g < TR0; ++g) WSP_TR(g);
    if (w == 1) {
      const unsigned fo[1] = {l < 28 ? fwi + (WS_FWO - WS_FWI) + (unsigned)(c * 32 + l) * 128u : ~0u};
      wait_lines<1, 1>(rs, fo, tag);
    }
    asm volatile("s_waitcnt vmcnt(%0)" :: "n"(16 - WB) : "memory");
    BAR();
    STAMPT(11);
  }
  unsigned bs_raw;
  unsigned z1d[7];
  constexpr int L_RED = 28672;
#define WOA(i) (((i) < 28 ? (i) : (i) < 108 ? (i) + 32 : (i) < 120 ? (i) + 37 : (i) - 92) * 1024)
#define RBAR() do { __builtin_amdgcn_s_barrier(); asm volatile("" ::: "memory"); } while (0)
#define ZAUX 16
#define NW 14
  unsigned wx8 = (unsigned)((w + (idx >> 2)) & 7) * 1024u, wx4 = wx8 & 3072u, wgb = WS_WOUTF + (unsigned)c * WBLK;
  asm volatile("" : "+s"(wx8), "+s"(wx4), "+s"(wgb));
#define WOD(i0, x) dma1k(rs, lds + WOA(i0) + (x), l16, wgb + (unsigned)(i0) * 1024u + (x))
  auto wo_dma = [&](int k) {};
  unsigned zs[8];
#pragma unroll
  for (int t = 0; t < 8; ++t) zs[t] = WS_Z2F + (unsigned)(c * 64 + 8 * w + 4 * ((t >> 2) ^ rotg) + ((t + rotb) & 3)) * 7168u;
  u32x4 zw[NW];
#define LDZ(f) do { if (((f) >> 3) == 4) { zs[(f) & 7] += 4096u; asm volatile("" : "+s"(zs[(f) & 7])); } \
    zw[(f) % NW] = __builtin_amdgcn_raw_buffer_load_b128(rs, l16 + (unsigned)(((f) >> 3) & 3) * 1024u, zs[(f) & 7], ZAUX); } while (0)
#define WD 24
#define WE 32
#define QS 5
#define PQ 3
#define NW0 NW
#define PP 1
#define WZ 4
#define WY 11
  {
    f32x4 acc[7];
#pragma unroll
    for (int j = 0; j < 7; ++j) acc[j] = (f32x4){0.f, 0.f, 0.f, 0.f};
    u32x4 bf[7];
#pragma unroll
    for (int j = 0; j < 7; ++j) bf[j] = BFRAG(4, j);
    const unsigned po = WS_PART + (unsigned)((R * 8 + (l >> 3)) * 8 + wq) * 128u + (unsigned)(l & 7) * 16u;
    u32x4 pv;
    auto z1 = [&](auto Q0_, auto Q1_) __attribute__((always_inline)) {
#pragma unroll
      for (int qi = decltype(Q0_)::value; qi < decltype(Q1_)::value; ++qi) {
        const int q = (qi + 4) % 10, qn = (qi + 5) % 10;
        SCHED();
#pragma unroll
        for (int j = 0; j < 7; ++j) {
          acc[j] = MFMA16(bf[j], h2[q], acc[j]);
          if (qi < 9 && qi != QS - 1) bf[j] = BFRAG(qn, j);
          if (qi < 2 && (j == 1 || j == 4)) { const int i = (2 * qi + (j >> 2)) * 7 + (w < 7 ? w : 0); dma1k(rs, lds + i * 1024, l16, WS_WINF + (unsigned)c * WBLK + (unsigned)i * 1024u); }
          if (qi == PQ && j == 0) pv = ld16(rs, po);
          if (!PP && qi >= QS && (j & 1) && WD + 3 * (qi - QS) + (j >> 1) < WE) LDW((WD + 3 * (qi - QS) + (j >> 1)) & 31);
          SCHED();
        }
      }
    };
    auto req1 = [&]() {
#pragma unroll
      for (int k = 16; k < WD; ++k) { LDW(k); SCHED(); }
    };
    auto req2 = [&]() {
#pragma unroll
      for (int k = WD; k < (PP ? WE : WD); ++k) { LDW(k); SCHED(); }
      sfor<0, WZ>([&](auto G_) __attribute__((always_inline)) { WOD(68 + 8 * decltype(G_)::value, wx8); SCHED(); });
    };
    if (kh1) req1();
    z1(IC<0>(), IC<QS>());
    if (!kh1) req1();
    STAMPT(29);
#pragma unroll
    for (int g = TR0; g < TR1; ++g) WSP_TR(g);
    __builtin_amdgcn_s_setprio(3);
    if (!__all(((pv[0] ^ tag) | (pv[2] ^ tag)) == 0u)) {
      _Pragma("unroll 1") for (unsigned spins = 0; spins < SPIN_CAP; ++spins) {
        pv = ld16(rs, po);
        if (__all(((pv[0] ^ tag) | (pv[2] ^ tag)) == 0u)) break;
        __builtin_amdgcn_s_sleep(1);
      }
    }
    {
      const float mean = red8(__uint_as_float(pv[1])) * (1.0f / FH);
      const float var = __builtin_fmaxf(red8(__uint_as_float(pv[3])) * (1.0f / FH) - mean * mean, 0.f);
      const float rstd = __builtin_amdgcn_rsqf(var + 1e-5f);
      if (l < 8) *(f32x2*)(lds + L_ST2 + (16 * rt1 + 4 * (l >> 1) + 2 * kh1 + (l & 1)) * 8) = (f32x2){rstd, -mean * rstd};
    }
    BAR();
    STAMP(12);
    u32x4 zf[2];
#pragma unroll
    for (int m = 0; m < 2; ++m) zf[m] = *(const u32x4*)(lds + L_Z + ((m * 7 + (w < 7 ? w : 0)) * 64 + l) * 16);
    if (w < 7) {
#pragma unroll
      for (int m = 0; m < 2; ++m) {
        const f32x4* sp = (const f32x4*)(lds + L_ST2 + (32 * m + 4 * h4) * 8);
        u32x4 o;
#pragma unroll
        for (int e = 0; e < 4; ++e) {
          f32x4 st = sp[(e & 1) + 8 * (e >> 1)];
          o[e] = pack2(__builtin_fmaf(bflo(zf[m][e]), st[0], st[1]), __builtin_fmaf(bfhi(zf[m][e]), st[2], st[3]));
        }
        st16(rs, WS_Z2F + (unsigned)((c * 64 + 4 * (R >> 1) + h4) * 7 + (w < 6 ? w + 1 : 0)) * 1024u + (unsigned)((2 * (R & 1) + m) * 256 + n16 * 16), o);
      }
    }
    SCHED();
    __builtin_amdgcn_s_setprio(0);
    STAMPT(13);
#pragma unroll
    for (int j = 0; j < 7; ++j) bf[j] = BFRAG((QS + 4) % 10, j);
    if (kh1) req2();
    z1(IC<QS>(), IC<10>());
    f32x2* red = (f32x2*)(lds + L_RED);
    f32x2 mine[7];
    if (kh1) {
#pragma unroll
      for (int j = 0; j < 7; ++j) { red[(w * 7 + j) * 64 + l] = (f32x2){acc[j][0], acc[j][1]}; mine[j] = (f32x2){acc[j][2], acc[j][3]}; }
    } else {
#pragma unroll
      for (int j = 0; j < 7; ++j) { red[(w * 7 + j) * 64 + l] = (f32x2){acc[j][2], acc[j][3]}; mine[j] = (f32x2){acc[j][0], acc[j][1]}; }
    }
    if (!kh1) req2();
#pragma unroll
    for (int g = TR1; g < TR2; ++g) WSP_TR(g);
    asm volatile("s_waitcnt vmcnt(%0)" :: "n"(WE - WD + WZ) : "memory");
    BAR();
    if (w == 3) st_flag4(rs, WS_FZ + (unsigned)(c * 32 + R) * 128u, tag, l);
    STAMP(6);
    auto req = [&](auto G0_, auto G1_) __attribute__((always_inline)) {
#pragma unroll
      for (int k = WE; k < 32; ++k) LDW(k);
      bs_raw = p.b_sp[R * 64 + tau(rt1, n16)];
      SCHED();
      sfor<decltype(G0_)::value, decltype(G1_)::value>([&](auto G_) __attribute__((always_inline)) {
        constexpr int i0 = 4 * decltype(G_)::value;
        WOD(i0 < 68 ? i0 : i0 + 8 * WZ, wx4); SCHED();
      });
    };
    if (kh1) req(IC<0>(), IC<WY>());
    f32x2 z[7];
    {
      f32x2 oth[7];
      f32x4 cc[7];
#pragma unroll
      for (int j = 0; j < 7; ++j) {
        oth[j] = red[((w ^ 4) * 7 + j) * 64 + l];
        cc[j] = *(const f32x4*)(lds + L_CONST + (16 * j + 4 * h4 + 2 * kh1) * 8);
      }
      SCHED();
#pragma unroll
      for (int j = 0; j < 7; ++j)
        z[j] = __builtin_elementwise_fma(__builtin_elementwise_fma((f32x2){-mu_n, -mu_n}, (f32x2){cc[j][0], cc[j][2]}, oth[j] + mine[j]),
                                         (f32x2){rs_n, rs_n}, (f32x2){cc[j][1], cc[j][3]});
    }
    gelu_n(z, IC<7>());
#pragma unroll
    for (int j = 0; j < 7; ++j) z1d[j] = pack2(z[j][0], z[j][1]);
    asm volatile("" : "+v"(z1d[0]), "+v"(z1d[1]), "+v"(z1d[2]), "+v"(z1d[3]), "+v"(z1d[4]), "+v"(z1d[5]), "+v"(z1d[6]));
    SCHED();
    STAMPT(30);
    if (!kh1) req(IC<WY>(), IC<30 - 2 * WZ>());
    const unsigned fo[1] = {l < 4 ? fz + (unsigned)(c * 32 + 4 * w + l) * 128u : ~0u};
    const unsigned fv = ld4(rs, fo[0]);
    SCHED();
#pragma unroll
    for (int g = TR2; g < TR3; ++g) WSP_TR(g);
    SCHED();
    STAMP(14);
    if (__builtin_amdgcn_ballot_w64(fv == tag) != 15ull) wait_lines<1, 1>(rs, fo, tag);
    STAMPT(23);
#pragma unroll
    for (int f = 0; f < NW; ++f) { LDZ(f); SCHED(); }
  }

#define WO3 (-92)
#define WO2 15
#define TRC TR3
#define VMW(n) asm volatile("s_waitcnt vmcnt(%0)" ::"n"(n) : "memory")
  {
    SCHED();
#pragma unroll
    for (int g = TRC; g < 8; ++g) WSP_TR(g);
    SCHED();
    STAMP(20);
    const int rt = rt1, kh = kh1;
    unsigned rw = (unsigned)(L_RED + w * 512 + l * 8), rr = (unsigned)(L_RED + w * 4096 + l * 8);
    asm volatile("" : "+v"(rw), "+v"(rr));
    const float bs = bflo(bs_raw);
    const unsigned uo = WS_UF + (unsigned)((R * 4 + rt) * 8 + c) * 4096u + (unsigned)kh * 2048u;
    const unsigned ui = WS_UF + (unsigned)((R * 4 + rt) * 8 + 4 * kh) * 4096u;
    unsigned long long hm = 0xffff000000000000ull;
    asm volatile("" : "+s"(hm));
    unsigned l8;
    f32x4 acc2[2][4];
    float rv[8][2];
    u32x4 ud;
    u32x4 P[2][8];
    u32x2 xp[3];
    auto ld_p = [&](int pt, int i) { P[pt][i] = __builtin_amdgcn_raw_buffer_load_b128(rs, l16 + (unsigned)pt * 1024u, ui + (unsigned)i * 2048u, SC1); };
    auto red_put = [&](int j, int r) {
      *(f32x2*)(lds + rw + r * 4096) = (f32x2){acc2[j & 1][r][0], acc2[j & 1][r][1]};
      *(f32x2*)(lds + rw + (r + 4) * 4096) = (f32x2){acc2[j & 1][r][2], acc2[j & 1][r][3]};
    };
    auto red_put1 = [&](int j, int i) { *(volatile LAS float*)(lds + rw + (i >> 2) * 4096 + ((i >> 1) & 1) * 16384 + (i & 1) * 4) = acc2[j & 1][i >> 2][i & 3]; };
    auto red_get = [&](int i) { const f32x2 x = *(const volatile LAS f32x2*)(lds + rr + i * 512); rv[i][0] = x[0]; rv[i][1] = x[1]; };
    float zz[2], gg[2];
    unsigned cs;
    auto sum_rows_u = [&](unsigned v) {
      auto a = __builtin_amdgcn_permlane16_swap(v, v, false, false);
      v = a[0] + a[1];
      auto b = __builtin_amdgcn_permlane32_swap(v, v, false, false);
      return b[0] + b[1];
    };
    auto red_fin = [&](int j, int k) {
      const int e = k & 1, i = k >> 1;
#define VADD(d, x, y) asm volatile("v_add_f32 %0, %1, %2" : "=v"(d) : "v"(x), "v"(y))
      if (i < 4) VADD(rv[2 * i][e], rv[2 * i][e], rv[2 * i + 1][e]);
      if (i == 4 || i == 5) VADD(rv[4 * (i & 1)][e], rv[4 * (i & 1)][e], rv[4 * (i & 1) + 2][e]);
      if (i == 6) VADD(rv[0][e], rv[0][e], bs);
      if (i == 7) VADD(gg[e], rv[0][e], rv[4][e]);
      if (i == 8) { zz[e] = e ? bfhi(z1d[(j + 6) % 7]) : bflo(z1d[(j + 6) % 7]); asm volatile("" : "+v"(zz[e])); }
      if (i == 9) { zz[e] *= gg[e]; asm volatile("" : "+v"(zz[e])); }
      if (k == 20) { ud[j & 3] = pack2(zz[0], zz[1]); asm volatile("" : "+v"(ud[j & 3])); }
      if (k == 21) {
        if (j == 0) asm volatile("v_cndmask_b32 %0, %0, 0, %1" : "+v"(ud[0]) : "s"(hm));
        if (j == 2) asm volatile("v_add3_u32 %0, %1, %2, %3" : "=v"(cs) : "v"(ud[0]), "v"(ud[1]), "v"(ud[2]));
        if (j == 3) {
          cs = tag - sum_rows_u(cs + ud[3]);
          asm volatile("v_cndmask_b32 %0, %0, %1, %2" : "+v"(ud[0]) : "v"(cs), "s"(hm));
        }
        if (j >= 4) { ud[3] = (j == 4 ? tag : ud[3]) - ud[j & 3]; asm volatile("" : "+v"(ud[3])); }
        if (j % 3 == 0 && j) __builtin_amdgcn_raw_buffer_store_b128(ud, rs, l16 + (unsigned)(j / 6) * 1024u, uo, SC1);
      }
    };
#define P1M 4
#define G2S 0
    struct Sch { int wide, lp, prio, lf, b1, b2, wp[16], rp[8], vp[22]; };
    constexpr Sch SC[4] = {
      {0, 2, 8, 1, 18, 29, {0, 1, 2, 3, 4, 5, 6, 7, 8, 9, 10, 11, 12, 13, 14, 15}, {19, 19, 20, 20, 21, 21, 22, 22}, {29, 29, 30, 30, 31, 31, 32, 33, 35, 36, 37, 38, 39, 40, 41, 42, 43, 44, 45, 46, 47, 48}},
      {1, 3, 4, 0, 18, 28, {3, 7, 11, 15, 99, 99, 99, 99, 99, 99, 99, 99, 99, 99, 99, 99}, {20, 20, 21, 21, 22, 22, 24, 24}, {28, 28, 29, 29, 30, 30, 31, 31, 32, 32, 33, 33, 34, 34, 35, 36, 36, 37, 37, 38, 38, 39}},
      {0, 1, 4, 1, 17, 29, {0, 1, 2, 3, 4, 5, 6, 7, 8, 9, 10, 11, 12, 13, 14, 15}, {17, 17, 18, 18, 19, 19, 20, 20}, {29, 29, 30, 30, 31, 31, 32, 34, 35, 36, 38, 39, 40, 42, 43, 44, 45, 46, 47, 48, 48, 49}},
      {0, 2, 0, 1, 17, 25, {0, 1, 2, 3, 4, 5, 6, 7, 8, 9, 10, 11, 12, 13, 14, 15}, {17, 17, 18, 18, 19, 19, 20, 20}, {25, 25, 26, 26, 27, 27, 28, 28, 29, 29, 30, 30, 31, 31, 32, 33, 23, 24, 34, 35, 36, 37}}};
#define JF 4
#define PC0 26
#define P1D 2
#define VL 2
    constexpr Sch S = SC[G2S];
    constexpr Sch S5 = {0, S.lp, S.prio, S.lf, 17, 24, {0, 1, 2, 3, 4, 5, 6, 7, 8, 9, 10, 11, 12, 13, 14, 15}, {17, 17, 18, 18, 19, 19, 20, 20}, {24, 24, 25, 25, 26, 26, 27, 27, 28, 28, 29, 29, 30, 30, 31, 31, 22, 23, 32, 32, 33, 33}};
    constexpr Sch S6 = {1, S.lp, S.prio, S.lf, 5, 12, {99, 99, 99, 3, 99, 99, 99, 99, 99, 99, 99, 99, 99, 99, 99, 99}, {5, 5, 6, 6, 7, 7, 8, 8},
                        {12, 12 + 1 / VL, 12 + 2 / VL, 12 + 3 / VL, 12 + 4 / VL, 12 + 5 / VL, 12 + 6 / VL, 12 + 7 / VL, 12 + 8 / VL, 12 + 9 / VL, 12 + 10 / VL, 12 + 11 / VL, 12 + 12 / VL, 12 + 13 / VL, 12 + 14 / VL, 12 + 15 / VL, 10, 11,
                         12 + 16 / VL, 12 + 17 / VL, 12 + 18 / VL, 12 + 19 / VL}};
    unsigned wb[3], xo;
#define XPM 19
    unsigned xs = WS_X1B + (unsigned)(R * 4 + rt) * 20480u + (unsigned)(5 * c + 2 * kh) * 512u, wl[3], wg = WS_WOUTF + (unsigned)c * WBLK - (unsigned)(WO3 * 1024);
#pragma unroll
    for (int k = 0; k < 3; ++k) wl[k] = (unsigned)(120 + WO3 + (k < 2 || w < 4 ? w + 8 * k : w)) * 1024u;
    asm volatile("" : "+s"(xs), "+s"(wg), "+s"(wl[0]), "+s"(wl[2]));
#define NAF 32
#define NA0 18
    u32x4 af[NAF];
#define LDA(m) af[(m) % NAF] = *(const u32x4*)(lds + wb[WOA(2 * (m)) >> 16] + (WOA(2 * (m)) & 65535))
    unsigned ck[2];
    const unsigned tag4 = tag * 4u;
    auto p_chk = [&](int pt, int pr, int k) {
      u32x4* q = P[pt] + 4 * pr;
      if (k < 2) ck[k & 1] = q[k & 1][0] + q[k & 1][1];
      else if (k < 8) ck[k & 1] = ck[k & 1] + q[(k & 1) + 2 * ((k >> 2) & 1)][k & 2] + q[(k & 1) + 2 * ((k >> 2) & 1)][(k & 2) + 1];
      else if (k == 8) ck[0] = pt ? ck[0] + ck[1] : sum_rows_u(ck[0] + ck[1]);
      if (k < 9) asm volatile("" : "+v"(ck[k & 1]));
      if (k == 9) {
        if (__builtin_expect(__any(ck[0] != tag4), 0)) {
          _Pragma("unroll 1") for (unsigned spins = 0; spins < SPIN_CAP; ++spins) {
            unsigned d = 0u;
#pragma unroll
            for (int i = 0; i < 4; ++i) ld_p(pt, 4 * pr + i);
#pragma unroll
            for (int i = 0; i < 4; ++i) d += q[i][0] + q[i][1] + q[i][2] + q[i][3];
            if (!pt) d = sum_rows_u(d);
            if (!__any(d != tag4)) break;
            __builtin_amdgcn_s_sleep(1);
          }
        }
      }
      if (k >= 10 && !pt) { unsigned x = q[(k - 10) & 3][0]; asm volatile("v_cndmask_b32 %0, %0, 0, %1" : "+v"(x) : "s"(hm)); q[(k - 10) & 3][0] = x; }
      if (k >= 10 && k < 13 && pt) { q[(k - 10) % 3][3] = q[3][(k - 10) % 3]; asm volatile("" : "+v"(q[(k - 10) % 3])); }
    };
    sfor<0, 224>([&](auto M_) __attribute__((always_inline)) {
      constexpr int mm = decltype(M_)::value, j = mm >> 5, m = mm & 31;
      constexpr bool ro = j >= 5 && m >= 16;
      constexpr int t = ro ? 4 + (m & 3) : m >> 2, r = ro ? (m >> 2) & 3 : m & 3, f = 8 * j + t;
      constexpr Sch C = j < JF ? S : j < 6 ? S5 : S6, B = j <= JF ? S : S5;
      if (t == 0) asm volatile("v_mfma_f32_16x16x32_bf16 %0, %1, %2, 0" : "=&v"(acc2[j & 1][r]) : "v"(zw[f % NW]), "v"(wsp[r][t]), "v"(zw[(f | 1) % NW]));
      else asm volatile("v_mfma_f32_16x16x32_bf16 %0, %1, %2, %0" : "+v"(acc2[j & 1][r]) : "v"(zw[f % NW]), "v"(wsp[r][t]), "v"(zw[(f | 1) % NW]));
      SCHED();
      if (S.prio && (m + 31 - S.lp) % (S.prio ? S.prio : 1) == 0) __builtin_amdgcn_s_setprio(1);
      auto ld = [&]() {
        if ((m & 3) == S.lp) {
          if (S.prio && (m + 32 - S.lp) % (S.prio ? S.prio : 1) == 0) __builtin_amdgcn_s_setprio(0);
          constexpr int g = S.lp == 3 ? mm >> 2 : (mm >> 2) - 1;
          if (g >= 0 && g + NW < 56) LDZ(g + NW);
          constexpr int y = g + NW - 56;
          if (y >= 0 && WO2 + y < 15) wo_dma(WO2 + y);
          SCHED();
        }
        if (j == 6 && m >= P1M && m < P1M + 8 * P1D && (m - P1M) % P1D == 0) { ld_p(0, ((m - P1M) / P1D) & 7); SCHED(); }
      };
      if (!S.lf) ld();
      if ((m == C.b1 && j > 0) || m == C.b2) {
        if (j == 6 && m == C.b2 && WO2 < 15) VMW(m > P1M ? (m - P1M + P1D - 1) / P1D : 0);
        asm volatile("" ::: "memory"); __builtin_amdgcn_s_waitcnt(0xC07F); RBAR(); SCHED();
      }
      sfor<0, 16>([&](auto I_) __attribute__((always_inline)) {
        constexpr int i = decltype(I_)::value;
        if (j > 0 && C.wp[i] == m) { if (C.wide) red_put(j + 1, i & 3); else red_put1(j + 1, i); SCHED(); }
        if (j > 0 && i < 8 && C.rp[i & 7] == m) { red_get(i & 7); SCHED(); }
      });
      if (j == 5 && m >= 27 && (m & 1)) { red_put(j, ((m - 27) >> 1) & 3); SCHED(); }
      if (j == 6 && m >= 23 && (m & 3) == 3) { red_put(j, ((m - 23) >> 2) & 3); SCHED(); }
      if (j == 6 && m == XPM) {
        asm volatile("v_lshrrev_b32 %0, 1, %1" : "=v"(l8) : "v"(l16));
        xo = (l8 & 0x180u) + (unsigned)psi((l8 >> 3) & 15) * 8u;
        asm volatile("" : "+v"(xo));
      }
      if (j == 6 && m > XPM && m < XPM + 4) { xp[(m - XPM - 1) % 3] = __builtin_amdgcn_raw_buffer_load_b64(rs, xo + (unsigned)(m - XPM - 1) * 512u, xs, BULK_AUX); SCHED(); }
      if (j == 6 && m == 4) {
#pragma unroll
        for (int i = 0; i < 3; ++i) { wb[i] = (unsigned)(kh * 1024 + i * 65536) + l16; asm volatile("" : "+v"(wb[i])); }
      }
      if (j == 6 && m != 23 && m != 27 && m != 31) {
        constexpr int n = NA0 - 1 - (30 - m) + (m < 27) + (m < 23);
        if (n >= 0) { LDA(n >= 0 ? n : 0); SCHED(); }
      }
      if (j == 6 && m >= PC0 && m < PC0 + 5) { p_chk(0, 0, 2 * (m - PC0)); SCHED(); if (m < PC0 + 4) p_chk(0, 0, 2 * (m - PC0) + 1); SCHED(); }
      if (j == 6 && m == PC0 - 1) { red_fin(6, 16); red_fin(6, 17); SCHED(); }
      sfor<0, 22>([&](auto K_) __attribute__((always_inline)) {
        constexpr int k = decltype(K_)::value;
        if (j > 1 && B.vp[k] == m + 32) { red_fin(j > 1 ? j - 2 : 0, k); SCHED(); }
        if (j > 0 && C.vp[k] == m) { red_fin(j > 0 ? j - 1 : 0, k); SCHED(); }
      });
      if (S.lf) ld();
    });
    STAMP(15);
#define NM0 0
#define GM 19
#define MB 24
    f32x4 acc[5];
    f32x4 xf[3], gf[3];
    unsigned go, oo, eo, ho, tw, tr, o1, ta, tb;
    unsigned s1 = (unsigned)((w ^ 4) * 3072 + 1024), s2 = (unsigned)(L_MISC + 128 * kh), s3 = (unsigned)(w * 3072 + 1024), s4 = s1 - (unsigned)kh * 1024u;
    unsigned os = (unsigned)((R * 64 + tau(rt, 0)) * D_ + 80 * c + 32 * kh) * 2u;
    unsigned long long om = kh ? ~0ull : 0xccccccccccccccccull;
    asm volatile("" : "+s"(s1), "+s"(s2), "+s"(s3), "+s"(s4), "+s"(os), "+s"(om));
#define PIN2(a, b) asm volatile("" : "+v"(a), "+v"(b))
    auto glue = [&](int k) {
      if (k == 0) { ta = l16 >> 6; tb = l16 & 48u; PIN2(ta, tb); }
      if (k == 1) { tr = ta * 112u + tb; oo = ta & 12u; PIN2(tr, oo); }
      if (k == 2) { tr += s1; oo += ta; PIN2(tr, oo); }
      if (k == 3) { oo = oo * 1280u + tb; ta = l16 >> 5; PIN2(oo, ta); }
      if (k == 4) { ta &= 24u; tb = l16 & 0xf0u; PIN2(ta, tb); }
      if (k == 5) { tw = tb * 7u + ta; go = (ta << 1) + s2; PIN2(tw, go); }
      if (k == 6) { tw += s1; asm volatile("v_cndmask_b32 %0, %1, -2.0, %2" : "=v"(o1) : "v"(oo), "s"(om)); asm volatile("" : "+v"(tw)); }
      if (k == 7) { eo = l16 + s3; ho = l16 + s4; PIN2(eo, ho); }
      if (k >= 8 && k < 14) {
        const unsigned x = xp[((k - 8) >> 1) % 3][k & 1];
        float lo = bflo(x), hi = bfhi(x);
        PIN2(lo, hi);
        xf[((k - 8) >> 1) % 3][2 * (k & 1)] = lo; xf[((k - 8) >> 1) % 3][2 * (k & 1) + 1] = hi;
      }
    };
    auto g3 = [&](auto M0_, auto M1_) __attribute__((always_inline)) {
      sfor<decltype(M0_)::value, decltype(M1_)::value>([&](auto M_) __attribute__((always_inline)) {
        constexpr int mi = decltype(M_)::value;
        constexpr int m = mi < 55 ? mi : mi < 64 ? 55 + 5 * ((mi - 55) / 3) + (mi - 55) % 3 : 58 + 5 * ((mi - 64) / 2) + (mi & 1), u2 = m / 5;
        const u32x4 fb = u2 < 8 ? P[0][u2 & 7] : P[1][u2 < 11 ? (u2 - 8) & 3 : (u2 - 7) & 7];
        if (m < 5) asm volatile("v_mfma_f32_16x16x32_bf16 %0, %1, %2, 0" : "=&v"(acc[m % 5]) : "v"(af[m % NAF]), "v"(fb));
        else asm volatile("v_mfma_f32_16x16x32_bf16 %0, %1, %2, %0" : "+v"(acc[m % 5]) : "v"(af[m % NAF]), "v"(fb));
        SCHED();
        if (mi == NM0) RBAR();
        if (mi >= NM0 && mi < NM0 + 3) dma1k(rs, lds + wl[(mi - NM0) % 3], l16, wg + wl[(mi - NM0) % 3]);
        if (mi >= 5 && mi < 19) p_chk(0, 1, mi - 5);
        if (mi >= GM && mi < GM + 14) glue(mi - GM);
        if (mi >= 47 && mi < 54) { p_chk(1, 1, 2 * (mi - 47)); SCHED(); p_chk(1, 1, 2 * (mi - 47) + 1); }
        if (mi >= NM0 && mi < 40 && NA0 + mi - NM0 < 60) LDA(NA0 + mi - NM0);
        if (mi >= 66 && mi < 69) *(f32x4*)(lds + eo + (mi - 66) * 1024) = acc[(mi - 66) % 3];
        SCHED();
      });
    };
    __builtin_amdgcn_s_setprio(3);
    asm volatile("s_nop 7");
    red_put(6, 3);
    SCHED();
    BAR();
#pragma unroll
    for (int i = 0; i < 8; ++i) red_get(i);
    SCHED();
#pragma unroll
    for (int k = 0; k < 20; ++k) red_fin(6, k < 16 ? k : k + 2);
    SCHED();
    __builtin_amdgcn_s_setprio(0);
    STAMPT(16);
#pragma unroll
    for (int k = 9; k < 14; ++k) p_chk(0, 0, k);
    SCHED();
    g3(IC<0>(), IC<MB>());
    STAMPT(22);
    DRAIN();
    RBAR();
    STAMPT(27);
#define MY 12
    if (MY == 0 || !kh) {
#pragma unroll
      for (int i = 0; i < 8; ++i) { ld_p(1, i); SCHED(); }
    }
    g3(IC<MB>(), IC<MB + MY>());
    if (MY > 0 && kh) {
#pragma unroll
      for (int i = 0; i < 8; ++i) { ld_p(1, i); SCHED(); }
    }
    STAMPT(26);
    g3(IC<MB + MY>(), IC<40>());
    STAMPT(24);
#pragma unroll
    for (int m = NA0 + 40 - NM0; m < 70; ++m) LDA(m);
#pragma unroll
    for (int x = 0; x < 3; ++x) gf[x] = *(const f32x4*)(lds + go + x * 64);
    SCHED();
    const rsrc_t ro = __builtin_amdgcn_make_buffer_rsrc((void*)p.out, 0, S_ * D_ * 2, 0x00020000);
    STAMPT(17);
#pragma unroll
    for (int k = 0; k < 14; ++k) p_chk(1, 0, k);
    asm volatile("s_nop 1");
    STAMPT(25);
    g3(IC<40>(), IC<70>());
    STAMPT(18);
    BAR();
    STAMPT(28);
    f32x4 o4[3];
    u32x4 ov[2];
#pragma unroll
    for (int j = 2; j >= 0; --j) o4[j] = *(const volatile LAS f32x4*)(lds + ho + j * 1024);
    asm volatile("" : "+v"(acc[2]), "+v"(acc[3]), "+v"(acc[4]));
    SCHED();
#pragma unroll
    for (int j = 2; j >= 0; --j) {
      const f32x4 v = __builtin_elementwise_fma(gf[j], o4[j] + acc[2 + j], xf[j]);
      *(volatile LAS u32x2*)(lds + tw + j * 32) = (u32x2){pack2(v[0], v[1]), pack2(v[2], v[3])};
      if (j < 2) ov[j] = *(const volatile LAS u32x4*)(lds + tr + j * 32);
    }
    __builtin_amdgcn_raw_buffer_store_b128(ov[1], ro, oo + 32u, os, 0);
    __builtin_amdgcn_raw_buffer_store_b128(ov[0], ro, o1, os, 0);
    if (bid == 0 && w == 5 && l8 == 0u) st4(rs, WS_EPOCH, tag);
  }
  STAMP(19);
}

extern "C" __device__ __attribute__((used, noinline, visibility("hidden"))) void mega_tail() {}

extern "C" void kernel_launch(void* const* d_in, const int* in_sizes, int n_in, void* d_out, int out_size,
                              void* d_ws, size_t ws_size, hipStream_t stream) {
  Params p;
  p.q = (const u16*)d_in[0];
  p.k = (const u16*)d_in[1];
  p.v = (const u16*)d_in[2];
  p.shift_k = (const u16*)d_in[3];
  p.ssm_k = (const u16*)d_in[4];
  p.ssm_D = (const u16*)d_in[5];
  p.w_in = (const u16*)d_in[7];
  p.w_sp = (const u16*)d_in[8];
  p.b_sp = (const u16*)d_in[9];
  p.w_out = (const u16*)d_in[10];
  p.cvec = (const u16*)d_in[11];
  p.w_mod = (const u16*)d_in[12];
  p.out = (u16*)d_out;
  p.ws = (char*)d_ws;
  mega<<<256, 512, 0, stream>>>(p);
}
```

```cpp
#include <hip/hip_runtime.h>
#include <stdint.h>

typedef unsigned short u16;
typedef __attribute__((ext_vector_type(8))) short bf16x8;
typedef __attribute__((ext_vector_type(4))) short bf16x4;
typedef __attribute__((ext_vector_type(4))) float f32x4;
typedef __attribute__((ext_vector_type(2))) float f32x2;
typedef __attribute__((ext_vector_type(4))) unsigned int u32x4;
typedef __attribute__((ext_vector_type(2))) unsigned int u32x2;
#define LAS __attribute__((address_space(3)))
typedef __amdgpu_buffer_rsrc_t rsrc_t;
typedef __attribute__((ext_vector_type(4))) short s16x4;

constexpr int S_ = 2048, D_ = 640, F_ = 1728, FH = 864, CB = 108;

constexpr unsigned WS_EPOCH = 0;
constexpr unsigned WS_FWI = 4096;
constexpr unsigned WS_FZ = WS_FWI + 384 * 128;
constexpr unsigned WS_FWO = WS_FZ + 256 * 128;
constexpr unsigned F4S = 896 * 128;
constexpr unsigned WS_CS = WS_FZ + 256 * 128;
constexpr unsigned WS_MOD = 131072;
constexpr unsigned WS_MODP = WS_MOD + 81920;
constexpr unsigned WS_ST1 = 393216;
constexpr unsigned WS_PART = 524288;
constexpr unsigned WS_CSP = WS_PART + 262144;
constexpr unsigned WS_FX = WS_CSP + 655360;
constexpr unsigned WS_X1B = WS_FX + 16 * 4 * 160 * 128;
constexpr unsigned WS_QB = WS_X1B + 2621440;
constexpr unsigned WS_KV3 = WS_QB + 2621440;
constexpr unsigned WS_MODQ = WS_KV3 + 1310720;
constexpr unsigned WBLK = 143360;
constexpr unsigned WS_WINF = WS_MODQ + 245760;
constexpr unsigned WS_WOUTF = WS_WINF + 16 * WBLK;
constexpr unsigned WS_Z2F = WS_WOUTF + 8 * WBLK;
constexpr unsigned WS_UF = WS_Z2F + 8 * 64 * 7168;
constexpr unsigned WS_F4 = WS_UF + 32 * 4 * 8 * 4096;
constexpr unsigned WS_END = WS_F4 + 4 * F4S;
constexpr unsigned WS_STAMP = 200u << 20;

constexpr int G_COPY = 4160;
constexpr int L_U = WBLK;
constexpr int U_CH = (256 + S_) * 2 + 32;
constexpr int L_FLG = L_U + 4 * U_CH;
constexpr int L_RT = 16384;
constexpr int L_CONST = WBLK;
constexpr int L_PST = L_CONST + 2048;
constexpr int L_Z = L_CONST + 5120;
constexpr int L_ST2 = L_Z + 14336;
constexpr int L_MISC = L_ST2 + 512;
constexpr int LDS_TOTAL = 163840;

#define AUX_T 610
#define RT2_T 360
#define CB_T 380
#define W_T 730
#define CS_T 830
#define Y_T 930
#define K_T 460
#ifndef M_T
#define M_T 520
#endif
#define WA_T 580
constexpr int SC1 = 16, NT = 2;
constexpr unsigned SPIN_CAP = 1u << 16;

__device__ __forceinline__ float bflo(unsigned x) { return __uint_as_float(x << 16); }
__device__ __forceinline__ float bfhi(unsigned x) { return __uint_as_float(x & 0xffff0000u); }
__device__ __forceinline__ unsigned f2bf(float x) {
  __bf16 b = (__bf16)x;
  return (unsigned)__builtin_bit_cast(unsigned short, b);
}
__device__ __forceinline__ float rbf(float x) { return bflo(f2bf(x)); }
typedef __attribute__((ext_vector_type(2))) __bf16 bf2_t;
__device__ __forceinline__ unsigned pack2(float lo, float hi) {
  f32x2 v = {lo, hi};
  return __builtin_bit_cast(unsigned, __builtin_convertvector(v, bf2_t));
}
__device__ __forceinline__ f32x2 rbf2(f32x2 v) {
  const unsigned p = __builtin_bit_cast(unsigned, __builtin_convertvector(v, bf2_t));
  return (f32x2){__uint_as_float(p << 16), __uint_as_float(p & 0xffff0000u)};
}
__device__ __forceinline__ f32x4 rbf4(f32x4 v) {
  const f32x2 a = rbf2((f32x2){v[0], v[1]}), b = rbf2((f32x2){v[2], v[3]});
  return (f32x4){a[0], a[1], b[0], b[1]};
}
template <int CTRL>
__device__ __forceinline__ float dpp_f(float v) {
  return __int_as_float(__builtin_amdgcn_update_dpp(0, __float_as_int(v), CTRL, 0xf, 0xf, true));
}
__device__ __forceinline__ float sum8(float v) {
  v += dpp_f<0xB1>(v);
  v += dpp_f<0x4E>(v);
  v += dpp_f<0x141>(v);
  return v;
}
__device__ __forceinline__ float sum16(float v) {
  v = sum8(v);
  v += dpp_f<0x140>(v);
  return v;
}
__device__ __forceinline__ float sum_rows(float v) {
  auto a = __builtin_amdgcn_permlane16_swap(__float_as_uint(v), __float_as_uint(v), false, false);
  v = __uint_as_float(a[0]) + __uint_as_float(a[1]);
  auto b = __builtin_amdgcn_permlane32_swap(__float_as_uint(v), __float_as_uint(v), false, false);
  return __uint_as_float(b[0]) + __uint_as_float(b[1]);
}
#define MFMA16(a, b, c) __builtin_amdgcn_mfma_f32_16x16x32_bf16(__builtin_bit_cast(bf16x8, a), __builtin_bit_cast(bf16x8, b), c, 0, 0, 0)
#define MFMA16K16(a, b, c) __builtin_amdgcn_mfma_f32_16x16x16bf16_1k(__builtin_bit_cast(bf16x4, a), __builtin_bit_cast(bf16x4, b), c, 0, 0, 0)
__device__ __forceinline__ u32x2 trd(const char* p) {
  return __builtin_bit_cast(u32x2, __builtin_amdgcn_ds_read_tr16_b64_v4i16((LAS s16x4*)p));
}
__device__ __forceinline__ u32x4 trfrag(const char* p, int pitch) {
  u32x2 a = trd(p), b = trd(p + 16 * pitch);
  return (u32x4){a[0], a[1], b[0], b[1]};
}
#define BAR() asm volatile("s_waitcnt lgkmcnt(0)\n\ts_barrier" ::: "memory")
#define DRAIN() do { __builtin_amdgcn_s_waitcnt(0x0F70); asm volatile("" ::: "memory"); } while (0)
#define SCHED() __builtin_amdgcn_sched_barrier(0)
template <int N> struct IC { static constexpr int value = N; };
template <int I, int N, class F>
__device__ __forceinline__ void sfor(F&& f) {
  if constexpr (I < N) { f(IC<I>()); sfor<I + 1, N>(f); }
}

__device__ __forceinline__ u32x4 ld16(rsrc_t r, unsigned off) { return __builtin_amdgcn_raw_buffer_load_b128(r, off, 0, SC1); }
__device__ __forceinline__ u32x2 ld8(rsrc_t r, unsigned off) { return __builtin_amdgcn_raw_buffer_load_b64(r, off, 0, SC1); }
__device__ __forceinline__ unsigned ld4(rsrc_t r, unsigned off) { return __builtin_amdgcn_raw_buffer_load_b32(r, off, 0, SC1); }
#define ST_AUX 16
__device__ __forceinline__ void st16(rsrc_t r, unsigned off, u32x4 v) { __builtin_amdgcn_raw_buffer_store_b128(v, r, off, 0, ST_AUX); }
__device__ __forceinline__ void st8(rsrc_t r, unsigned off, u32x2 v) { __builtin_amdgcn_raw_buffer_store_b64(v, r, off, 0, SC1); }
__device__ __forceinline__ void st_flag4(rsrc_t r, unsigned off, unsigned tag, int l) {
  if (l < 32) st16(r, WS_F4 - WS_FWI + off + (unsigned)(l >> 3) * F4S + (unsigned)(l & 7) * 16u, (u32x4){tag, tag, tag, tag});
}
__device__ __forceinline__ void st4(rsrc_t r, unsigned off, unsigned v) { __builtin_amdgcn_raw_buffer_store_b32(v, r, off, 0, SC1); }
#define BULK_AUX 16
__device__ __forceinline__ u32x4 ld16p(rsrc_t r, unsigned off) { return __builtin_amdgcn_raw_buffer_load_b128(r, off, 0, BULK_AUX); }
__device__ __forceinline__ u32x2 ld8p(rsrc_t r, unsigned off) { return __builtin_amdgcn_raw_buffer_load_b64(r, off, 0, BULK_AUX); }
__device__ __forceinline__ void dma1k(rsrc_t r, char* lds_dst, unsigned l16, unsigned soff) {
  __builtin_amdgcn_raw_ptr_buffer_load_lds(r, (LAS void*)lds_dst, 16, l16, soff, 0, BULK_AUX);
}

#define STAMP(i)
#define STAMPF(i)
#define STAMPT(i) STAMP(i)

template <int N, int SLP>
__device__ __forceinline__ void wait_lines(rsrc_t r, const unsigned (&off)[N], unsigned tag) {
  bool need[N];
#pragma unroll
  for (int i = 0; i < N; ++i) need[i] = off[i] != ~0u;
  _Pragma("unroll 1") for (unsigned spins = 0; spins < SPIN_CAP; ++spins) {
    unsigned v[N];
#pragma unroll
    for (int i = 0; i < N; ++i) {
      v[i] = tag;
      if (need[i]) v[i] = ld4(r, off[i]);
    }
    bool any = false;
#pragma unroll
    for (int i = 0; i < N; ++i) { need[i] = v[i] != tag; any = any || need[i]; }
    if (!__any(any)) break;
    __builtin_amdgcn_s_sleep(SLP);
  }
  asm volatile("" ::: "memory");
}

__device__ __forceinline__ int tau(int rt, int n) { return 32 * (rt >> 1) + 8 * (n >> 2) + 4 * (rt & 1) + (n & 3); }
__device__ __forceinline__ int psi(int n) { return 2 * (n & 7) + (n >> 3); }
#define QX_CM(d, s0, s1, P) "v_cndmask_b32_dpp " d ", " s0 ", " s1 ", vcc quad_perm:" P " row_mask:0xf bank_mask:0xf\n\t"
#define QX_BODY(M, P) \
  asm("s_mov_b32 vcc_lo, " M "\n\ts_mov_b32 vcc_hi, " M "\n\t" \
      QX_CM("%0", "%12", "%8", P) "v_xor_b32 %4, %8, %0\n\t" QX_CM("%1", "%13", "%9", P) "v_xor_b32 %5, %9, %1\n\t" \
      QX_CM("%2", "%14", "%10", P) "v_xor_b32 %6, %10, %2\n\t" QX_CM("%3", "%15", "%11", P) "v_xor_b32 %7, %11, %3" \
      : "=&v"(t[0]), "=&v"(t[1]), "=&v"(t[2]), "=&v"(t[3]), "=&v"(u[0]), "=&v"(u[1]), "=&v"(u[2]), "=&v"(u[3]) \
      : "v"(a[0]), "v"(a[1]), "v"(a[2]), "v"(a[3]), "v"(b[0]), "v"(b[1]), "v"(b[2]), "v"(b[3]) : "vcc")
template <int BIT>
__device__ __forceinline__ void quad_xchg3(u32x4& a, u32x4& b) {
  unsigned t[4], u[4];
  if (BIT == 0) QX_BODY("0x55555555", "[1,0,3,2]");
  else QX_BODY("0x33333333", "[2,3,0,1]");
  a = (u32x4){t[0], t[1], t[2], t[3]};
#pragma unroll
  for (int d = 0; d < 4; ++d) b[d] ^= (unsigned)__builtin_amdgcn_update_dpp(0, (int)u[d], BIT == 0 ? 0xB1 : 0x4E, 0xf, 0xf, true);
}
__device__ __forceinline__ void quad_transpose(u32x4& x0, u32x4& x1, u32x4& x2, u32x4& x3) {
  quad_xchg3<0>(x0, x1);
  quad_xchg3<0>(x2, x3);
  quad_xchg3<1>(x0, x2);
  quad_xchg3<1>(x1, x3);
  asm volatile("" : "+v"(x0), "+v"(x1), "+v"(x2), "+v"(x3));
}

struct Params {
  const u16 *q, *k, *v, *shift_k, *ssm_k, *ssm_D, *w_in, *w_sp, *b_sp, *w_out, *cvec, *w_mod;
  u16* out;
  char* ws;
};

__global__ __launch_bounds__(512, 2) void mega(Params p) {
  __shared__ __attribute__((aligned(16))) char lds[LDS_TOTAL];
  {
    unsigned long long pc0;
    unsigned vt;
    asm volatile("v_readfirstlane_b32 vcc_lo, %3\n\ts_cmpk_lt_u32 %2, 192\n\ts_cbranch_scc1 4f\n\ts_cmpk_lt_u32 vcc_lo, 64\n\ts_cbranch_scc0 4f\n\t"
                 "s_lshr_b32 vcc_hi, %2, 3\n\ts_sub_u32 vcc_hi, vcc_hi, 24\n\ts_lshl_b32 vcc_hi, vcc_hi, 13\n\t"
                 "v_lshlrev_b32 %1, 7, %3\n\tv_add_u32 %1, vcc_hi, %1\n\ts_getpc_b64 %0\n.Lmega_here:\n\t"
                 "v_min_u32 %1, mega_tail-.Lmega_here-4, %1\n\ts_movk_i32 m0, 0x2000\n\ts_nop 1\n\tglobal_load_lds_dword %1, %0\n4:\n\t"
                 "s_cmpk_lt_u32 %2, 160\n\ts_cbranch_scc0 1f\n\ts_cmpk_lt_u32 vcc_lo, 256\n\ts_cbranch_scc1 3f\n\t"
                 "s_call_b64 vcc, .Ltouch1\n\ts_branch 3f\n1:\n\t"
                 "s_cmpk_lt_u32 vcc_lo, 256\n\ts_cbranch_scc1 2f\n\ts_cmpk_lt_u32 %2, 224\n\ts_cbranch_scc0 2f\n\ts_call_b64 vcc, .Ltouch3\n\ts_branch 3f\n"
                 "2:\n\ts_call_b64 vcc, .Ltouch2\n3:"
                 : "=&s"(pc0), "=&v"(vt) : "s"(blockIdx.x), "v"(threadIdx.x) : "vcc", "scc", "memory");
  }
  SCHED();
  const int tid = threadIdx.x, l = tid & 63;
  const int w = __builtin_amdgcn_readfirstlane(tid >> 6);
  const int bid = blockIdx.x, xcd = bid & 7, idx = bid >> 3;
#define TOUCH_AT(n) asm volatile("s_branch 1f\n.Ltouch" #n ":\n\ts_setpc_b64 vcc\n1:")
  char* const ws = p.ws;
  asm volatile("" ::"s"(p.q), "s"(p.k), "s"(p.v), "s"(p.shift_k), "s"(p.ssm_k), "s"(p.ssm_D), "s"(p.w_in), "s"(p.w_sp), "s"(p.b_sp),
               "s"(p.w_out), "s"(p.cvec), "s"(p.w_mod), "s"(p.out), "s"(p.ws));
  unsigned epoch, tag, t_start;
  unsigned long long t64;
  asm volatile("s_load_dword %0, %2, 0x0\n\ts_memrealtime %1" : "=&s"(epoch), "=&s"(t64) : "s"(ws));
#define TAG_READY() do { SCHED(); \
    asm volatile("s_waitcnt lgkmcnt(0)\n\ts_mul_i32 %0, %2, 0x9e3779b1\n\ts_add_i32 %0, %0, 0x9e3779b9\n\ts_mov_b64 %1, %3" \
                 : "=&s"(tag), "=&s"(t64r) : "s"(epoch), "s"(t64) : "scc"); t_start = (unsigned)t64r; SCHED(); } while (0)
  unsigned long long t64r;
  const rsrc_t rs = __builtin_amdgcn_make_buffer_rsrc(ws, 0, WS_END, 0x00020000);
  const unsigned l16 = (unsigned)l * 16u;
  STAMP(0);
  const bool is_conv = idx < 20;
  const int quad = xcd * 20 + idx;
  const int R = is_conv ? (xcd & 3) * 5 + (idx >> 2) : (xcd & 3) * 3 + 15 + (idx >> 2);
  const int c = (xcd >> 2) * 4 + (idx & 3);
  const unsigned fwi = WS_F4 + (unsigned)(xcd & 3) * F4S, fz = fwi + (WS_FZ - WS_FWI);
  const int n16 = l & 15, h4 = l >> 4;
  u32x4 wsp[4][8];
#define ROTC 2
#define WAUX 0
  const int rotg = ((idx >> 4) ^ ((ROTC & 2) ? idx >> 1 : 0)) & 1, rotb = (idx >> 2) & 3, rotr = (ROTC & 1) ? (idx & 1) * 2 : 0;
  const rsrc_t rsp = __builtin_amdgcn_make_buffer_rsrc((void*)p.w_sp, 0, S_ * S_ * 2, 0x00020000);
  const unsigned wsp_vo = (unsigned)(((l >> 2) & 3) * 8 * 4096 + h4 * 64 + ((l + rotb) & 3) * 16);
#define LDW(k) LDW_(k)
#define WSP_TR(g) WSP_TR_(g)
#define WROW R
#define LDW_(k) wsp[((k) >> 2) & 3][((k) >> 4) * 4 + ((k) & 3)] = __builtin_amdgcn_raw_buffer_load_b128(rsp, wvo, \
    (unsigned)((WROW * 64 + 16 * ((((k) >> 2) & 2) ^ rotr) + 4 * (((k) >> 2) & 1) + ((k) & 3)) * 4096 + w * 512 + ((((k) >> 4) ^ rotg) * 256)), WAUX)
#define WSP_TR_(g) quad_transpose(wsp[(g) & 3][((g) >> 2) * 4], wsp[(g) & 3][((g) >> 2) * 4 + 1], wsp[(g) & 3][((g) >> 2) * 4 + 2], wsp[(g) & 3][((g) >> 2) * 4 + 3])
  const rsrc_t rwi = __builtin_amdgcn_make_buffer_rsrc((void*)p.w_in, 0, D_ * F_ * 2, 0x00020000);
  const rsrc_t rwo = __builtin_amdgcn_make_buffer_rsrc((void*)p.w_out, 0, FH * D_ * 2, 0x00020000);
  const rsrc_t rq = __builtin_amdgcn_make_buffer_rsrc((void*)p.q, 0, S_ * D_ * 2, 0x00020000);
  auto hold = [&](unsigned tt) { while ((unsigned)__builtin_amdgcn_s_memrealtime() - t_start < tt) __builtin_amdgcn_s_sleep(2); };
  volatile LAS unsigned* lf = (volatile LAS unsigned*)(lds + L_FLG);
  auto lf_set = [&](int i) { asm volatile("s_waitcnt lgkmcnt(0)" ::: "memory"); if (l == 0) lf[i] = tag; };
  auto lf_wait = [&](int i0, int n) {
    _Pragma("unroll 1") for (unsigned spins = 0; spins < (SPIN_CAP << 4); ++spins) {
      const unsigned v = lf[i0 + (l & (n - 1))];
      if (__all(v == tag)) break;
      __builtin_amdgcn_s_sleep(1);
    }
    asm volatile("" ::: "memory");
  };
  auto xflag = [&](int s) {
    st16(rs, WS_FX + (unsigned)(((4 * s + (w & 3)) * 4 + ((l >> 3) & 3)) * 160 + quad) * 128u + (unsigned)(l & 7) * 16u, (u32x4){tag, tag, tag, tag});
    SCHED();
  };
  auto load_b2 = [&]() {
#pragma unroll
    for (int k = 0; k < 35; ++k) {
      int i = k + (idx >> 2) * 4;
      i = (i >= 35 ? i - 35 : i) + 35 * w;
      dma1k(rs, lds + i * 1024, l16, WS_WINF + (unsigned)(8 + c) * WBLK + (unsigned)i * 1024u);
    }
  };
  char* const wo_st = lds + 122880;
  auto wo_ld = [&]() {
    const int U = R < 28 ? R : 0, u2 = U >> 1, fi = (u2 + 1) % 3;
    const int c0 = 4 * (U & 1) + (u2 < 8 ? u2 >> 1 : u2 < 11 ? 0 : 2);
    const int b0 = u2 < 8 ? CB * c0 + 2 * (u2 & 1) + 96 : CB * (c0 + (fi >> 1)) + 48 + 2 * (fi & 1), d1 = u2 < 8 ? -96 : 16, d2 = d1 + 16;
    const int b3 = u2 < 8 ? b0 - 64 : CB * (c0 + 1) + 50 + 16 * fi;
#pragma unroll
    for (int r = 0; r < 5; ++r) {
      const int g = r * 64 + l, rho = g / 10;
      const int fb = (rho & 16) ? ((rho & 2) ? b3 : b0 + d2) : ((rho & 2) ? b0 + d1 : b0);
      __builtin_amdgcn_raw_ptr_buffer_load_lds(rwo, (LAS void*)(wo_st + r * 1024), 16, (unsigned)((fb + (rho & 13)) * (D_ * 2) + (g % 10) * 16 + c * 160), 0, 0, 0);
    }
  };
  auto rt_ld = [&](int i0, char* st) {
    const int cb = (i0 < 160 ? 8 : 0) + (i0 & 7), Q = (i0 < 160 ? i0 : i0 - 160) >> 3;
#pragma unroll
    for (int r = 0; r < 7; ++r) {
      const int g = r * 64 + l;
      __builtin_amdgcn_raw_ptr_buffer_load_lds(rwi, (LAS void*)(st + r * 1024), 16, (unsigned)((g / 14) * (F_ * 2) + (g % 14) * 16 + Q * 32 * F_ * 2 + cb * CB * 2),
                                               0, 0, NT);
    }
  };
  auto rt_job = [&](int i0, char* st) {
    const int cb = (i0 < 160 ? 8 : 0) + (i0 & 7), Q = (i0 < 160 ? i0 : i0 - 160) >> 3;
    const int col = 640 * (l >> 5) + 32 * Q + (l & 31);
    const unsigned po = WS_MODP + (unsigned)col * 8u;
    u32x2 pp8[8];
    hold(CB_T);
    _Pragma("unroll 1") for (unsigned spins = 0; spins < SPIN_CAP; ++spins) {
      unsigned dd = 0u;
#pragma unroll
      for (int i = 0; i < 8; ++i) pp8[i] = ld8(rs, po + (unsigned)i * 10240u);
#pragma unroll
      for (int i = 0; i < 8; ++i) dd |= pp8[i][0] ^ tag;
      if (__all(dd == 0u)) break;
      __builtin_amdgcn_s_sleep(4);
    }
    STAMPF(9);
    {
      float t = 0.f;
#pragma unroll
      for (int i = 0; i < 8; ++i) t += __uint_as_float(pp8[i][1]);
      if (l >= 32) *(float*)(st + 7168 + (l - 32) * 4) = rbf(1.0f + rbf(t));
      else *(u16*)(st + 7296 + l * 2) = (u16)f2bf(t);
    }
    DRAIN();
    asm volatile("s_waitcnt lgkmcnt(0)" ::: "memory");
    const f32x4 ca = *(const f32x4*)(st + 7168 + h4 * 16), cc = *(const f32x4*)(st + 7168 + 64 + h4 * 16);
    const char* bp = st + (4 * h4 + (n16 >> 2)) * 224 + (n16 & 3) * 8;
    u32x4 x[7], o[7];
#pragma unroll
    for (int j = 0; j < 7; ++j) {
      x[j] = trfrag(bp + j * 32, 224);
      o[j] = (u32x4){pack2(bflo(x[j][0]) * ca[0], bfhi(x[j][0]) * ca[1]), pack2(bflo(x[j][1]) * ca[2], bfhi(x[j][1]) * ca[3]),
                     pack2(bflo(x[j][2]) * cc[0], bfhi(x[j][2]) * cc[1]), pack2(bflo(x[j][3]) * cc[2], bfhi(x[j][3]) * cc[3])};
      if (j == 6 && n16 == 12) o[j] = (u32x4){0x3F803F80u, 0x3F803F80u, 0x3F803F80u, 0x3F803F80u};
      st16(rs, WS_WINF + (unsigned)cb * WBLK + (unsigned)(Q * 7 + j) * 1024u + l16, o[j]);
    }
    STAMPF(11);
    const u32x2 e0 = *(const u32x2*)(st + 7296 + h4 * 8), e1 = *(const u32x2*)(st + 7328 + h4 * 8);
    f32x4 d[2] = {{0.f, 0.f, 0.f, 0.f}, {0.f, 0.f, 0.f, 0.f}};
#pragma unroll
    for (int j = 0; j < 7; ++j) {
      const unsigned one = n16 == 4 * (j & 3) ? 0x3F803F80u : 0u;
      const bool sr = n16 == 4 * (j & 3) + 1;
      d[j >> 2] = MFMA16(((u32x4){one, one, one, one}), o[j], d[j >> 2]);
      d[j >> 2] = MFMA16(((u32x4){sr ? e0[0] : 0u, sr ? e0[1] : 0u, sr ? e1[0] : 0u, sr ? e1[1] : 0u}), x[j], d[j >> 2]);
    }
#pragma unroll
    for (int m = 0; m < 2; ++m)
      st16(rs, WS_CSP + (unsigned)((cb * 20 + Q) * 2 + m) * 1024u + l16, (u32x4){tag, __float_as_uint(d[m][0]), tag, __float_as_uint(d[m][1])});
    STAMPF(13);
    asm volatile("s_waitcnt vmcnt(2)" ::: "memory");
    st_flag4(rs, WS_FWI + (unsigned)(cb * 20 + Q) * 128u, tag, l);
  };

  auto red8 = [&](float v) { v += dpp_f<0x128>(v); return sum_rows(v); };
  const rsrc_t rwm = __builtin_amdgcn_make_buffer_rsrc((void*)p.w_mod, 0, D_ * 3840 * 2, 0x00020000);
  auto gemv_load = [&](u32x4* pre, int blk, int sl, int np) {
    pre[10][0] = *(const unsigned*)(p.cvec + 8 * np * sl + 2 * (l < 4 * np ? l : 0));
#pragma unroll
    for (int pp = 0; pp < np; ++pp)
      pre[pp] = __builtin_amdgcn_raw_buffer_load_b128(rwm, (unsigned)((l >> 3) * 7680 + (l & 7) * 16), (unsigned)((8 * np * sl + pp * 8) * 7680 + blk * 128), NT);
  };
  auto gemv_fin = [&](float* acc, const u32x4* pre, int np, char* scr) {
    const unsigned cv2 = pre[10][0];
    unsigned* svt = (unsigned*)scr + w * 96;
#pragma unroll
    for (int e = 0; e < 2; ++e) {
      const int row = 2 * l + e;
      const float cv = e ? bfhi(cv2) : bflo(cv2);
      if (l < 4 * np) svt[(row & 7) * 12 + (row >> 3)] = __float_as_uint(rbf(cv * __builtin_amdgcn_rcpf(1.0f + __expf(-cv))));
    }
    asm volatile("s_waitcnt lgkmcnt(0)" ::: "memory");
    u32x4 sv[3];
#pragma unroll
    for (int i = 0; i < (np + 3) / 4; ++i) sv[i] = *(const u32x4*)(svt + (l >> 3) * 12 + 4 * i);
    SCHED();
#pragma unroll
    for (int e = 0; e < 8; ++e) acc[e] = 0.f;
#pragma unroll
    for (int pp = 0; pp < np; ++pp) {
      const unsigned sh = sv[pp >> 2][pp & 3], sl2 = sh >> 16;
#pragma unroll
      for (int e = 0; e < 4; ++e) {
        const unsigned x = pre[pp][e];
        acc[2 * e] = __builtin_amdgcn_fdot2_f32_bf16(__builtin_bit_cast(bf2_t, x), __builtin_bit_cast(bf2_t, sl2), acc[2 * e], false);
        acc[2 * e + 1] = __builtin_amdgcn_fdot2_f32_bf16(__builtin_bit_cast(bf2_t, x), __builtin_bit_cast(bf2_t, sh), acc[2 * e + 1], false);
      }
    }
#pragma unroll
    for (int e = 0; e < 8; ++e) acc[e] = red8(acc[e]);
  };
  auto gemv_put = [&](const float* acc, unsigned off) {
    float v = acc[0];
#pragma unroll
    for (int e = 1; e < 8; ++e) { v = (l >> 3) == e ? acc[e] : v; asm("" : "+v"(v)); }
    st8(rs, off + (unsigned)l * 8u, (u32x2){tag, (unsigned)__builtin_amdgcn_ds_bpermute(((l & 7) * 8 + (l >> 3)) * 4, (int)__float_as_uint(v))});
  };
  typedef const __attribute__((address_space(4))) unsigned* cptr_t;

  if (__builtin_expect(is_conv, 1)) {
    if (w >= 4) {
      TOUCH_AT(1);
      const int pw = w - 4;
      const rsrc_t ra = __builtin_amdgcn_make_buffer_rsrc((void*)((idx & 2) ? p.v : p.k), 0, S_ * D_ * 2, 0x00020000);
      const rsrc_t rb = __builtin_amdgcn_make_buffer_rsrc((void*)((idx & 2) ? p.k : p.v), 0, S_ * D_ * 2, 0x00020000);
      const rsrc_t rk = __builtin_amdgcn_make_buffer_rsrc((void*)p.k, 0, S_ * D_ * 2, 0x00020000);
      const unsigned vo = (unsigned)((128 * pw + 2 * l) * (D_ * 2));
      const unsigned so0 = (unsigned)((idx & 1) * (D_ * 2) + quad * 8), so1 = (unsigned)((1 - (idx & 1)) * (D_ * 2) + quad * 8);
      u32x2 g[3][4], kh;
      auto gl = [&](int s, int j0, int j1) {
        __builtin_amdgcn_s_setprio(0);
#pragma unroll
        for (int j = j0; j < j1; ++j) {
          g[s][j] = __builtin_amdgcn_raw_buffer_load_b64(j < 2 ? ra : rb, vo, ((j & 1) ? so1 : so0) + (unsigned)(s * 512 * D_ * 2), 0);
          SCHED();
        }
        __builtin_amdgcn_s_setprio(3);
      };
      const cptr_t sp = (cptr_t)(p.shift_k + quad * 16);
      unsigned spv[8];
#pragma unroll
      for (int i = 0; i < 8; ++i) spv[i] = sp[i];
      __builtin_amdgcn_s_sleep(3);
      gl(0, 0, 4);
      {
        const int row = 512 * (l >> 2) + 128 * pw - 3 + (l & 3);
        kh = __builtin_amdgcn_raw_buffer_load_b64(rk, (l >= 12 || (l & 3) == 3 || row < 0) ? 0x80000000u : (unsigned)(row * D_ * 2 + quad * 8), 0, 0);
      }
      TAG_READY();
      f32x2 wt[2][4];
#pragma unroll
      for (int d = 0; d < 2; ++d) {
        wt[d][0] = (f32x2){bflo(spv[4 * d]), bflo(spv[4 * d + 2])}; wt[d][1] = (f32x2){bfhi(spv[4 * d]), bfhi(spv[4 * d + 2])};
        wt[d][2] = (f32x2){bflo(spv[4 * d + 1]), bflo(spv[4 * d + 3])}; wt[d][3] = (f32x2){bfhi(spv[4 * d + 1]), bfhi(spv[4 * d + 3])};
        asm volatile("" : "+s"(wt[d][0]), "+s"(wt[d][1]), "+s"(wt[d][2]), "+s"(wt[d][3]));
      }
      SCHED();
      {
        const int j = idx * 4 + pw;
        const unsigned o = (unsigned)((512 + 64 * (j >> 2) + l) * (D_ * 2) + (((160 * xcd) >> 7) + ((j >> 1) & 1)) * 128);
        __builtin_amdgcn_raw_ptr_buffer_load_lds((j & 1) ? ra : rb, (LAS void*)(lds + 98304), 4, j < 64 ? o : 0x80000000u, 0, 0, 0);
      }
      SCHED();
      gl(1, 0, 4);
      STAMP(21);
      char* up = lds + L_U + 512 + 256 * pw + (l >> 2) * 16 + (3 - (l & 3)) * 4;
      auto kv = [&](auto S__, auto N__) {
        constexpr int s = decltype(S__)::value;
        u32x2* x = g[s];
        SCHED();
        asm volatile("s_waitcnt vmcnt(%5)" : "+v"(x[0]), "+v"(x[1]), "+v"(x[2]), "+v"(x[3]), "+v"(kh) : "n"(decltype(N__)::value));
        if (s == 2) STAMP(27);
        if (idx & 1) { asm volatile(""); const u32x2 t = x[0], u = x[2]; x[0] = x[1]; x[1] = t; x[2] = x[3]; x[3] = u; }
        asm volatile("" : "+v"(x[0]), "+v"(x[1]), "+v"(x[2]), "+v"(x[3]));
        if (idx & 2) { asm volatile(""); const u32x2 t = x[0], u = x[1]; x[0] = x[2]; x[1] = x[3]; x[2] = t; x[3] = u; }
        asm volatile("" : "+v"(x[0]), "+v"(x[1]), "+v"(x[2]), "+v"(x[3]));
        f32x2 kf[2][5], o[2][2];
#pragma unroll
        for (int d = 0; d < 2; ++d) {
          unsigned kk[5];
          kk[3] = x[0][d]; kk[4] = x[1][d];
          kk[2] = __builtin_amdgcn_update_dpp((unsigned)__builtin_amdgcn_readlane(kh[d], 4 * s + 2), kk[4], 0x138, 0xf, 0xf, false);
          kk[1] = __builtin_amdgcn_update_dpp((unsigned)__builtin_amdgcn_readlane(kh[d], 4 * s + 1), kk[3], 0x138, 0xf, 0xf, false);
          kk[0] = __builtin_amdgcn_update_dpp((unsigned)__builtin_amdgcn_readlane(kh[d], 4 * s), kk[2], 0x138, 0xf, 0xf, false);
#pragma unroll
          for (int j = 0; j < 5; ++j) kf[d][j] = (f32x2){bflo(kk[j]), bfhi(kk[j])};
        }
#pragma unroll
        for (int t = 0; t < 5; ++t) {
#pragma unroll
          for (int i = 0; i < 4; ++i) {
            const int d = i >> 1, r = i & 1;
            if (t == 4) o[d][r] *= (f32x2){bflo(x[2 + r][d]), bfhi(x[2 + r][d])};
            else if (t == 0) o[d][r] = kf[d][r + 3] * wt[d][0];
            else o[d][r] = __builtin_elementwise_fma(kf[d][r + 3 - t], wt[d][t], o[d][r]);
          }
          asm volatile("" : "+v"(o[0][0]), "+v"(o[0][1]), "+v"(o[1][0]), "+v"(o[1][1]));
        }
#pragma unroll
        for (int i = 0; i < 4; ++i) *(unsigned*)(up + i * U_CH + s * 1024) = pack2(o[i >> 1][1][i & 1], o[i >> 1][0][i & 1]);
        asm volatile("" ::: "memory");
        if (l == 0) lf[4 * s + pw] = tag;
        SCHED();
      };
      const char* yr = lds + L_U + (n16 >> 2) * U_CH + (128 * pw + 32 * h4 + 8 * (n16 & 1) + 4 * ((n16 >> 1) & 1)) * 2;
      const unsigned yo = (unsigned)(8 * pw + (l >> 3)) * 20480u + (unsigned)quad * 128u + (unsigned)(l & 7) * 16u;
      const unsigned so = WS_ST1 + (unsigned)xcd * 16384u + (unsigned)pw * 1024u + l16;
      const unsigned mo = WS_MODQ + (unsigned)((n16 < 12 ? n16 >> 2 : 0) * D_ + quad * 4 + (l & 3)) * 8u + (unsigned)h4 * 40960u;
      u32x4 sv[4], qv[4];
      u32x2 mv[2];
      auto ld_e = [&]() {
#pragma unroll
        for (int s = 0; s < 4; ++s) sv[s] = ld16(rs, so + (unsigned)s * 4096u);
        SCHED();
      };
      auto s_ok = [&]() {
        unsigned d = 0u;
#pragma unroll
        for (int s = 0; s < 4; ++s) d |= (sv[s][1] ^ tag) | (sv[s][3] ^ tag);
        return __all(d == 0u);
      };
      kv(IC<0>(), IC<5>());
      STAMP(23);
#define G2A 4
#define EPOS 0
      gl(2, 0, G2A);
      kv(IC<1>(), IC<G2A>());
      gl(2, G2A, 4);
      STAMPF(9);
      u32x4 k3[2];
      const unsigned k3o = WS_KV3 + (unsigned)(16 * pw + (l >> 2)) * 20480u + (unsigned)(((l >> 1) & 1) * 10240 + (2 * quad + (l & 1)) * 16);
      if (EPOS == 2) kv(IC<2>(), IC<0>());
      hold(K_T);
      k3[0] = ld16(rs, k3o); k3[1] = ld16(rs, k3o + 5120u);
      SCHED();
      if (EPOS != 1) ld_e();
      if (EPOS != 2) kv(IC<2>(), IC<EPOS ? 2 : 6>());
      if (EPOS == 1) ld_e();
      STAMPF(11);
      auto k_ok = [&]() { return __all(((k3[0][1] ^ tag) | (k3[0][3] ^ tag) | (k3[1][1] ^ tag) | (k3[1][3] ^ tag)) == 0u); };
      asm volatile("" : "+v"(k3[0]), "+v"(k3[1]));
      if (!k_ok()) {
        _Pragma("unroll 1") for (unsigned spins = 0; spins < SPIN_CAP; ++spins) {
          __builtin_amdgcn_s_sleep(4);
          k3[0] = ld16(rs, k3o); k3[1] = ld16(rs, k3o + 5120u);
          if (k_ok()) break;
        }
      }
      *(u32x4*)(lds + L_U + (2 * (l & 1) + ((l >> 1) & 1)) * U_CH + 512 + (1536 + 128 * pw + 8 * (l >> 2)) * 2) = (u32x4){k3[0][0], k3[0][2], k3[1][0], k3[1][2]};
      asm volatile("" ::: "memory");
      if (l == 0) lf[12 + pw] = tag;
      __builtin_amdgcn_s_setprio(3);
      STAMPF(18);
      asm volatile("" : "+v"(sv[0]), "+v"(sv[1]), "+v"(sv[2]), "+v"(sv[3]));
      if (!s_ok()) {
        _Pragma("unroll 1") for (unsigned spins = 0; spins < SPIN_CAP; ++spins) {
          __builtin_amdgcn_s_sleep(4);
          ld_e();
          if (s_ok()) break;
        }
      }
      asm volatile("" ::: "memory");
      SCHED();
      STAMPF(17);
#pragma unroll
      for (int s = 0; s < 4; ++s) qv[s] = ld16p(rs, WS_QB + yo + (unsigned)s * (32u * 20480u));
      hold(M_T);
      mv[0] = ld8(rs, mo); mv[1] = ld8(rs, mo + 20480u);
      SCHED();
      STAMP(2);
      u32x2 yv[4][2];
      auto ld_y = [&](int s) {
#pragma unroll
        for (int e = 0; e < 2; ++e) yv[s][e] = trd(yr + 1024 * s + 32 * e);
      };
      lf_wait(20, 4);
      ld_y(0); ld_y(1);
      SCHED();
      asm volatile("" : "+v"(mv[0]), "+v"(mv[1]), "+v"(qv[0]), "+v"(qv[1]), "+v"(qv[2]), "+v"(qv[3]));
      if (!__all(((mv[0][0] ^ tag) | (mv[1][0] ^ tag)) == 0u)) {
        _Pragma("unroll 1") for (unsigned spins = 0; spins < SPIN_CAP; ++spins) {
          __builtin_amdgcn_s_sleep(4);
          mv[0] = ld8(rs, mo); mv[1] = ld8(rs, mo + 20480u);
          if (__all(((mv[0][0] ^ tag) | (mv[1][0] ^ tag)) == 0u)) break;
        }
      }
      STAMPF(13);
      f32x4 al, be;
      {
        const float mgf = rbf(sum_rows(__uint_as_float(mv[0][1]) + __uint_as_float(mv[1][1])));
        auto rl = [&](int i) { return __uint_as_float(__builtin_amdgcn_readlane(__float_as_uint(mgf), i)); };
        const f32x4 sa = {rl(0), rl(1), rl(2), rl(3)}, ca = {rl(4), rl(5), rl(6), rl(7)}, ga = {rl(8), rl(9), rl(10), rl(11)};
        al = rbf4(ca + 1.0f) * ga;
        be = sa * ga;
      }
      f32x4 qf[2][2], P[2][2];
      auto xpre = [&](int s) {
#pragma unroll
        for (int e = 0; e < 2; ++e) {
          const float r1 = __builtin_amdgcn_rcpf(bfhi(sv[s][2 * e])), m1 = -bflo(sv[s][2 * e]) * r1;
          const f32x4 qq = {bflo(qv[s][2 * e]), bfhi(qv[s][2 * e]), bflo(qv[s][2 * e + 1]), bfhi(qv[s][2 * e + 1])};
          qf[s & 1][e] = qq;
          P[s & 1][e] = __builtin_elementwise_fma(__builtin_elementwise_fma(qq, (f32x4){r1, r1, r1, r1}, (f32x4){m1, m1, m1, m1}), al, be);
        }
        asm volatile("" : "+v"(qf[s & 1][0]), "+v"(qf[s & 1][1]), "+v"(P[s & 1][0]), "+v"(P[s & 1][1]));
      };
      auto xfin = [&](int s) {
        u32x4 o;
#pragma unroll
        for (int e = 0; e < 2; ++e) {
          const u32x2 y = yv[s][e];
          const f32x4 yf = {bflo(y[0]), bfhi(y[0]), bflo(y[1]), bfhi(y[1])};
          const f32x4 x = __builtin_elementwise_fma(P[s & 1][e], yf, qf[s & 1][e]);
          o[2 * e] = pack2(x[0], x[1]);
          o[2 * e + 1] = pack2(x[2], x[3]);
        }
        st16(rs, WS_X1B + yo + (unsigned)s * (32u * 20480u), o);
      };
      xpre(0); xfin(0);
      xpre(1); xfin(1);
      SCHED();
      xpre(2); xpre(3);
      lf_wait(24, 4);
      __builtin_amdgcn_s_setprio(3);
      ld_y(2); ld_y(3);
      xfin(2); xfin(3);
      STAMP(28);
      asm volatile("s_waitcnt vmcnt(2)" ::: "memory");
      xflag(l >> 5);
      asm volatile("s_waitcnt vmcnt(1)" ::: "memory");
      xflag(2 + (l >> 5));
      STAMP(5);
    } else {
      const int ch = w;
      char* U = lds + L_U + ch * U_CH;
      char* G = lds + ch * (4 * G_COPY);
      {
        const rsrc_t rf = __builtin_amdgcn_make_buffer_rsrc((void*)(p.ssm_k + (size_t)(quad * 4 + ch) * S_), 0, S_ * 2, 0x00020000);
        u32x4 fa[4];
        u32x2 fb[4];
        __builtin_amdgcn_s_setprio(3);
#pragma unroll
        for (int k = 0; k < 4; ++k) {
          fa[k] = __builtin_amdgcn_raw_buffer_load_b128(rf, l16 + (unsigned)k * 1024u, 0, NT);
        }
        __builtin_amdgcn_s_setprio(1);
        TAG_READY();
#pragma unroll
        for (int k = 0; k < 4; ++k) {
#pragma unroll
          for (int d = 0; d < 2; ++d)
            fb[k][d] = __builtin_amdgcn_update_dpp(k < 3 ? (unsigned)__builtin_amdgcn_readfirstlane(fa[k < 3 ? k + 1 : k][d]) : 0u, fa[k][d], 0x130, 0xf, 0xf, false);
          const unsigned x[6] = {fa[k][0], fa[k][1], fa[k][2], fa[k][3], fb[k][0], fb[k][1]};
          unsigned y[5];
#pragma unroll
          for (int d = 0; d < 5; ++d) y[d] = __builtin_amdgcn_alignbit(x[d + 1], x[d], 16);
          char* gp = G + 32 + k * 1024 + l16;
          *(u32x4*)(gp) = fa[k];
          *(u32x4*)(gp + G_COPY) = (u32x4){y[0], y[1], y[2], y[3]};
          *(u32x4*)(gp + 2 * G_COPY) = (u32x4){x[1], x[2], x[3], x[4]};
          *(u32x4*)(gp + 3 * G_COPY) = (u32x4){y[1], y[2], y[3], y[4]};
          if (k == 0) {
            const unsigned f0 = __builtin_amdgcn_readfirstlane(x[0]), f1 = __builtin_amdgcn_readfirstlane(x[1]);
            const unsigned long long f64 = (unsigned long long)f0 | ((unsigned long long)f1 << 32);
            const unsigned long long hi = l ? (f64 << (64 - 16 * (l & 3))) : 0ull;
            if (l < 4) {
              *(u32x4*)(G + l * G_COPY) = (u32x4){0u, 0u, 0u, 0u};
              *(u32x4*)(G + l * G_COPY + 16) = (u32x4){0u, 0u, (unsigned)hi, (unsigned)(hi >> 32)};
            }
            if (l >= 32) *(u32x4*)(U + (l - 32) * 16) = (u32x4){0u, 0u, 0u, 0u};
          }
        }
        asm volatile("s_waitcnt lgkmcnt(0)" ::: "memory");
        STAMP(1);
      }
      const int a = n16, h = h4, jj = h >> 1, e2 = h & 1;
      const char* ap = G + ((a + 1) & 3) * G_COPY + 2 * (16 * jj + a - 8 * e2 + 9 - ((a + 1) & 3));
      const char* bp = U + 512 + 32 * (a - jj) + 16 * e2;
      auto lda = [&](u32x4* A, int r, int p0, int np) {
#pragma unroll
        for (int pp = 0; pp < np; ++pp) {
          u32x2 lo = *(const volatile LAS u32x2*)(ap + 512 * (p0 + pp) + 64 * r);
          u32x2 hi = *(const volatile LAS u32x2*)(ap + 512 * (p0 + pp) + 64 * r + 8);
          A[pp] = (u32x4){lo[0], lo[1], hi[0], hi[1]};
        }
      };
      u32x4 AD;
      {
        const unsigned dDr = (*(cptr_t)(p.ssm_D + quad * 4 + (ch & 2)) >> (16 * (ch & 1))) & 0xffffu;
        const unsigned dv = h == (a >> 3) ? ((a & 1) ? dDr : dDr << 16) : 0u;
        const int dw = (7 - (a & 7)) >> 1;
#pragma unroll
        for (int d = 0; d < 4; ++d) AD[d] = dw == d ? dv : 0u;
      }
      u32x4 A0[8][4];
      f32x4 acc[8];
#pragma unroll
      for (int n = 0; n < 8; ++n) acc[n] = (f32x4){0.f, 0.f, 0.f, 0.f};
#pragma unroll
      for (int r = 0; r < 8; ++r) lda(A0[r], r, 0, 4);
      auto yput = [&](int s) {
#pragma unroll
        for (int e = 0; e < 2; ++e)
          *(u32x2*)(U + 1024 * s + 512 * e + (16 * a + 4 * h) * 2) = (u32x2){pack2(acc[2 * s + e][0], acc[2 * s + e][1]), pack2(acc[2 * s + e][2], acc[2 * s + e][3])};
        lf_set(16 + 4 * s + ch);
      };
      auto stage = [&](auto S__) {
        constexpr int s = decltype(S__)::value, NH = s < 2 ? 4 - 2 * s : 0, NP = 8 - 2 * s, NB = s < 2 ? 2 : 8;
        u32x4 A1[3][NH > 0 ? NH : 1], B[NB][2];
        auto ldb = [&](int r) {
#pragma unroll
          for (int e = 0; e < 2; ++e) B[r % NB][e] = *(const u32x4*)(bp + 512 * (2 * s + e) - 64 * r);
        };
        if (NH) { lda(A1[0], 0, 4, NH); lda(A1[1], 1, 4, NH); }
        SCHED();
        if (s > 0) yput(s - 1);
        lf_wait(4 * s, 4);
        if (s == 0) STAMP(3);
#pragma unroll
        for (int r = 0; r < NB; ++r) ldb(r);
        SCHED();
#pragma unroll
        for (int e = 0; e < 2; ++e) acc[2 * s + e] = MFMA16(AD, B[0][e], acc[2 * s + e]);
#pragma unroll
        for (int r = 0; r < 8; ++r) {
          if (NH && r + 2 < 8) lda(A1[(r + 2) % 3], r + 2, 4, NH);
          SCHED();
#pragma unroll
          for (int e = 0; e < 2; ++e)
#pragma unroll
            for (int pp = 0; pp < NP - e; ++pp) acc[2 * s + e + pp] = MFMA16(pp < 4 ? A0[r][pp] : A1[r % 3][pp < 4 ? 0 : pp - 4], B[r % NB][e], acc[2 * s + e + pp]);
          SCHED();
          if (r + NB < 8) ldb(r + NB);
        }
      };
      stage(IC<0>());
      STAMP(4);
      stage(IC<1>());
      STAMPF(9);
      const unsigned fo[1] = {fwi + (unsigned)((8 + c) * 20 + 5 * w + (l < 5 ? l : 0)) * 128u};
      unsigned fv = ~tag;
      {
        u32x4 B[4][4];
        auto ldb = [&](int r) {
#pragma unroll
          for (int m = 0; m < 4; ++m) B[r & 3][m] = *(const u32x4*)(bp + 512 * (4 + m) - 64 * r);
        };
        yput(1);
        if (w == 3) wo_ld();
        __builtin_amdgcn_s_setprio(3);
        lf_wait(8, 8);
        STAMP(22);
#pragma unroll
        for (int r = 0; r < 4; ++r) ldb(r);
        SCHED();
#pragma unroll
        for (int m = 0; m < 4; ++m) acc[4 + m] = MFMA16(AD, B[0][m], acc[4 + m]);
#pragma unroll
        for (int r = 0; r < 8; ++r) {
#pragma unroll
          for (int m = 0; m < 4; ++m)
#pragma unroll
            for (int pp = 0; pp < 4 - m; ++pp) acc[4 + m + pp] = MFMA16(A0[r][pp], B[r & 3][m], acc[4 + m + pp]);
          SCHED();
          if (r + 4 < 8) ldb(r + 4);
          if (r == 1) fv = ld4(rs, fo[0]);
          SCHED();
        }
#pragma unroll
        for (int e = 0; e < 4; ++e)
          *(u32x2*)(U + 2048 + 512 * e + (16 * a + 4 * h) * 2) = (u32x2){pack2(acc[4 + e][0], acc[4 + e][1]), pack2(acc[4 + e][2], acc[4 + e][3])};
        lf_set(24 + ch);
      }
      STAMP(28);
      __builtin_amdgcn_s_setprio(0);
      if (!__all(fv == tag)) wait_lines<1, 4>(rs, fo, tag);
      lf_wait(20, 4);
    }
    __builtin_amdgcn_s_setprio(0);
  } else {
    const int at = idx - 20;
    char* const scr = lds + WBLK + 14848;
    auto gv_blk = [&](int j) { return j < 160 ? 30 + (j >> 3) : j < 400 ? (j >> 3) - 20 : j >> 3; };
    auto gv_fin = [&](const u32x4* pre, int j) {
      float acc[8];
      gemv_fin(acc, pre, 10, scr);
      gemv_put(acc, j < 160 ? WS_MODP + (unsigned)((j & 7) * 1280 + (j >> 3) * 64) * 8u : WS_MODQ + (unsigned)((j & 7) * 2560 + ((j >> 3) - 20) * 64) * 8u);
    };
    const int ja = xcd * 12 + at, ai = xcd * 8 + at, bi = xcd * 4 + at - 8, g = w >> 2, wj = w & 3;
    const int rj = w == 0 ? ja : w == 1 ? (at >= 4 ? 92 + ai : -1) : w == 4 ? 160 + ja : w == 5 && at < 8 ? 256 + ai : -1;
    char* const rst = lds + (g ? WBLK : 65536) + wj * 7424;
    u32x4 pre[11];
    if (at >= 8 || g == 0) {
      TOUCH_AT(2);
      const int T = at >= 8 ? 64 + 2 * bi + g : ai;
      unsigned* mds = (unsigned*)(lds + 2048) + 16 * g;
      char* qt = lds + L_RT + g * (16 * 1312);
      u32x4 ld[5];
      const bool e1 = at >= 8 && w == 3;
      if (e1) gemv_load(pre, gv_blk(128 + bi), bi & 7, 10);
      else __builtin_amdgcn_s_sleep(8);
#pragma unroll
      for (int j = 0; j < 5; ++j)
        ld[j] = __builtin_amdgcn_raw_buffer_load_b128(rq, (unsigned)(h4 * D_ * 2 + n16 * 16 + j * 256), (unsigned)((32 * (T >> 1) + 8 * wj + 4 * (T & 1)) * D_ * 2), NT);
      TAG_READY();
      {
        const bf2_t ONE2 = {(__bf16)1.0f, (__bf16)1.0f};
        char* dst = qt + (4 * wj + h4) * 1312 + n16 * 16;
        float sa = 0.f, va = 0.f;
#pragma unroll
        for (int j = 0; j < 5; ++j) {
          *(u32x4*)(dst + j * 256) = ld[j];
#pragma unroll
          for (int d = 0; d < 4; ++d) {
            const unsigned a0 = ld[j][d];
            const bf2_t xa = __builtin_bit_cast(bf2_t, a0);
            sa = __builtin_amdgcn_fdot2_f32_bf16(xa, ONE2, sa, false); va = __builtin_amdgcn_fdot2_f32_bf16(xa, xa, va, false);
          }
        }
        lf_set(8 + w);
        const float ma = sum16(sa) * (1.0f / D_);
        va = __builtin_fmaxf(sum16(va) * (1.0f / D_) - ma * ma, 0.f);
        if (n16 == 0) mds[4 * wj + h4] = pack2(ma, __builtin_amdgcn_sqrtf(rbf(rbf(va) + 1e-5f)));
      }
      if (e1) gv_fin(pre, 128 + bi);
      lf_wait(8 + 4 * g, 4);
      STAMP(23);
      {
        const char* src = qt + (l & 7) * 1312 + (40 * wj + (l >> 3)) * 8;
#pragma unroll
        for (int j = 0; j < 5; ++j) {
          const u32x2 x = *(const u32x2*)(src + j * 64), y = *(const u32x2*)(src + j * 64 + 8 * 1312);
          st16(rs, WS_QB + (unsigned)T * 20480u + (unsigned)(5 * wj + j) * 1024u + l16, (u32x4){x[0], x[1], y[0], y[1]});
        }
      }
      SCHED();
      const int g2 = at < 8 || w < 3 || w == 4 ? 400 : w == 3 ? 384 + bi : 256 + wj * 32 + bi;
      if (g2 < 400) {
        gemv_load(pre, gv_blk(g2), g2 & 7, 10);
        asm volatile("s_waitcnt vmcnt(10)" ::: "memory");
      } else if (w < 2 && rj >= 0) {
        rt_ld(rj, rst);
        asm volatile("s_waitcnt vmcnt(7)" ::: "memory");
      } else {
        DRAIN();
      }
      lf_set(16 + w);
      if (wj == 2) {
        lf_wait(16 + 4 * g, 4);
        st16(rs, WS_ST1 + (unsigned)(l >> 3) * 16384u + (unsigned)T * 128u + (unsigned)(l & 7) * 16u, (u32x4){mds[l & 7], tag, mds[8 + (l & 7)], tag});
        STAMP(22);
      }
      if (w >= 4 && w < 7) lf_set(w);
      if (g2 < 400) gv_fin(pre, g2);
      STAMP(25);
    } else {
      constexpr int KB = L_RT + 16 * 1312;
      TOUCH_AT(3);
      const int g1 = w < 6 ? 2 * ai + wj : 32 + wj * 64 + ai;
      if (w >= 6) {
        const rsrc_t rk = __builtin_amdgcn_make_buffer_rsrc((void*)(w == 6 ? p.k : p.v), 0, S_ * D_ * 2, 0x00020000);
        const unsigned ko = (unsigned)((1536 + 8 * ai) * (D_ * 2) - (w == 6 ? 4096 : 0));
        char* dst = lds + KB + (w == 6 ? 0 : 14336);
        __builtin_amdgcn_s_sleep(3);
#pragma unroll
        for (int n = 0; n < 10; ++n) __builtin_amdgcn_raw_ptr_buffer_load_lds(rk, (LAS void*)(dst + n * 1024), 16, l16, ko + (unsigned)n * 1024u, 0, NT);
        if (w == 6) {
#pragma unroll
          for (int n = 10; n < 14; ++n) __builtin_amdgcn_raw_ptr_buffer_load_lds(rk, (LAS void*)(dst + n * 1024), 16, l16, ko + (unsigned)n * 1024u, 0, NT);
        }
      }
      SCHED();
      u32x4 swt[2];
      swt[0] = *(const u32x4*)(p.shift_k + (tid - 256) * 8);
      swt[1] = *(const u32x4*)(p.shift_k + (256 + l) * 8);
      SCHED();
      if (w >= 6) __builtin_amdgcn_s_sleep(8);
      gemv_load(pre, gv_blk(g1), g1 & 7, 10);
      TAG_READY();
      asm volatile("s_waitcnt vmcnt(11)" : "+v"(swt[0]), "+v"(swt[1]) :: "memory");
      lf_set(8 + w);
      if (w < 6) gv_fin(pre, g1);
      STAMP(25);
      lf_wait(12, 4);
      STAMP(23);
#pragma unroll 1
      for (int n = 0; n < (w == 7 ? 2 : 1); ++n) {
        {
          const int pr = n ? 256 + l : tid - 256;
          const char* kp = lds + KB + 4096 + pr * 4;
          unsigned kk[11], vv[8];
#pragma unroll
          for (int i = 0; i < 11; ++i) kk[i] = *(const unsigned*)(kp + (i - 3) * 1280);
#pragma unroll
          for (int i = 0; i < 8; ++i) vv[i] = *(const unsigned*)(kp + 10240 + i * 1280);
          const u32x4 sw = n ? swt[1] : swt[0];
          const f32x2 wt[4] = {{bflo(sw[0]), bflo(sw[2])}, {bfhi(sw[0]), bfhi(sw[2])}, {bflo(sw[1]), bflo(sw[3])}, {bfhi(sw[1]), bfhi(sw[3])}};
          f32x2 o[8];
#pragma unroll
          for (int i = 0; i < 8; ++i) {
            f32x2 t = (f32x2){bflo(kk[i + 3]), bfhi(kk[i + 3])} * wt[0];
#pragma unroll
            for (int j = 1; j < 4; ++j) t = __builtin_elementwise_fma((f32x2){bflo(kk[i + 3 - j]), bfhi(kk[i + 3 - j])}, wt[j], t);
            o[i] = t * (f32x2){bflo(vv[i]), bfhi(vv[i])};
          }
#pragma unroll
          for (int e = 0; e < 4; ++e) {
            const int i = 7 - 4 * (e & 1);
            st16(rs, WS_KV3 + (unsigned)ai * 20480u + (unsigned)(e * 5120 + pr * 16),
                 (u32x4){pack2(o[i][e >> 1], o[i - 1][e >> 1]), tag, pack2(o[i - 2][e >> 1], o[i - 3][e >> 1]), tag});
          }
        }
      }
      STAMP(22);
      if (w < 7) lf_set(w);
      if (w >= 6) gv_fin(pre, g1);
      STAMPF(30);
    }
    if (w == 6 && ja < 80) {
      hold(AUX_T);
      gemv_load(pre, gv_blk(400 + ja), ja & 7, 10);
      gv_fin(pre, 400 + ja);
    }
    if (rj >= 0) {
      if (w >= 2) { hold(RT2_T); rt_ld(rj, rst); }
      rt_job(rj, rst);
    }
    STAMP(26);
    lf_set(w);
    if (w == 3) { hold(WA_T); wo_ld(); }
    STAMP(3);
    if (w < 4) {
      {
        const unsigned fo[1] = {l < 5 ? fwi + (unsigned)((8 + c) * 20 + 5 * w + l) * 128u : ~0u};
        hold(W_T);
        wait_lines<1, 8>(rs, fo, tag);
      }
      lf_wait(0, 8);
      STAMP(4);
    }
    if ((at == 8 || at == 9) && w == 5 && xcd * 2 + at - 8 < 10) {
      const unsigned o = WS_MODQ + (unsigned)(h4 * 5120 + 1920 + (xcd * 2 + at - 8) * 64 + n16 * 4) * 8u;
      u32x4 v[4];
      hold(CS_T);
      _Pragma("unroll 1") for (unsigned spins = 0; spins < SPIN_CAP; ++spins) {
        unsigned dd = 0u;
#pragma unroll
        for (int k = 0; k < 4; ++k) v[k] = ld16(rs, o + (unsigned)((k >> 1) * 20480 + (k & 1) * 16));
#pragma unroll
        for (int k = 0; k < 4; ++k) dd |= (v[k][0] ^ tag) | (v[k][2] ^ tag);
        if (__all(dd == 0u)) break;
        __builtin_amdgcn_s_sleep(16);
      }
      float t[4];
#pragma unroll
      for (int k = 0; k < 4; ++k) t[k] = sum_rows(__uint_as_float(v[k >> 1][1 + 2 * (k & 1)]) + __uint_as_float(v[2 + (k >> 1)][1 + 2 * (k & 1)]));
#pragma unroll
      for (int e = 0; e < 2; ++e)
        st16(rs, WS_MOD + (unsigned)(4 * e + h4) * 2560u + (unsigned)((xcd * 2 + at - 8) * 16 + n16) * 16u, (u32x4){tag, pack2(t[0], t[1]), tag, pack2(t[2], t[3])});
    }
    if (w == 4 || (w == 5 && at >= 10)) {
      const int cj = w == 4 ? ja : 86 + xcd * 2 + at, cb = cj / 7, j = cj % 7;
      const unsigned o = WS_CSP + (unsigned)((cb * 20 + 5 * h4) * 2 + (j >> 2)) * 1024u + (unsigned)((j & 3) * 16 + n16) * 16u;
      u32x4 v[5];
      hold(CS_T);
      __builtin_amdgcn_s_setprio(3);
      _Pragma("unroll 1") for (unsigned spins = 0; spins < SPIN_CAP; ++spins) {
        unsigned dd = 0u;
#pragma unroll
        for (int k = 0; k < 5; ++k) v[k] = ld16(rs, o + (unsigned)k * 2048u);
#pragma unroll
        for (int k = 0; k < 5; ++k) dd |= (v[k][0] ^ tag) | (v[k][2] ^ tag);
        if (__all(dd == 0u)) break;
        __builtin_amdgcn_s_sleep(8);
      }
      float t0 = 0.f, t1 = 0.f;
#pragma unroll
      for (int k = 0; k < 5; ++k) { t0 += __uint_as_float(v[k][1]); t1 += __uint_as_float(v[k][3]); }
      t0 = sum_rows(t0); t1 = sum_rows(t1);
      if (l < 16) st16(rs, WS_CS + (unsigned)(cb * 2048 + j * 256) + l16, (u32x4){tag, __float_as_uint(t0), tag, __float_as_uint(t1)});
      __builtin_amdgcn_s_setprio(0);
      STAMP(24);
    }
  }

  STAMPF(16);
  u32x4 wox[5];
  DRAIN();
  if (w == 3) {
    const char* bp = wo_st + (4 * h4 + (n16 >> 2)) * 160 + (n16 & 3) * 8;
#pragma unroll
    for (int dj = 0; dj < 5; ++dj) wox[dj] = trfrag(bp + dj * 32, 160);
    asm volatile("s_waitcnt lgkmcnt(0)" : "+v"(wox[0]), "+v"(wox[1]), "+v"(wox[2]), "+v"(wox[3]), "+v"(wox[4]) :: "memory");
  }
  auto wo_put = [&]() {
    if (w == 3 && R < 28) {
      const unsigned base = WS_WOUTF + (unsigned)c * WBLK + (unsigned)((R >> 1) * 10240 + (R & 1) * 1024);
#pragma unroll
      for (int dj = 0; dj < 5; ++dj) st16(rs, base + (unsigned)((R & 1) ? dj : (dj + 2) % 5) * 2048u + l16, wox[dj]);
    }
  };
  if (w < 4) load_b2();
  wo_put();
  STAMPF(29);
  u32x4 k_c;
  unsigned kc_o, kc_d;
  {
    const int col = tid >= 112 ? (8 + c) * 128 + tid - 112 : c * 128 + tid;
    kc_o = tid < 224 ? WS_CS + (unsigned)col * 16u : WS_MOD + (unsigned)xcd * 2560u + (unsigned)(20 * c + (tid < 244 ? tid - 224 : 0)) * 16u;
    kc_d = tid < 112 ? (unsigned)(L_CONST + (tid >> 1) * 16 + (tid & 1) * 4) : tid < 224 ? (unsigned)(L_CONST + tid * 8) : (unsigned)(L_MISC + ((tid - 224) & 31) * 16);
  }
  auto consts_put = [&]() {
    if (__any(((k_c[0] ^ tag) | (k_c[2] ^ tag)) != 0u)) {
      _Pragma("unroll 1") for (unsigned spins = 0; spins < SPIN_CAP; ++spins) {
        __builtin_amdgcn_s_sleep(8);
        k_c = ld16(rs, kc_o);
        if (__all(k_c[0] == tag && k_c[2] == tag)) break;
      }
    }
    if (w < 3 || l < 32) { *(unsigned*)(lds + kc_d) = k_c[1]; *(unsigned*)(lds + kc_d + (tid < 112 ? 8 : 4)) = k_c[3]; }
    else *(f32x4*)(lds + kc_d) = (f32x4){bflo(k_c[1]), bfhi(k_c[1]), bflo(k_c[3]), bfhi(k_c[3])};
  };
  const int wq = w ^ rotr, rt1 = wq & 3, kh1 = w >> 2;
  unsigned bo0 = (unsigned)(kh1 * 71680) + l16, bo1 = bo0 + 35840u;
  asm volatile("" : "+v"(bo0), "+v"(bo1));
  unsigned pso[3], pqo[3], oto = (unsigned)(((w ^ 4) * 7 * 64 + l) * 8), rdo = (unsigned)((w * 7 * 64 + l) * 8), cso = (unsigned)(L_CONST + (112 + n16) * 8);
#pragma unroll
  for (int e = 0; e < 3; ++e) {
    const int row = rt1 * 16 + (e < 2 ? 4 * h4 + 2 * kh1 + e : n16);
    pso[e] = (unsigned)(L_PST + row * 4); pqo[e] = (unsigned)(L_PST + 512 + row * 16 + (row & 3) * 4);
  }
  unsigned spo = (unsigned)(L_Z + tid * 16), p6o = (unsigned)(L_PST + (wq * 16 + 4 * h4) * 4), pgo = (unsigned)(L_PST + 512 + (wq * 16 + n16) * 16);
  asm volatile("" : "+v"(spo), "+v"(p6o), "+v"(pgo));
  p6o = n16 == 12 ? p6o : spo;
  pgo = h4 == (n16 >> 2) ? pgo : spo;
  unsigned bsrc = (unsigned)(((l >> 1) & 3) * 64 + (l & 1) * 4), ppo = l < 8 ? l16 : 0x80000000u;
  asm volatile("" : "+v"(pso[0]), "+v"(pso[2]), "+v"(pqo[0]), "+v"(pqo[2]), "+v"(oto), "+v"(rdo), "+v"(cso), "+v"(p6o), "+v"(pgo), "+v"(bsrc), "+v"(ppo), "+v"(kc_o), "+v"(kc_d));
  pso[1] = pso[0] + 4u; pqo[1] = pqo[0] + 20u;
#define BFRAG(q, j) (*(const u32x4*)(lds + ((q) < 5 ? bo0 : bo1) + ((q) % 5) * 7168 + (j) * 1024))
  u32x4 h2[10];
  unsigned xv = (unsigned)((4 * ((l >> 3) & 1) + h4) * 128 + (l & 7) * 16), xs0 = WS_X1B + (unsigned)(R * 4 + rt1) * 20480u + (unsigned)(10 * kh1) * 1024u;
  unsigned wvo = wsp_vo, bso = (unsigned)tau(rt1, n16) * 2u, fgo = l < 32 ? (unsigned)(l >> 3) * F4S + (unsigned)(l & 7) * 16u : 0x80000000u;
  asm volatile("" : "+v"(xv), "+v"(wvo), "+v"(bso), "+v"(fgo));
#define WO3 (-92)
  unsigned zs[8];
#pragma unroll
  for (int t = 0; t < 8; ++t) zs[t] = WS_Z2F + (unsigned)(c * 64 + 8 * w + 4 * ((t >> 2) ^ rotg) + ((t + rotb) & 3)) * 7168u;
  unsigned wx8 = (unsigned)((w + (idx >> 2)) & 7) * 1024u, wx4 = wx8 & 3072u, wgb = WS_WOUTF + (unsigned)c * WBLK, fzs = fz + (unsigned)(c * 32 + 4 * w) * 128u;
  unsigned uo = WS_UF + (unsigned)((R * 4 + rt1) * 8 + c) * 4096u + (unsigned)kh1 * 2048u, ui = WS_UF + (unsigned)((R * 4 + rt1) * 8 + 4 * kh1) * 4096u;
  unsigned xs = WS_X1B + (unsigned)(R * 4 + rt1) * 20480u + (unsigned)(5 * c + 2 * kh1) * 512u, wl2 = (unsigned)(120 + WO3 + (w < 4 ? w + 16 : w)) * 1024u;
  unsigned os = (unsigned)__builtin_amdgcn_readfirstlane(((R * 64 + tau(rt1, 0)) * D_ + 80 * c + 32 * kh1) * 2);
#define PINS(x) do { x = (unsigned)__builtin_amdgcn_readfirstlane((int)x); asm volatile("" : "+s"(x)); } while (0)
#pragma unroll
  for (int t = 0; t < 8; ++t) PINS(zs[t]);
  PINS(xs0); PINS(wx8); PINS(wx4); PINS(wgb); PINS(fzs); PINS(uo); PINS(ui); PINS(xs); PINS(wl2); PINS(os);
  unsigned s1 = (unsigned)((w ^ 4) * 3072 + 1024), s3 = (unsigned)(w * 3072 + 1024);
  PINS(s1); PINS(s3);
#define LDX(q) h2[q] = __builtin_amdgcn_raw_buffer_load_b128(rs, xv + (unsigned)((q) & 3) * 1024u, xs0 + (unsigned)((q) >> 2) * 4096u, BULK_AUX)
  auto flag4 = [&](unsigned off) { __builtin_amdgcn_raw_buffer_store_b128((u32x4){tag, tag, tag, tag}, rs, fgo, WS_F4 - WS_FWI + off, SC1); };
  unsigned bs_raw;
  f32x4 acc[7];
  float mu_n, rs_n;
  {
    if (w == 6) {
      const unsigned fo[1] = {l < 20 ? fwi + (unsigned)(c * 20 + l) * 128u : ~0u};
      hold(CS_T);
      wait_lines<1, 8>(rs, fo, tag);
    }
    if (w == 7) {
      const unsigned fb = WS_FX + (unsigned)(((R >> 1) * 4 + (R & 1) * 2 + (c >> 2)) * 160 + l) * 128u;
      const unsigned fo[3] = {fb, fb + 8192u, l < 32 ? fb + 16384u : ~0u};
      if (!is_conv) hold(Y_T);
      wait_lines<3, 2>(rs, fo, tag);
      if (l == 0) lf[33] = tag;
    }
    lf_wait(33, 1);
    STAMP(7);
    SCHED();
#pragma unroll
    for (int q = 0; q < 3; ++q) LDX(q);
    SCHED();
    __builtin_amdgcn_s_waitcnt(0x0F73);
    BAR();
    STAMP(8);
#pragma unroll
    for (int j = 0; j < 7; ++j) acc[j] = (f32x4){0.f, 0.f, 0.f, 0.f};
    f32x4 accg = {0.f, 0.f, 0.f, 0.f};
    u32x4 bf[7];
#pragma unroll
    for (int j = 0; j < 7; ++j) bf[j] = BFRAG(0, j);
    SCHED();
#pragma unroll
    for (int q = 3; q < 6; ++q) LDX(q);
    if (w == 3) flag4(WS_FWO + (unsigned)(c * 32 + R) * 128u);
    SCHED();
    auto xswap = [&](int q, int e) {
#pragma unroll
      for (int d = 0; d < 2; ++d) {
        if (e == 1) h2[q][2 + d] ^= (unsigned)__builtin_amdgcn_update_dpp(0, (int)h2[q][d], 0x128, 0xf, 0x3, false);
        else h2[q][d] ^= (unsigned)__builtin_amdgcn_update_dpp(0, (int)h2[q][2 + d], 0x128, 0xf, 0xC, false);
      }
      asm volatile("" : "+v"(h2[q]));
    };
#define XSW 1
#pragma unroll
    for (int q = 0; q < 10; ++q) {
      if (q < XSW) { xswap(q, 0); xswap(q, 1); xswap(q, 2); }
      if (q == 5) { __builtin_amdgcn_s_barrier(); asm volatile("" ::: "memory"); }
      SCHED();
      accg = MFMA16(h2[q], h2[q], accg);
      SCHED();
#pragma unroll
      for (int j = 0; j < 7; ++j) {
        acc[j] = MFMA16(h2[q], bf[j], acc[j]);
        if (q == 9 && j == 1) *(f32x4*)(lds + pgo) = accg;
        if (q < 9) bf[j] = BFRAG(q + 1, j);
        if (q < 9 && q + 1 >= XSW && j >= 3 && j < 6) xswap(q + 1, j - 3);
        if (q == 2 && j == 0) bs_raw = __builtin_amdgcn_raw_buffer_load_b16(__builtin_amdgcn_make_buffer_rsrc((void*)p.b_sp, 0, S_ * 2, 0x00020000), bso, (unsigned)R * 128u, 0);
#define KCQ 4
#define WA 8
#define WB 12
#define WQ0 2
        if (WQ0 == 2 && q >= 2 && j == 2) LDW(q - 2);
        if (WQ0 == 6 && q >= 6 && (j == 2 || j == 4)) LDW(2 * (q - 6) + (j >> 2));
        if (WQ0 == 3 && q >= 2 && q < 5 && (j & 1) && 3 * (q - 2) + (j >> 1) < WA) LDW(3 * (q - 2) + (j >> 1));
        if (WQ0 == 4 && q >= 2 && !(q & 1) && j == 2) LDW((q - 2) >> 1);
        if ((j == 0 || j == 4) && 6 + 2 * q + (j >> 2) < 10) LDX(6 + 2 * q + (j >> 2));
        if (q == KCQ && j == 0) k_c = ld16(rs, kh1 ? 0x80000000u : kc_o);
        if (q >= 5 && j == 5) {
          const int i = (5 + q) * 7 + (w < 7 ? w : 0);
          dma1k(rs, lds + i * 1024, l16, WS_WINF + (unsigned)c * WBLK + (unsigned)i * 1024u);
          if (q == 9) { const int i2 = 28 + (w < 7 ? w : 0); dma1k(rs, lds + i2 * 1024, l16, WS_WINF + (unsigned)c * WBLK + (unsigned)i2 * 1024u); }
        }
        SCHED();
      }
    }
    STAMP(31);
  }
  auto gelu_n = [&](f32x2* x, auto N_) {
    constexpr int N = decltype(N_)::value;
    const float c0 = -2.0f * 0.7978845608028654f * 1.4426950408889634f, c1 = c0 * 0.044715f;
    f32x2 k0 = {c0, c0}, k1 = {c1, c1}, k2 = {1.0f, 1.0f};
    asm volatile("" : "+v"(k0), "+v"(k1), "+v"(k2));
    f32x2 t[N];
#define GST(e) do { _Pragma("unroll") for (int i = 0; i < N; ++i) { t[i] = (e); asm volatile("" : "+v"(t[i])); } } while (0)
    GST(x[i] * x[i]);
    GST(__builtin_elementwise_fma(t[i], k1, k0));
    GST(t[i] * x[i]);
    GST(((f32x2){__builtin_amdgcn_exp2f(t[i][0]), __builtin_amdgcn_exp2f(t[i][1])}));
    GST(t[i] + k2);
    GST(((f32x2){__builtin_amdgcn_rcpf(t[i][0]), __builtin_amdgcn_rcpf(t[i][1])}));
#pragma unroll
    for (int i = 0; i < N; ++i) { x[i] *= t[i]; asm volatile("" : "+v"(x[i])); }
  };
  {
    f32x2 mine[7];
    f32x2 csb[7];
    if (kh1) {
#pragma unroll
      for (int j = 0; j < 7; ++j) { *(f32x2*)(lds + rdo + j * 512) = (f32x2){acc[j][0], acc[j][1]}; mine[j] = (f32x2){acc[j][2], acc[j][3]}; }
      *(f32x4*)(lds + p6o) = acc[6];
    } else {
#pragma unroll
      for (int j = 0; j < 7; ++j) { *(f32x2*)(lds + rdo + j * 512) = (f32x2){acc[j][2], acc[j][3]}; mine[j] = (f32x2){acc[j][0], acc[j][1]}; }
      *(f32x4*)(lds + p6o) = acc[6];
      consts_put();
#pragma unroll
      for (int k = WA; k < WB; ++k) LDW(k);
    }
    BAR();
    if (kh1) {
#pragma unroll
      for (int k = WA; k < WB; ++k) LDW(k);
    }
    __builtin_amdgcn_s_setprio(3);
    STAMPT(9);
    f32x2 oth[7];
    f32x2 pa[3], pb[3];
#pragma unroll
    for (int e = 0; e < 3; ++e) {
      pa[e] = (f32x2){*(const float*)(lds + pso[e]), *(const float*)(lds + pqo[e])};
      pb[e] = (f32x2){*(const float*)(lds + pso[e] + 256), *(const float*)(lds + pqo[e] + 1024)};
    }
#pragma unroll
    for (int j = 0; j < 7; ++j) { oth[j] = *(const f32x2*)(lds + oto + j * 512); csb[j] = *(const f32x2*)(lds + cso + 128 * j); }
    SCHED();
    float mu2[2], rs2[2];
#pragma unroll
    for (int e = 0; e < 3; ++e) {
      const float mean = (pa[e][0] + pb[e][0]) * (1.0f / D_);
      const float var = __builtin_fmaxf((pa[e][1] + pb[e][1]) * (1.0f / D_) - mean * mean, 0.f);
      const float rstd = __builtin_amdgcn_rsqf(var + 1e-5f);
      if (e < 2) { mu2[e] = mean; rs2[e] = rstd; } else { mu_n = mean; rs_n = rstd; }
    }
    f32x2 zz[7];
    const f32x2 nmu = {-mu2[0], -mu2[1]}, rsv = {rs2[0], rs2[1]};
#pragma unroll
    for (int j = 0; j < 7; ++j)
      zz[j] = __builtin_elementwise_fma(__builtin_elementwise_fma(nmu, (f32x2){csb[j][0], csb[j][0]}, mine[j] + oth[j]), rsv, (f32x2){csb[j][1], csb[j][1]});
    gelu_n(zz, IC<7>());
    if (n16 >= 12) zz[6] = (f32x2){0.f, 0.f};
    f32x2 s1 = ((zz[0] + zz[1]) + (zz[2] + zz[3])) + ((zz[4] + zz[5]) + zz[6]);
    f32x2 s2 = zz[0] * zz[0], s3 = zz[1] * zz[1];
#pragma unroll
    for (int j = 2; j < 7; ++j) { if (j & 1) s3 = __builtin_elementwise_fma(zz[j], zz[j], s3); else s2 = __builtin_elementwise_fma(zz[j], zz[j], s2); }
    s2 += s3;
    asm("" : "+v"(s1), "+v"(s2));
    float a0 = s1[0] + dpp_f<0xB1>(s1[0]), a1 = s1[1] + dpp_f<0xB1>(s1[1]), b0 = s2[0] + dpp_f<0xB1>(s2[0]);
    a0 = (l & 1) ? a1 : a0;
    const float b1 = s2[1] + dpp_f<0xB1>(s2[1]);
    b0 = (l & 1) ? b1 : b0;
    a0 += dpp_f<0x4E>(a0); b0 += dpp_f<0x4E>(b0);
    a0 += dpp_f<0x124>(a0); b0 += dpp_f<0x124>(b0);
    a0 += dpp_f<0x128>(a0); b0 += dpp_f<0x128>(b0);
    {
      const unsigned pa0 = __builtin_amdgcn_ds_bpermute(bsrc, __float_as_uint(a0)), pb0 = __builtin_amdgcn_ds_bpermute(bsrc, __float_as_uint(b0));
      __builtin_amdgcn_raw_buffer_store_b128((u32x4){tag, pa0, tag, pb0}, rs, ppo, WS_PART + (unsigned)((R * 8 + c) * 8 + wq) * 128u, SC1);
    }
    SCHED();
    __builtin_amdgcn_s_setprio(0);
    STAMP(10);
#pragma unroll
    for (int k = 0; k < 10; ++k) {
      if (k < 9) {
        int x = k * 8 + w;
        x = x < 70 ? x : 69;
        const int i = x + (x < 35 ? 35 : 70);
        dma1k(rs, lds + i * 1024, l16, WS_WINF + (unsigned)c * WBLK + (unsigned)i * 1024u);
      }
    }
#pragma unroll
    for (int k = WB; k < 16; ++k) LDW(k);
#pragma unroll
    for (int j = 0; j < 7; ++j)
      *(unsigned*)(lds + L_Z + (((rt1 >> 1) * 7 + j) * 64 + l) * 16 + (4 * (rt1 & 1) + 2 * kh1) * 2) = pack2(zz[j][0], zz[j][1]);
    SCHED();
#define TR0 2
#define TR1 TR0
#define TR2 TR1
#define TR3 5
#pragma unroll
    for (int g = 0; g < TR0; ++g) WSP_TR(g);
    if (w == 1) {
      const unsigned fo[1] = {l < 28 ? fwi + (WS_FWO - WS_FWI) + (unsigned)(c * 32 + l) * 128u : ~0u};
      wait_lines<1, 1>(rs, fo, tag);
    }
    asm volatile("s_waitcnt vmcnt(%0)" :: "n"(16 - WB) : "memory");
    BAR();
    STAMPT(11);
  }
  unsigned z1d[7];
  constexpr int L_RED = 28672;
#define WOA(i) (((i) < 28 ? (i) : (i) < 108 ? (i) + 32 : (i) < 120 ? (i) + 37 : (i) - 92) * 1024)
#define RBAR() do { __builtin_amdgcn_s_barrier(); asm volatile("" ::: "memory"); } while (0)
#define ZAUX 16
#define NW 14
#define WOD(i0, x) dma1k(rs, lds + WOA(i0) + (x), l16, wgb + (unsigned)(i0) * 1024u + (x))
  auto wo_dma = [&](int k) {};
  u32x4 zw[NW];
#define LDZ(f) do { if (((f) >> 3) == 4) { zs[(f) & 7] += 4096u; asm volatile("" : "+s"(zs[(f) & 7])); } \
    zw[(f) % NW] = __builtin_amdgcn_raw_buffer_load_b128(rs, l16 + (unsigned)(((f) >> 3) & 3) * 1024u, zs[(f) & 7], ZAUX); } while (0)
#define WD 24
#define WE 32
#define QS 5
#define PQ 3
#define NW0 NW
#define PP 1
#define WZ 4
#define WY 11
  static_assert(QS >= 4 && QS <= 6 && PQ >= 2 && PQ < QS && (WZ < 4 || QS >= 5), "");
  {
    f32x4 acc[7];
#pragma unroll
    for (int j = 0; j < 7; ++j) acc[j] = (f32x4){0.f, 0.f, 0.f, 0.f};
    u32x4 bf[7];
#pragma unroll
    for (int j = 0; j < 7; ++j) bf[j] = BFRAG(4, j);
    const unsigned po = WS_PART + (unsigned)((R * 8 + (l >> 3)) * 8 + wq) * 128u + (unsigned)(l & 7) * 16u;
    u32x4 pv;
    auto z1 = [&](auto Q0_, auto Q1_) __attribute__((always_inline)) {
#pragma unroll
      for (int qi = decltype(Q0_)::value; qi < decltype(Q1_)::value; ++qi) {
        const int q = (qi + 4) % 10, qn = (qi + 5) % 10;
        SCHED();
#pragma unroll
        for (int j = 0; j < 7; ++j) {
          acc[j] = MFMA16(bf[j], h2[q], acc[j]);
          if (qi < 9 && qi != QS - 1) bf[j] = BFRAG(qn, j);
          if (qi < 2 && (j == 1 || j == 4)) { const int i = (2 * qi + (j >> 2)) * 7 + (w < 7 ? w : 0); dma1k(rs, lds + i * 1024, l16, WS_WINF + (unsigned)c * WBLK + (unsigned)i * 1024u); }
          if (qi == PQ && j == 0) pv = ld16(rs, po);
          if (!PP && qi >= QS && (j & 1) && WD + 3 * (qi - QS) + (j >> 1) < WE) LDW((WD + 3 * (qi - QS) + (j >> 1)) & 31);
          SCHED();
        }
      }
    };
    auto req1 = [&]() {
#pragma unroll
      for (int k = 16; k < WD; ++k) { LDW(k); SCHED(); }
    };
    auto req2 = [&]() {
#pragma unroll
      for (int k = WD; k < (PP ? WE : WD); ++k) { LDW(k); SCHED(); }
      sfor<0, WZ>([&](auto G_) __attribute__((always_inline)) { WOD(68 + 8 * decltype(G_)::value, wx8); SCHED(); });
    };
    if (kh1) req1();
    z1(IC<0>(), IC<QS>());
    STAMPT(29);
    f32x2 zv[2][4];
    unsigned sro = (unsigned)(L_ST2 + h4 * 16), swo = l < 8 ? (unsigned)(L_ST2 + (16 * rt1 + 4 * (l >> 1) + 2 * kh1 + (l & 1)) * 4) : (unsigned)(L_PST + l * 4);
    unsigned zo = w < 7 ? (unsigned)(h4 * 7168 + n16 * 16) : 0x80000000u;
#pragma unroll
    for (int m = 0; m < 2; ++m) {
      const u32x4 zf = *(const u32x4*)(lds + L_Z + ((m * 7 + (w < 7 ? w : 0)) * 64 + l) * 16);
#pragma unroll
      for (int e = 0; e < 4; ++e) { zv[m][e] = (f32x2){bflo(zf[e]), bfhi(zf[e])}; asm volatile("" : "+v"(zv[m][e])); }
    }
    asm volatile("" : "+v"(sro), "+v"(swo), "+v"(zo));
#pragma unroll
    for (int g = TR0; g < TR1; ++g) WSP_TR(g);
    __builtin_amdgcn_s_setprio(3);
    if (__any(((pv[0] ^ tag) | (pv[2] ^ tag)) != 0u)) {
      _Pragma("unroll 1") for (unsigned spins = 0; spins < SPIN_CAP; ++spins) {
        pv = ld16(rs, po);
        if (__all(((pv[0] ^ tag) | (pv[2] ^ tag)) == 0u)) break;
        __builtin_amdgcn_s_sleep(1);
      }
    }
    {
      const float mean = red8(__uint_as_float(pv[1])) * (1.0f / FH);
      const float var = __builtin_fmaxf(red8(__uint_as_float(pv[3])) * (1.0f / FH) - mean * mean, 0.f);
      const float rstd = __builtin_amdgcn_rsqf(var + 1e-5f);
      *(float*)(lds + swo) = rstd;
      *(float*)(lds + swo + 256) = -mean * rstd;
    }
    SCHED();
    if (!kh1) req1();
    BAR();
    STAMP(12);
    {
      f32x4 sa[2][2], sb[2][2];
#pragma unroll
      for (int i = 0; i < 4; ++i) { sa[i >> 1][i & 1] = *(const f32x4*)(lds + sro + i * 64); sb[i >> 1][i & 1] = *(const f32x4*)(lds + sro + i * 64 + 256); }
      SCHED();
#pragma unroll
      for (int m = 0; m < 2; ++m) {
        u32x4 o;
#pragma unroll
        for (int e = 0; e < 4; ++e) {
          const f32x4 a = sa[m][e >> 1], b = sb[m][e >> 1];
          const f32x2 x = __builtin_elementwise_fma(zv[m][e], (f32x2){a[2 * (e & 1)], a[2 * (e & 1) + 1]}, (f32x2){b[2 * (e & 1)], b[2 * (e & 1) + 1]});
          o[e] = pack2(x[0], x[1]);
        }
        __builtin_amdgcn_raw_buffer_store_b128(o, rs, zo + (unsigned)m * 256u, WS_Z2F + (unsigned)((c * 64 + 4 * (R >> 1)) * 7 + (w < 6 ? w + 1 : 0)) * 1024u + (unsigned)(R & 1) * 512u, SC1);
      }
    }
    SCHED();
    __builtin_amdgcn_s_setprio(0);
    STAMPT(13);
#pragma unroll
    for (int j = 0; j < 7; ++j) bf[j] = BFRAG((QS + 4) % 10, j);
    if (kh1) req2();
    z1(IC<QS>(), IC<10>());
    f32x2 mine[7];
    if (kh1) {
#pragma unroll
      for (int j = 0; j < 7; ++j) { *(f32x2*)(lds + rdo + L_RED + j * 512) = (f32x2){acc[j][0], acc[j][1]}; mine[j] = (f32x2){acc[j][2], acc[j][3]}; }
    } else {
#pragma unroll
      for (int j = 0; j < 7; ++j) { *(f32x2*)(lds + rdo + L_RED + j * 512) = (f32x2){acc[j][2], acc[j][3]}; mine[j] = (f32x2){acc[j][0], acc[j][1]}; }
    }
    if (!kh1) req2();
#pragma unroll
    for (int g = TR1; g < TR2; ++g) WSP_TR(g);
    asm volatile("s_waitcnt vmcnt(%0)" :: "n"(WE - WD + WZ) : "memory");
    BAR();
    if (w == 3) flag4(WS_FZ + (unsigned)(c * 32 + R) * 128u);
    STAMP(6);
    auto req = [&](auto G0_, auto G1_) __attribute__((always_inline)) {
#pragma unroll
      for (int k = WE; k < 32; ++k) LDW(k);
      SCHED();
      sfor<decltype(G0_)::value, decltype(G1_)::value>([&](auto G_) __attribute__((always_inline)) {
        constexpr int i0 = 4 * decltype(G_)::value;
        WOD(i0 < 68 ? i0 : i0 + 8 * WZ, wx4); SCHED();
      });
    };
    if (kh1) req(IC<0>(), IC<WY>());
    f32x2 z[7];
    {
      f32x2 oth[7];
      f32x4 cc[7];
#pragma unroll
      for (int j = 0; j < 7; ++j) {
        oth[j] = *(const f32x2*)(lds + oto + L_RED + j * 512);
        cc[j] = *(const f32x4*)(lds + L_CONST + (16 * j + 4 * h4 + 2 * kh1) * 8);
      }
      SCHED();
#pragma unroll
      for (int j = 0; j < 7; ++j)
        z[j] = __builtin_elementwise_fma(__builtin_elementwise_fma((f32x2){-mu_n, -mu_n}, (f32x2){cc[j][0], cc[j][1]}, oth[j] + mine[j]),
                                         (f32x2){rs_n, rs_n}, (f32x2){cc[j][2], cc[j][3]});
    }
    gelu_n(z, IC<7>());
#pragma unroll
    for (int j = 0; j < 7; ++j) z1d[j] = pack2(z[j][0], z[j][1]);
    asm volatile("" : "+v"(z1d[0]), "+v"(z1d[1]), "+v"(z1d[2]), "+v"(z1d[3]), "+v"(z1d[4]), "+v"(z1d[5]), "+v"(z1d[6]));
    SCHED();
    STAMPT(30);
    if (!kh1) req(IC<WY>(), IC<30 - 2 * WZ>());
    const unsigned fo[1] = {l < 4 ? fzs + (unsigned)l * 128u : ~0u};
    const unsigned fv = ld4(rs, fo[0]);
    SCHED();
#pragma unroll
    for (int g = TR2; g < TR3; ++g) WSP_TR(g);
    SCHED();
    STAMP(14);
    if (__builtin_amdgcn_ballot_w64(fv == tag) != 15ull) wait_lines<1, 1>(rs, fo, tag);
    STAMPT(23);
#pragma unroll
    for (int f = 0; f < NW; ++f) { LDZ(f); SCHED(); }
  }

#define WO2 15
#define TRC TR3
#define VMW(n) asm volatile("s_waitcnt vmcnt(%0)" ::"n"(n) : "memory")
  {
    SCHED();
#pragma unroll
    for (int g = TRC; g < 8; ++g) WSP_TR(g);
    SCHED();
    STAMP(20);
    const int rt = rt1, kh = kh1;
    unsigned rw = (unsigned)(L_RED + w * 512 + l * 8), rr = (unsigned)(L_RED + w * 4096 + l * 8);
    asm volatile("" : "+v"(rw), "+v"(rr));
    const float bs = bflo(bs_raw);
    unsigned long long hm = 0xffff000000000000ull;
    asm volatile("" : "+s"(hm));
    unsigned l8;
    f32x4 acc2[2][4];
    float rv[8][2];
    u32x4 ud;
    u32x4 P[2][8];
    u32x2 xp[3];
    auto ld_p = [&](int pt, int i) { P[pt][i] = __builtin_amdgcn_raw_buffer_load_b128(rs, l16 + (unsigned)pt * 1024u, ui + (unsigned)i * 2048u, SC1); };
    auto red_put = [&](int j, int r) {
      *(f32x2*)(lds + rw + r * 4096) = (f32x2){acc2[j & 1][r][0], acc2[j & 1][r][1]};
      *(f32x2*)(lds + rw + (r + 4) * 4096) = (f32x2){acc2[j & 1][r][2], acc2[j & 1][r][3]};
    };
    auto red_put1 = [&](int j, int i) { *(volatile LAS float*)(lds + rw + (i >> 2) * 4096 + ((i >> 1) & 1) * 16384 + (i & 1) * 4) = acc2[j & 1][i >> 2][i & 3]; };
    auto red_get = [&](int i) { const f32x2 x = *(const volatile LAS f32x2*)(lds + rr + i * 512); rv[i][0] = x[0]; rv[i][1] = x[1]; };
    float zz[2], gg[2];
    unsigned cs;
    auto sum_rows_u = [&](unsigned v) {
      auto a = __builtin_amdgcn_permlane16_swap(v, v, false, false);
      v = a[0] + a[1];
      auto b = __builtin_amdgcn_permlane32_swap(v, v, false, false);
      return b[0] + b[1];
    };
    auto red_fin = [&](int j, int k) {
      const int e = k & 1, i = k >> 1;
#define VADD(d, x, y) asm volatile("v_add_f32 %0, %1, %2" : "=v"(d) : "v"(x), "v"(y))
      if (i < 4) VADD(rv[2 * i][e], rv[2 * i][e], rv[2 * i + 1][e]);
      if (i == 4 || i == 5) VADD(rv[4 * (i & 1)][e], rv[4 * (i & 1)][e], rv[4 * (i & 1) + 2][e]);
      if (i == 6) VADD(rv[0][e], rv[0][e], bs);
      if (i == 7) VADD(gg[e], rv[0][e], rv[4][e]);
      if (i == 8) { zz[e] = e ? bfhi(z1d[(j + 6) % 7]) : bflo(z1d[(j + 6) % 7]); asm volatile("" : "+v"(zz[e])); }
      if (i == 9) { zz[e] *= gg[e]; asm volatile("" : "+v"(zz[e])); }
      if (k == 20) { ud[j & 3] = pack2(zz[0], zz[1]); asm volatile("" : "+v"(ud[j & 3])); }
      if (k == 21) {
        if (j == 0) asm volatile("v_cndmask_b32 %0, %0, 0, %1" : "+v"(ud[0]) : "s"(hm));
        if (j == 2) asm volatile("v_add3_u32 %0, %1, %2, %3" : "=v"(cs) : "v"(ud[0]), "v"(ud[1]), "v"(ud[2]));
        if (j == 3) {
          cs = tag - sum_rows_u(cs + ud[3]);
          asm volatile("v_cndmask_b32 %0, %0, %1, %2" : "+v"(ud[0]) : "v"(cs), "s"(hm));
        }
        if (j >= 4) { ud[3] = (j == 4 ? tag : ud[3]) - ud[j & 3]; asm volatile("" : "+v"(ud[3])); }
        if (j % 3 == 0 && j) __builtin_amdgcn_raw_buffer_store_b128(ud, rs, l16 + (unsigned)(j / 6) * 1024u, uo, SC1);
      }
    };
#define P1M 4
#define G2S 0
    struct Sch { int wide, lp, prio, lf, b1, b2, wp[16], rp[8], vp[22]; };
    constexpr Sch SC[4] = {
      {0, 2, 8, 1, 18, 29, {0, 1, 2, 3, 4, 5, 6, 7, 8, 9, 10, 11, 12, 13, 14, 15}, {19, 19, 20, 20, 21, 21, 22, 22}, {29, 29, 30, 30, 31, 31, 32, 33, 35, 36, 37, 38, 39, 40, 41, 42, 43, 44, 45, 46, 47, 48}},
      {1, 3, 4, 0, 18, 28, {3, 7, 11, 15, 99, 99, 99, 99, 99, 99, 99, 99, 99, 99, 99, 99}, {20, 20, 21, 21, 22, 22, 24, 24}, {28, 28, 29, 29, 30, 30, 31, 31, 32, 32, 33, 33, 34, 34, 35, 36, 36, 37, 37, 38, 38, 39}},
      {0, 1, 4, 1, 17, 29, {0, 1, 2, 3, 4, 5, 6, 7, 8, 9, 10, 11, 12, 13, 14, 15}, {17, 17, 18, 18, 19, 19, 20, 20}, {29, 29, 30, 30, 31, 31, 32, 34, 35, 36, 38, 39, 40, 42, 43, 44, 45, 46, 47, 48, 48, 49}},
      {0, 2, 0, 1, 17, 25, {0, 1, 2, 3, 4, 5, 6, 7, 8, 9, 10, 11, 12, 13, 14, 15}, {17, 17, 18, 18, 19, 19, 20, 20}, {25, 25, 26, 26, 27, 27, 28, 28, 29, 29, 30, 30, 31, 31, 32, 33, 23, 24, 34, 35, 36, 37}}};
#define JF 4
#define PC0 26
#define P1D 2
#define VL 2
    constexpr Sch S = SC[G2S];
    constexpr Sch S5 = {0, S.lp, S.prio, S.lf, 17, 24, {0, 1, 2, 3, 4, 5, 6, 7, 8, 9, 10, 11, 12, 13, 14, 15}, {17, 17, 18, 18, 19, 19, 20, 20}, {24, 24, 25, 25, 26, 26, 27, 27, 28, 28, 29, 29, 30, 30, 31, 31, 22, 23, 32, 32, 33, 33}};
    constexpr Sch S6 = {1, S.lp, S.prio, S.lf, 5, 12, {99, 99, 99, 3, 99, 99, 99, 99, 99, 99, 99, 99, 99, 99, 99, 99}, {5, 5, 6, 6, 7, 7, 8, 8},
                        {12, 12 + 1 / VL, 12 + 2 / VL, 12 + 3 / VL, 12 + 4 / VL, 12 + 5 / VL, 12 + 6 / VL, 12 + 7 / VL, 12 + 8 / VL, 12 + 9 / VL, 12 + 10 / VL, 12 + 11 / VL, 12 + 12 / VL, 12 + 13 / VL, 12 + 14 / VL, 12 + 15 / VL, 10, 11,
                         12 + 16 / VL, 12 + 17 / VL, 12 + 18 / VL, 12 + 19 / VL}};
    unsigned wb[3], xo;
#define XPM 19
    const unsigned wl[3] = {(unsigned)(120 + WO3 + w) * 1024u, (unsigned)(128 + WO3 + w) * 1024u, wl2}, wg = wgb - (unsigned)(WO3 * 1024);
#define NAF 32
#define NA0 18
    u32x4 af[NAF];
#define LDA(m) af[(m) % NAF] = *(const u32x4*)(lds + wb[WOA(2 * (m)) >> 16] + (WOA(2 * (m)) & 65535))
    unsigned ck[2];
    const unsigned tag4 = tag * 4u;
    auto p_chk = [&](int pt, int pr, int k) {
      u32x4* q = P[pt] + 4 * pr;
      if (k < 2) ck[k & 1] = q[k & 1][0] + q[k & 1][1];
      else if (k < 8) ck[k & 1] = ck[k & 1] + q[(k & 1) + 2 * ((k >> 2) & 1)][k & 2] + q[(k & 1) + 2 * ((k >> 2) & 1)][(k & 2) + 1];
      else if (k == 8) ck[0] = pt ? ck[0] + ck[1] : sum_rows_u(ck[0] + ck[1]);
      if (k < 9) asm volatile("" : "+v"(ck[k & 1]));
      if (k == 9) {
        if (__builtin_expect(__any(ck[0] != tag4), 0)) {
          _Pragma("unroll 1") for (unsigned spins = 0; spins < SPIN_CAP; ++spins) {
            unsigned d = 0u;
#pragma unroll
            for (int i = 0; i < 4; ++i) ld_p(pt, 4 * pr + i);
#pragma unroll
            for (int i = 0; i < 4; ++i) d += q[i][0] + q[i][1] + q[i][2] + q[i][3];
            if (!pt) d = sum_rows_u(d);
            if (!__any(d != tag4)) break;
            __builtin_amdgcn_s_sleep(1);
          }
        }
      }
      if (k >= 10 && !pt) { unsigned x = q[(k - 10) & 3][0]; asm volatile("v_cndmask_b32 %0, %0, 0, %1" : "+v"(x) : "s"(hm)); q[(k - 10) & 3][0] = x; }
      if (k >= 10 && k < 13 && pt) { q[(k - 10) % 3][3] = q[3][(k - 10) % 3]; asm volatile("" : "+v"(q[(k - 10) % 3])); }
    };
    sfor<0, 224>([&](auto M_) __attribute__((always_inline)) {
      constexpr int mm = decltype(M_)::value, j = mm >> 5, m = mm & 31;
      constexpr bool ro = j >= 5 && m >= 16;
      constexpr int t = ro ? 4 + (m & 3) : m >> 2, r = ro ? (m >> 2) & 3 : m & 3, f = 8 * j + t;
      constexpr Sch C = j < JF ? S : j < 6 ? S5 : S6, B = j <= JF ? S : S5;
      if (t == 0) asm volatile("v_mfma_f32_16x16x32_bf16 %0, %1, %2, 0" : "=&v"(acc2[j & 1][r]) : "v"(zw[f % NW]), "v"(wsp[r][t]), "v"(zw[(f | 1) % NW]));
      else asm volatile("v_mfma_f32_16x16x32_bf16 %0, %1, %2, %0" : "+v"(acc2[j & 1][r]) : "v"(zw[f % NW]), "v"(wsp[r][t]), "v"(zw[(f | 1) % NW]));
      SCHED();
      if (S.prio && (m + 31 - S.lp) % (S.prio ? S.prio : 1) == 0) __builtin_amdgcn_s_setprio(1);
      auto ld = [&]() {
        if ((m & 3) == S.lp) {
          if (S.prio && (m + 32 - S.lp) % (S.prio ? S.prio : 1) == 0) __builtin_amdgcn_s_setprio(0);
          constexpr int g = S.lp == 3 ? mm >> 2 : (mm >> 2) - 1;
          if (g >= 0 && g + NW < 56) LDZ(g + NW);
          constexpr int y = g + NW - 56;
          if (y >= 0 && WO2 + y < 15) wo_dma(WO2 + y);
          SCHED();
        }
        if (j == 6 && m >= P1M && m < P1M + 8 * P1D && (m - P1M) % P1D == 0) { ld_p(0, ((m - P1M) / P1D) & 7); SCHED(); }
      };
      if (!S.lf) ld();
      if ((m == C.b1 && j > 0) || m == C.b2) {
        if (j == 6 && m == C.b2 && WO2 < 15) VMW(m > P1M ? (m - P1M + P1D - 1) / P1D : 0);
        asm volatile("" ::: "memory"); __builtin_amdgcn_s_waitcnt(0xC07F); RBAR(); SCHED();
      }
      sfor<0, 16>([&](auto I_) __attribute__((always_inline)) {
        constexpr int i = decltype(I_)::value;
        if (j > 0 && C.wp[i] == m) { if (C.wide) red_put(j + 1, i & 3); else red_put1(j + 1, i); SCHED(); }
        if (j > 0 && i < 8 && C.rp[i & 7] == m) { red_get(i & 7); SCHED(); }
      });
      if (j == 5 && m >= 27 && (m & 1)) { red_put(j, ((m - 27) >> 1) & 3); SCHED(); }
      if (j == 6 && m >= 23 && (m & 3) == 3) { red_put(j, ((m - 23) >> 2) & 3); SCHED(); }
      if (j == 6 && m == XPM) {
        asm volatile("v_lshrrev_b32 %0, 1, %1" : "=v"(l8) : "v"(l16));
        xo = (l8 & 0x180u) + (unsigned)psi((l8 >> 3) & 15) * 8u;
        asm volatile("" : "+v"(xo));
      }
      if (j == 6 && m > XPM && m < XPM + 4) { xp[(m - XPM - 1) % 3] = __builtin_amdgcn_raw_buffer_load_b64(rs, xo + (unsigned)(m - XPM - 1) * 512u, xs, BULK_AUX); SCHED(); }
      if (j == 6 && m == 4) {
#pragma unroll
        for (int i = 0; i < 3; ++i) { wb[i] = (unsigned)(kh * 1024 + i * 65536) + l16; asm volatile("" : "+v"(wb[i])); }
      }
      if (j == 6 && m != 23 && m != 27 && m != 31) {
        constexpr int n = NA0 - 1 - (30 - m) + (m < 27) + (m < 23);
        if (n >= 0) { LDA(n >= 0 ? n : 0); SCHED(); }
      }
      if (j == 6 && m >= PC0 && m < PC0 + 5) { p_chk(0, 0, 2 * (m - PC0)); SCHED(); if (m < PC0 + 4) p_chk(0, 0, 2 * (m - PC0) + 1); SCHED(); }
      if (j == 6 && m == PC0 - 1) { red_fin(6, 16); red_fin(6, 17); SCHED(); }
      sfor<0, 22>([&](auto K_) __attribute__((always_inline)) {
        constexpr int k = decltype(K_)::value;
        if (j > 1 && B.vp[k] == m + 32) { red_fin(j > 1 ? j - 2 : 0, k); SCHED(); }
        if (j > 0 && C.vp[k] == m) { red_fin(j > 0 ? j - 1 : 0, k); SCHED(); }
      });
      if (S.lf) ld();
    });
    STAMP(15);
#define NM0 0
#define GM 19
#define MB 24
    f32x4 acc[5];
    f32x4 xf[3], gf[3];
    unsigned go, oo, eo, ho, tw, tr, o1, ta, tb;
    const unsigned s2 = (unsigned)(L_MISC + 128 * kh), s4 = s1 - (unsigned)kh * 1024u;
    unsigned long long om = kh ? ~0ull : 0xccccccccccccccccull;
#define PIN2(a, b) asm volatile("" : "+v"(a), "+v"(b))
    auto glue = [&](int k) {
      if (k == 0) { ta = l8 >> 5; tb = l16 & 48u; PIN2(ta, tb); }
      if (k == 1) { tr = __umul24(ta, 112u) + tb; oo = ta & 12u; PIN2(tr, oo); }
      if (k == 2) { tr += s1; oo += ta; PIN2(tr, oo); }
      if (k == 3) { oo = __umul24(oo, 1280u) + tb; ta = l8 >> 4; PIN2(oo, ta); }
      if (k == 4) { ta &= 24u; tb = l16 & 0xf0u; PIN2(ta, tb); }
      if (k == 5) { tw = __umul24(tb, 7u) + ta; go = (ta << 1) + s2; PIN2(tw, go); }
      if (k == 6) { tw += s1; asm volatile("v_cndmask_b32 %0, %1, -2.0, %2" : "=v"(o1) : "v"(oo), "s"(om)); asm volatile("" : "+v"(tw)); }
      if (k == 7) { eo = l16 + s3; ho = l16 + s4; PIN2(eo, ho); }
      if (k >= 8 && k < 14) {
        const unsigned x = xp[((k - 8) >> 1) % 3][k & 1];
        float lo = bflo(x), hi = bfhi(x);
        PIN2(lo, hi);
        xf[((k - 8) >> 1) % 3][2 * (k & 1)] = lo; xf[((k - 8) >> 1) % 3][2 * (k & 1) + 1] = hi;
      }
    };
    auto g3 = [&](auto M0_, auto M1_) __attribute__((always_inline)) {
      sfor<decltype(M0_)::value, decltype(M1_)::value>([&](auto M_) __attribute__((always_inline)) {
        constexpr int mi = decltype(M_)::value;
        constexpr int m = mi < 55 ? mi : mi < 64 ? 55 + 5 * ((mi - 55) / 3) + (mi - 55) % 3 : 58 + 5 * ((mi - 64) / 2) + (mi & 1), u2 = m / 5;
        const u32x4 fb = u2 < 8 ? P[0][u2 & 7] : P[1][u2 < 11 ? (u2 - 8) & 3 : (u2 - 7) & 7];
        if (m < 5) asm volatile("v_mfma_f32_16x16x32_bf16 %0, %1, %2, 0" : "=&v"(acc[m % 5]) : "v"(af[m % NAF]), "v"(fb));
        else asm volatile("v_mfma_f32_16x16x32_bf16 %0, %1, %2, %0" : "+v"(acc[m % 5]) : "v"(af[m % NAF]), "v"(fb));
        SCHED();
        if (mi == NM0) RBAR();
        if (mi >= NM0 && mi < NM0 + 3) dma1k(rs, lds + wl[(mi - NM0) % 3], l16, wg + wl[(mi - NM0) % 3]);
        if (mi >= 5 && mi < 19) p_chk(0, 1, mi - 5);
        if (mi >= GM && mi < GM + 14) glue(mi - GM);
        if (mi >= 47 && mi < 54) { p_chk(1, 1, 2 * (mi - 47)); SCHED(); p_chk(1, 1, 2 * (mi - 47) + 1); }
        if (mi >= NM0 && mi < 40 && NA0 + mi - NM0 < 60) LDA(NA0 + mi - NM0);
        if (mi >= 66 && mi < 69) *(f32x4*)(lds + eo + (mi - 66) * 1024) = acc[(mi - 66) % 3];
        SCHED();
      });
    };
    __builtin_amdgcn_s_setprio(3);
    asm volatile("s_nop 7");
    red_put(6, 3);
    SCHED();
    BAR();
#pragma unroll
    for (int i = 0; i < 8; ++i) red_get(i);
    SCHED();
#pragma unroll
    for (int k = 0; k < 20; ++k) red_fin(6, k < 16 ? k : k + 2);
    SCHED();
    __builtin_amdgcn_s_setprio(0);
    STAMPT(16);
#pragma unroll
    for (int k = 9; k < 14; ++k) p_chk(0, 0, k);
    SCHED();
    g3(IC<0>(), IC<MB>());
    STAMPT(22);
    DRAIN();
    RBAR();
    STAMPT(27);
#define MY 12
    if (MY == 0 || !kh) {
#pragma unroll
      for (int i = 0; i < 8; ++i) { ld_p(1, i); SCHED(); }
    }
    g3(IC<MB>(), IC<MB + MY>());
    if (MY > 0 && kh) {
#pragma unroll
      for (int i = 0; i < 8; ++i) { ld_p(1, i); SCHED(); }
    }
    STAMPT(26);
    g3(IC<MB + MY>(), IC<40>());
    STAMPT(24);
#pragma unroll
    for (int m = NA0 + 40 - NM0; m < 70; ++m) LDA(m);
#pragma unroll
    for (int x = 0; x < 3; ++x) gf[x] = *(const f32x4*)(lds + go + x * 64);
    SCHED();
    const rsrc_t ro = __builtin_amdgcn_make_buffer_rsrc((void*)p.out, 0, S_ * D_ * 2, 0x00020000);
    STAMPT(17);
#pragma unroll
    for (int k = 0; k < 14; ++k) p_chk(1, 0, k);
    asm volatile("s_nop 1");
    STAMPT(25);
    g3(IC<40>(), IC<70>());
    STAMPT(18);
    BAR();
    STAMPT(28);
    f32x4 o4[3];
    u32x4 ov[2];
#pragma unroll
    for (int j = 2; j >= 0; --j) o4[j] = *(const volatile LAS f32x4*)(lds + ho + j * 1024);
    asm volatile("" : "+v"(acc[2]), "+v"(acc[3]), "+v"(acc[4]));
    SCHED();
#pragma unroll
    for (int j = 2; j >= 0; --j) {
      const f32x4 v = __builtin_elementwise_fma(gf[j], o4[j] + acc[2 + j], xf[j]);
      *(volatile LAS u32x2*)(lds + tw + j * 32) = (u32x2){pack2(v[0], v[1]), pack2(v[2], v[3])};
      if (j < 2) ov[j] = *(const volatile LAS u32x4*)(lds + tr + j * 32);
    }
    __builtin_amdgcn_raw_buffer_store_b128(ov[1], ro, oo + 32u, os, 0);
    __builtin_amdgcn_raw_buffer_store_b128(ov[0], ro, o1, os, 0);
    if (bid == 0 && w == 5 && l8 == 0u) st4(rs, WS_EPOCH, tag);
  }
  STAMP(19);
}

extern "C" __device__ __attribute__((used, noinline, visibility("hidden"))) void mega_tail() {}

extern "C" void kernel_launch(void* const* d_in, const int* in_sizes, int n_in, void* d_out, int out_size,
                              void* d_ws, size_t ws_size, hipStream_t stream) {
  Params p;
  p.q = (const u16*)d_in[0];
  p.k = (const u16*)d_in[1];
  p.v = (const u16*)d_in[2];
  p.shift_k = (const u16*)d_in[3];
  p.ssm_k = (const u16*)d_in[4];
  p.ssm_D = (const u16*)d_in[5];
  p.w_in = (const u16*)d_in[7];
  p.w_sp = (const u16*)d_in[8];
  p.b_sp = (const u16*)d_in[9];
  p.w_out = (const u16*)d_in[10];
  p.cvec = (const u16*)d_in[11];
  p.w_mod = (const u16*)d_in[12];
  p.out = (u16*)d_out;
  p.ws = (char*)d_ws;
  mega<<<256, 512, 0, stream>>>(p);
}
```

```cpp
#include <hip/hip_runtime.h>
#include <stdint.h>

typedef unsigned short u16;
typedef __attribute__((ext_vector_type(8))) short bf16x8;
typedef __attribute__((ext_vector_type(4))) short bf16x4;
typedef __attribute__((ext_vector_type(4))) float f32x4;
typedef __attribute__((ext_vector_type(2))) float f32x2;
typedef __attribute__((ext_vector_type(4))) unsigned int u32x4;
typedef __attribute__((ext_vector_type(2))) unsigned int u32x2;
#define LAS __attribute__((address_space(3)))
typedef __amdgpu_buffer_rsrc_t rsrc_t;
typedef __attribute__((ext_vector_type(4))) short s16x4;

constexpr int S_ = 2048, D_ = 640, F_ = 1728, FH = 864, CB = 108;

constexpr unsigned WS_EPOCH = 0;
constexpr unsigned WS_FWI = 4096;
constexpr unsigned WS_FZ = WS_FWI + 384 * 128;
constexpr unsigned WS_FWO = WS_FZ + 256 * 128;
constexpr unsigned F4S = 896 * 128;
constexpr unsigned WS_CS = WS_FZ + 256 * 128;
constexpr unsigned WS_MOD = 131072;
constexpr unsigned WS_MODP = WS_MOD + 81920;
constexpr unsigned WS_ST1 = 393216;
constexpr unsigned WS_PART = 524288;
constexpr unsigned WS_CSP = WS_PART + 262144;
constexpr unsigned WS_FX = WS_CSP + 655360;
constexpr unsigned WS_X1B = WS_FX + 16 * 4 * 160 * 128;
constexpr unsigned WS_QB = WS_X1B + 2621440;
constexpr unsigned WS_KV3 = WS_QB + 2621440;
constexpr unsigned WS_MODQ = WS_KV3 + 1310720;
constexpr unsigned WBLK = 143360;
constexpr unsigned WS_WINF = WS_MODQ + 245760;
constexpr unsigned WS_WOUTF = WS_WINF + 16 * WBLK;
constexpr unsigned WS_Z2F = WS_WOUTF + 8 * WBLK;
constexpr unsigned WS_UF = WS_Z2F + 8 * 64 * 7168;
constexpr unsigned WS_F4 = WS_UF + 32 * 4 * 8 * 4096;
constexpr unsigned WS_END = WS_F4 + 4 * F4S;
constexpr unsigned WS_STAMP = 200u << 20;

constexpr int G_COPY = 4160;
constexpr int L_U = WBLK;
constexpr int U_CH = (256 + S_) * 2 + 32;
constexpr int L_FLG = L_U + 4 * U_CH;
constexpr int L_RT = 16384;
constexpr int L_CONST = WBLK;
constexpr int L_PST = L_CONST + 2048;
constexpr int L_Z = L_CONST + 5120;
constexpr int L_ST2 = L_Z + 14336;
constexpr int L_MISC = L_ST2 + 512;
constexpr int LDS_TOTAL = 163840;

#define AUX_T 610
#define RT2_T 360
#define CB_T 380
#define W_T 730
#define Y_T 930
#define K_T 460
#ifndef M_T
#define M_T 520
#endif
#define WA_T 580
constexpr int SC1 = 16, NT = 2;
constexpr unsigned SPIN_CAP = 1u << 16;

__device__ __forceinline__ float bflo(unsigned x) { return __uint_as_float(x << 16); }
__device__ __forceinline__ float bfhi(unsigned x) { return __uint_as_float(x & 0xffff0000u); }
__device__ __forceinline__ unsigned f2bf(float x) {
  __bf16 b = (__bf16)x;
  return (unsigned)__builtin_bit_cast(unsigned short, b);
}
__device__ __forceinline__ float rbf(float x) { return bflo(f2bf(x)); }
typedef __attribute__((ext_vector_type(2))) __bf16 bf2_t;
__device__ __forceinline__ unsigned pack2(float lo, float hi) {
  f32x2 v = {lo, hi};
  return __builtin_bit_cast(unsigned, __builtin_convertvector(v, bf2_t));
}
__device__ __forceinline__ f32x2 rbf2(f32x2 v) {
  const unsigned p = __builtin_bit_cast(unsigned, __builtin_convertvector(v, bf2_t));
  return (f32x2){__uint_as_float(p << 16), __uint_as_float(p & 0xffff0000u)};
}
__device__ __forceinline__ f32x4 rbf4(f32x4 v) {
  const f32x2 a = rbf2((f32x2){v[0], v[1]}), b = rbf2((f32x2){v[2], v[3]});
  return (f32x4){a[0], a[1], b[0], b[1]};
}
template <int CTRL>
__device__ __forceinline__ float dpp_f(float v) {
  return __int_as_float(__builtin_amdgcn_update_dpp(0, __float_as_int(v), CTRL, 0xf, 0xf, true));
}
__device__ __forceinline__ float sum8(float v) {
  v += dpp_f<0xB1>(v);
  v += dpp_f<0x4E>(v);
  v += dpp_f<0x141>(v);
  return v;
}
__device__ __forceinline__ float sum16(float v) {
  v = sum8(v);
  v += dpp_f<0x140>(v);
  return v;
}
__device__ __forceinline__ float sum_rows(float v) {
  auto a = __builtin_amdgcn_permlane16_swap(__float_as_uint(v), __float_as_uint(v), false, false);
  v = __uint_as_float(a[0]) + __uint_as_float(a[1]);
  auto b = __builtin_amdgcn_permlane32_swap(__float_as_uint(v), __float_as_uint(v), false, false);
  return __uint_as_float(b[0]) + __uint_as_float(b[1]);
}
#define MFMA16(a, b, c) __builtin_amdgcn_mfma_f32_16x16x32_bf16(__builtin_bit_cast(bf16x8, a), __builtin_bit_cast(bf16x8, b), c, 0, 0, 0)
#define MFMA16K16(a, b, c) __builtin_amdgcn_mfma_f32_16x16x16bf16_1k(__builtin_bit_cast(bf16x4, a), __builtin_bit_cast(bf16x4, b), c, 0, 0, 0)
__device__ __forceinline__ u32x2 trd(const char* p) {
  return __builtin_bit_cast(u32x2, __builtin_amdgcn_ds_read_tr16_b64_v4i16((LAS s16x4*)p));
}
__device__ __forceinline__ u32x4 trfrag(const char* p, int pitch) {
  u32x2 a = trd(p), b = trd(p + 16 * pitch);
  return (u32x4){a[0], a[1], b[0], b[1]};
}
#define BAR() asm volatile("s_waitcnt lgkmcnt(0)\n\ts_barrier" ::: "memory")
#define DRAIN() do { __builtin_amdgcn_s_waitcnt(0x0F70); asm volatile("" ::: "memory"); } while (0)
#define SCHED() __builtin_amdgcn_sched_barrier(0)
template <int N> struct IC { static constexpr int value = N; };
template <int I, int N, class F>
__device__ __forceinline__ void sfor(F&& f) {
  if constexpr (I < N) { f(IC<I>()); sfor<I + 1, N>(f); }
}

__device__ __forceinline__ u32x4 ld16(rsrc_t r, unsigned off) { return __builtin_amdgcn_raw_buffer_load_b128(r, off, 0, SC1); }
__device__ __forceinline__ u32x2 ld8(rsrc_t r, unsigned off) { return __builtin_amdgcn_raw_buffer_load_b64(r, off, 0, SC1); }
__device__ __forceinline__ unsigned ld4(rsrc_t r, unsigned off) { return __builtin_amdgcn_raw_buffer_load_b32(r, off, 0, SC1); }
#define ST_AUX 16
__device__ __forceinline__ void st16(rsrc_t r, unsigned off, u32x4 v) { __builtin_amdgcn_raw_buffer_store_b128(v, r, off, 0, ST_AUX); }
__device__ __forceinline__ void st8(rsrc_t r, unsigned off, u32x2 v) { __builtin_amdgcn_raw_buffer_store_b64(v, r, off, 0, SC1); }
__device__ __forceinline__ void st_flag4(rsrc_t r, unsigned off, unsigned tag, int l) {
  if (l < 32) st16(r, WS_F4 - WS_FWI + off + (unsigned)(l >> 3) * F4S + (unsigned)(l & 7) * 16u, (u32x4){tag, tag, tag, tag});
}
__device__ __forceinline__ void st4(rsrc_t r, unsigned off, unsigned v) { __builtin_amdgcn_raw_buffer_store_b32(v, r, off, 0, SC1); }
#define BULK_AUX 16
__device__ __forceinline__ u32x4 ld16p(rsrc_t r, unsigned off) { return __builtin_amdgcn_raw_buffer_load_b128(r, off, 0, BULK_AUX); }
__device__ __forceinline__ u32x2 ld8p(rsrc_t r, unsigned off) { return __builtin_amdgcn_raw_buffer_load_b64(r, off, 0, BULK_AUX); }
__device__ __forceinline__ void dma1k(rsrc_t r, char* lds_dst, unsigned l16, unsigned soff) {
  __builtin_amdgcn_raw_ptr_buffer_load_lds(r, (LAS void*)lds_dst, 16, l16, soff, 0, BULK_AUX);
}

#define STAMP(i)
#define STAMPF(i)
#define STAMPT(i) STAMP(i)

template <int N, int SLP>
__device__ __forceinline__ void wait_lines(rsrc_t r, const unsigned (&off)[N], unsigned tag) {
  bool need[N];
#pragma unroll
  for (int i = 0; i < N; ++i) need[i] = off[i] != ~0u;
  _Pragma("unroll 1") for (unsigned spins = 0; spins < SPIN_CAP; ++spins) {
    unsigned v[N];
#pragma unroll
    for (int i = 0; i < N; ++i) {
      v[i] = tag;
      if (need[i]) v[i] = ld4(r, off[i]);
    }
    bool any = false;
#pragma unroll
    for (int i = 0; i < N; ++i) { need[i] = v[i] != tag; any = any || need[i]; }
    if (!__any(any)) break;
    __builtin_amdgcn_s_sleep(SLP);
  }
  asm volatile("" ::: "memory");
}

__device__ __forceinline__ int tau(int rt, int n) { return 32 * (rt >> 1) + 8 * (n >> 2) + 4 * (rt & 1) + (n & 3); }
__device__ __forceinline__ int psi(int n) { return 2 * (n & 7) + (n >> 3); }
#define QX_CM(d, s0, s1, P) "v_cndmask_b32_dpp " d ", " s0 ", " s1 ", vcc quad_perm:" P " row_mask:0xf bank_mask:0xf\n\t"
#define QX_BODY(M, P) \
  asm("s_mov_b32 vcc_lo, " M "\n\ts_mov_b32 vcc_hi, " M "\n\t" \
      QX_CM("%0", "%12", "%8", P) "v_xor_b32 %4, %8, %0\n\t" QX_CM("%1", "%13", "%9", P) "v_xor_b32 %5, %9, %1\n\t" \
      QX_CM("%2", "%14", "%10", P) "v_xor_b32 %6, %10, %2\n\t" QX_CM("%3", "%15", "%11", P) "v_xor_b32 %7, %11, %3" \
      : "=&v"(t[0]), "=&v"(t[1]), "=&v"(t[2]), "=&v"(t[3]), "=&v"(u[0]), "=&v"(u[1]), "=&v"(u[2]), "=&v"(u[3]) \
      : "v"(a[0]), "v"(a[1]), "v"(a[2]), "v"(a[3]), "v"(b[0]), "v"(b[1]), "v"(b[2]), "v"(b[3]) : "vcc")
template <int BIT>
__device__ __forceinline__ void quad_xchg3(u32x4& a, u32x4& b) {
  unsigned t[4], u[4];
  if (BIT == 0) QX_BODY("0x55555555", "[1,0,3,2]");
  else QX_BODY("0x33333333", "[2,3,0,1]");
  a = (u32x4){t[0], t[1], t[2], t[3]};
#pragma unroll
  for (int d = 0; d < 4; ++d) b[d] ^= (unsigned)__builtin_amdgcn_update_dpp(0, (int)u[d], BIT == 0 ? 0xB1 : 0x4E, 0xf, 0xf, true);
}
__device__ __forceinline__ void quad_transpose(u32x4& x0, u32x4& x1, u32x4& x2, u32x4& x3) {
  quad_xchg3<0>(x0, x1);
  quad_xchg3<0>(x2, x3);
  quad_xchg3<1>(x0, x2);
  quad_xchg3<1>(x1, x3);
  asm volatile("" : "+v"(x0), "+v"(x1), "+v"(x2), "+v"(x3));
}

struct Params {
  const u16 *q, *k, *v, *shift_k, *ssm_k, *ssm_D, *w_in, *w_sp, *b_sp, *w_out, *cvec, *w_mod;
  u16* out;
  char* ws;
};

__global__ __launch_bounds__(512, 2) void mega(Params p) {
  __shared__ __attribute__((aligned(16))) char lds[LDS_TOTAL];
  {
    unsigned long long pc0;
    unsigned vt;
    asm volatile("v_readfirstlane_b32 vcc_lo, %3\n\ts_cmpk_lt_u32 %2, 192\n\ts_cbranch_scc1 4f\n\ts_cmpk_lt_u32 vcc_lo, 64\n\ts_cbranch_scc0 4f\n\t"
                 "s_lshr_b32 vcc_hi, %2, 3\n\ts_sub_u32 vcc_hi, vcc_hi, 24\n\ts_lshl_b32 vcc_hi, vcc_hi, 13\n\t"
                 "v_lshlrev_b32 %1, 7, %3\n\tv_add_u32 %1, vcc_hi, %1\n\ts_getpc_b64 %0\n.Lmega_here:\n\t"
                 "v_min_u32 %1, mega_tail-.Lmega_here-4, %1\n\ts_movk_i32 m0, 0x2000\n\ts_nop 1\n\tglobal_load_lds_dword %1, %0\n4:\n\t"
                 "s_cmpk_lt_u32 %2, 160\n\ts_cbranch_scc0 1f\n\ts_cmpk_lt_u32 vcc_lo, 256\n\ts_cbranch_scc1 3f\n\t"
                 "s_call_b64 vcc, .Ltouch1\n\ts_branch 3f\n1:\n\t"
                 "s_cmpk_lt_u32 vcc_lo, 256\n\ts_cbranch_scc1 2f\n\ts_cmpk_lt_u32 %2, 224\n\ts_cbranch_scc0 2f\n\ts_call_b64 vcc, .Ltouch3\n\ts_branch 3f\n"
                 "2:\n\ts_call_b64 vcc, .Ltouch2\n3:"
                 : "=&s"(pc0), "=&v"(vt) : "s"(blockIdx.x), "v"(threadIdx.x) : "vcc", "scc", "memory");
  }
  SCHED();
  const int tid = threadIdx.x, l = tid & 63;
  const int w = __builtin_amdgcn_readfirstlane(tid >> 6);
  const int bid = blockIdx.x, xcd = bid & 7, idx = bid >> 3;
#define TOUCH_AT(n) asm volatile("s_branch 1f\n.Ltouch" #n ":\n\ts_setpc_b64 vcc\n1:")
  char* const ws = p.ws;
  asm volatile("" ::"s"(p.q), "s"(p.k), "s"(p.v), "s"(p.shift_k), "s"(p.ssm_k), "s"(p.ssm_D), "s"(p.w_in), "s"(p.w_sp), "s"(p.b_sp),
               "s"(p.w_out), "s"(p.cvec), "s"(p.w_mod), "s"(p.out), "s"(p.ws));
  unsigned epoch, tag, t_start;
  unsigned long long t64;
  asm volatile("s_load_dword %0, %2, 0x0\n\ts_memrealtime %1" : "=&s"(epoch), "=&s"(t64) : "s"(ws));
#define TAG_READY() do { SCHED(); \
    asm volatile("s_waitcnt lgkmcnt(0)\n\ts_mul_i32 %0, %2, 0x9e3779b1\n\ts_add_i32 %0, %0, 0x9e3779b9\n\ts_mov_b64 %1, %3" \
                 : "=&s"(tag), "=&s"(t64r) : "s"(epoch), "s"(t64) : "scc"); t_start = (unsigned)t64r; SCHED(); } while (0)
  unsigned long long t64r;
  const rsrc_t rs = __builtin_amdgcn_make_buffer_rsrc(ws, 0, WS_END, 0x00020000);
  const unsigned l16 = (unsigned)l * 16u;
  STAMP(0);
  const bool is_conv = idx < 20;
  const int quad = xcd * 20 + idx;
  const int R = is_conv ? (xcd & 3) * 5 + (idx >> 2) : (xcd & 3) * 3 + 15 + (idx >> 2);
  const int c = (xcd >> 2) * 4 + (idx & 3);
  const unsigned fwi = WS_F4 + (unsigned)(xcd & 3) * F4S, fz = fwi + (WS_FZ - WS_FWI);
  const int n16 = l & 15, h4 = l >> 4;
  u32x4 wsp[4][8];
#define ROTC 2
#define WAUX 0
  const int rotg = ((idx >> 4) ^ ((ROTC & 2) ? idx >> 1 : 0)) & 1, rotb = (idx >> 2) & 3, rotr = (ROTC & 1) ? (idx & 1) * 2 : 0;
  const rsrc_t rsp = __builtin_amdgcn_make_buffer_rsrc((void*)p.w_sp, 0, S_ * S_ * 2, 0x00020000);
  const unsigned wsp_vo = (unsigned)(((l >> 2) & 3) * 8 * 4096 + h4 * 64 + ((l + rotb) & 3) * 16);
#define LDW(k) LDW_(k)
#define WSP_TR(g) WSP_TR_(g)
#define WROW R
#define LDW_(k) wsp[((k) >> 2) & 3][((k) >> 4) * 4 + ((k) & 3)] = __builtin_amdgcn_raw_buffer_load_b128(rsp, wvo, \
    (unsigned)((WROW * 64 + 16 * ((((k) >> 2) & 2) ^ rotr) + 4 * (((k) >> 2) & 1) + ((k) & 3)) * 4096 + w * 512 + ((((k) >> 4) ^ rotg) * 256)), WAUX)
#define WSP_TR_(g) quad_transpose(wsp[(g) & 3][((g) >> 2) * 4], wsp[(g) & 3][((g) >> 2) * 4 + 1], wsp[(g) & 3][((g) >> 2) * 4 + 2], wsp[(g) & 3][((g) >> 2) * 4 + 3])
  const rsrc_t rwi = __builtin_amdgcn_make_buffer_rsrc((void*)p.w_in, 0, D_ * F_ * 2, 0x00020000);
  const rsrc_t rwo = __builtin_amdgcn_make_buffer_rsrc((void*)p.w_out, 0, FH * D_ * 2, 0x00020000);
  const rsrc_t rq = __builtin_amdgcn_make_buffer_rsrc((void*)p.q, 0, S_ * D_ * 2, 0x00020000);
  auto hold = [&](unsigned tt) { while ((unsigned)__builtin_amdgcn_s_memrealtime() - t_start < tt) __builtin_amdgcn_s_sleep(2); };
  auto ne2 = [&](unsigned x, unsigned y) { unsigned d; asm("v_bitop3_b32 %0, %1, %2, %3 bitop3:0x7e" : "=v"(d) : "v"(x), "v"(y), "s"(tag)); return d; };
  volatile LAS unsigned* lf = (volatile LAS unsigned*)(lds + L_FLG);
  auto lf_set = [&](int i) { asm volatile("s_waitcnt lgkmcnt(0)" ::: "memory"); if (l == 0) lf[i] = tag; };
  auto lf_wait = [&](int i0, int n) {
    _Pragma("unroll 1") for (unsigned spins = 0; spins < (SPIN_CAP << 4); ++spins) {
      const unsigned v = lf[i0 + (l & (n - 1))];
      if (__all(v == tag)) break;
      __builtin_amdgcn_s_sleep(1);
    }
    asm volatile("" ::: "memory");
  };
  auto load_b2 = [&](int k0, int k1) {
#pragma unroll
    for (int k = k0; k < k1; ++k) {
      int i = k + (idx >> 2) * 4;
      i = (i >= 35 ? i - 35 : i) + 35 * w;
      dma1k(rs, lds + i * 1024, l16, WS_WINF + (unsigned)(8 + c) * WBLK + (unsigned)i * 1024u);
    }
  };
  char* const wo_st = lds + 122880;
  auto wo_ld = [&]() {
    const int U = R < 28 ? R : 0, u2 = U >> 1, fi = (u2 + 1) % 3;
    const int c0 = 4 * (U & 1) + (u2 < 8 ? u2 >> 1 : u2 < 11 ? 0 : 2);
    const int b0 = u2 < 8 ? CB * c0 + 2 * (u2 & 1) + 96 : CB * (c0 + (fi >> 1)) + 48 + 2 * (fi & 1), d1 = u2 < 8 ? -96 : 16, d2 = d1 + 16;
    const int b3 = u2 < 8 ? b0 - 64 : CB * (c0 + 1) + 50 + 16 * fi;
#pragma unroll
    for (int r = 0; r < 5; ++r) {
      const int g = r * 64 + l, rho = g / 10;
      const int fb = (rho & 16) ? ((rho & 2) ? b3 : b0 + d2) : ((rho & 2) ? b0 + d1 : b0);
      __builtin_amdgcn_raw_ptr_buffer_load_lds(rwo, (LAS void*)(wo_st + r * 1024), 16, (unsigned)((fb + (rho & 13)) * (D_ * 2) + (g % 10) * 16 + c * 160), 0, 0, 0);
    }
  };
  auto rt_ld = [&](int i0, char* st) {
    const int cb = (i0 < 160 ? 8 : 0) + (i0 & 7), Q = (i0 < 160 ? i0 : i0 - 160) >> 3;
#pragma unroll
    for (int r = 0; r < 7; ++r) {
      const int g = r * 64 + l;
      __builtin_amdgcn_raw_ptr_buffer_load_lds(rwi, (LAS void*)(st + r * 1024), 16, (unsigned)((g / 14) * (F_ * 2) + (g % 14) * 16 + Q * 32 * F_ * 2 + cb * CB * 2),
                                               0, 0, NT);
    }
  };
  auto rt_job = [&](int i0, char* st) {
    const int cb = (i0 < 160 ? 8 : 0) + (i0 & 7), Q = (i0 < 160 ? i0 : i0 - 160) >> 3;
    const int col = 640 * (l >> 5) + 32 * Q + (l & 31);
    DRAIN();
    u32x4 x[7], o[7];
    f32x2 xu[7][4];
    {
      const char* bp = st + (4 * h4 + (n16 >> 2)) * 224 + (n16 & 3) * 8;
#pragma unroll
      for (int j = 0; j < 7; ++j) x[j] = trfrag(bp + j * 32, 224);
#pragma unroll
      for (int j = 0; j < 7; ++j)
#pragma unroll
        for (int e = 0; e < 4; ++e) { xu[j][e] = (f32x2){bflo(x[j][e]), bfhi(x[j][e])}; asm volatile("" : "+v"(xu[j][e])); }
    }
    unsigned mk[4][2], pv = (unsigned)col * 8u, cw = l >= 32 ? (unsigned)(7168 + (l - 32) * 4) : (unsigned)(7298 + l * 2 - 4), m12 = n16 == 12 ? ~0u : 0u;
    unsigned fgo = l < 32 ? (unsigned)(l >> 3) * F4S + (unsigned)(l & 7) * 16u : 0x80000000u;
#pragma unroll
    for (int i = 0; i < 4; ++i) {
      mk[i][0] = n16 == 4 * i ? 0x3F803F80u : 0u; mk[i][1] = n16 == 4 * i + 1 ? ~0u : 0u;
      asm volatile("" : "+v"(mk[i][0]), "+v"(mk[i][1]));
    }
    asm volatile("" : "+v"(pv), "+v"(cw), "+v"(m12), "+v"(fgo));
    unsigned ob = WS_WINF + (unsigned)cb * WBLK + (unsigned)(Q * 7) * 1024u;
    asm volatile("" : "+s"(ob));
    u32x2 pp8[8];
    hold(CB_T);
    _Pragma("unroll 1") for (unsigned spins = 0; spins < SPIN_CAP; ++spins) {
#pragma unroll
      for (int i = 0; i < 8; ++i) pp8[i] = __builtin_amdgcn_raw_buffer_load_b64(rs, pv, WS_MODP + (unsigned)i * 10240u, SC1);
      if (!__any(((ne2(pp8[0][0], pp8[1][0]) | ne2(pp8[2][0], pp8[3][0]) | ne2(pp8[4][0], pp8[5][0])) | ne2(pp8[6][0], pp8[7][0])) != 0u)) break;
      __builtin_amdgcn_s_sleep(4);
    }
    STAMPF(9);
    __builtin_amdgcn_s_setprio(3);
    {
      auto pf = [&](int i) { return __uint_as_float(pp8[i][1]); };
      const float t = rbf(((pf(0) + pf(1)) + (pf(2) + pf(3))) + ((pf(4) + pf(5)) + (pf(6) + pf(7))));
      if (l >= 32) *(float*)(st + cw) = rbf(1.0f + t);
      else *(u16*)(st + cw + 2) = (u16)(__float_as_uint(t) >> 16);
    }
    asm volatile("" ::: "memory");
    const f32x4 ca = *(const volatile LAS f32x4*)(st + 7168 + h4 * 16), cc = *(const volatile LAS f32x4*)(st + 7168 + 64 + h4 * 16);
    const u32x2 e0 = *(const volatile LAS u32x2*)(st + 7296 + h4 * 8), e1 = *(const volatile LAS u32x2*)(st + 7328 + h4 * 8);
    SCHED();
#pragma unroll
    for (int j = 0; j < 7; ++j) {
      const f32x2 y0 = xu[j][0] * (f32x2){ca[0], ca[1]}, y1 = xu[j][1] * (f32x2){ca[2], ca[3]}, y2 = xu[j][2] * (f32x2){cc[0], cc[1]}, y3 = xu[j][3] * (f32x2){cc[2], cc[3]};
      o[j] = (u32x4){pack2(y0[0], y0[1]), pack2(y1[0], y1[1]), pack2(y2[0], y2[1]), pack2(y3[0], y3[1])};
      if (j == 6) {
#pragma unroll
        for (int e = 0; e < 4; ++e) o[j][e] = (o[j][e] & ~m12) | mk[3][0];
      }
      __builtin_amdgcn_raw_buffer_store_b128(o[j], rs, l16 + (unsigned)(j & 3) * 1024u, ob + (unsigned)(j >> 2) * 4096u, SC1);
      SCHED();
    }
    __builtin_amdgcn_s_setprio(0);
    STAMPF(11);
    f32x4 d[2] = {{0.f, 0.f, 0.f, 0.f}, {0.f, 0.f, 0.f, 0.f}};
#pragma unroll
    for (int j = 0; j < 7; ++j) {
      const unsigned one = mk[j & 3][0], sr = mk[j & 3][1];
      d[j >> 2] = MFMA16(((u32x4){one, one, one, one}), o[j], d[j >> 2]);
      d[j >> 2] = MFMA16(((u32x4){sr & e0[0], sr & e0[1], sr & e1[0], sr & e1[1]}), x[j], d[j >> 2]);
    }
#pragma unroll
    for (int m = 0; m < 2; ++m)
      st16(rs, WS_CSP + (unsigned)((cb * 20 + Q) * 2 + m) * 1024u + l16, (u32x4){tag, __float_as_uint(d[m][0]), tag, __float_as_uint(d[m][1])});
    STAMPF(13);
    u32x4 tg4 = {tag, tag, tag, tag};
    asm volatile("s_waitcnt vmcnt(2)" : "+v"(tg4) :: "memory");
    __builtin_amdgcn_raw_buffer_store_b128(tg4, rs, fgo, WS_F4 + (unsigned)(cb * 20 + Q) * 128u, SC1);
  };

  auto red8 = [&](float v) { v += dpp_f<0x128>(v); return sum_rows(v); };
  const rsrc_t rwm = __builtin_amdgcn_make_buffer_rsrc((void*)p.w_mod, 0, D_ * 3840 * 2, 0x00020000);
  auto gemv_load = [&](u32x4* pre, int blk, int sl, int np) {
    pre[10][0] = *(const unsigned*)(p.cvec + 8 * np * sl + 2 * (l < 4 * np ? l : 0));
#pragma unroll
    for (int pp = 0; pp < np; ++pp)
      pre[pp] = __builtin_amdgcn_raw_buffer_load_b128(rwm, (unsigned)((l >> 3) * 7680 + (l & 7) * 16), (unsigned)((8 * np * sl + pp * 8) * 7680 + blk * 128), NT);
  };
  auto gemv_fin = [&](float* acc, const u32x4* pre, int np, char* scr) {
    const unsigned cv2 = pre[10][0];
    unsigned* svt = (unsigned*)scr + w * 96;
#pragma unroll
    for (int e = 0; e < 2; ++e) {
      const int row = 2 * l + e;
      const float cv = e ? bfhi(cv2) : bflo(cv2);
      if (l < 4 * np) svt[(row & 7) * 12 + (row >> 3)] = __float_as_uint(rbf(cv * __builtin_amdgcn_rcpf(1.0f + __expf(-cv))));
    }
    asm volatile("s_waitcnt lgkmcnt(0)" ::: "memory");
    u32x4 sv[3];
#pragma unroll
    for (int i = 0; i < (np + 3) / 4; ++i) sv[i] = *(const u32x4*)(svt + (l >> 3) * 12 + 4 * i);
    SCHED();
#pragma unroll
    for (int e = 0; e < 8; ++e) acc[e] = 0.f;
#pragma unroll
    for (int pp = 0; pp < np; ++pp) {
      const unsigned sh = sv[pp >> 2][pp & 3], sl2 = sh >> 16;
#pragma unroll
      for (int e = 0; e < 4; ++e) {
        const unsigned x = pre[pp][e];
        acc[2 * e] = __builtin_amdgcn_fdot2_f32_bf16(__builtin_bit_cast(bf2_t, x), __builtin_bit_cast(bf2_t, sl2), acc[2 * e], false);
        acc[2 * e + 1] = __builtin_amdgcn_fdot2_f32_bf16(__builtin_bit_cast(bf2_t, x), __builtin_bit_cast(bf2_t, sh), acc[2 * e + 1], false);
      }
    }
#pragma unroll
    for (int e = 0; e < 8; ++e) acc[e] = red8(acc[e]);
  };
  auto gemv_put = [&](const float* acc, unsigned off) {
    float v = acc[0];
#pragma unroll
    for (int e = 1; e < 8; ++e) { v = (l >> 3) == e ? acc[e] : v; asm("" : "+v"(v)); }
    st8(rs, off + (unsigned)l * 8u, (u32x2){tag, (unsigned)__builtin_amdgcn_ds_bpermute(((l & 7) * 8 + (l >> 3)) * 4, (int)__float_as_uint(v))});
  };
  auto cs_ld = [&](u32x4* v, int cj) {
    const int cb = cj / 7, j = cj % 7;
#pragma unroll
    for (int k = 0; k < 5; ++k)
      v[k] = __builtin_amdgcn_raw_buffer_load_b128(rs, (unsigned)(h4 * 10240 + n16 * 16), WS_CSP + (unsigned)((cb * 40 + (j >> 2) + 2 * k) * 1024 + (j & 3) * 256), SC1);
  };
  auto cs_fin = [&](u32x4* v, int cj) {
    auto bad = [&]() { return __any(((ne2(v[0][0], v[0][2]) | ne2(v[1][0], v[1][2]) | ne2(v[2][0], v[2][2])) | ne2(v[3][0], v[3][2]) | ne2(v[4][0], v[4][2])) != 0u); };
    if (bad()) {
      _Pragma("unroll 1") for (unsigned spins = 0; spins < SPIN_CAP; ++spins) {
        __builtin_amdgcn_s_sleep(8);
        cs_ld(v, cj);
        if (!bad()) break;
      }
    }
    float t0 = 0.f, t1 = 0.f;
#pragma unroll
    for (int k = 0; k < 5; ++k) { t0 += __uint_as_float(v[k][1]); t1 += __uint_as_float(v[k][3]); }
    t0 = sum_rows(t0); t1 = sum_rows(t1);
    if (l < 16) st16(rs, WS_CS + (unsigned)((cj / 7) * 2048 + (cj % 7) * 256) + l16, (u32x4){tag, __float_as_uint(t0), tag, __float_as_uint(t1)});
  };
  const bool cs2 = w == 2 && R >= 20 && R < 27, cs1 = w == 2 && R >= 25;
  u32x4 csv[5];
  typedef const __attribute__((address_space(4))) unsigned* cptr_t;

  if (__builtin_expect(is_conv, 1)) {
    if (w >= 4) {
      TOUCH_AT(1);
      const int pw = w - 4;
      const rsrc_t ra = __builtin_amdgcn_make_buffer_rsrc((void*)((idx & 2) ? p.v : p.k), 0, S_ * D_ * 2, 0x00020000);
      const rsrc_t rb = __builtin_amdgcn_make_buffer_rsrc((void*)((idx & 2) ? p.k : p.v), 0, S_ * D_ * 2, 0x00020000);
      const rsrc_t rk = __builtin_amdgcn_make_buffer_rsrc((void*)p.k, 0, S_ * D_ * 2, 0x00020000);
      const rsrc_t rv = __builtin_amdgcn_make_buffer_rsrc((void*)p.v, 0, S_ * D_ * 2, 0x00020000);
#define NROT 2
      const unsigned vo = (unsigned)((128 * pw + 2 * l) * (D_ * 2));
      const unsigned so0 = (unsigned)((idx & 1) * (D_ * 2) + quad * 8), so1 = (unsigned)((1 - (idx & 1)) * (D_ * 2) + quad * 8);
      u32x2 g[3][4], kh;
      auto gl = [&](int s, int j0, int j1) {
        __builtin_amdgcn_s_setprio(0);
#pragma unroll
        for (int j = j0; j < j1; ++j) {
          if (s < NROT) g[s][j] = __builtin_amdgcn_raw_buffer_load_b64(j < 2 ? ra : rb, vo, ((j & 1) ? so1 : so0) + (unsigned)(s * 512 * D_ * 2), 0);
          else g[s][j] = __builtin_amdgcn_raw_buffer_load_b64(j < 2 ? rk : rv, vo + (unsigned)((j & 1) * D_ * 2), (unsigned)(quad * 8 + s * 512 * D_ * 2), 0);
          SCHED();
        }
        __builtin_amdgcn_s_setprio(3);
      };
      const cptr_t sp = (cptr_t)(p.shift_k + quad * 16);
      unsigned spv[8];
#pragma unroll
      for (int i = 0; i < 8; ++i) spv[i] = sp[i];
      __builtin_amdgcn_s_sleep(3);
      gl(0, 0, 4);
      {
        const int row = 512 * (l >> 2) + 128 * pw - 3 + (l & 3);
        kh = __builtin_amdgcn_raw_buffer_load_b64(rk, (l >= 12 || (l & 3) == 3 || row < 0) ? 0x80000000u : (unsigned)(row * D_ * 2 + quad * 8), 0, 0);
      }
      TAG_READY();
      f32x2 wt[2][4];
#pragma unroll
      for (int d = 0; d < 2; ++d) {
        wt[d][0] = (f32x2){bflo(spv[4 * d]), bflo(spv[4 * d + 2])}; wt[d][1] = (f32x2){bfhi(spv[4 * d]), bfhi(spv[4 * d + 2])};
        wt[d][2] = (f32x2){bflo(spv[4 * d + 1]), bflo(spv[4 * d + 3])}; wt[d][3] = (f32x2){bfhi(spv[4 * d + 1]), bfhi(spv[4 * d + 3])};
        asm volatile("" : "+s"(wt[d][0]), "+s"(wt[d][1]), "+s"(wt[d][2]), "+s"(wt[d][3]));
      }
      SCHED();
      {
        const int j = idx * 4 + pw;
        const unsigned o = (unsigned)((512 + 64 * (j >> 2) + l) * (D_ * 2) + (((160 * xcd) >> 7) + ((j >> 1) & 1)) * 128);
        __builtin_amdgcn_raw_ptr_buffer_load_lds((j & 1) ? ra : rb, (LAS void*)(lds + 98304), 4, j < 64 ? o : 0x80000000u, 0, 0, 0);
      }
      SCHED();
      gl(1, 0, 4);
      STAMP(21);
      unsigned up = (unsigned)(L_U + 512 + 256 * pw + (l >> 2) * 16 + (3 - (l & 3)) * 4);
      unsigned lfo = l == 0 ? (unsigned)(L_FLG + 4 * pw) : (unsigned)(100352 + tid * 4), lfr = (unsigned)(L_FLG + (l & 3) * 4);
      unsigned yr = (unsigned)(L_U + (n16 >> 2) * U_CH + (128 * pw + 32 * h4 + 8 * (n16 & 1) + 4 * ((n16 >> 1) & 1)) * 2);
      unsigned yo = (unsigned)(8 * pw + (l >> 3)) * 20480u + (unsigned)quad * 128u + (unsigned)(l & 7) * 16u;
      unsigned mo = (unsigned)((n16 < 12 ? n16 >> 2 : 0) * D_ + quad * 4 + (l & 3)) * 8u + (unsigned)h4 * 40960u;
      unsigned k3o = (unsigned)(16 * pw + (l >> 2)) * 20480u + (unsigned)(((l >> 1) & 1) * 10240 + (2 * quad + (l & 1)) * 16);
      unsigned k3d = (unsigned)(L_U + (2 * (l & 1) + ((l >> 1) & 1)) * U_CH + 512 + (1536 + 128 * pw + 8 * (l >> 2)) * 2);
      unsigned xfo = (unsigned)(((4 * (l >> 5) + pw) * 4 + ((l >> 3) & 3)) * 160 + quad) * 128u + (unsigned)(l & 7) * 16u;
      u32x4 tg4 = {tag, tag, tag, tag};
      asm volatile("" : "+v"(up), "+v"(lfo), "+v"(lfr), "+v"(yr), "+v"(yo), "+v"(mo), "+v"(k3o), "+v"(k3d), "+v"(xfo), "+v"(tg4));
      unsigned so = WS_ST1 + (unsigned)xcd * 16384u + (unsigned)pw * 1024u;
      asm volatile("" : "+s"(so));
      auto pf_set = [&](int i) { asm volatile("" ::: "memory"); *(volatile LAS unsigned*)(lds + lfo + 4 * i) = tg4[0]; };
      auto pf_wait = [&](int i0) {
        _Pragma("unroll 1") for (unsigned spins = 0; spins < (SPIN_CAP << 4); ++spins) {
          if (__all(*(volatile LAS unsigned*)(lds + lfr + 4 * i0) == tag)) break;
          __builtin_amdgcn_s_sleep(1);
        }
        asm volatile("" ::: "memory");
      };
      unsigned hl[2][3];
      auto halo = [&](int s) {
#pragma unroll
        for (int i = 0; i < 6; ++i) { hl[i & 1][i >> 1] = (unsigned)__builtin_amdgcn_readlane(kh[i & 1], 4 * s + (i >> 1)); asm volatile("" : "+v"(hl[i & 1][i >> 1])); }
      };
      auto kv = [&](auto S__, auto N__) {
        constexpr int s = decltype(S__)::value;
        u32x2* x = g[s];
        SCHED();
        asm volatile("s_waitcnt vmcnt(%5)" : "+v"(x[0]), "+v"(x[1]), "+v"(x[2]), "+v"(x[3]), "+v"(kh) : "n"(decltype(N__)::value));
        if (s == 2) STAMP(27);
        if (s == 0) halo(0);
        if (s < NROT) {
          if (idx & 1) { asm volatile(""); const u32x2 t = x[0], u = x[2]; x[0] = x[1]; x[1] = t; x[2] = x[3]; x[3] = u; }
          asm volatile("" : "+v"(x[0]), "+v"(x[1]), "+v"(x[2]), "+v"(x[3]));
          if (idx & 2) { asm volatile(""); const u32x2 t = x[0], u = x[1]; x[0] = x[2]; x[1] = x[3]; x[2] = t; x[3] = u; }
          asm volatile("" : "+v"(x[0]), "+v"(x[1]), "+v"(x[2]), "+v"(x[3]));
        }
        f32x2 kf[2][5], o[2][2];
#pragma unroll
        for (int d = 0; d < 2; ++d) {
          unsigned kk[5];
          kk[3] = x[0][d]; kk[4] = x[1][d];
          kk[2] = __builtin_amdgcn_update_dpp(hl[d][2], kk[4], 0x138, 0xf, 0xf, false);
          kk[1] = __builtin_amdgcn_update_dpp(hl[d][1], kk[3], 0x138, 0xf, 0xf, false);
          kk[0] = __builtin_amdgcn_update_dpp(hl[d][0], kk[2], 0x138, 0xf, 0xf, false);
#pragma unroll
          for (int j = 0; j < 5; ++j) kf[d][j] = (f32x2){bflo(kk[j]), bfhi(kk[j])};
        }
#pragma unroll
        for (int t = 0; t < 5; ++t) {
#pragma unroll
          for (int i = 0; i < 4; ++i) {
            const int d = i >> 1, r = i & 1;
            if (t == 4) o[d][r] *= (f32x2){bflo(x[2 + r][d]), bfhi(x[2 + r][d])};
            else if (t == 0) o[d][r] = kf[d][r + 3] * wt[d][0];
            else o[d][r] = __builtin_elementwise_fma(kf[d][r + 3 - t], wt[d][t], o[d][r]);
          }
          asm volatile("" : "+v"(o[0][0]), "+v"(o[0][1]), "+v"(o[1][0]), "+v"(o[1][1]));
        }
#pragma unroll
        for (int i = 0; i < 4; ++i) *(unsigned*)(lds + up + i * U_CH + s * 1024) = pack2(o[i >> 1][1][i & 1], o[i >> 1][0][i & 1]);
        pf_set(4 * s);
        SCHED();
        if (s < 2) halo(s + 1);
        SCHED();
      };
      u32x4 sv[4], qv[4];
      u32x2 mv[2];
      auto ld_e = [&]() {
#pragma unroll
        for (int s = 0; s < 4; ++s) sv[s] = __builtin_amdgcn_raw_buffer_load_b128(rs, l16, so + (unsigned)s * 4096u, SC1);
        SCHED();
      };
      auto s_bad = [&]() { return __any(((ne2(sv[0][1], sv[0][3]) | ne2(sv[1][1], sv[1][3]) | ne2(sv[2][1], sv[2][3])) | ne2(sv[3][1], sv[3][3])) != 0u); };
      kv(IC<0>(), IC<5>());
      STAMP(23);
#define G2A 4
      gl(2, 0, G2A);
      kv(IC<1>(), IC<G2A>());
      gl(2, G2A, 4);
      STAMPF(9);
      u32x4 k3[2];
      auto ld_k3 = [&]() {
        k3[0] = __builtin_amdgcn_raw_buffer_load_b128(rs, k3o, WS_KV3, SC1);
        k3[1] = __builtin_amdgcn_raw_buffer_load_b128(rs, k3o, WS_KV3 + 5120u, SC1);
      };
      hold(K_T);
      ld_k3();
      SCHED();
      ld_e();
      kv(IC<2>(), IC<6>());
      STAMPF(11);
      auto k_bad = [&]() { return __any((ne2(k3[0][1], k3[0][3]) | ne2(k3[1][1], k3[1][3])) != 0u); };
      if (k_bad()) {
        _Pragma("unroll 1") for (unsigned spins = 0; spins < SPIN_CAP; ++spins) {
          __builtin_amdgcn_s_sleep(4);
          ld_k3();
          if (!k_bad()) break;
        }
      }
      *(u32x4*)(lds + k3d) = (u32x4){k3[0][0], k3[0][2], k3[1][0], k3[1][2]};
      pf_set(12);
      SCHED();
      STAMPF(18);
      if (s_bad()) {
        _Pragma("unroll 1") for (unsigned spins = 0; spins < SPIN_CAP; ++spins) {
          __builtin_amdgcn_s_sleep(4);
          ld_e();
          if (!s_bad()) break;
        }
      }
      asm volatile("" ::: "memory");
      SCHED();
      STAMPF(17);
      auto ld_m = [&]() {
        mv[0] = __builtin_amdgcn_raw_buffer_load_b64(rs, mo, WS_MODQ, SC1);
        mv[1] = __builtin_amdgcn_raw_buffer_load_b64(rs, mo, WS_MODQ + 20480u, SC1);
      };
#define MFIRST 1
      if (MFIRST) ld_m();
      SCHED();
#pragma unroll
      for (int s = 0; s < 4; ++s) qv[s] = __builtin_amdgcn_raw_buffer_load_b128(rs, yo, WS_QB + (unsigned)s * (32u * 20480u), BULK_AUX);
      SCHED();
      if (!MFIRST) ld_m();
      SCHED();
      STAMP(2);
      u32x2 yv[4][2];
      auto ld_y = [&](int s) {
#pragma unroll
        for (int e = 0; e < 2; ++e) yv[s][e] = trd(lds + yr + 1024 * s + 32 * e);
      };
      float r1[4][2], m1[4][2];
#pragma unroll
      for (int i = 0; i < 8; ++i) {
        const unsigned x = sv[i >> 1][2 * (i & 1)];
        r1[i >> 1][i & 1] = __builtin_amdgcn_rcpf(bfhi(x));
        m1[i >> 1][i & 1] = -bflo(x) * r1[i >> 1][i & 1];
        asm volatile("" : "+v"(r1[i >> 1][i & 1]), "+v"(m1[i >> 1][i & 1]));
      }
      pf_wait(20);
      ld_y(0); ld_y(1);
      SCHED();
      if (__any(ne2(mv[0][0], mv[1][0]) != 0u)) {
        _Pragma("unroll 1") for (unsigned spins = 0; spins < SPIN_CAP; ++spins) {
          __builtin_amdgcn_s_sleep(4);
          ld_m();
          if (!__any(ne2(mv[0][0], mv[1][0]) != 0u)) break;
        }
      }
      STAMPF(13);
      f32x4 al, be;
      {
        const float mgf = rbf(sum_rows(__uint_as_float(mv[0][1]) + __uint_as_float(mv[1][1])));
        auto rl = [&](int i) { return __uint_as_float(__builtin_amdgcn_readlane(__float_as_uint(mgf), i)); };
        const f32x4 sa = {rl(0), rl(1), rl(2), rl(3)}, ca = {rl(4), rl(5), rl(6), rl(7)}, ga = {rl(8), rl(9), rl(10), rl(11)};
        al = rbf4(ca + 1.0f) * ga;
        be = sa * ga;
        asm volatile("" : "+v"(al), "+v"(be));
      }
      f32x4 qf[2][2], P[2][2];
      auto xpre = [&](int s) {
#pragma unroll
        for (int e = 0; e < 2; ++e) {
          const float r = r1[s][e], m = m1[s][e];
          const f32x4 qq = {bflo(qv[s][2 * e]), bfhi(qv[s][2 * e]), bflo(qv[s][2 * e + 1]), bfhi(qv[s][2 * e + 1])};
          qf[s & 1][e] = qq;
          P[s & 1][e] = __builtin_elementwise_fma(__builtin_elementwise_fma(qq, (f32x4){r, r, r, r}, (f32x4){m, m, m, m}), al, be);
        }
        asm volatile("" : "+v"(qf[s & 1][0]), "+v"(qf[s & 1][1]), "+v"(P[s & 1][0]), "+v"(P[s & 1][1]));
      };
      auto xfin = [&](int s) {
        u32x4 o;
#pragma unroll
        for (int e = 0; e < 2; ++e) {
          const u32x2 y = yv[s][e];
          const f32x4 yf = {bflo(y[0]), bfhi(y[0]), bflo(y[1]), bfhi(y[1])};
          const f32x4 x = __builtin_elementwise_fma(P[s & 1][e], yf, qf[s & 1][e]);
          o[2 * e] = pack2(x[0], x[1]);
          o[2 * e + 1] = pack2(x[2], x[3]);
        }
        __builtin_amdgcn_raw_buffer_store_b128(o, rs, yo, WS_X1B + (unsigned)s * (32u * 20480u), SC1);
      };
      xpre(0); xfin(0);
      xpre(1); xfin(1);
      SCHED();
      xpre(2); xpre(3);
#define FVR 5
#define MSPLIT 0
      if (MSPLIT) {
        pf_wait(24);
        ld_y(2);
        xfin(2);
        SCHED();
        pf_wait(28);
        ld_y(3);
        xfin(3);
      } else {
        pf_wait(28);
        ld_y(2); ld_y(3);
        xfin(2); xfin(3);
      }
      STAMP(28);
      asm volatile("s_waitcnt vmcnt(2)" ::: "memory");
      __builtin_amdgcn_raw_buffer_store_b128(tg4, rs, xfo, WS_FX, SC1);
      SCHED();
      asm volatile("s_waitcnt vmcnt(1)" ::: "memory");
      __builtin_amdgcn_raw_buffer_store_b128(tg4, rs, xfo, WS_FX + 32u * 160u * 128u, SC1);
      STAMP(5);
    } else {
      const int ch = w;
      char* U = lds + L_U + ch * U_CH;
      char* G = lds + ch * (4 * G_COPY);
      {
        const rsrc_t rf = __builtin_amdgcn_make_buffer_rsrc((void*)(p.ssm_k + (size_t)(quad * 4 + ch) * S_), 0, S_ * 2, 0x00020000);
        u32x4 fa[4];
        u32x2 fb[4];
        __builtin_amdgcn_s_setprio(3);
#pragma unroll
        for (int k = 0; k < 4; ++k) {
          fa[k] = __builtin_amdgcn_raw_buffer_load_b128(rf, l16 + (unsigned)k * 1024u, 0, NT);
        }
        __builtin_amdgcn_s_setprio(1);
        TAG_READY();
#pragma unroll
        for (int k = 0; k < 4; ++k) {
#pragma unroll
          for (int d = 0; d < 2; ++d)
            fb[k][d] = __builtin_amdgcn_update_dpp(k < 3 ? (unsigned)__builtin_amdgcn_readfirstlane(fa[k < 3 ? k + 1 : k][d]) : 0u, fa[k][d], 0x130, 0xf, 0xf, false);
          const unsigned x[6] = {fa[k][0], fa[k][1], fa[k][2], fa[k][3], fb[k][0], fb[k][1]};
          unsigned y[5];
#pragma unroll
          for (int d = 0; d < 5; ++d) y[d] = __builtin_amdgcn_alignbit(x[d + 1], x[d], 16);
          char* gp = G + 32 + k * 1024 + l16;
          *(u32x4*)(gp) = fa[k];
          *(u32x4*)(gp + G_COPY) = (u32x4){y[0], y[1], y[2], y[3]};
          *(u32x4*)(gp + 2 * G_COPY) = (u32x4){x[1], x[2], x[3], x[4]};
          *(u32x4*)(gp + 3 * G_COPY) = (u32x4){y[1], y[2], y[3], y[4]};
          if (k == 0) {
            const unsigned f0 = __builtin_amdgcn_readfirstlane(x[0]), f1 = __builtin_amdgcn_readfirstlane(x[1]);
            const unsigned long long f64 = (unsigned long long)f0 | ((unsigned long long)f1 << 32);
            const unsigned long long hi = l ? (f64 << (64 - 16 * (l & 3))) : 0ull;
            if (l < 4) {
              *(u32x4*)(G + l * G_COPY) = (u32x4){0u, 0u, 0u, 0u};
              *(u32x4*)(G + l * G_COPY + 16) = (u32x4){0u, 0u, (unsigned)hi, (unsigned)(hi >> 32)};
            }
            if (l >= 32) *(u32x4*)(U + (l - 32) * 16) = (u32x4){0u, 0u, 0u, 0u};
          }
        }
        asm volatile("s_waitcnt lgkmcnt(0)" ::: "memory");
        STAMP(1);
      }
      const int a = n16, h = h4, jj = h >> 1, e2 = h & 1;
      const char* ap = G + ((a + 1) & 3) * G_COPY + 2 * (16 * jj + a - 8 * e2 + 9 - ((a + 1) & 3));
      const char* bp = U + 512 + 32 * (a - jj) + 16 * e2;
      auto lda = [&](u32x4* A, int r, int p0, int np) {
#pragma unroll
        for (int pp = 0; pp < np; ++pp) {
          u32x2 lo = *(const volatile LAS u32x2*)(ap + 512 * (p0 + pp) + 64 * r);
          u32x2 hi = *(const volatile LAS u32x2*)(ap + 512 * (p0 + pp) + 64 * r + 8);
          A[pp] = (u32x4){lo[0], lo[1], hi[0], hi[1]};
        }
      };
      u32x4 AD;
      {
        const unsigned dDr = (*(cptr_t)(p.ssm_D + quad * 4 + (ch & 2)) >> (16 * (ch & 1))) & 0xffffu;
        const unsigned dv = h == (a >> 3) ? ((a & 1) ? dDr : dDr << 16) : 0u;
        const int dw = (7 - (a & 7)) >> 1;
#pragma unroll
        for (int d = 0; d < 4; ++d) AD[d] = dw == d ? dv : 0u;
      }
      u32x4 A0[8][4];
      f32x4 acc[8];
#pragma unroll
      for (int n = 0; n < 8; ++n) acc[n] = (f32x4){0.f, 0.f, 0.f, 0.f};
#pragma unroll
      for (int r = 0; r < 8; ++r) lda(A0[r], r, 0, 4);
      auto yput = [&](int s) {
#pragma unroll
        for (int e = 0; e < 2; ++e)
          *(u32x2*)(U + 1024 * s + 512 * e + (16 * a + 4 * h) * 2) = (u32x2){pack2(acc[2 * s + e][0], acc[2 * s + e][1]), pack2(acc[2 * s + e][2], acc[2 * s + e][3])};
        asm volatile("" ::: "memory");
        if (l == 0) lf[16 + 4 * s + ch] = tag;
      };
      auto stage = [&](auto S__) {
        constexpr int s = decltype(S__)::value, NH = s < 2 ? 4 - 2 * s : 0, NP = 8 - 2 * s, NB = s < 2 ? 2 : 8;
        u32x4 A1[3][NH > 0 ? NH : 1], B[NB][2];
        auto ldb = [&](int r) {
#pragma unroll
          for (int e = 0; e < 2; ++e) B[r % NB][e] = *(const u32x4*)(bp + 512 * (2 * s + e) - 64 * r);
        };
        if (NH) { lda(A1[0], 0, 4, NH); lda(A1[1], 1, 4, NH); }
        SCHED();
        if (s > 0) yput(s - 1);
        lf_wait(4 * s, 4);
        if (s == 0) STAMP(3);
#pragma unroll
        for (int r = 0; r < NB; ++r) ldb(r);
        SCHED();
#pragma unroll
        for (int e = 0; e < 2; ++e) acc[2 * s + e] = MFMA16(AD, B[0][e], acc[2 * s + e]);
#pragma unroll
        for (int r = 0; r < 8; ++r) {
          if (NH && r + 2 < 8) lda(A1[(r + 2) % 3], r + 2, 4, NH);
          SCHED();
#pragma unroll
          for (int e = 0; e < 2; ++e)
#pragma unroll
            for (int pp = 0; pp < NP - e; ++pp) acc[2 * s + e + pp] = MFMA16(pp < 4 ? A0[r][pp] : A1[r % 3][pp < 4 ? 0 : pp - 4], B[r % NB][e], acc[2 * s + e + pp]);
          SCHED();
          if (r + NB < 8) ldb(r + NB);
        }
      };
      stage(IC<0>());
      STAMP(4);
      stage(IC<1>());
      STAMPF(9);
      const unsigned fo[1] = {fwi + (unsigned)((8 + c) * 20 + 5 * w + (l < 5 ? l : 0)) * 128u};
      unsigned fv = ~tag;
      if (MSPLIT) {
        u32x4 B[8][2];
        auto ldb = [&](int r, int m0) {
#pragma unroll
          for (int m = 0; m < 2; ++m) B[r][m] = *(const u32x4*)(bp + 512 * (m0 + m) - 64 * r);
        };
        yput(1);
        if (w == 3) wo_ld();
        __builtin_amdgcn_s_setprio(3);
        lf_wait(8, 4);
        STAMP(22);
#pragma unroll
        for (int r = 0; r < 8; ++r) ldb(r, 4);
        SCHED();
#pragma unroll
        for (int m = 0; m < 2; ++m) acc[4 + m] = MFMA16(AD, B[0][m], acc[4 + m]);
#pragma unroll
        for (int r = 0; r < 8; ++r) {
          acc[4] = MFMA16(A0[r][0], B[r][0], acc[4]);
          acc[5] = MFMA16(A0[r][1], B[r][0], acc[5]);
          acc[5] = MFMA16(A0[r][0], B[r][1], acc[5]);
          SCHED();
        }
#pragma unroll
        for (int r = 0; r < 8; ++r) {
          acc[6] = MFMA16(A0[r][2], B[r][0], acc[6]);
          acc[7] = MFMA16(A0[r][3], B[r][0], acc[7]);
          acc[6] = MFMA16(A0[r][1], B[r][1], acc[6]);
          acc[7] = MFMA16(A0[r][2], B[r][1], acc[7]);
          SCHED();
          if (r == 0) yput(2);
          if (r == FVR) fv = ld4(rs, fo[0]);
          if (r == 3) {
            lf_wait(12, 4);
#pragma unroll
            for (int x = 0; x < 4; ++x) ldb(x, 6);
          }
          if (r > 3) ldb(r, 6);
          SCHED();
        }
#pragma unroll
        for (int m = 0; m < 2; ++m) acc[6 + m] = MFMA16(AD, B[0][m], acc[6 + m]);
#pragma unroll
        for (int r = 0; r < 8; ++r) {
          acc[6] = MFMA16(A0[r][0], B[r][0], acc[6]);
          acc[7] = MFMA16(A0[r][1], B[r][0], acc[7]);
          acc[7] = MFMA16(A0[r][0], B[r][1], acc[7]);
          SCHED();
        }
        yput(3);
      } else {
        u32x4 B[4][4];
        auto ldb = [&](int r) {
#pragma unroll
          for (int m = 0; m < 4; ++m) B[r & 3][m] = *(const u32x4*)(bp + 512 * (4 + m) - 64 * r);
        };
        yput(1);
        if (w == 3) wo_ld();
        __builtin_amdgcn_s_setprio(3);
        lf_wait(8, 8);
        STAMP(22);
#pragma unroll
        for (int r = 0; r < 4; ++r) ldb(r);
        SCHED();
#pragma unroll
        for (int m = 0; m < 4; ++m) acc[4 + m] = MFMA16(AD, B[0][m], acc[4 + m]);
#pragma unroll
        for (int r = 0; r < 8; ++r) {
#pragma unroll
          for (int m = 0; m < 4; ++m)
#pragma unroll
            for (int pp = 0; pp < 4 - m; ++pp) acc[4 + m + pp] = MFMA16(A0[r][pp], B[r & 3][m], acc[4 + m + pp]);
          SCHED();
          if (r + 4 < 8) ldb(r + 4);
          if (r == 1) fv = ld4(rs, fo[0]);
          SCHED();
        }
#pragma unroll
        for (int e = 0; e < 4; ++e)
          *(u32x2*)(U + 2048 + 512 * e + (16 * a + 4 * h) * 2) = (u32x2){pack2(acc[4 + e][0], acc[4 + e][1]), pack2(acc[4 + e][2], acc[4 + e][3])};
        lf_set(28 + ch);
      }
      STAMP(28);
      __builtin_amdgcn_s_setprio(0);
      if (!__all(fv == tag)) wait_lines<1, 4>(rs, fo, tag);
      lf_wait(20, 4);
    }
    __builtin_amdgcn_s_setprio(0);
  } else {
    const int at = idx - 20;
    char* const scr = lds + WBLK + 14848;
    auto gv_blk = [&](int j) { return j < 160 ? 30 + (j >> 3) : j < 400 ? (j >> 3) - 20 : j >> 3; };
    auto gv_fin = [&](const u32x4* pre, int j) {
      float acc[8];
      gemv_fin(acc, pre, 10, scr);
      gemv_put(acc, j < 160 ? WS_MODP + (unsigned)((j & 7) * 1280 + (j >> 3) * 64) * 8u : WS_MODQ + (unsigned)((j & 7) * 2560 + ((j >> 3) - 20) * 64) * 8u);
    };
    const int ja = xcd * 12 + at, ai = xcd * 8 + at, bi = xcd * 4 + at - 8, g = w >> 2, wj = w & 3;
    const int rj = w == 0 ? ja : w == 1 ? (at >= 4 ? 92 + ai : -1) : w == 4 ? 160 + ja : w == 5 && at < 8 ? 256 + ai : -1;
    char* const rst = lds + (g ? WBLK : 65536) + wj * 7424;
    u32x4 pre[11];
    if (at >= 8 || g == 0) {
      TOUCH_AT(2);
      const int T = at >= 8 ? 64 + 2 * bi + g : ai;
      unsigned* mds = (unsigned*)(lds + 2048) + 16 * g;
      char* qt = lds + L_RT + g * (16 * 1312);
      u32x4 ld[5];
      const bool e1 = at >= 8 && w == 3;
      if (e1) gemv_load(pre, gv_blk(128 + bi), bi & 7, 10);
      else __builtin_amdgcn_s_sleep(8);
#pragma unroll
      for (int j = 0; j < 5; ++j)
        ld[j] = __builtin_amdgcn_raw_buffer_load_b128(rq, (unsigned)(h4 * D_ * 2 + n16 * 16 + j * 256), (unsigned)((32 * (T >> 1) + 8 * wj + 4 * (T & 1)) * D_ * 2), NT);
      TAG_READY();
      {
        const bf2_t ONE2 = {(__bf16)1.0f, (__bf16)1.0f};
        char* dst = qt + (4 * wj + h4) * 1312 + n16 * 16;
        float sa = 0.f, va = 0.f;
#pragma unroll
        for (int j = 0; j < 5; ++j) {
          *(u32x4*)(dst + j * 256) = ld[j];
#pragma unroll
          for (int d = 0; d < 4; ++d) {
            const unsigned a0 = ld[j][d];
            const bf2_t xa = __builtin_bit_cast(bf2_t, a0);
            sa = __builtin_amdgcn_fdot2_f32_bf16(xa, ONE2, sa, false); va = __builtin_amdgcn_fdot2_f32_bf16(xa, xa, va, false);
          }
        }
        lf_set(8 + w);
        const float ma = sum16(sa) * (1.0f / D_);
        va = __builtin_fmaxf(sum16(va) * (1.0f / D_) - ma * ma, 0.f);
        if (n16 == 0) mds[4 * wj + h4] = pack2(ma, __builtin_amdgcn_sqrtf(rbf(rbf(va) + 1e-5f)));
      }
      if (e1) gv_fin(pre, 128 + bi);
      lf_wait(8 + 4 * g, 4);
      STAMP(23);
      {
        const char* src = qt + (l & 7) * 1312 + (40 * wj + (l >> 3)) * 8;
#pragma unroll
        for (int j = 0; j < 5; ++j) {
          const u32x2 x = *(const u32x2*)(src + j * 64), y = *(const u32x2*)(src + j * 64 + 8 * 1312);
          st16(rs, WS_QB + (unsigned)T * 20480u + (unsigned)(5 * wj + j) * 1024u + l16, (u32x4){x[0], x[1], y[0], y[1]});
        }
      }
      SCHED();
      const int g2 = at < 8 || w < 3 || w == 4 ? 400 : w == 3 ? 384 + bi : 256 + wj * 32 + bi;
      if (g2 < 400) {
        gemv_load(pre, gv_blk(g2), g2 & 7, 10);
        asm volatile("s_waitcnt vmcnt(10)" ::: "memory");
      } else if (w < 2 && rj >= 0) {
        rt_ld(rj, rst);
        asm volatile("s_waitcnt vmcnt(7)" ::: "memory");
      } else {
        DRAIN();
      }
      lf_set(16 + w);
      if (wj == 2) {
        lf_wait(16 + 4 * g, 4);
        st16(rs, WS_ST1 + (unsigned)(l >> 3) * 16384u + (unsigned)T * 128u + (unsigned)(l & 7) * 16u, (u32x4){mds[l & 7], tag, mds[8 + (l & 7)], tag});
        STAMP(22);
      }
      if (w >= 4 && w < 7) lf_set(w);
      if (g2 < 400) gv_fin(pre, g2);
      STAMP(25);
    } else {
      constexpr int KB = L_RT + 16 * 1312;
      TOUCH_AT(3);
      const int g1 = w < 6 ? 2 * ai + wj : 32 + wj * 64 + ai;
      if (w >= 6) {
        const rsrc_t rk = __builtin_amdgcn_make_buffer_rsrc((void*)(w == 6 ? p.k : p.v), 0, S_ * D_ * 2, 0x00020000);
        const unsigned ko = (unsigned)((1536 + 8 * ai) * (D_ * 2) - (w == 6 ? 4096 : 0));
        char* dst = lds + KB + (w == 6 ? 0 : 14336);
        __builtin_amdgcn_s_sleep(3);
#pragma unroll
        for (int n = 0; n < 10; ++n) __builtin_amdgcn_raw_ptr_buffer_load_lds(rk, (LAS void*)(dst + n * 1024), 16, l16, ko + (unsigned)n * 1024u, 0, NT);
        if (w == 6) {
#pragma unroll
          for (int n = 10; n < 14; ++n) __builtin_amdgcn_raw_ptr_buffer_load_lds(rk, (LAS void*)(dst + n * 1024), 16, l16, ko + (unsigned)n * 1024u, 0, NT);
        }
      }
      SCHED();
      u32x4 swt[2];
      swt[0] = *(const u32x4*)(p.shift_k + (tid - 256) * 8);
      swt[1] = *(const u32x4*)(p.shift_k + (256 + l) * 8);
      SCHED();
      if (w >= 6) __builtin_amdgcn_s_sleep(8);
      gemv_load(pre, gv_blk(g1), g1 & 7, 10);
      TAG_READY();
      asm volatile("s_waitcnt vmcnt(11)" : "+v"(swt[0]), "+v"(swt[1]) :: "memory");
      lf_set(8 + w);
      if (w < 6) gv_fin(pre, g1);
      STAMP(25);
      lf_wait(12, 4);
      STAMP(23);
#pragma unroll 1
      for (int n = 0; n < (w == 7 ? 2 : 1); ++n) {
        {
          const int pr = n ? 256 + l : tid - 256;
          const char* kp = lds + KB + 4096 + pr * 4;
          unsigned kk[11], vv[8];
#pragma unroll
          for (int i = 0; i < 11; ++i) kk[i] = *(const unsigned*)(kp + (i - 3) * 1280);
#pragma unroll
          for (int i = 0; i < 8; ++i) vv[i] = *(const unsigned*)(kp + 10240 + i * 1280);
          const u32x4 sw = n ? swt[1] : swt[0];
          const f32x2 wt[4] = {{bflo(sw[0]), bflo(sw[2])}, {bfhi(sw[0]), bfhi(sw[2])}, {bflo(sw[1]), bflo(sw[3])}, {bfhi(sw[1]), bfhi(sw[3])}};
          f32x2 o[8];
#pragma unroll
          for (int i = 0; i < 8; ++i) {
            f32x2 t = (f32x2){bflo(kk[i + 3]), bfhi(kk[i + 3])} * wt[0];
#pragma unroll
            for (int j = 1; j < 4; ++j) t = __builtin_elementwise_fma((f32x2){bflo(kk[i + 3 - j]), bfhi(kk[i + 3 - j])}, wt[j], t);
            o[i] = t * (f32x2){bflo(vv[i]), bfhi(vv[i])};
          }
#pragma unroll
          for (int e = 0; e < 4; ++e) {
            const int i = 7 - 4 * (e & 1);
            st16(rs, WS_KV3 + (unsigned)ai * 20480u + (unsigned)(e * 5120 + pr * 16),
                 (u32x4){pack2(o[i][e >> 1], o[i - 1][e >> 1]), tag, pack2(o[i - 2][e >> 1], o[i - 3][e >> 1]), tag});
          }
        }
      }
      STAMP(22);
      if (w < 7) lf_set(w);
      if (w >= 6) gv_fin(pre, g1);
      STAMPF(30);
    }
    if (w == 6 && ja < 80) {
      hold(AUX_T);
      gemv_load(pre, gv_blk(400 + ja), ja & 7, 10);
      gv_fin(pre, 400 + ja);
    }
    if (rj >= 0) {
      if (w >= 2) { hold(RT2_T); rt_ld(rj, rst); }
      rt_job(rj, rst);
    }
    STAMP(26);
    lf_set(w);
    if (w == 3) { hold(WA_T); wo_ld(); }
    STAMP(3);
    if (w < 4) {
      {
        const unsigned fo[1] = {l < 5 ? fwi + (unsigned)((8 + c) * 20 + 5 * w + l) * 128u : ~0u};
        hold(W_T);
        wait_lines<1, 8>(rs, fo, tag);
      }
      lf_wait(0, 8);
      STAMP(4);
    }
  }

  STAMPF(16);
  u32x4 wox[5];
  if (w == 3) {
    DRAIN();
    const char* bp = wo_st + (4 * h4 + (n16 >> 2)) * 160 + (n16 & 3) * 8;
#pragma unroll
    for (int dj = 0; dj < 5; ++dj) wox[dj] = trfrag(bp + dj * 32, 160);
    asm volatile("s_waitcnt lgkmcnt(0)" : "+v"(wox[0]), "+v"(wox[1]), "+v"(wox[2]), "+v"(wox[3]), "+v"(wox[4]) :: "memory");
  }
  auto wo_put = [&]() {
    if (w == 3 && R < 28) {
      const unsigned base = WS_WOUTF + (unsigned)c * WBLK + (unsigned)((R >> 1) * 10240 + (R & 1) * 1024);
#pragma unroll
      for (int dj = 0; dj < 5; ++dj) st16(rs, base + (unsigned)((R & 1) ? dj : (dj + 2) % 5) * 2048u + l16, wox[dj]);
    }
  };
  if (cs2) {
    cs_ld(csv, (8 + c) * 7 + R - 20);
    SCHED();
    load_b2(0, 12);
    SCHED();
    cs_fin(csv, (8 + c) * 7 + R - 20);
  } else if (w < 4) {
    asm volatile("" ::: "memory");
    load_b2(0, 12);
  }
  if (w < 4) load_b2(12, 35);
  wo_put();
  STAMPF(29);
  u32x4 k_c;
  unsigned kc_o = w < 2 ? (unsigned)(tid < 112 ? tid : 0) * 16u : 0x80000000u, kc_d = (unsigned)(L_CONST + (tid & 127) * 8);
  auto kc_ld = [&](int z2) { k_c = __builtin_amdgcn_raw_buffer_load_b128(rs, kc_o, WS_CS + (unsigned)(8 * z2 + c) * 2048u, SC1); };
  auto consts_put = [&](int z2) {
    if (w < 2) {
      if (__any(ne2(k_c[0], k_c[2]) != 0u)) {
        _Pragma("unroll 1") for (unsigned spins = 0; spins < SPIN_CAP; ++spins) {
          __builtin_amdgcn_s_sleep(8);
          kc_ld(z2);
          if (!__any(ne2(k_c[0], k_c[2]) != 0u)) break;
        }
      }
      if (z2) *(u32x2*)(lds + kc_d + 1024) = (u32x2){k_c[1], k_c[3]};
      else { const unsigned d = kc_d - ((kc_d >> 1) & 4u); *(unsigned*)(lds + d) = k_c[1]; *(unsigned*)(lds + d + 8) = k_c[3]; }
    }
  };
  const int wq = w ^ rotr, rt1 = wq & 3, kh1 = w >> 2;
  unsigned bo0 = (unsigned)(kh1 * 71680) + l16, bo1 = bo0 + 35840u;
  asm volatile("" : "+v"(bo0), "+v"(bo1));
  unsigned pso[3], pqo[3], oto = (unsigned)(((w ^ 4) * 7 * 64 + l) * 8), rdo = (unsigned)((w * 7 * 64 + l) * 8), cso = (unsigned)(L_CONST + (128 + n16) * 8);
#pragma unroll
  for (int e = 0; e < 3; ++e) {
    const int row = rt1 * 16 + (e < 2 ? 4 * h4 + 2 * kh1 + e : n16);
    pso[e] = (unsigned)(L_PST + row * 4); pqo[e] = (unsigned)(L_PST + 512 + row * 16 + (row & 3) * 4);
  }
  unsigned spo = (unsigned)(L_Z + tid * 16), p6o = (unsigned)(L_PST + (wq * 16 + 4 * h4) * 4), pgo = (unsigned)(L_PST + 512 + (wq * 16 + n16) * 16);
  asm volatile("" : "+v"(spo), "+v"(p6o), "+v"(pgo));
  p6o = n16 == 12 ? p6o : spo;
  pgo = h4 == (n16 >> 2) ? pgo : spo;
  unsigned bsrc = (unsigned)(((l >> 1) & 3) * 64 + (l & 1) * 4), ppo = l < 8 ? l16 : 0x80000000u;
  asm volatile("" : "+v"(pso[0]), "+v"(pso[2]), "+v"(pqo[0]), "+v"(pqo[2]), "+v"(oto), "+v"(rdo), "+v"(cso), "+v"(p6o), "+v"(pgo), "+v"(bsrc), "+v"(ppo), "+v"(kc_o), "+v"(kc_d));
  pso[1] = pso[0] + 4u; pqo[1] = pqo[0] + 20u;
#define BFRAG(q, j) (*(const u32x4*)(lds + ((q) < 5 ? bo0 : bo1) + ((q) % 5) * 7168 + (j) * 1024))
  u32x4 h2[10];
  unsigned xv = (unsigned)((4 * ((l >> 3) & 1) + h4) * 128 + (l & 7) * 16), xs0 = WS_X1B + (unsigned)(R * 4 + rt1) * 20480u + (unsigned)(10 * kh1) * 1024u;
  unsigned wvo = wsp_vo, bso = (unsigned)tau(rt1, n16) * 2u, fgo = l < 32 ? (unsigned)(l >> 3) * F4S + (unsigned)(l & 7) * 16u : 0x80000000u;
  asm volatile("" : "+v"(xv), "+v"(wvo), "+v"(bso), "+v"(fgo));
#define WO3 (-92)
  unsigned zs[8];
#pragma unroll
  for (int t = 0; t < 8; ++t) zs[t] = WS_Z2F + (unsigned)(c * 64 + 8 * w + 4 * ((t >> 2) ^ rotg) + ((t + rotb) & 3)) * 7168u;
  unsigned wx8 = (unsigned)((w + (idx >> 2)) & 7) * 1024u, wx4 = wx8 & 3072u, wgb = WS_WOUTF + (unsigned)c * WBLK, fzs = fz + (unsigned)(c * 32 + 4 * w) * 128u;
  unsigned uo = WS_UF + (unsigned)((R * 4 + rt1) * 8 + c) * 4096u + (unsigned)kh1 * 2048u, ui = WS_UF + (unsigned)((R * 4 + rt1) * 8 + 4 * kh1) * 4096u;
  unsigned xs = WS_X1B + (unsigned)(R * 4 + rt1) * 20480u + (unsigned)(5 * c + 2 * kh1) * 512u, wl2 = (unsigned)(120 + WO3 + (w < 4 ? w + 16 : w)) * 1024u;
  unsigned os = (unsigned)__builtin_amdgcn_readfirstlane(((R * 64 + tau(rt1, 0)) * D_ + 80 * c + 32 * kh1) * 2);
#define PINS(x) do { x = (unsigned)__builtin_amdgcn_readfirstlane((int)x); asm volatile("" : "+s"(x)); } while (0)
#pragma unroll
  for (int t = 0; t < 8; ++t) PINS(zs[t]);
  PINS(xs0); PINS(wx8); PINS(wx4); PINS(wgb); PINS(fzs); PINS(uo); PINS(ui); PINS(xs); PINS(wl2); PINS(os);
  unsigned s1 = (unsigned)((w ^ 4) * 3072 + 1024), s3 = (unsigned)(w * 3072 + 1024);
  PINS(s1); PINS(s3);
#define LDX(q) h2[q] = __builtin_amdgcn_raw_buffer_load_b128(rs, xv + (unsigned)((q) & 3) * 1024u, xs0 + (unsigned)((q) >> 2) * 4096u, BULK_AUX)
  auto flag4 = [&](unsigned off) { __builtin_amdgcn_raw_buffer_store_b128((u32x4){tag, tag, tag, tag}, rs, fgo, WS_F4 - WS_FWI + off, SC1); };
  unsigned bs_raw;
  f32x4 acc[7];
  float mu_n, rs_n;
  {
    unsigned f1o[1] = {w == 1 && l < 20 ? fwi + (unsigned)(c * 20 + l) * 128u : ~0u}, f1v;
    asm volatile("" : "+v"(f1o[0]));
    if (w == 7) {
      const unsigned fb = WS_FX + (unsigned)(((R >> 1) * 4 + (R & 1) * 2 + (c >> 2)) * 160 + l) * 128u;
      const unsigned fo[3] = {fb, fb + 8192u, l < 32 ? fb + 16384u : ~0u};
      if (!is_conv) hold(Y_T);
      wait_lines<3, 2>(rs, fo, tag);
      if (l == 0) lf[33] = tag;
    }
    lf_wait(33, 1);
    STAMP(7);
    SCHED();
#pragma unroll
    for (int q = 0; q < 3; ++q) LDX(q);
    SCHED();
    __builtin_amdgcn_s_waitcnt(0x0F73);
    BAR();
    STAMP(8);
#pragma unroll
    for (int j = 0; j < 7; ++j) acc[j] = (f32x4){0.f, 0.f, 0.f, 0.f};
    f32x4 accg = {0.f, 0.f, 0.f, 0.f};
    u32x4 bf[7];
#pragma unroll
    for (int j = 0; j < 7; ++j) bf[j] = BFRAG(0, j);
    SCHED();
#pragma unroll
    for (int q = 3; q < 6; ++q) LDX(q);
    if (w == 3) flag4(WS_FWO + (unsigned)(c * 32 + R) * 128u);
    SCHED();
    auto xswap = [&](int q, int e) {
#pragma unroll
      for (int d = 0; d < 2; ++d) {
        if (e == 1) h2[q][2 + d] ^= (unsigned)__builtin_amdgcn_update_dpp(0, (int)h2[q][d], 0x128, 0xf, 0x3, false);
        else h2[q][d] ^= (unsigned)__builtin_amdgcn_update_dpp(0, (int)h2[q][2 + d], 0x128, 0xf, 0xC, false);
      }
      asm volatile("" : "+v"(h2[q]));
    };
#define XSW 1
#pragma unroll
    for (int q = 0; q < 10; ++q) {
      if (q < XSW) { xswap(q, 0); xswap(q, 1); xswap(q, 2); }
      if (q == 5 && w == 1 && __builtin_amdgcn_ballot_w64(f1v == tag) != 0xfffffull) wait_lines<1, 4>(rs, f1o, tag);
      if (q == 5) { __builtin_amdgcn_s_barrier(); asm volatile("" ::: "memory"); }
      SCHED();
      accg = MFMA16(h2[q], h2[q], accg);
      SCHED();
#pragma unroll
      for (int j = 0; j < 7; ++j) {
        acc[j] = MFMA16(h2[q], bf[j], acc[j]);
        if (q == 9 && j == 1) *(f32x4*)(lds + pgo) = accg;
        if (q < 9) bf[j] = BFRAG(q + 1, j);
        if (q < 9 && q + 1 >= XSW && j >= 3 && j < 6) xswap(q + 1, j - 3);
        if (q == 1 && j == 6) f1v = ld4(rs, f1o[0]);
        if (q == 2 && j == 0) bs_raw = __builtin_amdgcn_raw_buffer_load_b16(__builtin_amdgcn_make_buffer_rsrc((void*)p.b_sp, 0, S_ * 2, 0x00020000), bso, (unsigned)R * 128u, 0);
#define KCQ 4
#define WA 8
#define WB 12
#define WQ0 2
        if (WQ0 == 2 && q >= 2 && j == 2) LDW(q - 2);
        if (WQ0 == 6 && q >= 6 && (j == 2 || j == 4)) LDW(2 * (q - 6) + (j >> 2));
        if (WQ0 == 3 && q >= 2 && q < 5 && (j & 1) && 3 * (q - 2) + (j >> 1) < WA) LDW(3 * (q - 2) + (j >> 1));
        if (WQ0 == 4 && q >= 2 && !(q & 1) && j == 2) LDW((q - 2) >> 1);
        if ((j == 0 || j == 4) && 6 + 2 * q + (j >> 2) < 10) LDX(6 + 2 * q + (j >> 2));
        if (q == KCQ && j == 0) kc_ld(1);
        if (q == 2 && j == 1 && cs1) cs_ld(csv, c * 7 + R - 25);
        if (q >= 5 && j == 5) {
          const int i = (5 + q) * 7 + (w < 7 ? w : 0);
          dma1k(rs, lds + i * 1024, l16, WS_WINF + (unsigned)c * WBLK + (unsigned)i * 1024u);
          if (q == 9) { const int i2 = 28 + (w < 7 ? w : 0); dma1k(rs, lds + i2 * 1024, l16, WS_WINF + (unsigned)c * WBLK + (unsigned)i2 * 1024u); }
        }
        SCHED();
      }
    }
    STAMP(31);
  }
  auto gelu_n = [&](f32x2* x, auto N_) {
    constexpr int N = decltype(N_)::value;
    const float c0 = -2.0f * 0.7978845608028654f * 1.4426950408889634f, c1 = c0 * 0.044715f;
    f32x2 k0 = {c0, c0}, k1 = {c1, c1}, k2 = {1.0f, 1.0f};
    asm volatile("" : "+v"(k0), "+v"(k1), "+v"(k2));
    f32x2 t[N];
#define GST(e) do { _Pragma("unroll") for (int i = 0; i < N; ++i) { t[i] = (e); asm volatile("" : "+v"(t[i])); } } while (0)
    GST(x[i] * x[i]);
    GST(__builtin_elementwise_fma(t[i], k1, k0));
    GST(t[i] * x[i]);
    GST(((f32x2){__builtin_amdgcn_exp2f(t[i][0]), __builtin_amdgcn_exp2f(t[i][1])}));
    GST(t[i] + k2);
    GST(((f32x2){__builtin_amdgcn_rcpf(t[i][0]), __builtin_amdgcn_rcpf(t[i][1])}));
#pragma unroll
    for (int i = 0; i < N; ++i) { x[i] *= t[i]; asm volatile("" : "+v"(x[i])); }
  };
  {
    f32x2 mine[7];
    f32x2 csb[7];
    if (kh1) {
#pragma unroll
      for (int j = 0; j < 7; ++j) { *(f32x2*)(lds + rdo + j * 512) = (f32x2){acc[j][0], acc[j][1]}; mine[j] = (f32x2){acc[j][2], acc[j][3]}; }
      *(f32x4*)(lds + p6o) = acc[6];
    } else {
#pragma unroll
      for (int j = 0; j < 7; ++j) { *(f32x2*)(lds + rdo + j * 512) = (f32x2){acc[j][2], acc[j][3]}; mine[j] = (f32x2){acc[j][0], acc[j][1]}; }
      *(f32x4*)(lds + p6o) = acc[6];
      consts_put(1);
      if (cs1) cs_fin(csv, c * 7 + R - 25);
#pragma unroll
      for (int k = WA; k < WB; ++k) LDW(k);
    }
    BAR();
    if (kh1) {
#pragma unroll
      for (int k = WA; k < WB; ++k) LDW(k);
    }
    __builtin_amdgcn_s_setprio(3);
    STAMPT(9);
    f32x2 oth[7];
    f32x2 pa[3], pb[3];
#pragma unroll
    for (int e = 0; e < 3; ++e) {
      pa[e] = (f32x2){*(const float*)(lds + pso[e]), *(const float*)(lds + pqo[e])};
      pb[e] = (f32x2){*(const float*)(lds + pso[e] + 256), *(const float*)(lds + pqo[e] + 1024)};
    }
#pragma unroll
    for (int j = 0; j < 7; ++j) { oth[j] = *(const f32x2*)(lds + oto + j * 512); csb[j] = *(const f32x2*)(lds + cso + 128 * j); }
    SCHED();
    float mu2[2], rs2[2];
#pragma unroll
    for (int e = 0; e < 3; ++e) {
      const float mean = (pa[e][0] + pb[e][0]) * (1.0f / D_);
      const float var = __builtin_fmaxf((pa[e][1] + pb[e][1]) * (1.0f / D_) - mean * mean, 0.f);
      const float rstd = __builtin_amdgcn_rsqf(var + 1e-5f);
      if (e < 2) { mu2[e] = mean; rs2[e] = rstd; } else { mu_n = mean; rs_n = rstd; }
    }
    f32x2 zz[7];
    const f32x2 nmu = {-mu2[0], -mu2[1]}, rsv = {rs2[0], rs2[1]};
#pragma unroll
    for (int j = 0; j < 7; ++j)
      zz[j] = __builtin_elementwise_fma(__builtin_elementwise_fma(nmu, (f32x2){csb[j][0], csb[j][0]}, mine[j] + oth[j]), rsv, (f32x2){csb[j][1], csb[j][1]});
    gelu_n(zz, IC<7>());
    if (n16 >= 12) zz[6] = (f32x2){0.f, 0.f};
    f32x2 s1 = ((zz[0] + zz[1]) + (zz[2] + zz[3])) + ((zz[4] + zz[5]) + zz[6]);
    f32x2 s2 = zz[0] * zz[0], s3 = zz[1] * zz[1];
#pragma unroll
    for (int j = 2; j < 7; ++j) { if (j & 1) s3 = __builtin_elementwise_fma(zz[j], zz[j], s3); else s2 = __builtin_elementwise_fma(zz[j], zz[j], s2); }
    s2 += s3;
    asm("" : "+v"(s1), "+v"(s2));
    float a0 = s1[0] + dpp_f<0xB1>(s1[0]), a1 = s1[1] + dpp_f<0xB1>(s1[1]), b0 = s2[0] + dpp_f<0xB1>(s2[0]);
    a0 = (l & 1) ? a1 : a0;
    const float b1 = s2[1] + dpp_f<0xB1>(s2[1]);
    b0 = (l & 1) ? b1 : b0;
    a0 += dpp_f<0x4E>(a0); b0 += dpp_f<0x4E>(b0);
    a0 += dpp_f<0x124>(a0); b0 += dpp_f<0x124>(b0);
    a0 += dpp_f<0x128>(a0); b0 += dpp_f<0x128>(b0);
    {
      const unsigned pa0 = __builtin_amdgcn_ds_bpermute(bsrc, __float_as_uint(a0)), pb0 = __builtin_amdgcn_ds_bpermute(bsrc, __float_as_uint(b0));
      __builtin_amdgcn_raw_buffer_store_b128((u32x4){tag, pa0, tag, pb0}, rs, ppo, WS_PART + (unsigned)((R * 8 + c) * 8 + wq) * 128u, SC1);
    }
    SCHED();
    __builtin_amdgcn_s_setprio(0);
    STAMP(10);
#pragma unroll
    for (int k = 0; k < 10; ++k) {
      if (k < 9) {
        int x = k * 8 + w;
        x = x < 70 ? x : 69;
        const int i = x + (x < 35 ? 35 : 70);
        dma1k(rs, lds + i * 1024, l16, WS_WINF + (unsigned)c * WBLK + (unsigned)i * 1024u);
      }
    }
#pragma unroll
    for (int k = WB; k < 16; ++k) LDW(k);
#pragma unroll
    for (int j = 0; j < 7; ++j)
      *(unsigned*)(lds + L_Z + (((rt1 >> 1) * 7 + j) * 64 + l) * 16 + (4 * (rt1 & 1) + 2 * kh1) * 2) = pack2(zz[j][0], zz[j][1]);
    SCHED();
#define TR0 2
#define TR1 TR0
#define TR2 TR1
#define TR3 5
#pragma unroll
    for (int g = 0; g < TR0; ++g) WSP_TR(g);
    if (w == 1) {
      const unsigned fo[1] = {l < 28 ? fwi + (WS_FWO - WS_FWI) + (unsigned)(c * 32 + l) * 128u : ~0u};
      wait_lines<1, 1>(rs, fo, tag);
    }
    asm volatile("s_waitcnt vmcnt(%0)" :: "n"(16 - WB) : "memory");
    BAR();
    STAMPT(11);
  }
  unsigned z1d[7];
  constexpr int L_RED = 28672;
#define WOA(i) (((i) < 28 ? (i) : (i) < 108 ? (i) + 32 : (i) < 120 ? (i) + 37 : (i) - 92) * 1024)
#define RBAR() do { __builtin_amdgcn_s_barrier(); asm volatile("" ::: "memory"); } while (0)
#define ZAUX 16
#define NW 14
#define WOD(i0, x) dma1k(rs, lds + WOA(i0) + (x), l16, wgb + (unsigned)(i0) * 1024u + (x))
  auto wo_dma = [&](int k) {};
  u32x4 zw[NW];
#define LDZ(f) do { if (((f) >> 3) == 4) { zs[(f) & 7] += 4096u; asm volatile("" : "+s"(zs[(f) & 7])); } \
    zw[(f) % NW] = __builtin_amdgcn_raw_buffer_load_b128(rs, l16 + (unsigned)(((f) >> 3) & 3) * 1024u, zs[(f) & 7], ZAUX); } while (0)
#define WD 24
#define WE 32
#define QS 5
#define PQ 3
#define NW0 NW
#define PP 1
#define WZ 4
#define WY 11
  static_assert(QS >= 4 && QS <= 6 && PQ >= 2 && PQ < QS && (WZ < 4 || QS >= 5), "");
  {
    f32x4 acc[7];
#pragma unroll
    for (int j = 0; j < 7; ++j) acc[j] = (f32x4){0.f, 0.f, 0.f, 0.f};
    u32x4 bf[7];
#pragma unroll
    for (int j = 0; j < 7; ++j) bf[j] = BFRAG(4, j);
    const unsigned po = WS_PART + (unsigned)((R * 8 + (l >> 3)) * 8 + wq) * 128u + (unsigned)(l & 7) * 16u;
    u32x4 pv;
    auto z1 = [&](auto Q0_, auto Q1_) __attribute__((always_inline)) {
#pragma unroll
      for (int qi = decltype(Q0_)::value; qi < decltype(Q1_)::value; ++qi) {
        const int q = (qi + 4) % 10, qn = (qi + 5) % 10;
        SCHED();
#pragma unroll
        for (int j = 0; j < 7; ++j) {
          acc[j] = MFMA16(bf[j], h2[q], acc[j]);
          if (qi < 9 && qi != QS - 1) bf[j] = BFRAG(qn, j);
          if (qi < 2 && (j == 1 || j == 4)) { const int i = (2 * qi + (j >> 2)) * 7 + (w < 7 ? w : 0); dma1k(rs, lds + i * 1024, l16, WS_WINF + (unsigned)c * WBLK + (unsigned)i * 1024u); }
          if (qi == 2 && j == 0) kc_ld(0);
          if (qi == PQ && j == 1) pv = ld16(rs, po);
          if (!PP && qi >= QS && (j & 1) && WD + 3 * (qi - QS) + (j >> 1) < WE) LDW((WD + 3 * (qi - QS) + (j >> 1)) & 31);
          SCHED();
        }
      }
    };
    auto req1 = [&]() {
#pragma unroll
      for (int k = 16; k < WD; ++k) { LDW(k); SCHED(); }
    };
    auto req2 = [&]() {
#pragma unroll
      for (int k = WD; k < (PP ? WE : WD); ++k) { LDW(k); SCHED(); }
      sfor<0, WZ>([&](auto G_) __attribute__((always_inline)) { WOD(68 + 8 * decltype(G_)::value, wx8); SCHED(); });
    };
    if (kh1) req1();
    z1(IC<0>(), IC<QS>());
    STAMPT(29);
    f32x2 zv[2][4];
    unsigned sro = (unsigned)(L_ST2 + h4 * 16), swo = l < 8 ? (unsigned)(L_ST2 + (16 * rt1 + 4 * (l >> 1) + 2 * kh1 + (l & 1)) * 4) : (unsigned)(L_PST + l * 4);
    unsigned zo = w < 7 ? (unsigned)(h4 * 7168 + n16 * 16) : 0x80000000u;
#pragma unroll
    for (int m = 0; m < 2; ++m) {
      const u32x4 zf = *(const u32x4*)(lds + L_Z + ((m * 7 + (w < 7 ? w : 0)) * 64 + l) * 16);
#pragma unroll
      for (int e = 0; e < 4; ++e) { zv[m][e] = (f32x2){bflo(zf[e]), bfhi(zf[e])}; asm volatile("" : "+v"(zv[m][e])); }
    }
    asm volatile("" : "+v"(sro), "+v"(swo), "+v"(zo));
#pragma unroll
    for (int g = TR0; g < TR1; ++g) WSP_TR(g);
    __builtin_amdgcn_s_setprio(3);
    if (__any(((pv[0] ^ tag) | (pv[2] ^ tag)) != 0u)) {
      _Pragma("unroll 1") for (unsigned spins = 0; spins < SPIN_CAP; ++spins) {
        pv = ld16(rs, po);
        if (__all(((pv[0] ^ tag) | (pv[2] ^ tag)) == 0u)) break;
        __builtin_amdgcn_s_sleep(1);
      }
    }
    {
      const float mean = red8(__uint_as_float(pv[1])) * (1.0f / FH);
      const float var = __builtin_fmaxf(red8(__uint_as_float(pv[3])) * (1.0f / FH) - mean * mean, 0.f);
      const float rstd = __builtin_amdgcn_rsqf(var + 1e-5f);
      *(float*)(lds + swo) = rstd;
      *(float*)(lds + swo + 256) = -mean * rstd;
    }
    SCHED();
    consts_put(0);
    if (!kh1) req1();
    BAR();
    STAMP(12);
    {
      f32x4 sa[2][2], sb[2][2];
#pragma unroll
      for (int i = 0; i < 4; ++i) { sa[i >> 1][i & 1] = *(const f32x4*)(lds + sro + i * 64); sb[i >> 1][i & 1] = *(const f32x4*)(lds + sro + i * 64 + 256); }
      SCHED();
#pragma unroll
      for (int m = 0; m < 2; ++m) {
        u32x4 o;
#pragma unroll
        for (int e = 0; e < 4; ++e) {
          const f32x4 a = sa[m][e >> 1], b = sb[m][e >> 1];
          const f32x2 x = __builtin_elementwise_fma(zv[m][e], (f32x2){a[2 * (e & 1)], a[2 * (e & 1) + 1]}, (f32x2){b[2 * (e & 1)], b[2 * (e & 1) + 1]});
          o[e] = pack2(x[0], x[1]);
        }
        __builtin_amdgcn_raw_buffer_store_b128(o, rs, zo + (unsigned)m * 256u, WS_Z2F + (unsigned)((c * 64 + 4 * (R >> 1)) * 7 + (w < 6 ? w + 1 : 0)) * 1024u + (unsigned)(R & 1) * 512u, SC1);
      }
    }
    SCHED();
    __builtin_amdgcn_s_setprio(0);
    STAMPT(13);
#pragma unroll
    for (int j = 0; j < 7; ++j) bf[j] = BFRAG((QS + 4) % 10, j);
    if (kh1) req2();
    z1(IC<QS>(), IC<10>());
    f32x2 mine[7];
    if (kh1) {
#pragma unroll
      for (int j = 0; j < 7; ++j) { *(f32x2*)(lds + rdo + L_RED + j * 512) = (f32x2){acc[j][0], acc[j][1]}; mine[j] = (f32x2){acc[j][2], acc[j][3]}; }
    } else {
#pragma unroll
      for (int j = 0; j < 7; ++j) { *(f32x2*)(lds + rdo + L_RED + j * 512) = (f32x2){acc[j][2], acc[j][3]}; mine[j] = (f32x2){acc[j][0], acc[j][1]}; }
    }
    if (!kh1) req2();
#pragma unroll
    for (int g = TR1; g < TR2; ++g) WSP_TR(g);
    asm volatile("s_waitcnt vmcnt(%0)" :: "n"(WE - WD + WZ) : "memory");
    BAR();
    if (w == 3) flag4(WS_FZ + (unsigned)(c * 32 + R) * 128u);
    STAMP(6);
    auto req = [&](auto G0_, auto G1_) __attribute__((always_inline)) {
#pragma unroll
      for (int k = WE; k < 32; ++k) LDW(k);
      SCHED();
      sfor<decltype(G0_)::value, decltype(G1_)::value>([&](auto G_) __attribute__((always_inline)) {
        constexpr int i0 = 4 * decltype(G_)::value;
        WOD(i0 < 68 ? i0 : i0 + 8 * WZ, wx4); SCHED();
      });
    };
    if (kh1) req(IC<0>(), IC<WY>());
    u32x4 gv[8];
    auto gb_ld = [&]() {
#pragma unroll
      for (int k = 0; k < 8; ++k) gv[k] = __builtin_amdgcn_raw_buffer_load_b128(rs, l < 40 ? l16 : 0u, WS_MODQ + (unsigned)(k * 2560 + 1920 + 80 * c) * 8u, SC1);
    };
    if (w == 2) gb_ld();
    f32x2 z[7];
    {
      f32x2 oth[7];
      f32x4 cc[7];
#pragma unroll
      for (int j = 0; j < 7; ++j) {
        oth[j] = *(const f32x2*)(lds + oto + L_RED + j * 512);
        cc[j] = *(const f32x4*)(lds + L_CONST + (16 * j + 4 * h4 + 2 * kh1) * 8);
      }
      SCHED();
#pragma unroll
      for (int j = 0; j < 7; ++j)
        z[j] = __builtin_elementwise_fma(__builtin_elementwise_fma((f32x2){-mu_n, -mu_n}, (f32x2){cc[j][0], cc[j][1]}, oth[j] + mine[j]),
                                         (f32x2){rs_n, rs_n}, (f32x2){cc[j][2], cc[j][3]});
    }
    gelu_n(z, IC<7>());
#pragma unroll
    for (int j = 0; j < 7; ++j) z1d[j] = pack2(z[j][0], z[j][1]);
    asm volatile("" : "+v"(z1d[0]), "+v"(z1d[1]), "+v"(z1d[2]), "+v"(z1d[3]), "+v"(z1d[4]), "+v"(z1d[5]), "+v"(z1d[6]));
    SCHED();
    STAMPT(30);
    if (w == 2) {
      auto bad = [&]() {
        unsigned d = 0u;
#pragma unroll
        for (int k = 0; k < 8; ++k) d |= ne2(gv[k][0], gv[k][2]);
        return __any(d != 0u);
      };
      if (bad()) {
        _Pragma("unroll 1") for (unsigned spins = 0; spins < SPIN_CAP; ++spins) {
          __builtin_amdgcn_s_sleep(8);
          gb_ld();
          if (!bad()) break;
        }
      }
      f32x2 t[4];
#pragma unroll
      for (int k = 0; k < 4; ++k) t[k] = (f32x2){__uint_as_float(gv[2 * k][1]) + __uint_as_float(gv[2 * k + 1][1]), __uint_as_float(gv[2 * k][3]) + __uint_as_float(gv[2 * k + 1][3])};
      *(f32x2*)(lds + L_MISC + l * 8) = rbf2((t[0] + t[1]) + (t[2] + t[3]));
    }
    if (!kh1) req(IC<WY>(), IC<30 - 2 * WZ>());
    const unsigned fo[1] = {l < 4 ? fzs + (unsigned)l * 128u : ~0u};
    const unsigned fv = ld4(rs, fo[0]);
    SCHED();
#pragma unroll
    for (int g = TR2; g < TR3; ++g) WSP_TR(g);
    SCHED();
    STAMP(14);
    if (__builtin_amdgcn_ballot_w64(fv == tag) != 15ull) wait_lines<1, 1>(rs, fo, tag);
    STAMPT(23);
#pragma unroll
    for (int f = 0; f < NW; ++f) { LDZ(f); SCHED(); }
  }

#define WO2 15
#define TRC TR3
#define VMW(n) asm volatile("s_waitcnt vmcnt(%0)" ::"n"(n) : "memory")
  {
    SCHED();
#pragma unroll
    for (int g = TRC; g < 8; ++g) WSP_TR(g);
    SCHED();
    STAMP(20);
    const int rt = rt1, kh = kh1;
    unsigned rw = (unsigned)(L_RED + w * 512 + l * 8), rr = (unsigned)(L_RED + w * 4096 + l * 8);
    asm volatile("" : "+v"(rw), "+v"(rr));
    const float bs = bflo(bs_raw);
    unsigned long long hm = 0xffff000000000000ull;
    asm volatile("" : "+s"(hm));
    unsigned l8;
    f32x4 acc2[2][4];
    float rv[8][2];
    u32x4 ud;
    u32x4 P[2][8];
    u32x2 xp[3];
    auto ld_p = [&](int pt, int i) { P[pt][i] = __builtin_amdgcn_raw_buffer_load_b128(rs, l16 + (unsigned)pt * 1024u, ui + (unsigned)i * 2048u, SC1); };
    auto red_put = [&](int j, int r) {
      *(f32x2*)(lds + rw + r * 4096) = (f32x2){acc2[j & 1][r][0], acc2[j & 1][r][1]};
      *(f32x2*)(lds + rw + (r + 4) * 4096) = (f32x2){acc2[j & 1][r][2], acc2[j & 1][r][3]};
    };
    auto red_put1 = [&](int j, int i) { *(volatile LAS float*)(lds + rw + (i >> 2) * 4096 + ((i >> 1) & 1) * 16384 + (i & 1) * 4) = acc2[j & 1][i >> 2][i & 3]; };
    auto red_get = [&](int i) { const f32x2 x = *(const volatile LAS f32x2*)(lds + rr + i * 512); rv[i][0] = x[0]; rv[i][1] = x[1]; };
    float zz[2], gg[2];
    unsigned cs;
    auto sum_rows_u = [&](unsigned v) {
      auto a = __builtin_amdgcn_permlane16_swap(v, v, false, false);
      v = a[0] + a[1];
      auto b = __builtin_amdgcn_permlane32_swap(v, v, false, false);
      return b[0] + b[1];
    };
    auto red_fin = [&](int j, int k) {
      const int e = k & 1, i = k >> 1;
#define VADD(d, x, y) asm volatile("v_add_f32 %0, %1, %2" : "=v"(d) : "v"(x), "v"(y))
      if (i < 4) VADD(rv[2 * i][e], rv[2 * i][e], rv[2 * i + 1][e]);
      if (i == 4 || i == 5) VADD(rv[4 * (i & 1)][e], rv[4 * (i & 1)][e], rv[4 * (i & 1) + 2][e]);
      if (i == 6) VADD(rv[0][e], rv[0][e], bs);
      if (i == 7) VADD(gg[e], rv[0][e], rv[4][e]);
      if (i == 8) { zz[e] = e ? bfhi(z1d[(j + 6) % 7]) : bflo(z1d[(j + 6) % 7]); asm volatile("" : "+v"(zz[e])); }
      if (i == 9) { zz[e] *= gg[e]; asm volatile("" : "+v"(zz[e])); }
      if (k == 20) { ud[j & 3] = pack2(zz[0], zz[1]); asm volatile("" : "+v"(ud[j & 3])); }
      if (k == 21) {
        if (j == 0) asm volatile("v_cndmask_b32 %0, %0, 0, %1" : "+v"(ud[0]) : "s"(hm));
        if (j == 2) asm volatile("v_add3_u32 %0, %1, %2, %3" : "=v"(cs) : "v"(ud[0]), "v"(ud[1]), "v"(ud[2]));
        if (j == 3) {
          cs = tag - sum_rows_u(cs + ud[3]);
          asm volatile("v_cndmask_b32 %0, %0, %1, %2" : "+v"(ud[0]) : "v"(cs), "s"(hm));
        }
        if (j >= 4) { ud[3] = (j == 4 ? tag : ud[3]) - ud[j & 3]; asm volatile("" : "+v"(ud[3])); }
        if (j % 3 == 0 && j) __builtin_amdgcn_raw_buffer_store_b128(ud, rs, l16 + (unsigned)(j / 6) * 1024u, uo, SC1);
      }
    };
#define P1M 4
#define G2S 0
    struct Sch { int wide, lp, prio, lf, b1, b2, wp[16], rp[8], vp[22]; };
    constexpr Sch SC[4] = {
      {0, 2, 8, 1, 18, 29, {0, 1, 2, 3, 4, 5, 6, 7, 8, 9, 10, 11, 12, 13, 14, 15}, {19, 19, 20, 20, 21, 21, 22, 22}, {29, 29, 30, 30, 31, 31, 32, 33, 35, 36, 37, 38, 39, 40, 41, 42, 43, 44, 45, 46, 47, 48}},
      {1, 3, 4, 0, 18, 28, {3, 7, 11, 15, 99, 99, 99, 99, 99, 99, 99, 99, 99, 99, 99, 99}, {20, 20, 21, 21, 22, 22, 24, 24}, {28, 28, 29, 29, 30, 30, 31, 31, 32, 32, 33, 33, 34, 34, 35, 36, 36, 37, 37, 38, 38, 39}},
      {0, 1, 4, 1, 17, 29, {0, 1, 2, 3, 4, 5, 6, 7, 8, 9, 10, 11, 12, 13, 14, 15}, {17, 17, 18, 18, 19, 19, 20, 20}, {29, 29, 30, 30, 31, 31, 32, 34, 35, 36, 38, 39, 40, 42, 43, 44, 45, 46, 47, 48, 48, 49}},
      {0, 2, 0, 1, 17, 25, {0, 1, 2, 3, 4, 5, 6, 7, 8, 9, 10, 11, 12, 13, 14, 15}, {17, 17, 18, 18, 19, 19, 20, 20}, {25, 25, 26, 26, 27, 27, 28, 28, 29, 29, 30, 30, 31, 31, 32, 33, 23, 24, 34, 35, 36, 37}}};
#define JF 4
#define PC0 26
#define P1D 2
#define VL 2
    constexpr Sch S = SC[G2S];
    constexpr Sch S5 = {0, S.lp, S.prio, S.lf, 17, 24, {0, 1, 2, 3, 4, 5, 6, 7, 8, 9, 10, 11, 12, 13, 14, 15}, {17, 17, 18, 18, 19, 19, 20, 20}, {24, 24, 25, 25, 26, 26, 27, 27, 28, 28, 29, 29, 30, 30, 31, 31, 22, 23, 32, 32, 33, 33}};
    constexpr Sch S6 = {1, S.lp, S.prio, S.lf, 5, 12, {99, 99, 99, 3, 99, 99, 99, 99, 99, 99, 99, 99, 99, 99, 99, 99}, {5, 5, 6, 6, 7, 7, 8, 8},
                        {12, 12 + 1 / VL, 12 + 2 / VL, 12 + 3 / VL, 12 + 4 / VL, 12 + 5 / VL, 12 + 6 / VL, 12 + 7 / VL, 12 + 8 / VL, 12 + 9 / VL, 12 + 10 / VL, 12 + 11 / VL, 12 + 12 / VL, 12 + 13 / VL, 12 + 14 / VL, 12 + 15 / VL, 10, 11,
                         12 + 16 / VL, 12 + 17 / VL, 12 + 18 / VL, 12 + 19 / VL}};
    unsigned wb[3], xo;
#define XPM 19
    const unsigned wl[3] = {(unsigned)(120 + WO3 + w) * 1024u, (unsigned)(128 + WO3 + w) * 1024u, wl2}, wg = wgb - (unsigned)(WO3 * 1024);
#define NAF 32
#define NA0 18
    u32x4 af[NAF];
#define LDA(m) af[(m) % NAF] = *(const u32x4*)(lds + wb[WOA(2 * (m)) >> 16] + (WOA(2 * (m)) & 65535))
    unsigned ck[2];
    const unsigned tag4 = tag * 4u;
    auto p_chk = [&](int pt, int pr, int k) {
      u32x4* q = P[pt] + 4 * pr;
      if (k < 2) ck[k & 1] = q[k & 1][0] + q[k & 1][1];
      else if (k < 8) ck[k & 1] = ck[k & 1] + q[(k & 1) + 2 * ((k >> 2) & 1)][k & 2] + q[(k & 1) + 2 * ((k >> 2) & 1)][(k & 2) + 1];
      else if (k == 8) ck[0] = pt ? ck[0] + ck[1] : sum_rows_u(ck[0] + ck[1]);
      if (k < 9) asm volatile("" : "+v"(ck[k & 1]));
      if (k == 9) {
        if (__builtin_expect(__any(ck[0] != tag4), 0)) {
          _Pragma("unroll 1") for (unsigned spins = 0; spins < SPIN_CAP; ++spins) {
            unsigned d = 0u;
#pragma unroll
            for (int i = 0; i < 4; ++i) ld_p(pt, 4 * pr + i);
#pragma unroll
            for (int i = 0; i < 4; ++i) d += q[i][0] + q[i][1] + q[i][2] + q[i][3];
            if (!pt) d = sum_rows_u(d);
            if (!__any(d != tag4)) break;
            __builtin_amdgcn_s_sleep(1);
          }
        }
      }
      if (k >= 10 && !pt) { unsigned x = q[(k - 10) & 3][0]; asm volatile("v_cndmask_b32 %0, %0, 0, %1" : "+v"(x) : "s"(hm)); q[(k - 10) & 3][0] = x; }
      if (k >= 10 && k < 13 && pt) { q[(k - 10) % 3][3] = q[3][(k - 10) % 3]; asm volatile("" : "+v"(q[(k - 10) % 3])); }
    };
    sfor<0, 224>([&](auto M_) __attribute__((always_inline)) {
      constexpr int mm = decltype(M_)::value, j = mm >> 5, m = mm & 31;
      constexpr bool ro = j >= 5 && m >= 16;
      constexpr int t = ro ? 4 + (m & 3) : m >> 2, r = ro ? (m >> 2) & 3 : m & 3, f = 8 * j + t;
      constexpr Sch C = j < JF ? S : j < 6 ? S5 : S6, B = j <= JF ? S : S5;
      if (t == 0) asm volatile("v_mfma_f32_16x16x32_bf16 %0, %1, %2, 0" : "=&v"(acc2[j & 1][r]) : "v"(zw[f % NW]), "v"(wsp[r][t]), "v"(zw[(f | 1) % NW]));
      else asm volatile("v_mfma_f32_16x16x32_bf16 %0, %1, %2, %0" : "+v"(acc2[j & 1][r]) : "v"(zw[f % NW]), "v"(wsp[r][t]), "v"(zw[(f | 1) % NW]));
      SCHED();
      if (S.prio && (m + 31 - S.lp) % (S.prio ? S.prio : 1) == 0) __builtin_amdgcn_s_setprio(1);
      auto ld = [&]() {
        if ((m & 3) == S.lp) {
          if (S.prio && (m + 32 - S.lp) % (S.prio ? S.prio : 1) == 0) __builtin_amdgcn_s_setprio(0);
          constexpr int g = S.lp == 3 ? mm >> 2 : (mm >> 2) - 1;
          if (g >= 0 && g + NW < 56) LDZ(g + NW);
          constexpr int y = g + NW - 56;
          if (y >= 0 && WO2 + y < 15) wo_dma(WO2 + y);
          SCHED();
        }
        if (j == 6 && m >= P1M && m < P1M + 8 * P1D && (m - P1M) % P1D == 0) { ld_p(0, ((m - P1M) / P1D) & 7); SCHED(); }
      };
      if (!S.lf) ld();
      if ((m == C.b1 && j > 0) || m == C.b2) {
        if (j == 6 && m == C.b2 && WO2 < 15) VMW(m > P1M ? (m - P1M + P1D - 1) / P1D : 0);
        asm volatile("" ::: "memory"); __builtin_amdgcn_s_waitcnt(0xC07F); RBAR(); SCHED();
      }
      sfor<0, 16>([&](auto I_) __attribute__((always_inline)) {
        constexpr int i = decltype(I_)::value;
        if (j > 0 && C.wp[i] == m) { if (C.wide) red_put(j + 1, i & 3); else red_put1(j + 1, i); SCHED(); }
        if (j > 0 && i < 8 && C.rp[i & 7] == m) { red_get(i & 7); SCHED(); }
      });
      if (j == 5 && m >= 27 && (m & 1)) { red_put(j, ((m - 27) >> 1) & 3); SCHED(); }
      if (j == 6 && m >= 23 && (m & 3) == 3) { red_put(j, ((m - 23) >> 2) & 3); SCHED(); }
      if (j == 6 && m == XPM) {
        asm volatile("v_lshrrev_b32 %0, 1, %1" : "=v"(l8) : "v"(l16));
        xo = (l8 & 0x180u) + (unsigned)psi((l8 >> 3) & 15) * 8u;
        asm volatile("" : "+v"(xo));
      }
      if (j == 6 && m > XPM && m < XPM + 4) { xp[(m - XPM - 1) % 3] = __builtin_amdgcn_raw_buffer_load_b64(rs, xo + (unsigned)(m - XPM - 1) * 512u, xs, BULK_AUX); SCHED(); }
      if (j == 6 && m == 4) {
#pragma unroll
        for (int i = 0; i < 3; ++i) { wb[i] = (unsigned)(kh * 1024 + i * 65536) + l16; asm volatile("" : "+v"(wb[i])); }
      }
      if (j == 6 && m != 23 && m != 27 && m != 31) {
        constexpr int n = NA0 - 1 - (30 - m) + (m < 27) + (m < 23);
        if (n >= 0) { LDA(n >= 0 ? n : 0); SCHED(); }
      }
      if (j == 6 && m >= PC0 && m < PC0 + 5) { p_chk(0, 0, 2 * (m - PC0)); SCHED(); if (m < PC0 + 4) p_chk(0, 0, 2 * (m - PC0) + 1); SCHED(); }
      if (j == 6 && m == PC0 - 1) { red_fin(6, 16); red_fin(6, 17); SCHED(); }
      sfor<0, 22>([&](auto K_) __attribute__((always_inline)) {
        constexpr int k = decltype(K_)::value;
        if (j > 1 && B.vp[k] == m + 32) { red_fin(j > 1 ? j - 2 : 0, k); SCHED(); }
        if (j > 0 && C.vp[k] == m) { red_fin(j > 0 ? j - 1 : 0, k); SCHED(); }
      });
      if (S.lf) ld();
    });
    STAMP(15);
#define NM0 0
#define GM 19
#define MB 24
    f32x4 acc[5];
    f32x4 xf[3], gf[3];
    unsigned go, oo, eo, ho, tw, tr, o1, ta, tb;
    const unsigned s2 = (unsigned)(L_MISC + 128 * kh), s4 = s1 - (unsigned)kh * 1024u;
    unsigned long long om = kh ? ~0ull : 0xccccccccccccccccull;
#define PIN2(a, b) asm volatile("" : "+v"(a), "+v"(b))
    auto glue = [&](int k) {
      if (k == 0) { ta = l8 >> 5; tb = l16 & 48u; PIN2(ta, tb); }
      if (k == 1) { tr = __umul24(ta, 112u) + tb; oo = ta & 12u; PIN2(tr, oo); }
      if (k == 2) { tr += s1; oo += ta; PIN2(tr, oo); }
      if (k == 3) { oo = __umul24(oo, 1280u) + tb; ta = l8 >> 4; PIN2(oo, ta); }
      if (k == 4) { ta &= 24u; tb = l16 & 0xf0u; PIN2(ta, tb); }
      if (k == 5) { tw = __umul24(tb, 7u) + ta; go = (ta << 1) + s2; PIN2(tw, go); }
      if (k == 6) { tw += s1; asm volatile("v_cndmask_b32 %0, %1, -2.0, %2" : "=v"(o1) : "v"(oo), "s"(om)); asm volatile("" : "+v"(tw)); }
      if (k == 7) { eo = l16 + s3; ho = l16 + s4; PIN2(eo, ho); }
      if (k >= 8 && k < 14) {
        const unsigned x = xp[((k - 8) >> 1) % 3][k & 1];
        float lo = bflo(x), hi = bfhi(x);
        PIN2(lo, hi);
        xf[((k - 8) >> 1) % 3][2 * (k & 1)] = lo; xf[((k - 8) >> 1) % 3][2 * (k & 1) + 1] = hi;
      }
    };
    auto g3 = [&](auto M0_, auto M1_) __attribute__((always_inline)) {
      sfor<decltype(M0_)::value, decltype(M1_)::value>([&](auto M_) __attribute__((always_inline)) {
        constexpr int mi = decltype(M_)::value;
        constexpr int m = mi < 55 ? mi : mi < 64 ? 55 + 5 * ((mi - 55) / 3) + (mi - 55) % 3 : 58 + 5 * ((mi - 64) / 2) + (mi & 1), u2 = m / 5;
        const u32x4 fb = u2 < 8 ? P[0][u2 & 7] : P[1][u2 < 11 ? (u2 - 8) & 3 : (u2 - 7) & 7];
        if (m < 5) asm volatile("v_mfma_f32_16x16x32_bf16 %0, %1, %2, 0" : "=&v"(acc[m % 5]) : "v"(af[m % NAF]), "v"(fb));
        else asm volatile("v_mfma_f32_16x16x32_bf16 %0, %1, %2, %0" : "+v"(acc[m % 5]) : "v"(af[m % NAF]), "v"(fb));
        SCHED();
        if (mi == NM0) RBAR();
        if (mi >= NM0 && mi < NM0 + 3) dma1k(rs, lds + wl[(mi - NM0) % 3], l16, wg + wl[(mi - NM0) % 3]);
        if (mi >= 5 && mi < 19) p_chk(0, 1, mi - 5);
        if (mi >= GM && mi < GM + 14) glue(mi - GM);
        if (mi >= 47 && mi < 54) { p_chk(1, 1, 2 * (mi - 47)); SCHED(); p_chk(1, 1, 2 * (mi - 47) + 1); }
        if (mi >= NM0 && mi < 40 && NA0 + mi - NM0 < 60) LDA(NA0 + mi - NM0);
        if (mi >= 66 && mi < 69) *(f32x4*)(lds + eo + (mi - 66) * 1024) = acc[(mi - 66) % 3];
        SCHED();
      });
    };
    __builtin_amdgcn_s_setprio(3);
    asm volatile("s_nop 7");
    red_put(6, 3);
    SCHED();
    BAR();
#pragma unroll
    for (int i = 0; i < 8; ++i) red_get(i);
    SCHED();
#pragma unroll
    for (int k = 0; k < 20; ++k) red_fin(6, k < 16 ? k : k + 2);
    SCHED();
    __builtin_amdgcn_s_setprio(0);
    STAMPT(16);
#pragma unroll
    for (int k = 9; k < 14; ++k) p_chk(0, 0, k);
    SCHED();
    g3(IC<0>(), IC<MB>());
    STAMPT(22);
    DRAIN();
    RBAR();
    STAMPT(27);
#define MY 12
    if (MY == 0 || !kh) {
#pragma unroll
      for (int i = 0; i < 8; ++i) { ld_p(1, i); SCHED(); }
    }
    g3(IC<MB>(), IC<MB + MY>());
    if (MY > 0 && kh) {
#pragma unroll
      for (int i = 0; i < 8; ++i) { ld_p(1, i); SCHED(); }
    }
    STAMPT(26);
    g3(IC<MB + MY>(), IC<40>());
    STAMPT(24);
#pragma unroll
    for (int m = NA0 + 40 - NM0; m < 70; ++m) LDA(m);
#pragma unroll
    for (int x = 0; x < 3; ++x) gf[x] = *(const f32x4*)(lds + go + x * 64);
    SCHED();
    const rsrc_t ro = __builtin_amdgcn_make_buffer_rsrc((void*)p.out, 0, S_ * D_ * 2, 0x00020000);
    STAMPT(17);
#pragma unroll
    for (int k = 0; k < 14; ++k) p_chk(1, 0, k);
    asm volatile("s_nop 1");
    STAMPT(25);
    g3(IC<40>(), IC<70>());
    STAMPT(18);
    BAR();
    STAMPT(28);
    f32x4 o4[3];
    u32x4 ov[2];
#pragma unroll
    for (int j = 2; j >= 0; --j) o4[j] = *(const volatile LAS f32x4*)(lds + ho + j * 1024);
    asm volatile("" : "+v"(acc[2]), "+v"(acc[3]), "+v"(acc[4]));
    SCHED();
#pragma unroll
    for (int j = 2; j >= 0; --j) {
      const f32x4 v = __builtin_elementwise_fma(gf[j], o4[j] + acc[2 + j], xf[j]);
      *(volatile LAS u32x2*)(lds + tw + j * 32) = (u32x2){pack2(v[0], v[1]), pack2(v[2], v[3])};
      if (j < 2) ov[j] = *(const volatile LAS u32x4*)(lds + tr + j * 32);
    }
    __builtin_amdgcn_raw_buffer_store_b128(ov[1], ro, oo + 32u, os, 0);
    __builtin_amdgcn_raw_buffer_store_b128(ov[0], ro, o1, os, 0);
    if (bid == 0 && w == 5 && l8 == 0u) st4(rs, WS_EPOCH, tag);
  }
  STAMP(19);
}

extern "C" __device__ __attribute__((used, noinline, visibility("hidden"))) void mega_tail() {}

extern "C" void kernel_launch(void* const* d_in, const int* in_sizes, int n_in, void* d_out, int out_size,
                              void* d_ws, size_t ws_size, hipStream_t stream) {
  Params p;
  p.q = (const u16*)d_in[0];
  p.k = (const u16*)d_in[1];
  p.v = (const u16*)d_in[2];
  p.shift_k = (const u16*)d_in[3];
  p.ssm_k = (const u16*)d_in[4];
  p.ssm_D = (const u16*)d_in[5];
  p.w_in = (const u16*)d_in[7];
  p.w_sp = (const u16*)d_in[8];
  p.b_sp = (const u16*)d_in[9];
  p.w_out = (const u16*)d_in[10];
  p.cvec = (const u16*)d_in[11];
  p.w_mod = (const u16*)d_in[12];
  p.out = (u16*)d_out;
  p.ws = (char*)d_ws;
  mega<<<256, 512, 0, stream>>>(p);
}
```

```cpp
#define YC_T 930
#define Y_T 960
#define AUX_T 700
#include <hip/hip_runtime.h>
#include <stdint.h>

typedef unsigned short u16;
typedef __attribute__((ext_vector_type(8))) short bf16x8;
typedef __attribute__((ext_vector_type(4))) short bf16x4;
typedef __attribute__((ext_vector_type(4))) float f32x4;
typedef __attribute__((ext_vector_type(2))) float f32x2;
typedef __attribute__((ext_vector_type(4))) unsigned int u32x4;
typedef __attribute__((ext_vector_type(2))) unsigned int u32x2;
#define LAS __attribute__((address_space(3)))
typedef __amdgpu_buffer_rsrc_t rsrc_t;
typedef __attribute__((ext_vector_type(4))) short s16x4;

constexpr int S_ = 2048, D_ = 640, F_ = 1728, FH = 864, CB = 108;

constexpr unsigned WS_EPOCH = 0;
constexpr unsigned WS_FWI = 4096;
constexpr unsigned WS_FZ = WS_FWI + 384 * 128;
constexpr unsigned WS_FWO = WS_FZ + 256 * 128;
constexpr unsigned F4S = 896 * 128;
constexpr unsigned WS_CS = WS_FZ + 256 * 128;
constexpr unsigned WS_MOD = 131072;
constexpr unsigned WS_MODP = WS_MOD + 81920;
constexpr unsigned WS_ST1 = 393216;
constexpr unsigned WS_PART = 524288;
constexpr unsigned WS_CSP = WS_PART + 262144;
constexpr unsigned WS_FX = WS_CSP + 655360;
constexpr unsigned WS_X1B = WS_FX + 16 * 4 * 160 * 128;
constexpr unsigned WS_QB = WS_X1B + 2621440;
constexpr unsigned WS_KV3 = WS_QB + 2621440;
constexpr unsigned WS_MODQ = WS_KV3 + 1310720;
constexpr unsigned WBLK = 143360;
constexpr unsigned WS_WINF = WS_MODQ + 245760;
constexpr unsigned WS_WOUTF = WS_WINF + 16 * WBLK;
constexpr unsigned WS_Z2F = WS_WOUTF + 8 * WBLK;
constexpr unsigned WS_UF = WS_Z2F + 8 * 64 * 7168;
constexpr unsigned WS_F4 = WS_UF + 32 * 4 * 8 * 4096;
constexpr unsigned WS_END = WS_F4 + 4 * F4S;
constexpr unsigned WS_STAMP = 200u << 20;

constexpr int G_COPY = 4160;
constexpr int L_U = WBLK;
constexpr int U_CH = (256 + S_) * 2 + 32;
constexpr int L_FLG = L_U + 4 * U_CH;
constexpr int L_RT = 16384;
constexpr int L_CONST = WBLK;
constexpr int L_PST = L_CONST + 2048;
constexpr int L_Z = L_CONST + 5120;
constexpr int L_ST2 = L_Z + 14336;
constexpr int L_MISC = L_ST2 + 512;
constexpr int LDS_TOTAL = 163840;

#define RT2_T 360
#define CB_T 380
#define W_T 730
#define YSL 2
#define K_T 460
#ifndef M_T
#define M_T 520
#endif
#define WA_T 580
constexpr int SC1 = 16, NT = 2;
constexpr unsigned SPIN_CAP = 1u << 16;

__device__ __forceinline__ float bflo(unsigned x) { return __uint_as_float(x << 16); }
__device__ __forceinline__ float bfhi(unsigned x) { return __uint_as_float(x & 0xffff0000u); }
__device__ __forceinline__ unsigned f2bf(float x) {
  __bf16 b = (__bf16)x;
  return (unsigned)__builtin_bit_cast(unsigned short, b);
}
__device__ __forceinline__ float rbf(float x) { return bflo(f2bf(x)); }
typedef __attribute__((ext_vector_type(2))) __bf16 bf2_t;
__device__ __forceinline__ unsigned pack2(float lo, float hi) {
  f32x2 v = {lo, hi};
  return __builtin_bit_cast(unsigned, __builtin_convertvector(v, bf2_t));
}
__device__ __forceinline__ f32x2 rbf2(f32x2 v) {
  const unsigned p = __builtin_bit_cast(unsigned, __builtin_convertvector(v, bf2_t));
  return (f32x2){__uint_as_float(p << 16), __uint_as_float(p & 0xffff0000u)};
}
__device__ __forceinline__ f32x4 rbf4(f32x4 v) {
  const f32x2 a = rbf2((f32x2){v[0], v[1]}), b = rbf2((f32x2){v[2], v[3]});
  return (f32x4){a[0], a[1], b[0], b[1]};
}
template <int CTRL>
__device__ __forceinline__ float dpp_f(float v) {
  return __int_as_float(__builtin_amdgcn_update_dpp(0, __float_as_int(v), CTRL, 0xf, 0xf, true));
}
__device__ __forceinline__ float sum8(float v) {
  v += dpp_f<0xB1>(v);
  v += dpp_f<0x4E>(v);
  v += dpp_f<0x141>(v);
  return v;
}
__device__ __forceinline__ float sum16(float v) {
  v = sum8(v);
  v += dpp_f<0x140>(v);
  return v;
}
__device__ __forceinline__ float sum_rows(float v) {
  auto a = __builtin_amdgcn_permlane16_swap(__float_as_uint(v), __float_as_uint(v), false, false);
  v = __uint_as_float(a[0]) + __uint_as_float(a[1]);
  auto b = __builtin_amdgcn_permlane32_swap(__float_as_uint(v), __float_as_uint(v), false, false);
  return __uint_as_float(b[0]) + __uint_as_float(b[1]);
}
#define MFMA16(a, b, c) __builtin_amdgcn_mfma_f32_16x16x32_bf16(__builtin_bit_cast(bf16x8, a), __builtin_bit_cast(bf16x8, b), c, 0, 0, 0)
#define MFMA16K16(a, b, c) __builtin_amdgcn_mfma_f32_16x16x16bf16_1k(__builtin_bit_cast(bf16x4, a), __builtin_bit_cast(bf16x4, b), c, 0, 0, 0)
__device__ __forceinline__ u32x2 trd(const char* p) {
  return __builtin_bit_cast(u32x2, __builtin_amdgcn_ds_read_tr16_b64_v4i16((LAS s16x4*)p));
}
__device__ __forceinline__ u32x4 trfrag(const char* p, int pitch) {
  u32x2 a = trd(p), b = trd(p + 16 * pitch);
  return (u32x4){a[0], a[1], b[0], b[1]};
}
#define BAR() asm volatile("s_waitcnt lgkmcnt(0)\n\ts_barrier" ::: "memory")
#define DRAIN() do { __builtin_amdgcn_s_waitcnt(0x0F70); asm volatile("" ::: "memory"); } while (0)
#define SCHED() __builtin_amdgcn_sched_barrier(0)
template <int N> struct IC { static constexpr int value = N; };
template <int I, int N, class F>
__device__ __forceinline__ void sfor(F&& f) {
  if constexpr (I < N) { f(IC<I>()); sfor<I + 1, N>(f); }
}

__device__ __forceinline__ u32x4 ld16(rsrc_t r, unsigned off) { return __builtin_amdgcn_raw_buffer_load_b128(r, off, 0, SC1); }
__device__ __forceinline__ u32x2 ld8(rsrc_t r, unsigned off) { return __builtin_amdgcn_raw_buffer_load_b64(r, off, 0, SC1); }
__device__ __forceinline__ unsigned ld4(rsrc_t r, unsigned off) { return __builtin_amdgcn_raw_buffer_load_b32(r, off, 0, SC1); }
#define ST_AUX 16
__device__ __forceinline__ void st16(rsrc_t r, unsigned off, u32x4 v) { __builtin_amdgcn_raw_buffer_store_b128(v, r, off, 0, ST_AUX); }
__device__ __forceinline__ void st8(rsrc_t r, unsigned off, u32x2 v) { __builtin_amdgcn_raw_buffer_store_b64(v, r, off, 0, SC1); }
__device__ __forceinline__ void st_flag4(rsrc_t r, unsigned off, unsigned tag, int l) {
  if (l < 32) st16(r, WS_F4 - WS_FWI + off + (unsigned)(l >> 3) * F4S + (unsigned)(l & 7) * 16u, (u32x4){tag, tag, tag, tag});
}
__device__ __forceinline__ void st4(rsrc_t r, unsigned off, unsigned v) { __builtin_amdgcn_raw_buffer_store_b32(v, r, off, 0, SC1); }
#define BULK_AUX 16
__device__ __forceinline__ u32x4 ld16p(rsrc_t r, unsigned off) { return __builtin_amdgcn_raw_buffer_load_b128(r, off, 0, BULK_AUX); }
__device__ __forceinline__ u32x2 ld8p(rsrc_t r, unsigned off) { return __builtin_amdgcn_raw_buffer_load_b64(r, off, 0, BULK_AUX); }
__device__ __forceinline__ void dma1k(rsrc_t r, char* lds_dst, unsigned l16, unsigned soff) {
  __builtin_amdgcn_raw_ptr_buffer_load_lds(r, (LAS void*)lds_dst, 16, l16, soff, 0, BULK_AUX);
}

#define STAMP(i)
#define STAMPF(i)
#define STAMPT(i) STAMP(i)

template <int N, int SLP>
__device__ __forceinline__ void wait_lines(rsrc_t r, const unsigned (&off)[N], unsigned tag) {
  bool need[N];
#pragma unroll
  for (int i = 0; i < N; ++i) need[i] = off[i] != ~0u;
  _Pragma("unroll 1") for (unsigned spins = 0; spins < SPIN_CAP; ++spins) {
    unsigned v[N];
#pragma unroll
    for (int i = 0; i < N; ++i) {
      v[i] = tag;
      if (need[i]) v[i] = ld4(r, off[i]);
    }
    bool any = false;
#pragma unroll
    for (int i = 0; i < N; ++i) { need[i] = v[i] != tag; any = any || need[i]; }
    if (!__any(any)) break;
    __builtin_amdgcn_s_sleep(SLP);
  }
  asm volatile("" ::: "memory");
}

__device__ __forceinline__ int tau(int rt, int n) { return 32 * (rt >> 1) + 8 * (n >> 2) + 4 * (rt & 1) + (n & 3); }
__device__ __forceinline__ int psi(int n) { return 2 * (n & 7) + (n >> 3); }
#define QX_CM(d, s0, s1, P) "v_cndmask_b32_dpp " d ", " s0 ", " s1 ", vcc quad_perm:" P " row_mask:0xf bank_mask:0xf\n\t"
#define QX_BODY(M, P) \
  asm("s_mov_b32 vcc_lo, " M "\n\ts_mov_b32 vcc_hi, " M "\n\t" \
      QX_CM("%0", "%12", "%8", P) "v_xor_b32 %4, %8, %0\n\t" QX_CM("%1", "%13", "%9", P) "v_xor_b32 %5, %9, %1\n\t" \
      QX_CM("%2", "%14", "%10", P) "v_xor_b32 %6, %10, %2\n\t" QX_CM("%3", "%15", "%11", P) "v_xor_b32 %7, %11, %3" \
      : "=&v"(t[0]), "=&v"(t[1]), "=&v"(t[2]), "=&v"(t[3]), "=&v"(u[0]), "=&v"(u[1]), "=&v"(u[2]), "=&v"(u[3]) \
      : "v"(a[0]), "v"(a[1]), "v"(a[2]), "v"(a[3]), "v"(b[0]), "v"(b[1]), "v"(b[2]), "v"(b[3]) : "vcc")
template <int BIT>
__device__ __forceinline__ void quad_xchg3(u32x4& a, u32x4& b) {
  unsigned t[4], u[4];
  if (BIT == 0) QX_BODY("0x55555555", "[1,0,3,2]");
  else QX_BODY("0x33333333", "[2,3,0,1]");
  a = (u32x4){t[0], t[1], t[2], t[3]};
#pragma unroll
  for (int d = 0; d < 4; ++d) b[d] ^= (unsigned)__builtin_amdgcn_update_dpp(0, (int)u[d], BIT == 0 ? 0xB1 : 0x4E, 0xf, 0xf, true);
}
__device__ __forceinline__ void quad_transpose(u32x4& x0, u32x4& x1, u32x4& x2, u32x4& x3) {
  quad_xchg3<0>(x0, x1);
  quad_xchg3<0>(x2, x3);
  quad_xchg3<1>(x0, x2);
  quad_xchg3<1>(x1, x3);
  asm volatile("" : "+v"(x0), "+v"(x1), "+v"(x2), "+v"(x3));
}

struct Params {
  const u16 *q, *k, *v, *shift_k, *ssm_k, *ssm_D, *w_in, *w_sp, *b_sp, *w_out, *cvec, *w_mod;
  u16* out;
  char* ws;
};

__global__ __launch_bounds__(512, 2) void mega(Params p) {
  __shared__ __attribute__((aligned(16))) char lds[LDS_TOTAL];
  {
    unsigned long long pc0;
    unsigned vt;
    asm volatile("v_readfirstlane_b32 vcc_lo, %3\n\ts_cmpk_lt_u32 %2, 192\n\ts_cbranch_scc1 4f\n\ts_cmpk_lt_u32 vcc_lo, 64\n\ts_cbranch_scc0 4f\n\t"
                 "s_lshr_b32 vcc_hi, %2, 3\n\ts_sub_u32 vcc_hi, vcc_hi, 24\n\ts_lshl_b32 vcc_hi, vcc_hi, 13\n\t"
                 "v_lshlrev_b32 %1, 7, %3\n\tv_add_u32 %1, vcc_hi, %1\n\ts_getpc_b64 %0\n.Lmega_here:\n\t"
                 "v_min_u32 %1, mega_tail-.Lmega_here-4, %1\n\ts_movk_i32 m0, 0x2000\n\ts_nop 1\n\tglobal_load_lds_dword %1, %0\n4:\n\t"
                 "s_cmpk_lt_u32 %2, 160\n\ts_cbranch_scc0 1f\n\ts_cmpk_lt_u32 vcc_lo, 256\n\ts_cbranch_scc1 3f\n\t"
                 "s_call_b64 vcc, .Ltouch1\n\ts_branch 3f\n1:\n\t"
                 "s_cmpk_lt_u32 vcc_lo, 256\n\ts_cbranch_scc1 2f\n\ts_cmpk_lt_u32 %2, 224\n\ts_cbranch_scc0 2f\n\ts_call_b64 vcc, .Ltouch3\n\ts_branch 3f\n"
                 "2:\n\ts_call_b64 vcc, .Ltouch2\n3:"
                 : "=&s"(pc0), "=&v"(vt) : "s"(blockIdx.x), "v"(threadIdx.x) : "vcc", "scc", "memory");
  }
  SCHED();
  const int tid = threadIdx.x, l = tid & 63;
  const int w = __builtin_amdgcn_readfirstlane(tid >> 6);
  const int bid = blockIdx.x, xcd = bid & 7, idx = bid >> 3;
#define TOUCH_AT(n) asm volatile("s_branch 1f\n.Ltouch" #n ":\n\ts_setpc_b64 vcc\n1:")
  char* const ws = p.ws;
  asm volatile("" ::"s"(p.q), "s"(p.k), "s"(p.v), "s"(p.shift_k), "s"(p.ssm_k), "s"(p.ssm_D), "s"(p.w_in), "s"(p.w_sp), "s"(p.b_sp),
               "s"(p.w_out), "s"(p.cvec), "s"(p.w_mod), "s"(p.out), "s"(p.ws));
  unsigned epoch, tag, t_start;
  unsigned long long t64;
  asm volatile("s_load_dword %0, %2, 0x0\n\ts_memrealtime %1" : "=&s"(epoch), "=&s"(t64) : "s"(ws));
#define TAG_READY() do { SCHED(); \
    asm volatile("s_waitcnt lgkmcnt(0)\n\ts_mul_i32 %0, %2, 0x9e3779b1\n\ts_add_i32 %0, %0, 0x9e3779b9\n\ts_mov_b64 %1, %3" \
                 : "=&s"(tag), "=&s"(t64r) : "s"(epoch), "s"(t64) : "scc"); t_start = (unsigned)t64r; SCHED(); } while (0)
  unsigned long long t64r;
  const rsrc_t rs = __builtin_amdgcn_make_buffer_rsrc(ws, 0, WS_END, 0x00020000);
  const unsigned l16 = (unsigned)l * 16u;
  STAMP(0);
  const bool is_conv = idx < 20;
  const int quad = xcd * 20 + idx;
  const int R = is_conv ? (xcd & 3) * 5 + (idx >> 2) : (xcd & 3) * 3 + 15 + (idx >> 2);
  const int c = (xcd >> 2) * 4 + (idx & 3);
  const unsigned fwi = WS_F4 + (unsigned)(xcd & 3) * F4S, fz = fwi + (WS_FZ - WS_FWI);
  const int n16 = l & 15, h4 = l >> 4;
  u32x4 wsp[4][8];
#define ROTC 2
#define WAUX 0
  const int rotg = ((idx >> 4) ^ ((ROTC & 2) ? idx >> 1 : 0)) & 1, rotb = (idx >> 2) & 3, rotr = (ROTC & 1) ? (idx & 1) * 2 : 0;
  const rsrc_t rsp = __builtin_amdgcn_make_buffer_rsrc((void*)p.w_sp, 0, S_ * S_ * 2, 0x00020000);
  const unsigned wsp_vo = (unsigned)(((l >> 2) & 3) * 8 * 4096 + h4 * 64 + ((l + rotb) & 3) * 16);
#define LDW(k) LDW_(k)
#define WSP_TR(g) WSP_TR_(g)
#define WROW R
#define LDW_(k) wsp[((k) >> 2) & 3][((k) >> 4) * 4 + ((k) & 3)] = __builtin_amdgcn_raw_buffer_load_b128(rsp, wvo, \
    (unsigned)((WROW * 64 + 16 * ((((k) >> 2) & 2) ^ rotr) + 4 * (((k) >> 2) & 1) + ((k) & 3)) * 4096 + w * 512 + ((((k) >> 4) ^ rotg) * 256)), WAUX)
#define WSP_TR_(g) quad_transpose(wsp[(g) & 3][((g) >> 2) * 4], wsp[(g) & 3][((g) >> 2) * 4 + 1], wsp[(g) & 3][((g) >> 2) * 4 + 2], wsp[(g) & 3][((g) >> 2) * 4 + 3])
  const rsrc_t rwi = __builtin_amdgcn_make_buffer_rsrc((void*)p.w_in, 0, D_ * F_ * 2, 0x00020000);
  const rsrc_t rwo = __builtin_amdgcn_make_buffer_rsrc((void*)p.w_out, 0, FH * D_ * 2, 0x00020000);
  const rsrc_t rq = __builtin_amdgcn_make_buffer_rsrc((void*)p.q, 0, S_ * D_ * 2, 0x00020000);
  auto hold = [&](unsigned tt) { while ((unsigned)__builtin_amdgcn_s_memrealtime() - t_start < tt) __builtin_amdgcn_s_sleep(2); };
  auto ne2 = [&](unsigned x, unsigned y) { unsigned d; asm("v_bitop3_b32 %0, %1, %2, %3 bitop3:0x7e" : "=v"(d) : "v"(x), "v"(y), "s"(tag)); return d; };
  volatile LAS unsigned* lf = (volatile LAS unsigned*)(lds + L_FLG);
  auto lf_set = [&](int i) { asm volatile("s_waitcnt lgkmcnt(0)" ::: "memory"); if (l == 0) lf[i] = tag; };
  auto lf_wait = [&](int i0, int n) {
    _Pragma("unroll 1") for (unsigned spins = 0; spins < (SPIN_CAP << 4); ++spins) {
      const unsigned v = lf[i0 + (l & (n - 1))];
      if (__all(v == tag)) break;
      __builtin_amdgcn_s_sleep(1);
    }
    asm volatile("" ::: "memory");
  };
  auto load_b2 = [&](int k0, int k1) {
#pragma unroll
    for (int k = k0; k < k1; ++k) {
      int i = k + (idx >> 2) * 4;
      i = (i >= 35 ? i - 35 : i) + 35 * w;
      dma1k(rs, lds + i * 1024, l16, WS_WINF + (unsigned)(8 + c) * WBLK + (unsigned)i * 1024u);
    }
  };
  char* const wo_st = lds + 122880;
  auto wo_ld = [&]() {
    const int U = R < 28 ? R : 0, u2 = U >> 1, fi = (u2 + 1) % 3;
    const int c0 = 4 * (U & 1) + (u2 < 8 ? u2 >> 1 : u2 < 11 ? 0 : 2);
    const int b0 = u2 < 8 ? CB * c0 + 2 * (u2 & 1) + 96 : CB * (c0 + (fi >> 1)) + 48 + 2 * (fi & 1), d1 = u2 < 8 ? -96 : 16, d2 = d1 + 16;
    const int b3 = u2 < 8 ? b0 - 64 : CB * (c0 + 1) + 50 + 16 * fi;
#pragma unroll
    for (int r = 0; r < 5; ++r) {
      const int g = r * 64 + l, rho = g / 10;
      const int fb = (rho & 16) ? ((rho & 2) ? b3 : b0 + d2) : ((rho & 2) ? b0 + d1 : b0);
      __builtin_amdgcn_raw_ptr_buffer_load_lds(rwo, (LAS void*)(wo_st + r * 1024), 16, (unsigned)((fb + (rho & 13)) * (D_ * 2) + (g % 10) * 16 + c * 160), 0, 0, 0);
    }
  };
  auto rt_ld = [&](int i0, char* st) {
    const int cb = (i0 < 160 ? 8 : 0) + (i0 & 7), Q = (i0 < 160 ? i0 : i0 - 160) >> 3;
#pragma unroll
    for (int r = 0; r < 7; ++r) {
      const int g = r * 64 + l;
      __builtin_amdgcn_raw_ptr_buffer_load_lds(rwi, (LAS void*)(st + r * 1024), 16, (unsigned)((g / 14) * (F_ * 2) + (g % 14) * 16 + Q * 32 * F_ * 2 + cb * CB * 2),
                                               0, 0, NT);
    }
  };
  auto rt_job = [&](int i0, char* st) {
    const int cb = (i0 < 160 ? 8 : 0) + (i0 & 7), Q = (i0 < 160 ? i0 : i0 - 160) >> 3;
    const int col = 640 * (l >> 5) + 32 * Q + (l & 31);
    DRAIN();
    u32x4 x[7], o[7];
    f32x2 xu[7][4];
    {
      const char* bp = st + (4 * h4 + (n16 >> 2)) * 224 + (n16 & 3) * 8;
#pragma unroll
      for (int j = 0; j < 7; ++j) x[j] = trfrag(bp + j * 32, 224);
#pragma unroll
      for (int j = 0; j < 7; ++j)
#pragma unroll
        for (int e = 0; e < 4; ++e) { xu[j][e] = (f32x2){bflo(x[j][e]), bfhi(x[j][e])}; asm volatile("" : "+v"(xu[j][e])); }
    }
    unsigned mk[4][2], pv = (unsigned)col * 8u, cw = l >= 32 ? (unsigned)(7168 + (l - 32) * 4) : (unsigned)(7298 + l * 2 - 4), m12 = n16 == 12 ? ~0u : 0u;
    unsigned fgo = l < 32 ? (unsigned)(l >> 3) * F4S + (unsigned)(l & 7) * 16u : 0x80000000u;
#pragma unroll
    for (int i = 0; i < 4; ++i) {
      mk[i][0] = n16 == 4 * i ? 0x3F803F80u : 0u; mk[i][1] = n16 == 4 * i + 1 ? ~0u : 0u;
      asm volatile("" : "+v"(mk[i][0]), "+v"(mk[i][1]));
    }
    asm volatile("" : "+v"(pv), "+v"(cw), "+v"(m12), "+v"(fgo));
    unsigned ob = WS_WINF + (unsigned)cb * WBLK + (unsigned)(Q * 7) * 1024u;
    asm volatile("" : "+s"(ob));
    u32x2 pp8[8];
    hold(CB_T);
    _Pragma("unroll 1") for (unsigned spins = 0; spins < SPIN_CAP; ++spins) {
#pragma unroll
      for (int i = 0; i < 8; ++i) pp8[i] = __builtin_amdgcn_raw_buffer_load_b64(rs, pv, WS_MODP + (unsigned)i * 10240u, SC1);
      if (!__any(((ne2(pp8[0][0], pp8[1][0]) | ne2(pp8[2][0], pp8[3][0]) | ne2(pp8[4][0], pp8[5][0])) | ne2(pp8[6][0], pp8[7][0])) != 0u)) break;
      __builtin_amdgcn_s_sleep(4);
    }
    STAMPF(9);
    __builtin_amdgcn_s_setprio(3);
    {
      auto pf = [&](int i) { return __uint_as_float(pp8[i][1]); };
      const float t = rbf(((pf(0) + pf(1)) + (pf(2) + pf(3))) + ((pf(4) + pf(5)) + (pf(6) + pf(7))));
      if (l >= 32) *(float*)(st + cw) = rbf(1.0f + t);
      else *(u16*)(st + cw + 2) = (u16)(__float_as_uint(t) >> 16);
    }
    asm volatile("" ::: "memory");
    const f32x4 ca = *(const volatile LAS f32x4*)(st + 7168 + h4 * 16), cc = *(const volatile LAS f32x4*)(st + 7168 + 64 + h4 * 16);
    const u32x2 e0 = *(const volatile LAS u32x2*)(st + 7296 + h4 * 8), e1 = *(const volatile LAS u32x2*)(st + 7328 + h4 * 8);
    SCHED();
#pragma unroll
    for (int j = 0; j < 7; ++j) {
      const f32x2 y0 = xu[j][0] * (f32x2){ca[0], ca[1]}, y1 = xu[j][1] * (f32x2){ca[2], ca[3]}, y2 = xu[j][2] * (f32x2){cc[0], cc[1]}, y3 = xu[j][3] * (f32x2){cc[2], cc[3]};
      o[j] = (u32x4){pack2(y0[0], y0[1]), pack2(y1[0], y1[1]), pack2(y2[0], y2[1]), pack2(y3[0], y3[1])};
      if (j == 6) {
#pragma unroll
        for (int e = 0; e < 4; ++e) o[j][e] = (o[j][e] & ~m12) | mk[3][0];
      }
      __builtin_amdgcn_raw_buffer_store_b128(o[j], rs, l16 + (unsigned)(j & 3) * 1024u, ob + (unsigned)(j >> 2) * 4096u, SC1);
      SCHED();
    }
    __builtin_amdgcn_s_setprio(0);
    STAMPF(11);
    f32x4 d[2] = {{0.f, 0.f, 0.f, 0.f}, {0.f, 0.f, 0.f, 0.f}};
#pragma unroll
    for (int j = 0; j < 7; ++j) {
      const unsigned one = mk[j & 3][0], sr = mk[j & 3][1];
      d[j >> 2] = MFMA16(((u32x4){one, one, one, one}), o[j], d[j >> 2]);
      d[j >> 2] = MFMA16(((u32x4){sr & e0[0], sr & e0[1], sr & e1[0], sr & e1[1]}), x[j], d[j >> 2]);
    }
#pragma unroll
    for (int m = 0; m < 2; ++m)
      st16(rs, WS_CSP + (unsigned)((cb * 20 + Q) * 2 + m) * 1024u + l16, (u32x4){tag, __float_as_uint(d[m][0]), tag, __float_as_uint(d[m][1])});
    STAMPF(13);
    u32x4 tg4 = {tag, tag, tag, tag};
    asm volatile("s_waitcnt vmcnt(2)" : "+v"(tg4) :: "memory");
    __builtin_amdgcn_raw_buffer_store_b128(tg4, rs, fgo, WS_F4 + (unsigned)(cb * 20 + Q) * 128u, SC1);
  };

  auto red8 = [&](float v) { v += dpp_f<0x128>(v); return sum_rows(v); };
  const rsrc_t rwm = __builtin_amdgcn_make_buffer_rsrc((void*)p.w_mod, 0, D_ * 3840 * 2, 0x00020000);
  auto gemv_load = [&](u32x4* pre, int blk, int sl, int np) {
    pre[10][0] = *(const unsigned*)(p.cvec + 8 * np * sl + 2 * (l < 4 * np ? l : 0));
#pragma unroll
    for (int pp = 0; pp < np; ++pp)
      pre[pp] = __builtin_amdgcn_raw_buffer_load_b128(rwm, (unsigned)((l >> 3) * 7680 + (l & 7) * 16), (unsigned)((8 * np * sl + pp * 8) * 7680 + blk * 128), NT);
  };
  auto gemv_fin = [&](float* acc, const u32x4* pre, int np, char* scr) {
    const unsigned cv2 = pre[10][0];
    unsigned* svt = (unsigned*)scr + w * 96;
#pragma unroll
    for (int e = 0; e < 2; ++e) {
      const int row = 2 * l + e;
      const float cv = e ? bfhi(cv2) : bflo(cv2);
      if (l < 4 * np) svt[(row & 7) * 12 + (row >> 3)] = __float_as_uint(rbf(cv * __builtin_amdgcn_rcpf(1.0f + __expf(-cv))));
    }
    asm volatile("s_waitcnt lgkmcnt(0)" ::: "memory");
    u32x4 sv[3];
#pragma unroll
    for (int i = 0; i < (np + 3) / 4; ++i) sv[i] = *(const u32x4*)(svt + (l >> 3) * 12 + 4 * i);
    SCHED();
#pragma unroll
    for (int e = 0; e < 8; ++e) acc[e] = 0.f;
#pragma unroll
    for (int pp = 0; pp < np; ++pp) {
      const unsigned sh = sv[pp >> 2][pp & 3], sl2 = sh >> 16;
#pragma unroll
      for (int e = 0; e < 4; ++e) {
        const unsigned x = pre[pp][e];
        acc[2 * e] = __builtin_amdgcn_fdot2_f32_bf16(__builtin_bit_cast(bf2_t, x), __builtin_bit_cast(bf2_t, sl2), acc[2 * e], false);
        acc[2 * e + 1] = __builtin_amdgcn_fdot2_f32_bf16(__builtin_bit_cast(bf2_t, x), __builtin_bit_cast(bf2_t, sh), acc[2 * e + 1], false);
      }
    }
#pragma unroll
    for (int e = 0; e < 8; ++e) acc[e] = red8(acc[e]);
  };
  auto gemv_put = [&](const float* acc, unsigned off) {
    float v = acc[0];
#pragma unroll
    for (int e = 1; e < 8; ++e) { v = (l >> 3) == e ? acc[e] : v; asm("" : "+v"(v)); }
    st8(rs, off + (unsigned)l * 8u, (u32x2){tag, (unsigned)__builtin_amdgcn_ds_bpermute(((l & 7) * 8 + (l >> 3)) * 4, (int)__float_as_uint(v))});
  };
  auto cs_ld = [&](u32x4* v, int cj) {
    const int cb = cj / 7, j = cj % 7;
#pragma unroll
    for (int k = 0; k < 5; ++k)
      v[k] = __builtin_amdgcn_raw_buffer_load_b128(rs, (unsigned)(h4 * 10240 + n16 * 16), WS_CSP + (unsigned)((cb * 40 + (j >> 2) + 2 * k) * 1024 + (j & 3) * 256), SC1);
  };
  auto cs_fin = [&](u32x4* v, int cj) {
    auto bad = [&]() { return __any(((ne2(v[0][0], v[0][2]) | ne2(v[1][0], v[1][2]) | ne2(v[2][0], v[2][2])) | ne2(v[3][0], v[3][2]) | ne2(v[4][0], v[4][2])) != 0u); };
    if (bad()) {
      _Pragma("unroll 1") for (unsigned spins = 0; spins < SPIN_CAP; ++spins) {
        __builtin_amdgcn_s_sleep(8);
        cs_ld(v, cj);
        if (!bad()) break;
      }
    }
    float t0 = 0.f, t1 = 0.f;
#pragma unroll
    for (int k = 0; k < 5; ++k) { t0 += __uint_as_float(v[k][1]); t1 += __uint_as_float(v[k][3]); }
    t0 = sum_rows(t0); t1 = sum_rows(t1);
    if (l < 16) st16(rs, WS_CS + (unsigned)((cj / 7) * 2048 + (cj % 7) * 256) + l16, (u32x4){tag, __float_as_uint(t0), tag, __float_as_uint(t1)});
  };
  const bool cs2 = w == 2 && R >= 20 && R < 27, cs1 = w == 2 && R >= 25;
  u32x4 csv[5];
  typedef const __attribute__((address_space(4))) unsigned* cptr_t;

  if (__builtin_expect(is_conv, 1)) {
    if (w >= 4) {
      TOUCH_AT(1);
      const int pw = w - 4;
      const rsrc_t ra = __builtin_amdgcn_make_buffer_rsrc((void*)((idx & 2) ? p.v : p.k), 0, S_ * D_ * 2, 0x00020000);
      const rsrc_t rb = __builtin_amdgcn_make_buffer_rsrc((void*)((idx & 2) ? p.k : p.v), 0, S_ * D_ * 2, 0x00020000);
      const rsrc_t rk = __builtin_amdgcn_make_buffer_rsrc((void*)p.k, 0, S_ * D_ * 2, 0x00020000);
      const rsrc_t rv = __builtin_amdgcn_make_buffer_rsrc((void*)p.v, 0, S_ * D_ * 2, 0x00020000);
#define NROT 2
      const unsigned vo = (unsigned)((128 * pw + 2 * l) * (D_ * 2));
      const unsigned so0 = (unsigned)((idx & 1) * (D_ * 2) + quad * 8), so1 = (unsigned)((1 - (idx & 1)) * (D_ * 2) + quad * 8);
      u32x2 g[3][4], kh;
      auto gl = [&](int s, int j0, int j1) {
        __builtin_amdgcn_s_setprio(0);
#pragma unroll
        for (int j = j0; j < j1; ++j) {
          if (s < NROT) g[s][j] = __builtin_amdgcn_raw_buffer_load_b64(j < 2 ? ra : rb, vo, ((j & 1) ? so1 : so0) + (unsigned)(s * 512 * D_ * 2), 0);
          else g[s][j] = __builtin_amdgcn_raw_buffer_load_b64(j < 2 ? rk : rv, vo + (unsigned)((j & 1) * D_ * 2), (unsigned)(quad * 8 + s * 512 * D_ * 2), 0);
          SCHED();
        }
        __builtin_amdgcn_s_setprio(3);
      };
      const cptr_t sp = (cptr_t)(p.shift_k + quad * 16);
      unsigned spv[8];
#pragma unroll
      for (int i = 0; i < 8; ++i) spv[i] = sp[i];
      __builtin_amdgcn_s_sleep(3);
      gl(0, 0, 4);
      {
        const int row = 512 * (l >> 2) + 128 * pw - 3 + (l & 3);
        kh = __builtin_amdgcn_raw_buffer_load_b64(rk, (l >= 12 || (l & 3) == 3 || row < 0) ? 0x80000000u : (unsigned)(row * D_ * 2 + quad * 8), 0, 0);
      }
      TAG_READY();
      f32x2 wt[2][4];
#pragma unroll
      for (int d = 0; d < 2; ++d) {
        wt[d][0] = (f32x2){bflo(spv[4 * d]), bflo(spv[4 * d + 2])}; wt[d][1] = (f32x2){bfhi(spv[4 * d]), bfhi(spv[4 * d + 2])};
        wt[d][2] = (f32x2){bflo(spv[4 * d + 1]), bflo(spv[4 * d + 3])}; wt[d][3] = (f32x2){bfhi(spv[4 * d + 1]), bfhi(spv[4 * d + 3])};
        asm volatile("" : "+s"(wt[d][0]), "+s"(wt[d][1]), "+s"(wt[d][2]), "+s"(wt[d][3]));
      }
      SCHED();
      {
        const int j = idx * 4 + pw;
        const unsigned o = (unsigned)((512 + 64 * (j >> 2) + l) * (D_ * 2) + (((160 * xcd) >> 7) + ((j >> 1) & 1)) * 128);
        __builtin_amdgcn_raw_ptr_buffer_load_lds((j & 1) ? ra : rb, (LAS void*)(lds + 98304), 4, j < 64 ? o : 0x80000000u, 0, 0, 0);
      }
      SCHED();
      gl(1, 0, 4);
      STAMP(21);
      unsigned up = (unsigned)(L_U + 512 + 256 * pw + (l >> 2) * 16 + (3 - (l & 3)) * 4);
      unsigned lfo = l == 0 ? (unsigned)(L_FLG + 4 * pw) : (unsigned)(100352 + tid * 4), lfr = (unsigned)(L_FLG + (l & 3) * 4);
      unsigned yr = (unsigned)(L_U + (n16 >> 2) * U_CH + (128 * pw + 32 * h4 + 8 * (n16 & 1) + 4 * ((n16 >> 1) & 1)) * 2);
      unsigned yo = (unsigned)(8 * pw + (l >> 3)) * 20480u + (unsigned)quad * 128u + (unsigned)(l & 7) * 16u;
      unsigned mo = (unsigned)((n16 < 12 ? n16 >> 2 : 0) * D_ + quad * 4 + (l & 3)) * 8u + (unsigned)h4 * 40960u;
      unsigned k3o = (unsigned)(16 * pw + (l >> 2)) * 20480u + (unsigned)(((l >> 1) & 1) * 10240 + (2 * quad + (l & 1)) * 16);
      unsigned k3d = (unsigned)(L_U + (2 * (l & 1) + ((l >> 1) & 1)) * U_CH + 512 + (1536 + 128 * pw + 8 * (l >> 2)) * 2);
      unsigned xfo = (unsigned)(((4 * (l >> 5) + pw) * 4 + ((l >> 3) & 3)) * 160 + quad) * 128u + (unsigned)(l & 7) * 16u;
      u32x4 tg4 = {tag, tag, tag, tag};
      asm volatile("" : "+v"(up), "+v"(lfo), "+v"(lfr), "+v"(yr), "+v"(yo), "+v"(mo), "+v"(k3o), "+v"(k3d), "+v"(xfo), "+v"(tg4));
      unsigned so = WS_ST1 + (unsigned)xcd * 16384u + (unsigned)pw * 1024u;
      asm volatile("" : "+s"(so));
      auto pf_set = [&](int i) { asm volatile("" ::: "memory"); *(volatile LAS unsigned*)(lds + lfo + 4 * i) = tg4[0]; };
      auto pf_wait = [&](int i0) {
        _Pragma("unroll 1") for (unsigned spins = 0; spins < (SPIN_CAP << 4); ++spins) {
          if (__all(*(volatile LAS unsigned*)(lds + lfr + 4 * i0) == tag)) break;
          __builtin_amdgcn_s_sleep(1);
        }
        asm volatile("" ::: "memory");
      };
      unsigned hl[2][3];
      auto halo = [&](int s) {
#pragma unroll
        for (int i = 0; i < 6; ++i) { hl[i & 1][i >> 1] = (unsigned)__builtin_amdgcn_readlane(kh[i & 1], 4 * s + (i >> 1)); asm volatile("" : "+v"(hl[i & 1][i >> 1])); }
      };
      auto kv = [&](auto S__, auto N__) {
        constexpr int s = decltype(S__)::value;
        u32x2* x = g[s];
        SCHED();
        asm volatile("s_waitcnt vmcnt(%5)" : "+v"(x[0]), "+v"(x[1]), "+v"(x[2]), "+v"(x[3]), "+v"(kh) : "n"(decltype(N__)::value));
        if (s == 2) STAMP(27);
        if (s == 0) halo(0);
        if (s < NROT) {
          if (idx & 1) { asm volatile(""); const u32x2 t = x[0], u = x[2]; x[0] = x[1]; x[1] = t; x[2] = x[3]; x[3] = u; }
          asm volatile("" : "+v"(x[0]), "+v"(x[1]), "+v"(x[2]), "+v"(x[3]));
          if (idx & 2) { asm volatile(""); const u32x2 t = x[0], u = x[1]; x[0] = x[2]; x[1] = x[3]; x[2] = t; x[3] = u; }
          asm volatile("" : "+v"(x[0]), "+v"(x[1]), "+v"(x[2]), "+v"(x[3]));
        }
        f32x2 kf[2][5], o[2][2];
#pragma unroll
        for (int d = 0; d < 2; ++d) {
          unsigned kk[5];
          kk[3] = x[0][d]; kk[4] = x[1][d];
          kk[2] = __builtin_amdgcn_update_dpp(hl[d][2], kk[4], 0x138, 0xf, 0xf, false);
          kk[1] = __builtin_amdgcn_update_dpp(hl[d][1], kk[3], 0x138, 0xf, 0xf, false);
          kk[0] = __builtin_amdgcn_update_dpp(hl[d][0], kk[2], 0x138, 0xf, 0xf, false);
#pragma unroll
          for (int j = 0; j < 5; ++j) kf[d][j] = (f32x2){bflo(kk[j]), bfhi(kk[j])};
        }
#pragma unroll
        for (int t = 0; t < 5; ++t) {
#pragma unroll
          for (int i = 0; i < 4; ++i) {
            const int d = i >> 1, r = i & 1;
            if (t == 4) o[d][r] *= (f32x2){bflo(x[2 + r][d]), bfhi(x[2 + r][d])};
            else if (t == 0) o[d][r] = kf[d][r + 3] * wt[d][0];
            else o[d][r] = __builtin_elementwise_fma(kf[d][r + 3 - t], wt[d][t], o[d][r]);
          }
          asm volatile("" : "+v"(o[0][0]), "+v"(o[0][1]), "+v"(o[1][0]), "+v"(o[1][1]));
        }
#pragma unroll
        for (int i = 0; i < 4; ++i) *(unsigned*)(lds + up + i * U_CH + s * 1024) = pack2(o[i >> 1][1][i & 1], o[i >> 1][0][i & 1]);
        pf_set(4 * s);
        SCHED();
        if (s < 2) halo(s + 1);
        SCHED();
      };
      u32x4 sv[4], qv[4];
      u32x2 mv[2];
      auto ld_e = [&]() {
#pragma unroll
        for (int s = 0; s < 4; ++s) sv[s] = __builtin_amdgcn_raw_buffer_load_b128(rs, l16, so + (unsigned)s * 4096u, SC1);
        SCHED();
      };
      auto s_bad = [&]() { return __any(((ne2(sv[0][1], sv[0][3]) | ne2(sv[1][1], sv[1][3]) | ne2(sv[2][1], sv[2][3])) | ne2(sv[3][1], sv[3][3])) != 0u); };
      kv(IC<0>(), IC<5>());
      STAMP(23);
#define G2A 4
      gl(2, 0, G2A);
      kv(IC<1>(), IC<G2A>());
      gl(2, G2A, 4);
      STAMPF(9);
      u32x4 k3[2];
      auto ld_k3 = [&]() {
        k3[0] = __builtin_amdgcn_raw_buffer_load_b128(rs, k3o, WS_KV3, SC1);
        k3[1] = __builtin_amdgcn_raw_buffer_load_b128(rs, k3o, WS_KV3 + 5120u, SC1);
      };
      hold(K_T);
      ld_k3();
      SCHED();
      ld_e();
      kv(IC<2>(), IC<6>());
      STAMPF(11);
      auto k_bad = [&]() { return __any((ne2(k3[0][1], k3[0][3]) | ne2(k3[1][1], k3[1][3])) != 0u); };
      if (k_bad()) {
        _Pragma("unroll 1") for (unsigned spins = 0; spins < SPIN_CAP; ++spins) {
          __builtin_amdgcn_s_sleep(4);
          ld_k3();
          if (!k_bad()) break;
        }
      }
      *(u32x4*)(lds + k3d) = (u32x4){k3[0][0], k3[0][2], k3[1][0], k3[1][2]};
      pf_set(12);
      SCHED();
      STAMPF(18);
      if (s_bad()) {
        _Pragma("unroll 1") for (unsigned spins = 0; spins < SPIN_CAP; ++spins) {
          __builtin_amdgcn_s_sleep(4);
          ld_e();
          if (!s_bad()) break;
        }
      }
      asm volatile("" ::: "memory");
      SCHED();
      STAMPF(17);
      auto ld_m = [&]() {
        mv[0] = __builtin_amdgcn_raw_buffer_load_b64(rs, mo, WS_MODQ, SC1);
        mv[1] = __builtin_amdgcn_raw_buffer_load_b64(rs, mo, WS_MODQ + 20480u, SC1);
      };
#define MFIRST 1
      if (MFIRST) ld_m();
      SCHED();
#pragma unroll
      for (int s = 0; s < 4; ++s) qv[s] = __builtin_amdgcn_raw_buffer_load_b128(rs, yo, WS_QB + (unsigned)s * (32u * 20480u), BULK_AUX);
      SCHED();
      if (!MFIRST) ld_m();
      SCHED();
      STAMP(2);
      u32x2 yv[4][2];
      auto ld_y = [&](int s) {
#pragma unroll
        for (int e = 0; e < 2; ++e) yv[s][e] = trd(lds + yr + 1024 * s + 32 * e);
      };
      float r1[4][2], m1[4][2];
#pragma unroll
      for (int i = 0; i < 8; ++i) {
        const unsigned x = sv[i >> 1][2 * (i & 1)];
        r1[i >> 1][i & 1] = __builtin_amdgcn_rcpf(bfhi(x));
        m1[i >> 1][i & 1] = -bflo(x) * r1[i >> 1][i & 1];
        asm volatile("" : "+v"(r1[i >> 1][i & 1]), "+v"(m1[i >> 1][i & 1]));
      }
      pf_wait(20);
      ld_y(0); ld_y(1);
      SCHED();
      if (__any(ne2(mv[0][0], mv[1][0]) != 0u)) {
        _Pragma("unroll 1") for (unsigned spins = 0; spins < SPIN_CAP; ++spins) {
          __builtin_amdgcn_s_sleep(4);
          ld_m();
          if (!__any(ne2(mv[0][0], mv[1][0]) != 0u)) break;
        }
      }
      STAMPF(13);
      f32x4 al, be;
      {
        const float mgf = rbf(sum_rows(__uint_as_float(mv[0][1]) + __uint_as_float(mv[1][1])));
        auto rl = [&](int i) { return __uint_as_float(__builtin_amdgcn_readlane(__float_as_uint(mgf), i)); };
        const f32x4 sa = {rl(0), rl(1), rl(2), rl(3)}, ca = {rl(4), rl(5), rl(6), rl(7)}, ga = {rl(8), rl(9), rl(10), rl(11)};
        al = rbf4(ca + 1.0f) * ga;
        be = sa * ga;
        asm volatile("" : "+v"(al), "+v"(be));
      }
      f32x4 qf[2][2], P[2][2];
      auto xpre = [&](int s) {
#pragma unroll
        for (int e = 0; e < 2; ++e) {
          const float r = r1[s][e], m = m1[s][e];
          const f32x4 qq = {bflo(qv[s][2 * e]), bfhi(qv[s][2 * e]), bflo(qv[s][2 * e + 1]), bfhi(qv[s][2 * e + 1])};
          qf[s & 1][e] = qq;
          P[s & 1][e] = __builtin_elementwise_fma(__builtin_elementwise_fma(qq, (f32x4){r, r, r, r}, (f32x4){m, m, m, m}), al, be);
        }
        asm volatile("" : "+v"(qf[s & 1][0]), "+v"(qf[s & 1][1]), "+v"(P[s & 1][0]), "+v"(P[s & 1][1]));
      };
      auto xfin = [&](int s) {
        u32x4 o;
#pragma unroll
        for (int e = 0; e < 2; ++e) {
          const u32x2 y = yv[s][e];
          const f32x4 yf = {bflo(y[0]), bfhi(y[0]), bflo(y[1]), bfhi(y[1])};
          const f32x4 x = __builtin_elementwise_fma(P[s & 1][e], yf, qf[s & 1][e]);
          o[2 * e] = pack2(x[0], x[1]);
          o[2 * e + 1] = pack2(x[2], x[3]);
        }
        __builtin_amdgcn_raw_buffer_store_b128(o, rs, yo, WS_X1B + (unsigned)s * (32u * 20480u), SC1);
      };
      xpre(0); xfin(0);
      xpre(1); xfin(1);
      SCHED();
      xpre(2); xpre(3);
#define FVR 5
#define MSPLIT 0
      if (MSPLIT) {
        pf_wait(24);
        ld_y(2);
        xfin(2);
        SCHED();
        pf_wait(28);
        ld_y(3);
        xfin(3);
      } else {
        pf_wait(28);
        ld_y(2); ld_y(3);
        xfin(2); xfin(3);
      }
      STAMP(28);
      asm volatile("s_waitcnt vmcnt(2)" ::: "memory");
      __builtin_amdgcn_raw_buffer_store_b128(tg4, rs, xfo, WS_FX, SC1);
      SCHED();
      asm volatile("s_waitcnt vmcnt(1)" ::: "memory");
      __builtin_amdgcn_raw_buffer_store_b128(tg4, rs, xfo, WS_FX + 32u * 160u * 128u, SC1);
      STAMP(5);
    } else {
      const int ch = w;
      char* U = lds + L_U + ch * U_CH;
      char* G = lds + ch * (4 * G_COPY);
      {
        const rsrc_t rf = __builtin_amdgcn_make_buffer_rsrc((void*)(p.ssm_k + (size_t)(quad * 4 + ch) * S_), 0, S_ * 2, 0x00020000);
        u32x4 fa[4];
        u32x2 fb[4];
        __builtin_amdgcn_s_setprio(3);
#pragma unroll
        for (int k = 0; k < 4; ++k) {
          fa[k] = __builtin_amdgcn_raw_buffer_load_b128(rf, l16 + (unsigned)k * 1024u, 0, NT);
        }
        __builtin_amdgcn_s_setprio(1);
        TAG_READY();
#pragma unroll
        for (int k = 0; k < 4; ++k) {
#pragma unroll
          for (int d = 0; d < 2; ++d)
            fb[k][d] = __builtin_amdgcn_update_dpp(k < 3 ? (unsigned)__builtin_amdgcn_readfirstlane(fa[k < 3 ? k + 1 : k][d]) : 0u, fa[k][d], 0x130, 0xf, 0xf, false);
          const unsigned x[6] = {fa[k][0], fa[k][1], fa[k][2], fa[k][3], fb[k][0], fb[k][1]};
          unsigned y[5];
#pragma unroll
          for (int d = 0; d < 5; ++d) y[d] = __builtin_amdgcn_alignbit(x[d + 1], x[d], 16);
          char* gp = G + 32 + k * 1024 + l16;
          *(u32x4*)(gp) = fa[k];
          *(u32x4*)(gp + G_COPY) = (u32x4){y[0], y[1], y[2], y[3]};
          *(u32x4*)(gp + 2 * G_COPY) = (u32x4){x[1], x[2], x[3], x[4]};
          *(u32x4*)(gp + 3 * G_COPY) = (u32x4){y[1], y[2], y[3], y[4]};
          if (k == 0) {
            const unsigned f0 = __builtin_amdgcn_readfirstlane(x[0]), f1 = __builtin_amdgcn_readfirstlane(x[1]);
            const unsigned long long f64 = (unsigned long long)f0 | ((unsigned long long)f1 << 32);
            const unsigned long long hi = l ? (f64 << (64 - 16 * (l & 3))) : 0ull;
            if (l < 4) {
              *(u32x4*)(G + l * G_COPY) = (u32x4){0u, 0u, 0u, 0u};
              *(u32x4*)(G + l * G_COPY + 16) = (u32x4){0u, 0u, (unsigned)hi, (unsigned)(hi >> 32)};
            }
            if (l >= 32) *(u32x4*)(U + (l - 32) * 16) = (u32x4){0u, 0u, 0u, 0u};
          }
        }
        asm volatile("s_waitcnt lgkmcnt(0)" ::: "memory");
        STAMP(1);
      }
      const int a = n16, h = h4, jj = h >> 1, e2 = h & 1;
      const char* ap = G + ((a + 1) & 3) * G_COPY + 2 * (16 * jj + a - 8 * e2 + 9 - ((a + 1) & 3));
      const char* bp = U + 512 + 32 * (a - jj) + 16 * e2;
      auto lda = [&](u32x4* A, int r, int p0, int np) {
#pragma unroll
        for (int pp = 0; pp < np; ++pp) {
          u32x2 lo = *(const volatile LAS u32x2*)(ap + 512 * (p0 + pp) + 64 * r);
          u32x2 hi = *(const volatile LAS u32x2*)(ap + 512 * (p0 + pp) + 64 * r + 8);
          A[pp] = (u32x4){lo[0], lo[1], hi[0], hi[1]};
        }
      };
      u32x4 AD;
      {
        const unsigned dDr = (*(cptr_t)(p.ssm_D + quad * 4 + (ch & 2)) >> (16 * (ch & 1))) & 0xffffu;
        const unsigned dv = h == (a >> 3) ? ((a & 1) ? dDr : dDr << 16) : 0u;
        const int dw = (7 - (a & 7)) >> 1;
#pragma unroll
        for (int d = 0; d < 4; ++d) AD[d] = dw == d ? dv : 0u;
      }
      u32x4 A0[8][4];
      f32x4 acc[8];
#pragma unroll
      for (int n = 0; n < 8; ++n) acc[n] = (f32x4){0.f, 0.f, 0.f, 0.f};
#pragma unroll
      for (int r = 0; r < 8; ++r) lda(A0[r], r, 0, 4);
      auto yput = [&](int s) {
#pragma unroll
        for (int e = 0; e < 2; ++e)
          *(u32x2*)(U + 1024 * s + 512 * e + (16 * a + 4 * h) * 2) = (u32x2){pack2(acc[2 * s + e][0], acc[2 * s + e][1]), pack2(acc[2 * s + e][2], acc[2 * s + e][3])};
        asm volatile("" ::: "memory");
        if (l == 0) lf[16 + 4 * s + ch] = tag;
      };
      auto stage = [&](auto S__) {
        constexpr int s = decltype(S__)::value, NH = s < 2 ? 4 - 2 * s : 0, NP = 8 - 2 * s, NB = s < 2 ? 2 : 8;
        u32x4 A1[3][NH > 0 ? NH : 1], B[NB][2];
        auto ldb = [&](int r) {
#pragma unroll
          for (int e = 0; e < 2; ++e) B[r % NB][e] = *(const u32x4*)(bp + 512 * (2 * s + e) - 64 * r);
        };
        if (NH) { lda(A1[0], 0, 4, NH); lda(A1[1], 1, 4, NH); }
        SCHED();
        if (s > 0) yput(s - 1);
        lf_wait(4 * s, 4);
        if (s == 0) STAMP(3);
#pragma unroll
        for (int r = 0; r < NB; ++r) ldb(r);
        SCHED();
#pragma unroll
        for (int e = 0; e < 2; ++e) acc[2 * s + e] = MFMA16(AD, B[0][e], acc[2 * s + e]);
#pragma unroll
        for (int r = 0; r < 8; ++r) {
          if (NH && r + 2 < 8) lda(A1[(r + 2) % 3], r + 2, 4, NH);
          SCHED();
#pragma unroll
          for (int e = 0; e < 2; ++e)
#pragma unroll
            for (int pp = 0; pp < NP - e; ++pp) acc[2 * s + e + pp] = MFMA16(pp < 4 ? A0[r][pp] : A1[r % 3][pp < 4 ? 0 : pp - 4], B[r % NB][e], acc[2 * s + e + pp]);
          SCHED();
          if (r + NB < 8) ldb(r + NB);
        }
      };
      stage(IC<0>());
      STAMP(4);
      stage(IC<1>());
      STAMPF(9);
      const unsigned fo[1] = {fwi + (unsigned)((8 + c) * 20 + 5 * w + (l < 5 ? l : 0)) * 128u};
      unsigned fv = ~tag;
      if (MSPLIT) {
        u32x4 B[8][2];
        auto ldb = [&](int r, int m0) {
#pragma unroll
          for (int m = 0; m < 2; ++m) B[r][m] = *(const u32x4*)(bp + 512 * (m0 + m) - 64 * r);
        };
        yput(1);
        if (w == 3) wo_ld();
        __builtin_amdgcn_s_setprio(3);
        lf_wait(8, 4);
        STAMP(22);
#pragma unroll
        for (int r = 0; r < 8; ++r) ldb(r, 4);
        SCHED();
#pragma unroll
        for (int m = 0; m < 2; ++m) acc[4 + m] = MFMA16(AD, B[0][m], acc[4 + m]);
#pragma unroll
        for (int r = 0; r < 8; ++r) {
          acc[4] = MFMA16(A0[r][0], B[r][0], acc[4]);
          acc[5] = MFMA16(A0[r][1], B[r][0], acc[5]);
          acc[5] = MFMA16(A0[r][0], B[r][1], acc[5]);
          SCHED();
        }
#pragma unroll
        for (int r = 0; r < 8; ++r) {
          acc[6] = MFMA16(A0[r][2], B[r][0], acc[6]);
          acc[7] = MFMA16(A0[r][3], B[r][0], acc[7]);
          acc[6] = MFMA16(A0[r][1], B[r][1], acc[6]);
          acc[7] = MFMA16(A0[r][2], B[r][1], acc[7]);
          SCHED();
          if (r == 0) yput(2);
          if (r == FVR) fv = ld4(rs, fo[0]);
          if (r == 3) {
            lf_wait(12, 4);
#pragma unroll
            for (int x = 0; x < 4; ++x) ldb(x, 6);
          }
          if (r > 3) ldb(r, 6);
          SCHED();
        }
#pragma unroll
        for (int m = 0; m < 2; ++m) acc[6 + m] = MFMA16(AD, B[0][m], acc[6 + m]);
#pragma unroll
        for (int r = 0; r < 8; ++r) {
          acc[6] = MFMA16(A0[r][0], B[r][0], acc[6]);
          acc[7] = MFMA16(A0[r][1], B[r][0], acc[7]);
          acc[7] = MFMA16(A0[r][0], B[r][1], acc[7]);
          SCHED();
        }
        yput(3);
      } else {
        u32x4 B[4][4];
        auto ldb = [&](int r) {
#pragma unroll
          for (int m = 0; m < 4; ++m) B[r & 3][m] = *(const u32x4*)(bp + 512 * (4 + m) - 64 * r);
        };
        yput(1);
        if (w == 3) wo_ld();
        __builtin_amdgcn_s_setprio(3);
        lf_wait(8, 8);
        STAMP(22);
#pragma unroll
        for (int r = 0; r < 4; ++r) ldb(r);
        SCHED();
#pragma unroll
        for (int m = 0; m < 4; ++m) acc[4 + m] = MFMA16(AD, B[0][m], acc[4 + m]);
#pragma unroll
        for (int r = 0; r < 8; ++r) {
#pragma unroll
          for (int m = 0; m < 4; ++m)
#pragma unroll
            for (int pp = 0; pp < 4 - m; ++pp) acc[4 + m + pp] = MFMA16(A0[r][pp], B[r & 3][m], acc[4 + m + pp]);
          SCHED();
          if (r + 4 < 8) ldb(r + 4);
          if (r == 1) fv = ld4(rs, fo[0]);
          SCHED();
        }
#pragma unroll
        for (int e = 0; e < 4; ++e)
          *(u32x2*)(U + 2048 + 512 * e + (16 * a + 4 * h) * 2) = (u32x2){pack2(acc[4 + e][0], acc[4 + e][1]), pack2(acc[4 + e][2], acc[4 + e][3])};
        lf_set(28 + ch);
      }
      STAMP(28);
      __builtin_amdgcn_s_setprio(0);
      if (!__all(fv == tag)) wait_lines<1, 4>(rs, fo, tag);
      lf_wait(20, 4);
    }
    __builtin_amdgcn_s_setprio(0);
  } else {
    const int at = idx - 20;
    char* const scr = lds + WBLK + 14848;
    auto gv_blk = [&](int j) { return j < 160 ? 30 + (j >> 3) : j < 400 ? (j >> 3) - 20 : j >> 3; };
    auto gv_fin = [&](const u32x4* pre, int j) {
      float acc[8];
      gemv_fin(acc, pre, 10, scr);
      gemv_put(acc, j < 160 ? WS_MODP + (unsigned)((j & 7) * 1280 + (j >> 3) * 64) * 8u : WS_MODQ + (unsigned)((j & 7) * 2560 + ((j >> 3) - 20) * 64) * 8u);
    };
    const int ja = xcd * 12 + at, ai = xcd * 8 + at, bi = xcd * 4 + at - 8, g = w >> 2, wj = w & 3;
    const int rj = w == 0 ? ja : w == 1 ? (at >= 4 ? 92 + ai : -1) : w == 4 ? 160 + ja : w == 5 && at < 8 ? 256 + ai : -1;
    char* const rst = lds + (g ? WBLK : 65536) + wj * 7424;
    u32x4 pre[11];
    if (at >= 8 || g == 0) {
      TOUCH_AT(2);
      const int T = at >= 8 ? 64 + 2 * bi + g : ai;
      unsigned* mds = (unsigned*)(lds + 2048) + 16 * g;
      char* qt = lds + L_RT + g * (16 * 1312);
      u32x4 ld[5];
      const bool e1 = at >= 8 && w == 3;
      if (e1) gemv_load(pre, gv_blk(128 + bi), bi & 7, 10);
      else __builtin_amdgcn_s_sleep(8);
#pragma unroll
      for (int j = 0; j < 5; ++j)
        ld[j] = __builtin_amdgcn_raw_buffer_load_b128(rq, (unsigned)(h4 * D_ * 2 + n16 * 16 + j * 256), (unsigned)((32 * (T >> 1) + 8 * wj + 4 * (T & 1)) * D_ * 2), NT);
      TAG_READY();
      {
        const bf2_t ONE2 = {(__bf16)1.0f, (__bf16)1.0f};
        char* dst = qt + (4 * wj + h4) * 1312 + n16 * 16;
        float sa = 0.f, va = 0.f;
#pragma unroll
        for (int j = 0; j < 5; ++j) {
          *(u32x4*)(dst + j * 256) = ld[j];
#pragma unroll
          for (int d = 0; d < 4; ++d) {
            const unsigned a0 = ld[j][d];
            const bf2_t xa = __builtin_bit_cast(bf2_t, a0);
            sa = __builtin_amdgcn_fdot2_f32_bf16(xa, ONE2, sa, false); va = __builtin_amdgcn_fdot2_f32_bf16(xa, xa, va, false);
          }
        }
        lf_set(8 + w);
        const float ma = sum16(sa) * (1.0f / D_);
        va = __builtin_fmaxf(sum16(va) * (1.0f / D_) - ma * ma, 0.f);
        if (n16 == 0) mds[4 * wj + h4] = pack2(ma, __builtin_amdgcn_sqrtf(rbf(rbf(va) + 1e-5f)));
      }
      if (e1) gv_fin(pre, 128 + bi);
      lf_wait(8 + 4 * g, 4);
      STAMP(23);
      {
        const char* src = qt + (l & 7) * 1312 + (40 * wj + (l >> 3)) * 8;
#pragma unroll
        for (int j = 0; j < 5; ++j) {
          const u32x2 x = *(const u32x2*)(src + j * 64), y = *(const u32x2*)(src + j * 64 + 8 * 1312);
          st16(rs, WS_QB + (unsigned)T * 20480u + (unsigned)(5 * wj + j) * 1024u + l16, (u32x4){x[0], x[1], y[0], y[1]});
        }
      }
      SCHED();
      const int g2 = at < 8 || w < 3 || w == 4 ? 400 : w == 3 ? 384 + bi : 256 + wj * 32 + bi;
      if (g2 < 400) {
        gemv_load(pre, gv_blk(g2), g2 & 7, 10);
        asm volatile("s_waitcnt vmcnt(10)" ::: "memory");
      } else if (w < 2 && rj >= 0) {
        rt_ld(rj, rst);
        asm volatile("s_waitcnt vmcnt(7)" ::: "memory");
      } else {
        DRAIN();
      }
      lf_set(16 + w);
      if (wj == 2) {
        lf_wait(16 + 4 * g, 4);
        st16(rs, WS_ST1 + (unsigned)(l >> 3) * 16384u + (unsigned)T * 128u + (unsigned)(l & 7) * 16u, (u32x4){mds[l & 7], tag, mds[8 + (l & 7)], tag});
        STAMP(22);
      }
      if (w >= 4 && w < 7) lf_set(w);
      if (g2 < 400) gv_fin(pre, g2);
      STAMP(25);
    } else {
      constexpr int KB = L_RT + 16 * 1312;
      TOUCH_AT(3);
      const int g1 = w < 6 ? 2 * ai + wj : 32 + wj * 64 + ai;
      if (w >= 6) {
        const rsrc_t rk = __builtin_amdgcn_make_buffer_rsrc((void*)(w == 6 ? p.k : p.v), 0, S_ * D_ * 2, 0x00020000);
        const unsigned ko = (unsigned)((1536 + 8 * ai) * (D_ * 2) - (w == 6 ? 4096 : 0));
        char* dst = lds + KB + (w == 6 ? 0 : 14336);
        __builtin_amdgcn_s_sleep(3);
#pragma unroll
        for (int n = 0; n < 10; ++n) __builtin_amdgcn_raw_ptr_buffer_load_lds(rk, (LAS void*)(dst + n * 1024), 16, l16, ko + (unsigned)n * 1024u, 0, NT);
        if (w == 6) {
#pragma unroll
          for (int n = 10; n < 14; ++n) __builtin_amdgcn_raw_ptr_buffer_load_lds(rk, (LAS void*)(dst + n * 1024), 16, l16, ko + (unsigned)n * 1024u, 0, NT);
        }
      }
      SCHED();
      u32x4 swt[2];
      swt[0] = *(const u32x4*)(p.shift_k + (tid - 256) * 8);
      swt[1] = *(const u32x4*)(p.shift_k + (256 + l) * 8);
      SCHED();
      if (w >= 6) __builtin_amdgcn_s_sleep(8);
      gemv_load(pre, gv_blk(g1), g1 & 7, 10);
      TAG_READY();
      asm volatile("s_waitcnt vmcnt(11)" : "+v"(swt[0]), "+v"(swt[1]) :: "memory");
      lf_set(8 + w);
      if (w < 6) gv_fin(pre, g1);
      STAMP(25);
      lf_wait(12, 4);
      STAMP(23);
#pragma unroll 1
      for (int n = 0; n < (w == 7 ? 2 : 1); ++n) {
        {
          const int pr = n ? 256 + l : tid - 256;
          const char* kp = lds + KB + 4096 + pr * 4;
          unsigned kk[11], vv[8];
#pragma unroll
          for (int i = 0; i < 11; ++i) kk[i] = *(const unsigned*)(kp + (i - 3) * 1280);
#pragma unroll
          for (int i = 0; i < 8; ++i) vv[i] = *(const unsigned*)(kp + 10240 + i * 1280);
          const u32x4 sw = n ? swt[1] : swt[0];
          const f32x2 wt[4] = {{bflo(sw[0]), bflo(sw[2])}, {bfhi(sw[0]), bfhi(sw[2])}, {bflo(sw[1]), bflo(sw[3])}, {bfhi(sw[1]), bfhi(sw[3])}};
          f32x2 o[8];
#pragma unroll
          for (int i = 0; i < 8; ++i) {
            f32x2 t = (f32x2){bflo(kk[i + 3]), bfhi(kk[i + 3])} * wt[0];
#pragma unroll
            for (int j = 1; j < 4; ++j) t = __builtin_elementwise_fma((f32x2){bflo(kk[i + 3 - j]), bfhi(kk[i + 3 - j])}, wt[j], t);
            o[i] = t * (f32x2){bflo(vv[i]), bfhi(vv[i])};
          }
#pragma unroll
          for (int e = 0; e < 4; ++e) {
            const int i = 7 - 4 * (e & 1);
            st16(rs, WS_KV3 + (unsigned)ai * 20480u + (unsigned)(e * 5120 + pr * 16),
                 (u32x4){pack2(o[i][e >> 1], o[i - 1][e >> 1]), tag, pack2(o[i - 2][e >> 1], o[i - 3][e >> 1]), tag});
          }
        }
      }
      STAMP(22);
      if (w < 7) lf_set(w);
      if (w >= 6) gv_fin(pre, g1);
      STAMPF(30);
    }
    if (w == 6 && ja < 80) {
      hold(AUX_T);
      gemv_load(pre, gv_blk(400 + ja), ja & 7, 10);
      gv_fin(pre, 400 + ja);
    }
    if (rj >= 0) {
      if (w >= 2) { hold(RT2_T); rt_ld(rj, rst); }
      rt_job(rj, rst);
    }
    STAMP(26);
    lf_set(w);
    if (w == 3) { hold(WA_T); wo_ld(); }
    STAMP(3);
    if (w < 4) {
      {
        const unsigned fo[1] = {l < 5 ? fwi + (unsigned)((8 + c) * 20 + 5 * w + l) * 128u : ~0u};
        hold(W_T);
        wait_lines<1, 8>(rs, fo, tag);
      }
      lf_wait(0, 8);
      STAMP(4);
    }
  }

  STAMPF(16);
  u32x4 wox[5];
  if (w == 3) {
    DRAIN();
    const char* bp = wo_st + (4 * h4 + (n16 >> 2)) * 160 + (n16 & 3) * 8;
#pragma unroll
    for (int dj = 0; dj < 5; ++dj) wox[dj] = trfrag(bp + dj * 32, 160);
    asm volatile("s_waitcnt lgkmcnt(0)" : "+v"(wox[0]), "+v"(wox[1]), "+v"(wox[2]), "+v"(wox[3]), "+v"(wox[4]) :: "memory");
  }
  auto wo_put = [&]() {
    if (w == 3 && R < 28) {
      const unsigned base = WS_WOUTF + (unsigned)c * WBLK + (unsigned)((R >> 1) * 10240 + (R & 1) * 1024);
#pragma unroll
      for (int dj = 0; dj < 5; ++dj) st16(rs, base + (unsigned)((R & 1) ? dj : (dj + 2) % 5) * 2048u + l16, wox[dj]);
    }
  };
  if (cs2) {
    cs_ld(csv, (8 + c) * 7 + R - 20);
    SCHED();
    load_b2(0, 12);
    SCHED();
    cs_fin(csv, (8 + c) * 7 + R - 20);
  } else if (w < 4) {
    asm volatile("" ::: "memory");
    load_b2(0, 12);
  }
  if (w < 4) load_b2(12, 35);
  wo_put();
  STAMPF(29);
  u32x4 k_c;
  unsigned kc_o = w < 2 ? (unsigned)(tid < 112 ? tid : 0) * 16u : 0x80000000u, kc_d = (unsigned)(L_CONST + (tid & 127) * 8);
  auto kc_ld = [&](int z2) { k_c = __builtin_amdgcn_raw_buffer_load_b128(rs, kc_o, WS_CS + (unsigned)(8 * z2 + c) * 2048u, SC1); };
  auto consts_put = [&](int z2) {
    if (w < 2) {
      if (__any(ne2(k_c[0], k_c[2]) != 0u)) {
        _Pragma("unroll 1") for (unsigned spins = 0; spins < SPIN_CAP; ++spins) {
          __builtin_amdgcn_s_sleep(8);
          kc_ld(z2);
          if (!__any(ne2(k_c[0], k_c[2]) != 0u)) break;
        }
      }
      if (z2) *(u32x2*)(lds + kc_d + 1024) = (u32x2){k_c[1], k_c[3]};
      else { const unsigned d = kc_d - ((kc_d >> 1) & 4u); *(unsigned*)(lds + d) = k_c[1]; *(unsigned*)(lds + d + 8) = k_c[3]; }
    }
  };
  const int wq = w ^ rotr, rt1 = wq & 3, kh1 = w >> 2;
  unsigned bo0 = (unsigned)(kh1 * 71680) + l16, bo1 = bo0 + 35840u;
  asm volatile("" : "+v"(bo0), "+v"(bo1));
  unsigned pso[3], pqo[3], oto = (unsigned)(((w ^ 4) * 7 * 64 + l) * 8), rdo = (unsigned)((w * 7 * 64 + l) * 8), cso = (unsigned)(L_CONST + (128 + n16) * 8);
#pragma unroll
  for (int e = 0; e < 3; ++e) {
    const int row = rt1 * 16 + (e < 2 ? 4 * h4 + 2 * kh1 + e : n16);
    pso[e] = (unsigned)(L_PST + row * 4); pqo[e] = (unsigned)(L_PST + 512 + row * 16 + (row & 3) * 4);
  }
  unsigned spo = (unsigned)(L_Z + tid * 16), p6o = (unsigned)(L_PST + (wq * 16 + 4 * h4) * 4), pgo = (unsigned)(L_PST + 512 + (wq * 16 + n16) * 16);
  asm volatile("" : "+v"(spo), "+v"(p6o), "+v"(pgo));
  p6o = n16 == 12 ? p6o : spo;
  pgo = h4 == (n16 >> 2) ? pgo : spo;
  unsigned bsrc = (unsigned)(((l >> 1) & 3) * 64 + (l & 1) * 4), ppo = l < 8 ? l16 : 0x80000000u;
  asm volatile("" : "+v"(pso[0]), "+v"(pso[2]), "+v"(pqo[0]), "+v"(pqo[2]), "+v"(oto), "+v"(rdo), "+v"(cso), "+v"(p6o), "+v"(pgo), "+v"(bsrc), "+v"(ppo), "+v"(kc_o), "+v"(kc_d));
  pso[1] = pso[0] + 4u; pqo[1] = pqo[0] + 20u;
#define BFRAG(q, j) (*(const u32x4*)(lds + ((q) < 5 ? bo0 : bo1) + ((q) % 5) * 7168 + (j) * 1024))
  u32x4 h2[10];
  unsigned xv = (unsigned)((4 * ((l >> 3) & 1) + h4) * 128 + (l & 7) * 16), xs0 = WS_X1B + (unsigned)(R * 4 + rt1) * 20480u + (unsigned)(10 * kh1) * 1024u;
  unsigned wvo = wsp_vo, bso = (unsigned)tau(rt1, n16) * 2u, fgo = l < 32 ? (unsigned)(l >> 3) * F4S + (unsigned)(l & 7) * 16u : 0x80000000u;
  asm volatile("" : "+v"(xv), "+v"(wvo), "+v"(bso), "+v"(fgo));
#define WO3 (-92)
  unsigned zs[8];
#pragma unroll
  for (int t = 0; t < 8; ++t) zs[t] = WS_Z2F + (unsigned)(c * 64 + 8 * w + 4 * ((t >> 2) ^ rotg) + ((t + rotb) & 3)) * 7168u;
  unsigned wx8 = (unsigned)((w + (idx >> 2)) & 7) * 1024u, wx4 = wx8 & 3072u, wgb = WS_WOUTF + (unsigned)c * WBLK, fzs = fz + (unsigned)(c * 32 + 4 * w) * 128u;
  unsigned uo = WS_UF + (unsigned)((R * 4 + rt1) * 8 + c) * 4096u + (unsigned)kh1 * 2048u, ui = WS_UF + (unsigned)((R * 4 + rt1) * 8 + 4 * kh1) * 4096u;
  unsigned xs = WS_X1B + (unsigned)(R * 4 + rt1) * 20480u + (unsigned)(5 * c + 2 * kh1) * 512u, wl2 = (unsigned)(120 + WO3 + (w < 4 ? w + 16 : w)) * 1024u;
  unsigned os = (unsigned)__builtin_amdgcn_readfirstlane(((R * 64 + tau(rt1, 0)) * D_ + 80 * c + 32 * kh1) * 2);
#define PINS(x) do { x = (unsigned)__builtin_amdgcn_readfirstlane((int)x); asm volatile("" : "+s"(x)); } while (0)
#pragma unroll
  for (int t = 0; t < 8; ++t) PINS(zs[t]);
  PINS(xs0); PINS(wx8); PINS(wx4); PINS(wgb); PINS(fzs); PINS(uo); PINS(ui); PINS(xs); PINS(wl2); PINS(os);
  unsigned s1 = (unsigned)((w ^ 4) * 3072 + 1024), s3 = (unsigned)(w * 3072 + 1024);
  PINS(s1); PINS(s3);
#define LDX(q) h2[q] = __builtin_amdgcn_raw_buffer_load_b128(rs, xv + (unsigned)((q) & 3) * 1024u, xs0 + (unsigned)((q) >> 2) * 4096u, BULK_AUX)
  auto flag4 = [&](unsigned off) { __builtin_amdgcn_raw_buffer_store_b128((u32x4){tag, tag, tag, tag}, rs, fgo, WS_F4 - WS_FWI + off, SC1); };
  unsigned bs_raw;
  f32x4 acc[7];
  float mu_n, rs_n;
  {
    unsigned f1o[1] = {w == 1 && l < 20 ? fwi + (unsigned)(c * 20 + l) * 128u : ~0u}, f1v;
    asm volatile("" : "+v"(f1o[0]));
    if (w == 7) {
      const unsigned fb = WS_FX + (unsigned)(((R >> 1) * 4 + (R & 1) * 2 + (c >> 2)) * 160 + l) * 128u;
      const unsigned fo[3] = {fb, fb + 8192u, l < 32 ? fb + 16384u : ~0u};
      hold(is_conv ? YC_T : Y_T);
      wait_lines<3, YSL>(rs, fo, tag);
      if (l == 0) lf[33] = tag;
    }
    lf_wait(33, 1);
    STAMP(7);
    SCHED();
#pragma unroll
    for (int q = 0; q < 3; ++q) LDX(q);
    SCHED();
    __builtin_amdgcn_s_waitcnt(0x0F73);
    BAR();
    STAMP(8);
#pragma unroll
    for (int j = 0; j < 7; ++j) acc[j] = (f32x4){0.f, 0.f, 0.f, 0.f};
    f32x4 accg = {0.f, 0.f, 0.f, 0.f};
    u32x4 bf[7];
#pragma unroll
    for (int j = 0; j < 7; ++j) bf[j] = BFRAG(0, j);
    SCHED();
#pragma unroll
    for (int q = 3; q < 6; ++q) LDX(q);
    if (w == 3) flag4(WS_FWO + (unsigned)(c * 32 + R) * 128u);
    SCHED();
    auto xswap = [&](int q, int e) {
#pragma unroll
      for (int d = 0; d < 2; ++d) {
        if (e == 1) h2[q][2 + d] ^= (unsigned)__builtin_amdgcn_update_dpp(0, (int)h2[q][d], 0x128, 0xf, 0x3, false);
        else h2[q][d] ^= (unsigned)__builtin_amdgcn_update_dpp(0, (int)h2[q][2 + d], 0x128, 0xf, 0xC, false);
      }
      asm volatile("" : "+v"(h2[q]));
    };
#define XSW 1
#pragma unroll
    for (int q = 0; q < 10; ++q) {
      if (q < XSW) { xswap(q, 0); xswap(q, 1); xswap(q, 2); }
      if (q == 5 && w == 1 && __builtin_amdgcn_ballot_w64(f1v == tag) != 0xfffffull) wait_lines<1, 4>(rs, f1o, tag);
      if (q == 5) { __builtin_amdgcn_s_barrier(); asm volatile("" ::: "memory"); }
      SCHED();
      accg = MFMA16(h2[q], h2[q], accg);
      SCHED();
#pragma unroll
      for (int j = 0; j < 7; ++j) {
        acc[j] = MFMA16(h2[q], bf[j], acc[j]);
        if (q == 9 && j == 1) *(f32x4*)(lds + pgo) = accg;
        if (q < 9) bf[j] = BFRAG(q + 1, j);
        if (q < 9 && q + 1 >= XSW && j >= 3 && j < 6) xswap(q + 1, j - 3);
        if (q == 1 && j == 6) f1v = ld4(rs, f1o[0]);
        if (q == 2 && j == 0) bs_raw = __builtin_amdgcn_raw_buffer_load_b16(__builtin_amdgcn_make_buffer_rsrc((void*)p.b_sp, 0, S_ * 2, 0x00020000), bso, (unsigned)R * 128u, 0);
#define KCQ 4
#define WA 8
#define WB 12
#define WQ0 2
        if (WQ0 == 2 && q >= 2 && j == 2) LDW(q - 2);
        if (WQ0 == 6 && q >= 6 && (j == 2 || j == 4)) LDW(2 * (q - 6) + (j >> 2));
        if (WQ0 == 3 && q >= 2 && q < 5 && (j & 1) && 3 * (q - 2) + (j >> 1) < WA) LDW(3 * (q - 2) + (j >> 1));
        if (WQ0 == 4 && q >= 2 && !(q & 1) && j == 2) LDW((q - 2) >> 1);
        if ((j == 0 || j == 4) && 6 + 2 * q + (j >> 2) < 10) LDX(6 + 2 * q + (j >> 2));
        if (q == KCQ && j == 0) kc_ld(1);
        if (q == 2 && j == 1 && cs1) cs_ld(csv, c * 7 + R - 25);
        if (q >= 5 && j == 5) {
          const int i = (5 + q) * 7 + (w < 7 ? w : 0);
          dma1k(rs, lds + i * 1024, l16, WS_WINF + (unsigned)c * WBLK + (unsigned)i * 1024u);
          if (q == 9) { const int i2 = 28 + (w < 7 ? w : 0); dma1k(rs, lds + i2 * 1024, l16, WS_WINF + (unsigned)c * WBLK + (unsigned)i2 * 1024u); }
        }
        SCHED();
      }
    }
    STAMP(31);
  }
  auto gelu_n = [&](f32x2* x, auto N_) {
    constexpr int N = decltype(N_)::value;
    const float c0 = -2.0f * 0.7978845608028654f * 1.4426950408889634f, c1 = c0 * 0.044715f;
    f32x2 k0 = {c0, c0}, k1 = {c1, c1}, k2 = {1.0f, 1.0f};
    asm volatile("" : "+v"(k0), "+v"(k1), "+v"(k2));
    f32x2 t[N];
#define GST(e) do { _Pragma("unroll") for (int i = 0; i < N; ++i) { t[i] = (e); asm volatile("" : "+v"(t[i])); } } while (0)
    GST(x[i] * x[i]);
    GST(__builtin_elementwise_fma(t[i], k1, k0));
    GST(t[i] * x[i]);
    GST(((f32x2){__builtin_amdgcn_exp2f(t[i][0]), __builtin_amdgcn_exp2f(t[i][1])}));
    GST(t[i] + k2);
    GST(((f32x2){__builtin_amdgcn_rcpf(t[i][0]), __builtin_amdgcn_rcpf(t[i][1])}));
#pragma unroll
    for (int i = 0; i < N; ++i) { x[i] *= t[i]; asm volatile("" : "+v"(x[i])); }
  };
  {
    f32x2 mine[7];
    f32x2 csb[7];
    if (kh1) {
#pragma unroll
      for (int j = 0; j < 7; ++j) { *(f32x2*)(lds + rdo + j * 512) = (f32x2){acc[j][0], acc[j][1]}; mine[j] = (f32x2){acc[j][2], acc[j][3]}; }
      *(f32x4*)(lds + p6o) = acc[6];
    } else {
#pragma unroll
      for (int j = 0; j < 7; ++j) { *(f32x2*)(lds + rdo + j * 512) = (f32x2){acc[j][2], acc[j][3]}; mine[j] = (f32x2){acc[j][0], acc[j][1]}; }
      *(f32x4*)(lds + p6o) = acc[6];
      consts_put(1);
      if (cs1) cs_fin(csv, c * 7 + R - 25);
#pragma unroll
      for (int k = WA; k < WB; ++k) LDW(k);
    }
    BAR();
    if (kh1) {
#pragma unroll
      for (int k = WA; k < WB; ++k) LDW(k);
    }
    __builtin_amdgcn_s_setprio(3);
    STAMPT(9);
    f32x2 oth[7];
    f32x2 pa[3], pb[3];
#pragma unroll
    for (int e = 0; e < 3; ++e) {
      pa[e] = (f32x2){*(const float*)(lds + pso[e]), *(const float*)(lds + pqo[e])};
      pb[e] = (f32x2){*(const float*)(lds + pso[e] + 256), *(const float*)(lds + pqo[e] + 1024)};
    }
#pragma unroll
    for (int j = 0; j < 7; ++j) { oth[j] = *(const f32x2*)(lds + oto + j * 512); csb[j] = *(const f32x2*)(lds + cso + 128 * j); }
    SCHED();
    float mu2[2], rs2[2];
#pragma unroll
    for (int e = 0; e < 3; ++e) {
      const float mean = (pa[e][0] + pb[e][0]) * (1.0f / D_);
      const float var = __builtin_fmaxf((pa[e][1] + pb[e][1]) * (1.0f / D_) - mean * mean, 0.f);
      const float rstd = __builtin_amdgcn_rsqf(var + 1e-5f);
      if (e < 2) { mu2[e] = mean; rs2[e] = rstd; } else { mu_n = mean; rs_n = rstd; }
    }
    f32x2 zz[7];
    const f32x2 nmu = {-mu2[0], -mu2[1]}, rsv = {rs2[0], rs2[1]};
#pragma unroll
    for (int j = 0; j < 7; ++j)
      zz[j] = __builtin_elementwise_fma(__builtin_elementwise_fma(nmu, (f32x2){csb[j][0], csb[j][0]}, mine[j] + oth[j]), rsv, (f32x2){csb[j][1], csb[j][1]});
    gelu_n(zz, IC<7>());
    if (n16 >= 12) zz[6] = (f32x2){0.f, 0.f};
    f32x2 s1 = ((zz[0] + zz[1]) + (zz[2] + zz[3])) + ((zz[4] + zz[5]) + zz[6]);
    f32x2 s2 = zz[0] * zz[0], s3 = zz[1] * zz[1];
#pragma unroll
    for (int j = 2; j < 7; ++j) { if (j & 1) s3 = __builtin_elementwise_fma(zz[j], zz[j], s3); else s2 = __builtin_elementwise_fma(zz[j], zz[j], s2); }
    s2 += s3;
    asm("" : "+v"(s1), "+v"(s2));
    float a0 = s1[0] + dpp_f<0xB1>(s1[0]), a1 = s1[1] + dpp_f<0xB1>(s1[1]), b0 = s2[0] + dpp_f<0xB1>(s2[0]);
    a0 = (l & 1) ? a1 : a0;
    const float b1 = s2[1] + dpp_f<0xB1>(s2[1]);
    b0 = (l & 1) ? b1 : b0;
    a0 += dpp_f<0x4E>(a0); b0 += dpp_f<0x4E>(b0);
    a0 += dpp_f<0x124>(a0); b0 += dpp_f<0x124>(b0);
    a0 += dpp_f<0x128>(a0); b0 += dpp_f<0x128>(b0);
    {
      const unsigned pa0 = __builtin_amdgcn_ds_bpermute(bsrc, __float_as_uint(a0)), pb0 = __builtin_amdgcn_ds_bpermute(bsrc, __float_as_uint(b0));
      __builtin_amdgcn_raw_buffer_store_b128((u32x4){tag, pa0, tag, pb0}, rs, ppo, WS_PART + (unsigned)((R * 8 + c) * 8 + wq) * 128u, SC1);
    }
    SCHED();
    __builtin_amdgcn_s_setprio(0);
    STAMP(10);
#pragma unroll
    for (int k = 0; k < 10; ++k) {
      if (k < 9) {
        int x = k * 8 + w;
        x = x < 70 ? x : 69;
        const int i = x + (x < 35 ? 35 : 70);
        dma1k(rs, lds + i * 1024, l16, WS_WINF + (unsigned)c * WBLK + (unsigned)i * 1024u);
      }
    }
#pragma unroll
    for (int k = WB; k < 16; ++k) LDW(k);
#pragma unroll
    for (int j = 0; j < 7; ++j)
      *(unsigned*)(lds + L_Z + (((rt1 >> 1) * 7 + j) * 64 + l) * 16 + (4 * (rt1 & 1) + 2 * kh1) * 2) = pack2(zz[j][0], zz[j][1]);
    SCHED();
#define TR0 2
#define TR1 TR0
#define TR2 TR1
#define TR3 5
#pragma unroll
    for (int g = 0; g < TR0; ++g) WSP_TR(g);
    if (w == 1) {
      const unsigned fo[1] = {l < 28 ? fwi + (WS_FWO - WS_FWI) + (unsigned)(c * 32 + l) * 128u : ~0u};
      wait_lines<1, 1>(rs, fo, tag);
    }
    asm volatile("s_waitcnt vmcnt(%0)" :: "n"(16 - WB) : "memory");
    BAR();
    STAMPT(11);
  }
  unsigned z1d[7];
  constexpr int L_RED = 28672;
#define WOA(i) (((i) < 28 ? (i) : (i) < 108 ? (i) + 32 : (i) < 120 ? (i) + 37 : (i) - 92) * 1024)
#define RBAR() do { __builtin_amdgcn_s_barrier(); asm volatile("" ::: "memory"); } while (0)
#define ZAUX 16
#define NW 14
#define WOD(i0, x) dma1k(rs, lds + WOA(i0) + (x), l16, wgb + (unsigned)(i0) * 1024u + (x))
  auto wo_dma = [&](int k) {};
  u32x4 zw[NW];
#define LDZ(f) do { if (((f) >> 3) == 4) { zs[(f) & 7] += 4096u; asm volatile("" : "+s"(zs[(f) & 7])); } \
    zw[(f) % NW] = __builtin_amdgcn_raw_buffer_load_b128(rs, l16 + (unsigned)(((f) >> 3) & 3) * 1024u, zs[(f) & 7], ZAUX); } while (0)
#define WD 24
#define WE 32
#define QS 5
#define PQ 3
#define NW0 NW
#define PP 1
#define WZ 4
#define WY 11
  static_assert(QS >= 4 && QS <= 6 && PQ >= 2 && PQ < QS && (WZ < 4 || QS >= 5), "");
  {
    f32x4 acc[7];
#pragma unroll
    for (int j = 0; j < 7; ++j) acc[j] = (f32x4){0.f, 0.f, 0.f, 0.f};
    u32x4 bf[7];
#pragma unroll
    for (int j = 0; j < 7; ++j) bf[j] = BFRAG(4, j);
    const unsigned po = WS_PART + (unsigned)((R * 8 + (l >> 3)) * 8 + wq) * 128u + (unsigned)(l & 7) * 16u;
    u32x4 pv;
    auto z1 = [&](auto Q0_, auto Q1_) __attribute__((always_inline)) {
#pragma unroll
      for (int qi = decltype(Q0_)::value; qi < decltype(Q1_)::value; ++qi) {
        const int q = (qi + 4) % 10, qn = (qi + 5) % 10;
        SCHED();
#pragma unroll
        for (int j = 0; j < 7; ++j) {
          acc[j] = MFMA16(bf[j], h2[q], acc[j]);
          if (qi < 9 && qi != QS - 1) bf[j] = BFRAG(qn, j);
          if (qi < 2 && (j == 1 || j == 4)) { const int i = (2 * qi + (j >> 2)) * 7 + (w < 7 ? w : 0); dma1k(rs, lds + i * 1024, l16, WS_WINF + (unsigned)c * WBLK + (unsigned)i * 1024u); }
          if (qi == 2 && j == 0) kc_ld(0);
          if (qi == PQ && j == 1) pv = ld16(rs, po);
          if (!PP && qi >= QS && (j & 1) && WD + 3 * (qi - QS) + (j >> 1) < WE) LDW((WD + 3 * (qi - QS) + (j >> 1)) & 31);
          SCHED();
        }
      }
    };
    auto req1 = [&]() {
#pragma unroll
      for (int k = 16; k < WD; ++k) { LDW(k); SCHED(); }
    };
    auto req2 = [&]() {
#pragma unroll
      for (int k = WD; k < (PP ? WE : WD); ++k) { LDW(k); SCHED(); }
      sfor<0, WZ>([&](auto G_) __attribute__((always_inline)) { WOD(68 + 8 * decltype(G_)::value, wx8); SCHED(); });
    };
    if (kh1) req1();
    z1(IC<0>(), IC<QS>());
    STAMPT(29);
    f32x2 zv[2][4];
    unsigned sro = (unsigned)(L_ST2 + h4 * 16), swo = l < 8 ? (unsigned)(L_ST2 + (16 * rt1 + 4 * (l >> 1) + 2 * kh1 + (l & 1)) * 4) : (unsigned)(L_PST + l * 4);
    unsigned zo = w < 7 ? (unsigned)(h4 * 7168 + n16 * 16) : 0x80000000u;
#pragma unroll
    for (int m = 0; m < 2; ++m) {
      const u32x4 zf = *(const u32x4*)(lds + L_Z + ((m * 7 + (w < 7 ? w : 0)) * 64 + l) * 16);
#pragma unroll
      for (int e = 0; e < 4; ++e) { zv[m][e] = (f32x2){bflo(zf[e]), bfhi(zf[e])}; asm volatile("" : "+v"(zv[m][e])); }
    }
    asm volatile("" : "+v"(sro), "+v"(swo), "+v"(zo));
#pragma unroll
    for (int g = TR0; g < TR1; ++g) WSP_TR(g);
    __builtin_amdgcn_s_setprio(3);
    if (__any(((pv[0] ^ tag) | (pv[2] ^ tag)) != 0u)) {
      _Pragma("unroll 1") for (unsigned spins = 0; spins < SPIN_CAP; ++spins) {
        pv = ld16(rs, po);
        if (__all(((pv[0] ^ tag) | (pv[2] ^ tag)) == 0u)) break;
        __builtin_amdgcn_s_sleep(1);
      }
    }
    {
      const float mean = red8(__uint_as_float(pv[1])) * (1.0f / FH);
      const float var = __builtin_fmaxf(red8(__uint_as_float(pv[3])) * (1.0f / FH) - mean * mean, 0.f);
      const float rstd = __builtin_amdgcn_rsqf(var + 1e-5f);
      *(float*)(lds + swo) = rstd;
      *(float*)(lds + swo + 256) = -mean * rstd;
    }
    SCHED();
    consts_put(0);
    if (!kh1) req1();
    BAR();
    STAMP(12);
    {
      f32x4 sa[2][2], sb[2][2];
#pragma unroll
      for (int i = 0; i < 4; ++i) { sa[i >> 1][i & 1] = *(const f32x4*)(lds + sro + i * 64); sb[i >> 1][i & 1] = *(const f32x4*)(lds + sro + i * 64 + 256); }
      SCHED();
#pragma unroll
      for (int m = 0; m < 2; ++m) {
        u32x4 o;
#pragma unroll
        for (int e = 0; e < 4; ++e) {
          const f32x4 a = sa[m][e >> 1], b = sb[m][e >> 1];
          const f32x2 x = __builtin_elementwise_fma(zv[m][e], (f32x2){a[2 * (e & 1)], a[2 * (e & 1) + 1]}, (f32x2){b[2 * (e & 1)], b[2 * (e & 1) + 1]});
          o[e] = pack2(x[0], x[1]);
        }
        __builtin_amdgcn_raw_buffer_store_b128(o, rs, zo + (unsigned)m * 256u, WS_Z2F + (unsigned)((c * 64 + 4 * (R >> 1)) * 7 + (w < 6 ? w + 1 : 0)) * 1024u + (unsigned)(R & 1) * 512u, SC1);
      }
    }
    SCHED();
    __builtin_amdgcn_s_setprio(0);
    STAMPT(13);
#pragma unroll
    for (int j = 0; j < 7; ++j) bf[j] = BFRAG((QS + 4) % 10, j);
    if (kh1) req2();
    z1(IC<QS>(), IC<10>());
    f32x2 mine[7];
    if (kh1) {
#pragma unroll
      for (int j = 0; j < 7; ++j) { *(f32x2*)(lds + rdo + L_RED + j * 512) = (f32x2){acc[j][0], acc[j][1]}; mine[j] = (f32x2){acc[j][2], acc[j][3]}; }
    } else {
#pragma unroll
      for (int j = 0; j < 7; ++j) { *(f32x2*)(lds + rdo + L_RED + j * 512) = (f32x2){acc[j][2], acc[j][3]}; mine[j] = (f32x2){acc[j][0], acc[j][1]}; }
    }
    if (!kh1) req2();
#pragma unroll
    for (int g = TR1; g < TR2; ++g) WSP_TR(g);
    asm volatile("s_waitcnt vmcnt(%0)" :: "n"(WE - WD + WZ) : "memory");
    BAR();
    if (w == 3) flag4(WS_FZ + (unsigned)(c * 32 + R) * 128u);
    STAMP(6);
    auto req = [&](auto G0_, auto G1_) __attribute__((always_inline)) {
#pragma unroll
      for (int k = WE; k < 32; ++k) LDW(k);
      SCHED();
      sfor<decltype(G0_)::value, decltype(G1_)::value>([&](auto G_) __attribute__((always_inline)) {
        constexpr int i0 = 4 * decltype(G_)::value;
        WOD(i0 < 68 ? i0 : i0 + 8 * WZ, wx4); SCHED();
      });
    };
    if (kh1) req(IC<0>(), IC<WY>());
    u32x4 gv[8];
    auto gb_ld = [&]() {
#pragma unroll
      for (int k = 0; k < 8; ++k) gv[k] = __builtin_amdgcn_raw_buffer_load_b128(rs, l < 40 ? l16 : 0u, WS_MODQ + (unsigned)(k * 2560 + 1920 + 80 * c) * 8u, SC1);
    };
    if (w == 2) gb_ld();
    f32x2 z[7];
    {
      f32x2 oth[7];
      f32x4 cc[7];
#pragma unroll
      for (int j = 0; j < 7; ++j) {
        oth[j] = *(const f32x2*)(lds + oto + L_RED + j * 512);
        cc[j] = *(const f32x4*)(lds + L_CONST + (16 * j + 4 * h4 + 2 * kh1) * 8);
      }
      SCHED();
#pragma unroll
      for (int j = 0; j < 7; ++j)
        z[j] = __builtin_elementwise_fma(__builtin_elementwise_fma((f32x2){-mu_n, -mu_n}, (f32x2){cc[j][0], cc[j][1]}, oth[j] + mine[j]),
                                         (f32x2){rs_n, rs_n}, (f32x2){cc[j][2], cc[j][3]});
    }
    gelu_n(z, IC<7>());
#pragma unroll
    for (int j = 0; j < 7; ++j) z1d[j] = pack2(z[j][0], z[j][1]);
    asm volatile("" : "+v"(z1d[0]), "+v"(z1d[1]), "+v"(z1d[2]), "+v"(z1d[3]), "+v"(z1d[4]), "+v"(z1d[5]), "+v"(z1d[6]));
    SCHED();
    STAMPT(30);
    if (w == 2) {
      auto bad = [&]() {
        unsigned d = 0u;
#pragma unroll
        for (int k = 0; k < 8; ++k) d |= ne2(gv[k][0], gv[k][2]);
        return __any(d != 0u);
      };
      if (bad()) {
        _Pragma("unroll 1") for (unsigned spins = 0; spins < SPIN_CAP; ++spins) {
          __builtin_amdgcn_s_sleep(8);
          gb_ld();
          if (!bad()) break;
        }
      }
      f32x2 t[4];
#pragma unroll
      for (int k = 0; k < 4; ++k) t[k] = (f32x2){__uint_as_float(gv[2 * k][1]) + __uint_as_float(gv[2 * k + 1][1]), __uint_as_float(gv[2 * k][3]) + __uint_as_float(gv[2 * k + 1][3])};
      *(f32x2*)(lds + L_MISC + l * 8) = rbf2((t[0] + t[1]) + (t[2] + t[3]));
    }
    if (!kh1) req(IC<WY>(), IC<30 - 2 * WZ>());
    const unsigned fo[1] = {l < 4 ? fzs + (unsigned)l * 128u : ~0u};
    const unsigned fv = ld4(rs, fo[0]);
    SCHED();
#pragma unroll
    for (int g = TR2; g < TR3; ++g) WSP_TR(g);
    SCHED();
    STAMP(14);
    if (__builtin_amdgcn_ballot_w64(fv == tag) != 15ull) wait_lines<1, 1>(rs, fo, tag);
    STAMPT(23);
#pragma unroll
    for (int f = 0; f < NW; ++f) { LDZ(f); SCHED(); }
  }

#define WO2 15
#define TRC TR3
#define VMW(n) asm volatile("s_waitcnt vmcnt(%0)" ::"n"(n) : "memory")
  {
    SCHED();
#pragma unroll
    for (int g = TRC; g < 8; ++g) WSP_TR(g);
    SCHED();
    STAMP(20);
    const int rt = rt1, kh = kh1;
    unsigned rw = (unsigned)(L_RED + w * 512 + l * 8), rr = (unsigned)(L_RED + w * 4096 + l * 8);
    asm volatile("" : "+v"(rw), "+v"(rr));
    const float bs = bflo(bs_raw);
    unsigned long long hm = 0xffff000000000000ull;
    asm volatile("" : "+s"(hm));
    unsigned l8;
    f32x4 acc2[2][4];
    float rv[8][2];
    u32x4 ud;
    u32x4 P[2][8];
    u32x2 xp[3];
    auto ld_p = [&](int pt, int i) { P[pt][i] = __builtin_amdgcn_raw_buffer_load_b128(rs, l16 + (unsigned)pt * 1024u, ui + (unsigned)i * 2048u, SC1); };
    auto red_put = [&](int j, int r) {
      *(f32x2*)(lds + rw + r * 4096) = (f32x2){acc2[j & 1][r][0], acc2[j & 1][r][1]};
      *(f32x2*)(lds + rw + (r + 4) * 4096) = (f32x2){acc2[j & 1][r][2], acc2[j & 1][r][3]};
    };
    auto red_put1 = [&](int j, int i) { *(volatile LAS float*)(lds + rw + (i >> 2) * 4096 + ((i >> 1) & 1) * 16384 + (i & 1) * 4) = acc2[j & 1][i >> 2][i & 3]; };
    auto red_get = [&](int i) { const f32x2 x = *(const volatile LAS f32x2*)(lds + rr + i * 512); rv[i][0] = x[0]; rv[i][1] = x[1]; };
    float zz[2], gg[2];
    unsigned cs;
    auto sum_rows_u = [&](unsigned v) {
      auto a = __builtin_amdgcn_permlane16_swap(v, v, false, false);
      v = a[0] + a[1];
      auto b = __builtin_amdgcn_permlane32_swap(v, v, false, false);
      return b[0] + b[1];
    };
    auto red_fin = [&](int j, int k) {
      const int e = k & 1, i = k >> 1;
#define VADD(d, x, y) asm volatile("v_add_f32 %0, %1, %2" : "=v"(d) : "v"(x), "v"(y))
      if (i < 4) VADD(rv[2 * i][e], rv[2 * i][e], rv[2 * i + 1][e]);
      if (i == 4 || i == 5) VADD(rv[4 * (i & 1)][e], rv[4 * (i & 1)][e], rv[4 * (i & 1) + 2][e]);
      if (i == 6) VADD(rv[0][e], rv[0][e], bs);
      if (i == 7) VADD(gg[e], rv[0][e], rv[4][e]);
      if (i == 8) { zz[e] = e ? bfhi(z1d[(j + 6) % 7]) : bflo(z1d[(j + 6) % 7]); asm volatile("" : "+v"(zz[e])); }
      if (i == 9) { zz[e] *= gg[e]; asm volatile("" : "+v"(zz[e])); }
      if (k == 20) { ud[j & 3] = pack2(zz[0], zz[1]); asm volatile("" : "+v"(ud[j & 3])); }
      if (k == 21) {
        if (j == 0) asm volatile("v_cndmask_b32 %0, %0, 0, %1" : "+v"(ud[0]) : "s"(hm));
        if (j == 2) asm volatile("v_add3_u32 %0, %1, %2, %3" : "=v"(cs) : "v"(ud[0]), "v"(ud[1]), "v"(ud[2]));
        if (j == 3) {
          cs = tag - sum_rows_u(cs + ud[3]);
          asm volatile("v_cndmask_b32 %0, %0, %1, %2" : "+v"(ud[0]) : "v"(cs), "s"(hm));
        }
        if (j >= 4) { ud[3] = (j == 4 ? tag : ud[3]) - ud[j & 3]; asm volatile("" : "+v"(ud[3])); }
        if (j % 3 == 0 && j) __builtin_amdgcn_raw_buffer_store_b128(ud, rs, l16 + (unsigned)(j / 6) * 1024u, uo, SC1);
      }
    };
#define P1M 4
#define G2S 0
    struct Sch { int wide, lp, prio, lf, b1, b2, wp[16], rp[8], vp[22]; };
    constexpr Sch SC[4] = {
      {0, 2, 8, 1, 18, 29, {0, 1, 2, 3, 4, 5, 6, 7, 8, 9, 10, 11, 12, 13, 14, 15}, {19, 19, 20, 20, 21, 21, 22, 22}, {29, 29, 30, 30, 31, 31, 32, 33, 35, 36, 37, 38, 39, 40, 41, 42, 43, 44, 45, 46, 47, 48}},
      {1, 3, 4, 0, 18, 28, {3, 7, 11, 15, 99, 99, 99, 99, 99, 99, 99, 99, 99, 99, 99, 99}, {20, 20, 21, 21, 22, 22, 24, 24}, {28, 28, 29, 29, 30, 30, 31, 31, 32, 32, 33, 33, 34, 34, 35, 36, 36, 37, 37, 38, 38, 39}},
      {0, 1, 4, 1, 17, 29, {0, 1, 2, 3, 4, 5, 6, 7, 8, 9, 10, 11, 12, 13, 14, 15}, {17, 17, 18, 18, 19, 19, 20, 20}, {29, 29, 30, 30, 31, 31, 32, 34, 35, 36, 38, 39, 40, 42, 43, 44, 45, 46, 47, 48, 48, 49}},
      {0, 2, 0, 1, 17, 25, {0, 1, 2, 3, 4, 5, 6, 7, 8, 9, 10, 11, 12, 13, 14, 15}, {17, 17, 18, 18, 19, 19, 20, 20}, {25, 25, 26, 26, 27, 27, 28, 28, 29, 29, 30, 30, 31, 31, 32, 33, 23, 24, 34, 35, 36, 37}}};
#define JF 4
#define PC0 26
#define P1D 2
#define VL 2
    constexpr Sch S = SC[G2S];
    constexpr Sch S5 = {0, S.lp, S.prio, S.lf, 17, 24, {0, 1, 2, 3, 4, 5, 6, 7, 8, 9, 10, 11, 12, 13, 14, 15}, {17, 17, 18, 18, 19, 19, 20, 20}, {24, 24, 25, 25, 26, 26, 27, 27, 28, 28, 29, 29, 30, 30, 31, 31, 22, 23, 32, 32, 33, 33}};
    constexpr Sch S6 = {1, S.lp, S.prio, S.lf, 5, 12, {99, 99, 99, 3, 99, 99, 99, 99, 99, 99, 99, 99, 99, 99, 99, 99}, {5, 5, 6, 6, 7, 7, 8, 8},
                        {12, 12 + 1 / VL, 12 + 2 / VL, 12 + 3 / VL, 12 + 4 / VL, 12 + 5 / VL, 12 + 6 / VL, 12 + 7 / VL, 12 + 8 / VL, 12 + 9 / VL, 12 + 10 / VL, 12 + 11 / VL, 12 + 12 / VL, 12 + 13 / VL, 12 + 14 / VL, 12 + 15 / VL, 10, 11,
                         12 + 16 / VL, 12 + 17 / VL, 12 + 18 / VL, 12 + 19 / VL}};
    unsigned wb[3], xo;
#define XPM 19
    const unsigned wl[3] = {(unsigned)(120 + WO3 + w) * 1024u, (unsigned)(128 + WO3 + w) * 1024u, wl2}, wg = wgb - (unsigned)(WO3 * 1024);
#define NAF 32
#define NA0 18
    u32x4 af[NAF];
#define LDA(m) af[(m) % NAF] = *(const u32x4*)(lds + wb[WOA(2 * (m)) >> 16] + (WOA(2 * (m)) & 65535))
    unsigned ck[2];
    const unsigned tag4 = tag * 4u;
    auto p_chk = [&](int pt, int pr, int k) {
      u32x4* q = P[pt] + 4 * pr;
      if (k < 2) ck[k & 1] = q[k & 1][0] + q[k & 1][1];
      else if (k < 8) ck[k & 1] = ck[k & 1] + q[(k & 1) + 2 * ((k >> 2) & 1)][k & 2] + q[(k & 1) + 2 * ((k >> 2) & 1)][(k & 2) + 1];
      else if (k == 8) ck[0] = pt ? ck[0] + ck[1] : sum_rows_u(ck[0] + ck[1]);
      if (k < 9) asm volatile("" : "+v"(ck[k & 1]));
      if (k == 9) {
        if (__builtin_expect(__any(ck[0] != tag4), 0)) {
          _Pragma("unroll 1") for (unsigned spins = 0; spins < SPIN_CAP; ++spins) {
            unsigned d = 0u;
#pragma unroll
            for (int i = 0; i < 4; ++i) ld_p(pt, 4 * pr + i);
#pragma unroll
            for (int i = 0; i < 4; ++i) d += q[i][0] + q[i][1] + q[i][2] + q[i][3];
            if (!pt) d = sum_rows_u(d);
            if (!__any(d != tag4)) break;
            __builtin_amdgcn_s_sleep(1);
          }
        }
      }
      if (k >= 10 && !pt) { unsigned x = q[(k - 10) & 3][0]; asm volatile("v_cndmask_b32 %0, %0, 0, %1" : "+v"(x) : "s"(hm)); q[(k - 10) & 3][0] = x; }
      if (k >= 10 && k < 13 && pt) { q[(k - 10) % 3][3] = q[3][(k - 10) % 3]; asm volatile("" : "+v"(q[(k - 10) % 3])); }
    };
    sfor<0, 224>([&](auto M_) __attribute__((always_inline)) {
      constexpr int mm = decltype(M_)::value, j = mm >> 5, m = mm & 31;
      constexpr bool ro = j >= 5 && m >= 16;
      constexpr int t = ro ? 4 + (m & 3) : m >> 2, r = ro ? (m >> 2) & 3 : m & 3, f = 8 * j + t;
      constexpr Sch C = j < JF ? S : j < 6 ? S5 : S6, B = j <= JF ? S : S5;
      if (t == 0) asm volatile("v_mfma_f32_16x16x32_bf16 %0, %1, %2, 0" : "=&v"(acc2[j & 1][r]) : "v"(zw[f % NW]), "v"(wsp[r][t]), "v"(zw[(f | 1) % NW]));
      else asm volatile("v_mfma_f32_16x16x32_bf16 %0, %1, %2, %0" : "+v"(acc2[j & 1][r]) : "v"(zw[f % NW]), "v"(wsp[r][t]), "v"(zw[(f | 1) % NW]));
      SCHED();
      if (S.prio && (m + 31 - S.lp) % (S.prio ? S.prio : 1) == 0) __builtin_amdgcn_s_setprio(1);
      auto ld = [&]() {
        if ((m & 3) == S.lp) {
          if (S.prio && (m + 32 - S.lp) % (S.prio ? S.prio : 1) == 0) __builtin_amdgcn_s_setprio(0);
          constexpr int g = S.lp == 3 ? mm >> 2 : (mm >> 2) - 1;
          if (g >= 0 && g + NW < 56) LDZ(g + NW);
          constexpr int y = g + NW - 56;
          if (y >= 0 && WO2 + y < 15) wo_dma(WO2 + y);
          SCHED();
        }
        if (j == 6 && m >= P1M && m < P1M + 8 * P1D && (m - P1M) % P1D == 0) { ld_p(0, ((m - P1M) / P1D) & 7); SCHED(); }
      };
      if (!S.lf) ld();
      if ((m == C.b1 && j > 0) || m == C.b2) {
        if (j == 6 && m == C.b2 && WO2 < 15) VMW(m > P1M ? (m - P1M + P1D - 1) / P1D : 0);
        asm volatile("" ::: "memory"); __builtin_amdgcn_s_waitcnt(0xC07F); RBAR(); SCHED();
      }
      sfor<0, 16>([&](auto I_) __attribute__((always_inline)) {
        constexpr int i = decltype(I_)::value;
        if (j > 0 && C.wp[i] == m) { if (C.wide) red_put(j + 1, i & 3); else red_put1(j + 1, i); SCHED(); }
        if (j > 0 && i < 8 && C.rp[i & 7] == m) { red_get(i & 7); SCHED(); }
      });
      if (j == 5 && m >= 27 && (m & 1)) { red_put(j, ((m - 27) >> 1) & 3); SCHED(); }
      if (j == 6 && m >= 23 && (m & 3) == 3) { red_put(j, ((m - 23) >> 2) & 3); SCHED(); }
      if (j == 6 && m == XPM) {
        asm volatile("v_lshrrev_b32 %0, 1, %1" : "=v"(l8) : "v"(l16));
        xo = (l8 & 0x180u) + (unsigned)psi((l8 >> 3) & 15) * 8u;
        asm volatile("" : "+v"(xo));
      }
      if (j == 6 && m > XPM && m < XPM + 4) { xp[(m - XPM - 1) % 3] = __builtin_amdgcn_raw_buffer_load_b64(rs, xo + (unsigned)(m - XPM - 1) * 512u, xs, BULK_AUX); SCHED(); }
      if (j == 6 && m == 4) {
#pragma unroll
        for (int i = 0; i < 3; ++i) { wb[i] = (unsigned)(kh * 1024 + i * 65536) + l16; asm volatile("" : "+v"(wb[i])); }
      }
      if (j == 6 && m != 23 && m != 27 && m != 31) {
        constexpr int n = NA0 - 1 - (30 - m) + (m < 27) + (m < 23);
        if (n >= 0) { LDA(n >= 0 ? n : 0); SCHED(); }
      }
      if (j == 6 && m >= PC0 && m < PC0 + 5) { p_chk(0, 0, 2 * (m - PC0)); SCHED(); if (m < PC0 + 4) p_chk(0, 0, 2 * (m - PC0) + 1); SCHED(); }
      if (j == 6 && m == PC0 - 1) { red_fin(6, 16); red_fin(6, 17); SCHED(); }
      sfor<0, 22>([&](auto K_) __attribute__((always_inline)) {
        constexpr int k = decltype(K_)::value;
        if (j > 1 && B.vp[k] == m + 32) { red_fin(j > 1 ? j - 2 : 0, k); SCHED(); }
        if (j > 0 && C.vp[k] == m) { red_fin(j > 0 ? j - 1 : 0, k); SCHED(); }
      });
      if (S.lf) ld();
    });
    STAMP(15);
#define NM0 0
#define GM 19
#define MB 24
    f32x4 acc[5];
    f32x4 xf[3], gf[3];
    unsigned go, oo, eo, ho, tw, tr, o1, ta, tb;
    const unsigned s2 = (unsigned)(L_MISC + 128 * kh), s4 = s1 - (unsigned)kh * 1024u;
    unsigned long long om = kh ? ~0ull : 0xccccccccccccccccull;
#define PIN2(a, b) asm volatile("" : "+v"(a), "+v"(b))
    auto glue = [&](int k) {
      if (k == 0) { ta = l8 >> 5; tb = l16 & 48u; PIN2(ta, tb); }
      if (k == 1) { tr = __umul24(ta, 112u) + tb; oo = ta & 12u; PIN2(tr, oo); }
      if (k == 2) { tr += s1; oo += ta; PIN2(tr, oo); }
      if (k == 3) { oo = __umul24(oo, 1280u) + tb; ta = l8 >> 4; PIN2(oo, ta); }
      if (k == 4) { ta &= 24u; tb = l16 & 0xf0u; PIN2(ta, tb); }
      if (k == 5) { tw = __umul24(tb, 7u) + ta; go = (ta << 1) + s2; PIN2(tw, go); }
      if (k == 6) { tw += s1; asm volatile("v_cndmask_b32 %0, %1, -2.0, %2" : "=v"(o1) : "v"(oo), "s"(om)); asm volatile("" : "+v"(tw)); }
      if (k == 7) { eo = l16 + s3; ho = l16 + s4; PIN2(eo, ho); }
      if (k >= 8 && k < 14) {
        const unsigned x = xp[((k - 8) >> 1) % 3][k & 1];
        float lo = bflo(x), hi = bfhi(x);
        PIN2(lo, hi);
        xf[((k - 8) >> 1) % 3][2 * (k & 1)] = lo; xf[((k - 8) >> 1) % 3][2 * (k & 1) + 1] = hi;
      }
    };
    auto g3 = [&](auto M0_, auto M1_) __attribute__((always_inline)) {
      sfor<decltype(M0_)::value, decltype(M1_)::value>([&](auto M_) __attribute__((always_inline)) {
        constexpr int mi = decltype(M_)::value;
        constexpr int m = mi < 55 ? mi : mi < 64 ? 55 + 5 * ((mi - 55) / 3) + (mi - 55) % 3 : 58 + 5 * ((mi - 64) / 2) + (mi & 1), u2 = m / 5;
        const u32x4 fb = u2 < 8 ? P[0][u2 & 7] : P[1][u2 < 11 ? (u2 - 8) & 3 : (u2 - 7) & 7];
        if (m < 5) asm volatile("v_mfma_f32_16x16x32_bf16 %0, %1, %2, 0" : "=&v"(acc[m % 5]) : "v"(af[m % NAF]), "v"(fb));
        else asm volatile("v_mfma_f32_16x16x32_bf16 %0, %1, %2, %0" : "+v"(acc[m % 5]) : "v"(af[m % NAF]), "v"(fb));
        SCHED();
        if (mi == NM0) RBAR();
        if (mi >= NM0 && mi < NM0 + 3) dma1k(rs, lds + wl[(mi - NM0) % 3], l16, wg + wl[(mi - NM0) % 3]);
        if (mi >= 5 && mi < 19) p_chk(0, 1, mi - 5);
        if (mi >= GM && mi < GM + 14) glue(mi - GM);
        if (mi >= 47 && mi < 54) { p_chk(1, 1, 2 * (mi - 47)); SCHED(); p_chk(1, 1, 2 * (mi - 47) + 1); }
        if (mi >= NM0 && mi < 40 && NA0 + mi - NM0 < 60) LDA(NA0 + mi - NM0);
        if (mi >= 66 && mi < 69) *(f32x4*)(lds + eo + (mi - 66) * 1024) = acc[(mi - 66) % 3];
        SCHED();
      });
    };
    __builtin_amdgcn_s_setprio(3);
    asm volatile("s_nop 7");
    red_put(6, 3);
    SCHED();
    BAR();
#pragma unroll
    for (int i = 0; i < 8; ++i) red_get(i);
    SCHED();
#pragma unroll
    for (int k = 0; k < 20; ++k) red_fin(6, k < 16 ? k : k + 2);
    SCHED();
    __builtin_amdgcn_s_setprio(0);
    STAMPT(16);
#pragma unroll
    for (int k = 9; k < 14; ++k) p_chk(0, 0, k);
    SCHED();
    g3(IC<0>(), IC<MB>());
    STAMPT(22);
    DRAIN();
    RBAR();
    STAMPT(27);
#define MY 12
    if (MY == 0 || !kh) {
#pragma unroll
      for (int i = 0; i < 8; ++i) { ld_p(1, i); SCHED(); }
    }
    g3(IC<MB>(), IC<MB + MY>());
    if (MY > 0 && kh) {
#pragma unroll
      for (int i = 0; i < 8; ++i) { ld_p(1, i); SCHED(); }
    }
    STAMPT(26);
    g3(IC<MB + MY>(), IC<40>());
    STAMPT(24);
#pragma unroll
    for (int m = NA0 + 40 - NM0; m < 70; ++m) LDA(m);
#pragma unroll
    for (int x = 0; x < 3; ++x) gf[x] = *(const f32x4*)(lds + go + x * 64);
    SCHED();
    const rsrc_t ro = __builtin_amdgcn_make_buffer_rsrc((void*)p.out, 0, S_ * D_ * 2, 0x00020000);
    STAMPT(17);
#pragma unroll
    for (int k = 0; k < 14; ++k) p_chk(1, 0, k);
    asm volatile("s_nop 1");
    STAMPT(25);
    g3(IC<40>(), IC<70>());
    STAMPT(18);
    BAR();
    STAMPT(28);
    f32x4 o4[3];
    u32x4 ov[2];
#pragma unroll
    for (int j = 2; j >= 0; --j) o4[j] = *(const volatile LAS f32x4*)(lds + ho + j * 1024);
    asm volatile("" : "+v"(acc[2]), "+v"(acc[3]), "+v"(acc[4]));
    SCHED();
#pragma unroll
    for (int j = 2; j >= 0; --j) {
      const f32x4 v = __builtin_elementwise_fma(gf[j], o4[j] + acc[2 + j], xf[j]);
      *(volatile LAS u32x2*)(lds + tw + j * 32) = (u32x2){pack2(v[0], v[1]), pack2(v[2], v[3])};
      if (j < 2) ov[j] = *(const volatile LAS u32x4*)(lds + tr + j * 32);
    }
    __builtin_amdgcn_raw_buffer_store_b128(ov[1], ro, oo + 32u, os, 0);
    __builtin_amdgcn_raw_buffer_store_b128(ov[0], ro, o1, os, 0);
    if (bid == 0 && w == 5 && l8 == 0u) st4(rs, WS_EPOCH, tag);
  }
  STAMP(19);
}

extern "C" __device__ __attribute__((used, noinline, visibility("hidden"))) void mega_tail() {}

extern "C" void kernel_launch(void* const* d_in, const int* in_sizes, int n_in, void* d_out, int out_size,
                              void* d_ws, size_t ws_size, hipStream_t stream) {
  Params p;
  p.q = (const u16*)d_in[0];
  p.k = (const u16*)d_in[1];
  p.v = (const u16*)d_in[2];
  p.shift_k = (const u16*)d_in[3];
  p.ssm_k = (const u16*)d_in[4];
  p.ssm_D = (const u16*)d_in[5];
  p.w_in = (const u16*)d_in[7];
  p.w_sp = (const u16*)d_in[8];
  p.b_sp = (const u16*)d_in[9];
  p.w_out = (const u16*)d_in[10];
  p.cvec = (const u16*)d_in[11];
  p.w_mod = (const u16*)d_in[12];
  p.out = (u16*)d_out;
  p.ws = (char*)d_ws;
  mega<<<256, 512, 0, stream>>>(p);
}
```

```cpp
#include <hip/hip_runtime.h>
#include <stdint.h>

typedef unsigned short u16;
typedef __attribute__((ext_vector_type(8))) short bf16x8;
typedef __attribute__((ext_vector_type(4))) short bf16x4;
typedef __attribute__((ext_vector_type(4))) float f32x4;
typedef __attribute__((ext_vector_type(2))) float f32x2;
typedef __attribute__((ext_vector_type(4))) unsigned int u32x4;
typedef __attribute__((ext_vector_type(2))) unsigned int u32x2;
#define LAS __attribute__((address_space(3)))
typedef __amdgpu_buffer_rsrc_t rsrc_t;
typedef __attribute__((ext_vector_type(4))) short s16x4;

constexpr int S_ = 2048, D_ = 640, F_ = 1728, FH = 864, CB = 108;

constexpr unsigned WS_EPOCH = 0;
constexpr unsigned WS_FWI = 4096;
constexpr unsigned WS_FZ = WS_FWI + 384 * 128;
constexpr unsigned WS_FWO = WS_FZ + 256 * 128;
constexpr unsigned F4S = 896 * 128;
constexpr unsigned WS_CS = WS_FZ + 256 * 128;
constexpr unsigned WS_MOD = 131072;
constexpr unsigned WS_MODP = WS_MOD + 81920;
constexpr unsigned WS_ST1 = 393216;
constexpr unsigned WS_PART = 524288;
constexpr unsigned WS_CSP = WS_PART + 262144;
constexpr unsigned WS_FX = WS_CSP + 655360;
constexpr unsigned WS_X1B = WS_FX + 16 * 4 * 160 * 128;
constexpr unsigned WS_QB = WS_X1B + 2621440;
constexpr unsigned WS_KV3 = WS_QB + 2621440;
constexpr unsigned WS_MODQ = WS_KV3 + 1310720;
constexpr unsigned WBLK = 143360;
constexpr unsigned WS_WINF = WS_MODQ + 245760;
constexpr unsigned WS_WOUTF = WS_WINF + 16 * WBLK;
constexpr unsigned WS_Z2F = WS_WOUTF + 8 * WBLK;
constexpr unsigned WS_UF = WS_Z2F + 8 * 64 * 7168;
constexpr unsigned WS_F4 = WS_UF + 32 * 4 * 8 * 4096;
constexpr unsigned WS_END = WS_F4 + 4 * F4S;
constexpr unsigned WS_STAMP = 200u << 20;

constexpr int G_COPY = 4160;
constexpr int L_U = WBLK;
constexpr int U_CH = (256 + S_) * 2 + 32;
constexpr int L_FLG = L_U + 4 * U_CH;
constexpr int L_RT = 16384;
constexpr int L_CONST = WBLK;
constexpr int L_PST = L_CONST + 2048;
constexpr int L_Z = L_CONST + 5120;
constexpr int L_ST2 = L_Z + 14336;
constexpr int L_MISC = L_ST2 + 512;
constexpr int LDS_TOTAL = 163840;

#define AUX_T 610
#define RT2_T 360
#define CB_T 380
#define W_T 700
#define Y_T 930
#define YC_T 910
#define YG_T 860
#define YPR 0
#define YSL 2
#define K_T 460
#ifndef M_T
#define M_T 520
#endif
#define WA_T 580
constexpr int SC1 = 16, NT = 2;
constexpr unsigned SPIN_CAP = 1u << 16;

__device__ __forceinline__ float bflo(unsigned x) { return __uint_as_float(x << 16); }
__device__ __forceinline__ float bfhi(unsigned x) { return __uint_as_float(x & 0xffff0000u); }
__device__ __forceinline__ unsigned f2bf(float x) {
  __bf16 b = (__bf16)x;
  return (unsigned)__builtin_bit_cast(unsigned short, b);
}
__device__ __forceinline__ float rbf(float x) { return bflo(f2bf(x)); }
typedef __attribute__((ext_vector_type(2))) __bf16 bf2_t;
__device__ __forceinline__ unsigned pack2(float lo, float hi) {
  f32x2 v = {lo, hi};
  return __builtin_bit_cast(unsigned, __builtin_convertvector(v, bf2_t));
}
__device__ __forceinline__ f32x2 rbf2(f32x2 v) {
  const unsigned p = __builtin_bit_cast(unsigned, __builtin_convertvector(v, bf2_t));
  return (f32x2){__uint_as_float(p << 16), __uint_as_float(p & 0xffff0000u)};
}
__device__ __forceinline__ f32x4 rbf4(f32x4 v) {
  const f32x2 a = rbf2((f32x2){v[0], v[1]}), b = rbf2((f32x2){v[2], v[3]});
  return (f32x4){a[0], a[1], b[0], b[1]};
}
template <int CTRL>
__device__ __forceinline__ float dpp_f(float v) {
  return __int_as_float(__builtin_amdgcn_update_dpp(0, __float_as_int(v), CTRL, 0xf, 0xf, true));
}
__device__ __forceinline__ float sum8(float v) {
  v += dpp_f<0xB1>(v);
  v += dpp_f<0x4E>(v);
  v += dpp_f<0x141>(v);
  return v;
}
__device__ __forceinline__ float sum16(float v) {
  v = sum8(v);
  v += dpp_f<0x140>(v);
  return v;
}
__device__ __forceinline__ float sum_rows(float v) {
  auto a = __builtin_amdgcn_permlane16_swap(__float_as_uint(v), __float_as_uint(v), false, false);
  v = __uint_as_float(a[0]) + __uint_as_float(a[1]);
  auto b = __builtin_amdgcn_permlane32_swap(__float_as_uint(v), __float_as_uint(v), false, false);
  return __uint_as_float(b[0]) + __uint_as_float(b[1]);
}
#define MFMA16(a, b, c) __builtin_amdgcn_mfma_f32_16x16x32_bf16(__builtin_bit_cast(bf16x8, a), __builtin_bit_cast(bf16x8, b), c, 0, 0, 0)
#define MFMA16K16(a, b, c) __builtin_amdgcn_mfma_f32_16x16x16bf16_1k(__builtin_bit_cast(bf16x4, a), __builtin_bit_cast(bf16x4, b), c, 0, 0, 0)
__device__ __forceinline__ u32x2 trd(const char* p) {
  return __builtin_bit_cast(u32x2, __builtin_amdgcn_ds_read_tr16_b64_v4i16((LAS s16x4*)p));
}
__device__ __forceinline__ u32x4 trfrag(const char* p, int pitch) {
  u32x2 a = trd(p), b = trd(p + 16 * pitch);
  return (u32x4){a[0], a[1], b[0], b[1]};
}
#define BAR() asm volatile("s_waitcnt lgkmcnt(0)\n\ts_barrier" ::: "memory")
#define DRAIN() do { __builtin_amdgcn_s_waitcnt(0x0F70); asm volatile("" ::: "memory"); } while (0)
#define SCHED() __builtin_amdgcn_sched_barrier(0)
template <int N> struct IC { static constexpr int value = N; };
template <int I, int N, class F>
__device__ __forceinline__ void sfor(F&& f) {
  if constexpr (I < N) { f(IC<I>()); sfor<I + 1, N>(f); }
}

__device__ __forceinline__ u32x4 ld16(rsrc_t r, unsigned off) { return __builtin_amdgcn_raw_buffer_load_b128(r, off, 0, SC1); }
__device__ __forceinline__ u32x2 ld8(rsrc_t r, unsigned off) { return __builtin_amdgcn_raw_buffer_load_b64(r, off, 0, SC1); }
__device__ __forceinline__ unsigned ld4(rsrc_t r, unsigned off) { return __builtin_amdgcn_raw_buffer_load_b32(r, off, 0, SC1); }
#define ST_AUX 16
__device__ __forceinline__ void st16(rsrc_t r, unsigned off, u32x4 v) { __builtin_amdgcn_raw_buffer_store_b128(v, r, off, 0, ST_AUX); }
__device__ __forceinline__ void st8(rsrc_t r, unsigned off, u32x2 v) { __builtin_amdgcn_raw_buffer_store_b64(v, r, off, 0, SC1); }
__device__ __forceinline__ void st_flag4(rsrc_t r, unsigned off, unsigned tag, int l) {
  if (l < 32) st16(r, WS_F4 - WS_FWI + off + (unsigned)(l >> 3) * F4S + (unsigned)(l & 7) * 16u, (u32x4){tag, tag, tag, tag});
}
__device__ __forceinline__ void st4(rsrc_t r, unsigned off, unsigned v) { __builtin_amdgcn_raw_buffer_store_b32(v, r, off, 0, SC1); }
#define BULK_AUX 16
__device__ __forceinline__ u32x4 ld16p(rsrc_t r, unsigned off) { return __builtin_amdgcn_raw_buffer_load_b128(r, off, 0, BULK_AUX); }
__device__ __forceinline__ u32x2 ld8p(rsrc_t r, unsigned off) { return __builtin_amdgcn_raw_buffer_load_b64(r, off, 0, BULK_AUX); }
__device__ __forceinline__ void dma1k(rsrc_t r, char* lds_dst, unsigned l16, unsigned soff) {
  __builtin_amdgcn_raw_ptr_buffer_load_lds(r, (LAS void*)lds_dst, 16, l16, soff, 0, BULK_AUX);
}

#define STAMP(i)
#define STAMPF(i)
#define STAMPT(i) STAMP(i)

template <int N, int SLP>
__device__ __forceinline__ void wait_lines(rsrc_t r, const unsigned (&off)[N], unsigned tag) {
  bool need[N];
#pragma unroll
  for (int i = 0; i < N; ++i) need[i] = off[i] != ~0u;
  _Pragma("unroll 1") for (unsigned spins = 0; spins < SPIN_CAP; ++spins) {
    unsigned v[N];
#pragma unroll
    for (int i = 0; i < N; ++i) {
      v[i] = tag;
      if (need[i]) v[i] = ld4(r, off[i]);
    }
    bool any = false;
#pragma unroll
    for (int i = 0; i < N; ++i) { need[i] = v[i] != tag; any = any || need[i]; }
    if (!__any(any)) break;
    __builtin_amdgcn_s_sleep(SLP);
  }
  asm volatile("" ::: "memory");
}

__device__ __forceinline__ int tau(int rt, int n) { return 32 * (rt >> 1) + 8 * (n >> 2) + 4 * (rt & 1) + (n & 3); }
__device__ __forceinline__ int psi(int n) { return 2 * (n & 7) + (n >> 3); }
#define QX_CM(d, s0, s1, P) "v_cndmask_b32_dpp " d ", " s0 ", " s1 ", vcc quad_perm:" P " row_mask:0xf bank_mask:0xf\n\t"
#define QX_BODY(M, P) \
  asm("s_mov_b32 vcc_lo, " M "\n\ts_mov_b32 vcc_hi, " M "\n\t" \
      QX_CM("%0", "%12", "%8", P) "v_xor_b32 %4, %8, %0\n\t" QX_CM("%1", "%13", "%9", P) "v_xor_b32 %5, %9, %1\n\t" \
      QX_CM("%2", "%14", "%10", P) "v_xor_b32 %6, %10, %2\n\t" QX_CM("%3", "%15", "%11", P) "v_xor_b32 %7, %11, %3" \
      : "=&v"(t[0]), "=&v"(t[1]), "=&v"(t[2]), "=&v"(t[3]), "=&v"(u[0]), "=&v"(u[1]), "=&v"(u[2]), "=&v"(u[3]) \
      : "v"(a[0]), "v"(a[1]), "v"(a[2]), "v"(a[3]), "v"(b[0]), "v"(b[1]), "v"(b[2]), "v"(b[3]) : "vcc")
template <int BIT>
__device__ __forceinline__ void quad_xchg3(u32x4& a, u32x4& b) {
  unsigned t[4], u[4];
  if (BIT == 0) QX_BODY("0x55555555", "[1,0,3,2]");
  else QX_BODY("0x33333333", "[2,3,0,1]");
  a = (u32x4){t[0], t[1], t[2], t[3]};
#pragma unroll
  for (int d = 0; d < 4; ++d) b[d] ^= (unsigned)__builtin_amdgcn_update_dpp(0, (int)u[d], BIT == 0 ? 0xB1 : 0x4E, 0xf, 0xf, true);
}
__device__ __forceinline__ void quad_transpose(u32x4& x0, u32x4& x1, u32x4& x2, u32x4& x3) {
  quad_xchg3<0>(x0, x1);
  quad_xchg3<0>(x2, x3);
  quad_xchg3<1>(x0, x2);
  quad_xchg3<1>(x1, x3);
  asm volatile("" : "+v"(x0), "+v"(x1), "+v"(x2), "+v"(x3));
}

struct Params {
  const u16 *q, *k, *v, *shift_k, *ssm_k, *ssm_D, *w_in, *w_sp, *b_sp, *w_out, *cvec, *w_mod;
  u16* out;
  char* ws;
};

__global__ __launch_bounds__(512, 2) void mega(Params p) {
  __shared__ __attribute__((aligned(16))) char lds[LDS_TOTAL];
  {
    unsigned long long pc0;
    unsigned vt;
    asm volatile("v_readfirstlane_b32 vcc_lo, %3\n\ts_cmpk_lt_u32 %2, 192\n\ts_cbranch_scc1 4f\n\ts_cmpk_lt_u32 vcc_lo, 64\n\ts_cbranch_scc0 4f\n\t"
                 "s_lshr_b32 vcc_hi, %2, 3\n\ts_sub_u32 vcc_hi, vcc_hi, 24\n\ts_lshl_b32 vcc_hi, vcc_hi, 13\n\t"
                 "v_lshlrev_b32 %1, 7, %3\n\tv_add_u32 %1, vcc_hi, %1\n\ts_getpc_b64 %0\n.Lmega_here:\n\t"
                 "v_min_u32 %1, mega_tail-.Lmega_here-4, %1\n\ts_movk_i32 m0, 0x2000\n\ts_nop 1\n\tglobal_load_lds_dword %1, %0\n4:\n\t"
                 "s_cmpk_lt_u32 %2, 160\n\ts_cbranch_scc0 1f\n\ts_cmpk_lt_u32 vcc_lo, 256\n\ts_cbranch_scc1 3f\n\t"
                 "s_call_b64 vcc, .Ltouch1\n\ts_branch 3f\n1:\n\t"
                 "s_cmpk_lt_u32 vcc_lo, 256\n\ts_cbranch_scc1 2f\n\ts_cmpk_lt_u32 %2, 224\n\ts_cbranch_scc0 2f\n\ts_call_b64 vcc, .Ltouch3\n\ts_branch 3f\n"
                 "2:\n\ts_call_b64 vcc, .Ltouch2\n3:"
                 : "=&s"(pc0), "=&v"(vt) : "s"(blockIdx.x), "v"(threadIdx.x) : "vcc", "scc", "memory");
  }
  SCHED();
  const int tid = threadIdx.x, l = tid & 63;
  const int w = __builtin_amdgcn_readfirstlane(tid >> 6);
  const int bid = blockIdx.x, xcd = bid & 7, idx = bid >> 3;
#define TOUCH_AT(n) asm volatile("s_branch 1f\n.Ltouch" #n ":\n\ts_setpc_b64 vcc\n1:")
  char* const ws = p.ws;
  asm volatile("" ::"s"(p.q), "s"(p.k), "s"(p.v), "s"(p.shift_k), "s"(p.ssm_k), "s"(p.ssm_D), "s"(p.w_in), "s"(p.w_sp), "s"(p.b_sp),
               "s"(p.w_out), "s"(p.cvec), "s"(p.w_mod), "s"(p.out), "s"(p.ws));
  unsigned epoch, tag, t_start;
  unsigned long long t64;
  asm volatile("s_load_dword %0, %2, 0x0\n\ts_memrealtime %1" : "=&s"(epoch), "=&s"(t64) : "s"(ws));
#define TAG_READY() do { SCHED(); \
    asm volatile("s_waitcnt lgkmcnt(0)\n\ts_mul_i32 %0, %2, 0x9e3779b1\n\ts_add_i32 %0, %0, 0x9e3779b9\n\ts_mov_b64 %1, %3" \
                 : "=&s"(tag), "=&s"(t64r) : "s"(epoch), "s"(t64) : "scc"); t_start = (unsigned)t64r; SCHED(); } while (0)
  unsigned long long t64r;
  const rsrc_t rs = __builtin_amdgcn_make_buffer_rsrc(ws, 0, WS_END, 0x00020000);
  const unsigned l16 = (unsigned)l * 16u;
  STAMP(0);
  const bool is_conv = idx < 20;
  const int quad = xcd * 20 + idx;
  const int R = is_conv ? (xcd & 3) * 5 + (idx >> 2) : (xcd & 3) * 3 + 15 + (idx >> 2);
  const int c = (xcd >> 2) * 4 + (idx & 3);
  const unsigned fwi = WS_F4 + (unsigned)(xcd & 3) * F4S, fz = fwi + (WS_FZ - WS_FWI);
  const int n16 = l & 15, h4 = l >> 4;
  u32x4 wsp[4][8];
#define ROTC 2
#define WAUX 0
  const int rotg = ((idx >> 4) ^ ((ROTC & 2) ? idx >> 1 : 0)) & 1, rotb = (idx >> 2) & 3, rotr = (ROTC & 1) ? (idx & 1) * 2 : 0;
  const rsrc_t rsp = __builtin_amdgcn_make_buffer_rsrc((void*)p.w_sp, 0, S_ * S_ * 2, 0x00020000);
  const unsigned wsp_vo = (unsigned)(((l >> 2) & 3) * 8 * 4096 + h4 * 64 + ((l + rotb) & 3) * 16);
#define LDW(k) LDW_(k)
#define WSP_TR(g) WSP_TR_(g)
#define WROW R
#define LDW_(k) wsp[((k) >> 2) & 3][((k) >> 4) * 4 + ((k) & 3)] = __builtin_amdgcn_raw_buffer_load_b128(rsp, wvo, \
    (unsigned)((WROW * 64 + 16 * ((((k) >> 2) & 2) ^ rotr) + 4 * (((k) >> 2) & 1) + ((k) & 3)) * 4096 + w * 512 + ((((k) >> 4) ^ rotg) * 256)), WAUX)
#define WSP_TR_(g) quad_transpose(wsp[(g) & 3][((g) >> 2) * 4], wsp[(g) & 3][((g) >> 2) * 4 + 1], wsp[(g) & 3][((g) >> 2) * 4 + 2], wsp[(g) & 3][((g) >> 2) * 4 + 3])
  const rsrc_t rwi = __builtin_amdgcn_make_buffer_rsrc((void*)p.w_in, 0, D_ * F_ * 2, 0x00020000);
  const rsrc_t rwo = __builtin_amdgcn_make_buffer_rsrc((void*)p.w_out, 0, FH * D_ * 2, 0x00020000);
  const rsrc_t rq = __builtin_amdgcn_make_buffer_rsrc((void*)p.q, 0, S_ * D_ * 2, 0x00020000);
  auto hold = [&](unsigned tt) { while ((unsigned)__builtin_amdgcn_s_memrealtime() - t_start < tt) __builtin_amdgcn_s_sleep(2); };
  auto ne2 = [&](unsigned x, unsigned y) { unsigned d; asm("v_bitop3_b32 %0, %1, %2, %3 bitop3:0x7e" : "=v"(d) : "v"(x), "v"(y), "s"(tag)); return d; };
  volatile LAS unsigned* lf = (volatile LAS unsigned*)(lds + L_FLG);
  auto lf_set = [&](int i) { asm volatile("s_waitcnt lgkmcnt(0)" ::: "memory"); if (l == 0) lf[i] = tag; };
  auto lf_wait = [&](int i0, int n) {
    _Pragma("unroll 1") for (unsigned spins = 0; spins < (SPIN_CAP << 4); ++spins) {
      const unsigned v = lf[i0 + (l & (n - 1))];
      if (__all(v == tag)) break;
      __builtin_amdgcn_s_sleep(1);
    }
    asm volatile("" ::: "memory");
  };
  const unsigned b2o = fwi + (unsigned)((8 + c) * 20 + 5 * (w & 3) + (l < 5 ? l : 0)) * 128u;
  auto b2_wait = [&](u32x2 fv, int slp) {
    if (!__all(fv[0] == tag)) {
      _Pragma("unroll 1") for (unsigned spins = 0; spins < SPIN_CAP; ++spins) {
        if (fv[0] != tag) fv = ld8(rs, b2o);
        if (__all(fv[0] == tag)) break;
        if (slp) __builtin_amdgcn_s_sleep(8); else __builtin_amdgcn_s_sleep(4);
      }
    }
    asm volatile("" ::: "memory");
    if (l < 5) lf[44 + 5 * w + l] = fv[1];
    if (l == 0) lf[36 + w] = tag;
  };
  auto load_b2 = [&](int k0, int k1) {
#pragma unroll
    for (int k = k0; k < k1; ++k) {
      int i = k + (idx >> 2) * 4;
      i = (i >= 35 ? i - 35 : i) + 35 * w;
      dma1k(rs, lds + i * 1024, l16, WS_WINF + (unsigned)(8 + c) * WBLK + (unsigned)i * 1024u);
    }
  };
  char* const wo_st = lds + 122880;
  auto wo_ld = [&]() {
    const int U = R < 28 ? R : 0, u2 = U >> 1, fi = (u2 + 1) % 3;
    const int c0 = 4 * (U & 1) + (u2 < 8 ? u2 >> 1 : u2 < 11 ? 0 : 2);
    const int b0 = u2 < 8 ? CB * c0 + 2 * (u2 & 1) + 96 : CB * (c0 + (fi >> 1)) + 48 + 2 * (fi & 1), d1 = u2 < 8 ? -96 : 16, d2 = d1 + 16;
    const int b3 = u2 < 8 ? b0 - 64 : CB * (c0 + 1) + 50 + 16 * fi;
#pragma unroll
    for (int r = 0; r < 5; ++r) {
      const int g = r * 64 + l, rho = g / 10;
      const int fb = (rho & 16) ? ((rho & 2) ? b3 : b0 + d2) : ((rho & 2) ? b0 + d1 : b0);
      __builtin_amdgcn_raw_ptr_buffer_load_lds(rwo, (LAS void*)(wo_st + r * 1024), 16, (unsigned)((fb + (rho & 13)) * (D_ * 2) + (g % 10) * 16 + c * 160), 0, 0, 0);
    }
  };
  auto rt_ld = [&](int i0, char* st) {
    const int cb = (i0 < 160 ? 8 : 0) + (i0 & 7), Q = (i0 < 160 ? i0 : i0 - 160) >> 3;
#pragma unroll
    for (int r = 0; r < 7; ++r) {
      const int g = r * 64 + l;
      __builtin_amdgcn_raw_ptr_buffer_load_lds(rwi, (LAS void*)(st + r * 1024), 16, (unsigned)((g / 14) * (F_ * 2) + (g % 14) * 16 + Q * 32 * F_ * 2 + cb * CB * 2),
                                               0, 0, NT);
    }
  };
  auto rt_job = [&](int i0, char* st) {
    const int cb = (i0 < 160 ? 8 : 0) + (i0 & 7), Q = (i0 < 160 ? i0 : i0 - 160) >> 3;
    const int col = 640 * (l >> 5) + 32 * Q + (l & 31);
    DRAIN();
    u32x4 x[7], o[7];
    f32x2 xu[7][4];
    {
      const char* bp = st + (4 * h4 + (n16 >> 2)) * 224 + (n16 & 3) * 8;
#pragma unroll
      for (int j = 0; j < 7; ++j) x[j] = trfrag(bp + j * 32, 224);
#pragma unroll
      for (int j = 0; j < 7; ++j)
#pragma unroll
        for (int e = 0; e < 4; ++e) { xu[j][e] = (f32x2){bflo(x[j][e]), bfhi(x[j][e])}; asm volatile("" : "+v"(xu[j][e])); }
    }
    unsigned mk[4][2], pv = (unsigned)col * 8u, cw = l >= 32 ? (unsigned)(7168 + (l - 32) * 4) : (unsigned)(7298 + l * 2 - 4), m12 = n16 == 12 ? ~0u : 0u;
    unsigned fgo = l < 32 ? (unsigned)(l >> 3) * F4S + (unsigned)(l & 7) * 16u : 0x80000000u;
#pragma unroll
    for (int i = 0; i < 4; ++i) {
      mk[i][0] = n16 == 4 * i ? 0x3F803F80u : 0u; mk[i][1] = n16 == 4 * i + 1 ? ~0u : 0u;
      asm volatile("" : "+v"(mk[i][0]), "+v"(mk[i][1]));
    }
    asm volatile("" : "+v"(pv), "+v"(cw), "+v"(m12), "+v"(fgo));
    unsigned ob = WS_WINF + (unsigned)cb * WBLK + (unsigned)(Q * 7) * 1024u;
    asm volatile("" : "+s"(ob));
    u32x2 pp8[8];
    hold(CB_T);
    _Pragma("unroll 1") for (unsigned spins = 0; spins < SPIN_CAP; ++spins) {
#pragma unroll
      for (int i = 0; i < 8; ++i) pp8[i] = __builtin_amdgcn_raw_buffer_load_b64(rs, pv, WS_MODP + (unsigned)i * 10240u, SC1);
      if (!__any(((ne2(pp8[0][0], pp8[1][0]) | ne2(pp8[2][0], pp8[3][0]) | ne2(pp8[4][0], pp8[5][0])) | ne2(pp8[6][0], pp8[7][0])) != 0u)) break;
      __builtin_amdgcn_s_sleep(4);
    }
    STAMPF(9);
    __builtin_amdgcn_s_setprio(3);
    {
      auto pf = [&](int i) { return __uint_as_float(pp8[i][1]); };
      const float t = rbf(((pf(0) + pf(1)) + (pf(2) + pf(3))) + ((pf(4) + pf(5)) + (pf(6) + pf(7))));
      if (l >= 32) *(float*)(st + cw) = rbf(1.0f + t);
      else *(u16*)(st + cw + 2) = (u16)(__float_as_uint(t) >> 16);
    }
    asm volatile("" ::: "memory");
    const f32x4 ca = *(const volatile LAS f32x4*)(st + 7168 + h4 * 16), cc = *(const volatile LAS f32x4*)(st + 7168 + 64 + h4 * 16);
    const u32x2 e0 = *(const volatile LAS u32x2*)(st + 7296 + h4 * 8), e1 = *(const volatile LAS u32x2*)(st + 7328 + h4 * 8);
    SCHED();
#pragma unroll
    for (int j = 0; j < 7; ++j) {
      const f32x2 y0 = xu[j][0] * (f32x2){ca[0], ca[1]}, y1 = xu[j][1] * (f32x2){ca[2], ca[3]}, y2 = xu[j][2] * (f32x2){cc[0], cc[1]}, y3 = xu[j][3] * (f32x2){cc[2], cc[3]};
      o[j] = (u32x4){pack2(y0[0], y0[1]), pack2(y1[0], y1[1]), pack2(y2[0], y2[1]), pack2(y3[0], y3[1])};
      if (j == 6) {
#pragma unroll
        for (int e = 0; e < 4; ++e) o[j][e] = (o[j][e] & ~m12) | mk[3][0];
      }
      __builtin_amdgcn_raw_buffer_store_b128(o[j], rs, l16 + (unsigned)(j & 3) * 1024u, ob + (unsigned)(j >> 2) * 4096u, SC1);
      SCHED();
    }
    __builtin_amdgcn_s_setprio(0);
    STAMPF(11);
    f32x4 d[2] = {{0.f, 0.f, 0.f, 0.f}, {0.f, 0.f, 0.f, 0.f}};
#pragma unroll
    for (int j = 0; j < 7; ++j) {
      const unsigned one = mk[j & 3][0], sr = mk[j & 3][1];
      d[j >> 2] = MFMA16(((u32x4){one, one, one, one}), o[j], d[j >> 2]);
      d[j >> 2] = MFMA16(((u32x4){sr & e0[0], sr & e0[1], sr & e1[0], sr & e1[1]}), x[j], d[j >> 2]);
    }
#pragma unroll
    for (int m = 0; m < 2; ++m)
      st16(rs, WS_CSP + (unsigned)((cb * 20 + Q) * 2 + m) * 1024u + l16, (u32x4){tag, __float_as_uint(d[m][0]), tag, __float_as_uint(d[m][1])});
    STAMPF(13);
    u32x4 tg4 = {tag, t_start, tag, tag};
    asm volatile("s_waitcnt vmcnt(2)" : "+v"(tg4) :: "memory");
    __builtin_amdgcn_raw_buffer_store_b128(tg4, rs, fgo, WS_F4 + (unsigned)(cb * 20 + Q) * 128u, SC1);
  };

  auto red8 = [&](float v) { v += dpp_f<0x128>(v); return sum_rows(v); };
  const rsrc_t rwm = __builtin_amdgcn_make_buffer_rsrc((void*)p.w_mod, 0, D_ * 3840 * 2, 0x00020000);
  auto gemv_load = [&](u32x4* pre, int blk, int sl, int np) {
    pre[10][0] = *(const unsigned*)(p.cvec + 8 * np * sl + 2 * (l < 4 * np ? l : 0));
#pragma unroll
    for (int pp = 0; pp < np; ++pp)
      pre[pp] = __builtin_amdgcn_raw_buffer_load_b128(rwm, (unsigned)((l >> 3) * 7680 + (l & 7) * 16), (unsigned)((8 * np * sl + pp * 8) * 7680 + blk * 128), NT);
  };
  auto gemv_fin = [&](float* acc, const u32x4* pre, int np, char* scr) {
    const unsigned cv2 = pre[10][0];
    unsigned* svt = (unsigned*)scr + w * 96;
#pragma unroll
    for (int e = 0; e < 2; ++e) {
      const int row = 2 * l + e;
      const float cv = e ? bfhi(cv2) : bflo(cv2);
      if (l < 4 * np) svt[(row & 7) * 12 + (row >> 3)] = __float_as_uint(rbf(cv * __builtin_amdgcn_rcpf(1.0f + __expf(-cv))));
    }
    asm volatile("s_waitcnt lgkmcnt(0)" ::: "memory");
    u32x4 sv[3];
#pragma unroll
    for (int i = 0; i < (np + 3) / 4; ++i) sv[i] = *(const u32x4*)(svt + (l >> 3) * 12 + 4 * i);
    SCHED();
#pragma unroll
    for (int e = 0; e < 8; ++e) acc[e] = 0.f;
#pragma unroll
    for (int pp = 0; pp < np; ++pp) {
      const unsigned sh = sv[pp >> 2][pp & 3], sl2 = sh >> 16;
#pragma unroll
      for (int e = 0; e < 4; ++e) {
        const unsigned x = pre[pp][e];
        acc[2 * e] = __builtin_amdgcn_fdot2_f32_bf16(__builtin_bit_cast(bf2_t, x), __builtin_bit_cast(bf2_t, sl2), acc[2 * e], false);
        acc[2 * e + 1] = __builtin_amdgcn_fdot2_f32_bf16(__builtin_bit_cast(bf2_t, x), __builtin_bit_cast(bf2_t, sh), acc[2 * e + 1], false);
      }
    }
#pragma unroll
    for (int e = 0; e < 8; ++e) acc[e] = red8(acc[e]);
  };
  auto gemv_put = [&](const float* acc, unsigned off) {
    float v = acc[0];
#pragma unroll
    for (int e = 1; e < 8; ++e) { v = (l >> 3) == e ? acc[e] : v; asm("" : "+v"(v)); }
    st8(rs, off + (unsigned)l * 8u, (u32x2){tag, (unsigned)__builtin_amdgcn_ds_bpermute(((l & 7) * 8 + (l >> 3)) * 4, (int)__float_as_uint(v))});
  };
  auto cs_ld = [&](u32x4* v, int cj) {
    const int cb = cj / 7, j = cj % 7;
#pragma unroll
    for (int k = 0; k < 5; ++k)
      v[k] = __builtin_amdgcn_raw_buffer_load_b128(rs, (unsigned)(h4 * 10240 + n16 * 16), WS_CSP + (unsigned)((cb * 40 + (j >> 2) + 2 * k) * 1024 + (j & 3) * 256), SC1);
  };
  auto cs_fin = [&](u32x4* v, int cj) {
    auto bad = [&]() { return __any(((ne2(v[0][0], v[0][2]) | ne2(v[1][0], v[1][2]) | ne2(v[2][0], v[2][2])) | ne2(v[3][0], v[3][2]) | ne2(v[4][0], v[4][2])) != 0u); };
    if (bad()) {
      _Pragma("unroll 1") for (unsigned spins = 0; spins < SPIN_CAP; ++spins) {
        __builtin_amdgcn_s_sleep(8);
        cs_ld(v, cj);
        if (!bad()) break;
      }
    }
    float t0 = 0.f, t1 = 0.f;
#pragma unroll
    for (int k = 0; k < 5; ++k) { t0 += __uint_as_float(v[k][1]); t1 += __uint_as_float(v[k][3]); }
    t0 = sum_rows(t0); t1 = sum_rows(t1);
    if (l < 16) st16(rs, WS_CS + (unsigned)((cj / 7) * 2048 + (cj % 7) * 256) + l16, (u32x4){tag, __float_as_uint(t0), tag, __float_as_uint(t1)});
  };
  const bool cs2 = w == 2 && R >= 20 && R < 27, cs1 = w == 2 && R >= 25;
  u32x4 csv[5];
  typedef const __attribute__((address_space(4))) unsigned* cptr_t;

  if (__builtin_expect(is_conv, 1)) {
    if (w >= 4) {
      TOUCH_AT(1);
      const int pw = w - 4;
      const rsrc_t ra = __builtin_amdgcn_make_buffer_rsrc((void*)((idx & 2) ? p.v : p.k), 0, S_ * D_ * 2, 0x00020000);
      const rsrc_t rb = __builtin_amdgcn_make_buffer_rsrc((void*)((idx & 2) ? p.k : p.v), 0, S_ * D_ * 2, 0x00020000);
      const rsrc_t rk = __builtin_amdgcn_make_buffer_rsrc((void*)p.k, 0, S_ * D_ * 2, 0x00020000);
      const rsrc_t rv = __builtin_amdgcn_make_buffer_rsrc((void*)p.v, 0, S_ * D_ * 2, 0x00020000);
#define NROT 2
      const unsigned vo = (unsigned)((128 * pw + 2 * l) * (D_ * 2));
      const unsigned so0 = (unsigned)((idx & 1) * (D_ * 2) + quad * 8), so1 = (unsigned)((1 - (idx & 1)) * (D_ * 2) + quad * 8);
      u32x2 g[3][4], kh;
      auto gl = [&](int s, int j0, int j1) {
        __builtin_amdgcn_s_setprio(0);
#pragma unroll
        for (int j = j0; j < j1; ++j) {
          if (s < NROT) g[s][j] = __builtin_amdgcn_raw_buffer_load_b64(j < 2 ? ra : rb, vo, ((j & 1) ? so1 : so0) + (unsigned)(s * 512 * D_ * 2), 0);
          else g[s][j] = __builtin_amdgcn_raw_buffer_load_b64(j < 2 ? rk : rv, vo + (unsigned)((j & 1) * D_ * 2), (unsigned)(quad * 8 + s * 512 * D_ * 2), 0);
          SCHED();
        }
        __builtin_amdgcn_s_setprio(3);
      };
      const cptr_t sp = (cptr_t)(p.shift_k + quad * 16);
      unsigned spv[8];
#pragma unroll
      for (int i = 0; i < 8; ++i) spv[i] = sp[i];
      __builtin_amdgcn_s_sleep(3);
      gl(0, 0, 4);
      {
        const int row = 512 * (l >> 2) + 128 * pw - 3 + (l & 3);
        kh = __builtin_amdgcn_raw_buffer_load_b64(rk, (l >= 12 || (l & 3) == 3 || row < 0) ? 0x80000000u : (unsigned)(row * D_ * 2 + quad * 8), 0, 0);
      }
      TAG_READY();
      f32x2 wt[2][4];
#pragma unroll
      for (int d = 0; d < 2; ++d) {
        wt[d][0] = (f32x2){bflo(spv[4 * d]), bflo(spv[4 * d + 2])}; wt[d][1] = (f32x2){bfhi(spv[4 * d]), bfhi(spv[4 * d + 2])};
        wt[d][2] = (f32x2){bflo(spv[4 * d + 1]), bflo(spv[4 * d + 3])}; wt[d][3] = (f32x2){bfhi(spv[4 * d + 1]), bfhi(spv[4 * d + 3])};
        asm volatile("" : "+s"(wt[d][0]), "+s"(wt[d][1]), "+s"(wt[d][2]), "+s"(wt[d][3]));
      }
      SCHED();
      {
        const int j = idx * 4 + pw;
        const unsigned o = (unsigned)((512 + 64 * (j >> 2) + l) * (D_ * 2) + (((160 * xcd) >> 7) + ((j >> 1) & 1)) * 128);
        __builtin_amdgcn_raw_ptr_buffer_load_lds((j & 1) ? ra : rb, (LAS void*)(lds + 98304), 4, j < 64 ? o : 0x80000000u, 0, 0, 0);
      }
      SCHED();
      gl(1, 0, 4);
      STAMP(21);
      unsigned up = (unsigned)(L_U + 512 + 256 * pw + (l >> 2) * 16 + (3 - (l & 3)) * 4);
      unsigned lfo = l == 0 ? (unsigned)(L_FLG + 4 * pw) : (unsigned)(100352 + tid * 4), lfr = (unsigned)(L_FLG + (l & 3) * 4);
      unsigned yr = (unsigned)(L_U + (n16 >> 2) * U_CH + (128 * pw + 32 * h4 + 8 * (n16 & 1) + 4 * ((n16 >> 1) & 1)) * 2);
      unsigned yo = (unsigned)(8 * pw + (l >> 3)) * 20480u + (unsigned)quad * 128u + (unsigned)(l & 7) * 16u;
      unsigned mo = (unsigned)((n16 < 12 ? n16 >> 2 : 0) * D_ + quad * 4 + (l & 3)) * 8u + (unsigned)h4 * 40960u;
      unsigned k3o = (unsigned)(16 * pw + (l >> 2)) * 20480u + (unsigned)(((l >> 1) & 1) * 10240 + (2 * quad + (l & 1)) * 16);
      unsigned k3d = (unsigned)(L_U + (2 * (l & 1) + ((l >> 1) & 1)) * U_CH + 512 + (1536 + 128 * pw + 8 * (l >> 2)) * 2);
      unsigned xfo = (unsigned)(((4 * (l >> 5) + pw) * 4 + ((l >> 3) & 3)) * 160 + quad) * 128u + (unsigned)(l & 7) * 16u;
      u32x4 tg4 = {tag, tag, tag, tag};
      asm volatile("" : "+v"(up), "+v"(lfo), "+v"(lfr), "+v"(yr), "+v"(yo), "+v"(mo), "+v"(k3o), "+v"(k3d), "+v"(xfo), "+v"(tg4));
      unsigned so = WS_ST1 + (unsigned)xcd * 16384u + (unsigned)pw * 1024u;
      asm volatile("" : "+s"(so));
      auto pf_set = [&](int i) { asm volatile("" ::: "memory"); *(volatile LAS unsigned*)(lds + lfo + 4 * i) = tg4[0]; };
      auto pf_wait = [&](int i0) {
        _Pragma("unroll 1") for (unsigned spins = 0; spins < (SPIN_CAP << 4); ++spins) {
          if (__all(*(volatile LAS unsigned*)(lds + lfr + 4 * i0) == tag)) break;
          __builtin_amdgcn_s_sleep(1);
        }
        asm volatile("" ::: "memory");
      };
      unsigned hl[2][3];
      auto halo = [&](int s) {
#pragma unroll
        for (int i = 0; i < 6; ++i) { hl[i & 1][i >> 1] = (unsigned)__builtin_amdgcn_readlane(kh[i & 1], 4 * s + (i >> 1)); asm volatile("" : "+v"(hl[i & 1][i >> 1])); }
      };
      auto kv = [&](auto S__, auto N__) {
        constexpr int s = decltype(S__)::value;
        u32x2* x = g[s];
        SCHED();
        asm volatile("s_waitcnt vmcnt(%5)" : "+v"(x[0]), "+v"(x[1]), "+v"(x[2]), "+v"(x[3]), "+v"(kh) : "n"(decltype(N__)::value));
        if (s == 2) STAMP(27);
        if (s == 0) halo(0);
        if (s < NROT) {
          if (idx & 1) { asm volatile(""); const u32x2 t = x[0], u = x[2]; x[0] = x[1]; x[1] = t; x[2] = x[3]; x[3] = u; }
          asm volatile("" : "+v"(x[0]), "+v"(x[1]), "+v"(x[2]), "+v"(x[3]));
          if (idx & 2) { asm volatile(""); const u32x2 t = x[0], u = x[1]; x[0] = x[2]; x[1] = x[3]; x[2] = t; x[3] = u; }
          asm volatile("" : "+v"(x[0]), "+v"(x[1]), "+v"(x[2]), "+v"(x[3]));
        }
        f32x2 kf[2][5], o[2][2];
#pragma unroll
        for (int d = 0; d < 2; ++d) {
          unsigned kk[5];
          kk[3] = x[0][d]; kk[4] = x[1][d];
          kk[2] = __builtin_amdgcn_update_dpp(hl[d][2], kk[4], 0x138, 0xf, 0xf, false);
          kk[1] = __builtin_amdgcn_update_dpp(hl[d][1], kk[3], 0x138, 0xf, 0xf, false);
          kk[0] = __builtin_amdgcn_update_dpp(hl[d][0], kk[2], 0x138, 0xf, 0xf, false);
#pragma unroll
          for (int j = 0; j < 5; ++j) kf[d][j] = (f32x2){bflo(kk[j]), bfhi(kk[j])};
        }
#pragma unroll
        for (int t = 0; t < 5; ++t) {
#pragma unroll
          for (int i = 0; i < 4; ++i) {
            const int d = i >> 1, r = i & 1;
            if (t == 4) o[d][r] *= (f32x2){bflo(x[2 + r][d]), bfhi(x[2 + r][d])};
            else if (t == 0) o[d][r] = kf[d][r + 3] * wt[d][0];
            else o[d][r] = __builtin_elementwise_fma(kf[d][r + 3 - t], wt[d][t], o[d][r]);
          }
          asm volatile("" : "+v"(o[0][0]), "+v"(o[0][1]), "+v"(o[1][0]), "+v"(o[1][1]));
        }
#pragma unroll
        for (int i = 0; i < 4; ++i) *(unsigned*)(lds + up + i * U_CH + s * 1024) = pack2(o[i >> 1][1][i & 1], o[i >> 1][0][i & 1]);
        pf_set(4 * s);
        SCHED();
        if (s < 2) halo(s + 1);
        SCHED();
      };
      u32x4 sv[4], qv[4];
      u32x2 mv[2];
      auto ld_e = [&]() {
#pragma unroll
        for (int s = 0; s < 4; ++s) sv[s] = __builtin_amdgcn_raw_buffer_load_b128(rs, l16, so + (unsigned)s * 4096u, SC1);
        SCHED();
      };
      auto s_bad = [&]() { return __any(((ne2(sv[0][1], sv[0][3]) | ne2(sv[1][1], sv[1][3]) | ne2(sv[2][1], sv[2][3])) | ne2(sv[3][1], sv[3][3])) != 0u); };
      kv(IC<0>(), IC<5>());
      STAMP(23);
#define G2A 4
      gl(2, 0, G2A);
      kv(IC<1>(), IC<G2A>());
      gl(2, G2A, 4);
      STAMPF(9);
      u32x4 k3[2];
      auto ld_k3 = [&]() {
        k3[0] = __builtin_amdgcn_raw_buffer_load_b128(rs, k3o, WS_KV3, SC1);
        k3[1] = __builtin_amdgcn_raw_buffer_load_b128(rs, k3o, WS_KV3 + 5120u, SC1);
      };
      hold(K_T);
      ld_k3();
      SCHED();
      ld_e();
      kv(IC<2>(), IC<6>());
      STAMPF(11);
      auto k_bad = [&]() { return __any((ne2(k3[0][1], k3[0][3]) | ne2(k3[1][1], k3[1][3])) != 0u); };
      if (k_bad()) {
        _Pragma("unroll 1") for (unsigned spins = 0; spins < SPIN_CAP; ++spins) {
          __builtin_amdgcn_s_sleep(4);
          ld_k3();
          if (!k_bad()) break;
        }
      }
      *(u32x4*)(lds + k3d) = (u32x4){k3[0][0], k3[0][2], k3[1][0], k3[1][2]};
      pf_set(12);
      SCHED();
      STAMPF(18);
      if (s_bad()) {
        _Pragma("unroll 1") for (unsigned spins = 0; spins < SPIN_CAP; ++spins) {
          __builtin_amdgcn_s_sleep(4);
          ld_e();
          if (!s_bad()) break;
        }
      }
      asm volatile("" ::: "memory");
      SCHED();
      STAMPF(17);
      auto ld_m = [&]() {
        mv[0] = __builtin_amdgcn_raw_buffer_load_b64(rs, mo, WS_MODQ, SC1);
        mv[1] = __builtin_amdgcn_raw_buffer_load_b64(rs, mo, WS_MODQ + 20480u, SC1);
      };
#define MFIRST 1
      if (MFIRST) ld_m();
      SCHED();
#pragma unroll
      for (int s = 0; s < 4; ++s) qv[s] = __builtin_amdgcn_raw_buffer_load_b128(rs, yo, WS_QB + (unsigned)s * (32u * 20480u), BULK_AUX);
      SCHED();
      if (!MFIRST) ld_m();
      SCHED();
      STAMP(2);
      u32x2 yv[4][2];
      auto ld_y = [&](int s) {
#pragma unroll
        for (int e = 0; e < 2; ++e) yv[s][e] = trd(lds + yr + 1024 * s + 32 * e);
      };
      float r1[4][2], m1[4][2];
#pragma unroll
      for (int i = 0; i < 8; ++i) {
        const unsigned x = sv[i >> 1][2 * (i & 1)];
        r1[i >> 1][i & 1] = __builtin_amdgcn_rcpf(bfhi(x));
        m1[i >> 1][i & 1] = -bflo(x) * r1[i >> 1][i & 1];
        asm volatile("" : "+v"(r1[i >> 1][i & 1]), "+v"(m1[i >> 1][i & 1]));
      }
      pf_wait(20);
      ld_y(0); ld_y(1);
      SCHED();
      if (__any(ne2(mv[0][0], mv[1][0]) != 0u)) {
        _Pragma("unroll 1") for (unsigned spins = 0; spins < SPIN_CAP; ++spins) {
          __builtin_amdgcn_s_sleep(4);
          ld_m();
          if (!__any(ne2(mv[0][0], mv[1][0]) != 0u)) break;
        }
      }
      STAMPF(13);
      f32x4 al, be;
      {
        const float mgf = rbf(sum_rows(__uint_as_float(mv[0][1]) + __uint_as_float(mv[1][1])));
        auto rl = [&](int i) { return __uint_as_float(__builtin_amdgcn_readlane(__float_as_uint(mgf), i)); };
        const f32x4 sa = {rl(0), rl(1), rl(2), rl(3)}, ca = {rl(4), rl(5), rl(6), rl(7)}, ga = {rl(8), rl(9), rl(10), rl(11)};
        al = rbf4(ca + 1.0f) * ga;
        be = sa * ga;
        asm volatile("" : "+v"(al), "+v"(be));
      }
      f32x4 qf[2][2], P[2][2];
      auto xpre = [&](int s) {
#pragma unroll
        for (int e = 0; e < 2; ++e) {
          const float r = r1[s][e], m = m1[s][e];
          const f32x4 qq = {bflo(qv[s][2 * e]), bfhi(qv[s][2 * e]), bflo(qv[s][2 * e + 1]), bfhi(qv[s][2 * e + 1])};
          qf[s & 1][e] = qq;
          P[s & 1][e] = __builtin_elementwise_fma(__builtin_elementwise_fma(qq, (f32x4){r, r, r, r}, (f32x4){m, m, m, m}), al, be);
        }
        asm volatile("" : "+v"(qf[s & 1][0]), "+v"(qf[s & 1][1]), "+v"(P[s & 1][0]), "+v"(P[s & 1][1]));
      };
      auto xfin = [&](int s) {
        u32x4 o;
#pragma unroll
        for (int e = 0; e < 2; ++e) {
          const u32x2 y = yv[s][e];
          const f32x4 yf = {bflo(y[0]), bfhi(y[0]), bflo(y[1]), bfhi(y[1])};
          const f32x4 x = __builtin_elementwise_fma(P[s & 1][e], yf, qf[s & 1][e]);
          o[2 * e] = pack2(x[0], x[1]);
          o[2 * e + 1] = pack2(x[2], x[3]);
        }
        __builtin_amdgcn_raw_buffer_store_b128(o, rs, yo, WS_X1B + (unsigned)s * (32u * 20480u), SC1);
      };
      xpre(0); xfin(0);
      xpre(1); xfin(1);
      SCHED();
      xpre(2); xpre(3);
#define FVR 5
#define MSPLIT 0
      if (MSPLIT) {
        pf_wait(24);
        ld_y(2);
        xfin(2);
        SCHED();
        pf_wait(28);
        ld_y(3);
        xfin(3);
      } else {
        pf_wait(28);
        ld_y(2); ld_y(3);
        xfin(2); xfin(3);
      }
      STAMP(28);
      asm volatile("s_waitcnt vmcnt(2)" ::: "memory");
      __builtin_amdgcn_raw_buffer_store_b128(tg4, rs, xfo, WS_FX, SC1);
      SCHED();
      asm volatile("s_waitcnt vmcnt(1)" ::: "memory");
      __builtin_amdgcn_raw_buffer_store_b128(tg4, rs, xfo, WS_FX + 32u * 160u * 128u, SC1);
      STAMP(5);
    } else {
      const int ch = w;
      char* U = lds + L_U + ch * U_CH;
      char* G = lds + ch * (4 * G_COPY);
      {
        const rsrc_t rf = __builtin_amdgcn_make_buffer_rsrc((void*)(p.ssm_k + (size_t)(quad * 4 + ch) * S_), 0, S_ * 2, 0x00020000);
        u32x4 fa[4];
        u32x2 fb[4];
        __builtin_amdgcn_s_setprio(3);
#pragma unroll
        for (int k = 0; k < 4; ++k) {
          fa[k] = __builtin_amdgcn_raw_buffer_load_b128(rf, l16 + (unsigned)k * 1024u, 0, NT);
        }
        __builtin_amdgcn_s_setprio(1);
        TAG_READY();
#pragma unroll
        for (int k = 0; k < 4; ++k) {
#pragma unroll
          for (int d = 0; d < 2; ++d)
            fb[k][d] = __builtin_amdgcn_update_dpp(k < 3 ? (unsigned)__builtin_amdgcn_readfirstlane(fa[k < 3 ? k + 1 : k][d]) : 0u, fa[k][d], 0x130, 0xf, 0xf, false);
          const unsigned x[6] = {fa[k][0], fa[k][1], fa[k][2], fa[k][3], fb[k][0], fb[k][1]};
          unsigned y[5];
#pragma unroll
          for (int d = 0; d < 5; ++d) y[d] = __builtin_amdgcn_alignbit(x[d + 1], x[d], 16);
          char* gp = G + 32 + k * 1024 + l16;
          *(u32x4*)(gp) = fa[k];
          *(u32x4*)(gp + G_COPY) = (u32x4){y[0], y[1], y[2], y[3]};
          *(u32x4*)(gp + 2 * G_COPY) = (u32x4){x[1], x[2], x[3], x[4]};
          *(u32x4*)(gp + 3 * G_COPY) = (u32x4){y[1], y[2], y[3], y[4]};
          if (k == 0) {
            const unsigned f0 = __builtin_amdgcn_readfirstlane(x[0]), f1 = __builtin_amdgcn_readfirstlane(x[1]);
            const unsigned long long f64 = (unsigned long long)f0 | ((unsigned long long)f1 << 32);
            const unsigned long long hi = l ? (f64 << (64 - 16 * (l & 3))) : 0ull;
            if (l < 4) {
              *(u32x4*)(G + l * G_COPY) = (u32x4){0u, 0u, 0u, 0u};
              *(u32x4*)(G + l * G_COPY + 16) = (u32x4){0u, 0u, (unsigned)hi, (unsigned)(hi >> 32)};
            }
            if (l >= 32) *(u32x4*)(U + (l - 32) * 16) = (u32x4){0u, 0u, 0u, 0u};
          }
        }
        asm volatile("s_waitcnt lgkmcnt(0)" ::: "memory");
        STAMP(1);
      }
      const int a = n16, h = h4, jj = h >> 1, e2 = h & 1;
      const char* ap = G + ((a + 1) & 3) * G_COPY + 2 * (16 * jj + a - 8 * e2 + 9 - ((a + 1) & 3));
      const char* bp = U + 512 + 32 * (a - jj) + 16 * e2;
      auto lda = [&](u32x4* A, int r, int p0, int np) {
#pragma unroll
        for (int pp = 0; pp < np; ++pp) {
          u32x2 lo = *(const volatile LAS u32x2*)(ap + 512 * (p0 + pp) + 64 * r);
          u32x2 hi = *(const volatile LAS u32x2*)(ap + 512 * (p0 + pp) + 64 * r + 8);
          A[pp] = (u32x4){lo[0], lo[1], hi[0], hi[1]};
        }
      };
      u32x4 AD;
      {
        const unsigned dDr = (*(cptr_t)(p.ssm_D + quad * 4 + (ch & 2)) >> (16 * (ch & 1))) & 0xffffu;
        const unsigned dv = h == (a >> 3) ? ((a & 1) ? dDr : dDr << 16) : 0u;
        const int dw = (7 - (a & 7)) >> 1;
#pragma unroll
        for (int d = 0; d < 4; ++d) AD[d] = dw == d ? dv : 0u;
      }
      u32x4 A0[8][4];
      f32x4 acc[8];
#pragma unroll
      for (int n = 0; n < 8; ++n) acc[n] = (f32x4){0.f, 0.f, 0.f, 0.f};
#pragma unroll
      for (int r = 0; r < 8; ++r) lda(A0[r], r, 0, 4);
      auto yput = [&](int s) {
#pragma unroll
        for (int e = 0; e < 2; ++e)
          *(u32x2*)(U + 1024 * s + 512 * e + (16 * a + 4 * h) * 2) = (u32x2){pack2(acc[2 * s + e][0], acc[2 * s + e][1]), pack2(acc[2 * s + e][2], acc[2 * s + e][3])};
        asm volatile("" ::: "memory");
        if (l == 0) lf[16 + 4 * s + ch] = tag;
      };
      auto stage = [&](auto S__) {
        constexpr int s = decltype(S__)::value, NH = s < 2 ? 4 - 2 * s : 0, NP = 8 - 2 * s, NB = s < 2 ? 2 : 8;
        u32x4 A1[3][NH > 0 ? NH : 1], B[NB][2];
        auto ldb = [&](int r) {
#pragma unroll
          for (int e = 0; e < 2; ++e) B[r % NB][e] = *(const u32x4*)(bp + 512 * (2 * s + e) - 64 * r);
        };
        if (NH) { lda(A1[0], 0, 4, NH); lda(A1[1], 1, 4, NH); }
        SCHED();
        if (s > 0) yput(s - 1);
        lf_wait(4 * s, 4);
        if (s == 0) STAMP(3);
#pragma unroll
        for (int r = 0; r < NB; ++r) ldb(r);
        SCHED();
#pragma unroll
        for (int e = 0; e < 2; ++e) acc[2 * s + e] = MFMA16(AD, B[0][e], acc[2 * s + e]);
#pragma unroll
        for (int r = 0; r < 8; ++r) {
          if (NH && r + 2 < 8) lda(A1[(r + 2) % 3], r + 2, 4, NH);
          SCHED();
#pragma unroll
          for (int e = 0; e < 2; ++e)
#pragma unroll
            for (int pp = 0; pp < NP - e; ++pp) acc[2 * s + e + pp] = MFMA16(pp < 4 ? A0[r][pp] : A1[r % 3][pp < 4 ? 0 : pp - 4], B[r % NB][e], acc[2 * s + e + pp]);
          SCHED();
          if (r + NB < 8) ldb(r + NB);
        }
      };
      stage(IC<0>());
      STAMP(4);
      stage(IC<1>());
      STAMPF(9);
      u32x2 fv;
      if (MSPLIT) {
        u32x4 B[8][2];
        auto ldb = [&](int r, int m0) {
#pragma unroll
          for (int m = 0; m < 2; ++m) B[r][m] = *(const u32x4*)(bp + 512 * (m0 + m) - 64 * r);
        };
        yput(1);
        if (w == 3) wo_ld();
        __builtin_amdgcn_s_setprio(3);
        lf_wait(8, 4);
        STAMP(22);
#pragma unroll
        for (int r = 0; r < 8; ++r) ldb(r, 4);
        SCHED();
#pragma unroll
        for (int m = 0; m < 2; ++m) acc[4 + m] = MFMA16(AD, B[0][m], acc[4 + m]);
#pragma unroll
        for (int r = 0; r < 8; ++r) {
          acc[4] = MFMA16(A0[r][0], B[r][0], acc[4]);
          acc[5] = MFMA16(A0[r][1], B[r][0], acc[5]);
          acc[5] = MFMA16(A0[r][0], B[r][1], acc[5]);
          SCHED();
        }
#pragma unroll
        for (int r = 0; r < 8; ++r) {
          acc[6] = MFMA16(A0[r][2], B[r][0], acc[6]);
          acc[7] = MFMA16(A0[r][3], B[r][0], acc[7]);
          acc[6] = MFMA16(A0[r][1], B[r][1], acc[6]);
          acc[7] = MFMA16(A0[r][2], B[r][1], acc[7]);
          SCHED();
          if (r == 0) yput(2);
          if (r == FVR) fv = ld8(rs, b2o);
          if (r == 3) {
            lf_wait(12, 4);
#pragma unroll
            for (int x = 0; x < 4; ++x) ldb(x, 6);
          }
          if (r > 3) ldb(r, 6);
          SCHED();
        }
#pragma unroll
        for (int m = 0; m < 2; ++m) acc[6 + m] = MFMA16(AD, B[0][m], acc[6 + m]);
#pragma unroll
        for (int r = 0; r < 8; ++r) {
          acc[6] = MFMA16(A0[r][0], B[r][0], acc[6]);
          acc[7] = MFMA16(A0[r][1], B[r][0], acc[7]);
          acc[7] = MFMA16(A0[r][0], B[r][1], acc[7]);
          SCHED();
        }
        yput(3);
      } else {
        u32x4 B[4][4];
        auto ldb = [&](int r) {
#pragma unroll
          for (int m = 0; m < 4; ++m) B[r & 3][m] = *(const u32x4*)(bp + 512 * (4 + m) - 64 * r);
        };
        yput(1);
        if (w == 3) wo_ld();
        __builtin_amdgcn_s_setprio(3);
        lf_wait(8, 8);
        STAMP(22);
#pragma unroll
        for (int r = 0; r < 4; ++r) ldb(r);
        SCHED();
#pragma unroll
        for (int m = 0; m < 4; ++m) acc[4 + m] = MFMA16(AD, B[0][m], acc[4 + m]);
#pragma unroll
        for (int r = 0; r < 8; ++r) {
#pragma unroll
          for (int m = 0; m < 4; ++m)
#pragma unroll
            for (int pp = 0; pp < 4 - m; ++pp) acc[4 + m + pp] = MFMA16(A0[r][pp], B[r & 3][m], acc[4 + m + pp]);
          SCHED();
          if (r + 4 < 8) ldb(r + 4);
          if (r == 1) fv = ld8(rs, b2o);
          SCHED();
        }
#pragma unroll
        for (int e = 0; e < 4; ++e)
          *(u32x2*)(U + 2048 + 512 * e + (16 * a + 4 * h) * 2) = (u32x2){pack2(acc[4 + e][0], acc[4 + e][1]), pack2(acc[4 + e][2], acc[4 + e][3])};
        lf_set(28 + ch);
      }
      STAMP(28);
      __builtin_amdgcn_s_setprio(0);
      b2_wait(fv, 0);
      lf_wait(20, 4);
    }
    __builtin_amdgcn_s_setprio(0);
  } else {
    const int at = idx - 20;
    char* const scr = lds + WBLK + 14848;
    auto gv_blk = [&](int j) { return j < 160 ? 30 + (j >> 3) : j < 400 ? (j >> 3) - 20 : j >> 3; };
    auto gv_fin = [&](const u32x4* pre, int j) {
      float acc[8];
      gemv_fin(acc, pre, 10, scr);
      gemv_put(acc, j < 160 ? WS_MODP + (unsigned)((j & 7) * 1280 + (j >> 3) * 64) * 8u : WS_MODQ + (unsigned)((j & 7) * 2560 + ((j >> 3) - 20) * 64) * 8u);
    };
    const int ja = xcd * 12 + at, ai = xcd * 8 + at, bi = xcd * 4 + at - 8, g = w >> 2, wj = w & 3;
    const int rj = w == 0 ? ja : w == 1 ? (at >= 4 ? 92 + ai : -1) : w == 4 ? 160 + ja : w == 5 && at < 8 ? 256 + ai : -1;
    char* const rst = lds + (g ? WBLK : 65536) + wj * 7424;
    u32x4 pre[11];
    if (at >= 8 || g == 0) {
      TOUCH_AT(2);
      const int T = at >= 8 ? 64 + 2 * bi + g : ai;
      unsigned* mds = (unsigned*)(lds + 2048) + 16 * g;
      char* qt = lds + L_RT + g * (16 * 1312);
      u32x4 ld[5];
      const bool e1 = at >= 8 && w == 3;
      if (e1) gemv_load(pre, gv_blk(128 + bi), bi & 7, 10);
      else __builtin_amdgcn_s_sleep(8);
#pragma unroll
      for (int j = 0; j < 5; ++j)
        ld[j] = __builtin_amdgcn_raw_buffer_load_b128(rq, (unsigned)(h4 * D_ * 2 + n16 * 16 + j * 256), (unsigned)((32 * (T >> 1) + 8 * wj + 4 * (T & 1)) * D_ * 2), NT);
      TAG_READY();
      {
        const bf2_t ONE2 = {(__bf16)1.0f, (__bf16)1.0f};
        char* dst = qt + (4 * wj + h4) * 1312 + n16 * 16;
        float sa = 0.f, va = 0.f;
#pragma unroll
        for (int j = 0; j < 5; ++j) {
          *(u32x4*)(dst + j * 256) = ld[j];
#pragma unroll
          for (int d = 0; d < 4; ++d) {
            const unsigned a0 = ld[j][d];
            const bf2_t xa = __builtin_bit_cast(bf2_t, a0);
            sa = __builtin_amdgcn_fdot2_f32_bf16(xa, ONE2, sa, false); va = __builtin_amdgcn_fdot2_f32_bf16(xa, xa, va, false);
          }
        }
        lf_set(8 + w);
        const float ma = sum16(sa) * (1.0f / D_);
        va = __builtin_fmaxf(sum16(va) * (1.0f / D_) - ma * ma, 0.f);
        if (n16 == 0) mds[4 * wj + h4] = pack2(ma, __builtin_amdgcn_sqrtf(rbf(rbf(va) + 1e-5f)));
      }
      if (e1) gv_fin(pre, 128 + bi);
      lf_wait(8 + 4 * g, 4);
      STAMP(23);
      {
        const char* src = qt + (l & 7) * 1312 + (40 * wj + (l >> 3)) * 8;
#pragma unroll
        for (int j = 0; j < 5; ++j) {
          const u32x2 x = *(const u32x2*)(src + j * 64), y = *(const u32x2*)(src + j * 64 + 8 * 1312);
          st16(rs, WS_QB + (unsigned)T * 20480u + (unsigned)(5 * wj + j) * 1024u + l16, (u32x4){x[0], x[1], y[0], y[1]});
        }
      }
      SCHED();
      const int g2 = at < 8 || w < 3 || w == 4 ? 400 : w == 3 ? 384 + bi : 256 + wj * 32 + bi;
      if (g2 < 400) {
        gemv_load(pre, gv_blk(g2), g2 & 7, 10);
        asm volatile("s_waitcnt vmcnt(10)" ::: "memory");
      } else if (w < 2 && rj >= 0) {
        rt_ld(rj, rst);
        asm volatile("s_waitcnt vmcnt(7)" ::: "memory");
      } else {
        DRAIN();
      }
      lf_set(16 + w);
      if (wj == 2) {
        lf_wait(16 + 4 * g, 4);
        st16(rs, WS_ST1 + (unsigned)(l >> 3) * 16384u + (unsigned)T * 128u + (unsigned)(l & 7) * 16u, (u32x4){mds[l & 7], tag, mds[8 + (l & 7)], tag});
        STAMP(22);
      }
      if (w >= 4 && w < 7) lf_set(w);
      if (g2 < 400) gv_fin(pre, g2);
      STAMP(25);
    } else {
      constexpr int KB = L_RT + 16 * 1312;
      TOUCH_AT(3);
      const int g1 = w < 6 ? 2 * ai + wj : 32 + wj * 64 + ai;
      if (w >= 6) {
        const rsrc_t rk = __builtin_amdgcn_make_buffer_rsrc((void*)(w == 6 ? p.k : p.v), 0, S_ * D_ * 2, 0x00020000);
        const unsigned ko = (unsigned)((1536 + 8 * ai) * (D_ * 2) - (w == 6 ? 4096 : 0));
        char* dst = lds + KB + (w == 6 ? 0 : 14336);
        __builtin_amdgcn_s_sleep(3);
#pragma unroll
        for (int n = 0; n < 10; ++n) __builtin_amdgcn_raw_ptr_buffer_load_lds(rk, (LAS void*)(dst + n * 1024), 16, l16, ko + (unsigned)n * 1024u, 0, NT);
        if (w == 6) {
#pragma unroll
          for (int n = 10; n < 14; ++n) __builtin_amdgcn_raw_ptr_buffer_load_lds(rk, (LAS void*)(dst + n * 1024), 16, l16, ko + (unsigned)n * 1024u, 0, NT);
        }
      }
      SCHED();
      u32x4 swt[2];
      swt[0] = *(const u32x4*)(p.shift_k + (tid - 256) * 8);
      swt[1] = *(const u32x4*)(p.shift_k + (256 + l) * 8);
      SCHED();
      if (w >= 6) __builtin_amdgcn_s_sleep(8);
      gemv_load(pre, gv_blk(g1), g1 & 7, 10);
      TAG_READY();
      asm volatile("s_waitcnt vmcnt(11)" : "+v"(swt[0]), "+v"(swt[1]) :: "memory");
      lf_set(8 + w);
      if (w < 6) gv_fin(pre, g1);
      STAMP(25);
      lf_wait(12, 4);
      STAMP(23);
#pragma unroll 1
      for (int n = 0; n < (w == 7 ? 2 : 1); ++n) {
        {
          const int pr = n ? 256 + l : tid - 256;
          const char* kp = lds + KB + 4096 + pr * 4;
          unsigned kk[11], vv[8];
#pragma unroll
          for (int i = 0; i < 11; ++i) kk[i] = *(const unsigned*)(kp + (i - 3) * 1280);
#pragma unroll
          for (int i = 0; i < 8; ++i) vv[i] = *(const unsigned*)(kp + 10240 + i * 1280);
          const u32x4 sw = n ? swt[1] : swt[0];
          const f32x2 wt[4] = {{bflo(sw[0]), bflo(sw[2])}, {bfhi(sw[0]), bfhi(sw[2])}, {bflo(sw[1]), bflo(sw[3])}, {bfhi(sw[1]), bfhi(sw[3])}};
          f32x2 o[8];
#pragma unroll
          for (int i = 0; i < 8; ++i) {
            f32x2 t = (f32x2){bflo(kk[i + 3]), bfhi(kk[i + 3])} * wt[0];
#pragma unroll
            for (int j = 1; j < 4; ++j) t = __builtin_elementwise_fma((f32x2){bflo(kk[i + 3 - j]), bfhi(kk[i + 3 - j])}, wt[j], t);
            o[i] = t * (f32x2){bflo(vv[i]), bfhi(vv[i])};
          }
#pragma unroll
          for (int e = 0; e < 4; ++e) {
            const int i = 7 - 4 * (e & 1);
            st16(rs, WS_KV3 + (unsigned)ai * 20480u + (unsigned)(e * 5120 + pr * 16),
                 (u32x4){pack2(o[i][e >> 1], o[i - 1][e >> 1]), tag, pack2(o[i - 2][e >> 1], o[i - 3][e >> 1]), tag});
          }
        }
      }
      STAMP(22);
      if (w < 7) lf_set(w);
      if (w >= 6) gv_fin(pre, g1);
      STAMPF(30);
    }
    if (w == 6 && ja < 80) {
      hold(AUX_T);
      gemv_load(pre, gv_blk(400 + ja), ja & 7, 10);
      gv_fin(pre, 400 + ja);
    }
    if (rj >= 0) {
      if (w >= 2) { hold(RT2_T); rt_ld(rj, rst); }
      rt_job(rj, rst);
    }
    STAMP(26);
    lf_set(w);
    if (w == 3) { hold(WA_T); wo_ld(); }
    STAMP(3);
    if (w < 4) {
      hold(W_T);
      b2_wait((u32x2){~tag, 0u}, 1);
      lf_wait(0, 8);
      STAMP(4);
    }
  }

  STAMPF(16);
  u32x4 wox[5];
  if (w == 3) {
    DRAIN();
    const char* bp = wo_st + (4 * h4 + (n16 >> 2)) * 160 + (n16 & 3) * 8;
#pragma unroll
    for (int dj = 0; dj < 5; ++dj) wox[dj] = trfrag(bp + dj * 32, 160);
    asm volatile("s_waitcnt lgkmcnt(0)" : "+v"(wox[0]), "+v"(wox[1]), "+v"(wox[2]), "+v"(wox[3]), "+v"(wox[4]) :: "memory");
  }
  auto wo_put = [&]() {
    if (w == 3 && R < 28) {
      const unsigned base = WS_WOUTF + (unsigned)c * WBLK + (unsigned)((R >> 1) * 10240 + (R & 1) * 1024);
#pragma unroll
      for (int dj = 0; dj < 5; ++dj) st16(rs, base + (unsigned)((R & 1) ? dj : (dj + 2) % 5) * 2048u + l16, wox[dj]);
    }
  };
  if (cs2) {
    cs_ld(csv, (8 + c) * 7 + R - 20);
    SCHED();
    load_b2(0, 12);
    SCHED();
    cs_fin(csv, (8 + c) * 7 + R - 20);
  } else if (w < 4) {
    asm volatile("" ::: "memory");
    load_b2(0, 12);
  }
  if (w < 4) load_b2(12, 35);
  wo_put();
  STAMPF(29);
  u32x4 k_c;
  unsigned kc_o = w < 2 ? (unsigned)(tid < 112 ? tid : 0) * 16u : 0x80000000u, kc_d = (unsigned)(L_CONST + (tid & 127) * 8);
  auto kc_ld = [&](int z2) { k_c = __builtin_amdgcn_raw_buffer_load_b128(rs, kc_o, WS_CS + (unsigned)(8 * z2 + c) * 2048u, SC1); };
  auto consts_put = [&](int z2) {
    if (w < 2) {
      if (__any(ne2(k_c[0], k_c[2]) != 0u)) {
        _Pragma("unroll 1") for (unsigned spins = 0; spins < SPIN_CAP; ++spins) {
          __builtin_amdgcn_s_sleep(8);
          kc_ld(z2);
          if (!__any(ne2(k_c[0], k_c[2]) != 0u)) break;
        }
      }
      if (z2) *(u32x2*)(lds + kc_d + 1024) = (u32x2){k_c[1], k_c[3]};
      else { const unsigned d = kc_d - ((kc_d >> 1) & 4u); *(unsigned*)(lds + d) = k_c[1]; *(unsigned*)(lds + d + 8) = k_c[3]; }
    }
  };
  const int wq = w ^ rotr, rt1 = wq & 3, kh1 = w >> 2;
  unsigned bo0 = (unsigned)(kh1 * 71680) + l16, bo1 = bo0 + 35840u;
  asm volatile("" : "+v"(bo0), "+v"(bo1));
  unsigned pso[3], pqo[3], oto = (unsigned)(((w ^ 4) * 7 * 64 + l) * 8), rdo = (unsigned)((w * 7 * 64 + l) * 8), cso = (unsigned)(L_CONST + (128 + n16) * 8);
#pragma unroll
  for (int e = 0; e < 3; ++e) {
    const int row = rt1 * 16 + (e < 2 ? 4 * h4 + 2 * kh1 + e : n16);
    pso[e] = (unsigned)(L_PST + row * 4); pqo[e] = (unsigned)(L_PST + 512 + row * 16 + (row & 3) * 4);
  }
  unsigned spo = (unsigned)(L_Z + tid * 16), p6o = (unsigned)(L_PST + (wq * 16 + 4 * h4) * 4), pgo = (unsigned)(L_PST + 512 + (wq * 16 + n16) * 16);
  asm volatile("" : "+v"(spo), "+v"(p6o), "+v"(pgo));
  p6o = n16 == 12 ? p6o : spo;
  pgo = h4 == (n16 >> 2) ? pgo : spo;
  unsigned bsrc = (unsigned)(((l >> 1) & 3) * 64 + (l & 1) * 4), ppo = l < 8 ? l16 : 0x80000000u;
  asm volatile("" : "+v"(pso[0]), "+v"(pso[2]), "+v"(pqo[0]), "+v"(pqo[2]), "+v"(oto), "+v"(rdo), "+v"(cso), "+v"(p6o), "+v"(pgo), "+v"(bsrc), "+v"(ppo), "+v"(kc_o), "+v"(kc_d));
  pso[1] = pso[0] + 4u; pqo[1] = pqo[0] + 20u;
#define BFRAG(q, j) (*(const u32x4*)(lds + ((q) < 5 ? bo0 : bo1) + ((q) % 5) * 7168 + (j) * 1024))
  u32x4 h2[10];
  unsigned xv = (unsigned)((4 * ((l >> 3) & 1) + h4) * 128 + (l & 7) * 16), xs0 = WS_X1B + (unsigned)(R * 4 + rt1) * 20480u + (unsigned)(10 * kh1) * 1024u;
  unsigned wvo = wsp_vo, bso = (unsigned)tau(rt1, n16) * 2u, fgo = l < 32 ? (unsigned)(l >> 3) * F4S + (unsigned)(l & 7) * 16u : 0x80000000u;
  asm volatile("" : "+v"(xv), "+v"(wvo), "+v"(bso), "+v"(fgo));
#define WO3 (-92)
  unsigned zs[8];
#pragma unroll
  for (int t = 0; t < 8; ++t) zs[t] = WS_Z2F + (unsigned)(c * 64 + 8 * w + 4 * ((t >> 2) ^ rotg) + ((t + rotb) & 3)) * 7168u;
  unsigned wx8 = (unsigned)((w + (idx >> 2)) & 7) * 1024u, wx4 = wx8 & 3072u, wgb = WS_WOUTF + (unsigned)c * WBLK, fzs = fz + (unsigned)(c * 32 + 4 * w) * 128u;
  unsigned uo = WS_UF + (unsigned)((R * 4 + rt1) * 8 + c) * 4096u + (unsigned)kh1 * 2048u, ui = WS_UF + (unsigned)((R * 4 + rt1) * 8 + 4 * kh1) * 4096u;
  unsigned xs = WS_X1B + (unsigned)(R * 4 + rt1) * 20480u + (unsigned)(5 * c + 2 * kh1) * 512u, wl2 = (unsigned)(120 + WO3 + (w < 4 ? w + 16 : w)) * 1024u;
  unsigned os = (unsigned)__builtin_amdgcn_readfirstlane(((R * 64 + tau(rt1, 0)) * D_ + 80 * c + 32 * kh1) * 2);
#define PINS(x) do { x = (unsigned)__builtin_amdgcn_readfirstlane((int)x); asm volatile("" : "+s"(x)); } while (0)
#pragma unroll
  for (int t = 0; t < 8; ++t) PINS(zs[t]);
  PINS(xs0); PINS(wx8); PINS(wx4); PINS(wgb); PINS(fzs); PINS(uo); PINS(ui); PINS(xs); PINS(wl2); PINS(os);
  unsigned s1 = (unsigned)((w ^ 4) * 3072 + 1024), s3 = (unsigned)(w * 3072 + 1024);
  PINS(s1); PINS(s3);
#define LDX(q) h2[q] = __builtin_amdgcn_raw_buffer_load_b128(rs, xv + (unsigned)((q) & 3) * 1024u, xs0 + (unsigned)((q) >> 2) * 4096u, BULK_AUX)
  auto flag4 = [&](unsigned off) { __builtin_amdgcn_raw_buffer_store_b128((u32x4){tag, tag, tag, tag}, rs, fgo, WS_F4 - WS_FWI + off, SC1); };
  unsigned bs_raw;
  f32x4 acc[7];
  float mu_n, rs_n;
  {
    unsigned f1o[1] = {w == 1 && l < 20 ? fwi + (unsigned)(c * 20 + l) * 128u : ~0u}, f1v;
    asm volatile("" : "+v"(f1o[0]));
    if (w == 7) {
      const unsigned fb = WS_FX + (unsigned)(((R >> 1) * 4 + (R & 1) * 2 + (c >> 2)) * 160 + l) * 128u;
      const unsigned fo[3] = {fb, fb + 8192u, l < 32 ? fb + 16384u : ~0u};
      lf_wait(36, 4);
      int dt = (int)(lf[44 + (l < 20 ? l : 0)] - t_start);
      dt = max(dt, __builtin_amdgcn_update_dpp(0, dt, 0xB1, 0xf, 0xf, true));
      dt = max(dt, __builtin_amdgcn_update_dpp(0, dt, 0x4E, 0xf, 0xf, true));
      dt = max(dt, __builtin_amdgcn_update_dpp(0, dt, 0x124, 0xf, 0xf, true));
      dt = max(dt, __builtin_amdgcn_update_dpp(0, dt, 0x128, 0xf, 0xf, true));
      dt = min(max(max(__builtin_amdgcn_readlane(dt, 0), __builtin_amdgcn_readlane(dt, 16)), 0), 300);
      __builtin_amdgcn_s_setprio(YPR);
      hold((unsigned)(YG_T + dt));
      wait_lines<3, YSL>(rs, fo, tag);
      if (l == 0) lf[33] = tag;
      __builtin_amdgcn_s_setprio(0);
    }
    lf_wait(33, 1);
    STAMP(7);
    SCHED();
#pragma unroll
    for (int q = 0; q < 3; ++q) LDX(q);
    SCHED();
    __builtin_amdgcn_s_waitcnt(0x0F73);
    BAR();
    STAMP(8);
#pragma unroll
    for (int j = 0; j < 7; ++j) acc[j] = (f32x4){0.f, 0.f, 0.f, 0.f};
    f32x4 accg = {0.f, 0.f, 0.f, 0.f};
    u32x4 bf[7];
#pragma unroll
    for (int j = 0; j < 7; ++j) bf[j] = BFRAG(0, j);
    SCHED();
#pragma unroll
    for (int q = 3; q < 6; ++q) LDX(q);
    if (w == 3) flag4(WS_FWO + (unsigned)(c * 32 + R) * 128u);
    SCHED();
    auto xswap = [&](int q, int e) {
#pragma unroll
      for (int d = 0; d < 2; ++d) {
        if (e == 1) h2[q][2 + d] ^= (unsigned)__builtin_amdgcn_update_dpp(0, (int)h2[q][d], 0x128, 0xf, 0x3, false);
        else h2[q][d] ^= (unsigned)__builtin_amdgcn_update_dpp(0, (int)h2[q][2 + d], 0x128, 0xf, 0xC, false);
      }
      asm volatile("" : "+v"(h2[q]));
    };
#define XSW 1
#pragma unroll
    for (int q = 0; q < 10; ++q) {
      if (q < XSW) { xswap(q, 0); xswap(q, 1); xswap(q, 2); }
      if (q == 5 && w == 1 && __builtin_amdgcn_ballot_w64(f1v == tag) != 0xfffffull) wait_lines<1, 4>(rs, f1o, tag);
      if (q == 5) { __builtin_amdgcn_s_barrier(); asm volatile("" ::: "memory"); }
      SCHED();
      accg = MFMA16(h2[q], h2[q], accg);
      SCHED();
#pragma unroll
      for (int j = 0; j < 7; ++j) {
        acc[j] = MFMA16(h2[q], bf[j], acc[j]);
        if (q == 9 && j == 1) *(f32x4*)(lds + pgo) = accg;
        if (q < 9) bf[j] = BFRAG(q + 1, j);
        if (q < 9 && q + 1 >= XSW && j >= 3 && j < 6) xswap(q + 1, j - 3);
        if (q == 1 && j == 6) f1v = ld4(rs, f1o[0]);
        if (q == 2 && j == 0) bs_raw = __builtin_amdgcn_raw_buffer_load_b16(__builtin_amdgcn_make_buffer_rsrc((void*)p.b_sp, 0, S_ * 2, 0x00020000), bso, (unsigned)R * 128u, 0);
#define KCQ 4
#define WA 8
#define WB 12
#define WQ0 2
        if (WQ0 == 2 && q >= 2 && j == 2) LDW(q - 2);
        if (WQ0 == 6 && q >= 6 && (j == 2 || j == 4)) LDW(2 * (q - 6) + (j >> 2));
        if (WQ0 == 3 && q >= 2 && q < 5 && (j & 1) && 3 * (q - 2) + (j >> 1) < WA) LDW(3 * (q - 2) + (j >> 1));
        if (WQ0 == 4 && q >= 2 && !(q & 1) && j == 2) LDW((q - 2) >> 1);
        if ((j == 0 || j == 4) && 6 + 2 * q + (j >> 2) < 10) LDX(6 + 2 * q + (j >> 2));
        if (q == KCQ && j == 0) kc_ld(1);
        if (q == 2 && j == 1 && cs1) cs_ld(csv, c * 7 + R - 25);
        if (q >= 5 && j == 5) {
          const int i = (5 + q) * 7 + (w < 7 ? w : 0);
          dma1k(rs, lds + i * 1024, l16, WS_WINF + (unsigned)c * WBLK + (unsigned)i * 1024u);
          if (q == 9) { const int i2 = 28 + (w < 7 ? w : 0); dma1k(rs, lds + i2 * 1024, l16, WS_WINF + (unsigned)c * WBLK + (unsigned)i2 * 1024u); }
        }
        SCHED();
      }
    }
    STAMP(31);
  }
  auto gelu_n = [&](f32x2* x, auto N_) {
    constexpr int N = decltype(N_)::value;
    const float c0 = -2.0f * 0.7978845608028654f * 1.4426950408889634f, c1 = c0 * 0.044715f;
    f32x2 k0 = {c0, c0}, k1 = {c1, c1}, k2 = {1.0f, 1.0f};
    asm volatile("" : "+v"(k0), "+v"(k1), "+v"(k2));
    f32x2 t[N];
#define GST(e) do { _Pragma("unroll") for (int i = 0; i < N; ++i) { t[i] = (e); asm volatile("" : "+v"(t[i])); } } while (0)
    GST(x[i] * x[i]);
    GST(__builtin_elementwise_fma(t[i], k1, k0));
    GST(t[i] * x[i]);
    GST(((f32x2){__builtin_amdgcn_exp2f(t[i][0]), __builtin_amdgcn_exp2f(t[i][1])}));
    GST(t[i] + k2);
    GST(((f32x2){__builtin_amdgcn_rcpf(t[i][0]), __builtin_amdgcn_rcpf(t[i][1])}));
#pragma unroll
    for (int i = 0; i < N; ++i) { x[i] *= t[i]; asm volatile("" : "+v"(x[i])); }
  };
  {
    f32x2 mine[7];
    f32x2 csb[7];
    if (kh1) {
#pragma unroll
      for (int j = 0; j < 7; ++j) { *(f32x2*)(lds + rdo + j * 512) = (f32x2){acc[j][0], acc[j][1]}; mine[j] = (f32x2){acc[j][2], acc[j][3]}; }
      *(f32x4*)(lds + p6o) = acc[6];
    } else {
#pragma unroll
      for (int j = 0; j < 7; ++j) { *(f32x2*)(lds + rdo + j * 512) = (f32x2){acc[j][2], acc[j][3]}; mine[j] = (f32x2){acc[j][0], acc[j][1]}; }
      *(f32x4*)(lds + p6o) = acc[6];
      consts_put(1);
      if (cs1) cs_fin(csv, c * 7 + R - 25);
#pragma unroll
      for (int k = WA; k < WB; ++k) LDW(k);
    }
    BAR();
    if (kh1) {
#pragma unroll
      for (int k = WA; k < WB; ++k) LDW(k);
    }
    __builtin_amdgcn_s_setprio(3);
    STAMPT(9);
    f32x2 oth[7];
    f32x2 pa[3], pb[3];
#pragma unroll
    for (int e = 0; e < 3; ++e) {
      pa[e] = (f32x2){*(const float*)(lds + pso[e]), *(const float*)(lds + pqo[e])};
      pb[e] = (f32x2){*(const float*)(lds + pso[e] + 256), *(const float*)(lds + pqo[e] + 1024)};
    }
#pragma unroll
    for (int j = 0; j < 7; ++j) { oth[j] = *(const f32x2*)(lds + oto + j * 512); csb[j] = *(const f32x2*)(lds + cso + 128 * j); }
    SCHED();
    float mu2[2], rs2[2];
#pragma unroll
    for (int e = 0; e < 3; ++e) {
      const float mean = (pa[e][0] + pb[e][0]) * (1.0f / D_);
      const float var = __builtin_fmaxf((pa[e][1] + pb[e][1]) * (1.0f / D_) - mean * mean, 0.f);
      const float rstd = __builtin_amdgcn_rsqf(var + 1e-5f);
      if (e < 2) { mu2[e] = mean; rs2[e] = rstd; } else { mu_n = mean; rs_n = rstd; }
    }
    f32x2 zz[7];
    const f32x2 nmu = {-mu2[0], -mu2[1]}, rsv = {rs2[0], rs2[1]};
#pragma unroll
    for (int j = 0; j < 7; ++j)
      zz[j] = __builtin_elementwise_fma(__builtin_elementwise_fma(nmu, (f32x2){csb[j][0], csb[j][0]}, mine[j] + oth[j]), rsv, (f32x2){csb[j][1], csb[j][1]});
    gelu_n(zz, IC<7>());
    if (n16 >= 12) zz[6] = (f32x2){0.f, 0.f};
    f32x2 s1 = ((zz[0] + zz[1]) + (zz[2] + zz[3])) + ((zz[4] + zz[5]) + zz[6]);
    f32x2 s2 = zz[0] * zz[0], s3 = zz[1] * zz[1];
#pragma unroll
    for (int j = 2; j < 7; ++j) { if (j & 1) s3 = __builtin_elementwise_fma(zz[j], zz[j], s3); else s2 = __builtin_elementwise_fma(zz[j], zz[j], s2); }
    s2 += s3;
    asm("" : "+v"(s1), "+v"(s2));
    float a0 = s1[0] + dpp_f<0xB1>(s1[0]), a1 = s1[1] + dpp_f<0xB1>(s1[1]), b0 = s2[0] + dpp_f<0xB1>(s2[0]);
    a0 = (l & 1) ? a1 : a0;
    const float b1 = s2[1] + dpp_f<0xB1>(s2[1]);
    b0 = (l & 1) ? b1 : b0;
    a0 += dpp_f<0x4E>(a0); b0 += dpp_f<0x4E>(b0);
    a0 += dpp_f<0x124>(a0); b0 += dpp_f<0x124>(b0);
    a0 += dpp_f<0x128>(a0); b0 += dpp_f<0x128>(b0);
    {
      const unsigned pa0 = __builtin_amdgcn_ds_bpermute(bsrc, __float_as_uint(a0)), pb0 = __builtin_amdgcn_ds_bpermute(bsrc, __float_as_uint(b0));
      __builtin_amdgcn_raw_buffer_store_b128((u32x4){tag, pa0, tag, pb0}, rs, ppo, WS_PART + (unsigned)((R * 8 + c) * 8 + wq) * 128u, SC1);
    }
    SCHED();
    __builtin_amdgcn_s_setprio(0);
    STAMP(10);
#pragma unroll
    for (int k = 0; k < 10; ++k) {
      if (k < 9) {
        int x = k * 8 + w;
        x = x < 70 ? x : 69;
        const int i = x + (x < 35 ? 35 : 70);
        dma1k(rs, lds + i * 1024, l16, WS_WINF + (unsigned)c * WBLK + (unsigned)i * 1024u);
      }
    }
#pragma unroll
    for (int k = WB; k < 16; ++k) LDW(k);
#pragma unroll
    for (int j = 0; j < 7; ++j)
      *(unsigned*)(lds + L_Z + (((rt1 >> 1) * 7 + j) * 64 + l) * 16 + (4 * (rt1 & 1) + 2 * kh1) * 2) = pack2(zz[j][0], zz[j][1]);
    SCHED();
#define TR0 2
#define TR1 TR0
#define TR2 TR1
#define TR3 5
#pragma unroll
    for (int g = 0; g < TR0; ++g) WSP_TR(g);
    if (w == 1) {
      const unsigned fo[1] = {l < 28 ? fwi + (WS_FWO - WS_FWI) + (unsigned)(c * 32 + l) * 128u : ~0u};
      wait_lines<1, 1>(rs, fo, tag);
    }
    asm volatile("s_waitcnt vmcnt(%0)" :: "n"(16 - WB) : "memory");
    BAR();
    STAMPT(11);
  }
  unsigned z1d[7];
  constexpr int L_RED = 28672;
#define WOA(i) (((i) < 28 ? (i) : (i) < 108 ? (i) + 32 : (i) < 120 ? (i) + 37 : (i) - 92) * 1024)
#define RBAR() do { __builtin_amdgcn_s_barrier(); asm volatile("" ::: "memory"); } while (0)
#define ZAUX 16
#define NW 14
#define WOD(i0, x) dma1k(rs, lds + WOA(i0) + (x), l16, wgb + (unsigned)(i0) * 1024u + (x))
  auto wo_dma = [&](int k) {};
  u32x4 zw[NW];
#define LDZ(f) do { if (((f) >> 3) == 4) { zs[(f) & 7] += 4096u; asm volatile("" : "+s"(zs[(f) & 7])); } \
    zw[(f) % NW] = __builtin_amdgcn_raw_buffer_load_b128(rs, l16 + (unsigned)(((f) >> 3) & 3) * 1024u, zs[(f) & 7], ZAUX); } while (0)
#define WD 24
#define WE 32
#define QS 5
#define PQ 3
#define NW0 NW
#define PP 1
#define WZ 4
#define WY 11
  static_assert(QS >= 4 && QS <= 6 && PQ >= 2 && PQ < QS && (WZ < 4 || QS >= 5), "");
  {
    f32x4 acc[7];
#pragma unroll
    for (int j = 0; j < 7; ++j) acc[j] = (f32x4){0.f, 0.f, 0.f, 0.f};
    u32x4 bf[7];
#pragma unroll
    for (int j = 0; j < 7; ++j) bf[j] = BFRAG(4, j);
    const unsigned po = WS_PART + (unsigned)((R * 8 + (l >> 3)) * 8 + wq) * 128u + (unsigned)(l & 7) * 16u;
    u32x4 pv;
    auto z1 = [&](auto Q0_, auto Q1_) __attribute__((always_inline)) {
#pragma unroll
      for (int qi = decltype(Q0_)::value; qi < decltype(Q1_)::value; ++qi) {
        const int q = (qi + 4) % 10, qn = (qi + 5) % 10;
        SCHED();
#pragma unroll
        for (int j = 0; j < 7; ++j) {
          acc[j] = MFMA16(bf[j], h2[q], acc[j]);
          if (qi < 9 && qi != QS - 1) bf[j] = BFRAG(qn, j);
          if (qi < 2 && (j == 1 || j == 4)) { const int i = (2 * qi + (j >> 2)) * 7 + (w < 7 ? w : 0); dma1k(rs, lds + i * 1024, l16, WS_WINF + (unsigned)c * WBLK + (unsigned)i * 1024u); }
          if (qi == 2 && j == 0) kc_ld(0);
          if (qi == PQ && j == 1) pv = ld16(rs, po);
          if (!PP && qi >= QS && (j & 1) && WD + 3 * (qi - QS) + (j >> 1) < WE) LDW((WD + 3 * (qi - QS) + (j >> 1)) & 31);
          SCHED();
        }
      }
    };
    auto req1 = [&]() {
#pragma unroll
      for (int k = 16; k < WD; ++k) { LDW(k); SCHED(); }
    };
    auto req2 = [&]() {
#pragma unroll
      for (int k = WD; k < (PP ? WE : WD); ++k) { LDW(k); SCHED(); }
      sfor<0, WZ>([&](auto G_) __attribute__((always_inline)) { WOD(68 + 8 * decltype(G_)::value, wx8); SCHED(); });
    };
    if (kh1) req1();
    z1(IC<0>(), IC<QS>());
    STAMPT(29);
    f32x2 zv[2][4];
    unsigned sro = (unsigned)(L_ST2 + h4 * 16), swo = l < 8 ? (unsigned)(L_ST2 + (16 * rt1 + 4 * (l >> 1) + 2 * kh1 + (l & 1)) * 4) : (unsigned)(L_PST + l * 4);
    unsigned zo = w < 7 ? (unsigned)(h4 * 7168 + n16 * 16) : 0x80000000u;
#pragma unroll
    for (int m = 0; m < 2; ++m) {
      const u32x4 zf = *(const u32x4*)(lds + L_Z + ((m * 7 + (w < 7 ? w : 0)) * 64 + l) * 16);
#pragma unroll
      for (int e = 0; e < 4; ++e) { zv[m][e] = (f32x2){bflo(zf[e]), bfhi(zf[e])}; asm volatile("" : "+v"(zv[m][e])); }
    }
    asm volatile("" : "+v"(sro), "+v"(swo), "+v"(zo));
#pragma unroll
    for (int g = TR0; g < TR1; ++g) WSP_TR(g);
    __builtin_amdgcn_s_setprio(3);
    if (__any(((pv[0] ^ tag) | (pv[2] ^ tag)) != 0u)) {
      _Pragma("unroll 1") for (unsigned spins = 0; spins < SPIN_CAP; ++spins) {
        pv = ld16(rs, po);
        if (__all(((pv[0] ^ tag) | (pv[2] ^ tag)) == 0u)) break;
        __builtin_amdgcn_s_sleep(1);
      }
    }
    {
      const float mean = red8(__uint_as_float(pv[1])) * (1.0f / FH);
      const float var = __builtin_fmaxf(red8(__uint_as_float(pv[3])) * (1.0f / FH) - mean * mean, 0.f);
      const float rstd = __builtin_amdgcn_rsqf(var + 1e-5f);
      *(float*)(lds + swo) = rstd;
      *(float*)(lds + swo + 256) = -mean * rstd;
    }
    SCHED();
    consts_put(0);
    if (!kh1) req1();
    BAR();
    STAMP(12);
    {
      f32x4 sa[2][2], sb[2][2];
#pragma unroll
      for (int i = 0; i < 4; ++i) { sa[i >> 1][i & 1] = *(const f32x4*)(lds + sro + i * 64); sb[i >> 1][i & 1] = *(const f32x4*)(lds + sro + i * 64 + 256); }
      SCHED();
#pragma unroll
      for (int m = 0; m < 2; ++m) {
        u32x4 o;
#pragma unroll
        for (int e = 0; e < 4; ++e) {
          const f32x4 a = sa[m][e >> 1], b = sb[m][e >> 1];
          const f32x2 x = __builtin_elementwise_fma(zv[m][e], (f32x2){a[2 * (e & 1)], a[2 * (e & 1) + 1]}, (f32x2){b[2 * (e & 1)], b[2 * (e & 1) + 1]});
          o[e] = pack2(x[0], x[1]);
        }
        __builtin_amdgcn_raw_buffer_store_b128(o, rs, zo + (unsigned)m * 256u, WS_Z2F + (unsigned)((c * 64 + 4 * (R >> 1)) * 7 + (w < 6 ? w + 1 : 0)) * 1024u + (unsigned)(R & 1) * 512u, SC1);
      }
    }
    SCHED();
    __builtin_amdgcn_s_setprio(0);
    STAMPT(13);
#pragma unroll
    for (int j = 0; j < 7; ++j) bf[j] = BFRAG((QS + 4) % 10, j);
    if (kh1) req2();
    z1(IC<QS>(), IC<10>());
    f32x2 mine[7];
    if (kh1) {
#pragma unroll
      for (int j = 0; j < 7; ++j) { *(f32x2*)(lds + rdo + L_RED + j * 512) = (f32x2){acc[j][0], acc[j][1]}; mine[j] = (f32x2){acc[j][2], acc[j][3]}; }
    } else {
#pragma unroll
      for (int j = 0; j < 7; ++j) { *(f32x2*)(lds + rdo + L_RED + j * 512) = (f32x2){acc[j][2], acc[j][3]}; mine[j] = (f32x2){acc[j][0], acc[j][1]}; }
    }
    if (!kh1) req2();
#pragma unroll
    for (int g = TR1; g < TR2; ++g) WSP_TR(g);
    asm volatile("s_waitcnt vmcnt(%0)" :: "n"(WE - WD + WZ) : "memory");
    BAR();
    if (w == 3) flag4(WS_FZ + (unsigned)(c * 32 + R) * 128u);
    STAMP(6);
    auto req = [&](auto G0_, auto G1_) __attribute__((always_inline)) {
#pragma unroll
      for (int k = WE; k < 32; ++k) LDW(k);
      SCHED();
      sfor<decltype(G0_)::value, decltype(G1_)::value>([&](auto G_) __attribute__((always_inline)) {
        constexpr int i0 = 4 * decltype(G_)::value;
        WOD(i0 < 68 ? i0 : i0 + 8 * WZ, wx4); SCHED();
      });
    };
    if (kh1) req(IC<0>(), IC<WY>());
    u32x4 gv[8];
    auto gb_ld = [&]() {
#pragma unroll
      for (int k = 0; k < 8; ++k) gv[k] = __builtin_amdgcn_raw_buffer_load_b128(rs, l < 40 ? l16 : 0u, WS_MODQ + (unsigned)(k * 2560 + 1920 + 80 * c) * 8u, SC1);
    };
    if (w == 2) gb_ld();
    f32x2 z[7];
    {
      f32x2 oth[7];
      f32x4 cc[7];
#pragma unroll
      for (int j = 0; j < 7; ++j) {
        oth[j] = *(const f32x2*)(lds + oto + L_RED + j * 512);
        cc[j] = *(const f32x4*)(lds + L_CONST + (16 * j + 4 * h4 + 2 * kh1) * 8);
      }
      SCHED();
#pragma unroll
      for (int j = 0; j < 7; ++j)
        z[j] = __builtin_elementwise_fma(__builtin_elementwise_fma((f32x2){-mu_n, -mu_n}, (f32x2){cc[j][0], cc[j][1]}, oth[j] + mine[j]),
                                         (f32x2){rs_n, rs_n}, (f32x2){cc[j][2], cc[j][3]});
    }
    gelu_n(z, IC<7>());
#pragma unroll
    for (int j = 0; j < 7; ++j) z1d[j] = pack2(z[j][0], z[j][1]);
    asm volatile("" : "+v"(z1d[0]), "+v"(z1d[1]), "+v"(z1d[2]), "+v"(z1d[3]), "+v"(z1d[4]), "+v"(z1d[5]), "+v"(z1d[6]));
    SCHED();
    STAMPT(30);
    if (w == 2) {
      auto bad = [&]() {
        unsigned d = 0u;
#pragma unroll
        for (int k = 0; k < 8; ++k) d |= ne2(gv[k][0], gv[k][2]);
        return __any(d != 0u);
      };
      if (bad()) {
        _Pragma("unroll 1") for (unsigned spins = 0; spins < SPIN_CAP; ++spins) {
          __builtin_amdgcn_s_sleep(8);
          gb_ld();
          if (!bad()) break;
        }
      }
      f32x2 t[4];
#pragma unroll
      for (int k = 0; k < 4; ++k) t[k] = (f32x2){__uint_as_float(gv[2 * k][1]) + __uint_as_float(gv[2 * k + 1][1]), __uint_as_float(gv[2 * k][3]) + __uint_as_float(gv[2 * k + 1][3])};
      *(f32x2*)(lds + L_MISC + l * 8) = rbf2((t[0] + t[1]) + (t[2] + t[3]));
    }
    if (!kh1) req(IC<WY>(), IC<30 - 2 * WZ>());
    const unsigned fo[1] = {l < 4 ? fzs + (unsigned)l * 128u : ~0u};
    const unsigned fv = ld4(rs, fo[0]);
    SCHED();
#pragma unroll
    for (int g = TR2; g < TR3; ++g) WSP_TR(g);
    SCHED();
    STAMP(14);
    if (__builtin_amdgcn_ballot_w64(fv == tag) != 15ull) wait_lines<1, 1>(rs, fo, tag);
    STAMPT(23);
#pragma unroll
    for (int f = 0; f < NW; ++f) { LDZ(f); SCHED(); }
  }

#define WO2 15
#define TRC TR3
#define VMW(n) asm volatile("s_waitcnt vmcnt(%0)" ::"n"(n) : "memory")
  {
    SCHED();
#pragma unroll
    for (int g = TRC; g < 8; ++g) WSP_TR(g);
    SCHED();
    STAMP(20);
    const int rt = rt1, kh = kh1;
    const unsigned rw = (unsigned)(L_RED + w * 512);
    unsigned rr = (unsigned)(L_RED + w * 4096 + l * 4);
    asm volatile("; m0 is set here, not in front of the first store (one wait state)" : "+v"(rr) : "{m0}"(rw));
    const float bs = bflo(bs_raw);
    unsigned long long hm = 0xffff000000000000ull;
    asm volatile("" : "+s"(hm));
    unsigned l8;
    f32x4 acc2[2][4];
    float rv[8][2];
    u32x4 ud;
    u32x4 P[2][8];
    u32x2 xp[3];
    auto ld_p = [&](int pt, int i) { P[pt][i] = __builtin_amdgcn_raw_buffer_load_b128(rs, l16 + (unsigned)pt * 1024u, ui + (unsigned)i * 2048u, SC1); };
#define RED_PUT1(j, i) asm volatile("ds_write_addtid_b32 %0 offset:%2" :: "v"(acc2[(j) & 1][((i) >> 2) & 3][(i) & 3]), "{m0}"(rw), "n"((((i) >> 2) & 3) * 4096 + (((i) >> 1) & 1) * 16384 + ((i) & 1) * 256) : "memory")
#define RED_PUT(j, r) do { RED_PUT1(j, 4 * (r)); RED_PUT1(j, 4 * (r) + 1); RED_PUT1(j, 4 * (r) + 2); RED_PUT1(j, 4 * (r) + 3); } while (0)
#define RED_GET(i) do { f32x2 x_; asm volatile("ds_read2st64_b32 %0, %1 offset0:%2 offset1:%3" : "=v"(x_) : "v"(rr), "n"(2 * (i)), "n"(2 * (i) + 1) : "memory"); rv[i][0] = x_[0]; rv[i][1] = x_[1]; } while (0)
    float zz[2], gg[2];
    unsigned cs;
    auto sum_rows_u = [&](unsigned v) {
      auto a = __builtin_amdgcn_permlane16_swap(v, v, false, false);
      v = a[0] + a[1];
      auto b = __builtin_amdgcn_permlane32_swap(v, v, false, false);
      return b[0] + b[1];
    };
    auto red_fin = [&](int j, int k, bool fuse = false) {
      const int e = k & 1, i = k >> 1;
#define VADD(d, x, y) asm volatile("v_add_f32 %0, %1, %2" : "=v"(d) : "v"(x), "v"(y))
      if (i < 4) VADD(rv[2 * i][e], rv[2 * i][e], rv[2 * i + 1][e]);
      if (i == 4 || i == 5) VADD(rv[4 * (i & 1)][e], rv[4 * (i & 1)][e], rv[4 * (i & 1) + 2][e]);
      if (i == 6) VADD(rv[0][e], rv[0][e], bs);
      if (i == 7) VADD(gg[e], rv[0][e], rv[4][e]);
      if (i == (fuse ? 9 : 8)) zz[e] = e ? bfhi(z1d[(j + 6) % 7]) : bflo(z1d[(j + 6) % 7]);
      if (i == 9) zz[e] *= gg[e];
      if (i == 9 || (i == 8 && !fuse)) asm volatile("" : "+v"(zz[e]));
      if (k == 20) { ud[j & 3] = pack2(zz[0], zz[1]); asm volatile("" : "+v"(ud[j & 3])); }
      if (k == 21) {
        if (j == 0) asm volatile("v_cndmask_b32 %0, %0, 0, %1" : "+v"(ud[0]) : "s"(hm));
        if (j == 2) asm volatile("v_add3_u32 %0, %1, %2, %3" : "=v"(cs) : "v"(ud[0]), "v"(ud[1]), "v"(ud[2]));
        if (j == 3) {
          cs = tag - sum_rows_u(cs + ud[3]);
          asm volatile("v_cndmask_b32 %0, %0, %1, %2" : "+v"(ud[0]) : "v"(cs), "s"(hm));
        }
        if (j >= 4) { ud[3] = (j == 4 ? tag : ud[3]) - ud[j & 3]; asm volatile("" : "+v"(ud[3])); }
        if (j % 3 == 0 && j) __builtin_amdgcn_raw_buffer_store_b128(ud, rs, l16 + (unsigned)(j / 6) * 1024u, uo, SC1);
      }
    };
    struct Sch { int b1, b2, wp[16], rp[8], vp[22], lp[8], pr[32]; };
    constexpr Sch S = {18, 28, {0, 1, 2, 3, 4, 5, 6, 7, 8, 9, 10, 11, 12, 12, 15, 15}, {19, 19, 20, 22, 23, 23, 24, 24}, {29, 29, 30, 30, 31, 31, 32, 33, 35, 36, 37, 38, 39, 40, 41, 42, 43, 46, 43, 46, 48, 48},
                       {1, 1, 1, 1, 1, 1, 1, 1}, {0, 0, 2, 1, 0, 0, 0, 0, 0, 0, 2, 2, 0, 0, 0, 0, 0, 0, 0, 1, 0, 0, 0, 1, 0, 0, 2, 1, 0, 0, 0, 0}};
    constexpr Sch F = {15, 23, {0, 1, 1, 2, 3, 4, 4, 5, 6, 7, 8, 9, 10, 11, 12, 12}, {15, 16, 16, 17, 17, 18, 18, 19}, {23, 23, 24, 24, 25, 26, 26, 27, 27, 28, 28, 29, 30, 30, 31, 31, 22, 22, 32, 33, 34, 35},
                       {1, 2, 1, 1, 1, 1, 1, 1}, {0, 2, 1, 0, 0, 1, 1, 0, 0, 2, 1, 0, 0, 2, 1, 0, 0, 2, 1, 0, 0, 2, 1, 0, 0, 2, 1, 0, 0, 2, 1, 0}};
#define VM 3
#define JF 4
#define RO 3
#define XE 1
#define CE 0
#define AP0 2
#define P1S 0
    unsigned wb[3], xo;
    const unsigned wl[3] = {(unsigned)(120 + WO3 + w) * 1024u, (unsigned)(128 + WO3 + w) * 1024u, wl2}, wg = wgb - (unsigned)(WO3 * 1024);
#define NAF 32
#define NA0 16
#define NA1 NA0
    u32x4 af[NAF];
#define LDA(m) af[(m) % NAF] = *(const u32x4*)(lds + wb[WOA(2 * (m)) >> 16] + (WOA(2 * (m)) & 65535))
    unsigned ck[2];
    const unsigned tag4 = tag * 4u;
    auto p_chk = [&](int pt, int pr, int k) {
      u32x4* q = P[pt] + 4 * pr;
      if (k < 2) ck[k & 1] = q[k & 1][0] + q[k & 1][1];
      else if (k < 8) ck[k & 1] = ck[k & 1] + q[(k & 1) + 2 * ((k >> 2) & 1)][k & 2] + q[(k & 1) + 2 * ((k >> 2) & 1)][(k & 2) + 1];
      else if (k == 8) ck[0] = pt ? ck[0] + ck[1] : sum_rows_u(ck[0] + ck[1]);
      if (k < 9) asm volatile("" : "+v"(ck[k & 1]));
      if (k == 9) {
        if (__builtin_expect(__any(ck[0] != tag4), 0)) {
          _Pragma("unroll 1") for (unsigned spins = 0; spins < SPIN_CAP; ++spins) {
            unsigned d = 0u;
#pragma unroll
            for (int i = 0; i < 4; ++i) ld_p(pt, 4 * pr + i);
#pragma unroll
            for (int i = 0; i < 4; ++i) d += q[i][0] + q[i][1] + q[i][2] + q[i][3];
            if (!pt) d = sum_rows_u(d);
            if (!__any(d != tag4)) break;
            __builtin_amdgcn_s_sleep(1);
          }
        }
      }
      if (k >= 10 && !pt) { unsigned x = q[(k - 10) & 3][0]; asm volatile("v_cndmask_b32 %0, %0, 0, %1" : "+v"(x) : "s"(hm)); q[(k - 10) & 3][0] = x; }
      if (k >= 10 && k < 13 && pt) { q[(k - 10) % 3][3] = q[3][(k - 10) % 3]; asm volatile("" : "+v"(q[(k - 10) % 3])); }
    };
    sfor<0, 224>([&](auto M_) __attribute__((always_inline)) {
      constexpr int mm = decltype(M_)::value, j = mm >> 5, m = mm & 31, y = m - (32 - 4 * RO);
      constexpr int t = j == 6 && y >= 0 ? 8 - RO + (y % (2 * RO + !RO)) / 2 : m >> 2, r = j == 6 && y >= 0 ? (y & 1) + 2 * (y / (2 * RO + !RO)) : m & 3, f = 8 * j + t;
      constexpr Sch C = j < JF ? S : F, B = j <= JF ? S : F;
      if (t == 0) asm volatile("v_mfma_f32_16x16x32_bf16 %0, %1, %2, 0" : "=&v"(acc2[j & 1][r]) : "v"(zw[f % NW]), "v"(wsp[r][t]), "v"(zw[(f | (f < 8 ? 1 : VM)) % NW]));
      else asm volatile("v_mfma_f32_16x16x32_bf16 %0, %1, %2, %0" : "+v"(acc2[j & 1][r]) : "v"(zw[f % NW]), "v"(wsp[r][t]), "v"(zw[(f | (f < 8 ? 1 : VM)) % NW]));
      SCHED();
      if (C.pr[m] == 1) __builtin_amdgcn_s_setprio(1);
      if ((m == C.b1 && j > 0) || (m == C.b2 && j > 0) || mm == 30) { asm volatile("" ::: "memory"); __builtin_amdgcn_s_waitcnt(0xC07F); RBAR(); SCHED(); }
      sfor<0, 16>([&](auto I_) __attribute__((always_inline)) {
        constexpr int i = decltype(I_)::value;
        if (j > 0 && C.wp[i] == m) { RED_PUT1(j + 1, i); SCHED(); }
        if (j > 0 && i < 8 && C.rp[i & 7] == m) { RED_GET(i & 7); SCHED(); }
      });
      if (j == 5 && m == 20) {
#pragma unroll
        for (int i = 0; i < 3; ++i) { wb[i] = (unsigned)(kh * 1024 + i * 65536) + l16; asm volatile("" : "+v"(wb[i])); }
      }
      if (XE && j == 5 && m == 13) {
        asm volatile("v_lshrrev_b32 %0, 1, %1" : "=v"(l8) : "v"(l16));
        xo = (l8 & 0x180u) + (unsigned)psi((l8 >> 3) & 15) * 8u;
        asm volatile("" : "+v"(xo));
      }
      if (RO && j == 6 && m >= 28) { RED_PUT1(6, 2 * (m - 28)); RED_PUT1(6, 2 * (m - 28) + 1); SCHED(); }
      if (CE && j == 6 && m >= 19 && m < 22) sfor<0, 3>([&](auto K_) __attribute__((always_inline)) { p_chk(0, 0, 3 * (m - 19) + decltype(K_)::value); SCHED(); });
      constexpr int ap[20] = {7, 8, 9, 10, 11, 13, 13, 14, 14, 19, 20, 21, 22, 23, 24, 25, 26, 27, 28, 29};
      sfor<0, NA0>([&](auto N_) __attribute__((always_inline)) { if (j == 6 && ap[AP0 + decltype(N_)::value] == m) { LDA(decltype(N_)::value); SCHED(); } });
      sfor<0, 22>([&](auto K_) __attribute__((always_inline)) {
        constexpr int k = decltype(K_)::value;
        if (j > 1 && B.vp[k] == m + 32) { red_fin(j > 1 ? j - 2 : 0, k, B.vp[16] == B.vp[18]); SCHED(); }
        if (j > 0 && C.vp[k] == m) { red_fin(j > 0 ? j - 1 : 0, k); SCHED(); }
      });
      if (C.pr[m] == 2) __builtin_amdgcn_s_setprio(0);
      if ((m & 3) == C.lp[m >> 2]) {
        if (f > 0 && f - 1 + NW < 56) LDZ(f - 1 + NW);
        if (j == 6 && (m >> 2) >= P1S) ld_p(0, ((m >> 2) - P1S) & 7);
        if (XE && j == 5 && m >= 20) xp[((m >> 2) - 5) % 3] = __builtin_amdgcn_raw_buffer_load_b64(rs, xo + (unsigned)((m >> 2) - 5) * 512u, xs, BULK_AUX);
        SCHED();
      }
    });
    STAMP(15);
#define NM0 0
#define GM 19
#define MB 24
    f32x4 acc[5];
    f32x4 xf[3], gf[3];
    unsigned go, oo, eo, ho, tw, tr, o1, ta, tb;
    const unsigned s2 = (unsigned)(L_MISC + 128 * kh), s4 = s1 - (unsigned)kh * 1024u;
    unsigned long long om = kh ? ~0ull : 0xccccccccccccccccull;
#define PIN2(a, b) asm volatile("" : "+v"(a), "+v"(b))
    auto glue = [&](int k) {
      if (k == 0) { ta = l8 >> 5; tb = l16 & 48u; PIN2(ta, tb); }
      if (k == 1) { tr = __umul24(ta, 112u) + tb; oo = ta & 12u; PIN2(tr, oo); }
      if (k == 2) { tr += s1; oo += ta; PIN2(tr, oo); }
      if (k == 3) { oo = __umul24(oo, 1280u) + tb; ta = l8 >> 4; PIN2(oo, ta); }
      if (k == 4) { ta &= 24u; tb = l16 & 0xf0u; PIN2(ta, tb); }
      if (k == 5) { tw = __umul24(tb, 7u) + ta; go = (ta << 1) + s2; PIN2(tw, go); }
      if (k == 6) { tw += s1; asm volatile("v_cndmask_b32 %0, %1, -2.0, %2" : "=v"(o1) : "v"(oo), "s"(om)); asm volatile("" : "+v"(tw)); }
      if (k == 7) { eo = l16 + s3; ho = l16 + s4; PIN2(eo, ho); }
      if (k >= 8 && k < 14) {
        const unsigned x = xp[((k - 8) >> 1) % 3][k & 1];
        float lo = bflo(x), hi = bfhi(x);
        PIN2(lo, hi);
        xf[((k - 8) >> 1) % 3][2 * (k & 1)] = lo; xf[((k - 8) >> 1) % 3][2 * (k & 1) + 1] = hi;
      }
    };
    auto g3 = [&](auto M0_, auto M1_) __attribute__((always_inline)) {
      sfor<decltype(M0_)::value, decltype(M1_)::value>([&](auto M_) __attribute__((always_inline)) {
        constexpr int mi = decltype(M_)::value;
        constexpr int m = mi < 55 ? mi : mi < 64 ? 55 + 5 * ((mi - 55) / 3) + (mi - 55) % 3 : 58 + 5 * ((mi - 64) / 2) + (mi & 1), u2 = m / 5;
        const u32x4 fb = u2 < 8 ? P[0][u2 & 7] : P[1][u2 < 11 ? (u2 - 8) & 3 : (u2 - 7) & 7];
        if (m < 5) asm volatile("v_mfma_f32_16x16x32_bf16 %0, %1, %2, 0" : "=&v"(acc[m % 5]) : "v"(af[m % NAF]), "v"(fb));
        else asm volatile("v_mfma_f32_16x16x32_bf16 %0, %1, %2, %0" : "+v"(acc[m % 5]) : "v"(af[m % NAF]), "v"(fb));
        SCHED();
        if (mi == NM0) RBAR();
        if (mi >= NM0 && mi < NM0 + 3) dma1k(rs, lds + wl[(mi - NM0) % 3], l16, wg + wl[(mi - NM0) % 3]);
        if (mi >= 5 && mi < 19) p_chk(0, 1, mi - 5);
        if (mi >= GM && mi < GM + 14) glue(mi - GM);
        if (mi >= 47 && mi < 54) { p_chk(1, 1, 2 * (mi - 47)); SCHED(); p_chk(1, 1, 2 * (mi - 47) + 1); }
        if (!XE && mi == 3) { xo = (l8 & 0x180u) + (unsigned)psi((l8 >> 3) & 15) * 8u; asm volatile("" : "+v"(xo)); }
        if (!XE && mi >= 4 && mi < 7) xp[(mi - 4) % 3] = __builtin_amdgcn_raw_buffer_load_b64(rs, xo + (unsigned)(mi - 4) * 512u, xs, BULK_AUX);
        if (mi >= NM0 && mi < 40 && NA1 + mi - NM0 < 60) LDA(NA1 + mi - NM0);
        if (mi >= 66 && mi < 69) *(f32x4*)(lds + eo + (mi - 66) * 1024) = acc[(mi - 66) % 3];
        SCHED();
      });
    };
    __builtin_amdgcn_s_setprio(3);
#pragma unroll
    for (int k = 18; k < 22; ++k) red_fin(5, k);
    if (!XE) asm volatile("v_lshrrev_b32 %0, 1, %1" : "=v"(l8) : "v"(l16));
    if (RO) asm volatile("s_nop 2"); else { RED_PUT(6, 0); RED_PUT(6, 1); }
    RED_PUT(6, 2); RED_PUT(6, 3);
    red_fin(6, 16); red_fin(6, 17);
    SCHED();
    BAR();
    sfor<0, 8>([&](auto I_) __attribute__((always_inline)) { RED_GET(decltype(I_)::value); });
    SCHED();
#pragma unroll
    for (int n = NA0; n < NA1; ++n) LDA(n);
    SCHED();
    sfor<0, 20>([&](auto K_) __attribute__((always_inline)) {
      constexpr int k = decltype(K_)::value;
      if (k < 8 && !(k & 1)) asm volatile("s_waitcnt lgkmcnt(%0)" :: "n"(6 - k + NA1 - NA0) : "memory");
      red_fin(6, k < 16 ? k : k + 2);
    });
    SCHED();
    __builtin_amdgcn_s_setprio(0);
    STAMPT(16);
#pragma unroll
    for (int i = 8 - P1S; i < 8; ++i) ld_p(0, i);
#pragma unroll
    for (int k = CE ? 9 : 0; k < 14; ++k) p_chk(0, 0, k);
    SCHED();
    g3(IC<0>(), IC<MB>());
    STAMPT(22);
    DRAIN();
    RBAR();
    STAMPT(27);
#define MY 12
    if (MY == 0 || !kh) {
#pragma unroll
      for (int i = 0; i < 8; ++i) { ld_p(1, i); SCHED(); }
    }
    g3(IC<MB>(), IC<MB + MY>());
    if (MY > 0 && kh) {
#pragma unroll
      for (int i = 0; i < 8; ++i) { ld_p(1, i); SCHED(); }
    }
    STAMPT(26);
    g3(IC<MB + MY>(), IC<40>());
    STAMPT(24);
#pragma unroll
    for (int m = NA1 + 40 - NM0; m < 70; ++m) LDA(m);
#pragma unroll
    for (int x = 0; x < 3; ++x) gf[x] = *(const f32x4*)(lds + go + x * 64);
    SCHED();
    const rsrc_t ro = __builtin_amdgcn_make_buffer_rsrc((void*)p.out, 0, S_ * D_ * 2, 0x00020000);
    STAMPT(17);
#pragma unroll
    for (int k = 0; k < 14; ++k) p_chk(1, 0, k);
    asm volatile("s_nop 1");
    STAMPT(25);
    g3(IC<40>(), IC<70>());
    STAMPT(18);
    BAR();
    STAMPT(28);
    f32x4 o4[3];
    u32x4 ov[2];
#pragma unroll
    for (int j = 2; j >= 0; --j) o4[j] = *(const volatile LAS f32x4*)(lds + ho + j * 1024);
    asm volatile("" : "+v"(acc[2]), "+v"(acc[3]), "+v"(acc[4]));
    SCHED();
#pragma unroll
    for (int j = 2; j >= 0; --j) {
      const f32x4 v = __builtin_elementwise_fma(gf[j], o4[j] + acc[2 + j], xf[j]);
      *(volatile LAS u32x2*)(lds + tw + j * 32) = (u32x2){pack2(v[0], v[1]), pack2(v[2], v[3])};
      if (j < 2) ov[j] = *(const volatile LAS u32x4*)(lds + tr + j * 32);
    }
    __builtin_amdgcn_raw_buffer_store_b128(ov[1], ro, oo + 32u, os, 0);
    __builtin_amdgcn_raw_buffer_store_b128(ov[0], ro, o1, os, 0);
    if (bid == 0 && w == 5 && l8 == 0u) st4(rs, WS_EPOCH, tag);
  }
  STAMP(19);
}

extern "C" __device__ __attribute__((used, noinline, visibility("hidden"))) void mega_tail() {}

extern "C" void kernel_launch(void* const* d_in, const int* in_sizes, int n_in, void* d_out, int out_size,
                              void* d_ws, size_t ws_size, hipStream_t stream) {
  Params p;
  p.q = (const u16*)d_in[0];
  p.k = (const u16*)d_in[1];
  p.v = (const u16*)d_in[2];
  p.shift_k = (const u16*)d_in[3];
  p.ssm_k = (const u16*)d_in[4];
  p.ssm_D = (const u16*)d_in[5];
  p.w_in = (const u16*)d_in[7];
  p.w_sp = (const u16*)d_in[8];
  p.b_sp = (const u16*)d_in[9];
  p.w_out = (const u16*)d_in[10];
  p.cvec = (const u16*)d_in[11];
  p.w_mod = (const u16*)d_in[12];
  p.out = (u16*)d_out;
  p.ws = (char*)d_ws;
  mega<<<256, 512, 0, stream>>>(p);
}
```

```cpp
#include <hip/hip_runtime.h>
#include <stdint.h>

typedef unsigned short u16;
typedef __attribute__((ext_vector_type(8))) short bf16x8;
typedef __attribute__((ext_vector_type(4))) short bf16x4;
typedef __attribute__((ext_vector_type(4))) float f32x4;
typedef __attribute__((ext_vector_type(2))) float f32x2;
typedef __attribute__((ext_vector_type(4))) unsigned int u32x4;
typedef __attribute__((ext_vector_type(2))) unsigned int u32x2;
#define LAS __attribute__((address_space(3)))
typedef __amdgpu_buffer_rsrc_t rsrc_t;
typedef __attribute__((ext_vector_type(4))) short s16x4;

constexpr int S_ = 2048, D_ = 640, F_ = 1728, FH = 864, CB = 108;

constexpr unsigned WS_EPOCH = 0;
constexpr unsigned WS_FWI = 4096;
constexpr unsigned WS_FZ = WS_FWI + 384 * 128;
constexpr unsigned WS_FWO = WS_FZ + 256 * 128;
constexpr unsigned F4S = 896 * 128;
constexpr unsigned WS_CS = WS_FZ + 256 * 128;
constexpr unsigned WS_MOD = 131072;
constexpr unsigned WS_MODP = WS_MOD + 81920;
constexpr unsigned WS_ST1 = 393216;
constexpr unsigned WS_PART = 524288;
constexpr unsigned WS_CSP = WS_PART + 262144;
constexpr unsigned WS_FX = WS_CSP + 655360;
constexpr unsigned WS_X1B = WS_FX + 16 * 4 * 160 * 128;
constexpr unsigned WS_QB = WS_X1B + 2621440;
constexpr unsigned WS_KV3 = WS_QB + 2621440;
constexpr unsigned WS_MODQ = WS_KV3 + 1310720;
constexpr unsigned WBLK = 143360;
constexpr unsigned WS_WINF = WS_MODQ + 245760;
constexpr unsigned WS_WOUTF = WS_WINF + 16 * WBLK;
constexpr unsigned WS_Z2F = WS_WOUTF + 8 * WBLK;
constexpr unsigned WS_UF = WS_Z2F + 8 * 64 * 7168;
constexpr unsigned WS_F4 = WS_UF + 32 * 4 * 8 * 4096;
constexpr unsigned WS_END = WS_F4 + 4 * F4S;
constexpr unsigned WS_STAMP = 200u << 20;

constexpr int G_COPY = 4160;
constexpr int L_U = WBLK;
constexpr int U_CH = (256 + S_) * 2 + 32;
constexpr int L_FLG = L_U + 4 * U_CH;
constexpr int L_RT = 16384;
constexpr int L_CONST = WBLK;
constexpr int L_PST = L_CONST + 2048;
constexpr int L_Z = L_CONST + 5120;
constexpr int L_ST2 = L_Z + 14336;
constexpr int L_MISC = L_ST2 + 512;
constexpr int LDS_TOTAL = 163840;

#define AUX_T 610
#define RT2_T 360
#define CB_T 380
#define W_T 700
#define Y_T 930
#define YC_T 910
#define YG_T 860
#define YPR 0
#define YSL 2
#define K_T 460
#ifndef M_T
#define M_T 520
#endif
#define WA_T 580
constexpr int SC1 = 16, NT = 2;
constexpr unsigned SPIN_CAP = 1u << 16;

__device__ __forceinline__ float bflo(unsigned x) { return __uint_as_float(x << 16); }
__device__ __forceinline__ float bfhi(unsigned x) { return __uint_as_float(x & 0xffff0000u); }
__device__ __forceinline__ unsigned f2bf(float x) {
  __bf16 b = (__bf16)x;
  return (unsigned)__builtin_bit_cast(unsigned short, b);
}
__device__ __forceinline__ float rbf(float x) { return bflo(f2bf(x)); }
typedef __attribute__((ext_vector_type(2))) __bf16 bf2_t;
__device__ __forceinline__ unsigned pack2(float lo, float hi) {
  f32x2 v = {lo, hi};
  return __builtin_bit_cast(unsigned, __builtin_convertvector(v, bf2_t));
}
__device__ __forceinline__ f32x2 rbf2(f32x2 v) {
  const unsigned p = __builtin_bit_cast(unsigned, __builtin_convertvector(v, bf2_t));
  return (f32x2){__uint_as_float(p << 16), __uint_as_float(p & 0xffff0000u)};
}
__device__ __forceinline__ f32x4 rbf4(f32x4 v) {
  const f32x2 a = rbf2((f32x2){v[0], v[1]}), b = rbf2((f32x2){v[2], v[3]});
  return (f32x4){a[0], a[1], b[0], b[1]};
}
template <int CTRL>
__device__ __forceinline__ float dpp_f(float v) {
  return __int_as_float(__builtin_amdgcn_update_dpp(0, __float_as_int(v), CTRL, 0xf, 0xf, true));
}
__device__ __forceinline__ float sum8(float v) {
  v += dpp_f<0xB1>(v);
  v += dpp_f<0x4E>(v);
  v += dpp_f<0x141>(v);
  return v;
}
__device__ __forceinline__ float sum16(float v) {
  v = sum8(v);
  v += dpp_f<0x140>(v);
  return v;
}
__device__ __forceinline__ float sum_rows(float v) {
  auto a = __builtin_amdgcn_permlane16_swap(__float_as_uint(v), __float_as_uint(v), false, false);
  v = __uint_as_float(a[0]) + __uint_as_float(a[1]);
  auto b = __builtin_amdgcn_permlane32_swap(__float_as_uint(v), __float_as_uint(v), false, false);
  return __uint_as_float(b[0]) + __uint_as_float(b[1]);
}
#define MFMA16(a, b, c) __builtin_amdgcn_mfma_f32_16x16x32_bf16(__builtin_bit_cast(bf16x8, a), __builtin_bit_cast(bf16x8, b), c, 0, 0, 0)
#define MFMA16K16(a, b, c) __builtin_amdgcn_mfma_f32_16x16x16bf16_1k(__builtin_bit_cast(bf16x4, a), __builtin_bit_cast(bf16x4, b), c, 0, 0, 0)
__device__ __forceinline__ u32x2 trd(const char* p) {
  return __builtin_bit_cast(u32x2, __builtin_amdgcn_ds_read_tr16_b64_v4i16((LAS s16x4*)p));
}
__device__ __forceinline__ u32x4 trfrag(const char* p, int pitch) {
  u32x2 a = trd(p), b = trd(p + 16 * pitch);
  return (u32x4){a[0], a[1], b[0], b[1]};
}
#define BAR() asm volatile("s_waitcnt lgkmcnt(0)\n\ts_barrier" ::: "memory")
#define DRAIN() do { __builtin_amdgcn_s_waitcnt(0x0F70); asm volatile("" ::: "memory"); } while (0)
#define SCHED() __builtin_amdgcn_sched_barrier(0)
template <int N> struct IC { static constexpr int value = N; };
template <int I, int N, class F>
__device__ __forceinline__ void sfor(F&& f) {
  if constexpr (I < N) { f(IC<I>()); sfor<I + 1, N>(f); }
}

__device__ __forceinline__ u32x4 ld16(rsrc_t r, unsigned off) { return __builtin_amdgcn_raw_buffer_load_b128(r, off, 0, SC1); }
__device__ __forceinline__ u32x2 ld8(rsrc_t r, unsigned off) { return __builtin_amdgcn_raw_buffer_load_b64(r, off, 0, SC1); }
__device__ __forceinline__ unsigned ld4(rsrc_t r, unsigned off) { return __builtin_amdgcn_raw_buffer_load_b32(r, off, 0, SC1); }
#define ST_AUX 16
__device__ __forceinline__ void st16(rsrc_t r, unsigned off, u32x4 v) { __builtin_amdgcn_raw_buffer_store_b128(v, r, off, 0, ST_AUX); }
#define ST16S(v, r, voff, soff, aux) do { const u32x4 v_ = (v); __builtin_amdgcn_raw_buffer_store_b128(v_, r, voff, soff, aux); asm volatile("s_nop 1" :: "v"(v_)); } while (0)
__device__ __forceinline__ void st8(rsrc_t r, unsigned off, u32x2 v) { __builtin_amdgcn_raw_buffer_store_b64(v, r, off, 0, SC1); }
__device__ __forceinline__ void st_flag4(rsrc_t r, unsigned off, unsigned tag, int l) {
  if (l < 32) st16(r, WS_F4 - WS_FWI + off + (unsigned)(l >> 3) * F4S + (unsigned)(l & 7) * 16u, (u32x4){tag, tag, tag, tag});
}
__device__ __forceinline__ void st4(rsrc_t r, unsigned off, unsigned v) { __builtin_amdgcn_raw_buffer_store_b32(v, r, off, 0, SC1); }
#define BULK_AUX 16
__device__ __forceinline__ u32x4 ld16p(rsrc_t r, unsigned off) { return __builtin_amdgcn_raw_buffer_load_b128(r, off, 0, BULK_AUX); }
__device__ __forceinline__ u32x2 ld8p(rsrc_t r, unsigned off) { return __builtin_amdgcn_raw_buffer_load_b64(r, off, 0, BULK_AUX); }
__device__ __forceinline__ void dma1k(rsrc_t r, char* lds_dst, unsigned l16, unsigned soff) {
  __builtin_amdgcn_raw_ptr_buffer_load_lds(r, (LAS void*)lds_dst, 16, l16, soff, 0, BULK_AUX);
}

#define STAMP(i)
#define STAMPF(i)
#define STAMPT(i) STAMP(i)

template <int N, int SLP>
__device__ __forceinline__ void wait_lines(rsrc_t r, const unsigned (&off)[N], unsigned tag) {
  bool need[N];
#pragma unroll
  for (int i = 0; i < N; ++i) need[i] = off[i] != ~0u;
  _Pragma("unroll 1") for (unsigned spins = 0; spins < SPIN_CAP; ++spins) {
    unsigned v[N];
#pragma unroll
    for (int i = 0; i < N; ++i) {
      v[i] = tag;
      if (need[i]) v[i] = ld4(r, off[i]);
    }
    bool any = false;
#pragma unroll
    for (int i = 0; i < N; ++i) { need[i] = v[i] != tag; any = any || need[i]; }
    if (!__any(any)) break;
    __builtin_amdgcn_s_sleep(SLP);
  }
  asm volatile("" ::: "memory");
}

__device__ __forceinline__ int tau(int rt, int n) { return 32 * (rt >> 1) + 8 * (n >> 2) + 4 * (rt & 1) + (n & 3); }
__device__ __forceinline__ int psi(int n) { return 2 * (n & 7) + (n >> 3); }
#define QX_CM(d, s0, s1, P) "v_cndmask_b32_dpp " d ", " s0 ", " s1 ", vcc quad_perm:" P " row_mask:0xf bank_mask:0xf\n\t"
#define QX_BODY(M, P) \
  asm("s_mov_b32 vcc_lo, " M "\n\ts_mov_b32 vcc_hi, " M "\n\t" \
      QX_CM("%0", "%12", "%8", P) "v_xor_b32 %4, %8, %0\n\t" QX_CM("%1", "%13", "%9", P) "v_xor_b32 %5, %9, %1\n\t" \
      QX_CM("%2", "%14", "%10", P) "v_xor_b32 %6, %10, %2\n\t" QX_CM("%3", "%15", "%11", P) "v_xor_b32 %7, %11, %3" \
      : "=&v"(t[0]), "=&v"(t[1]), "=&v"(t[2]), "=&v"(t[3]), "=&v"(u[0]), "=&v"(u[1]), "=&v"(u[2]), "=&v"(u[3]) \
      : "v"(a[0]), "v"(a[1]), "v"(a[2]), "v"(a[3]), "v"(b[0]), "v"(b[1]), "v"(b[2]), "v"(b[3]) : "vcc")
template <int BIT>
__device__ __forceinline__ void quad_xchg3(u32x4& a, u32x4& b) {
  unsigned t[4], u[4];
  if (BIT == 0) QX_BODY("0x55555555", "[1,0,3,2]");
  else QX_BODY("0x33333333", "[2,3,0,1]");
  a = (u32x4){t[0], t[1], t[2], t[3]};
#pragma unroll
  for (int d = 0; d < 4; ++d) b[d] ^= (unsigned)__builtin_amdgcn_update_dpp(0, (int)u[d], BIT == 0 ? 0xB1 : 0x4E, 0xf, 0xf, true);
}
__device__ __forceinline__ void quad_transpose(u32x4& x0, u32x4& x1, u32x4& x2, u32x4& x3) {
  quad_xchg3<0>(x0, x1);
  quad_xchg3<0>(x2, x3);
  quad_xchg3<1>(x0, x2);
  quad_xchg3<1>(x1, x3);
  asm volatile("" : "+v"(x0), "+v"(x1), "+v"(x2), "+v"(x3));
}

struct Params {
  const u16 *q, *k, *v, *shift_k, *ssm_k, *ssm_D, *w_in, *w_sp, *b_sp, *w_out, *cvec, *w_mod;
  u16* out;
  char* ws;
};

__global__ __launch_bounds__(512, 2) void mega(Params p) {
  __shared__ __attribute__((aligned(16))) char lds[LDS_TOTAL];
  {
    unsigned long long pc0;
    unsigned vt;
    asm volatile("v_readfirstlane_b32 vcc_lo, %3\n\ts_cmpk_lt_u32 %2, 192\n\ts_cbranch_scc1 4f\n\ts_cmpk_lt_u32 vcc_lo, 64\n\ts_cbranch_scc0 4f\n\t"
                 "s_lshr_b32 vcc_hi, %2, 3\n\ts_sub_u32 vcc_hi, vcc_hi, 24\n\ts_lshl_b32 vcc_hi, vcc_hi, 13\n\t"
                 "v_lshlrev_b32 %1, 7, %3\n\tv_add_u32 %1, vcc_hi, %1\n\ts_getpc_b64 %0\n.Lmega_here:\n\t"
                 "v_min_u32 %1, mega_tail-.Lmega_here-4, %1\n\ts_movk_i32 m0, 0x2000\n\ts_nop 1\n\tglobal_load_lds_dword %1, %0\n4:\n\t"
                 "s_cmpk_lt_u32 %2, 160\n\ts_cbranch_scc0 1f\n\ts_cmpk_lt_u32 vcc_lo, 256\n\ts_cbranch_scc1 3f\n\t"
                 "s_call_b64 vcc, .Ltouch1\n\ts_branch 3f\n1:\n\t"
                 "s_cmpk_lt_u32 vcc_lo, 256\n\ts_cbranch_scc1 2f\n\ts_cmpk_lt_u32 %2, 224\n\ts_cbranch_scc0 2f\n\ts_call_b64 vcc, .Ltouch3\n\ts_branch 3f\n"
                 "2:\n\ts_call_b64 vcc, .Ltouch2\n3:"
                 : "=&s"(pc0), "=&v"(vt) : "s"(blockIdx.x), "v"(threadIdx.x) : "vcc", "scc", "memory");
  }
  SCHED();
  const int tid = threadIdx.x, l = tid & 63;
  const int w = __builtin_amdgcn_readfirstlane(tid >> 6);
  const int bid = blockIdx.x, xcd = bid & 7, idx = bid >> 3;
#define TOUCH_AT(n) asm volatile("s_branch 1f\n.Ltouch" #n ":\n\ts_setpc_b64 vcc\n1:")
  char* const ws = p.ws;
  asm volatile("" ::"s"(p.q), "s"(p.k), "s"(p.v), "s"(p.shift_k), "s"(p.ssm_k), "s"(p.ssm_D), "s"(p.w_in), "s"(p.w_sp), "s"(p.b_sp),
               "s"(p.w_out), "s"(p.cvec), "s"(p.w_mod), "s"(p.out), "s"(p.ws));
  unsigned epoch, tag, t_start;
  unsigned long long t64;
  asm volatile("s_load_dword %0, %2, 0x0\n\ts_memrealtime %1" : "=&s"(epoch), "=&s"(t64) : "s"(ws));
#define TAG_READY() do { SCHED(); \
    asm volatile("s_waitcnt lgkmcnt(0)\n\ts_mul_i32 %0, %2, 0x9e3779b1\n\ts_add_i32 %0, %0, 0x9e3779b9\n\ts_mov_b64 %1, %3" \
                 : "=&s"(tag), "=&s"(t64r) : "s"(epoch), "s"(t64) : "scc"); t_start = (unsigned)t64r; SCHED(); } while (0)
  unsigned long long t64r;
  const rsrc_t rs = __builtin_amdgcn_make_buffer_rsrc(ws, 0, WS_END, 0x00020000);
  const unsigned l16 = (unsigned)l * 16u;
  STAMP(0);
  const bool is_conv = idx < 20;
  const int quad = xcd * 20 + idx;
  const int R = is_conv ? (xcd & 3) * 5 + (idx >> 2) : (xcd & 3) * 3 + 15 + (idx >> 2);
  const int c = (xcd >> 2) * 4 + (idx & 3);
  const unsigned fwi = WS_F4 + (unsigned)(xcd & 3) * F4S, fz = fwi + (WS_FZ - WS_FWI);
  const int n16 = l & 15, h4 = l >> 4;
  u32x4 wsp[4][8];
#define ROTC 2
#define WAUX 0
  const int rotg = ((idx >> 4) ^ ((ROTC & 2) ? idx >> 1 : 0)) & 1, rotb = (idx >> 2) & 3, rotr = (ROTC & 1) ? (idx & 1) * 2 : 0;
  const rsrc_t rsp = __builtin_amdgcn_make_buffer_rsrc((void*)p.w_sp, 0, S_ * S_ * 2, 0x00020000);
  const unsigned wsp_vo = (unsigned)(((l >> 2) & 3) * 8 * 4096 + h4 * 64 + ((l + rotb) & 3) * 16);
#define LDW(k) LDW_(k)
#define WSP_TR(g) WSP_TR_(g)
#define WROW R
#define LDW_(k) wsp[((k) >> 2) & 3][((k) >> 4) * 4 + ((k) & 3)] = __builtin_amdgcn_raw_buffer_load_b128(rsp, wvo, \
    (unsigned)((WROW * 64 + 16 * ((((k) >> 2) & 2) ^ rotr) + 4 * (((k) >> 2) & 1) + ((k) & 3)) * 4096 + w * 512 + ((((k) >> 4) ^ rotg) * 256)), WAUX)
#define WSP_TR_(g) quad_transpose(wsp[(g) & 3][((g) >> 2) * 4], wsp[(g) & 3][((g) >> 2) * 4 + 1], wsp[(g) & 3][((g) >> 2) * 4 + 2], wsp[(g) & 3][((g) >> 2) * 4 + 3])
  const rsrc_t rwi = __builtin_amdgcn_make_buffer_rsrc((void*)p.w_in, 0, D_ * F_ * 2, 0x00020000);
  const rsrc_t rwo = __builtin_amdgcn_make_buffer_rsrc((void*)p.w_out, 0, FH * D_ * 2, 0x00020000);
  const rsrc_t rq = __builtin_amdgcn_make_buffer_rsrc((void*)p.q, 0, S_ * D_ * 2, 0x00020000);
  auto hold = [&](unsigned tt) { while ((unsigned)__builtin_amdgcn_s_memrealtime() - t_start < tt) __builtin_amdgcn_s_sleep(2); };
  auto ne2 = [&](unsigned x, unsigned y) { unsigned d; asm("v_bitop3_b32 %0, %1, %2, %3 bitop3:0x7e" : "=v"(d) : "v"(x), "v"(y), "s"(tag)); return d; };
  volatile LAS unsigned* lf = (volatile LAS unsigned*)(lds + L_FLG);
  auto lf_set = [&](int i) { asm volatile("s_waitcnt lgkmcnt(0)" ::: "memory"); if (l == 0) lf[i] = tag; };
  auto lf_wait = [&](int i0, int n) {
    _Pragma("unroll 1") for (unsigned spins = 0; spins < (SPIN_CAP << 4); ++spins) {
      const unsigned v = lf[i0 + (l & (n - 1))];
      if (__all(v == tag)) break;
      __builtin_amdgcn_s_sleep(1);
    }
    asm volatile("" ::: "memory");
  };
  const unsigned b2o = fwi + (unsigned)((8 + c) * 20 + 5 * (w & 3) + (l < 5 ? l : 0)) * 128u;
  auto b2_wait = [&](u32x2 fv, int slp) {
    if (!__all(fv[0] == tag)) {
      _Pragma("unroll 1") for (unsigned spins = 0; spins < SPIN_CAP; ++spins) {
        if (fv[0] != tag) fv = ld8(rs, b2o);
        if (__all(fv[0] == tag)) break;
        if (slp) __builtin_amdgcn_s_sleep(8); else __builtin_amdgcn_s_sleep(4);
      }
    }
    asm volatile("" ::: "memory");
    if (l < 5) lf[44 + 5 * w + l] = fv[1];
    if (l == 0) lf[36 + w] = tag;
  };
  auto load_b2 = [&](int k0, int k1) {
#pragma unroll
    for (int k = k0; k < k1; ++k) {
      int i = k + (idx >> 2) * 4;
      i = (i >= 35 ? i - 35 : i) + 35 * w;
      dma1k(rs, lds + i * 1024, l16, WS_WINF + (unsigned)(8 + c) * WBLK + (unsigned)i * 1024u);
    }
  };
  char* const wo_st = lds + 122880;
  auto wo_ld = [&]() {
    const int U = R < 28 ? R : 0, u2 = U >> 1, fi = (u2 + 1) % 3;
    const int c0 = 4 * (U & 1) + (u2 < 8 ? u2 >> 1 : u2 < 11 ? 0 : 2);
    const int b0 = u2 < 8 ? CB * c0 + 2 * (u2 & 1) + 96 : CB * (c0 + (fi >> 1)) + 48 + 2 * (fi & 1), d1 = u2 < 8 ? -96 : 16, d2 = d1 + 16;
    const int b3 = u2 < 8 ? b0 - 64 : CB * (c0 + 1) + 50 + 16 * fi;
#pragma unroll
    for (int r = 0; r < 5; ++r) {
      const int g = r * 64 + l, rho = g / 10;
      const int fb = (rho & 16) ? ((rho & 2) ? b3 : b0 + d2) : ((rho & 2) ? b0 + d1 : b0);
      __builtin_amdgcn_raw_ptr_buffer_load_lds(rwo, (LAS void*)(wo_st + r * 1024), 16, (unsigned)((fb + (rho & 13)) * (D_ * 2) + (g % 10) * 16 + c * 160), 0, 0, 0);
    }
  };
  auto rt_ld = [&](int i0, char* st) {
    const int cb = (i0 < 160 ? 8 : 0) + (i0 & 7), Q = (i0 < 160 ? i0 : i0 - 160) >> 3;
#pragma unroll
    for (int r = 0; r < 7; ++r) {
      const int g = r * 64 + l;
      __builtin_amdgcn_raw_ptr_buffer_load_lds(rwi, (LAS void*)(st + r * 1024), 16, (unsigned)((g / 14) * (F_ * 2) + (g % 14) * 16 + Q * 32 * F_ * 2 + cb * CB * 2),
                                               0, 0, NT);
    }
  };
  auto rt_job = [&](int i0, char* st) {
    const int cb = (i0 < 160 ? 8 : 0) + (i0 & 7), Q = (i0 < 160 ? i0 : i0 - 160) >> 3;
    const int col = 640 * (l >> 5) + 32 * Q + (l & 31);
    DRAIN();
    u32x4 x[7], o[7];
    f32x2 xu[7][4];
    {
      const char* bp = st + (4 * h4 + (n16 >> 2)) * 224 + (n16 & 3) * 8;
#pragma unroll
      for (int j = 0; j < 7; ++j) x[j] = trfrag(bp + j * 32, 224);
#pragma unroll
      for (int j = 0; j < 7; ++j)
#pragma unroll
        for (int e = 0; e < 4; ++e) { xu[j][e] = (f32x2){bflo(x[j][e]), bfhi(x[j][e])}; asm volatile("" : "+v"(xu[j][e])); }
    }
    unsigned mk[4][2], pv = (unsigned)col * 8u, cw = l >= 32 ? (unsigned)(7168 + (l - 32) * 4) : (unsigned)(7298 + l * 2 - 4), m12 = n16 == 12 ? ~0u : 0u;
    unsigned fgo = l < 32 ? (unsigned)(l >> 3) * F4S + (unsigned)(l & 7) * 16u : 0x80000000u;
#pragma unroll
    for (int i = 0; i < 4; ++i) {
      mk[i][0] = n16 == 4 * i ? 0x3F803F80u : 0u; mk[i][1] = n16 == 4 * i + 1 ? ~0u : 0u;
      asm volatile("" : "+v"(mk[i][0]), "+v"(mk[i][1]));
    }
    asm volatile("" : "+v"(pv), "+v"(cw), "+v"(m12), "+v"(fgo));
    unsigned ob = WS_WINF + (unsigned)cb * WBLK + (unsigned)(Q * 7) * 1024u;
    asm volatile("" : "+s"(ob));
    u32x2 pp8[8];
    hold(CB_T);
    _Pragma("unroll 1") for (unsigned spins = 0; spins < SPIN_CAP; ++spins) {
#pragma unroll
      for (int i = 0; i < 8; ++i) pp8[i] = __builtin_amdgcn_raw_buffer_load_b64(rs, pv, WS_MODP + (unsigned)i * 10240u, SC1);
      if (!__any(((ne2(pp8[0][0], pp8[1][0]) | ne2(pp8[2][0], pp8[3][0]) | ne2(pp8[4][0], pp8[5][0])) | ne2(pp8[6][0], pp8[7][0])) != 0u)) break;
      __builtin_amdgcn_s_sleep(4);
    }
    STAMPF(9);
    __builtin_amdgcn_s_setprio(3);
    {
      auto pf = [&](int i) { return __uint_as_float(pp8[i][1]); };
      const float t = rbf(((pf(0) + pf(1)) + (pf(2) + pf(3))) + ((pf(4) + pf(5)) + (pf(6) + pf(7))));
      if (l >= 32) *(float*)(st + cw) = rbf(1.0f + t);
      else *(u16*)(st + cw + 2) = (u16)(__float_as_uint(t) >> 16);
    }
    asm volatile("" ::: "memory");
    const f32x4 ca = *(const volatile LAS f32x4*)(st + 7168 + h4 * 16), cc = *(const volatile LAS f32x4*)(st + 7168 + 64 + h4 * 16);
    const u32x2 e0 = *(const volatile LAS u32x2*)(st + 7296 + h4 * 8), e1 = *(const volatile LAS u32x2*)(st + 7328 + h4 * 8);
    SCHED();
#pragma unroll
    for (int j = 0; j < 7; ++j) {
      const f32x2 y0 = xu[j][0] * (f32x2){ca[0], ca[1]}, y1 = xu[j][1] * (f32x2){ca[2], ca[3]}, y2 = xu[j][2] * (f32x2){cc[0], cc[1]}, y3 = xu[j][3] * (f32x2){cc[2], cc[3]};
      o[j] = (u32x4){pack2(y0[0], y0[1]), pack2(y1[0], y1[1]), pack2(y2[0], y2[1]), pack2(y3[0], y3[1])};
      if (j == 6) {
#pragma unroll
        for (int e = 0; e < 4; ++e) o[j][e] = (o[j][e] & ~m12) | mk[3][0];
      }
      ST16S(o[j], rs, l16 + (unsigned)(j & 3) * 1024u, ob + (unsigned)(j >> 2) * 4096u, SC1);
      SCHED();
    }
    __builtin_amdgcn_s_setprio(0);
    STAMPF(11);
    f32x4 d[2] = {{0.f, 0.f, 0.f, 0.f}, {0.f, 0.f, 0.f, 0.f}};
#pragma unroll
    for (int j = 0; j < 7; ++j) {
      const unsigned one = mk[j & 3][0], sr = mk[j & 3][1];
      d[j >> 2] = MFMA16(((u32x4){one, one, one, one}), o[j], d[j >> 2]);
      d[j >> 2] = MFMA16(((u32x4){sr & e0[0], sr & e0[1], sr & e1[0], sr & e1[1]}), x[j], d[j >> 2]);
    }
#pragma unroll
    for (int m = 0; m < 2; ++m)
      st16(rs, WS_CSP + (unsigned)((cb * 20 + Q) * 2 + m) * 1024u + l16, (u32x4){tag, __float_as_uint(d[m][0]), tag, __float_as_uint(d[m][1])});
    STAMPF(13);
    u32x4 tg4 = {tag, t_start, tag, tag};
    asm volatile("s_waitcnt vmcnt(2)" : "+v"(tg4) :: "memory");
    ST16S(tg4, rs, fgo, WS_F4 + (unsigned)(cb * 20 + Q) * 128u, SC1);
  };

  auto red8 = [&](float v) { v += dpp_f<0x128>(v); return sum_rows(v); };
  const rsrc_t rwm = __builtin_amdgcn_make_buffer_rsrc((void*)p.w_mod, 0, D_ * 3840 * 2, 0x00020000);
  auto gemv_load = [&](u32x4* pre, int blk, int sl, int np) {
    pre[10][0] = *(const unsigned*)(p.cvec + 8 * np * sl + 2 * (l < 4 * np ? l : 0));
#pragma unroll
    for (int pp = 0; pp < np; ++pp)
      pre[pp] = __builtin_amdgcn_raw_buffer_load_b128(rwm, (unsigned)((l >> 3) * 7680 + (l & 7) * 16), (unsigned)((8 * np * sl + pp * 8) * 7680 + blk * 128), NT);
  };
  auto gemv_fin = [&](float* acc, const u32x4* pre, int np, char* scr) {
    const unsigned cv2 = pre[10][0];
    unsigned* svt = (unsigned*)scr + w * 96;
#pragma unroll
    for (int e = 0; e < 2; ++e) {
      const int row = 2 * l + e;
      const float cv = e ? bfhi(cv2) : bflo(cv2);
      if (l < 4 * np) svt[(row & 7) * 12 + (row >> 3)] = __float_as_uint(rbf(cv * __builtin_amdgcn_rcpf(1.0f + __expf(-cv))));
    }
    asm volatile("s_waitcnt lgkmcnt(0)" ::: "memory");
    u32x4 sv[3];
#pragma unroll
    for (int i = 0; i < (np + 3) / 4; ++i) sv[i] = *(const u32x4*)(svt + (l >> 3) * 12 + 4 * i);
    SCHED();
#pragma unroll
    for (int e = 0; e < 8; ++e) acc[e] = 0.f;
#pragma unroll
    for (int pp = 0; pp < np; ++pp) {
      const unsigned sh = sv[pp >> 2][pp & 3], sl2 = sh >> 16;
#pragma unroll
      for (int e = 0; e < 4; ++e) {
        const unsigned x = pre[pp][e];
        acc[2 * e] = __builtin_amdgcn_fdot2_f32_bf16(__builtin_bit_cast(bf2_t, x), __builtin_bit_cast(bf2_t, sl2), acc[2 * e], false);
        acc[2 * e + 1] = __builtin_amdgcn_fdot2_f32_bf16(__builtin_bit_cast(bf2_t, x), __builtin_bit_cast(bf2_t, sh), acc[2 * e + 1], false);
      }
    }
#pragma unroll
    for (int e = 0; e < 8; ++e) acc[e] = red8(acc[e]);
  };
  auto gemv_put = [&](const float* acc, unsigned off) {
    float v = acc[0];
#pragma unroll
    for (int e = 1; e < 8; ++e) { v = (l >> 3) == e ? acc[e] : v; asm("" : "+v"(v)); }
    st8(rs, off + (unsigned)l * 8u, (u32x2){tag, (unsigned)__builtin_amdgcn_ds_bpermute(((l & 7) * 8 + (l >> 3)) * 4, (int)__float_as_uint(v))});
  };
  auto cs_ld = [&](u32x4* v, int cj) {
    const int cb = cj / 7, j = cj % 7;
#pragma unroll
    for (int k = 0; k < 5; ++k)
      v[k] = __builtin_amdgcn_raw_buffer_load_b128(rs, (unsigned)(h4 * 10240 + n16 * 16), WS_CSP + (unsigned)((cb * 40 + (j >> 2) + 2 * k) * 1024 + (j & 3) * 256), SC1);
  };
  auto cs_fin = [&](u32x4* v, int cj) {
    auto bad = [&]() { return __any(((ne2(v[0][0], v[0][2]) | ne2(v[1][0], v[1][2]) | ne2(v[2][0], v[2][2])) | ne2(v[3][0], v[3][2]) | ne2(v[4][0], v[4][2])) != 0u); };
    if (bad()) {
      _Pragma("unroll 1") for (unsigned spins = 0; spins < SPIN_CAP; ++spins) {
        __builtin_amdgcn_s_sleep(8);
        cs_ld(v, cj);
        if (!bad()) break;
      }
    }
    float t0 = 0.f, t1 = 0.f;
#pragma unroll
    for (int k = 0; k < 5; ++k) { t0 += __uint_as_float(v[k][1]); t1 += __uint_as_float(v[k][3]); }
    t0 = sum_rows(t0); t1 = sum_rows(t1);
    if (l < 16) st16(rs, WS_CS + (unsigned)((cj / 7) * 2048 + (cj % 7) * 256) + l16, (u32x4){tag, __float_as_uint(t0), tag, __float_as_uint(t1)});
  };
  const bool cs2 = w == 2 && R >= 20 && R < 27, cs1 = w == 2 && R >= 25;
  u32x4 csv[5];
  typedef const __attribute__((address_space(4))) unsigned* cptr_t;

  if (__builtin_expect(is_conv, 1)) {
    if (w >= 4) {
      TOUCH_AT(1);
      const int pw = w - 4;
      const rsrc_t ra = __builtin_amdgcn_make_buffer_rsrc((void*)((idx & 2) ? p.v : p.k), 0, S_ * D_ * 2, 0x00020000);
      const rsrc_t rb = __builtin_amdgcn_make_buffer_rsrc((void*)((idx & 2) ? p.k : p.v), 0, S_ * D_ * 2, 0x00020000);
      const rsrc_t rk = __builtin_amdgcn_make_buffer_rsrc((void*)p.k, 0, S_ * D_ * 2, 0x00020000);
      const rsrc_t rv = __builtin_amdgcn_make_buffer_rsrc((void*)p.v, 0, S_ * D_ * 2, 0x00020000);
#define NROT 2
      const unsigned vo = (unsigned)((128 * pw + 2 * l) * (D_ * 2));
      const unsigned so0 = (unsigned)((idx & 1) * (D_ * 2) + quad * 8), so1 = (unsigned)((1 - (idx & 1)) * (D_ * 2) + quad * 8);
      u32x2 g[3][4], kh;
      auto gl = [&](int s, int j0, int j1) {
        __builtin_amdgcn_s_setprio(0);
#pragma unroll
        for (int j = j0; j < j1; ++j) {
          if (s < NROT) g[s][j] = __builtin_amdgcn_raw_buffer_load_b64(j < 2 ? ra : rb, vo, ((j & 1) ? so1 : so0) + (unsigned)(s * 512 * D_ * 2), 0);
          else g[s][j] = __builtin_amdgcn_raw_buffer_load_b64(j < 2 ? rk : rv, vo + (unsigned)((j & 1) * D_ * 2), (unsigned)(quad * 8 + s * 512 * D_ * 2), 0);
          SCHED();
        }
        __builtin_amdgcn_s_setprio(3);
      };
      const cptr_t sp = (cptr_t)(p.shift_k + quad * 16);
      unsigned spv[8];
#pragma unroll
      for (int i = 0; i < 8; ++i) spv[i] = sp[i];
      __builtin_amdgcn_s_sleep(3);
      gl(0, 0, 4);
      {
        const int row = 512 * (l >> 2) + 128 * pw - 3 + (l & 3);
        kh = __builtin_amdgcn_raw_buffer_load_b64(rk, (l >= 12 || (l & 3) == 3 || row < 0) ? 0x80000000u : (unsigned)(row * D_ * 2 + quad * 8), 0, 0);
      }
      TAG_READY();
      f32x2 wt[2][4];
#pragma unroll
      for (int d = 0; d < 2; ++d) {
        wt[d][0] = (f32x2){bflo(spv[4 * d]), bflo(spv[4 * d + 2])}; wt[d][1] = (f32x2){bfhi(spv[4 * d]), bfhi(spv[4 * d + 2])};
        wt[d][2] = (f32x2){bflo(spv[4 * d + 1]), bflo(spv[4 * d + 3])}; wt[d][3] = (f32x2){bfhi(spv[4 * d + 1]), bfhi(spv[4 * d + 3])};
        asm volatile("" : "+s"(wt[d][0]), "+s"(wt[d][1]), "+s"(wt[d][2]), "+s"(wt[d][3]));
      }
      SCHED();
      {
        const int j = idx * 4 + pw;
        const unsigned o = (unsigned)((512 + 64 * (j >> 2) + l) * (D_ * 2) + (((160 * xcd) >> 7) + ((j >> 1) & 1)) * 128);
        __builtin_amdgcn_raw_ptr_buffer_load_lds((j & 1) ? ra : rb, (LAS void*)(lds + 98304), 4, j < 64 ? o : 0x80000000u, 0, 0, 0);
      }
      SCHED();
      gl(1, 0, 4);
      STAMP(21);
      unsigned up = (unsigned)(L_U + 512 + 256 * pw + (l >> 2) * 16 + (3 - (l & 3)) * 4);
      unsigned lfo = l == 0 ? (unsigned)(L_FLG + 4 * pw) : (unsigned)(100352 + tid * 4), lfr = (unsigned)(L_FLG + (l & 3) * 4);
      unsigned yr = (unsigned)(L_U + (n16 >> 2) * U_CH + (128 * pw + 32 * h4 + 8 * (n16 & 1) + 4 * ((n16 >> 1) & 1)) * 2);
      unsigned yo = (unsigned)(8 * pw + (l >> 3)) * 20480u + (unsigned)quad * 128u + (unsigned)(l & 7) * 16u;
      unsigned mo = (unsigned)((n16 < 12 ? n16 >> 2 : 0) * D_ + quad * 4 + (l & 3)) * 8u + (unsigned)h4 * 40960u;
      unsigned k3o = (unsigned)(16 * pw + (l >> 2)) * 20480u + (unsigned)(((l >> 1) & 1) * 10240 + (2 * quad + (l & 1)) * 16);
      unsigned k3d = (unsigned)(L_U + (2 * (l & 1) + ((l >> 1) & 1)) * U_CH + 512 + (1536 + 128 * pw + 8 * (l >> 2)) * 2);
      unsigned xfo = (unsigned)(((4 * (l >> 5) + pw) * 4 + ((l >> 3) & 3)) * 160 + quad) * 128u + (unsigned)(l & 7) * 16u;
      u32x4 tg4 = {tag, tag, tag, tag};
      asm volatile("" : "+v"(up), "+v"(lfo), "+v"(lfr), "+v"(yr), "+v"(yo), "+v"(mo), "+v"(k3o), "+v"(k3d), "+v"(xfo), "+v"(tg4));
      unsigned so = WS_ST1 + (unsigned)xcd * 16384u + (unsigned)pw * 1024u;
      asm volatile("" : "+s"(so));
      auto pf_set = [&](int i) { asm volatile("" ::: "memory"); *(volatile LAS unsigned*)(lds + lfo + 4 * i) = tg4[0]; };
      auto pf_wait = [&](int i0) {
        _Pragma("unroll 1") for (unsigned spins = 0; spins < (SPIN_CAP << 4); ++spins) {
          if (__all(*(volatile LAS unsigned*)(lds + lfr + 4 * i0) == tag)) break;
          __builtin_amdgcn_s_sleep(1);
        }
        asm volatile("" ::: "memory");
      };
      unsigned hl[2][3];
      auto halo = [&](int s) {
#pragma unroll
        for (int i = 0; i < 6; ++i) { hl[i & 1][i >> 1] = (unsigned)__builtin_amdgcn_readlane(kh[i & 1], 4 * s + (i >> 1)); asm volatile("" : "+v"(hl[i & 1][i >> 1])); }
      };
      auto kv = [&](auto S__, auto N__) {
        constexpr int s = decltype(S__)::value;
        u32x2* x = g[s];
        SCHED();
        asm volatile("s_waitcnt vmcnt(%5)" : "+v"(x[0]), "+v"(x[1]), "+v"(x[2]), "+v"(x[3]), "+v"(kh) : "n"(decltype(N__)::value));
        if (s == 2) STAMP(27);
        if (s == 0) halo(0);
        if (s < NROT) {
          if (idx & 1) { asm volatile(""); const u32x2 t = x[0], u = x[2]; x[0] = x[1]; x[1] = t; x[2] = x[3]; x[3] = u; }
          asm volatile("" : "+v"(x[0]), "+v"(x[1]), "+v"(x[2]), "+v"(x[3]));
          if (idx & 2) { asm volatile(""); const u32x2 t = x[0], u = x[1]; x[0] = x[2]; x[1] = x[3]; x[2] = t; x[3] = u; }
          asm volatile("" : "+v"(x[0]), "+v"(x[1]), "+v"(x[2]), "+v"(x[3]));
        }
        f32x2 kf[2][5], o[2][2];
#pragma unroll
        for (int d = 0; d < 2; ++d) {
          unsigned kk[5];
          kk[3] = x[0][d]; kk[4] = x[1][d];
          kk[2] = __builtin_amdgcn_update_dpp(hl[d][2], kk[4], 0x138, 0xf, 0xf, false);
          kk[1] = __builtin_amdgcn_update_dpp(hl[d][1], kk[3], 0x138, 0xf, 0xf, false);
          kk[0] = __builtin_amdgcn_update_dpp(hl[d][0], kk[2], 0x138, 0xf, 0xf, false);
#pragma unroll
          for (int j = 0; j < 5; ++j) kf[d][j] = (f32x2){bflo(kk[j]), bfhi(kk[j])};
        }
#pragma unroll
        for (int t = 0; t < 5; ++t) {
#pragma unroll
          for (int i = 0; i < 4; ++i) {
            const int d = i >> 1, r = i & 1;
            if (t == 4) o[d][r] *= (f32x2){bflo(x[2 + r][d]), bfhi(x[2 + r][d])};
            else if (t == 0) o[d][r] = kf[d][r + 3] * wt[d][0];
            else o[d][r] = __builtin_elementwise_fma(kf[d][r + 3 - t], wt[d][t], o[d][r]);
          }
          asm volatile("" : "+v"(o[0][0]), "+v"(o[0][1]), "+v"(o[1][0]), "+v"(o[1][1]));
        }
#pragma unroll
        for (int i = 0; i < 4; ++i) *(unsigned*)(lds + up + i * U_CH + s * 1024) = pack2(o[i >> 1][1][i & 1], o[i >> 1][0][i & 1]);
        pf_set(4 * s);
        SCHED();
        if (s < 2) halo(s + 1);
        SCHED();
      };
      u32x4 sv[4], qv[4];
      u32x2 mv[2];
      auto ld_e = [&]() {
#pragma unroll
        for (int s = 0; s < 4; ++s) sv[s] = __builtin_amdgcn_raw_buffer_load_b128(rs, l16, so + (unsigned)s * 4096u, SC1);
        SCHED();
      };
      auto s_bad = [&]() { return __any(((ne2(sv[0][1], sv[0][3]) | ne2(sv[1][1], sv[1][3]) | ne2(sv[2][1], sv[2][3])) | ne2(sv[3][1], sv[3][3])) != 0u); };
      kv(IC<0>(), IC<5>());
      STAMP(23);
#define G2A 4
      gl(2, 0, G2A);
      kv(IC<1>(), IC<G2A>());
      gl(2, G2A, 4);
      STAMPF(9);
      u32x4 k3[2];
      auto ld_k3 = [&]() {
        k3[0] = __builtin_amdgcn_raw_buffer_load_b128(rs, k3o, WS_KV3, SC1);
        k3[1] = __builtin_amdgcn_raw_buffer_load_b128(rs, k3o, WS_KV3 + 5120u, SC1);
      };
      hold(K_T);
      ld_k3();
      SCHED();
      ld_e();
      kv(IC<2>(), IC<6>());
      STAMPF(11);
      auto k_bad = [&]() { return __any((ne2(k3[0][1], k3[0][3]) | ne2(k3[1][1], k3[1][3])) != 0u); };
      if (k_bad()) {
        _Pragma("unroll 1") for (unsigned spins = 0; spins < SPIN_CAP; ++spins) {
          __builtin_amdgcn_s_sleep(4);
          ld_k3();
          if (!k_bad()) break;
        }
      }
      *(u32x4*)(lds + k3d) = (u32x4){k3[0][0], k3[0][2], k3[1][0], k3[1][2]};
      pf_set(12);
      SCHED();
      STAMPF(18);
      if (s_bad()) {
        _Pragma("unroll 1") for (unsigned spins = 0; spins < SPIN_CAP; ++spins) {
          __builtin_amdgcn_s_sleep(4);
          ld_e();
          if (!s_bad()) break;
        }
      }
      asm volatile("" ::: "memory");
      SCHED();
      STAMPF(17);
      auto ld_m = [&]() {
        mv[0] = __builtin_amdgcn_raw_buffer_load_b64(rs, mo, WS_MODQ, SC1);
        mv[1] = __builtin_amdgcn_raw_buffer_load_b64(rs, mo, WS_MODQ + 20480u, SC1);
      };
#define MFIRST 1
      if (MFIRST) ld_m();
      SCHED();
#pragma unroll
      for (int s = 0; s < 4; ++s) qv[s] = __builtin_amdgcn_raw_buffer_load_b128(rs, yo, WS_QB + (unsigned)s * (32u * 20480u), BULK_AUX);
      SCHED();
      if (!MFIRST) ld_m();
      SCHED();
      STAMP(2);
      u32x2 yv[4][2];
      auto ld_y = [&](int s) {
#pragma unroll
        for (int e = 0; e < 2; ++e) yv[s][e] = trd(lds + yr + 1024 * s + 32 * e);
      };
      float r1[4][2], m1[4][2];
#pragma unroll
      for (int i = 0; i < 8; ++i) {
        const unsigned x = sv[i >> 1][2 * (i & 1)];
        r1[i >> 1][i & 1] = __builtin_amdgcn_rcpf(bfhi(x));
        m1[i >> 1][i & 1] = -bflo(x) * r1[i >> 1][i & 1];
        asm volatile("" : "+v"(r1[i >> 1][i & 1]), "+v"(m1[i >> 1][i & 1]));
      }
      pf_wait(20);
      ld_y(0); ld_y(1);
      SCHED();
      if (__any(ne2(mv[0][0], mv[1][0]) != 0u)) {
        _Pragma("unroll 1") for (unsigned spins = 0; spins < SPIN_CAP; ++spins) {
          __builtin_amdgcn_s_sleep(4);
          ld_m();
          if (!__any(ne2(mv[0][0], mv[1][0]) != 0u)) break;
        }
      }
      STAMPF(13);
      f32x4 al, be;
      {
        const float mgf = rbf(sum_rows(__uint_as_float(mv[0][1]) + __uint_as_float(mv[1][1])));
        auto rl = [&](int i) { return __uint_as_float(__builtin_amdgcn_readlane(__float_as_uint(mgf), i)); };
        const f32x4 sa = {rl(0), rl(1), rl(2), rl(3)}, ca = {rl(4), rl(5), rl(6), rl(7)}, ga = {rl(8), rl(9), rl(10), rl(11)};
        al = rbf4(ca + 1.0f) * ga;
        be = sa * ga;
        asm volatile("" : "+v"(al), "+v"(be));
      }
      f32x4 qf[2][2], P[2][2];
      auto xpre = [&](int s) {
#pragma unroll
        for (int e = 0; e < 2; ++e) {
          const float r = r1[s][e], m = m1[s][e];
          const f32x4 qq = {bflo(qv[s][2 * e]), bfhi(qv[s][2 * e]), bflo(qv[s][2 * e + 1]), bfhi(qv[s][2 * e + 1])};
          qf[s & 1][e] = qq;
          P[s & 1][e] = __builtin_elementwise_fma(__builtin_elementwise_fma(qq, (f32x4){r, r, r, r}, (f32x4){m, m, m, m}), al, be);
        }
        asm volatile("" : "+v"(qf[s & 1][0]), "+v"(qf[s & 1][1]), "+v"(P[s & 1][0]), "+v"(P[s & 1][1]));
      };
      auto xfin = [&](int s) {
        u32x4 o;
#pragma unroll
        for (int e = 0; e < 2; ++e) {
          const u32x2 y = yv[s][e];
          const f32x4 yf = {bflo(y[0]), bfhi(y[0]), bflo(y[1]), bfhi(y[1])};
          const f32x4 x = __builtin_elementwise_fma(P[s & 1][e], yf, qf[s & 1][e]);
          o[2 * e] = pack2(x[0], x[1]);
          o[2 * e + 1] = pack2(x[2], x[3]);
        }
        ST16S(o, rs, yo, WS_X1B + (unsigned)s * (32u * 20480u), SC1);
      };
      xpre(0); xfin(0);
      xpre(1); xfin(1);
      SCHED();
      xpre(2); xpre(3);
#define FVR 5
#define MSPLIT 0
      if (MSPLIT) {
        pf_wait(24);
        ld_y(2);
        xfin(2);
        SCHED();
        pf_wait(28);
        ld_y(3);
        xfin(3);
      } else {
        pf_wait(28);
        ld_y(2); ld_y(3);
        xfin(2); xfin(3);
      }
      STAMP(28);
      asm volatile("s_waitcnt vmcnt(2)" ::: "memory");
      ST16S(tg4, rs, xfo, WS_FX, SC1);
      SCHED();
      asm volatile("s_waitcnt vmcnt(1)" ::: "memory");
      ST16S(tg4, rs, xfo, WS_FX + 32u * 160u * 128u, SC1);
      STAMP(5);
    } else {
      const int ch = w;
      char* U = lds + L_U + ch * U_CH;
      char* G = lds + ch * (4 * G_COPY);
      {
        const rsrc_t rf = __builtin_amdgcn_make_buffer_rsrc((void*)(p.ssm_k + (size_t)(quad * 4 + ch) * S_), 0, S_ * 2, 0x00020000);
        u32x4 fa[4];
        u32x2 fb[4];
        __builtin_amdgcn_s_setprio(3);
#pragma unroll
        for (int k = 0; k < 4; ++k) {
          fa[k] = __builtin_amdgcn_raw_buffer_load_b128(rf, l16 + (unsigned)k * 1024u, 0, NT);
        }
        __builtin_amdgcn_s_setprio(1);
        TAG_READY();
#pragma unroll
        for (int k = 0; k < 4; ++k) {
#pragma unroll
          for (int d = 0; d < 2; ++d)
            fb[k][d] = __builtin_amdgcn_update_dpp(k < 3 ? (unsigned)__builtin_amdgcn_readfirstlane(fa[k < 3 ? k + 1 : k][d]) : 0u, fa[k][d], 0x130, 0xf, 0xf, false);
          const unsigned x[6] = {fa[k][0], fa[k][1], fa[k][2], fa[k][3], fb[k][0], fb[k][1]};
          unsigned y[5];
#pragma unroll
          for (int d = 0; d < 5; ++d) y[d] = __builtin_amdgcn_alignbit(x[d + 1], x[d], 16);
          char* gp = G + 32 + k * 1024 + l16;
          *(u32x4*)(gp) = fa[k];
          *(u32x4*)(gp + G_COPY) = (u32x4){y[0], y[1], y[2], y[3]};
          *(u32x4*)(gp + 2 * G_COPY) = (u32x4){x[1], x[2], x[3], x[4]};
          *(u32x4*)(gp + 3 * G_COPY) = (u32x4){y[1], y[2], y[3], y[4]};
          if (k == 0) {
            const unsigned f0 = __builtin_amdgcn_readfirstlane(x[0]), f1 = __builtin_amdgcn_readfirstlane(x[1]);
            const unsigned long long f64 = (unsigned long long)f0 | ((unsigned long long)f1 << 32);
            const unsigned long long hi = l ? (f64 << (64 - 16 * (l & 3))) : 0ull;
            if (l < 4) {
              *(u32x4*)(G + l * G_COPY) = (u32x4){0u, 0u, 0u, 0u};
              *(u32x4*)(G + l * G_COPY + 16) = (u32x4){0u, 0u, (unsigned)hi, (unsigned)(hi >> 32)};
            }
            if (l >= 32) *(u32x4*)(U + (l - 32) * 16) = (u32x4){0u, 0u, 0u, 0u};
          }
        }
        asm volatile("s_waitcnt lgkmcnt(0)" ::: "memory");
        STAMP(1);
      }
      const int a = n16, h = h4, jj = h >> 1, e2 = h & 1;
      const char* ap = G + ((a + 1) & 3) * G_COPY + 2 * (16 * jj + a - 8 * e2 + 9 - ((a + 1) & 3));
      const char* bp = U + 512 + 32 * (a - jj) + 16 * e2;
      auto lda = [&](u32x4* A, int r, int p0, int np) {
#pragma unroll
        for (int pp = 0; pp < np; ++pp) {
          u32x2 lo = *(const volatile LAS u32x2*)(ap + 512 * (p0 + pp) + 64 * r);
          u32x2 hi = *(const volatile LAS u32x2*)(ap + 512 * (p0 + pp) + 64 * r + 8);
          A[pp] = (u32x4){lo[0], lo[1], hi[0], hi[1]};
        }
      };
      u32x4 AD;
      {
        const unsigned dDr = (*(cptr_t)(p.ssm_D + quad * 4 + (ch & 2)) >> (16 * (ch & 1))) & 0xffffu;
        const unsigned dv = h == (a >> 3) ? ((a & 1) ? dDr : dDr << 16) : 0u;
        const int dw = (7 - (a & 7)) >> 1;
#pragma unroll
        for (int d = 0; d < 4; ++d) AD[d] = dw == d ? dv : 0u;
      }
      u32x4 A0[8][4];
      f32x4 acc[8];
#pragma unroll
      for (int n = 0; n < 8; ++n) acc[n] = (f32x4){0.f, 0.f, 0.f, 0.f};
#pragma unroll
      for (int r = 0; r < 8; ++r) lda(A0[r], r, 0, 4);
      auto yput = [&](int s) {
#pragma unroll
        for (int e = 0; e < 2; ++e)
          *(u32x2*)(U + 1024 * s + 512 * e + (16 * a + 4 * h) * 2) = (u32x2){pack2(acc[2 * s + e][0], acc[2 * s + e][1]), pack2(acc[2 * s + e][2], acc[2 * s + e][3])};
        asm volatile("" ::: "memory");
        if (l == 0) lf[16 + 4 * s + ch] = tag;
      };
      auto stage = [&](auto S__) {
        constexpr int s = decltype(S__)::value, NH = s < 2 ? 4 - 2 * s : 0, NP = 8 - 2 * s, NB = s < 2 ? 2 : 8;
        u32x4 A1[3][NH > 0 ? NH : 1], B[NB][2];
        auto ldb = [&](int r) {
#pragma unroll
          for (int e = 0; e < 2; ++e) B[r % NB][e] = *(const u32x4*)(bp + 512 * (2 * s + e) - 64 * r);
        };
        if (NH) { lda(A1[0], 0, 4, NH); lda(A1[1], 1, 4, NH); }
        SCHED();
        if (s > 0) yput(s - 1);
        lf_wait(4 * s, 4);
        if (s == 0) STAMP(3);
#pragma unroll
        for (int r = 0; r < NB; ++r) ldb(r);
        SCHED();
#pragma unroll
        for (int e = 0; e < 2; ++e) acc[2 * s + e] = MFMA16(AD, B[0][e], acc[2 * s + e]);
#pragma unroll
        for (int r = 0; r < 8; ++r) {
          if (NH && r + 2 < 8) lda(A1[(r + 2) % 3], r + 2, 4, NH);
          SCHED();
#pragma unroll
          for (int e = 0; e < 2; ++e)
#pragma unroll
            for (int pp = 0; pp < NP - e; ++pp) acc[2 * s + e + pp] = MFMA16(pp < 4 ? A0[r][pp] : A1[r % 3][pp < 4 ? 0 : pp - 4], B[r % NB][e], acc[2 * s + e + pp]);
          SCHED();
          if (r + NB < 8) ldb(r + NB);
        }
      };
      stage(IC<0>());
      STAMP(4);
      stage(IC<1>());
      STAMPF(9);
      u32x2 fv;
      if (MSPLIT) {
        u32x4 B[8][2];
        auto ldb = [&](int r, int m0) {
#pragma unroll
          for (int m = 0; m < 2; ++m) B[r][m] = *(const u32x4*)(bp + 512 * (m0 + m) - 64 * r);
        };
        yput(1);
        if (w == 3) wo_ld();
        __builtin_amdgcn_s_setprio(3);
        lf_wait(8, 4);
        STAMP(22);
#pragma unroll
        for (int r = 0; r < 8; ++r) ldb(r, 4);
        SCHED();
#pragma unroll
        for (int m = 0; m < 2; ++m) acc[4 + m] = MFMA16(AD, B[0][m], acc[4 + m]);
#pragma unroll
        for (int r = 0; r < 8; ++r) {
          acc[4] = MFMA16(A0[r][0], B[r][0], acc[4]);
          acc[5] = MFMA16(A0[r][1], B[r][0], acc[5]);
          acc[5] = MFMA16(A0[r][0], B[r][1], acc[5]);
          SCHED();
        }
#pragma unroll
        for (int r = 0; r < 8; ++r) {
          acc[6] = MFMA16(A0[r][2], B[r][0], acc[6]);
          acc[7] = MFMA16(A0[r][3], B[r][0], acc[7]);
          acc[6] = MFMA16(A0[r][1], B[r][1], acc[6]);
          acc[7] = MFMA16(A0[r][2], B[r][1], acc[7]);
          SCHED();
          if (r == 0) yput(2);
          if (r == FVR) fv = ld8(rs, b2o);
          if (r == 3) {
            lf_wait(12, 4);
#pragma unroll
            for (int x = 0; x < 4; ++x) ldb(x, 6);
          }
          if (r > 3) ldb(r, 6);
          SCHED();
        }
#pragma unroll
        for (int m = 0; m < 2; ++m) acc[6 + m] = MFMA16(AD, B[0][m], acc[6 + m]);
#pragma unroll
        for (int r = 0; r < 8; ++r) {
          acc[6] = MFMA16(A0[r][0], B[r][0], acc[6]);
          acc[7] = MFMA16(A0[r][1], B[r][0], acc[7]);
          acc[7] = MFMA16(A0[r][0], B[r][1], acc[7]);
          SCHED();
        }
        yput(3);
      } else {
        u32x4 B[4][4];
        auto ldb = [&](int r) {
#pragma unroll
          for (int m = 0; m < 4; ++m) B[r & 3][m] = *(const u32x4*)(bp + 512 * (4 + m) - 64 * r);
        };
        yput(1);
        if (w == 3) wo_ld();
        __builtin_amdgcn_s_setprio(3);
        lf_wait(8, 8);
        STAMP(22);
#pragma unroll
        for (int r = 0; r < 4; ++r) ldb(r);
        SCHED();
#pragma unroll
        for (int m = 0; m < 4; ++m) acc[4 + m] = MFMA16(AD, B[0][m], acc[4 + m]);
#pragma unroll
        for (int r = 0; r < 8; ++r) {
#pragma unroll
          for (int m = 0; m < 4; ++m)
#pragma unroll
            for (int pp = 0; pp < 4 - m; ++pp) acc[4 + m + pp] = MFMA16(A0[r][pp], B[r & 3][m], acc[4 + m + pp]);
          SCHED();
          if (r + 4 < 8) ldb(r + 4);
          if (r == 1) fv = ld8(rs, b2o);
          SCHED();
        }
#pragma unroll
        for (int e = 0; e < 4; ++e)
          *(u32x2*)(U + 2048 + 512 * e + (16 * a + 4 * h) * 2) = (u32x2){pack2(acc[4 + e][0], acc[4 + e][1]), pack2(acc[4 + e][2], acc[4 + e][3])};
        lf_set(28 + ch);
      }
      STAMP(28);
      __builtin_amdgcn_s_setprio(0);
      b2_wait(fv, 0);
      lf_wait(20, 4);
    }
    __builtin_amdgcn_s_setprio(0);
  } else {
    const int at = idx - 20;
    char* const scr = lds + WBLK + 14848;
    auto gv_blk = [&](int j) { return j < 160 ? 30 + (j >> 3) : j < 400 ? (j >> 3) - 20 : j >> 3; };
    auto gv_fin = [&](const u32x4* pre, int j) {
      float acc[8];
      gemv_fin(acc, pre, 10, scr);
      gemv_put(acc, j < 160 ? WS_MODP + (unsigned)((j & 7) * 1280 + (j >> 3) * 64) * 8u : WS_MODQ + (unsigned)((j & 7) * 2560 + ((j >> 3) - 20) * 64) * 8u);
    };
    const int ja = xcd * 12 + at, ai = xcd * 8 + at, bi = xcd * 4 + at - 8, g = w >> 2, wj = w & 3;
    const int rj = w == 0 ? ja : w == 1 ? (at >= 4 ? 92 + ai : -1) : w == 4 ? 160 + ja : w == 5 && at < 8 ? 256 + ai : -1;
    char* const rst = lds + (g ? WBLK : 65536) + wj * 7424;
    u32x4 pre[11];
    if (at >= 8 || g == 0) {
      TOUCH_AT(2);
      const int T = at >= 8 ? 64 + 2 * bi + g : ai;
      unsigned* mds = (unsigned*)(lds + 2048) + 16 * g;
      char* qt = lds + L_RT + g * (16 * 1312);
      u32x4 ld[5];
      const bool e1 = at >= 8 && w == 3;
      if (e1) gemv_load(pre, gv_blk(128 + bi), bi & 7, 10);
      else __builtin_amdgcn_s_sleep(8);
#pragma unroll
      for (int j = 0; j < 5; ++j)
        ld[j] = __builtin_amdgcn_raw_buffer_load_b128(rq, (unsigned)(h4 * D_ * 2 + n16 * 16 + j * 256), (unsigned)((32 * (T >> 1) + 8 * wj + 4 * (T & 1)) * D_ * 2), NT);
      TAG_READY();
      {
        const bf2_t ONE2 = {(__bf16)1.0f, (__bf16)1.0f};
        char* dst = qt + (4 * wj + h4) * 1312 + n16 * 16;
        float sa = 0.f, va = 0.f;
#pragma unroll
        for (int j = 0; j < 5; ++j) {
          *(u32x4*)(dst + j * 256) = ld[j];
#pragma unroll
          for (int d = 0; d < 4; ++d) {
            const unsigned a0 = ld[j][d];
            const bf2_t xa = __builtin_bit_cast(bf2_t, a0);
            sa = __builtin_amdgcn_fdot2_f32_bf16(xa, ONE2, sa, false); va = __builtin_amdgcn_fdot2_f32_bf16(xa, xa, va, false);
          }
        }
        lf_set(8 + w);
        const float ma = sum16(sa) * (1.0f / D_);
        va = __builtin_fmaxf(sum16(va) * (1.0f / D_) - ma * ma, 0.f);
        if (n16 == 0) mds[4 * wj + h4] = pack2(ma, __builtin_amdgcn_sqrtf(rbf(rbf(va) + 1e-5f)));
      }
      if (e1) gv_fin(pre, 128 + bi);
      lf_wait(8 + 4 * g, 4);
      STAMP(23);
      {
        const char* src = qt + (l & 7) * 1312 + (40 * wj + (l >> 3)) * 8;
#pragma unroll
        for (int j = 0; j < 5; ++j) {
          const u32x2 x = *(const u32x2*)(src + j * 64), y = *(const u32x2*)(src + j * 64 + 8 * 1312);
          st16(rs, WS_QB + (unsigned)T * 20480u + (unsigned)(5 * wj + j) * 1024u + l16, (u32x4){x[0], x[1], y[0], y[1]});
        }
      }
      SCHED();
      const int g2 = at < 8 || w < 3 || w == 4 ? 400 : w == 3 ? 384 + bi : 256 + wj * 32 + bi;
      if (g2 < 400) {
        gemv_load(pre, gv_blk(g2), g2 & 7, 10);
        asm volatile("s_waitcnt vmcnt(10)" ::: "memory");
      } else if (w < 2 && rj >= 0) {
        rt_ld(rj, rst);
        asm volatile("s_waitcnt vmcnt(7)" ::: "memory");
      } else {
        DRAIN();
      }
      lf_set(16 + w);
      if (wj == 2) {
        lf_wait(16 + 4 * g, 4);
        st16(rs, WS_ST1 + (unsigned)(l >> 3) * 16384u + (unsigned)T * 128u + (unsigned)(l & 7) * 16u, (u32x4){mds[l & 7], tag, mds[8 + (l & 7)], tag});
        STAMP(22);
      }
      if (w >= 4 && w < 7) lf_set(w);
      if (g2 < 400) gv_fin(pre, g2);
      STAMP(25);
    } else {
      constexpr int KB = L_RT + 16 * 1312;
      TOUCH_AT(3);
      const int g1 = w < 6 ? 2 * ai + wj : 32 + wj * 64 + ai;
      if (w >= 6) {
        const rsrc_t rk = __builtin_amdgcn_make_buffer_rsrc((void*)(w == 6 ? p.k : p.v), 0, S_ * D_ * 2, 0x00020000);
        const unsigned ko = (unsigned)((1536 + 8 * ai) * (D_ * 2) - (w == 6 ? 4096 : 0));
        char* dst = lds + KB + (w == 6 ? 0 : 14336);
        __builtin_amdgcn_s_sleep(3);
#pragma unroll
        for (int n = 0; n < 10; ++n) __builtin_amdgcn_raw_ptr_buffer_load_lds(rk, (LAS void*)(dst + n * 1024), 16, l16, ko + (unsigned)n * 1024u, 0, NT);
        if (w == 6) {
#pragma unroll
          for (int n = 10; n < 14; ++n) __builtin_amdgcn_raw_ptr_buffer_load_lds(rk, (LAS void*)(dst + n * 1024), 16, l16, ko + (unsigned)n * 1024u, 0, NT);
        }
      }
      SCHED();
      u32x4 swt[2];
      swt[0] = *(const u32x4*)(p.shift_k + (tid - 256) * 8);
      swt[1] = *(const u32x4*)(p.shift_k + (256 + l) * 8);
      SCHED();
      if (w >= 6) __builtin_amdgcn_s_sleep(8);
      gemv_load(pre, gv_blk(g1), g1 & 7, 10);
      TAG_READY();
      asm volatile("s_waitcnt vmcnt(11)" : "+v"(swt[0]), "+v"(swt[1]) :: "memory");
      lf_set(8 + w);
      if (w < 6) gv_fin(pre, g1);
      STAMP(25);
      lf_wait(12, 4);
      STAMP(23);
#pragma unroll 1
      for (int n = 0; n < (w == 7 ? 2 : 1); ++n) {
        {
          const int pr = n ? 256 + l : tid - 256;
          const char* kp = lds + KB + 4096 + pr * 4;
          unsigned kk[11], vv[8];
#pragma unroll
          for (int i = 0; i < 11; ++i) kk[i] = *(const unsigned*)(kp + (i - 3) * 1280);
#pragma unroll
          for (int i = 0; i < 8; ++i) vv[i] = *(const unsigned*)(kp + 10240 + i * 1280);
          const u32x4 sw = n ? swt[1] : swt[0];
          const f32x2 wt[4] = {{bflo(sw[0]), bflo(sw[2])}, {bfhi(sw[0]), bfhi(sw[2])}, {bflo(sw[1]), bflo(sw[3])}, {bfhi(sw[1]), bfhi(sw[3])}};
          f32x2 o[8];
#pragma unroll
          for (int i = 0; i < 8; ++i) {
            f32x2 t = (f32x2){bflo(kk[i + 3]), bfhi(kk[i + 3])} * wt[0];
#pragma unroll
            for (int j = 1; j < 4; ++j) t = __builtin_elementwise_fma((f32x2){bflo(kk[i + 3 - j]), bfhi(kk[i + 3 - j])}, wt[j], t);
            o[i] = t * (f32x2){bflo(vv[i]), bfhi(vv[i])};
          }
#pragma unroll
          for (int e = 0; e < 4; ++e) {
            const int i = 7 - 4 * (e & 1);
            st16(rs, WS_KV3 + (unsigned)ai * 20480u + (unsigned)(e * 5120 + pr * 16),
                 (u32x4){pack2(o[i][e >> 1], o[i - 1][e >> 1]), tag, pack2(o[i - 2][e >> 1], o[i - 3][e >> 1]), tag});
          }
        }
      }
      STAMP(22);
      if (w < 7) lf_set(w);
      if (w >= 6) gv_fin(pre, g1);
      STAMPF(30);
    }
    if (w == 6 && ja < 80) {
      hold(AUX_T);
      gemv_load(pre, gv_blk(400 + ja), ja & 7, 10);
      gv_fin(pre, 400 + ja);
    }
    if (rj >= 0) {
      if (w >= 2) { hold(RT2_T); rt_ld(rj, rst); }
      rt_job(rj, rst);
    }
    STAMP(26);
    lf_set(w);
    if (w == 3) { hold(WA_T); wo_ld(); }
    STAMP(3);
    if (w < 4) {
      hold(W_T);
      b2_wait((u32x2){~tag, 0u}, 1);
      lf_wait(0, 8);
      STAMP(4);
    }
  }

  STAMPF(16);
  u32x4 wox[5];
  if (w == 3) {
    DRAIN();
    const char* bp = wo_st + (4 * h4 + (n16 >> 2)) * 160 + (n16 & 3) * 8;
#pragma unroll
    for (int dj = 0; dj < 5; ++dj) wox[dj] = trfrag(bp + dj * 32, 160);
    asm volatile("s_waitcnt lgkmcnt(0)" : "+v"(wox[0]), "+v"(wox[1]), "+v"(wox[2]), "+v"(wox[3]), "+v"(wox[4]) :: "memory");
  }
  auto wo_put = [&]() {
    if (w == 3 && R < 28) {
      const unsigned base = WS_WOUTF + (unsigned)c * WBLK + (unsigned)((R >> 1) * 10240 + (R & 1) * 1024);
#pragma unroll
      for (int dj = 0; dj < 5; ++dj) st16(rs, base + (unsigned)((R & 1) ? dj : (dj + 2) % 5) * 2048u + l16, wox[dj]);
    }
  };
  if (cs2) {
    cs_ld(csv, (8 + c) * 7 + R - 20);
    SCHED();
    load_b2(0, 12);
    SCHED();
    cs_fin(csv, (8 + c) * 7 + R - 20);
  } else if (w < 4) {
    asm volatile("" ::: "memory");
    load_b2(0, 12);
  }
  if (w < 4) load_b2(12, 35);
  wo_put();
  STAMPF(29);
  u32x4 k_c;
  unsigned kc_o = w < 2 ? (unsigned)(tid < 112 ? tid : 0) * 16u : 0x80000000u, kc_d = (unsigned)(L_CONST + (tid & 127) * 8);
  auto kc_ld = [&](int z2) { k_c = __builtin_amdgcn_raw_buffer_load_b128(rs, kc_o, WS_CS + (unsigned)(8 * z2 + c) * 2048u, SC1); };
  auto consts_put = [&](int z2) {
    if (w < 2) {
      if (__any(ne2(k_c[0], k_c[2]) != 0u)) {
        _Pragma("unroll 1") for (unsigned spins = 0; spins < SPIN_CAP; ++spins) {
          __builtin_amdgcn_s_sleep(8);
          kc_ld(z2);
          if (!__any(ne2(k_c[0], k_c[2]) != 0u)) break;
        }
      }
      if (z2) *(u32x2*)(lds + kc_d + 1024) = (u32x2){k_c[1], k_c[3]};
      else { const unsigned d = kc_d - ((kc_d >> 1) & 4u); *(unsigned*)(lds + d) = k_c[1]; *(unsigned*)(lds + d + 8) = k_c[3]; }
    }
  };
  const int wq = w ^ rotr, rt1 = wq & 3, kh1 = w >> 2;
  unsigned bo0 = (unsigned)(kh1 * 71680) + l16, bo1 = bo0 + 35840u;
  asm volatile("" : "+v"(bo0), "+v"(bo1));
  unsigned pso[3], pqo[3], oto = (unsigned)(((w ^ 4) * 7 * 64 + l) * 8), rdo = (unsigned)((w * 7 * 64 + l) * 8), cso = (unsigned)(L_CONST + (128 + n16) * 8);
#pragma unroll
  for (int e = 0; e < 3; ++e) {
    const int row = rt1 * 16 + (e < 2 ? 4 * h4 + 2 * kh1 + e : n16);
    pso[e] = (unsigned)(L_PST + row * 4); pqo[e] = (unsigned)(L_PST + 512 + row * 16 + (row & 3) * 4);
  }
  unsigned spo = (unsigned)(L_Z + tid * 16), p6o = (unsigned)(L_PST + (wq * 16 + 4 * h4) * 4), pgo = (unsigned)(L_PST + 512 + (wq * 16 + n16) * 16);
  asm volatile("" : "+v"(spo), "+v"(p6o), "+v"(pgo));
  p6o = n16 == 12 ? p6o : spo;
  pgo = h4 == (n16 >> 2) ? pgo : spo;
  unsigned bsrc = (unsigned)(((l >> 1) & 3) * 64 + (l & 1) * 4), ppo = l < 8 ? l16 : 0x80000000u;
  asm volatile("" : "+v"(pso[0]), "+v"(pso[2]), "+v"(pqo[0]), "+v"(pqo[2]), "+v"(oto), "+v"(rdo), "+v"(cso), "+v"(p6o), "+v"(pgo), "+v"(bsrc), "+v"(ppo), "+v"(kc_o), "+v"(kc_d));
  pso[1] = pso[0] + 4u; pqo[1] = pqo[0] + 20u;
#define BFRAG(q, j) (*(const u32x4*)(lds + ((q) < 5 ? bo0 : bo1) + ((q) % 5) * 7168 + (j) * 1024))
  u32x4 h2[10];
  unsigned xv = (unsigned)((4 * ((l >> 3) & 1) + h4) * 128 + (l & 7) * 16), xs0 = WS_X1B + (unsigned)(R * 4 + rt1) * 20480u + (unsigned)(10 * kh1) * 1024u;
  unsigned wvo = wsp_vo, bso = (unsigned)tau(rt1, n16) * 2u, fgo = l < 32 ? (unsigned)(l >> 3) * F4S + (unsigned)(l & 7) * 16u : 0x80000000u;
  asm volatile("" : "+v"(xv), "+v"(wvo), "+v"(bso), "+v"(fgo));
#define WO3 (-92)
  unsigned zs[8];
#pragma unroll
  for (int t = 0; t < 8; ++t) zs[t] = WS_Z2F + (unsigned)(c * 64 + 8 * w + 4 * ((t >> 2) ^ rotg) + ((t + rotb) & 3)) * 7168u;
  unsigned wx8 = (unsigned)((w + (idx >> 2)) & 7) * 1024u, wx4 = wx8 & 3072u, wgb = WS_WOUTF + (unsigned)c * WBLK, fzs = fz + (unsigned)(c * 32 + 4 * w) * 128u;
  unsigned uo = WS_UF + (unsigned)((R * 4 + rt1) * 8 + c) * 4096u + (unsigned)kh1 * 2048u, ui = WS_UF + (unsigned)((R * 4 + rt1) * 8 + 4 * kh1) * 4096u;
  unsigned xs = WS_X1B + (unsigned)(R * 4 + rt1) * 20480u + (unsigned)(5 * c + 2 * kh1) * 512u, wl2 = (unsigned)(120 + WO3 + (w < 4 ? w + 16 : w)) * 1024u;
  unsigned os = (unsigned)__builtin_amdgcn_readfirstlane(((R * 64 + tau(rt1, 0)) * D_ + 80 * c + 32 * kh1) * 2);
#define PINS(x) do { x = (unsigned)__builtin_amdgcn_readfirstlane((int)x); asm volatile("" : "+s"(x)); } while (0)
#pragma unroll
  for (int t = 0; t < 8; ++t) PINS(zs[t]);
  PINS(xs0); PINS(wx8); PINS(wx4); PINS(wgb); PINS(fzs); PINS(uo); PINS(ui); PINS(xs); PINS(wl2); PINS(os);
  unsigned zos = WS_Z2F + (unsigned)((c * 64 + 4 * (R >> 1)) * 7 + (w < 6 ? w + 1 : 0)) * 1024u + (unsigned)(R & 1) * 512u, pos = WS_PART + (unsigned)(R * 64 + wq) * 128u;
  PINS(zos); PINS(pos);
  unsigned s1 = (unsigned)((w ^ 4) * 3072 + 1024), s3 = (unsigned)(w * 3072 + 1024);
  PINS(s1); PINS(s3);
#define LDX(q) h2[q] = __builtin_amdgcn_raw_buffer_load_b128(rs, xv + (unsigned)((q) & 3) * 1024u, xs0 + (unsigned)((q) >> 2) * 4096u, BULK_AUX)
  auto flag4 = [&](unsigned off) { ST16S(((u32x4){tag, tag, tag, tag}), rs, fgo, WS_F4 - WS_FWI + off, SC1); };
  unsigned bs_raw, fwo[1];
  f32x4 acc[7];
  float mu_n, rs_n;
  {
    unsigned f1o[1] = {w == 1 && l < 20 ? fwi + (unsigned)(c * 20 + l) * 128u : ~0u}, f1v;
    fwo[0] = w == 3 && l < 28 ? fwi + (WS_FWO - WS_FWI) + (unsigned)(c * 32 + l) * 128u : ~0u;
    asm volatile("" : "+v"(f1o[0]), "+v"(fwo[0]));
    if (w == 7) {
      const unsigned fb = WS_FX + (unsigned)(((R >> 1) * 4 + (R & 1) * 2 + (c >> 2)) * 160 + l) * 128u;
      const unsigned fo[3] = {fb, fb + 8192u, l < 32 ? fb + 16384u : ~0u};
      lf_wait(36, 4);
      int dt = (int)(lf[44 + (l < 20 ? l : 0)] - t_start);
      dt = max(dt, __builtin_amdgcn_update_dpp(0, dt, 0xB1, 0xf, 0xf, true));
      dt = max(dt, __builtin_amdgcn_update_dpp(0, dt, 0x4E, 0xf, 0xf, true));
      dt = max(dt, __builtin_amdgcn_update_dpp(0, dt, 0x124, 0xf, 0xf, true));
      dt = max(dt, __builtin_amdgcn_update_dpp(0, dt, 0x128, 0xf, 0xf, true));
      dt = min(max(max(__builtin_amdgcn_readlane(dt, 0), __builtin_amdgcn_readlane(dt, 16)), 0), 300);
      __builtin_amdgcn_s_setprio(YPR);
      hold((unsigned)(YG_T + dt));
      wait_lines<3, YSL>(rs, fo, tag);
      if (l == 0) lf[33] = tag;
      __builtin_amdgcn_s_setprio(0);
    }
    lf_wait(33, 1);
    STAMP(7);
    SCHED();
#pragma unroll
    for (int q = 0; q < 3; ++q) LDX(q);
    SCHED();
    __builtin_amdgcn_s_waitcnt(0x0F73);
    BAR();
    STAMP(8);
#pragma unroll
    for (int j = 0; j < 7; ++j) acc[j] = (f32x4){0.f, 0.f, 0.f, 0.f};
    f32x4 accg = {0.f, 0.f, 0.f, 0.f};
    u32x4 bf[7];
#pragma unroll
    for (int j = 0; j < 7; ++j) bf[j] = BFRAG(0, j);
    SCHED();
#pragma unroll
    for (int q = 3; q < 6; ++q) LDX(q);
    if (w == 3) flag4(WS_FWO + (unsigned)(c * 32 + R) * 128u);
    SCHED();
    auto xswap = [&](int q, int e) {
#pragma unroll
      for (int d = 0; d < 2; ++d) {
        if (e == 1) h2[q][2 + d] ^= (unsigned)__builtin_amdgcn_update_dpp(0, (int)h2[q][d], 0x128, 0xf, 0x3, false);
        else h2[q][d] ^= (unsigned)__builtin_amdgcn_update_dpp(0, (int)h2[q][2 + d], 0x128, 0xf, 0xC, false);
      }
      asm volatile("" : "+v"(h2[q]));
    };
#define XSW 1
#pragma unroll
    for (int q = 0; q < 10; ++q) {
      if (q < XSW) { xswap(q, 0); xswap(q, 1); xswap(q, 2); }
      if (q == 5 && w == 1 && __builtin_amdgcn_ballot_w64(f1v == tag) != 0xfffffull) wait_lines<1, 4>(rs, f1o, tag);
      if (q == 5) { __builtin_amdgcn_s_barrier(); asm volatile("" ::: "memory"); }
      SCHED();
      accg = MFMA16(h2[q], h2[q], accg);
      SCHED();
#pragma unroll
      for (int j = 0; j < 7; ++j) {
        acc[j] = MFMA16(h2[q], bf[j], acc[j]);
        if (q == 9 && j == 1) *(f32x4*)(lds + pgo) = accg;
        if (q < 9) bf[j] = BFRAG(q + 1, j);
        if (q < 9 && q + 1 >= XSW && j >= 3 && j < 6) xswap(q + 1, j - 3);
        if (q == 1 && j == 6) f1v = ld4(rs, f1o[0]);
        if (q == 2 && j == 0) bs_raw = __builtin_amdgcn_raw_buffer_load_b16(__builtin_amdgcn_make_buffer_rsrc((void*)p.b_sp, 0, S_ * 2, 0x00020000), bso, (unsigned)R * 128u, 0);
#define KCQ 4
#define WA 8
#define WB 12
#define WQ0 2
        if (WQ0 == 2 && q >= 2 && j == 2) LDW(q - 2);
        if (WQ0 == 6 && q >= 6 && (j == 2 || j == 4)) LDW(2 * (q - 6) + (j >> 2));
        if (WQ0 == 3 && q >= 2 && q < 5 && (j & 1) && 3 * (q - 2) + (j >> 1) < WA) LDW(3 * (q - 2) + (j >> 1));
        if (WQ0 == 4 && q >= 2 && !(q & 1) && j == 2) LDW((q - 2) >> 1);
        if ((j == 0 || j == 4) && 6 + 2 * q + (j >> 2) < 10) LDX(6 + 2 * q + (j >> 2));
        if (q == KCQ && j == 0) kc_ld(1);
        if (q == 2 && j == 1 && cs1) cs_ld(csv, c * 7 + R - 25);
        if (q >= 5 && j == 5) {
          const int i = (5 + q) * 7 + (w < 7 ? w : 0);
          dma1k(rs, lds + i * 1024, l16, WS_WINF + (unsigned)c * WBLK + (unsigned)i * 1024u);
          if (q == 9) { const int i2 = 28 + (w < 7 ? w : 0); dma1k(rs, lds + i2 * 1024, l16, WS_WINF + (unsigned)c * WBLK + (unsigned)i2 * 1024u); }
        }
        SCHED();
      }
    }
    STAMP(31);
  }
  unsigned sro, swo, zo, zro, po;
  auto gelu_n = [&](f32x2* x, auto N_) {
    constexpr int N = decltype(N_)::value;
    const float c0 = -2.0f * 0.7978845608028654f * 1.4426950408889634f, c1 = c0 * 0.044715f;
    f32x2 k0 = {c0, c0}, k1 = {c1, c1}, k2 = {1.0f, 1.0f};
    asm volatile("" : "+v"(k0), "+v"(k1), "+v"(k2));
    f32x2 t[N];
#define GST(e) do { _Pragma("unroll") for (int i = 0; i < N; ++i) { t[i] = (e); asm volatile("" : "+v"(t[i])); } } while (0)
    GST(x[i] * x[i]);
    GST(__builtin_elementwise_fma(t[i], k1, k0));
    GST(t[i] * x[i]);
    GST(((f32x2){__builtin_amdgcn_exp2f(t[i][0]), __builtin_amdgcn_exp2f(t[i][1])}));
    GST(t[i] + k2);
    GST(((f32x2){__builtin_amdgcn_rcpf(t[i][0]), __builtin_amdgcn_rcpf(t[i][1])}));
#pragma unroll
    for (int i = 0; i < N; ++i) { x[i] *= t[i]; asm volatile("" : "+v"(x[i])); }
  };
  {
    f32x2 mine[7];
    f32x2 csb[7];
    if (kh1) {
#pragma unroll
      for (int j = 0; j < 7; ++j) { *(f32x2*)(lds + rdo + j * 512) = (f32x2){acc[j][0], acc[j][1]}; mine[j] = (f32x2){acc[j][2], acc[j][3]}; }
      *(f32x4*)(lds + p6o) = acc[6];
    } else {
#pragma unroll
      for (int j = 0; j < 7; ++j) { *(f32x2*)(lds + rdo + j * 512) = (f32x2){acc[j][2], acc[j][3]}; mine[j] = (f32x2){acc[j][0], acc[j][1]}; }
      *(f32x4*)(lds + p6o) = acc[6];
      consts_put(1);
      if (cs1) cs_fin(csv, c * 7 + R - 25);
#pragma unroll
      for (int k = WA; k < WB; ++k) LDW(k);
    }
    BAR();
#define BY 0
    auto b1p = [&](int k) {
      const int i = 4 * k + (w & 3) + (k < 8 ? 35 : k > 8 ? 70 : (w & 3) < 3 ? 35 : 70) - (k == 17 ? (w & 2) : 0);
      dma1k(rs, lds + i * 1024, l16, WS_WINF + (unsigned)c * WBLK + (unsigned)i * 1024u);
    };
    if (kh1) {
#pragma unroll
      for (int k = 0; k < BY; ++k) b1p(k);
#pragma unroll
      for (int k = WA; k < WB; ++k) LDW(k);
    }
    __builtin_amdgcn_s_setprio(3);
    STAMPT(9);
    f32x2 oth[7];
    f32x2 pa[3], pb[3];
#pragma unroll
    for (int e = 0; e < 3; ++e) {
      pa[e] = (f32x2){*(const float*)(lds + pso[e]), *(const float*)(lds + pqo[e])};
      pb[e] = (f32x2){*(const float*)(lds + pso[e] + 256), *(const float*)(lds + pqo[e] + 1024)};
    }
#pragma unroll
    for (int j = 0; j < 7; ++j) { oth[j] = *(const f32x2*)(lds + oto + j * 512); csb[j] = *(const f32x2*)(lds + cso + 128 * j); }
    SCHED();
    float mu2[2], rs2[2];
#pragma unroll
    for (int e = 0; e < 3; ++e) {
      const float mean = (pa[e][0] + pb[e][0]) * (1.0f / D_);
      const float var = __builtin_fmaxf((pa[e][1] + pb[e][1]) * (1.0f / D_) - mean * mean, 0.f);
      const float rstd = __builtin_amdgcn_rsqf(var + 1e-5f);
      if (e < 2) { mu2[e] = mean; rs2[e] = rstd; } else { mu_n = mean; rs_n = rstd; }
    }
    f32x2 zz[7];
    const f32x2 nmu = {-mu2[0], -mu2[1]}, rsv = {rs2[0], rs2[1]};
#pragma unroll
    for (int j = 0; j < 7; ++j)
      zz[j] = __builtin_elementwise_fma(__builtin_elementwise_fma(nmu, (f32x2){csb[j][0], csb[j][0]}, mine[j] + oth[j]), rsv, (f32x2){csb[j][1], csb[j][1]});
    gelu_n(zz, IC<7>());
    if (n16 >= 12) zz[6] = (f32x2){0.f, 0.f};
    f32x2 s1 = ((zz[0] + zz[1]) + (zz[2] + zz[3])) + ((zz[4] + zz[5]) + zz[6]);
    f32x2 s2 = zz[0] * zz[0], s3 = zz[1] * zz[1];
#pragma unroll
    for (int j = 2; j < 7; ++j) { if (j & 1) s3 = __builtin_elementwise_fma(zz[j], zz[j], s3); else s2 = __builtin_elementwise_fma(zz[j], zz[j], s2); }
    s2 += s3;
    asm("" : "+v"(s1), "+v"(s2));
    float a0 = s1[0] + dpp_f<0xB1>(s1[0]), a1 = s1[1] + dpp_f<0xB1>(s1[1]), b0 = s2[0] + dpp_f<0xB1>(s2[0]);
    a0 = (l & 1) ? a1 : a0;
    const float b1 = s2[1] + dpp_f<0xB1>(s2[1]);
    b0 = (l & 1) ? b1 : b0;
    a0 += dpp_f<0x4E>(a0); b0 += dpp_f<0x4E>(b0);
    a0 += dpp_f<0x124>(a0); b0 += dpp_f<0x124>(b0);
    a0 += dpp_f<0x128>(a0); b0 += dpp_f<0x128>(b0);
    {
      const unsigned pa0 = __builtin_amdgcn_ds_bpermute(bsrc, __float_as_uint(a0)), pb0 = __builtin_amdgcn_ds_bpermute(bsrc, __float_as_uint(b0));
      ST16S(((u32x4){tag, pa0, tag, pb0}), rs, ppo, pos + (unsigned)c * 1024u, SC1);
    }
    SCHED();
    __builtin_amdgcn_s_setprio(0);
    STAMP(10);
#define BO 14
    if (kh1) {
#pragma unroll
      for (int k = BY + BO; k < 18; ++k) b1p(k);
    } else {
#pragma unroll
      for (int k = BY; k < BY + BO; ++k) b1p(k);
    }
#pragma unroll
    for (int k = WB; k < 16; ++k) LDW(k);
#pragma unroll
    for (int j = 0; j < 7; ++j)
      *(unsigned*)(lds + L_Z + (((rt1 >> 1) * 7 + j) * 64 + l) * 16 + (4 * (rt1 & 1) + 2 * kh1) * 2) = pack2(zz[j][0], zz[j][1]);
    SCHED();
#define TR0 2
#define TR1 TR0
#define TR2 TR1
#define TR3 5
#pragma unroll
    for (int g = 0; g < TR0; ++g) WSP_TR(g);
    sro = (unsigned)(L_ST2 + h4 * 16);
    swo = (unsigned)(L_ST2 + (16 * rt1 + 4 * ((l >> 1) & 3) + 2 * kh1 + (l & 1)) * 4);
    zo = w < 7 ? (unsigned)(h4 * 7168 + n16 * 16) : 0x80000000u;
    zro = (unsigned)(L_Z + (w < 7 ? w : 0) * 1024) + l16;
    po = (unsigned)((l >> 3) * 1024 + (l & 7) * 16);
    asm volatile("" : "+v"(sro), "+v"(swo), "+v"(zo), "+v"(zro), "+v"(po));
    asm volatile("s_waitcnt vmcnt(%0)" :: "n"(16 - WB) : "memory");
    BAR();
    STAMPT(11);
  }
  unsigned z1d[7];
  constexpr int L_RED = 28672;
#define WOA(i) (((i) < 28 ? (i) : (i) < 108 ? (i) + 32 : (i) < 120 ? (i) + 37 : (i) - 92) * 1024)
#define RBAR() do { __builtin_amdgcn_s_barrier(); asm volatile("" ::: "memory"); } while (0)
#define ZAUX 16
#define NW 14
#define WOD(i0, x) dma1k(rs, lds + WOA(i0) + (x), l16, wgb + (unsigned)(i0) * 1024u + (x))
  auto wo_dma = [&](int k) {};
  u32x4 zw[NW];
#define LDZ(f) do { if (((f) >> 3) == 4) { zs[(f) & 7] += 4096u; asm volatile("" : "+s"(zs[(f) & 7])); } \
    zw[(f) % NW] = __builtin_amdgcn_raw_buffer_load_b128(rs, l16 + (unsigned)(((f) >> 3) & 3) * 1024u, zs[(f) & 7], ZAUX); } while (0)
#define WD 24
#define WE 32
#define QS 5
#define PQ 3
#define NW0 NW
#define PP 1
#define WZ 4
#define WY 11
  static_assert(QS >= 4 && QS <= 6 && PQ >= 2 && PQ < QS && (WZ < 4 || QS >= 5), "");
  {
    f32x4 acc[7];
#pragma unroll
    for (int j = 0; j < 7; ++j) acc[j] = (f32x4){0.f, 0.f, 0.f, 0.f};
    u32x4 bf[7];
#pragma unroll
    for (int j = 0; j < 7; ++j) bf[j] = BFRAG(4, j);
    u32x4 pv, zf[2];
    unsigned fwv;
    auto ld_pv = [&]() { pv = __builtin_amdgcn_raw_buffer_load_b128(rs, po, pos, SC1); };
    auto z1 = [&](auto Q0_, auto Q1_) __attribute__((always_inline)) {
#pragma unroll
      for (int qi = decltype(Q0_)::value; qi < decltype(Q1_)::value; ++qi) {
        const int q = (qi + 4) % 10, qn = (qi + 5) % 10;
        SCHED();
#pragma unroll
        for (int j = 0; j < 7; ++j) {
          acc[j] = MFMA16(bf[j], h2[q], acc[j]);
          if (qi < 9 && qi != QS - 1) bf[j] = BFRAG(qn, j);
          if (qi < 2 && (j == 1 || j == 4)) { const int i = (2 * qi + (j >> 2)) * 7 + (w < 7 ? w : 0); dma1k(rs, lds + i * 1024, l16, WS_WINF + (unsigned)c * WBLK + (unsigned)i * 1024u); }
          if (qi == 2 && j == 0) kc_ld(0);
          if (qi == 2 && j == 3) fwv = ld4(rs, fwo[0]);
          if (qi == PQ && j == 1) ld_pv();
          if (qi == QS - 1 && j < 2) zf[j] = *(const u32x4*)(lds + zro + j * 7168);
          if (!PP && qi >= QS && (j & 1) && WD + 3 * (qi - QS) + (j >> 1) < WE) LDW((WD + 3 * (qi - QS) + (j >> 1)) & 31);
          SCHED();
        }
      }
    };
    auto req1 = [&]() {
#pragma unroll
      for (int k = 16; k < WD; ++k) { LDW(k); SCHED(); }
    };
    auto req2 = [&]() {
#pragma unroll
      for (int k = WD; k < (PP ? WE : WD); ++k) { LDW(k); SCHED(); }
      sfor<0, WZ>([&](auto G_) __attribute__((always_inline)) { WOD(68 + 8 * decltype(G_)::value, wx8); SCHED(); });
    };
    if (kh1) req1();
    z1(IC<0>(), IC<QS>());
    STAMPT(29);
    f32x2 zv[2][4];
#pragma unroll
    for (int i = 0; i < 8; ++i) { zv[i >> 2][i & 3] = (f32x2){bflo(zf[i >> 2][i & 3]), bfhi(zf[i >> 2][i & 3])}; asm volatile("" : "+v"(zv[i >> 2][i & 3])); }
#pragma unroll
    for (int g = TR0; g < TR1; ++g) WSP_TR(g);
    __builtin_amdgcn_s_setprio(3);
    if (__any(ne2(pv[0], pv[2]) != 0u)) {
      _Pragma("unroll 1") for (unsigned spins = 0; spins < SPIN_CAP; ++spins) {
        ld_pv();
        if (!__any(ne2(pv[0], pv[2]) != 0u)) break;
        __builtin_amdgcn_s_sleep(1);
      }
    }
    {
      float t1 = __uint_as_float(pv[1]), t2 = __uint_as_float(pv[3]);
      t1 += dpp_f<0x128>(t1); t2 += dpp_f<0x128>(t2);
      auto x = __builtin_amdgcn_permlane16_swap(__float_as_uint(t1), __float_as_uint(t2), false, false);
      const unsigned y = __float_as_uint(__uint_as_float(x[0]) + __uint_as_float(x[1]));
      auto z = __builtin_amdgcn_permlane32_swap(y, y, false, false);
      const unsigned u = __float_as_uint(__uint_as_float(z[0]) + __uint_as_float(z[1]));
      auto v = __builtin_amdgcn_permlane16_swap(u, u, false, false);
      const float mean = __uint_as_float(v[0]) * (1.0f / FH);
      const float var = __builtin_fmaxf(__uint_as_float(v[1]) * (1.0f / FH) - mean * mean, 0.f);
      const float rstd = __builtin_amdgcn_rsqf(var + 1e-5f);
      *(float*)(lds + swo) = rstd;
      *(float*)(lds + swo + 256) = -mean * rstd;
    }
    SCHED();
    consts_put(0);
    if (w == 3 && __builtin_amdgcn_ballot_w64(fwv == tag) != 0xfffffffull) wait_lines<1, 1>(rs, fwo, tag);
    if (!kh1) req1();
    BAR();
    STAMP(12);
    {
      f32x4 sa[2][2], sb[2][2];
#pragma unroll
      for (int i = 0; i < 4; ++i) { sa[i >> 1][i & 1] = *(const f32x4*)(lds + sro + i * 64); sb[i >> 1][i & 1] = *(const f32x4*)(lds + sro + i * 64 + 256); }
      SCHED();
#pragma unroll
      for (int m = 0; m < 2; ++m) {
        u32x4 o;
#pragma unroll
        for (int e = 0; e < 4; ++e) {
          const f32x4 a = sa[m][e >> 1], b = sb[m][e >> 1];
          const f32x2 x = __builtin_elementwise_fma(zv[m][e], (f32x2){a[2 * (e & 1)], a[2 * (e & 1) + 1]}, (f32x2){b[2 * (e & 1)], b[2 * (e & 1) + 1]});
          o[e] = pack2(x[0], x[1]);
        }
        ST16S(o, rs, zo + (unsigned)m * 256u, zos, SC1);
      }
    }
    SCHED();
    __builtin_amdgcn_s_setprio(0);
    STAMPT(13);
#pragma unroll
    for (int j = 0; j < 7; ++j) bf[j] = BFRAG((QS + 4) % 10, j);
    if (kh1) req2();
    z1(IC<QS>(), IC<10>());
    f32x2 mine[7];
    if (kh1) {
#pragma unroll
      for (int j = 0; j < 7; ++j) { *(f32x2*)(lds + rdo + L_RED + j * 512) = (f32x2){acc[j][0], acc[j][1]}; mine[j] = (f32x2){acc[j][2], acc[j][3]}; }
    } else {
#pragma unroll
      for (int j = 0; j < 7; ++j) { *(f32x2*)(lds + rdo + L_RED + j * 512) = (f32x2){acc[j][2], acc[j][3]}; mine[j] = (f32x2){acc[j][0], acc[j][1]}; }
    }
    if (!kh1) req2();
#pragma unroll
    for (int g = TR1; g < TR2; ++g) WSP_TR(g);
    asm volatile("s_waitcnt vmcnt(%0)" :: "n"(WE - WD + WZ) : "memory");
    BAR();
    if (w == 3) flag4(WS_FZ + (unsigned)(c * 32 + R) * 128u);
    STAMP(6);
    auto req = [&](auto G0_, auto G1_) __attribute__((always_inline)) {
#pragma unroll
      for (int k = WE; k < 32; ++k) LDW(k);
      SCHED();
      sfor<decltype(G0_)::value, decltype(G1_)::value>([&](auto G_) __attribute__((always_inline)) {
        constexpr int i0 = 4 * decltype(G_)::value;
        WOD(i0 < 68 ? i0 : i0 + 8 * WZ, wx4); SCHED();
      });
    };
    if (kh1) req(IC<0>(), IC<WY>());
    u32x4 gv[8];
    auto gb_ld = [&]() {
#pragma unroll
      for (int k = 0; k < 8; ++k) gv[k] = __builtin_amdgcn_raw_buffer_load_b128(rs, l < 40 ? l16 : 0u, WS_MODQ + (unsigned)(k * 2560 + 1920 + 80 * c) * 8u, SC1);
    };
    if (w == 2) gb_ld();
    f32x2 z[7];
    {
      f32x2 oth[7];
      f32x4 cc[7];
#pragma unroll
      for (int j = 0; j < 7; ++j) {
        oth[j] = *(const f32x2*)(lds + oto + L_RED + j * 512);
        cc[j] = *(const f32x4*)(lds + L_CONST + (16 * j + 4 * h4 + 2 * kh1) * 8);
      }
      SCHED();
#pragma unroll
      for (int j = 0; j < 7; ++j)
        z[j] = __builtin_elementwise_fma(__builtin_elementwise_fma((f32x2){-mu_n, -mu_n}, (f32x2){cc[j][0], cc[j][1]}, oth[j] + mine[j]),
                                         (f32x2){rs_n, rs_n}, (f32x2){cc[j][2], cc[j][3]});
    }
    gelu_n(z, IC<7>());
#pragma unroll
    for (int j = 0; j < 7; ++j) z1d[j] = pack2(z[j][0], z[j][1]);
    asm volatile("" : "+v"(z1d[0]), "+v"(z1d[1]), "+v"(z1d[2]), "+v"(z1d[3]), "+v"(z1d[4]), "+v"(z1d[5]), "+v"(z1d[6]));
    SCHED();
    STAMPT(30);
    if (w == 2) {
      auto bad = [&]() {
        unsigned d = 0u;
#pragma unroll
        for (int k = 0; k < 8; ++k) d |= ne2(gv[k][0], gv[k][2]);
        return __any(d != 0u);
      };
      if (bad()) {
        _Pragma("unroll 1") for (unsigned spins = 0; spins < SPIN_CAP; ++spins) {
          __builtin_amdgcn_s_sleep(8);
          gb_ld();
          if (!bad()) break;
        }
      }
      f32x2 t[4];
#pragma unroll
      for (int k = 0; k < 4; ++k) t[k] = (f32x2){__uint_as_float(gv[2 * k][1]) + __uint_as_float(gv[2 * k + 1][1]), __uint_as_float(gv[2 * k][3]) + __uint_as_float(gv[2 * k + 1][3])};
      *(f32x2*)(lds + L_MISC + l * 8) = rbf2((t[0] + t[1]) + (t[2] + t[3]));
    }
    if (!kh1) req(IC<WY>(), IC<30 - 2 * WZ>());
    const unsigned fo[1] = {l < 4 ? fzs + (unsigned)l * 128u : ~0u};
    const unsigned fv = ld4(rs, fo[0]);
    SCHED();
#pragma unroll
    for (int g = TR2; g < TR3; ++g) WSP_TR(g);
    SCHED();
    STAMP(14);
    if (__builtin_amdgcn_ballot_w64(fv == tag) != 15ull) wait_lines<1, 1>(rs, fo, tag);
    STAMPT(23);
#pragma unroll
    for (int f = 0; f < NW; ++f) { LDZ(f); SCHED(); }
  }

#define WO2 15
#define TRC TR3
#define VMW(n) asm volatile("s_waitcnt vmcnt(%0)" ::"n"(n) : "memory")
  {
    SCHED();
#pragma unroll
    for (int g = TRC; g < 8; ++g) WSP_TR(g);
    SCHED();
    STAMP(20);
    const int rt = rt1, kh = kh1;
    const unsigned rw = (unsigned)(L_RED + w * 512);
    unsigned rr = (unsigned)(L_RED + w * 4096 + l * 4);
    asm volatile("; m0 is set here, not in front of the first store (one wait state)" : "+v"(rr) : "{m0}"(rw));
    const float bs = bflo(bs_raw);
    unsigned long long hm = 0xffff000000000000ull;
    asm volatile("" : "+s"(hm));
    unsigned l8;
    f32x4 acc2[2][4];
    float rv[8][2];
    u32x4 ud;
    u32x4 P[2][8];
    u32x2 xp[3];
    auto ld_p = [&](int pt, int i) { P[pt][i] = __builtin_amdgcn_raw_buffer_load_b128(rs, l16 + (unsigned)pt * 1024u, ui + (unsigned)i * 2048u, SC1); };
#define RED_PUT1(j, i) asm volatile("ds_write_addtid_b32 %0 offset:%2" :: "v"(acc2[(j) & 1][((i) >> 2) & 3][(i) & 3]), "{m0}"(rw), "n"((((i) >> 2) & 3) * 4096 + (((i) >> 1) & 1) * 16384 + ((i) & 1) * 256) : "memory")
#define RED_PUT(j, r) do { RED_PUT1(j, 4 * (r)); RED_PUT1(j, 4 * (r) + 1); RED_PUT1(j, 4 * (r) + 2); RED_PUT1(j, 4 * (r) + 3); } while (0)
#define RED_GET(i) do { f32x2 x_; asm volatile("ds_read2st64_b32 %0, %1 offset0:%2 offset1:%3" : "=v"(x_) : "v"(rr), "n"(2 * (i)), "n"(2 * (i) + 1) : "memory"); rv[i][0] = x_[0]; rv[i][1] = x_[1]; } while (0)
    float zz[2], gg[2];
    unsigned cs;
    auto sum_rows_u = [&](unsigned v) {
      auto a = __builtin_amdgcn_permlane16_swap(v, v, false, false);
      v = a[0] + a[1];
      auto b = __builtin_amdgcn_permlane32_swap(v, v, false, false);
      return b[0] + b[1];
    };
    auto red_fin = [&](int j, int k, bool fuse = false) {
      const int e = k & 1, i = k >> 1;
#define VADD(d, x, y) asm volatile("v_add_f32 %0, %1, %2" : "=v"(d) : "v"(x), "v"(y))
      if (i < 4) VADD(rv[2 * i][e], rv[2 * i][e], rv[2 * i + 1][e]);
      if (i == 4 || i == 5) VADD(rv[4 * (i & 1)][e], rv[4 * (i & 1)][e], rv[4 * (i & 1) + 2][e]);
      if (i == 6) VADD(rv[0][e], rv[0][e], bs);
      if (i == 7) VADD(gg[e], rv[0][e], rv[4][e]);
      if (i == (fuse ? 9 : 8)) zz[e] = e ? bfhi(z1d[(j + 6) % 7]) : bflo(z1d[(j + 6) % 7]);
      if (i == 9) zz[e] *= gg[e];
      if (i == 9 || (i == 8 && !fuse)) asm volatile("" : "+v"(zz[e]));
      if (k == 20) { ud[j & 3] = pack2(zz[0], zz[1]); asm volatile("" : "+v"(ud[j & 3])); }
      if (k == 21) {
        if (j == 0) asm volatile("v_cndmask_b32 %0, %0, 0, %1" : "+v"(ud[0]) : "s"(hm));
        if (j == 2) asm volatile("v_add3_u32 %0, %1, %2, %3" : "=v"(cs) : "v"(ud[0]), "v"(ud[1]), "v"(ud[2]));
        if (j == 3) {
          cs = tag - sum_rows_u(cs + ud[3]);
          asm volatile("v_cndmask_b32 %0, %0, %1, %2" : "+v"(ud[0]) : "v"(cs), "s"(hm));
        }
        if (j >= 4) { ud[3] = (j == 4 ? tag : ud[3]) - ud[j & 3]; asm volatile("" : "+v"(ud[3])); }
        if (j % 3 == 0 && j) ST16S(ud, rs, l16 + (unsigned)(j / 6) * 1024u, uo, SC1);
      }
    };
    struct Sch { int b1, b2, wp[16], rp[8], vp[22], lp[8], pr[32]; };
    constexpr Sch S = {18, 28, {0, 1, 2, 3, 4, 5, 6, 7, 8, 9, 10, 11, 12, 12, 15, 15}, {19, 19, 20, 22, 23, 23, 24, 24}, {29, 29, 30, 30, 31, 31, 32, 33, 35, 36, 37, 38, 39, 40, 41, 42, 43, 46, 43, 46, 48, 48},
                       {1, 1, 1, 1, 1, 1, 1, 1}, {0, 0, 2, 1, 0, 0, 0, 0, 0, 0, 2, 2, 0, 0, 0, 0, 0, 0, 0, 1, 0, 0, 0, 1, 0, 0, 2, 1, 0, 0, 0, 0}};
    constexpr Sch F = {15, 23, {0, 1, 1, 2, 3, 4, 4, 5, 6, 7, 8, 9, 10, 11, 12, 12}, {15, 16, 16, 17, 17, 18, 18, 19}, {23, 23, 24, 24, 25, 26, 26, 27, 27, 28, 28, 29, 30, 30, 31, 31, 22, 22, 32, 33, 34, 35},
                       {1, 2, 1, 1, 1, 1, 1, 1}, {0, 2, 1, 0, 0, 1, 1, 0, 0, 2, 1, 0, 0, 2, 1, 0, 0, 2, 1, 0, 0, 2, 1, 0, 0, 2, 1, 0, 0, 2, 1, 0}};
#define VM 3
#define JF 4
#define RO 3
#define XE 1
#define CE 0
#define AP0 2
#define P1S 0
    unsigned wb[3], xo;
    const unsigned wl[3] = {(unsigned)(120 + WO3 + w) * 1024u, (unsigned)(128 + WO3 + w) * 1024u, wl2}, wg = wgb - (unsigned)(WO3 * 1024);
#define NAF 32
#define NA0 16
#define NA1 NA0
    u32x4 af[NAF];
#define LDA(m) af[(m) % NAF] = *(const u32x4*)(lds + wb[WOA(2 * (m)) >> 16] + (WOA(2 * (m)) & 65535))
    unsigned ck[2];
    const unsigned tag4 = tag * 4u;
    auto p_chk = [&](int pt, int pr, int k) {
      u32x4* q = P[pt] + 4 * pr;
      if (k < 2) ck[k & 1] = q[k & 1][0] + q[k & 1][1];
      else if (k < 8) ck[k & 1] = ck[k & 1] + q[(k & 1) + 2 * ((k >> 2) & 1)][k & 2] + q[(k & 1) + 2 * ((k >> 2) & 1)][(k & 2) + 1];
      else if (k == 8) ck[0] = pt ? ck[0] + ck[1] : sum_rows_u(ck[0] + ck[1]);
      if (k < 9) asm volatile("" : "+v"(ck[k & 1]));
      if (k == 9) {
        if (__builtin_expect(__any(ck[0] != tag4), 0)) {
          _Pragma("unroll 1") for (unsigned spins = 0; spins < SPIN_CAP; ++spins) {
            unsigned d = 0u;
#pragma unroll
            for (int i = 0; i < 4; ++i) ld_p(pt, 4 * pr + i);
#pragma unroll
            for (int i = 0; i < 4; ++i) d += q[i][0] + q[i][1] + q[i][2] + q[i][3];
            if (!pt) d = sum_rows_u(d);
            if (!__any(d != tag4)) break;
            __builtin_amdgcn_s_sleep(1);
          }
        }
      }
      if (k >= 10 && !pt) { unsigned x = q[(k - 10) & 3][0]; asm volatile("v_cndmask_b32 %0, %0, 0, %1" : "+v"(x) : "s"(hm)); q[(k - 10) & 3][0] = x; }
      if (k >= 10 && k < 13 && pt) { q[(k - 10) % 3][3] = q[3][(k - 10) % 3]; asm volatile("" : "+v"(q[(k - 10) % 3])); }
    };
    sfor<0, 224>([&](auto M_) __attribute__((always_inline)) {
      constexpr int mm = decltype(M_)::value, j = mm >> 5, m = mm & 31, y = m - (32 - 4 * RO);
      constexpr int t = j == 6 && y >= 0 ? 8 - RO + (y % (2 * RO + !RO)) / 2 : m >> 2, r = j == 6 && y >= 0 ? (y & 1) + 2 * (y / (2 * RO + !RO)) : m & 3, f = 8 * j + t;
      constexpr Sch C = j < JF ? S : F, B = j <= JF ? S : F;
      if (t == 0) asm volatile("v_mfma_f32_16x16x32_bf16 %0, %1, %2, 0" : "=&v"(acc2[j & 1][r]) : "v"(zw[f % NW]), "v"(wsp[r][t]), "v"(zw[(f | (f < 8 ? 1 : VM)) % NW]));
      else asm volatile("v_mfma_f32_16x16x32_bf16 %0, %1, %2, %0" : "+v"(acc2[j & 1][r]) : "v"(zw[f % NW]), "v"(wsp[r][t]), "v"(zw[(f | (f < 8 ? 1 : VM)) % NW]));
      SCHED();
      if (C.pr[m] == 1) __builtin_amdgcn_s_setprio(1);
      if ((m == C.b1 && j > 0) || (m == C.b2 && j > 0) || mm == 30) { asm volatile("" ::: "memory"); __builtin_amdgcn_s_waitcnt(0xC07F); RBAR(); SCHED(); }
      sfor<0, 16>([&](auto I_) __attribute__((always_inline)) {
        constexpr int i = decltype(I_)::value;
        if (j > 0 && C.wp[i] == m) { RED_PUT1(j + 1, i); SCHED(); }
        if (j > 0 && i < 8 && C.rp[i & 7] == m) { RED_GET(i & 7); SCHED(); }
      });
      if (j == 5 && m == 20) {
#pragma unroll
        for (int i = 0; i < 3; ++i) { wb[i] = (unsigned)(kh * 1024 + i * 65536) + l16; asm volatile("" : "+v"(wb[i])); }
      }
      if (XE && j == 5 && m == 13) {
        asm volatile("v_lshrrev_b32 %0, 1, %1" : "=v"(l8) : "v"(l16));
        xo = (l8 & 0x180u) + (unsigned)psi((l8 >> 3) & 15) * 8u;
        asm volatile("" : "+v"(xo));
      }
      if (RO && j == 6 && m >= 28) { RED_PUT1(6, 2 * (m - 28)); RED_PUT1(6, 2 * (m - 28) + 1); SCHED(); }
      if (CE && j == 6 && m >= 19 && m < 22) sfor<0, 3>([&](auto K_) __attribute__((always_inline)) { p_chk(0, 0, 3 * (m - 19) + decltype(K_)::value); SCHED(); });
      constexpr int ap[20] = {7, 8, 9, 10, 11, 13, 13, 14, 14, 19, 20, 21, 22, 23, 24, 25, 26, 27, 28, 29};
      sfor<0, NA0>([&](auto N_) __attribute__((always_inline)) { if (j == 6 && ap[AP0 + decltype(N_)::value] == m) { LDA(decltype(N_)::value); SCHED(); } });
      sfor<0, 22>([&](auto K_) __attribute__((always_inline)) {
        constexpr int k = decltype(K_)::value;
        if (j > 1 && B.vp[k] == m + 32) { red_fin(j > 1 ? j - 2 : 0, k, B.vp[16] == B.vp[18]); SCHED(); }
        if (j > 0 && C.vp[k] == m) { red_fin(j > 0 ? j - 1 : 0, k); SCHED(); }
      });
      if (C.pr[m] == 2) __builtin_amdgcn_s_setprio(0);
      if ((m & 3) == C.lp[m >> 2]) {
        if (f > 0 && f - 1 + NW < 56) LDZ(f - 1 + NW);
        if (j == 6 && (m >> 2) >= P1S) ld_p(0, ((m >> 2) - P1S) & 7);
        if (XE && j == 5 && m >= 20) xp[((m >> 2) - 5) % 3] = __builtin_amdgcn_raw_buffer_load_b64(rs, xo + (unsigned)((m >> 2) - 5) * 512u, xs, BULK_AUX);
        SCHED();
      }
    });
    STAMP(15);
#define NM0 0
#define GM 19
#define MB 24
    f32x4 acc[5];
    f32x4 xf[3], gf[3];
    unsigned go, oo, eo, ho, tw, tr, o1, ta, tb;
    const unsigned s2 = (unsigned)(L_MISC + 128 * kh), s4 = s1 - (unsigned)kh * 1024u;
    unsigned long long om = kh ? ~0ull : 0xccccccccccccccccull;
#define PIN2(a, b) asm volatile("" : "+v"(a), "+v"(b))
    auto glue = [&](int k) {
      if (k == 0) { ta = l8 >> 5; tb = l16 & 48u; PIN2(ta, tb); }
      if (k == 1) { tr = __umul24(ta, 112u) + tb; oo = ta & 12u; PIN2(tr, oo); }
      if (k == 2) { tr += s1; oo += ta; PIN2(tr, oo); }
      if (k == 3) { oo = __umul24(oo, 1280u) + tb; ta = l8 >> 4; PIN2(oo, ta); }
      if (k == 4) { ta &= 24u; tb = l16 & 0xf0u; PIN2(ta, tb); }
      if (k == 5) { tw = __umul24(tb, 7u) + ta; go = (ta << 1) + s2; PIN2(tw, go); }
      if (k == 6) { tw += s1; asm volatile("v_cndmask_b32 %0, %1, -2.0, %2" : "=v"(o1) : "v"(oo), "s"(om)); asm volatile("" : "+v"(tw)); }
      if (k == 7) { eo = l16 + s3; ho = l16 + s4; PIN2(eo, ho); }
      if (k >= 8 && k < 14) {
        const unsigned x = xp[((k - 8) >> 1) % 3][k & 1];
        float lo = bflo(x), hi = bfhi(x);
        PIN2(lo, hi);
        xf[((k - 8) >> 1) % 3][2 * (k & 1)] = lo; xf[((k - 8) >> 1) % 3][2 * (k & 1) + 1] = hi;
      }
    };
    auto g3 = [&](auto M0_, auto M1_) __attribute__((always_inline)) {
      sfor<decltype(M0_)::value, decltype(M1_)::value>([&](auto M_) __attribute__((always_inline)) {
        constexpr int mi = decltype(M_)::value;
        constexpr int m = mi < 55 ? mi : mi < 64 ? 55 + 5 * ((mi - 55) / 3) + (mi - 55) % 3 : 58 + 5 * ((mi - 64) / 2) + (mi & 1), u2 = m / 5;
        const u32x4 fb = u2 < 8 ? P[0][u2 & 7] : P[1][u2 < 11 ? (u2 - 8) & 3 : (u2 - 7) & 7];
        if (m < 5) asm volatile("v_mfma_f32_16x16x32_bf16 %0, %1, %2, 0" : "=&v"(acc[m % 5]) : "v"(af[m % NAF]), "v"(fb));
        else asm volatile("v_mfma_f32_16x16x32_bf16 %0, %1, %2, %0" : "+v"(acc[m % 5]) : "v"(af[m % NAF]), "v"(fb));
        SCHED();
        if (mi == NM0) RBAR();
        if (mi >= NM0 && mi < NM0 + 3) dma1k(rs, lds + wl[(mi - NM0) % 3], l16, wg + wl[(mi - NM0) % 3]);
        if (mi >= 5 && mi < 19) p_chk(0, 1, mi - 5);
        if (mi >= GM && mi < GM + 14) glue(mi - GM);
        if (mi >= 47 && mi < 54) { p_chk(1, 1, 2 * (mi - 47)); SCHED(); p_chk(1, 1, 2 * (mi - 47) + 1); }
        if (!XE && mi == 3) { xo = (l8 & 0x180u) + (unsigned)psi((l8 >> 3) & 15) * 8u; asm volatile("" : "+v"(xo)); }
        if (!XE && mi >= 4 && mi < 7) xp[(mi - 4) % 3] = __builtin_amdgcn_raw_buffer_load_b64(rs, xo + (unsigned)(mi - 4) * 512u, xs, BULK_AUX);
        if (mi >= NM0 && mi < 40 && NA1 + mi - NM0 < 60) LDA(NA1 + mi - NM0);
        if (mi >= 66 && mi < 69) *(f32x4*)(lds + eo + (mi - 66) * 1024) = acc[(mi - 66) % 3];
        SCHED();
      });
    };
    __builtin_amdgcn_s_setprio(3);
#pragma unroll
    for (int k = 18; k < 22; ++k) red_fin(5, k);
    if (!XE) asm volatile("v_lshrrev_b32 %0, 1, %1" : "=v"(l8) : "v"(l16));
    if (RO) asm volatile("s_nop 2"); else { RED_PUT(6, 0); RED_PUT(6, 1); }
    RED_PUT(6, 2); RED_PUT(6, 3);
    red_fin(6, 16); red_fin(6, 17);
    SCHED();
    BAR();
    sfor<0, 8>([&](auto I_) __attribute__((always_inline)) { RED_GET(decltype(I_)::value); });
    SCHED();
#pragma unroll
    for (int n = NA0; n < NA1; ++n) LDA(n);
    SCHED();
    sfor<0, 20>([&](auto K_) __attribute__((always_inline)) {
      constexpr int k = decltype(K_)::value;
      if (k < 8 && !(k & 1)) asm volatile("s_waitcnt lgkmcnt(%0)" :: "n"(6 - k + NA1 - NA0) : "memory");
      red_fin(6, k < 16 ? k : k + 2);
    });
    SCHED();
    __builtin_amdgcn_s_setprio(0);
    STAMPT(16);
#pragma unroll
    for (int i = 8 - P1S; i < 8; ++i) ld_p(0, i);
#pragma unroll
    for (int k = CE ? 9 : 0; k < 14; ++k) p_chk(0, 0, k);
    SCHED();
    g3(IC<0>(), IC<MB>());
    STAMPT(22);
    DRAIN();
    RBAR();
    STAMPT(27);
#define MY 12
    if (MY == 0 || !kh) {
#pragma unroll
      for (int i = 0; i < 8; ++i) { ld_p(1, i); SCHED(); }
    }
    g3(IC<MB>(), IC<MB + MY>());
    if (MY > 0 && kh) {
#pragma unroll
      for (int i = 0; i < 8; ++i) { ld_p(1, i); SCHED(); }
    }
    STAMPT(26);
    g3(IC<MB + MY>(), IC<40>());
    STAMPT(24);
#pragma unroll
    for (int m = NA1 + 40 - NM0; m < 70; ++m) LDA(m);
#pragma unroll
    for (int x = 0; x < 3; ++x) gf[x] = *(const f32x4*)(lds + go + x * 64);
    SCHED();
    const rsrc_t ro = __builtin_amdgcn_make_buffer_rsrc((void*)p.out, 0, S_ * D_ * 2, 0x00020000);
    STAMPT(17);
#pragma unroll
    for (int k = 0; k < 14; ++k) p_chk(1, 0, k);
    asm volatile("s_nop 1");
    STAMPT(25);
    g3(IC<40>(), IC<70>());
    STAMPT(18);
    BAR();
    STAMPT(28);
    f32x4 o4[3];
    u32x4 ov[2];
#pragma unroll
    for (int j = 2; j >= 0; --j) o4[j] = *(const volatile LAS f32x4*)(lds + ho + j * 1024);
    asm volatile("" : "+v"(acc[2]), "+v"(acc[3]), "+v"(acc[4]));
    SCHED();
#pragma unroll
    for (int j = 2; j >= 0; --j) {
      const f32x4 v = __builtin_elementwise_fma(gf[j], o4[j] + acc[2 + j], xf[j]);
      *(volatile LAS u32x2*)(lds + tw + j * 32) = (u32x2){pack2(v[0], v[1]), pack2(v[2], v[3])};
      if (j < 2) ov[j] = *(const volatile LAS u32x4*)(lds + tr + j * 32);
    }
    ST16S(ov[1], ro, oo + 32u, os, 0);
    ST16S(ov[0], ro, o1, os, 0);
    if (bid == 0 && w == 5 && l8 == 0u) st4(rs, WS_EPOCH, tag);
  }
  STAMP(19);
}

extern "C" __device__ __attribute__((used, noinline, visibility("hidden"))) void mega_tail() {}

extern "C" void kernel_launch(void* const* d_in, const int* in_sizes, int n_in, void* d_out, int out_size,
                              void* d_ws, size_t ws_size, hipStream_t stream) {
  Params p;
  p.q = (const u16*)d_in[0];
  p.k = (const u16*)d_in[1];
  p.v = (const u16*)d_in[2];
  p.shift_k = (const u16*)d_in[3];
  p.ssm_k = (const u16*)d_in[4];
  p.ssm_D = (const u16*)d_in[5];
  p.w_in = (const u16*)d_in[7];
  p.w_sp = (const u16*)d_in[8];
  p.b_sp = (const u16*)d_in[9];
  p.w_out = (const u16*)d_in[10];
  p.cvec = (const u16*)d_in[11];
  p.w_mod = (const u16*)d_in[12];
  p.out = (u16*)d_out;
  p.ws = (char*)d_ws;
  mega<<<256, 512, 0, stream>>>(p);
}
```

```cpp
#include <hip/hip_runtime.h>
#include <stdint.h>

typedef unsigned short u16;
typedef __attribute__((ext_vector_type(8))) short bf16x8;
typedef __attribute__((ext_vector_type(4))) short bf16x4;
typedef __attribute__((ext_vector_type(4))) float f32x4;
typedef __attribute__((ext_vector_type(2))) float f32x2;
typedef __attribute__((ext_vector_type(4))) unsigned int u32x4;
typedef __attribute__((ext_vector_type(2))) unsigned int u32x2;
#define LAS __attribute__((address_space(3)))
typedef __amdgpu_buffer_rsrc_t rsrc_t;
typedef __attribute__((ext_vector_type(4))) short s16x4;

constexpr int S_ = 2048, D_ = 640, F_ = 1728, FH = 864, CB = 108;

constexpr unsigned WS_EPOCH = 0;
constexpr unsigned WS_FWI = 4096;
constexpr unsigned WS_FZ = WS_FWI + 384 * 128;
constexpr unsigned WS_FWO = WS_FZ + 256 * 128;
constexpr unsigned F4S = 896 * 128;
constexpr unsigned WS_CS = WS_FZ + 256 * 128;
constexpr unsigned WS_MOD = 131072;
constexpr unsigned WS_MODP = WS_MOD + 81920;
constexpr unsigned WS_ST1 = 393216;
constexpr unsigned WS_PART = 524288;
constexpr unsigned WS_CSP = WS_PART + 262144;
constexpr unsigned WS_FX = WS_CSP + 655360;
constexpr unsigned WS_X1B = WS_FX + 16 * 4 * 160 * 128;
constexpr unsigned WS_QB = WS_X1B + 2621440;
constexpr unsigned WS_KV3 = WS_QB + 2621440;
constexpr unsigned WS_MODQ = WS_KV3 + 1310720;
constexpr unsigned WBLK = 143360;
constexpr unsigned WS_WINF = WS_MODQ + 245760;
constexpr unsigned WS_WOUTF = WS_WINF + 16 * WBLK;
constexpr unsigned WS_Z2F = WS_WOUTF + 8 * WBLK;
constexpr unsigned WS_UF = WS_Z2F + 8 * 64 * 7168;
constexpr unsigned WS_F4 = WS_UF + 32 * 4 * 8 * 4096;
constexpr unsigned WS_END = WS_F4 + 4 * F4S;
constexpr unsigned WS_STAMP = 200u << 20;

constexpr int G_COPY = 4160;
constexpr int L_U = WBLK;
constexpr int U_CH = (256 + S_) * 2 + 32;
constexpr int L_FLG = L_U + 4 * U_CH;
constexpr int L_RT = 16384;
constexpr int L_CONST = WBLK;
constexpr int L_PST = L_CONST + 2048;
constexpr int L_Z = L_CONST + 5120;
constexpr int L_ST2 = L_Z + 14336;
constexpr int L_MISC = L_ST2 + 512;
constexpr int LDS_TOTAL = 163840;

#define AUX_T 610
#define RT2_T 360
#define CB_T 380
#define W_T 700
#define YG_T 890
#define YPR 0
#define YSL 2
#define K_T 460
#ifndef M_T
#define M_T 520
#endif
#define WA_T 580
constexpr int SC1 = 16, NT = 2;
constexpr unsigned SPIN_CAP = 1u << 16;

__device__ __forceinline__ float bflo(unsigned x) { return __uint_as_float(x << 16); }
__device__ __forceinline__ float bfhi(unsigned x) { return __uint_as_float(x & 0xffff0000u); }
__device__ __forceinline__ unsigned f2bf(float x) {
  __bf16 b = (__bf16)x;
  return (unsigned)__builtin_bit_cast(unsigned short, b);
}
__device__ __forceinline__ float rbf(float x) { return bflo(f2bf(x)); }
typedef __attribute__((ext_vector_type(2))) __bf16 bf2_t;
__device__ __forceinline__ unsigned pack2(float lo, float hi) {
  f32x2 v = {lo, hi};
  return __builtin_bit_cast(unsigned, __builtin_convertvector(v, bf2_t));
}
__device__ __forceinline__ f32x2 rbf2(f32x2 v) {
  const unsigned p = __builtin_bit_cast(unsigned, __builtin_convertvector(v, bf2_t));
  return (f32x2){__uint_as_float(p << 16), __uint_as_float(p & 0xffff0000u)};
}
__device__ __forceinline__ f32x4 rbf4(f32x4 v) {
  const f32x2 a = rbf2((f32x2){v[0], v[1]}), b = rbf2((f32x2){v[2], v[3]});
  return (f32x4){a[0], a[1], b[0], b[1]};
}
template <int CTRL>
__device__ __forceinline__ float dpp_f(float v) {
  return __int_as_float(__builtin_amdgcn_update_dpp(0, __float_as_int(v), CTRL, 0xf, 0xf, true));
}
__device__ __forceinline__ float sum8(float v) {
  v += dpp_f<0xB1>(v);
  v += dpp_f<0x4E>(v);
  v += dpp_f<0x141>(v);
  return v;
}
__device__ __forceinline__ float sum16(float v) {
  v = sum8(v);
  v += dpp_f<0x140>(v);
  return v;
}
__device__ __forceinline__ float sum_rows(float v) {
  auto a = __builtin_amdgcn_permlane16_swap(__float_as_uint(v), __float_as_uint(v), false, false);
  v = __uint_as_float(a[0]) + __uint_as_float(a[1]);
  auto b = __builtin_amdgcn_permlane32_swap(__float_as_uint(v), __float_as_uint(v), false, false);
  return __uint_as_float(b[0]) + __uint_as_float(b[1]);
}
#define MFMA16(a, b, c) __builtin_amdgcn_mfma_f32_16x16x32_bf16(__builtin_bit_cast(bf16x8, a), __builtin_bit_cast(bf16x8, b), c, 0, 0, 0)
#define MFMA16K16(a, b, c) __builtin_amdgcn_mfma_f32_16x16x16bf16_1k(__builtin_bit_cast(bf16x4, a), __builtin_bit_cast(bf16x4, b), c, 0, 0, 0)
__device__ __forceinline__ u32x2 trd(const char* p) {
  return __builtin_bit_cast(u32x2, __builtin_amdgcn_ds_read_tr16_b64_v4i16((LAS s16x4*)p));
}
__device__ __forceinline__ u32x4 trfrag(const char* p, int pitch) {
  u32x2 a = trd(p), b = trd(p + 16 * pitch);
  return (u32x4){a[0], a[1], b[0], b[1]};
}
#define BAR() asm volatile("s_waitcnt lgkmcnt(0)\n\ts_barrier" ::: "memory")
#define DRAIN() do { __builtin_amdgcn_s_waitcnt(0x0F70); asm volatile("" ::: "memory"); } while (0)
#define SCHED() __builtin_amdgcn_sched_barrier(0)
template <int N> struct IC { static constexpr int value = N; };
template <int I, int N, class F>
__device__ __forceinline__ void sfor(F&& f) {
  if constexpr (I < N) { f(IC<I>()); sfor<I + 1, N>(f); }
}

__device__ __forceinline__ u32x4 ld16(rsrc_t r, unsigned off) { return __builtin_amdgcn_raw_buffer_load_b128(r, off, 0, SC1); }
__device__ __forceinline__ u32x2 ld8(rsrc_t r, unsigned off) { return __builtin_amdgcn_raw_buffer_load_b64(r, off, 0, SC1); }
__device__ __forceinline__ unsigned ld4(rsrc_t r, unsigned off) { return __builtin_amdgcn_raw_buffer_load_b32(r, off, 0, SC1); }
#define ST_AUX 16
__device__ __forceinline__ void st16(rsrc_t r, unsigned off, u32x4 v) { __builtin_amdgcn_raw_buffer_store_b128(v, r, off, 0, ST_AUX); }
#define ST16S(v, r, voff, soff, aux) do { const u32x4 v_ = (v); __builtin_amdgcn_raw_buffer_store_b128(v_, r, voff, soff, aux); asm volatile("s_nop 1" :: "v"(v_)); } while (0)
__device__ __forceinline__ void st8(rsrc_t r, unsigned off, u32x2 v) { __builtin_amdgcn_raw_buffer_store_b64(v, r, off, 0, SC1); }
__device__ __forceinline__ void st_flag4(rsrc_t r, unsigned off, unsigned tag, int l) {
  if (l < 32) st16(r, WS_F4 - WS_FWI + off + (unsigned)(l >> 3) * F4S + (unsigned)(l & 7) * 16u, (u32x4){tag, tag, tag, tag});
}
__device__ __forceinline__ void st4(rsrc_t r, unsigned off, unsigned v) { __builtin_amdgcn_raw_buffer_store_b32(v, r, off, 0, SC1); }
#define BULK_AUX 16
__device__ __forceinline__ u32x4 ld16p(rsrc_t r, unsigned off) { return __builtin_amdgcn_raw_buffer_load_b128(r, off, 0, BULK_AUX); }
__device__ __forceinline__ u32x2 ld8p(rsrc_t r, unsigned off) { return __builtin_amdgcn_raw_buffer_load_b64(r, off, 0, BULK_AUX); }
__device__ __forceinline__ void dma1k(rsrc_t r, char* lds_dst, unsigned l16, unsigned soff) {
  __builtin_amdgcn_raw_ptr_buffer_load_lds(r, (LAS void*)lds_dst, 16, l16, soff, 0, BULK_AUX);
}

#define STAMP(i)
#define STAMPF(i)
#define STAMPT(i) STAMP(i)

template <int N, int SLP>
__device__ __forceinline__ void wait_lines(rsrc_t r, const unsigned (&off)[N], unsigned tag) {
  bool need[N];
#pragma unroll
  for (int i = 0; i < N; ++i) need[i] = off[i] != ~0u;
  _Pragma("unroll 1") for (unsigned spins = 0; spins < SPIN_CAP; ++spins) {
    unsigned v[N];
#pragma unroll
    for (int i = 0; i < N; ++i) {
      v[i] = tag;
      if (need[i]) v[i] = ld4(r, off[i]);
    }
    bool any = false;
#pragma unroll
    for (int i = 0; i < N; ++i) { need[i] = v[i] != tag; any = any || need[i]; }
    if (!__any(any)) break;
    __builtin_amdgcn_s_sleep(SLP);
  }
  asm volatile("" ::: "memory");
}

__device__ __forceinline__ int tau(int rt, int n) { return 32 * (rt >> 1) + 8 * (n >> 2) + 4 * (rt & 1) + (n & 3); }
__device__ __forceinline__ int psi(int n) { return 2 * (n & 7) + (n >> 3); }
#define QX_CM(d, s0, s1, P) "v_cndmask_b32_dpp " d ", " s0 ", " s1 ", vcc quad_perm:" P " row_mask:0xf bank_mask:0xf\n\t"
#define QX_BODY(M, P) \
  asm("s_mov_b32 vcc_lo, " M "\n\ts_mov_b32 vcc_hi, " M "\n\t" \
      QX_CM("%0", "%12", "%8", P) "v_xor_b32 %4, %8, %0\n\t" QX_CM("%1", "%13", "%9", P) "v_xor_b32 %5, %9, %1\n\t" \
      QX_CM("%2", "%14", "%10", P) "v_xor_b32 %6, %10, %2\n\t" QX_CM("%3", "%15", "%11", P) "v_xor_b32 %7, %11, %3" \
      : "=&v"(t[0]), "=&v"(t[1]), "=&v"(t[2]), "=&v"(t[3]), "=&v"(u[0]), "=&v"(u[1]), "=&v"(u[2]), "=&v"(u[3]) \
      : "v"(a[0]), "v"(a[1]), "v"(a[2]), "v"(a[3]), "v"(b[0]), "v"(b[1]), "v"(b[2]), "v"(b[3]) : "vcc")
template <int BIT>
__device__ __forceinline__ void quad_xchg3(u32x4& a, u32x4& b) {
  unsigned t[4], u[4];
  if (BIT == 0) QX_BODY("0x55555555", "[1,0,3,2]");
  else QX_BODY("0x33333333", "[2,3,0,1]");
  a = (u32x4){t[0], t[1], t[2], t[3]};
#pragma unroll
  for (int d = 0; d < 4; ++d) b[d] ^= (unsigned)__builtin_amdgcn_update_dpp(0, (int)u[d], BIT == 0 ? 0xB1 : 0x4E, 0xf, 0xf, true);
}
__device__ __forceinline__ void quad_transpose(u32x4& x0, u32x4& x1, u32x4& x2, u32x4& x3) {
  quad_xchg3<0>(x0, x1);
  quad_xchg3<0>(x2, x3);
  quad_xchg3<1>(x0, x2);
  quad_xchg3<1>(x1, x3);
  asm volatile("" : "+v"(x0), "+v"(x1), "+v"(x2), "+v"(x3));
}

struct Params {
  const u16 *q, *k, *v, *shift_k, *ssm_k, *ssm_D, *w_in, *w_sp, *b_sp, *w_out, *cvec, *w_mod;
  u16* out;
  char* ws;
};

__global__ __launch_bounds__(512, 2) void mega(Params p) {
  __shared__ __attribute__((aligned(16))) char lds[LDS_TOTAL];
  {
    unsigned long long pc0;
    unsigned vt;
    asm volatile("v_readfirstlane_b32 vcc_lo, %3\n\ts_cmpk_lt_u32 %2, 192\n\ts_cbranch_scc1 4f\n\ts_cmpk_lt_u32 vcc_lo, 64\n\ts_cbranch_scc0 4f\n\t"
                 "s_lshr_b32 vcc_hi, %2, 3\n\ts_sub_u32 vcc_hi, vcc_hi, 24\n\ts_lshl_b32 vcc_hi, vcc_hi, 13\n\t"
                 "v_lshlrev_b32 %1, 7, %3\n\tv_add_u32 %1, vcc_hi, %1\n\ts_getpc_b64 %0\n.Lmega_here:\n\t"
                 "v_min_u32 %1, mega_tail-.Lmega_here-4, %1\n\ts_movk_i32 m0, 0x2000\n\ts_nop 1\n\tglobal_load_lds_dword %1, %0\n4:\n\t"
                 "s_cmpk_lt_u32 %2, 160\n\ts_cbranch_scc0 1f\n\ts_cmpk_lt_u32 vcc_lo, 256\n\ts_cbranch_scc1 3f\n\t"
                 "s_call_b64 vcc, .Ltouch1\n\ts_branch 3f\n1:\n\t"
                 "s_cmpk_lt_u32 vcc_lo, 256\n\ts_cbranch_scc1 2f\n\ts_cmpk_lt_u32 %2, 224\n\ts_cbranch_scc0 2f\n\ts_call_b64 vcc, .Ltouch3\n\ts_branch 3f\n"
                 "2:\n\ts_call_b64 vcc, .Ltouch2\n3:"
                 : "=&s"(pc0), "=&v"(vt) : "s"(blockIdx.x), "v"(threadIdx.x) : "vcc", "scc", "memory");
  }
  SCHED();
  const int tid = threadIdx.x, l = tid & 63;
  const int w = __builtin_amdgcn_readfirstlane(tid >> 6);
  const int bid = blockIdx.x, xcd = bid & 7, idx = bid >> 3;
#define TOUCH_AT(n) asm volatile("s_branch 1f\n.Ltouch" #n ":\n\ts_setpc_b64 vcc\n1:")
  char* const ws = p.ws;
  asm volatile("" ::"s"(p.q), "s"(p.k), "s"(p.v), "s"(p.shift_k), "s"(p.ssm_k), "s"(p.ssm_D), "s"(p.w_in), "s"(p.w_sp), "s"(p.b_sp),
               "s"(p.w_out), "s"(p.cvec), "s"(p.w_mod), "s"(p.out), "s"(p.ws));
  unsigned epoch, tag, t_start;
  unsigned long long t64;
  asm volatile("s_load_dword %0, %2, 0x0\n\ts_memrealtime %1" : "=&s"(epoch), "=&s"(t64) : "s"(ws));
#define TAG_READY() do { SCHED(); \
    asm volatile("s_waitcnt lgkmcnt(0)\n\ts_mul_i32 %0, %2, 0x9e3779b1\n\ts_add_i32 %0, %0, 0x9e3779b9\n\ts_mov_b64 %1, %3" \
                 : "=&s"(tag), "=&s"(t64r) : "s"(epoch), "s"(t64) : "scc"); t_start = (unsigned)t64r; SCHED(); } while (0)
  unsigned long long t64r;
  const rsrc_t rs = __builtin_amdgcn_make_buffer_rsrc(ws, 0, WS_END, 0x00020000);
  const unsigned l16 = (unsigned)l * 16u;
  STAMP(0);
  const bool is_conv = idx < 20;
  const int quad = xcd * 20 + idx;
  const int R = is_conv ? (xcd & 3) * 5 + (idx >> 2) : (xcd & 3) * 3 + 15 + (idx >> 2);
  const int c = (xcd >> 2) * 4 + (idx & 3);
  const unsigned fwi = WS_F4 + (unsigned)(xcd & 3) * F4S, fz = fwi + (WS_FZ - WS_FWI);
  const int n16 = l & 15, h4 = l >> 4;
  u32x4 wsp[4][8];
#define ROTC 2
#define WAUX 0
  const int rotg = ((idx >> 4) ^ ((ROTC & 2) ? idx >> 1 : 0)) & 1, rotb = (idx >> 2) & 3, rotr = (ROTC & 1) ? (idx & 1) * 2 : 0;
  const rsrc_t rsp = __builtin_amdgcn_make_buffer_rsrc((void*)p.w_sp, 0, S_ * S_ * 2, 0x00020000);
  const unsigned wsp_vo = (unsigned)(((l >> 2) & 3) * 8 * 4096 + h4 * 64 + ((l + rotb) & 3) * 16);
#define LDW(k) LDW_(k)
#define WSP_TR(g) WSP_TR_(g)
#define WROW R
#define LDW_(k) wsp[((k) >> 2) & 3][((k) >> 4) * 4 + ((k) & 3)] = __builtin_amdgcn_raw_buffer_load_b128(rsp, wvo, \
    (unsigned)((WROW * 64 + 16 * ((((k) >> 2) & 2) ^ rotr) + 4 * (((k) >> 2) & 1) + ((k) & 3)) * 4096 + w * 512 + ((((k) >> 4) ^ rotg) * 256)), WAUX)
#define WSP_TR_(g) quad_transpose(wsp[(g) & 3][((g) >> 2) * 4], wsp[(g) & 3][((g) >> 2) * 4 + 1], wsp[(g) & 3][((g) >> 2) * 4 + 2], wsp[(g) & 3][((g) >> 2) * 4 + 3])
  const rsrc_t rwi = __builtin_amdgcn_make_buffer_rsrc((void*)p.w_in, 0, D_ * F_ * 2, 0x00020000);
  const rsrc_t rwo = __builtin_amdgcn_make_buffer_rsrc((void*)p.w_out, 0, FH * D_ * 2, 0x00020000);
  const rsrc_t rq = __builtin_amdgcn_make_buffer_rsrc((void*)p.q, 0, S_ * D_ * 2, 0x00020000);
  auto hold = [&](unsigned tt) { while ((unsigned)__builtin_amdgcn_s_memrealtime() - t_start < tt) __builtin_amdgcn_s_sleep(2); };
  auto ne2 = [&](unsigned x, unsigned y) { unsigned d; asm("v_bitop3_b32 %0, %1, %2, %3 bitop3:0x7e" : "=v"(d) : "v"(x), "v"(y), "s"(tag)); return d; };
  volatile LAS unsigned* lf = (volatile LAS unsigned*)(lds + L_FLG);
  auto lf_set = [&](int i) { asm volatile("s_waitcnt lgkmcnt(0)" ::: "memory"); if (l == 0) lf[i] = tag; };
  auto lf_wait = [&](int i0, int n) {
    _Pragma("unroll 1") for (unsigned spins = 0; spins < (SPIN_CAP << 4); ++spins) {
      const unsigned v = lf[i0 + (l & (n - 1))];
      if (__all(v == tag)) break;
      __builtin_amdgcn_s_sleep(1);
    }
    asm volatile("" ::: "memory");
  };
  const unsigned b2o = fwi + (unsigned)((8 + c) * 20 + 5 * (w & 3) + (l < 5 ? l : 0)) * 128u;
  auto b2_wait = [&](u32x2 fv, int slp) {
    if (!__all(fv[0] == tag)) {
      _Pragma("unroll 1") for (unsigned spins = 0; spins < SPIN_CAP; ++spins) {
        if (fv[0] != tag) fv = ld8(rs, b2o);
        if (__all(fv[0] == tag)) break;
        if (slp) __builtin_amdgcn_s_sleep(8); else __builtin_amdgcn_s_sleep(4);
      }
    }
    asm volatile("" ::: "memory");
    if (l < 5) lf[44 + 5 * w + l] = fv[1];
    if (l == 0) lf[36 + w] = tag;
  };
  auto load_b2 = [&](int k0, int k1) {
    const unsigned a0 = (unsigned)(35 * w + (idx >> 2) * 4) * 1024u, gb = WS_WINF + (unsigned)(8 + c) * WBLK;
    unsigned a = a0, g = gb + a0;
#pragma unroll
    for (int k = k0; k < k1; ++k) {
      if (k == k0 || (k & 3) == 3) {
        a = k + (idx >> 2) * 4 >= 35 ? a0 - 35840u : a0;
        g = gb + a;
        asm volatile("" : "+s"(a), "+s"(g));
      }
      dma1k(rs, lds + a + k * 1024, l16, g + (unsigned)k * 1024u);
    }
  };
  char* const wo_st = lds + 122880;
  auto wo_ld = [&]() {
    const int U = R < 28 ? R : 0, u2 = U >> 1, fi = (u2 + 1) % 3;
    const int c0 = 4 * (U & 1) + (u2 < 8 ? u2 >> 1 : u2 < 11 ? 0 : 2);
    const int b0 = u2 < 8 ? CB * c0 + 2 * (u2 & 1) + 96 : CB * (c0 + (fi >> 1)) + 48 + 2 * (fi & 1), d1 = u2 < 8 ? -96 : 16, d2 = d1 + 16;
    const int b3 = u2 < 8 ? b0 - 64 : CB * (c0 + 1) + 50 + 16 * fi;
#pragma unroll
    for (int r = 0; r < 5; ++r) {
      const int g = r * 64 + l, rho = g / 10;
      const int fb = (rho & 16) ? ((rho & 2) ? b3 : b0 + d2) : ((rho & 2) ? b0 + d1 : b0);
      __builtin_amdgcn_raw_ptr_buffer_load_lds(rwo, (LAS void*)(wo_st + r * 1024), 16, (unsigned)((fb + (rho & 13)) * (D_ * 2) + (g % 10) * 16 + c * 160), 0, 0, 0);
    }
  };
  auto rt_ld = [&](int i0, char* st) {
    const int cb = (i0 < 160 ? 8 : 0) + (i0 & 7), Q = (i0 < 160 ? i0 : i0 - 160) >> 3;
#pragma unroll
    for (int r = 0; r < 7; ++r) {
      const int g = r * 64 + l;
      __builtin_amdgcn_raw_ptr_buffer_load_lds(rwi, (LAS void*)(st + r * 1024), 16, (unsigned)((g / 14) * (F_ * 2) + (g % 14) * 16 + Q * 32 * F_ * 2 + cb * CB * 2),
                                               0, 0, NT);
    }
  };
  auto rt_job = [&](int i0, char* st) {
    const int cb = (i0 < 160 ? 8 : 0) + (i0 & 7), Q = (i0 < 160 ? i0 : i0 - 160) >> 3;
    const int col = 640 * (l >> 5) + 32 * Q + (l & 31);
    DRAIN();
    u32x4 x[7], o[7];
    f32x2 xu[7][4];
    {
      const char* bp = st + (4 * h4 + (n16 >> 2)) * 224 + (n16 & 3) * 8;
#pragma unroll
      for (int j = 0; j < 7; ++j) x[j] = trfrag(bp + j * 32, 224);
#pragma unroll
      for (int j = 0; j < 7; ++j)
#pragma unroll
        for (int e = 0; e < 4; ++e) { xu[j][e] = (f32x2){bflo(x[j][e]), bfhi(x[j][e])}; asm volatile("" : "+v"(xu[j][e])); }
    }
    unsigned mk[4][2], pv = (unsigned)col * 8u, cw = l >= 32 ? (unsigned)(7168 + (l - 32) * 4) : (unsigned)(7298 + l * 2 - 4), m12 = n16 == 12 ? ~0u : 0u;
    unsigned fgo = l < 32 ? (unsigned)(l >> 3) * F4S + (unsigned)(l & 7) * 16u : 0x80000000u;
#pragma unroll
    for (int i = 0; i < 4; ++i) {
      mk[i][0] = n16 == 4 * i ? 0x3F803F80u : 0u; mk[i][1] = n16 == 4 * i + 1 ? ~0u : 0u;
      asm volatile("" : "+v"(mk[i][0]), "+v"(mk[i][1]));
    }
    asm volatile("" : "+v"(pv), "+v"(cw), "+v"(m12), "+v"(fgo));
    unsigned ob = WS_WINF + (unsigned)cb * WBLK + (unsigned)(Q * 7) * 1024u;
    asm volatile("" : "+s"(ob));
    u32x2 pp8[8];
    hold(CB_T);
    _Pragma("unroll 1") for (unsigned spins = 0; spins < SPIN_CAP; ++spins) {
#pragma unroll
      for (int i = 0; i < 8; ++i) pp8[i] = __builtin_amdgcn_raw_buffer_load_b64(rs, pv, WS_MODP + (unsigned)i * 10240u, SC1);
      if (!__any(((ne2(pp8[0][0], pp8[1][0]) | ne2(pp8[2][0], pp8[3][0]) | ne2(pp8[4][0], pp8[5][0])) | ne2(pp8[6][0], pp8[7][0])) != 0u)) break;
      __builtin_amdgcn_s_sleep(4);
    }
    STAMPF(9);
    __builtin_amdgcn_s_setprio(3);
    {
      auto pf = [&](int i) { return __uint_as_float(pp8[i][1]); };
      const float t = rbf(((pf(0) + pf(1)) + (pf(2) + pf(3))) + ((pf(4) + pf(5)) + (pf(6) + pf(7))));
      if (l >= 32) *(float*)(st + cw) = rbf(1.0f + t);
      else *(u16*)(st + cw + 2) = (u16)(__float_as_uint(t) >> 16);
    }
    asm volatile("" ::: "memory");
    const f32x4 ca = *(const volatile LAS f32x4*)(st + 7168 + h4 * 16), cc = *(const volatile LAS f32x4*)(st + 7168 + 64 + h4 * 16);
    const u32x2 e0 = *(const volatile LAS u32x2*)(st + 7296 + h4 * 8), e1 = *(const volatile LAS u32x2*)(st + 7328 + h4 * 8);
    SCHED();
#pragma unroll
    for (int j = 0; j < 7; ++j) {
      const f32x2 y0 = xu[j][0] * (f32x2){ca[0], ca[1]}, y1 = xu[j][1] * (f32x2){ca[2], ca[3]}, y2 = xu[j][2] * (f32x2){cc[0], cc[1]}, y3 = xu[j][3] * (f32x2){cc[2], cc[3]};
      o[j] = (u32x4){pack2(y0[0], y0[1]), pack2(y1[0], y1[1]), pack2(y2[0], y2[1]), pack2(y3[0], y3[1])};
      if (j == 6) {
#pragma unroll
        for (int e = 0; e < 4; ++e) o[j][e] = (o[j][e] & ~m12) | mk[3][0];
      }
      ST16S(o[j], rs, l16 + (unsigned)(j & 3) * 1024u, ob + (unsigned)(j >> 2) * 4096u, SC1);
      SCHED();
    }
    __builtin_amdgcn_s_setprio(0);
    STAMPF(11);
    f32x4 d[2] = {{0.f, 0.f, 0.f, 0.f}, {0.f, 0.f, 0.f, 0.f}};
#pragma unroll
    for (int j = 0; j < 7; ++j) {
      const unsigned one = mk[j & 3][0], sr = mk[j & 3][1];
      d[j >> 2] = MFMA16(((u32x4){one, one, one, one}), o[j], d[j >> 2]);
      d[j >> 2] = MFMA16(((u32x4){sr & e0[0], sr & e0[1], sr & e1[0], sr & e1[1]}), x[j], d[j >> 2]);
    }
#pragma unroll
    for (int m = 0; m < 2; ++m)
      st16(rs, WS_CSP + (unsigned)((cb * 20 + Q) * 2 + m) * 1024u + l16, (u32x4){tag, __float_as_uint(d[m][0]), tag, __float_as_uint(d[m][1])});
    STAMPF(13);
    u32x4 tg4 = {tag, t_start, tag, tag};
    asm volatile("s_waitcnt vmcnt(2)" : "+v"(tg4) :: "memory");
    ST16S(tg4, rs, fgo, WS_F4 + (unsigned)(cb * 20 + Q) * 128u, SC1);
  };

  auto red8 = [&](float v) { v += dpp_f<0x128>(v); return sum_rows(v); };
  const rsrc_t rwm = __builtin_amdgcn_make_buffer_rsrc((void*)p.w_mod, 0, D_ * 3840 * 2, 0x00020000);
  auto gemv_load = [&](u32x4* pre, int blk, int sl, int np) {
    pre[10][0] = *(const unsigned*)(p.cvec + 8 * np * sl + 2 * (l < 4 * np ? l : 0));
#pragma unroll
    for (int pp = 0; pp < np; ++pp)
      pre[pp] = __builtin_amdgcn_raw_buffer_load_b128(rwm, (unsigned)((l >> 3) * 7680 + (l & 7) * 16), (unsigned)((8 * np * sl + pp * 8) * 7680 + blk * 128), NT);
  };
  auto gemv_fin = [&](float* acc, const u32x4* pre, int np, char* scr) {
    const unsigned cv2 = pre[10][0];
    unsigned* svt = (unsigned*)scr + w * 96;
#pragma unroll
    for (int e = 0; e < 2; ++e) {
      const int row = 2 * l + e;
      const float cv = e ? bfhi(cv2) : bflo(cv2);
      if (l < 4 * np) svt[(row & 7) * 12 + (row >> 3)] = __float_as_uint(rbf(cv * __builtin_amdgcn_rcpf(1.0f + __expf(-cv))));
    }
    asm volatile("s_waitcnt lgkmcnt(0)" ::: "memory");
    u32x4 sv[3];
#pragma unroll
    for (int i = 0; i < (np + 3) / 4; ++i) sv[i] = *(const u32x4*)(svt + (l >> 3) * 12 + 4 * i);
    SCHED();
#pragma unroll
    for (int e = 0; e < 8; ++e) acc[e] = 0.f;
#pragma unroll
    for (int pp = 0; pp < np; ++pp) {
      const unsigned sh = sv[pp >> 2][pp & 3], sl2 = sh >> 16;
#pragma unroll
      for (int e = 0; e < 4; ++e) {
        const unsigned x = pre[pp][e];
        acc[2 * e] = __builtin_amdgcn_fdot2_f32_bf16(__builtin_bit_cast(bf2_t, x), __builtin_bit_cast(bf2_t, sl2), acc[2 * e], false);
        acc[2 * e + 1] = __builtin_amdgcn_fdot2_f32_bf16(__builtin_bit_cast(bf2_t, x), __builtin_bit_cast(bf2_t, sh), acc[2 * e + 1], false);
      }
    }
#pragma unroll
    for (int e = 0; e < 8; ++e) acc[e] = red8(acc[e]);
  };
  auto gemv_put = [&](const float* acc, unsigned off) {
    float v = acc[0];
#pragma unroll
    for (int e = 1; e < 8; ++e) { v = (l >> 3) == e ? acc[e] : v; asm("" : "+v"(v)); }
    st8(rs, off + (unsigned)l * 8u, (u32x2){tag, (unsigned)__builtin_amdgcn_ds_bpermute(((l & 7) * 8 + (l >> 3)) * 4, (int)__float_as_uint(v))});
  };
  auto cs_ld = [&](u32x4* v, int cj) {
    const int cb = cj / 7, j = cj % 7;
#pragma unroll
    for (int k = 0; k < 5; ++k)
      v[k] = __builtin_amdgcn_raw_buffer_load_b128(rs, (unsigned)(h4 * 10240 + n16 * 16), WS_CSP + (unsigned)((cb * 40 + (j >> 2) + 2 * k) * 1024 + (j & 3) * 256), SC1);
  };
  auto cs_fin = [&](u32x4* v, int cj) {
    auto bad = [&]() { return __any(((ne2(v[0][0], v[0][2]) | ne2(v[1][0], v[1][2]) | ne2(v[2][0], v[2][2])) | ne2(v[3][0], v[3][2]) | ne2(v[4][0], v[4][2])) != 0u); };
    if (bad()) {
      _Pragma("unroll 1") for (unsigned spins = 0; spins < SPIN_CAP; ++spins) {
        __builtin_amdgcn_s_sleep(8);
        cs_ld(v, cj);
        if (!bad()) break;
      }
    }
    float t0 = 0.f, t1 = 0.f;
#pragma unroll
    for (int k = 0; k < 5; ++k) { t0 += __uint_as_float(v[k][1]); t1 += __uint_as_float(v[k][3]); }
    t0 = sum_rows(t0); t1 = sum_rows(t1);
    if (l < 16) st16(rs, WS_CS + (unsigned)((cj / 7) * 2048 + (cj % 7) * 256) + l16, (u32x4){tag, __float_as_uint(t0), tag, __float_as_uint(t1)});
  };
  const bool cs2 = w == 2 && R >= 20 && R < 27, cs1 = w == 2 && R >= 25;
  u32x4 csv[5];
  typedef const __attribute__((address_space(4))) unsigned* cptr_t;

  if (__builtin_expect(is_conv, 1)) {
    if (w >= 4) {
      TOUCH_AT(1);
      const int pw = w - 4;
      const rsrc_t ra = __builtin_amdgcn_make_buffer_rsrc((void*)((idx & 2) ? p.v : p.k), 0, S_ * D_ * 2, 0x00020000);
      const rsrc_t rb = __builtin_amdgcn_make_buffer_rsrc((void*)((idx & 2) ? p.k : p.v), 0, S_ * D_ * 2, 0x00020000);
      const rsrc_t rk = __builtin_amdgcn_make_buffer_rsrc((void*)p.k, 0, S_ * D_ * 2, 0x00020000);
      const rsrc_t rv = __builtin_amdgcn_make_buffer_rsrc((void*)p.v, 0, S_ * D_ * 2, 0x00020000);
#define NROT 2
      const unsigned vo = (unsigned)((128 * pw + 2 * l) * (D_ * 2));
      const unsigned so0 = (unsigned)((idx & 1) * (D_ * 2) + quad * 8), so1 = (unsigned)((1 - (idx & 1)) * (D_ * 2) + quad * 8);
      u32x2 g[3][4], kh;
      auto gl = [&](int s, int j0, int j1) {
        __builtin_amdgcn_s_setprio(0);
#pragma unroll
        for (int j = j0; j < j1; ++j) {
          if (s < NROT) g[s][j] = __builtin_amdgcn_raw_buffer_load_b64(j < 2 ? ra : rb, vo, ((j & 1) ? so1 : so0) + (unsigned)(s * 512 * D_ * 2), 0);
          else g[s][j] = __builtin_amdgcn_raw_buffer_load_b64(j < 2 ? rk : rv, vo + (unsigned)((j & 1) * D_ * 2), (unsigned)(quad * 8 + s * 512 * D_ * 2), 0);
          SCHED();
        }
        __builtin_amdgcn_s_setprio(3);
      };
      const cptr_t sp = (cptr_t)(p.shift_k + quad * 16);
      unsigned spv[8];
#pragma unroll
      for (int i = 0; i < 8; ++i) spv[i] = sp[i];
      __builtin_amdgcn_s_sleep(3);
      gl(0, 0, 4);
      {
        const int row = 512 * (l >> 2) + 128 * pw - 3 + (l & 3);
        kh = __builtin_amdgcn_raw_buffer_load_b64(rk, (l >= 12 || (l & 3) == 3 || row < 0) ? 0x80000000u : (unsigned)(row * D_ * 2 + quad * 8), 0, 0);
      }
      TAG_READY();
      f32x2 wt[2][4];
#pragma unroll
      for (int d = 0; d < 2; ++d) {
        wt[d][0] = (f32x2){bflo(spv[4 * d]), bflo(spv[4 * d + 2])}; wt[d][1] = (f32x2){bfhi(spv[4 * d]), bfhi(spv[4 * d + 2])};
        wt[d][2] = (f32x2){bflo(spv[4 * d + 1]), bflo(spv[4 * d + 3])}; wt[d][3] = (f32x2){bfhi(spv[4 * d + 1]), bfhi(spv[4 * d + 3])};
        asm volatile("" : "+s"(wt[d][0]), "+s"(wt[d][1]), "+s"(wt[d][2]), "+s"(wt[d][3]));
      }
      SCHED();
      {
        const int j = idx * 4 + pw;
        const unsigned o = (unsigned)((512 + 64 * (j >> 2) + l) * (D_ * 2) + (((160 * xcd) >> 7) + ((j >> 1) & 1)) * 128);
        __builtin_amdgcn_raw_ptr_buffer_load_lds((j & 1) ? ra : rb, (LAS void*)(lds + 98304), 4, j < 64 ? o : 0x80000000u, 0, 0, 0);
      }
      SCHED();
      gl(1, 0, 4);
      STAMP(21);
      unsigned up = (unsigned)(L_U + 512 + 256 * pw + (l >> 2) * 16 + (3 - (l & 3)) * 4);
      unsigned lfo = l == 0 ? (unsigned)(L_FLG + 4 * pw) : (unsigned)(100352 + tid * 4), lfr = (unsigned)(L_FLG + (l & 3) * 4);
      unsigned yr = (unsigned)(L_U + (n16 >> 2) * U_CH + (128 * pw + 32 * h4 + 8 * (n16 & 1) + 4 * ((n16 >> 1) & 1)) * 2);
      unsigned yo = (unsigned)(8 * pw + (l >> 3)) * 20480u + (unsigned)quad * 128u + (unsigned)(l & 7) * 16u;
      unsigned mo = (unsigned)((n16 < 12 ? n16 >> 2 : 0) * D_ + quad * 4 + (l & 3)) * 8u + (unsigned)h4 * 40960u;
      unsigned k3o = (unsigned)(16 * pw + (l >> 2)) * 20480u + (unsigned)(((l >> 1) & 1) * 10240 + (2 * quad + (l & 1)) * 16);
      unsigned k3d = (unsigned)(L_U + (2 * (l & 1) + ((l >> 1) & 1)) * U_CH + 512 + (1536 + 128 * pw + 8 * (l >> 2)) * 2);
      unsigned xfo = (unsigned)(((4 * (l >> 5) + pw) * 4 + ((l >> 3) & 3)) * 160 + quad) * 128u + (unsigned)(l & 7) * 16u;
      u32x4 tg4 = {tag, tag, tag, tag};
      asm volatile("" : "+v"(up), "+v"(lfo), "+v"(lfr), "+v"(yr), "+v"(yo), "+v"(mo), "+v"(k3o), "+v"(k3d), "+v"(xfo), "+v"(tg4));
      unsigned so = WS_ST1 + (unsigned)xcd * 16384u + (unsigned)pw * 1024u;
      asm volatile("" : "+s"(so));
      auto pf_set = [&](int i) { asm volatile("" ::: "memory"); *(volatile LAS unsigned*)(lds + lfo + 4 * i) = tg4[0]; };
      auto pf_wait = [&](int i0) {
        _Pragma("unroll 1") for (unsigned spins = 0; spins < (SPIN_CAP << 4); ++spins) {
          if (__all(*(volatile LAS unsigned*)(lds + lfr + 4 * i0) == tag)) break;
          __builtin_amdgcn_s_sleep(1);
        }
        asm volatile("" ::: "memory");
      };
      unsigned hl[2][3];
      auto halo = [&](int s) {
#pragma unroll
        for (int i = 0; i < 6; ++i) { hl[i & 1][i >> 1] = (unsigned)__builtin_amdgcn_readlane(kh[i & 1], 4 * s + (i >> 1)); asm volatile("" : "+v"(hl[i & 1][i >> 1])); }
      };
      auto kv = [&](auto S__, auto N__) {
        constexpr int s = decltype(S__)::value;
        u32x2* x = g[s];
        SCHED();
        asm volatile("s_waitcnt vmcnt(%5)" : "+v"(x[0]), "+v"(x[1]), "+v"(x[2]), "+v"(x[3]), "+v"(kh) : "n"(decltype(N__)::value));
        if (s == 2) STAMP(27);
        if (s == 0) halo(0);
        if (s < NROT) {
          if (idx & 1) { asm volatile(""); const u32x2 t = x[0], u = x[2]; x[0] = x[1]; x[1] = t; x[2] = x[3]; x[3] = u; }
          asm volatile("" : "+v"(x[0]), "+v"(x[1]), "+v"(x[2]), "+v"(x[3]));
          if (idx & 2) { asm volatile(""); const u32x2 t = x[0], u = x[1]; x[0] = x[2]; x[1] = x[3]; x[2] = t; x[3] = u; }
          asm volatile("" : "+v"(x[0]), "+v"(x[1]), "+v"(x[2]), "+v"(x[3]));
        }
        f32x2 kf[2][5], o[2][2];
#pragma unroll
        for (int d = 0; d < 2; ++d) {
          unsigned kk[5];
          kk[3] = x[0][d]; kk[4] = x[1][d];
          kk[2] = __builtin_amdgcn_update_dpp(hl[d][2], kk[4], 0x138, 0xf, 0xf, false);
          kk[1] = __builtin_amdgcn_update_dpp(hl[d][1], kk[3], 0x138, 0xf, 0xf, false);
          kk[0] = __builtin_amdgcn_update_dpp(hl[d][0], kk[2], 0x138, 0xf, 0xf, false);
#pragma unroll
          for (int j = 0; j < 5; ++j) kf[d][j] = (f32x2){bflo(kk[j]), bfhi(kk[j])};
        }
#pragma unroll
        for (int t = 0; t < 5; ++t) {
#pragma unroll
          for (int i = 0; i < 4; ++i) {
            const int d = i >> 1, r = i & 1;
            if (t == 4) o[d][r] *= (f32x2){bflo(x[2 + r][d]), bfhi(x[2 + r][d])};
            else if (t == 0) o[d][r] = kf[d][r + 3] * wt[d][0];
            else o[d][r] = __builtin_elementwise_fma(kf[d][r + 3 - t], wt[d][t], o[d][r]);
          }
          asm volatile("" : "+v"(o[0][0]), "+v"(o[0][1]), "+v"(o[1][0]), "+v"(o[1][1]));
        }
#pragma unroll
        for (int i = 0; i < 4; ++i) *(unsigned*)(lds + up + i * U_CH + s * 1024) = pack2(o[i >> 1][1][i & 1], o[i >> 1][0][i & 1]);
        pf_set(4 * s);
        SCHED();
        if (s < 2) halo(s + 1);
        SCHED();
      };
      u32x4 sv[4], qv[4];
      u32x2 mv[2];
      auto ld_e = [&]() {
#pragma unroll
        for (int s = 0; s < 4; ++s) sv[s] = __builtin_amdgcn_raw_buffer_load_b128(rs, l16, so + (unsigned)s * 4096u, SC1);
        SCHED();
      };
      auto s_bad = [&]() { return __any(((ne2(sv[0][1], sv[0][3]) | ne2(sv[1][1], sv[1][3]) | ne2(sv[2][1], sv[2][3])) | ne2(sv[3][1], sv[3][3])) != 0u); };
      kv(IC<0>(), IC<5>());
      STAMP(23);
#define G2A 4
      gl(2, 0, G2A);
      kv(IC<1>(), IC<G2A>());
      gl(2, G2A, 4);
      STAMPF(9);
      u32x4 k3[2];
      auto ld_k3 = [&]() {
        k3[0] = __builtin_amdgcn_raw_buffer_load_b128(rs, k3o, WS_KV3, SC1);
        k3[1] = __builtin_amdgcn_raw_buffer_load_b128(rs, k3o, WS_KV3 + 5120u, SC1);
      };
      hold(K_T);
      ld_k3();
      SCHED();
      ld_e();
      kv(IC<2>(), IC<6>());
      STAMPF(11);
      auto k_bad = [&]() { return __any((ne2(k3[0][1], k3[0][3]) | ne2(k3[1][1], k3[1][3])) != 0u); };
      if (k_bad()) {
        _Pragma("unroll 1") for (unsigned spins = 0; spins < SPIN_CAP; ++spins) {
          __builtin_amdgcn_s_sleep(4);
          ld_k3();
          if (!k_bad()) break;
        }
      }
      *(u32x4*)(lds + k3d) = (u32x4){k3[0][0], k3[0][2], k3[1][0], k3[1][2]};
      pf_set(12);
      SCHED();
      STAMPF(18);
      if (s_bad()) {
        _Pragma("unroll 1") for (unsigned spins = 0; spins < SPIN_CAP; ++spins) {
          __builtin_amdgcn_s_sleep(4);
          ld_e();
          if (!s_bad()) break;
        }
      }
      asm volatile("" ::: "memory");
      SCHED();
      STAMPF(17);
      auto ld_m = [&]() {
        mv[0] = __builtin_amdgcn_raw_buffer_load_b64(rs, mo, WS_MODQ, SC1);
        mv[1] = __builtin_amdgcn_raw_buffer_load_b64(rs, mo, WS_MODQ + 20480u, SC1);
      };
#define MFIRST 1
      if (MFIRST) ld_m();
      SCHED();
#pragma unroll
      for (int s = 0; s < 4; ++s) qv[s] = __builtin_amdgcn_raw_buffer_load_b128(rs, yo, WS_QB + (unsigned)s * (32u * 20480u), BULK_AUX);
      SCHED();
      if (!MFIRST) ld_m();
      SCHED();
      STAMP(2);
      u32x2 yv[4][2];
      auto ld_y = [&](int s) {
#pragma unroll
        for (int e = 0; e < 2; ++e) yv[s][e] = trd(lds + yr + 1024 * s + 32 * e);
      };
      float r1[4][2], m1[4][2];
#pragma unroll
      for (int i = 0; i < 8; ++i) {
        const unsigned x = sv[i >> 1][2 * (i & 1)];
        r1[i >> 1][i & 1] = __builtin_amdgcn_rcpf(bfhi(x));
        m1[i >> 1][i & 1] = -bflo(x) * r1[i >> 1][i & 1];
        asm volatile("" : "+v"(r1[i >> 1][i & 1]), "+v"(m1[i >> 1][i & 1]));
      }
      pf_wait(20);
      ld_y(0); ld_y(1);
      SCHED();
      if (__any(ne2(mv[0][0], mv[1][0]) != 0u)) {
        _Pragma("unroll 1") for (unsigned spins = 0; spins < SPIN_CAP; ++spins) {
          __builtin_amdgcn_s_sleep(4);
          ld_m();
          if (!__any(ne2(mv[0][0], mv[1][0]) != 0u)) break;
        }
      }
      STAMPF(13);
      f32x4 al, be;
      {
        const float mgf = rbf(sum_rows(__uint_as_float(mv[0][1]) + __uint_as_float(mv[1][1])));
        auto rl = [&](int i) { return __uint_as_float(__builtin_amdgcn_readlane(__float_as_uint(mgf), i)); };
        const f32x4 sa = {rl(0), rl(1), rl(2), rl(3)}, ca = {rl(4), rl(5), rl(6), rl(7)}, ga = {rl(8), rl(9), rl(10), rl(11)};
        al = rbf4(ca + 1.0f) * ga;
        be = sa * ga;
        asm volatile("" : "+v"(al), "+v"(be));
      }
      f32x4 qf[2][2], P[2][2];
      auto xpre = [&](int s) {
#pragma unroll
        for (int e = 0; e < 2; ++e) {
          const float r = r1[s][e], m = m1[s][e];
          const f32x4 qq = {bflo(qv[s][2 * e]), bfhi(qv[s][2 * e]), bflo(qv[s][2 * e + 1]), bfhi(qv[s][2 * e + 1])};
          qf[s & 1][e] = qq;
          P[s & 1][e] = __builtin_elementwise_fma(__builtin_elementwise_fma(qq, (f32x4){r, r, r, r}, (f32x4){m, m, m, m}), al, be);
        }
        asm volatile("" : "+v"(qf[s & 1][0]), "+v"(qf[s & 1][1]), "+v"(P[s & 1][0]), "+v"(P[s & 1][1]));
      };
      auto xfin = [&](int s) {
        u32x4 o;
#pragma unroll
        for (int e = 0; e < 2; ++e) {
          const u32x2 y = yv[s][e];
          const f32x4 yf = {bflo(y[0]), bfhi(y[0]), bflo(y[1]), bfhi(y[1])};
          const f32x4 x = __builtin_elementwise_fma(P[s & 1][e], yf, qf[s & 1][e]);
          o[2 * e] = pack2(x[0], x[1]);
          o[2 * e + 1] = pack2(x[2], x[3]);
        }
        ST16S(o, rs, yo, WS_X1B + (unsigned)s * (32u * 20480u), SC1);
      };
      xpre(0); xfin(0);
      xpre(1); xfin(1);
      SCHED();
      xpre(2); xpre(3);
#define FVR 5
#define MSPLIT 0
      if (MSPLIT) {
        pf_wait(24);
        ld_y(2);
        xfin(2);
        SCHED();
        pf_wait(28);
        ld_y(3);
        xfin(3);
      } else {
        pf_wait(28);
        ld_y(2); ld_y(3);
        xfin(2); xfin(3);
      }
      STAMP(28);
      asm volatile("s_waitcnt vmcnt(2)" ::: "memory");
      ST16S(tg4, rs, xfo, WS_FX, SC1);
      SCHED();
      asm volatile("s_waitcnt vmcnt(1)" ::: "memory");
      ST16S(tg4, rs, xfo, WS_FX + 32u * 160u * 128u, SC1);
      STAMP(5);
    } else {
      const int ch = w;
      char* U = lds + L_U + ch * U_CH;
      char* G = lds + ch * (4 * G_COPY);
      {
        const rsrc_t rf = __builtin_amdgcn_make_buffer_rsrc((void*)(p.ssm_k + (size_t)(quad * 4 + ch) * S_), 0, S_ * 2, 0x00020000);
        u32x4 fa[4];
        u32x2 fb[4];
        __builtin_amdgcn_s_setprio(3);
#pragma unroll
        for (int k = 0; k < 4; ++k) {
          fa[k] = __builtin_amdgcn_raw_buffer_load_b128(rf, l16 + (unsigned)k * 1024u, 0, NT);
        }
        __builtin_amdgcn_s_setprio(1);
        TAG_READY();
#pragma unroll
        for (int k = 0; k < 4; ++k) {
#pragma unroll
          for (int d = 0; d < 2; ++d)
            fb[k][d] = __builtin_amdgcn_update_dpp(k < 3 ? (unsigned)__builtin_amdgcn_readfirstlane(fa[k < 3 ? k + 1 : k][d]) : 0u, fa[k][d], 0x130, 0xf, 0xf, false);
          const unsigned x[6] = {fa[k][0], fa[k][1], fa[k][2], fa[k][3], fb[k][0], fb[k][1]};
          unsigned y[5];
#pragma unroll
          for (int d = 0; d < 5; ++d) y[d] = __builtin_amdgcn_alignbit(x[d + 1], x[d], 16);
          char* gp = G + 32 + k * 1024 + l16;
          *(u32x4*)(gp) = fa[k];
          *(u32x4*)(gp + G_COPY) = (u32x4){y[0], y[1], y[2], y[3]};
          *(u32x4*)(gp + 2 * G_COPY) = (u32x4){x[1], x[2], x[3], x[4]};
          *(u32x4*)(gp + 3 * G_COPY) = (u32x4){y[1], y[2], y[3], y[4]};
          if (k == 0) {
            const unsigned f0 = __builtin_amdgcn_readfirstlane(x[0]), f1 = __builtin_amdgcn_readfirstlane(x[1]);
            const unsigned long long f64 = (unsigned long long)f0 | ((unsigned long long)f1 << 32);
            const unsigned long long hi = l ? (f64 << (64 - 16 * (l & 3))) : 0ull;
            if (l < 4) {
              *(u32x4*)(G + l * G_COPY) = (u32x4){0u, 0u, 0u, 0u};
              *(u32x4*)(G + l * G_COPY + 16) = (u32x4){0u, 0u, (unsigned)hi, (unsigned)(hi >> 32)};
            }
            if (l >= 32) *(u32x4*)(U + (l - 32) * 16) = (u32x4){0u, 0u, 0u, 0u};
          }
        }
        asm volatile("s_waitcnt lgkmcnt(0)" ::: "memory");
        STAMP(1);
      }
      const int a = n16, h = h4, jj = h >> 1, e2 = h & 1;
      const char* ap = G + ((a + 1) & 3) * G_COPY + 2 * (16 * jj + a - 8 * e2 + 9 - ((a + 1) & 3));
      const char* bp = U + 512 + 32 * (a - jj) + 16 * e2;
      auto lda = [&](u32x4* A, int r, int p0, int np) {
#pragma unroll
        for (int pp = 0; pp < np; ++pp) {
          u32x2 lo = *(const volatile LAS u32x2*)(ap + 512 * (p0 + pp) + 64 * r);
          u32x2 hi = *(const volatile LAS u32x2*)(ap + 512 * (p0 + pp) + 64 * r + 8);
          A[pp] = (u32x4){lo[0], lo[1], hi[0], hi[1]};
        }
      };
      u32x4 AD;
      {
        const unsigned dDr = (*(cptr_t)(p.ssm_D + quad * 4 + (ch & 2)) >> (16 * (ch & 1))) & 0xffffu;
        const unsigned dv = h == (a >> 3) ? ((a & 1) ? dDr : dDr << 16) : 0u;
        const int dw = (7 - (a & 7)) >> 1;
#pragma unroll
        for (int d = 0; d < 4; ++d) AD[d] = dw == d ? dv : 0u;
      }
      u32x4 A0[8][4];
      f32x4 acc[8];
#pragma unroll
      for (int n = 0; n < 8; ++n) acc[n] = (f32x4){0.f, 0.f, 0.f, 0.f};
#pragma unroll
      for (int r = 0; r < 8; ++r) lda(A0[r], r, 0, 4);
      auto yput = [&](int s) {
#pragma unroll
        for (int e = 0; e < 2; ++e)
          *(u32x2*)(U + 1024 * s + 512 * e + (16 * a + 4 * h) * 2) = (u32x2){pack2(acc[2 * s + e][0], acc[2 * s + e][1]), pack2(acc[2 * s + e][2], acc[2 * s + e][3])};
        asm volatile("" ::: "memory");
        if (l == 0) lf[16 + 4 * s + ch] = tag;
      };
      auto stage = [&](auto S__) {
        constexpr int s = decltype(S__)::value, NH = s < 2 ? 4 - 2 * s : 0, NP = 8 - 2 * s, NB = s < 2 ? 2 : 8;
        u32x4 A1[3][NH > 0 ? NH : 1], B[NB][2];
        auto ldb = [&](int r) {
#pragma unroll
          for (int e = 0; e < 2; ++e) B[r % NB][e] = *(const u32x4*)(bp + 512 * (2 * s + e) - 64 * r);
        };
        if (NH) { lda(A1[0], 0, 4, NH); lda(A1[1], 1, 4, NH); }
        SCHED();
        if (s > 0) yput(s - 1);
        lf_wait(4 * s, 4);
        if (s == 0) STAMP(3);
#pragma unroll
        for (int r = 0; r < NB; ++r) ldb(r);
        SCHED();
#pragma unroll
        for (int e = 0; e < 2; ++e) acc[2 * s + e] = MFMA16(AD, B[0][e], acc[2 * s + e]);
#pragma unroll
        for (int r = 0; r < 8; ++r) {
          if (NH && r + 2 < 8) lda(A1[(r + 2) % 3], r + 2, 4, NH);
          SCHED();
#pragma unroll
          for (int e = 0; e < 2; ++e)
#pragma unroll
            for (int pp = 0; pp < NP - e; ++pp) acc[2 * s + e + pp] = MFMA16(pp < 4 ? A0[r][pp] : A1[r % 3][pp < 4 ? 0 : pp - 4], B[r % NB][e], acc[2 * s + e + pp]);
          SCHED();
          if (r + NB < 8) ldb(r + NB);
        }
      };
      stage(IC<0>());
      STAMP(4);
      stage(IC<1>());
      STAMPF(9);
      u32x2 fv;
      if (MSPLIT) {
        u32x4 B[8][2];
        auto ldb = [&](int r, int m0) {
#pragma unroll
          for (int m = 0; m < 2; ++m) B[r][m] = *(const u32x4*)(bp + 512 * (m0 + m) - 64 * r);
        };
        yput(1);
        if (w == 3) wo_ld();
        __builtin_amdgcn_s_setprio(3);
        lf_wait(8, 4);
        STAMP(22);
#pragma unroll
        for (int r = 0; r < 8; ++r) ldb(r, 4);
        SCHED();
#pragma unroll
        for (int m = 0; m < 2; ++m) acc[4 + m] = MFMA16(AD, B[0][m], acc[4 + m]);
#pragma unroll
        for (int r = 0; r < 8; ++r) {
          acc[4] = MFMA16(A0[r][0], B[r][0], acc[4]);
          acc[5] = MFMA16(A0[r][1], B[r][0], acc[5]);
          acc[5] = MFMA16(A0[r][0], B[r][1], acc[5]);
          SCHED();
        }
#pragma unroll
        for (int r = 0; r < 8; ++r) {
          acc[6] = MFMA16(A0[r][2], B[r][0], acc[6]);
          acc[7] = MFMA16(A0[r][3], B[r][0], acc[7]);
          acc[6] = MFMA16(A0[r][1], B[r][1], acc[6]);
          acc[7] = MFMA16(A0[r][2], B[r][1], acc[7]);
          SCHED();
          if (r == 0) yput(2);
          if (r == FVR) fv = ld8(rs, b2o);
          if (r == 3) {
            lf_wait(12, 4);
#pragma unroll
            for (int x = 0; x < 4; ++x) ldb(x, 6);
          }
          if (r > 3) ldb(r, 6);
          SCHED();
        }
#pragma unroll
        for (int m = 0; m < 2; ++m) acc[6 + m] = MFMA16(AD, B[0][m], acc[6 + m]);
#pragma unroll
        for (int r = 0; r < 8; ++r) {
          acc[6] = MFMA16(A0[r][0], B[r][0], acc[6]);
          acc[7] = MFMA16(A0[r][1], B[r][0], acc[7]);
          acc[7] = MFMA16(A0[r][0], B[r][1], acc[7]);
          SCHED();
        }
        yput(3);
      } else {
        u32x4 B[4][4];
        auto ldb = [&](int r) {
#pragma unroll
          for (int m = 0; m < 4; ++m) B[r & 3][m] = *(const u32x4*)(bp + 512 * (4 + m) - 64 * r);
        };
        yput(1);
        if (w == 3) wo_ld();
        __builtin_amdgcn_s_setprio(3);
        lf_wait(8, 8);
        STAMP(22);
#pragma unroll
        for (int r = 0; r < 4; ++r) ldb(r);
        SCHED();
#pragma unroll
        for (int m = 0; m < 4; ++m) acc[4 + m] = MFMA16(AD, B[0][m], acc[4 + m]);
#pragma unroll
        for (int r = 0; r < 8; ++r) {
#pragma unroll
          for (int m = 0; m < 4; ++m)
#pragma unroll
            for (int pp = 0; pp < 4 - m; ++pp) acc[4 + m + pp] = MFMA16(A0[r][pp], B[r & 3][m], acc[4 + m + pp]);
          SCHED();
          if (r + 4 < 8) ldb(r + 4);
          if (r == 1) fv = ld8(rs, b2o);
          SCHED();
        }
#pragma unroll
        for (int e = 0; e < 4; ++e)
          *(u32x2*)(U + 2048 + 512 * e + (16 * a + 4 * h) * 2) = (u32x2){pack2(acc[4 + e][0], acc[4 + e][1]), pack2(acc[4 + e][2], acc[4 + e][3])};
        lf_set(28 + ch);
      }
      STAMP(28);
      __builtin_amdgcn_s_setprio(0);
      b2_wait(fv, 0);
      lf_wait(20, 4);
      DRAIN();
    }
    __builtin_amdgcn_s_setprio(0);
  } else {
    const int at = idx - 20;
    char* const scr = lds + WBLK + 14848;
    auto gv_blk = [&](int j) { return j < 160 ? 30 + (j >> 3) : j < 400 ? (j >> 3) - 20 : j >> 3; };
    auto gv_fin = [&](const u32x4* pre, int j) {
      float acc[8];
      gemv_fin(acc, pre, 10, scr);
      gemv_put(acc, j < 160 ? WS_MODP + (unsigned)((j & 7) * 1280 + (j >> 3) * 64) * 8u : WS_MODQ + (unsigned)((j & 7) * 2560 + ((j >> 3) - 20) * 64) * 8u);
    };
    const int ja = xcd * 12 + at, ai = xcd * 8 + at, bi = xcd * 4 + at - 8, g = w >> 2, wj = w & 3;
    const int rj = w == 0 ? ja : w == 1 ? (at >= 4 ? 92 + ai : -1) : w == 4 ? 160 + ja : w == 5 && at < 8 ? 256 + ai : -1;
    char* const rst = lds + (g ? WBLK : 65536) + wj * 7424;
    u32x4 pre[11];
    if (at >= 8 || g == 0) {
      TOUCH_AT(2);
      const int T = at >= 8 ? 64 + 2 * bi + g : ai;
      unsigned* mds = (unsigned*)(lds + 2048) + 16 * g;
      char* qt = lds + L_RT + g * (16 * 1312);
      u32x4 ld[5];
      const bool e1 = at >= 8 && w == 3;
      if (e1) gemv_load(pre, gv_blk(128 + bi), bi & 7, 10);
      else __builtin_amdgcn_s_sleep(8);
#pragma unroll
      for (int j = 0; j < 5; ++j)
        ld[j] = __builtin_amdgcn_raw_buffer_load_b128(rq, (unsigned)(h4 * D_ * 2 + n16 * 16 + j * 256), (unsigned)((32 * (T >> 1) + 8 * wj + 4 * (T & 1)) * D_ * 2), NT);
      TAG_READY();
      {
        const bf2_t ONE2 = {(__bf16)1.0f, (__bf16)1.0f};
        char* dst = qt + (4 * wj + h4) * 1312 + n16 * 16;
        float sa = 0.f, va = 0.f;
#pragma unroll
        for (int j = 0; j < 5; ++j) {
          *(u32x4*)(dst + j * 256) = ld[j];
#pragma unroll
          for (int d = 0; d < 4; ++d) {
            const unsigned a0 = ld[j][d];
            const bf2_t xa = __builtin_bit_cast(bf2_t, a0);
            sa = __builtin_amdgcn_fdot2_f32_bf16(xa, ONE2, sa, false); va = __builtin_amdgcn_fdot2_f32_bf16(xa, xa, va, false);
          }
        }
        lf_set(8 + w);
        const float ma = sum16(sa) * (1.0f / D_);
        va = __builtin_fmaxf(sum16(va) * (1.0f / D_) - ma * ma, 0.f);
        if (n16 == 0) mds[4 * wj + h4] = pack2(ma, __builtin_amdgcn_sqrtf(rbf(rbf(va) + 1e-5f)));
      }
      if (e1) gv_fin(pre, 128 + bi);
      lf_wait(8 + 4 * g, 4);
      STAMP(23);
      {
        const char* src = qt + (l & 7) * 1312 + (40 * wj + (l >> 3)) * 8;
#pragma unroll
        for (int j = 0; j < 5; ++j) {
          const u32x2 x = *(const u32x2*)(src + j * 64), y = *(const u32x2*)(src + j * 64 + 8 * 1312);
          st16(rs, WS_QB + (unsigned)T * 20480u + (unsigned)(5 * wj + j) * 1024u + l16, (u32x4){x[0], x[1], y[0], y[1]});
        }
      }
      SCHED();
      const int g2 = at < 8 || w < 3 || w == 4 ? 400 : w == 3 ? 384 + bi : 256 + wj * 32 + bi;
      if (g2 < 400) {
        gemv_load(pre, gv_blk(g2), g2 & 7, 10);
        asm volatile("s_waitcnt vmcnt(10)" ::: "memory");
      } else if (w < 2 && rj >= 0) {
        rt_ld(rj, rst);
        asm volatile("s_waitcnt vmcnt(7)" ::: "memory");
      } else {
        DRAIN();
      }
      lf_set(16 + w);
      if (wj == 2) {
        lf_wait(16 + 4 * g, 4);
        st16(rs, WS_ST1 + (unsigned)(l >> 3) * 16384u + (unsigned)T * 128u + (unsigned)(l & 7) * 16u, (u32x4){mds[l & 7], tag, mds[8 + (l & 7)], tag});
        STAMP(22);
      }
      if (w >= 4 && w < 7) lf_set(w);
      if (g2 < 400) gv_fin(pre, g2);
      STAMP(25);
    } else {
      constexpr int KB = L_RT + 16 * 1312;
      TOUCH_AT(3);
      const int g1 = w < 6 ? 2 * ai + wj : 32 + wj * 64 + ai;
      if (w >= 6) {
        const rsrc_t rk = __builtin_amdgcn_make_buffer_rsrc((void*)(w == 6 ? p.k : p.v), 0, S_ * D_ * 2, 0x00020000);
        const unsigned ko = (unsigned)((1536 + 8 * ai) * (D_ * 2) - (w == 6 ? 4096 : 0));
        char* dst = lds + KB + (w == 6 ? 0 : 14336);
        __builtin_amdgcn_s_sleep(3);
#pragma unroll
        for (int n = 0; n < 10; ++n) __builtin_amdgcn_raw_ptr_buffer_load_lds(rk, (LAS void*)(dst + n * 1024), 16, l16, ko + (unsigned)n * 1024u, 0, NT);
        if (w == 6) {
#pragma unroll
          for (int n = 10; n < 14; ++n) __builtin_amdgcn_raw_ptr_buffer_load_lds(rk, (LAS void*)(dst + n * 1024), 16, l16, ko + (unsigned)n * 1024u, 0, NT);
        }
      }
      SCHED();
      u32x4 swt[2];
      swt[0] = *(const u32x4*)(p.shift_k + (tid - 256) * 8);
      swt[1] = *(const u32x4*)(p.shift_k + (256 + l) * 8);
      SCHED();
      if (w >= 6) __builtin_amdgcn_s_sleep(8);
      gemv_load(pre, gv_blk(g1), g1 & 7, 10);
      TAG_READY();
      asm volatile("s_waitcnt vmcnt(11)" : "+v"(swt[0]), "+v"(swt[1]) :: "memory");
      lf_set(8 + w);
      if (w < 6) gv_fin(pre, g1);
      STAMP(25);
      lf_wait(12, 4);
      STAMP(23);
#pragma unroll 1
      for (int n = 0; n < (w == 7 ? 2 : 1); ++n) {
        {
          const int pr = n ? 256 + l : tid - 256;
          const char* kp = lds + KB + 4096 + pr * 4;
          unsigned kk[11], vv[8];
#pragma unroll
          for (int i = 0; i < 11; ++i) kk[i] = *(const unsigned*)(kp + (i - 3) * 1280);
#pragma unroll
          for (int i = 0; i < 8; ++i) vv[i] = *(const unsigned*)(kp + 10240 + i * 1280);
          const u32x4 sw = n ? swt[1] : swt[0];
          const f32x2 wt[4] = {{bflo(sw[0]), bflo(sw[2])}, {bfhi(sw[0]), bfhi(sw[2])}, {bflo(sw[1]), bflo(sw[3])}, {bfhi(sw[1]), bfhi(sw[3])}};
          f32x2 o[8];
#pragma unroll
          for (int i = 0; i < 8; ++i) {
            f32x2 t = (f32x2){bflo(kk[i + 3]), bfhi(kk[i + 3])} * wt[0];
#pragma unroll
            for (int j = 1; j < 4; ++j) t = __builtin_elementwise_fma((f32x2){bflo(kk[i + 3 - j]), bfhi(kk[i + 3 - j])}, wt[j], t);
            o[i] = t * (f32x2){bflo(vv[i]), bfhi(vv[i])};
          }
#pragma unroll
          for (int e = 0; e < 4; ++e) {
            const int i = 7 - 4 * (e & 1);
            st16(rs, WS_KV3 + (unsigned)ai * 20480u + (unsigned)(e * 5120 + pr * 16),
                 (u32x4){pack2(o[i][e >> 1], o[i - 1][e >> 1]), tag, pack2(o[i - 2][e >> 1], o[i - 3][e >> 1]), tag});
          }
        }
      }
      STAMP(22);
      if (w < 7) lf_set(w);
      if (w >= 6) gv_fin(pre, g1);
      STAMPF(30);
    }
    if (w == 6 && ja < 80) {
      hold(AUX_T);
      gemv_load(pre, gv_blk(400 + ja), ja & 7, 10);
      gv_fin(pre, 400 + ja);
    }
    if (rj >= 0) {
      if (w >= 2) { hold(RT2_T); rt_ld(rj, rst); }
      rt_job(rj, rst);
    }
    STAMP(26);
    lf_set(w);
    if (w == 3) { hold(WA_T); wo_ld(); }
    STAMP(3);
    if (w < 4) {
      hold(W_T);
      b2_wait((u32x2){~tag, 0u}, 1);
      lf_wait(0, 8);
      STAMP(4);
    }
    DRAIN();
  }

  STAMPF(16);
  u32x4 wox[5];
  if (w == 3) {
    const char* bp = wo_st + (4 * h4 + (n16 >> 2)) * 160 + (n16 & 3) * 8;
#pragma unroll
    for (int dj = 0; dj < 5; ++dj) wox[dj] = trfrag(bp + dj * 32, 160);
    asm volatile("s_waitcnt lgkmcnt(0)" : "+v"(wox[0]), "+v"(wox[1]), "+v"(wox[2]), "+v"(wox[3]), "+v"(wox[4]) :: "memory");
  }
  auto wo_put = [&]() {
    if (w == 3 && R < 28) {
      const unsigned base = WS_WOUTF + (unsigned)c * WBLK + (unsigned)((R >> 1) * 10240 + (R & 1) * 1024);
#pragma unroll
      for (int dj = 0; dj < 5; ++dj) st16(rs, base + (unsigned)((R & 1) ? dj : (dj + 2) % 5) * 2048u + l16, wox[dj]);
    }
  };
  if (cs2) {
    cs_ld(csv, (8 + c) * 7 + R - 20);
    SCHED();
    load_b2(0, 12);
    SCHED();
    cs_fin(csv, (8 + c) * 7 + R - 20);
  } else if (w < 4) {
    asm volatile("" ::: "memory");
    load_b2(0, 12);
  }
  if (w < 4) load_b2(12, 35);
  wo_put();
  STAMPF(29);
  const unsigned xfb = WS_FX + (unsigned)(((R >> 1) * 4 + (R & 1) * 2 + (c >> 2)) * 160 + l) * 128u;
  unsigned xf3[3];
  if (w == 7) {
    lf_wait(36, 4);
    int dt = (int)(lf[44 + (l < 20 ? l : 0)] - t_start);
    dt = max(dt, __builtin_amdgcn_update_dpp(0, dt, 0xB1, 0xf, 0xf, true));
    dt = max(dt, __builtin_amdgcn_update_dpp(0, dt, 0x4E, 0xf, 0xf, true));
    dt = max(dt, __builtin_amdgcn_update_dpp(0, dt, 0x124, 0xf, 0xf, true));
    dt = max(dt, __builtin_amdgcn_update_dpp(0, dt, 0x128, 0xf, 0xf, true));
    dt = min(max(max(__builtin_amdgcn_readlane(dt, 0), __builtin_amdgcn_readlane(dt, 16)), 0), 300);
    __builtin_amdgcn_s_setprio(YPR);
    STAMPF(24);
    hold((unsigned)(YG_T + dt));
    STAMPF(30);
    xf3[0] = ld4(rs, xfb); xf3[1] = ld4(rs, xfb + 8192u); xf3[2] = ld4(rs, l < 32 ? xfb + 16384u : ~0u);
    __builtin_amdgcn_s_setprio(0);
  }
  SCHED();
  u32x4 k_c;
  unsigned kc_o = w < 2 ? (unsigned)(tid < 112 ? tid : 0) * 16u : 0x80000000u, kc_d = (unsigned)(L_CONST + (tid & 127) * 8);
  auto kc_ld = [&](int z2) { k_c = __builtin_amdgcn_raw_buffer_load_b128(rs, kc_o, WS_CS + (unsigned)(8 * z2 + c) * 2048u, SC1); };
  auto consts_put = [&](int z2) {
    if (w < 2) {
      if (__any(ne2(k_c[0], k_c[2]) != 0u)) {
        _Pragma("unroll 1") for (unsigned spins = 0; spins < SPIN_CAP; ++spins) {
          __builtin_amdgcn_s_sleep(8);
          kc_ld(z2);
          if (!__any(ne2(k_c[0], k_c[2]) != 0u)) break;
        }
      }
      if (z2) *(u32x2*)(lds + kc_d + 1024) = (u32x2){k_c[1], k_c[3]};
      else { const unsigned d = kc_d - ((kc_d >> 1) & 4u); *(unsigned*)(lds + d) = k_c[1]; *(unsigned*)(lds + d + 8) = k_c[3]; }
    }
  };
  const int wq = w ^ rotr, rt1 = wq & 3, kh1 = w >> 2;
  unsigned bo0 = (unsigned)(kh1 * 71680) + l16, bo1 = bo0 + 35840u;
  asm volatile("" : "+v"(bo0), "+v"(bo1));
  unsigned pso[3], pqo[3], oto = (unsigned)(((w ^ 4) * 7 * 64 + l) * 8), rdo = (unsigned)((w * 7 * 64 + l) * 8), cso = (unsigned)(L_CONST + (128 + n16) * 8);
#pragma unroll
  for (int e = 0; e < 3; ++e) {
    const int row = rt1 * 16 + (e < 2 ? 4 * h4 + 2 * kh1 + e : n16);
    pso[e] = (unsigned)(L_PST + row * 4); pqo[e] = (unsigned)(L_PST + 512 + row * 16 + (row & 3) * 4);
  }
  unsigned spo = (unsigned)(L_Z + tid * 16), p6o = (unsigned)(L_PST + (wq * 16 + 4 * h4) * 4), pgo = (unsigned)(L_PST + 512 + (wq * 16 + n16) * 16);
  asm volatile("" : "+v"(spo), "+v"(p6o), "+v"(pgo));
  p6o = n16 == 12 ? p6o : spo;
  pgo = h4 == (n16 >> 2) ? pgo : spo;
  unsigned bsrc = (unsigned)(((l >> 1) & 3) * 64 + (l & 1) * 4), ppo = l < 8 ? l16 : 0x80000000u;
  asm volatile("" : "+v"(pso[0]), "+v"(pso[2]), "+v"(pqo[0]), "+v"(pqo[2]), "+v"(oto), "+v"(rdo), "+v"(cso), "+v"(p6o), "+v"(pgo), "+v"(bsrc), "+v"(ppo), "+v"(kc_o), "+v"(kc_d));
  pso[1] = pso[0] + 4u; pqo[1] = pqo[0] + 20u;
#define BFRAG(q, j) (*(const u32x4*)(lds + ((q) < 5 ? bo0 : bo1) + ((q) % 5) * 7168 + (j) * 1024))
  u32x4 h2[10];
  unsigned xv = (unsigned)((4 * ((l >> 3) & 1) + h4) * 128 + (l & 7) * 16), xs0 = WS_X1B + (unsigned)(R * 4 + rt1) * 20480u + (unsigned)(10 * kh1) * 1024u;
  unsigned wvo = wsp_vo, bso = (unsigned)tau(rt1, n16) * 2u, fgo = l < 32 ? (unsigned)(l >> 3) * F4S + (unsigned)(l & 7) * 16u : 0x80000000u;
  asm volatile("" : "+v"(xv), "+v"(wvo), "+v"(bso), "+v"(fgo));
#define WO3 (-92)
#define PINS(x) do { x = (unsigned)__builtin_amdgcn_readfirstlane((int)x); asm volatile("" : "+s"(x)); } while (0)
  PINS(xs0);
  unsigned zs[8], wx8, wx4, wgb, fzs, uo, ui, xs, wl2, os, zos, pos, s1, s3;
#define LDX(q) h2[q] = __builtin_amdgcn_raw_buffer_load_b128(rs, xv + (unsigned)((q) & 3) * 1024u, xs0 + (unsigned)((q) >> 2) * 4096u, BULK_AUX)
  auto flag4 = [&](unsigned off) { ST16S(((u32x4){tag, tag, tag, tag}), rs, fgo, WS_F4 - WS_FWI + off, SC1); };
  unsigned bs_raw, fwo[1];
  f32x4 acc[7];
  float mu_n, rs_n;
  {
    unsigned f1o[1] = {w == 1 && l < 20 ? fwi + (unsigned)(c * 20 + l) * 128u : ~0u}, f1v;
    fwo[0] = w == 3 && l < 28 ? fwi + (WS_FWO - WS_FWI) + (unsigned)(c * 32 + l) * 128u : ~0u;
    asm volatile("" : "+v"(f1o[0]), "+v"(fwo[0]));
    if (w == 7) {
      asm volatile("" : "+v"(xf3[0]), "+v"(xf3[1]), "+v"(xf3[2]));
      if (__any(ne2(xf3[0], xf3[1]) != 0u) || __builtin_amdgcn_ballot_w64(xf3[2] == tag) != 0xffffffffull) {
        const unsigned fo[3] = {xf3[0] != tag ? xfb : ~0u, xf3[1] != tag ? xfb + 8192u : ~0u, l < 32 && xf3[2] != tag ? xfb + 16384u : ~0u};
        wait_lines<3, YSL>(rs, fo, tag);
      }
      if (l == 0) lf[33] = tag;
    }
    lf_wait(33, 1);
    STAMP(7);
    SCHED();
#pragma unroll
    for (int q = 0; q < 3; ++q) LDX(q);
    SCHED();
    __builtin_amdgcn_s_waitcnt(0x0F73);
    BAR();
    STAMP(8);
#pragma unroll
    for (int j = 0; j < 7; ++j) acc[j] = (f32x4){0.f, 0.f, 0.f, 0.f};
    f32x4 accg = {0.f, 0.f, 0.f, 0.f};
    u32x4 bf[7];
#pragma unroll
    for (int j = 0; j < 7; ++j) bf[j] = BFRAG(0, j);
    SCHED();
#pragma unroll
    for (int q = 3; q < 6; ++q) LDX(q);
    if (w == 3) flag4(WS_FWO + (unsigned)(c * 32 + R) * 128u);
    SCHED();
#pragma unroll
    for (int t = 0; t < 8; ++t) { zs[t] = WS_Z2F + (unsigned)(c * 64 + 8 * w + 4 * ((t >> 2) ^ rotg) + ((t + rotb) & 3)) * 7168u; PINS(zs[t]); }
    wx8 = (unsigned)((w + (idx >> 2)) & 7) * 1024u; wx4 = wx8 & 3072u; wgb = WS_WOUTF + (unsigned)c * WBLK; fzs = fz + (unsigned)(c * 32 + 4 * w) * 128u;
    uo = WS_UF + (unsigned)((R * 4 + rt1) * 8 + c) * 4096u + (unsigned)kh1 * 2048u; ui = WS_UF + (unsigned)((R * 4 + rt1) * 8 + 4 * kh1) * 4096u;
    xs = WS_X1B + (unsigned)(R * 4 + rt1) * 20480u + (unsigned)(5 * c + 2 * kh1) * 512u; wl2 = (unsigned)(120 + WO3 + (w < 4 ? w + 16 : w)) * 1024u;
    os = (unsigned)(((R * 64 + tau(rt1, 0)) * D_ + 80 * c + 32 * kh1) * 2);
    zos = WS_Z2F + (unsigned)((c * 64 + 4 * (R >> 1)) * 7 + (w < 6 ? w + 1 : 0)) * 1024u + (unsigned)(R & 1) * 512u; pos = WS_PART + (unsigned)(R * 64 + wq) * 128u;
    s1 = (unsigned)((w ^ 4) * 3072 + 1024); s3 = (unsigned)(w * 3072 + 1024);
    PINS(wx8); PINS(wx4); PINS(wgb); PINS(fzs); PINS(uo); PINS(ui); PINS(xs); PINS(wl2); PINS(os); PINS(zos); PINS(pos); PINS(s1); PINS(s3);
    SCHED();
    auto xswap = [&](int q, int e) {
#pragma unroll
      for (int d = 0; d < 2; ++d) {
        if (e == 1) h2[q][2 + d] ^= (unsigned)__builtin_amdgcn_update_dpp(0, (int)h2[q][d], 0x128, 0xf, 0x3, false);
        else h2[q][d] ^= (unsigned)__builtin_amdgcn_update_dpp(0, (int)h2[q][2 + d], 0x128, 0xf, 0xC, false);
      }
      asm volatile("" : "+v"(h2[q]));
    };
#define XSW 1
#pragma unroll
    for (int q = 0; q < 10; ++q) {
      if (q < XSW) { xswap(q, 0); xswap(q, 1); xswap(q, 2); }
      if (q == 5 && w == 1 && __builtin_amdgcn_ballot_w64(f1v == tag) != 0xfffffull) wait_lines<1, 4>(rs, f1o, tag);
      if (q == 5) { __builtin_amdgcn_s_barrier(); asm volatile("" ::: "memory"); }
      SCHED();
      accg = MFMA16(h2[q], h2[q], accg);
      SCHED();
#pragma unroll
      for (int j = 0; j < 7; ++j) {
        acc[j] = MFMA16(h2[q], bf[j], acc[j]);
        if (q == 9 && j == 1) *(f32x4*)(lds + pgo) = accg;
        if (q < 9) bf[j] = BFRAG(q + 1, j);
        if (q < 9 && q + 1 >= XSW && j >= 3 && j < 6) xswap(q + 1, j - 3);
        if (q == 1 && j == 6) f1v = ld4(rs, f1o[0]);
        if (q == 2 && j == 0) bs_raw = __builtin_amdgcn_raw_buffer_load_b16(__builtin_amdgcn_make_buffer_rsrc((void*)p.b_sp, 0, S_ * 2, 0x00020000), bso, (unsigned)R * 128u, 0);
#define KCQ 4
#define WA 8
#define WB 12
#define WQ0 2
        if (WQ0 == 2 && q >= 2 && j == 2) LDW(q - 2);
        if (WQ0 == 6 && q >= 6 && (j == 2 || j == 4)) LDW(2 * (q - 6) + (j >> 2));
        if (WQ0 == 3 && q >= 2 && q < 5 && (j & 1) && 3 * (q - 2) + (j >> 1) < WA) LDW(3 * (q - 2) + (j >> 1));
        if (WQ0 == 4 && q >= 2 && !(q & 1) && j == 2) LDW((q - 2) >> 1);
        if ((j == 0 || j == 4) && 6 + 2 * q + (j >> 2) < 10) LDX(6 + 2 * q + (j >> 2));
        if (q == KCQ && j == 0) kc_ld(1);
        if (q == 2 && j == 1 && cs1) cs_ld(csv, c * 7 + R - 25);
        if (q >= 5 && j == 5) {
          const int i = (5 + q) * 7 + (w < 7 ? w : 0);
          dma1k(rs, lds + i * 1024, l16, WS_WINF + (unsigned)c * WBLK + (unsigned)i * 1024u);
          if (q == 9) { const int i2 = 28 + (w < 7 ? w : 0); dma1k(rs, lds + i2 * 1024, l16, WS_WINF + (unsigned)c * WBLK + (unsigned)i2 * 1024u); }
        }
        SCHED();
      }
    }
    STAMP(31);
  }
  unsigned sro, swo, zo, zro, po;
  auto gelu_n = [&](f32x2* x, auto N_) {
    constexpr int N = decltype(N_)::value;
    const float c0 = -2.0f * 0.7978845608028654f * 1.4426950408889634f, c1 = c0 * 0.044715f;
    f32x2 k0 = {c0, c0}, k1 = {c1, c1}, k2 = {1.0f, 1.0f};
    asm volatile("" : "+v"(k0), "+v"(k1), "+v"(k2));
    f32x2 t[N];
#define GST(e) do { _Pragma("unroll") for (int i = 0; i < N; ++i) { t[i] = (e); asm volatile("" : "+v"(t[i])); } } while (0)
    GST(x[i] * x[i]);
    GST(__builtin_elementwise_fma(t[i], k1, k0));
    GST(t[i] * x[i]);
    GST(((f32x2){__builtin_amdgcn_exp2f(t[i][0]), __builtin_amdgcn_exp2f(t[i][1])}));
    GST(t[i] + k2);
    GST(((f32x2){__builtin_amdgcn_rcpf(t[i][0]), __builtin_amdgcn_rcpf(t[i][1])}));
#pragma unroll
    for (int i = 0; i < N; ++i) { x[i] *= t[i]; asm volatile("" : "+v"(x[i])); }
  };
  {
    f32x2 mine[7];
    f32x2 csb[7];
    if (kh1) {
#pragma unroll
      for (int j = 0; j < 7; ++j) { *(f32x2*)(lds + rdo + j * 512) = (f32x2){acc[j][0], acc[j][1]}; mine[j] = (f32x2){acc[j][2], acc[j][3]}; }
      *(f32x4*)(lds + p6o) = acc[6];
    } else {
#pragma unroll
      for (int j = 0; j < 7; ++j) { *(f32x2*)(lds + rdo + j * 512) = (f32x2){acc[j][2], acc[j][3]}; mine[j] = (f32x2){acc[j][0], acc[j][1]}; }
      *(f32x4*)(lds + p6o) = acc[6];
      consts_put(1);
      if (cs1) cs_fin(csv, c * 7 + R - 25);
#pragma unroll
      for (int k = WA; k < WB; ++k) LDW(k);
    }
    BAR();
#define BY 0
    auto b1p = [&](int k) {
      const int i = 4 * k + (w & 3) + (k < 8 ? 35 : k > 8 ? 70 : (w & 3) < 3 ? 35 : 70) - (k == 17 ? (w & 2) : 0);
      dma1k(rs, lds + i * 1024, l16, WS_WINF + (unsigned)c * WBLK + (unsigned)i * 1024u);
    };
    if (kh1) {
#pragma unroll
      for (int k = 0; k < BY; ++k) b1p(k);
#pragma unroll
      for (int k = WA; k < WB; ++k) LDW(k);
    }
    __builtin_amdgcn_s_setprio(3);
    STAMPT(9);
    f32x2 oth[7];
    f32x2 pa[3], pb[3];
#pragma unroll
    for (int e = 0; e < 3; ++e) {
      pa[e] = (f32x2){*(const float*)(lds + pso[e]), *(const float*)(lds + pqo[e])};
      pb[e] = (f32x2){*(const float*)(lds + pso[e] + 256), *(const float*)(lds + pqo[e] + 1024)};
    }
#pragma unroll
    for (int j = 0; j < 7; ++j) { oth[j] = *(const f32x2*)(lds + oto + j * 512); csb[j] = *(const f32x2*)(lds + cso + 128 * j); }
    SCHED();
    float mu2[2], rs2[2];
#pragma unroll
    for (int e = 0; e < 3; ++e) {
      const float mean = (pa[e][0] + pb[e][0]) * (1.0f / D_);
      const float var = __builtin_fmaxf((pa[e][1] + pb[e][1]) * (1.0f / D_) - mean * mean, 0.f);
      const float rstd = __builtin_amdgcn_rsqf(var + 1e-5f);
      if (e < 2) { mu2[e] = mean; rs2[e] = rstd; } else { mu_n = mean; rs_n = rstd; }
    }
    f32x2 zz[7];
    const f32x2 nmu = {-mu2[0], -mu2[1]}, rsv = {rs2[0], rs2[1]};
#pragma unroll
    for (int j = 0; j < 7; ++j)
      zz[j] = __builtin_elementwise_fma(__builtin_elementwise_fma(nmu, (f32x2){csb[j][0], csb[j][0]}, mine[j] + oth[j]), rsv, (f32x2){csb[j][1], csb[j][1]});
    gelu_n(zz, IC<7>());
    if (n16 >= 12) zz[6] = (f32x2){0.f, 0.f};
    f32x2 s1 = ((zz[0] + zz[1]) + (zz[2] + zz[3])) + ((zz[4] + zz[5]) + zz[6]);
    f32x2 s2 = zz[0] * zz[0], s3 = zz[1] * zz[1];
#pragma unroll
    for (int j = 2; j < 7; ++j) { if (j & 1) s3 = __builtin_elementwise_fma(zz[j], zz[j], s3); else s2 = __builtin_elementwise_fma(zz[j], zz[j], s2); }
    s2 += s3;
    asm("" : "+v"(s1), "+v"(s2));
    float a0 = s1[0] + dpp_f<0xB1>(s1[0]), a1 = s1[1] + dpp_f<0xB1>(s1[1]), b0 = s2[0] + dpp_f<0xB1>(s2[0]);
    a0 = (l & 1) ? a1 : a0;
    const float b1 = s2[1] + dpp_f<0xB1>(s2[1]);
    b0 = (l & 1) ? b1 : b0;
    a0 += dpp_f<0x4E>(a0); b0 += dpp_f<0x4E>(b0);
    a0 += dpp_f<0x124>(a0); b0 += dpp_f<0x124>(b0);
    a0 += dpp_f<0x128>(a0); b0 += dpp_f<0x128>(b0);
    {
      const unsigned pa0 = __builtin_amdgcn_ds_bpermute(bsrc, __float_as_uint(a0)), pb0 = __builtin_amdgcn_ds_bpermute(bsrc, __float_as_uint(b0));
      ST16S(((u32x4){tag, pa0, tag, pb0}), rs, ppo, pos + (unsigned)c * 1024u, SC1);
    }
    SCHED();
    __builtin_amdgcn_s_setprio(0);
    STAMP(10);
#define BO 14
    if (kh1) {
#pragma unroll
      for (int k = BY + BO; k < 18; ++k) b1p(k);
    } else {
#pragma unroll
      for (int k = BY; k < BY + BO; ++k) b1p(k);
    }
#pragma unroll
    for (int k = WB; k < 16; ++k) LDW(k);
#pragma unroll
    for (int j = 0; j < 7; ++j)
      *(unsigned*)(lds + L_Z + (((rt1 >> 1) * 7 + j) * 64 + l) * 16 + (4 * (rt1 & 1) + 2 * kh1) * 2) = pack2(zz[j][0], zz[j][1]);
    SCHED();
#define TR0 2
#define TR1 TR0
#define TR2 TR1
#define TR3 5
#pragma unroll
    for (int g = 0; g < TR0; ++g) WSP_TR(g);
    sro = (unsigned)(L_ST2 + h4 * 16);
    swo = (unsigned)(L_ST2 + (16 * rt1 + 4 * ((l >> 1) & 3) + 2 * kh1 + (l & 1)) * 4);
    zo = w < 7 ? (unsigned)(h4 * 7168 + n16 * 16) : 0x80000000u;
    zro = (unsigned)(L_Z + (w < 7 ? w : 0) * 1024) + l16;
    po = (unsigned)((l >> 3) * 1024 + (l & 7) * 16);
    asm volatile("" : "+v"(sro), "+v"(swo), "+v"(zo), "+v"(zro), "+v"(po));
    asm volatile("s_waitcnt vmcnt(%0)" :: "n"(16 - WB) : "memory");
    BAR();
    STAMPT(11);
  }
  unsigned z1d[7];
  constexpr int L_RED = 28672;
#define WOA(i) (((i) < 28 ? (i) : (i) < 108 ? (i) + 32 : (i) < 120 ? (i) + 37 : (i) - 92) * 1024)
#define RBAR() do { __builtin_amdgcn_s_barrier(); asm volatile("" ::: "memory"); } while (0)
#define ZAUX 16
#define NW 14
#define WOD(i0, x) dma1k(rs, lds + WOA(i0) + (x), l16, wgb + (unsigned)(i0) * 1024u + (x))
  auto wo_dma = [&](int k) {};
  u32x4 zw[NW];
#define LDZ(f) do { if (((f) >> 3) == 4) { zs[(f) & 7] += 4096u; asm volatile("" : "+s"(zs[(f) & 7])); } \
    zw[(f) % NW] = __builtin_amdgcn_raw_buffer_load_b128(rs, l16 + (unsigned)(((f) >> 3) & 3) * 1024u, zs[(f) & 7], ZAUX); } while (0)
#define WD 24
#define WE 32
#define QS 5
#define PQ 3
#define NW0 NW
#define VMW(n) asm volatile("s_waitcnt vmcnt(%0)" ::"n"(n) : "memory")
#define FQ 10
#define PP 1
#define WZ 2
#define WY 8
  static_assert(QS >= 4 && QS <= 6 && PQ >= 2 && PQ < QS && (WZ < 4 || QS >= 5), "");
  {
    f32x4 acc[7];
#pragma unroll
    for (int j = 0; j < 7; ++j) acc[j] = (f32x4){0.f, 0.f, 0.f, 0.f};
    u32x4 bf[7];
#pragma unroll
    for (int j = 0; j < 7; ++j) bf[j] = BFRAG(4, j);
    u32x4 pv, zf[2];
    unsigned fwv;
    auto ld_pv = [&]() { pv = __builtin_amdgcn_raw_buffer_load_b128(rs, po, pos, SC1); };
    auto z1 = [&](auto Q0_, auto Q1_) __attribute__((always_inline)) {
#pragma unroll
      for (int qi = decltype(Q0_)::value; qi < decltype(Q1_)::value; ++qi) {
        const int q = (qi + 4) % 10, qn = (qi + 5) % 10;
        if (qi == FQ) {
          if (kh1) VMW(WE - WD + WZ); else VMW(0);
          RBAR();
          if (w == 3) flag4(WS_FZ + (unsigned)(c * 32 + R) * 128u);
        }
        SCHED();
#pragma unroll
        for (int j = 0; j < 7; ++j) {
          acc[j] = MFMA16(bf[j], h2[q], acc[j]);
          if (qi < 9 && qi != QS - 1) bf[j] = BFRAG(qn, j);
          if (qi < 2 && (j == 1 || j == 4)) { const int i = (2 * qi + (j >> 2)) * 7 + (w < 7 ? w : 0); dma1k(rs, lds + i * 1024, l16, WS_WINF + (unsigned)c * WBLK + (unsigned)i * 1024u); }
          if (qi == 2 && j == 0) kc_ld(0);
          if (qi == 2 && j == 3) fwv = ld4(rs, fwo[0]);
          if (qi == PQ && j == 1) ld_pv();
          if (qi == QS - 1 && j < 2) zf[j] = *(const u32x4*)(lds + zro + j * 7168);
          if (!PP && qi >= QS && (j & 1) && WD + 3 * (qi - QS) + (j >> 1) < WE) LDW((WD + 3 * (qi - QS) + (j >> 1)) & 31);
          SCHED();
        }
      }
    };
    auto req1 = [&]() {
#pragma unroll
      for (int k = 16; k < WD; ++k) { LDW(k); SCHED(); }
    };
    auto req2 = [&]() {
#pragma unroll
      for (int k = WD; k < (PP ? WE : WD); ++k) { LDW(k); SCHED(); }
      sfor<0, WZ>([&](auto G_) __attribute__((always_inline)) { WOD(68 + 8 * decltype(G_)::value, wx8); SCHED(); });
    };
    if (kh1) req1();
    z1(IC<0>(), IC<QS>());
    STAMPT(29);
    f32x2 zv[2][4];
#pragma unroll
    for (int i = 0; i < 8; ++i) { zv[i >> 2][i & 3] = (f32x2){bflo(zf[i >> 2][i & 3]), bfhi(zf[i >> 2][i & 3])}; asm volatile("" : "+v"(zv[i >> 2][i & 3])); }
#pragma unroll
    for (int g = TR0; g < TR1; ++g) WSP_TR(g);
    __builtin_amdgcn_s_setprio(3);
    if (__any(ne2(pv[0], pv[2]) != 0u)) {
      _Pragma("unroll 1") for (unsigned spins = 0; spins < SPIN_CAP; ++spins) {
        ld_pv();
        if (!__any(ne2(pv[0], pv[2]) != 0u)) break;
        __builtin_amdgcn_s_sleep(1);
      }
    }
    {
      float t1 = __uint_as_float(pv[1]), t2 = __uint_as_float(pv[3]);
      t1 += dpp_f<0x128>(t1); t2 += dpp_f<0x128>(t2);
      auto x = __builtin_amdgcn_permlane16_swap(__float_as_uint(t1), __float_as_uint(t2), false, false);
      const unsigned y = __float_as_uint(__uint_as_float(x[0]) + __uint_as_float(x[1]));
      auto z = __builtin_amdgcn_permlane32_swap(y, y, false, false);
      const unsigned u = __float_as_uint(__uint_as_float(z[0]) + __uint_as_float(z[1]));
      auto v = __builtin_amdgcn_permlane16_swap(u, u, false, false);
      const float mean = __uint_as_float(v[0]) * (1.0f / FH);
      const float var = __builtin_fmaxf(__uint_as_float(v[1]) * (1.0f / FH) - mean * mean, 0.f);
      const float rstd = __builtin_amdgcn_rsqf(var + 1e-5f);
      *(float*)(lds + swo) = rstd;
      *(float*)(lds + swo + 256) = -mean * rstd;
    }
    SCHED();
    consts_put(0);
    if (w == 3 && __builtin_amdgcn_ballot_w64(fwv == tag) != 0xfffffffull) wait_lines<1, 1>(rs, fwo, tag);
    if (!kh1) req1();
    BAR();
    STAMP(12);
    {
      f32x4 sa[2][2], sb[2][2];
#pragma unroll
      for (int i = 0; i < 4; ++i) { sa[i >> 1][i & 1] = *(const f32x4*)(lds + sro + i * 64); sb[i >> 1][i & 1] = *(const f32x4*)(lds + sro + i * 64 + 256); }
      SCHED();
#pragma unroll
      for (int m = 0; m < 2; ++m) {
        u32x4 o;
#pragma unroll
        for (int e = 0; e < 4; ++e) {
          const f32x4 a = sa[m][e >> 1], b = sb[m][e >> 1];
          const f32x2 x = __builtin_elementwise_fma(zv[m][e], (f32x2){a[2 * (e & 1)], a[2 * (e & 1) + 1]}, (f32x2){b[2 * (e & 1)], b[2 * (e & 1) + 1]});
          o[e] = pack2(x[0], x[1]);
        }
        ST16S(o, rs, zo + (unsigned)m * 256u, zos, SC1);
      }
    }
    SCHED();
    __builtin_amdgcn_s_setprio(0);
    STAMPT(13);
#pragma unroll
    for (int j = 0; j < 7; ++j) bf[j] = BFRAG((QS + 4) % 10, j);
    if (kh1) req2();
    z1(IC<QS>(), IC<10>());
    f32x2 mine[7];
    if (kh1) {
#pragma unroll
      for (int j = 0; j < 7; ++j) { *(f32x2*)(lds + rdo + L_RED + j * 512) = (f32x2){acc[j][0], acc[j][1]}; mine[j] = (f32x2){acc[j][2], acc[j][3]}; }
    } else {
#pragma unroll
      for (int j = 0; j < 7; ++j) { *(f32x2*)(lds + rdo + L_RED + j * 512) = (f32x2){acc[j][2], acc[j][3]}; mine[j] = (f32x2){acc[j][0], acc[j][1]}; }
    }
    if (!kh1) req2();
#pragma unroll
    for (int g = TR1; g < TR2; ++g) WSP_TR(g);
    if (FQ > 9) VMW(WE - WD + WZ);
    BAR();
    if (FQ > 9 && w == 3) flag4(WS_FZ + (unsigned)(c * 32 + R) * 128u);
    STAMP(6);
    auto req = [&](auto G0_, auto G1_) __attribute__((always_inline)) {
#pragma unroll
      for (int k = WE; k < 32; ++k) LDW(k);
      SCHED();
      sfor<decltype(G0_)::value, decltype(G1_)::value>([&](auto G_) __attribute__((always_inline)) {
        constexpr int i0 = 4 * decltype(G_)::value;
        WOD(i0 < 68 ? i0 : i0 + 8 * WZ, wx4); SCHED();
      });
    };
    if (kh1) req(IC<0>(), IC<WY>());
    u32x4 gv[8];
    auto gb_ld = [&]() {
#pragma unroll
      for (int k = 0; k < 8; ++k) gv[k] = __builtin_amdgcn_raw_buffer_load_b128(rs, l < 40 ? l16 : 0u, WS_MODQ + (unsigned)(k * 2560 + 1920 + 80 * c) * 8u, SC1);
    };
    if (w == 2) gb_ld();
    f32x2 z[7];
    {
      f32x2 oth[7];
      f32x4 cc[7];
#pragma unroll
      for (int j = 0; j < 7; ++j) {
        oth[j] = *(const f32x2*)(lds + oto + L_RED + j * 512);
        cc[j] = *(const f32x4*)(lds + L_CONST + (16 * j + 4 * h4 + 2 * kh1) * 8);
      }
      SCHED();
#pragma unroll
      for (int j = 0; j < 7; ++j)
        z[j] = __builtin_elementwise_fma(__builtin_elementwise_fma((f32x2){-mu_n, -mu_n}, (f32x2){cc[j][0], cc[j][1]}, oth[j] + mine[j]),
                                         (f32x2){rs_n, rs_n}, (f32x2){cc[j][2], cc[j][3]});
    }
    gelu_n(z, IC<7>());
#pragma unroll
    for (int j = 0; j < 7; ++j) z1d[j] = pack2(z[j][0], z[j][1]);
    asm volatile("" : "+v"(z1d[0]), "+v"(z1d[1]), "+v"(z1d[2]), "+v"(z1d[3]), "+v"(z1d[4]), "+v"(z1d[5]), "+v"(z1d[6]));
    SCHED();
    STAMPT(30);
    if (w == 2) {
      auto bad = [&]() {
        unsigned d = 0u;
#pragma unroll
        for (int k = 0; k < 8; ++k) d |= ne2(gv[k][0], gv[k][2]);
        return __any(d != 0u);
      };
      if (bad()) {
        _Pragma("unroll 1") for (unsigned spins = 0; spins < SPIN_CAP; ++spins) {
          __builtin_amdgcn_s_sleep(8);
          gb_ld();
          if (!bad()) break;
        }
      }
      f32x2 t[4];
#pragma unroll
      for (int k = 0; k < 4; ++k) t[k] = (f32x2){__uint_as_float(gv[2 * k][1]) + __uint_as_float(gv[2 * k + 1][1]), __uint_as_float(gv[2 * k][3]) + __uint_as_float(gv[2 * k + 1][3])};
      *(f32x2*)(lds + L_MISC + l * 8) = rbf2((t[0] + t[1]) + (t[2] + t[3]));
    }
    if (!kh1) req(IC<WY>(), IC<30 - 2 * WZ>());
    const unsigned fo[1] = {l < 4 ? fzs + (unsigned)l * 128u : ~0u};
#define FPY 1
#define FPO FPY
#define FSL 1
    unsigned fv;
#pragma unroll
    for (int g = TR2; g <= TR3; ++g) {
      if (FPY == FPO) { if (g == TR2 + FPY) fv = ld4(rs, fo[0]); }
      else if (g == TR2 + FPY) { if (kh1) fv = ld4(rs, fo[0]); }
      else if (g == TR2 + FPO) { if (!kh1) fv = ld4(rs, fo[0]); }
      SCHED();
      if (g < TR3) WSP_TR(g);
      SCHED();
    }
    STAMP(14);
    if (__builtin_amdgcn_ballot_w64(fv == tag) != 15ull) wait_lines<1, FSL>(rs, fo, tag);
    STAMPT(23);
#pragma unroll
    for (int f = 0; f < NW; ++f) { LDZ(f); SCHED(); }
  }

#define WO2 15
#define TRC TR3
  {
    SCHED();
#pragma unroll
    for (int g = TRC; g < 8; ++g) WSP_TR(g);
    SCHED();
    STAMP(20);
    const int rt = rt1, kh = kh1;
    const unsigned rw = (unsigned)(L_RED + w * 512);
    unsigned rr = (unsigned)(L_RED + w * 4096 + l * 4);
    asm volatile("; m0 is set here, not in front of the first store (one wait state)" : "+v"(rr) : "{m0}"(rw));
    const float bs = bflo(bs_raw);
    unsigned long long hm = 0xffff000000000000ull;
    asm volatile("" : "+s"(hm));
    unsigned l8;
    f32x4 acc2[2][4];
    float rv[8][2];
    u32x4 ud;
    u32x4 P[2][8];
    u32x2 xp[3];
    auto ld_p = [&](int pt, int i) { P[pt][i] = __builtin_amdgcn_raw_buffer_load_b128(rs, l16 + (unsigned)pt * 1024u, ui + (unsigned)i * 2048u, SC1); };
#define RED_PUT1(j, i) asm volatile("ds_write_addtid_b32 %0 offset:%2" :: "v"(acc2[(j) & 1][((i) >> 2) & 3][(i) & 3]), "{m0}"(rw), "n"((((i) >> 2) & 3) * 4096 + (((i) >> 1) & 1) * 16384 + ((i) & 1) * 256) : "memory")
#define RED_PUT(j, r) do { RED_PUT1(j, 4 * (r)); RED_PUT1(j, 4 * (r) + 1); RED_PUT1(j, 4 * (r) + 2); RED_PUT1(j, 4 * (r) + 3); } while (0)
#define RED_GET(i) do { f32x2 x_; asm volatile("ds_read2st64_b32 %0, %1 offset0:%2 offset1:%3" : "=v"(x_) : "v"(rr), "n"(2 * (i)), "n"(2 * (i) + 1) : "memory"); rv[i][0] = x_[0]; rv[i][1] = x_[1]; } while (0)
    float zz[2], gg[2];
    unsigned cs;
    auto sum_rows_u = [&](unsigned v) {
      auto a = __builtin_amdgcn_permlane16_swap(v, v, false, false);
      v = a[0] + a[1];
      auto b = __builtin_amdgcn_permlane32_swap(v, v, false, false);
      return b[0] + b[1];
    };
    auto red_fin = [&](int j, int k, bool fuse = false) {
      const int e = k & 1, i = k >> 1;
#define VADD(d, x, y) asm volatile("v_add_f32 %0, %1, %2" : "=v"(d) : "v"(x), "v"(y))
      if (i < 4) VADD(rv[2 * i][e], rv[2 * i][e], rv[2 * i + 1][e]);
      if (i == 4 || i == 5) VADD(rv[4 * (i & 1)][e], rv[4 * (i & 1)][e], rv[4 * (i & 1) + 2][e]);
      if (i == 6) VADD(rv[0][e], rv[0][e], bs);
      if (i == 7) VADD(gg[e], rv[0][e], rv[4][e]);
      if (i == (fuse ? 9 : 8)) zz[e] = e ? bfhi(z1d[(j + 6) % 7]) : bflo(z1d[(j + 6) % 7]);
      if (i == 9) zz[e] *= gg[e];
      if (i == 9 || (i == 8 && !fuse)) asm volatile("" : "+v"(zz[e]));
      if (k == 20) { ud[j & 3] = pack2(zz[0], zz[1]); asm volatile("" : "+v"(ud[j & 3])); }
      if (k == 21) {
        if (j == 0) asm volatile("v_cndmask_b32 %0, %0, 0, %1" : "+v"(ud[0]) : "s"(hm));
        if (j == 2) asm volatile("v_add3_u32 %0, %1, %2, %3" : "=v"(cs) : "v"(ud[0]), "v"(ud[1]), "v"(ud[2]));
        if (j == 3) {
          cs = tag - sum_rows_u(cs + ud[3]);
          asm volatile("v_cndmask_b32 %0, %0, %1, %2" : "+v"(ud[0]) : "v"(cs), "s"(hm));
        }
        if (j >= 4) { ud[3] = (j == 4 ? tag : ud[3]) - ud[j & 3]; asm volatile("" : "+v"(ud[3])); }
        if (j % 3 == 0 && j) ST16S(ud, rs, l16 + (unsigned)(j / 6) * 1024u, uo, SC1);
      }
    };
    struct Sch { int b1, b2, wp[16], rp[8], vp[22], lp[8], pr[32]; };
    constexpr Sch S = {18, 28, {0, 1, 2, 3, 4, 5, 6, 7, 8, 9, 10, 11, 12, 12, 15, 15}, {19, 19, 20, 22, 23, 23, 24, 24}, {29, 29, 30, 30, 31, 31, 32, 33, 35, 36, 37, 38, 39, 40, 41, 42, 43, 46, 43, 46, 48, 48},
                       {1, 1, 1, 1, 1, 1, 1, 1}, {0, 0, 2, 1, 0, 0, 0, 0, 0, 0, 2, 2, 0, 0, 0, 0, 0, 0, 0, 1, 0, 0, 0, 1, 0, 0, 2, 1, 0, 0, 0, 0}};
    constexpr Sch F = {15, 23, {0, 1, 1, 2, 3, 4, 4, 5, 6, 7, 8, 9, 10, 11, 12, 12}, {15, 16, 16, 17, 17, 18, 18, 19}, {23, 23, 24, 24, 25, 26, 26, 27, 27, 28, 28, 29, 30, 30, 31, 31, 22, 22, 32, 33, 34, 35},
                       {1, 2, 1, 1, 1, 1, 1, 1}, {0, 2, 1, 0, 0, 1, 1, 0, 0, 2, 1, 0, 0, 2, 1, 0, 0, 2, 1, 0, 0, 2, 1, 0, 0, 2, 1, 0, 0, 2, 1, 0}};
#define VM 3
#define JF 4
#define RO 3
#define XE 1
#define CE 0
#define AP0 2
#define P1B 2
#define P1S 3
    unsigned wb[3], xo;
    const unsigned wl[3] = {(unsigned)(120 + WO3 + w) * 1024u, (unsigned)(128 + WO3 + w) * 1024u, wl2}, wg = wgb - (unsigned)(WO3 * 1024);
#define NAF 32
#define NA0 16
#define NA1 NA0
    u32x4 af[NAF];
#define LDA(m) af[(m) % NAF] = *(const u32x4*)(lds + wb[WOA(2 * (m)) >> 16] + (WOA(2 * (m)) & 65535))
    unsigned ck[2];
    const unsigned tag4 = tag * 4u;
    auto p_chk = [&](int pt, int pr, int k) {
      u32x4* q = P[pt] + 4 * pr;
      if (k < 2) ck[k & 1] = q[k & 1][0] + q[k & 1][1];
      else if (k < 8) ck[k & 1] = ck[k & 1] + q[(k & 1) + 2 * ((k >> 2) & 1)][k & 2] + q[(k & 1) + 2 * ((k >> 2) & 1)][(k & 2) + 1];
      else if (k == 8) ck[0] = pt ? ck[0] + ck[1] : sum_rows_u(ck[0] + ck[1]);
      if (k < 9) asm volatile("" : "+v"(ck[k & 1]));
      if (k == 9) {
        if (__builtin_expect(__any(ck[0] != tag4), 0)) {
          _Pragma("unroll 1") for (unsigned spins = 0; spins < SPIN_CAP; ++spins) {
            unsigned d = 0u;
#pragma unroll
            for (int i = 0; i < 4; ++i) ld_p(pt, 4 * pr + i);
#pragma unroll
            for (int i = 0; i < 4; ++i) d += q[i][0] + q[i][1] + q[i][2] + q[i][3];
            if (!pt) d = sum_rows_u(d);
            if (!__any(d != tag4)) break;
            __builtin_amdgcn_s_sleep(1);
          }
        }
      }
      if (k >= 10 && !pt) { unsigned x = q[(k - 10) & 3][0]; asm volatile("v_cndmask_b32 %0, %0, 0, %1" : "+v"(x) : "s"(hm)); q[(k - 10) & 3][0] = x; }
      if (k >= 10 && k < 13 && pt) { q[(k - 10) % 3][3] = q[3][(k - 10) % 3]; asm volatile("" : "+v"(q[(k - 10) % 3])); }
    };
    sfor<0, 224>([&](auto M_) __attribute__((always_inline)) {
      constexpr int mm = decltype(M_)::value, j = mm >> 5, m = mm & 31, y = m - (32 - 4 * RO);
      constexpr int t = j == 6 && y >= 0 ? 8 - RO + (y % (2 * RO + !RO)) / 2 : m >> 2, r = j == 6 && y >= 0 ? (y & 1) + 2 * (y / (2 * RO + !RO)) : m & 3, f = 8 * j + t;
      constexpr Sch C = j < JF ? S : F, B = j <= JF ? S : F;
      if (t == 0) asm volatile("v_mfma_f32_16x16x32_bf16 %0, %1, %2, 0" : "=&v"(acc2[j & 1][r]) : "v"(zw[f % NW]), "v"(wsp[r][t]), "v"(zw[(f | (f < 8 ? 1 : VM)) % NW]));
      else asm volatile("v_mfma_f32_16x16x32_bf16 %0, %1, %2, %0" : "+v"(acc2[j & 1][r]) : "v"(zw[f % NW]), "v"(wsp[r][t]), "v"(zw[(f | (f < 8 ? 1 : VM)) % NW]));
      SCHED();
      if (C.pr[m] == 1) __builtin_amdgcn_s_setprio(1);
      if ((m == C.b1 && j > 0) || (m == C.b2 && j > 0) || mm == 30) { asm volatile("" ::: "memory"); __builtin_amdgcn_s_waitcnt(0xC07F); RBAR(); SCHED(); }
      sfor<0, 16>([&](auto I_) __attribute__((always_inline)) {
        constexpr int i = decltype(I_)::value;
        if (j > 0 && C.wp[i] == m) { RED_PUT1(j + 1, i); SCHED(); }
        if (j > 0 && i < 8 && C.rp[i & 7] == m) { RED_GET(i & 7); SCHED(); }
      });
      if (j == 5 && m == 20) {
#pragma unroll
        for (int i = 0; i < 3; ++i) { wb[i] = (unsigned)(kh * 1024 + i * 65536) + l16; asm volatile("" : "+v"(wb[i])); }
      }
      if (XE && j == 5 && m == 13) {
        asm volatile("v_lshrrev_b32 %0, 1, %1" : "=v"(l8) : "v"(l16));
        xo = (l8 & 0x180u) + (unsigned)psi((l8 >> 3) & 15) * 8u;
        asm volatile("" : "+v"(xo));
      }
      if (RO && j == 6 && m >= 28) { RED_PUT1(6, 2 * (m - 28)); RED_PUT1(6, 2 * (m - 28) + 1); SCHED(); }
      if (CE && j == 6 && m >= 19 && m < 22) sfor<0, 3>([&](auto K_) __attribute__((always_inline)) { p_chk(0, 0, 3 * (m - 19) + decltype(K_)::value); SCHED(); });
      constexpr int ap[20] = {7, 8, 9, 10, 11, 13, 13, 14, 14, 19, 20, 21, 22, 23, 24, 25, 26, 27, 28, 29};
      sfor<0, NA0>([&](auto N_) __attribute__((always_inline)) { if (j == 6 && ap[AP0 + decltype(N_)::value] == m) { LDA(decltype(N_)::value); SCHED(); } });
      sfor<0, 22>([&](auto K_) __attribute__((always_inline)) {
        constexpr int k = decltype(K_)::value;
        if (j > 1 && B.vp[k] == m + 32) { red_fin(j > 1 ? j - 2 : 0, k, B.vp[16] == B.vp[18]); SCHED(); }
        if (j > 0 && C.vp[k] == m) { red_fin(j > 0 ? j - 1 : 0, k); SCHED(); }
      });
      if (C.pr[m] == 2) __builtin_amdgcn_s_setprio(0);
      if ((m & 3) == C.lp[m >> 2]) {
        if (f > 0 && f - 1 + NW < 56) LDZ(f - 1 + NW);
        if (j == 6 && (m >> 2) >= P1S) ld_p(0, ((m >> 2) - P1S) & 7);
        if (XE && j == 5 && m >= 20) xp[((m >> 2) - 5) % 3] = __builtin_amdgcn_raw_buffer_load_b64(rs, xo + (unsigned)((m >> 2) - 5) * 512u, xs, BULK_AUX);
        SCHED();
      }
    });
    STAMP(15);
#define NM0 0
#define GM 19
#define MB 24
    f32x4 acc[5];
    f32x4 xf[3], gf[3];
    unsigned go, oo, eo, ho, tw, tr, o1, ta, tb;
    const unsigned s2 = (unsigned)(L_MISC + 128 * kh), s4 = s1 - (unsigned)kh * 1024u;
    unsigned long long om = kh ? ~0ull : 0xccccccccccccccccull;
#define PIN2(a, b) asm volatile("" : "+v"(a), "+v"(b))
    auto glue = [&](int k) {
      if (k == 0) { ta = l8 >> 5; tb = l16 & 48u; PIN2(ta, tb); }
      if (k == 1) { tr = __umul24(ta, 112u) + tb; oo = ta & 12u; PIN2(tr, oo); }
      if (k == 2) { tr += s1; oo += ta; PIN2(tr, oo); }
      if (k == 3) { oo = __umul24(oo, 1280u) + tb; ta = l8 >> 4; PIN2(oo, ta); }
      if (k == 4) { ta &= 24u; tb = l16 & 0xf0u; PIN2(ta, tb); }
      if (k == 5) { tw = __umul24(tb, 7u) + ta; go = (ta << 1) + s2; PIN2(tw, go); }
      if (k == 6) { tw += s1; asm volatile("v_cndmask_b32 %0, %1, -2.0, %2" : "=v"(o1) : "v"(oo), "s"(om)); asm volatile("" : "+v"(tw)); }
      if (k == 7) { eo = l16 + s3; ho = l16 + s4; PIN2(eo, ho); }
      if (k >= 8 && k < 14) {
        const unsigned x = xp[((k - 8) >> 1) % 3][k & 1];
        float lo = bflo(x), hi = bfhi(x);
        PIN2(lo, hi);
        xf[((k - 8) >> 1) % 3][2 * (k & 1)] = lo; xf[((k - 8) >> 1) % 3][2 * (k & 1) + 1] = hi;
      }
    };
    auto g3 = [&](auto M0_, auto M1_) __attribute__((always_inline)) {
      sfor<decltype(M0_)::value, decltype(M1_)::value>([&](auto M_) __attribute__((always_inline)) {
        constexpr int mi = decltype(M_)::value;
        constexpr int m = mi < 55 ? mi : mi < 64 ? 55 + 5 * ((mi - 55) / 3) + (mi - 55) % 3 : 58 + 5 * ((mi - 64) / 2) + (mi & 1), u2 = m / 5;
        const u32x4 fb = u2 < 8 ? P[0][u2 & 7] : P[1][u2 < 11 ? (u2 - 8) & 3 : (u2 - 7) & 7];
        if (m < 5) asm volatile("v_mfma_f32_16x16x32_bf16 %0, %1, %2, 0" : "=&v"(acc[m % 5]) : "v"(af[m % NAF]), "v"(fb));
        else asm volatile("v_mfma_f32_16x16x32_bf16 %0, %1, %2, %0" : "+v"(acc[m % 5]) : "v"(af[m % NAF]), "v"(fb));
        SCHED();
        if (mi == NM0) RBAR();
        if (mi >= NM0 && mi < NM0 + 3) dma1k(rs, lds + wl[(mi - NM0) % 3], l16, wg + wl[(mi - NM0) % 3]);
        if (mi >= 5 && mi < 19) p_chk(0, 1, mi - 5);
        if (mi >= GM && mi < GM + 14) glue(mi - GM);
        if (mi >= 47 && mi < 54) { p_chk(1, 1, 2 * (mi - 47)); SCHED(); p_chk(1, 1, 2 * (mi - 47) + 1); }
        if (!XE && mi == 3) { xo = (l8 & 0x180u) + (unsigned)psi((l8 >> 3) & 15) * 8u; asm volatile("" : "+v"(xo)); }
        if (!XE && mi >= 4 && mi < 7) xp[(mi - 4) % 3] = __builtin_amdgcn_raw_buffer_load_b64(rs, xo + (unsigned)(mi - 4) * 512u, xs, BULK_AUX);
        if (mi >= NM0 && mi < 40 && NA1 + mi - NM0 < 60) LDA(NA1 + mi - NM0);
        if (mi >= 66 && mi < 69) *(f32x4*)(lds + eo + (mi - 66) * 1024) = acc[(mi - 66) % 3];
        SCHED();
      });
    };
    __builtin_amdgcn_s_setprio(3);
#pragma unroll
    for (int k = 18; k < 22; ++k) red_fin(5, k);
    if (!XE) asm volatile("v_lshrrev_b32 %0, 1, %1" : "=v"(l8) : "v"(l16));
    if (RO) asm volatile("s_nop 2"); else { RED_PUT(6, 0); RED_PUT(6, 1); }
    RED_PUT(6, 2); RED_PUT(6, 3);
    red_fin(6, 16); red_fin(6, 17);
    SCHED();
#pragma unroll
    for (int i = 8 - P1S; i < 8 - P1S + P1B; ++i) ld_p(0, i);
    SCHED();
    BAR();
    sfor<0, 8>([&](auto I_) __attribute__((always_inline)) { RED_GET(decltype(I_)::value); });
    SCHED();
#pragma unroll
    for (int n = NA0; n < NA1; ++n) LDA(n);
#pragma unroll
    for (int i = 8 - P1S + P1B; i < 8; ++i) ld_p(0, i);
    SCHED();
    sfor<0, 20>([&](auto K_) __attribute__((always_inline)) {
      constexpr int k = decltype(K_)::value;
      if (k < 8 && !(k & 1)) asm volatile("s_waitcnt lgkmcnt(%0)" :: "n"(6 - k + NA1 - NA0) : "memory");
      red_fin(6, k < 16 ? k : k + 2);
    });
    SCHED();
    __builtin_amdgcn_s_setprio(0);
    STAMPT(16);
#pragma unroll
    for (int k = CE ? 9 : 0; k < 14; ++k) p_chk(0, 0, k);
    SCHED();
    g3(IC<0>(), IC<MB>());
    STAMPT(22);
    DRAIN();
    RBAR();
    STAMPT(27);
#define MY 12
    if (MY == 0 || !kh) {
#pragma unroll
      for (int i = 0; i < 8; ++i) { ld_p(1, i); SCHED(); }
    }
    g3(IC<MB>(), IC<MB + MY>());
    if (MY > 0 && kh) {
#pragma unroll
      for (int i = 0; i < 8; ++i) { ld_p(1, i); SCHED(); }
    }
    STAMPT(26);
    g3(IC<MB + MY>(), IC<40>());
    STAMPT(24);
#pragma unroll
    for (int m = NA1 + 40 - NM0; m < 70; ++m) LDA(m);
#pragma unroll
    for (int x = 0; x < 3; ++x) gf[x] = *(const f32x4*)(lds + go + x * 64);
    SCHED();
    const rsrc_t ro = __builtin_amdgcn_make_buffer_rsrc((void*)p.out, 0, S_ * D_ * 2, 0x00020000);
    STAMPT(17);
#pragma unroll
    for (int k = 0; k < 14; ++k) p_chk(1, 0, k);
    asm volatile("s_nop 1");
    STAMPT(25);
    g3(IC<40>(), IC<70>());
    STAMPT(18);
    BAR();
    STAMPT(28);
    f32x4 o4[3];
    u32x4 ov[2];
#pragma unroll
    for (int j = 2; j >= 0; --j) o4[j] = *(const volatile LAS f32x4*)(lds + ho + j * 1024);
    asm volatile("" : "+v"(acc[2]), "+v"(acc[3]), "+v"(acc[4]));
    SCHED();
#pragma unroll
    for (int j = 2; j >= 0; --j) {
      const f32x4 v = __builtin_elementwise_fma(gf[j], o4[j] + acc[2 + j], xf[j]);
      *(volatile LAS u32x2*)(lds + tw + j * 32) = (u32x2){pack2(v[0], v[1]), pack2(v[2], v[3])};
      if (j < 2) ov[j] = *(const volatile LAS u32x4*)(lds + tr + j * 32);
    }
    ST16S(ov[1], ro, oo + 32u, os, 0);
    ST16S(ov[0], ro, o1, os, 0);
    if (bid == 0 && w == 5 && l8 == 0u) st4(rs, WS_EPOCH, tag);
  }
  STAMP(19);
}

extern "C" __device__ __attribute__((used, noinline, visibility("hidden"))) void mega_tail() {}

extern "C" void kernel_launch(void* const* d_in, const int* in_sizes, int n_in, void* d_out, int out_size,
                              void* d_ws, size_t ws_size, hipStream_t stream) {
  Params p;
  p.q = (const u16*)d_in[0];
  p.k = (const u16*)d_in[1];
  p.v = (const u16*)d_in[2];
  p.shift_k = (const u16*)d_in[3];
  p.ssm_k = (const u16*)d_in[4];
  p.ssm_D = (const u16*)d_in[5];
  p.w_in = (const u16*)d_in[7];
  p.w_sp = (const u16*)d_in[8];
  p.b_sp = (const u16*)d_in[9];
  p.w_out = (const u16*)d_in[10];
  p.cvec = (const u16*)d_in[11];
  p.w_mod = (const u16*)d_in[12];
  p.out = (u16*)d_out;
  p.ws = (char*)d_ws;
  mega<<<256, 512, 0, stream>>>(p);
}
```

```cpp
#include <hip/hip_runtime.h>
#include <stdint.h>

typedef unsigned short u16;
typedef __attribute__((ext_vector_type(8))) short bf16x8;
typedef __attribute__((ext_vector_type(4))) short bf16x4;
typedef __attribute__((ext_vector_type(4))) float f32x4;
typedef __attribute__((ext_vector_type(2))) float f32x2;
typedef __attribute__((ext_vector_type(4))) unsigned int u32x4;
typedef __attribute__((ext_vector_type(2))) unsigned int u32x2;
#define LAS __attribute__((address_space(3)))
typedef __amdgpu_buffer_rsrc_t rsrc_t;
typedef __attribute__((ext_vector_type(4))) short s16x4;

constexpr int S_ = 2048, D_ = 640, F_ = 1728, FH = 864, CB = 108;

constexpr unsigned WS_EPOCH = 0;
constexpr unsigned WS_FWI = 4096;
constexpr unsigned WS_FZ = WS_FWI + 384 * 128;
constexpr unsigned WS_FWO = WS_FZ + 256 * 128;
constexpr unsigned F4S = 896 * 128;
constexpr unsigned WS_CS = WS_FZ + 256 * 128;
constexpr unsigned WS_MOD = 131072;
constexpr unsigned WS_MODP = WS_MOD + 81920;
constexpr unsigned WS_ST1 = 393216;
constexpr unsigned WS_PART = 524288;
constexpr unsigned WS_CSP = WS_PART + 262144;
constexpr unsigned WS_FX = WS_CSP + 655360;
constexpr unsigned WS_X1B = WS_FX + 16 * 4 * 160 * 128;
constexpr unsigned WS_QB = WS_X1B + 2621440;
constexpr unsigned WS_KV3 = WS_QB + 2621440;
constexpr unsigned WS_MODQ = WS_KV3 + 1310720;
constexpr unsigned WBLK = 143360;
constexpr unsigned WS_WINF = WS_MODQ + 245760;
constexpr unsigned WS_WOUTF = WS_WINF + 16 * WBLK;
constexpr unsigned WS_Z2F = WS_WOUTF + 8 * WBLK;
constexpr unsigned WS_UF = WS_Z2F + 8 * 64 * 7168;
constexpr unsigned WS_F4 = WS_UF + 32 * 4 * 8 * 4096;
constexpr unsigned WS_END = WS_F4 + 4 * F4S;
constexpr unsigned WS_STAMP = 200u << 20;

constexpr int G_COPY = 4160;
constexpr int L_U = WBLK;
constexpr int U_CH = (256 + S_) * 2 + 32;
constexpr int L_FLG = L_U + 4 * U_CH;
constexpr int L_RT = 16384;
constexpr int L_CONST = WBLK;
constexpr int L_PST = L_CONST + 2048;
constexpr int L_Z = L_CONST + 5120;
constexpr int L_ST2 = L_Z + 14336;
constexpr int L_MISC = L_ST2 + 512;
constexpr int LDS_TOTAL = 163840;

#define AUX_T 610
#define RT2_T 360
#define CB_T 380
#define W_T 700
#define YG_T 920
#define BG 2
#define BK 12
#define YW 4
#define YPR 0
#define YSL 2
#define K_T 460
#ifndef M_T
#define M_T 520
#endif
#define WA_T 580
constexpr int SC1 = 16, NT = 2;
constexpr unsigned SPIN_CAP = 1u << 16;

__device__ __forceinline__ float bflo(unsigned x) { return __uint_as_float(x << 16); }
__device__ __forceinline__ float bfhi(unsigned x) { return __uint_as_float(x & 0xffff0000u); }
__device__ __forceinline__ unsigned f2bf(float x) {
  __bf16 b = (__bf16)x;
  return (unsigned)__builtin_bit_cast(unsigned short, b);
}
__device__ __forceinline__ float rbf(float x) { return bflo(f2bf(x)); }
typedef __attribute__((ext_vector_type(2))) __bf16 bf2_t;
__device__ __forceinline__ unsigned pack2(float lo, float hi) {
  f32x2 v = {lo, hi};
  return __builtin_bit_cast(unsigned, __builtin_convertvector(v, bf2_t));
}
__device__ __forceinline__ f32x2 rbf2(f32x2 v) {
  const unsigned p = __builtin_bit_cast(unsigned, __builtin_convertvector(v, bf2_t));
  return (f32x2){__uint_as_float(p << 16), __uint_as_float(p & 0xffff0000u)};
}
__device__ __forceinline__ f32x4 rbf4(f32x4 v) {
  const f32x2 a = rbf2((f32x2){v[0], v[1]}), b = rbf2((f32x2){v[2], v[3]});
  return (f32x4){a[0], a[1], b[0], b[1]};
}
template <int CTRL>
__device__ __forceinline__ float dpp_f(float v) {
  return __int_as_float(__builtin_amdgcn_update_dpp(0, __float_as_int(v), CTRL, 0xf, 0xf, true));
}
__device__ __forceinline__ float sum8(float v) {
  v += dpp_f<0xB1>(v);
  v += dpp_f<0x4E>(v);
  v += dpp_f<0x141>(v);
  return v;
}
__device__ __forceinline__ float sum16(float v) {
  v = sum8(v);
  v += dpp_f<0x140>(v);
  return v;
}
__device__ __forceinline__ float sum_rows(float v) {
  auto a = __builtin_amdgcn_permlane16_swap(__float_as_uint(v), __float_as_uint(v), false, false);
  v = __uint_as_float(a[0]) + __uint_as_float(a[1]);
  auto b = __builtin_amdgcn_permlane32_swap(__float_as_uint(v), __float_as_uint(v), false, false);
  return __uint_as_float(b[0]) + __uint_as_float(b[1]);
}
#define MFMA16(a, b, c) __builtin_amdgcn_mfma_f32_16x16x32_bf16(__builtin_bit_cast(bf16x8, a), __builtin_bit_cast(bf16x8, b), c, 0, 0, 0)
#define MFMA16K16(a, b, c) __builtin_amdgcn_mfma_f32_16x16x16bf16_1k(__builtin_bit_cast(bf16x4, a), __builtin_bit_cast(bf16x4, b), c, 0, 0, 0)
__device__ __forceinline__ u32x2 trd(const char* p) {
  return __builtin_bit_cast(u32x2, __builtin_amdgcn_ds_read_tr16_b64_v4i16((LAS s16x4*)p));
}
__device__ __forceinline__ u32x4 trfrag(const char* p, int pitch) {
  u32x2 a = trd(p), b = trd(p + 16 * pitch);
  return (u32x4){a[0], a[1], b[0], b[1]};
}
#define BAR() asm volatile("s_waitcnt lgkmcnt(0)\n\ts_barrier" ::: "memory")
#define DRAIN() do { __builtin_amdgcn_s_waitcnt(0x0F70); asm volatile("" ::: "memory"); } while (0)
#define SCHED() __builtin_amdgcn_sched_barrier(0)
template <int N> struct IC { static constexpr int value = N; };
template <int I, int N, class F>
__device__ __forceinline__ void sfor(F&& f) {
  if constexpr (I < N) { f(IC<I>()); sfor<I + 1, N>(f); }
}

__device__ __forceinline__ u32x4 ld16(rsrc_t r, unsigned off) { return __builtin_amdgcn_raw_buffer_load_b128(r, off, 0, SC1); }
__device__ __forceinline__ u32x2 ld8(rsrc_t r, unsigned off) { return __builtin_amdgcn_raw_buffer_load_b64(r, off, 0, SC1); }
__device__ __forceinline__ unsigned ld4(rsrc_t r, unsigned off) { return __builtin_amdgcn_raw_buffer_load_b32(r, off, 0, SC1); }
#define ST_AUX 16
__device__ __forceinline__ void st16(rsrc_t r, unsigned off, u32x4 v) { __builtin_amdgcn_raw_buffer_store_b128(v, r, off, 0, ST_AUX); }
#define ST16S(v, r, voff, soff, aux) do { const u32x4 v_ = (v); __builtin_amdgcn_raw_buffer_store_b128(v_, r, voff, soff, aux); asm volatile("s_nop 1" :: "v"(v_)); } while (0)
__device__ __forceinline__ void st8(rsrc_t r, unsigned off, u32x2 v) { __builtin_amdgcn_raw_buffer_store_b64(v, r, off, 0, SC1); }
__device__ __forceinline__ void st_flag4(rsrc_t r, unsigned off, unsigned tag, int l) {
  if (l < 32) st16(r, WS_F4 - WS_FWI + off + (unsigned)(l >> 3) * F4S + (unsigned)(l & 7) * 16u, (u32x4){tag, tag, tag, tag});
}
__device__ __forceinline__ void st4(rsrc_t r, unsigned off, unsigned v) { __builtin_amdgcn_raw_buffer_store_b32(v, r, off, 0, SC1); }
#define BULK_AUX 16
__device__ __forceinline__ u32x4 ld16p(rsrc_t r, unsigned off) { return __builtin_amdgcn_raw_buffer_load_b128(r, off, 0, BULK_AUX); }
__device__ __forceinline__ u32x2 ld8p(rsrc_t r, unsigned off) { return __builtin_amdgcn_raw_buffer_load_b64(r, off, 0, BULK_AUX); }
__device__ __forceinline__ void dma1k(rsrc_t r, char* lds_dst, unsigned l16, unsigned soff) {
  __builtin_amdgcn_raw_ptr_buffer_load_lds(r, (LAS void*)lds_dst, 16, l16, soff, 0, BULK_AUX);
}

#define STAMP(i)
#define STAMPF(i)
#define STAMPT(i) STAMP(i)

template <int N, int SLP>
__device__ __forceinline__ void wait_lines(rsrc_t r, const unsigned (&off)[N], unsigned tag) {
  bool need[N];
#pragma unroll
  for (int i = 0; i < N; ++i) need[i] = off[i] != ~0u;
  _Pragma("unroll 1") for (unsigned spins = 0; spins < SPIN_CAP; ++spins) {
    unsigned v[N];
#pragma unroll
    for (int i = 0; i < N; ++i) {
      v[i] = tag;
      if (need[i]) v[i] = ld4(r, off[i]);
    }
    bool any = false;
#pragma unroll
    for (int i = 0; i < N; ++i) { need[i] = v[i] != tag; any = any || need[i]; }
    if (!__any(any)) break;
    __builtin_amdgcn_s_sleep(SLP);
  }
  asm volatile("" ::: "memory");
}

__device__ __forceinline__ int tau(int rt, int n) { return 32 * (rt >> 1) + 8 * (n >> 2) + 4 * (rt & 1) + (n & 3); }
__device__ __forceinline__ int psi(int n) { return 2 * (n & 7) + (n >> 3); }
#define QX_CM(d, s0, s1, P) "v_cndmask_b32_dpp " d ", " s0 ", " s1 ", vcc quad_perm:" P " row_mask:0xf bank_mask:0xf\n\t"
#define QX_BODY(M, P) \
  asm("s_mov_b32 vcc_lo, " M "\n\ts_mov_b32 vcc_hi, " M "\n\t" \
      QX_CM("%0", "%12", "%8", P) "v_xor_b32 %4, %8, %0\n\t" QX_CM("%1", "%13", "%9", P) "v_xor_b32 %5, %9, %1\n\t" \
      QX_CM("%2", "%14", "%10", P) "v_xor_b32 %6, %10, %2\n\t" QX_CM("%3", "%15", "%11", P) "v_xor_b32 %7, %11, %3" \
      : "=&v"(t[0]), "=&v"(t[1]), "=&v"(t[2]), "=&v"(t[3]), "=&v"(u[0]), "=&v"(u[1]), "=&v"(u[2]), "=&v"(u[3]) \
      : "v"(a[0]), "v"(a[1]), "v"(a[2]), "v"(a[3]), "v"(b[0]), "v"(b[1]), "v"(b[2]), "v"(b[3]) : "vcc")
template <int BIT>
__device__ __forceinline__ void quad_xchg3(u32x4& a, u32x4& b) {
  unsigned t[4], u[4];
  if (BIT == 0) QX_BODY("0x55555555", "[1,0,3,2]");
  else QX_BODY("0x33333333", "[2,3,0,1]");
  a = (u32x4){t[0], t[1], t[2], t[3]};
#pragma unroll
  for (int d = 0; d < 4; ++d) b[d] ^= (unsigned)__builtin_amdgcn_update_dpp(0, (int)u[d], BIT == 0 ? 0xB1 : 0x4E, 0xf, 0xf, true);
}
__device__ __forceinline__ void quad_transpose(u32x4& x0, u32x4& x1, u32x4& x2, u32x4& x3) {
  quad_xchg3<0>(x0, x1);
  quad_xchg3<0>(x2, x3);
  quad_xchg3<1>(x0, x2);
  quad_xchg3<1>(x1, x3);
  asm volatile("" : "+v"(x0), "+v"(x1), "+v"(x2), "+v"(x3));
}

struct Params {
  const u16 *q, *k, *v, *shift_k, *ssm_k, *ssm_D, *w_in, *w_sp, *b_sp, *w_out, *cvec, *w_mod;
  u16* out;
  char* ws;
};

__global__ __launch_bounds__(512, 2) void mega(Params p) {
  __shared__ __attribute__((aligned(16))) char lds[LDS_TOTAL];
  {
    unsigned long long pc0;
    unsigned vt;
    asm volatile("v_readfirstlane_b32 vcc_lo, %3\n\ts_cmpk_lt_u32 %2, 192\n\ts_cbranch_scc1 4f\n\ts_cmpk_lt_u32 vcc_lo, 64\n\ts_cbranch_scc0 4f\n\t"
                 "s_lshr_b32 vcc_hi, %2, 3\n\ts_sub_u32 vcc_hi, vcc_hi, 24\n\ts_lshl_b32 vcc_hi, vcc_hi, 13\n\t"
                 "v_lshlrev_b32 %1, 7, %3\n\tv_add_u32 %1, vcc_hi, %1\n\ts_getpc_b64 %0\n.Lmega_here:\n\t"
                 "v_min_u32 %1, mega_tail-.Lmega_here-4, %1\n\ts_movk_i32 m0, 0x2000\n\ts_nop 1\n\tglobal_load_lds_dword %1, %0\n4:\n\t"
                 "s_cmpk_lt_u32 %2, 160\n\ts_cbranch_scc0 1f\n\ts_cmpk_lt_u32 vcc_lo, 256\n\ts_cbranch_scc1 3f\n\t"
                 "s_call_b64 vcc, .Ltouch1\n\ts_branch 3f\n1:\n\t"
                 "s_cmpk_lt_u32 vcc_lo, 256\n\ts_cbranch_scc1 2f\n\ts_cmpk_lt_u32 %2, 224\n\ts_cbranch_scc0 2f\n\ts_call_b64 vcc, .Ltouch3\n\ts_branch 3f\n"
                 "2:\n\ts_call_b64 vcc, .Ltouch2\n3:"
                 : "=&s"(pc0), "=&v"(vt) : "s"(blockIdx.x), "v"(threadIdx.x) : "vcc", "scc", "memory");
  }
  SCHED();
  const int tid = threadIdx.x, l = tid & 63;
  const int w = __builtin_amdgcn_readfirstlane(tid >> 6);
  const int bid = blockIdx.x, xcd = bid & 7, idx = bid >> 3;
#define TOUCH_AT(n) asm volatile("s_branch 1f\n.Ltouch" #n ":\n\ts_setpc_b64 vcc\n1:")
  char* const ws = p.ws;
  asm volatile("" ::"s"(p.q), "s"(p.k), "s"(p.v), "s"(p.shift_k), "s"(p.ssm_k), "s"(p.ssm_D), "s"(p.w_in), "s"(p.w_sp), "s"(p.b_sp),
               "s"(p.w_out), "s"(p.cvec), "s"(p.w_mod), "s"(p.out), "s"(p.ws));
  unsigned epoch, tag, t_start;
  unsigned long long t64;
  asm volatile("s_load_dword %0, %2, 0x0\n\ts_memrealtime %1" : "=&s"(epoch), "=&s"(t64) : "s"(ws));
#define TAG_READY() do { SCHED(); \
    asm volatile("s_waitcnt lgkmcnt(0)\n\ts_mul_i32 %0, %2, 0x9e3779b1\n\ts_add_i32 %0, %0, 0x9e3779b9\n\ts_mov_b64 %1, %3" \
                 : "=&s"(tag), "=&s"(t64r) : "s"(epoch), "s"(t64) : "scc"); t_start = (unsigned)t64r; SCHED(); } while (0)
  unsigned long long t64r;
  const rsrc_t rs = __builtin_amdgcn_make_buffer_rsrc(ws, 0, WS_END, 0x00020000);
  const unsigned l16 = (unsigned)l * 16u;
  STAMP(0);
  const bool is_conv = idx < 20;
  const int quad = xcd * 20 + idx;
  const int R = is_conv ? (xcd & 3) * 5 + (idx >> 2) : (xcd & 3) * 3 + 15 + (idx >> 2);
  const int c = (xcd >> 2) * 4 + (idx & 3);
  const unsigned fwi = WS_F4 + (unsigned)(xcd & 3) * F4S, fz = fwi + (WS_FZ - WS_FWI);
  const int n16 = l & 15, h4 = l >> 4;
  u32x4 wsp[4][8];
#define ROTC 2
#define WAUX 0
  const int rotg = ((idx >> 4) ^ ((ROTC & 2) ? idx >> 1 : 0)) & 1, rotb = (idx >> 2) & 3, rotr = (ROTC & 1) ? (idx & 1) * 2 : 0;
  const rsrc_t rsp = __builtin_amdgcn_make_buffer_rsrc((void*)p.w_sp, 0, S_ * S_ * 2, 0x00020000);
  const unsigned wsp_vo = (unsigned)(((l >> 2) & 3) * 8 * 4096 + h4 * 64 + ((l + rotb) & 3) * 16);
#define LDW(k) LDW_(k)
#define WSP_TR(g) WSP_TR_(g)
#define WROW R
#define LDW_(k) wsp[((k) >> 2) & 3][((k) >> 4) * 4 + ((k) & 3)] = __builtin_amdgcn_raw_buffer_load_b128(rsp, wvo, \
    (unsigned)((WROW * 64 + 16 * ((((k) >> 2) & 2) ^ rotr) + 4 * (((k) >> 2) & 1) + ((k) & 3)) * 4096 + w * 512 + ((((k) >> 4) ^ rotg) * 256)), WAUX)
#define WSP_TR_(g) quad_transpose(wsp[(g) & 3][((g) >> 2) * 4], wsp[(g) & 3][((g) >> 2) * 4 + 1], wsp[(g) & 3][((g) >> 2) * 4 + 2], wsp[(g) & 3][((g) >> 2) * 4 + 3])
  const rsrc_t rwi = __builtin_amdgcn_make_buffer_rsrc((void*)p.w_in, 0, D_ * F_ * 2, 0x00020000);
  const rsrc_t rwo = __builtin_amdgcn_make_buffer_rsrc((void*)p.w_out, 0, FH * D_ * 2, 0x00020000);
  const rsrc_t rq = __builtin_amdgcn_make_buffer_rsrc((void*)p.q, 0, S_ * D_ * 2, 0x00020000);
  auto hold = [&](unsigned tt) { while ((unsigned)__builtin_amdgcn_s_memrealtime() - t_start < tt) __builtin_amdgcn_s_sleep(2); };
  auto ne2 = [&](unsigned x, unsigned y) { unsigned d; asm("v_bitop3_b32 %0, %1, %2, %3 bitop3:0x7e" : "=v"(d) : "v"(x), "v"(y), "s"(tag)); return d; };
  volatile LAS unsigned* lf = (volatile LAS unsigned*)(lds + L_FLG);
  auto lf_set = [&](int i) { asm volatile("s_waitcnt lgkmcnt(0)" ::: "memory"); if (l == 0) lf[i] = tag; };
  auto lf_wait = [&](int i0, int n) {
    _Pragma("unroll 1") for (unsigned spins = 0; spins < (SPIN_CAP << 4); ++spins) {
      const unsigned v = lf[i0 + (l & (n - 1))];
      if (__all(v == tag)) break;
      __builtin_amdgcn_s_sleep(1);
    }
    asm volatile("" ::: "memory");
  };
  const unsigned b2o = fwi + (unsigned)((8 + c) * 20 + 5 * (w & 3) + (l < 5 ? l : 0)) * 128u;
  auto b2_wait = [&](u32x2 fv, int slp) {
    if (!__all(fv[0] == tag)) {
      _Pragma("unroll 1") for (unsigned spins = 0; spins < SPIN_CAP; ++spins) {
        if (fv[0] != tag) fv = ld8(rs, b2o);
        if (__all(fv[0] == tag)) break;
        if (slp) __builtin_amdgcn_s_sleep(8); else __builtin_amdgcn_s_sleep(4);
      }
    }
    asm volatile("" ::: "memory");
    if (l < 5) lf[44 + 5 * w + l] = fv[1];
    if (l == 0) lf[36 + w] = tag;
  };
  auto load_b2 = [&](int k0, int k1) {
    const unsigned a0 = (unsigned)(35 * w + (idx >> 2) * 4) * 1024u, gb = WS_WINF + (unsigned)(8 + c) * WBLK;
    unsigned a = a0, g = gb + a0;
#pragma unroll
    for (int k = k0; k < k1; ++k) {
      if (k == k0 || (k & 3) == 3) {
        a = k + (idx >> 2) * 4 >= 35 ? a0 - 35840u : a0;
        g = gb + a;
        asm volatile("" : "+s"(a), "+s"(g));
      }
      dma1k(rs, lds + a + k * 1024, l16, g + (unsigned)k * 1024u);
    }
  };
  char* const wo_st = lds + 122880;
  auto wo_ld = [&]() {
    const int U = R < 28 ? R : 0, u2 = U >> 1, fi = (u2 + 1) % 3;
    const int c0 = 4 * (U & 1) + (u2 < 8 ? u2 >> 1 : u2 < 11 ? 0 : 2);
    const int b0 = u2 < 8 ? CB * c0 + 2 * (u2 & 1) + 96 : CB * (c0 + (fi >> 1)) + 48 + 2 * (fi & 1), d1 = u2 < 8 ? -96 : 16, d2 = d1 + 16;
    const int b3 = u2 < 8 ? b0 - 64 : CB * (c0 + 1) + 50 + 16 * fi;
#pragma unroll
    for (int r = 0; r < 5; ++r) {
      const int g = r * 64 + l, rho = g / 10;
      const int fb = (rho & 16) ? ((rho & 2) ? b3 : b0 + d2) : ((rho & 2) ? b0 + d1 : b0);
      __builtin_amdgcn_raw_ptr_buffer_load_lds(rwo, (LAS void*)(wo_st + r * 1024), 16, (unsigned)((fb + (rho & 13)) * (D_ * 2) + (g % 10) * 16 + c * 160), 0, 0, 0);
    }
  };
  auto rt_ld = [&](int i0, char* st) {
    const int cb = (i0 < 160 ? 8 : 0) + (i0 & 7), Q = (i0 < 160 ? i0 : i0 - 160) >> 3;
#pragma unroll
    for (int r = 0; r < 7; ++r) {
      const int g = r * 64 + l;
      __builtin_amdgcn_raw_ptr_buffer_load_lds(rwi, (LAS void*)(st + r * 1024), 16, (unsigned)((g / 14) * (F_ * 2) + (g % 14) * 16 + Q * 32 * F_ * 2 + cb * CB * 2),
                                               0, 0, NT);
    }
  };
  auto rt_job = [&](int i0, char* st) {
    const int cb = (i0 < 160 ? 8 : 0) + (i0 & 7), Q = (i0 < 160 ? i0 : i0 - 160) >> 3;
    const int col = 640 * (l >> 5) + 32 * Q + (l & 31);
    DRAIN();
    u32x4 x[7], o[7];
    f32x2 xu[7][4];
    {
      const char* bp = st + (4 * h4 + (n16 >> 2)) * 224 + (n16 & 3) * 8;
#pragma unroll
      for (int j = 0; j < 7; ++j) x[j] = trfrag(bp + j * 32, 224);
#pragma unroll
      for (int j = 0; j < 7; ++j)
#pragma unroll
        for (int e = 0; e < 4; ++e) { xu[j][e] = (f32x2){bflo(x[j][e]), bfhi(x[j][e])}; asm volatile("" : "+v"(xu[j][e])); }
    }
    unsigned mk[4][2], pv = (unsigned)col * 8u, cw = l >= 32 ? (unsigned)(7168 + (l - 32) * 4) : (unsigned)(7298 + l * 2 - 4), m12 = n16 == 12 ? ~0u : 0u;
    unsigned fgo = l < 32 ? (unsigned)(l >> 3) * F4S + (unsigned)(l & 7) * 16u : 0x80000000u;
#pragma unroll
    for (int i = 0; i < 4; ++i) {
      mk[i][0] = n16 == 4 * i ? 0x3F803F80u : 0u; mk[i][1] = n16 == 4 * i + 1 ? ~0u : 0u;
      asm volatile("" : "+v"(mk[i][0]), "+v"(mk[i][1]));
    }
    asm volatile("" : "+v"(pv), "+v"(cw), "+v"(m12), "+v"(fgo));
    unsigned ob = WS_WINF + (unsigned)cb * WBLK + (unsigned)(Q * 7) * 1024u;
    asm volatile("" : "+s"(ob));
    u32x2 pp8[8];
    hold(CB_T);
    _Pragma("unroll 1") for (unsigned spins = 0; spins < SPIN_CAP; ++spins) {
#pragma unroll
      for (int i = 0; i < 8; ++i) pp8[i] = __builtin_amdgcn_raw_buffer_load_b64(rs, pv, WS_MODP + (unsigned)i * 10240u, SC1);
      if (!__any(((ne2(pp8[0][0], pp8[1][0]) | ne2(pp8[2][0], pp8[3][0]) | ne2(pp8[4][0], pp8[5][0])) | ne2(pp8[6][0], pp8[7][0])) != 0u)) break;
      __builtin_amdgcn_s_sleep(4);
    }
    STAMPF(9);
    __builtin_amdgcn_s_setprio(3);
    {
      auto pf = [&](int i) { return __uint_as_float(pp8[i][1]); };
      const float t = rbf(((pf(0) + pf(1)) + (pf(2) + pf(3))) + ((pf(4) + pf(5)) + (pf(6) + pf(7))));
      if (l >= 32) *(float*)(st + cw) = rbf(1.0f + t);
      else *(u16*)(st + cw + 2) = (u16)(__float_as_uint(t) >> 16);
    }
    asm volatile("" ::: "memory");
    const f32x4 ca = *(const volatile LAS f32x4*)(st + 7168 + h4 * 16), cc = *(const volatile LAS f32x4*)(st + 7168 + 64 + h4 * 16);
    const u32x2 e0 = *(const volatile LAS u32x2*)(st + 7296 + h4 * 8), e1 = *(const volatile LAS u32x2*)(st + 7328 + h4 * 8);
    SCHED();
#pragma unroll
    for (int j = 0; j < 7; ++j) {
      const f32x2 y0 = xu[j][0] * (f32x2){ca[0], ca[1]}, y1 = xu[j][1] * (f32x2){ca[2], ca[3]}, y2 = xu[j][2] * (f32x2){cc[0], cc[1]}, y3 = xu[j][3] * (f32x2){cc[2], cc[3]};
      o[j] = (u32x4){pack2(y0[0], y0[1]), pack2(y1[0], y1[1]), pack2(y2[0], y2[1]), pack2(y3[0], y3[1])};
      if (j == 6) {
#pragma unroll
        for (int e = 0; e < 4; ++e) o[j][e] = (o[j][e] & ~m12) | mk[3][0];
      }
      ST16S(o[j], rs, l16 + (unsigned)(j & 3) * 1024u, ob + (unsigned)(j >> 2) * 4096u, SC1);
      SCHED();
    }
    __builtin_amdgcn_s_setprio(0);
    STAMPF(11);
    f32x4 d[2] = {{0.f, 0.f, 0.f, 0.f}, {0.f, 0.f, 0.f, 0.f}};
#pragma unroll
    for (int j = 0; j < 7; ++j) {
      const unsigned one = mk[j & 3][0], sr = mk[j & 3][1];
      d[j >> 2] = MFMA16(((u32x4){one, one, one, one}), o[j], d[j >> 2]);
      d[j >> 2] = MFMA16(((u32x4){sr & e0[0], sr & e0[1], sr & e1[0], sr & e1[1]}), x[j], d[j >> 2]);
    }
#pragma unroll
    for (int m = 0; m < 2; ++m)
      st16(rs, WS_CSP + (unsigned)((cb * 20 + Q) * 2 + m) * 1024u + l16, (u32x4){tag, __float_as_uint(d[m][0]), tag, __float_as_uint(d[m][1])});
    STAMPF(13);
    u32x4 tg4 = {tag, t_start, tag, tag};
    asm volatile("s_waitcnt vmcnt(2)" : "+v"(tg4) :: "memory");
    ST16S(tg4, rs, fgo, WS_F4 + (unsigned)(cb * 20 + Q) * 128u, SC1);
  };

  auto red8 = [&](float v) { v += dpp_f<0x128>(v); return sum_rows(v); };
  const rsrc_t rwm = __builtin_amdgcn_make_buffer_rsrc((void*)p.w_mod, 0, D_ * 3840 * 2, 0x00020000);
  auto gemv_load = [&](u32x4* pre, int blk, int sl, int np) {
    pre[10][0] = *(const unsigned*)(p.cvec + 8 * np * sl + 2 * (l < 4 * np ? l : 0));
#pragma unroll
    for (int pp = 0; pp < np; ++pp)
      pre[pp] = __builtin_amdgcn_raw_buffer_load_b128(rwm, (unsigned)((l >> 3) * 7680 + (l & 7) * 16), (unsigned)((8 * np * sl + pp * 8) * 7680 + blk * 128), NT);
  };
  auto gemv_fin = [&](float* acc, const u32x4* pre, int np, char* scr) {
    const unsigned cv2 = pre[10][0];
    unsigned* svt = (unsigned*)scr + w * 96;
#pragma unroll
    for (int e = 0; e < 2; ++e) {
      const int row = 2 * l + e;
      const float cv = e ? bfhi(cv2) : bflo(cv2);
      if (l < 4 * np) svt[(row & 7) * 12 + (row >> 3)] = __float_as_uint(rbf(cv * __builtin_amdgcn_rcpf(1.0f + __expf(-cv))));
    }
    asm volatile("s_waitcnt lgkmcnt(0)" ::: "memory");
    u32x4 sv[3];
#pragma unroll
    for (int i = 0; i < (np + 3) / 4; ++i) sv[i] = *(const u32x4*)(svt + (l >> 3) * 12 + 4 * i);
    SCHED();
#pragma unroll
    for (int e = 0; e < 8; ++e) acc[e] = 0.f;
#pragma unroll
    for (int pp = 0; pp < np; ++pp) {
      const unsigned sh = sv[pp >> 2][pp & 3], sl2 = sh >> 16;
#pragma unroll
      for (int e = 0; e < 4; ++e) {
        const unsigned x = pre[pp][e];
        acc[2 * e] = __builtin_amdgcn_fdot2_f32_bf16(__builtin_bit_cast(bf2_t, x), __builtin_bit_cast(bf2_t, sl2), acc[2 * e], false);
        acc[2 * e + 1] = __builtin_amdgcn_fdot2_f32_bf16(__builtin_bit_cast(bf2_t, x), __builtin_bit_cast(bf2_t, sh), acc[2 * e + 1], false);
      }
    }
#pragma unroll
    for (int e = 0; e < 8; ++e) acc[e] = red8(acc[e]);
  };
  auto gemv_put = [&](const float* acc, unsigned off) {
    float v = acc[0];
#pragma unroll
    for (int e = 1; e < 8; ++e) { v = (l >> 3) == e ? acc[e] : v; asm("" : "+v"(v)); }
    st8(rs, off + (unsigned)l * 8u, (u32x2){tag, (unsigned)__builtin_amdgcn_ds_bpermute(((l & 7) * 8 + (l >> 3)) * 4, (int)__float_as_uint(v))});
  };
  auto cs_ld = [&](u32x4* v, int cj) {
    const int cb = cj / 7, j = cj % 7;
#pragma unroll
    for (int k = 0; k < 5; ++k)
      v[k] = __builtin_amdgcn_raw_buffer_load_b128(rs, (unsigned)(h4 * 10240 + n16 * 16), WS_CSP + (unsigned)((cb * 40 + (j >> 2) + 2 * k) * 1024 + (j & 3) * 256), SC1);
  };
  auto cs_fin = [&](u32x4* v, int cj) {
    auto bad = [&]() { return __any(((ne2(v[0][0], v[0][2]) | ne2(v[1][0], v[1][2]) | ne2(v[2][0], v[2][2])) | ne2(v[3][0], v[3][2]) | ne2(v[4][0], v[4][2])) != 0u); };
    if (bad()) {
      _Pragma("unroll 1") for (unsigned spins = 0; spins < SPIN_CAP; ++spins) {
        __builtin_amdgcn_s_sleep(8);
        cs_ld(v, cj);
        if (!bad()) break;
      }
    }
    float t0 = 0.f, t1 = 0.f;
#pragma unroll
    for (int k = 0; k < 5; ++k) { t0 += __uint_as_float(v[k][1]); t1 += __uint_as_float(v[k][3]); }
    t0 = sum_rows(t0); t1 = sum_rows(t1);
    if (l < 16) st16(rs, WS_CS + (unsigned)((cj / 7) * 2048 + (cj % 7) * 256) + l16, (u32x4){tag, __float_as_uint(t0), tag, __float_as_uint(t1)});
  };
  const bool cs2 = w == 2 && R >= 20 && R < 27, cs1 = w == 2 && R >= 25;
  u32x4 csv[5];
  typedef const __attribute__((address_space(4))) unsigned* cptr_t;

  if (__builtin_expect(is_conv, 1)) {
    if (w >= 4) {
      TOUCH_AT(1);
      const int pw = w - 4;
      const rsrc_t ra = __builtin_amdgcn_make_buffer_rsrc((void*)((idx & 2) ? p.v : p.k), 0, S_ * D_ * 2, 0x00020000);
      const rsrc_t rb = __builtin_amdgcn_make_buffer_rsrc((void*)((idx & 2) ? p.k : p.v), 0, S_ * D_ * 2, 0x00020000);
      const rsrc_t rk = __builtin_amdgcn_make_buffer_rsrc((void*)p.k, 0, S_ * D_ * 2, 0x00020000);
      const rsrc_t rv = __builtin_amdgcn_make_buffer_rsrc((void*)p.v, 0, S_ * D_ * 2, 0x00020000);
#define NROT 2
      const unsigned vo = (unsigned)((128 * pw + 2 * l) * (D_ * 2));
      const unsigned so0 = (unsigned)((idx & 1) * (D_ * 2) + quad * 8), so1 = (unsigned)((1 - (idx & 1)) * (D_ * 2) + quad * 8);
      u32x2 g[3][4], kh;
      auto gl = [&](int s, int j0, int j1) {
        __builtin_amdgcn_s_setprio(0);
#pragma unroll
        for (int j = j0; j < j1; ++j) {
          if (s < NROT) g[s][j] = __builtin_amdgcn_raw_buffer_load_b64(j < 2 ? ra : rb, vo, ((j & 1) ? so1 : so0) + (unsigned)(s * 512 * D_ * 2), 0);
          else g[s][j] = __builtin_amdgcn_raw_buffer_load_b64(j < 2 ? rk : rv, vo + (unsigned)((j & 1) * D_ * 2), (unsigned)(quad * 8 + s * 512 * D_ * 2), 0);
          SCHED();
        }
        __builtin_amdgcn_s_setprio(3);
      };
      const cptr_t sp = (cptr_t)(p.shift_k + quad * 16);
      unsigned spv[8];
#pragma unroll
      for (int i = 0; i < 8; ++i) spv[i] = sp[i];
      __builtin_amdgcn_s_sleep(3);
      gl(0, 0, 4);
      {
        const int row = 512 * (l >> 2) + 128 * pw - 3 + (l & 3);
        kh = __builtin_amdgcn_raw_buffer_load_b64(rk, (l >= 12 || (l & 3) == 3 || row < 0) ? 0x80000000u : (unsigned)(row * D_ * 2 + quad * 8), 0, 0);
      }
      TAG_READY();
      f32x2 wt[2][4];
#pragma unroll
      for (int d = 0; d < 2; ++d) {
        wt[d][0] = (f32x2){bflo(spv[4 * d]), bflo(spv[4 * d + 2])}; wt[d][1] = (f32x2){bfhi(spv[4 * d]), bfhi(spv[4 * d + 2])};
        wt[d][2] = (f32x2){bflo(spv[4 * d + 1]), bflo(spv[4 * d + 3])}; wt[d][3] = (f32x2){bfhi(spv[4 * d + 1]), bfhi(spv[4 * d + 3])};
        asm volatile("" : "+s"(wt[d][0]), "+s"(wt[d][1]), "+s"(wt[d][2]), "+s"(wt[d][3]));
      }
      SCHED();
      {
        const int j = idx * 4 + pw;
        const unsigned o = (unsigned)((512 + 64 * (j >> 2) + l) * (D_ * 2) + (((160 * xcd) >> 7) + ((j >> 1) & 1)) * 128);
        __builtin_amdgcn_raw_ptr_buffer_load_lds((j & 1) ? ra : rb, (LAS void*)(lds + 98304), 4, j < 64 ? o : 0x80000000u, 0, 0, 0);
      }
      SCHED();
      gl(1, 0, 4);
      STAMP(21);
      unsigned up = (unsigned)(L_U + 512 + 256 * pw + (l >> 2) * 16 + (3 - (l & 3)) * 4);
      unsigned lfo = l == 0 ? (unsigned)(L_FLG + 4 * pw) : (unsigned)(100352 + tid * 4), lfr = (unsigned)(L_FLG + (l & 3) * 4);
      unsigned yr = (unsigned)(L_U + (n16 >> 2) * U_CH + (128 * pw + 32 * h4 + 8 * (n16 & 1) + 4 * ((n16 >> 1) & 1)) * 2);
      unsigned yo = (unsigned)(8 * pw + (l >> 3)) * 20480u + (unsigned)quad * 128u + (unsigned)(l & 7) * 16u;
      unsigned mo = (unsigned)((n16 < 12 ? n16 >> 2 : 0) * D_ + quad * 4 + (l & 3)) * 8u + (unsigned)h4 * 40960u;
      unsigned k3o = (unsigned)(16 * pw + (l >> 2)) * 20480u + (unsigned)(((l >> 1) & 1) * 10240 + (2 * quad + (l & 1)) * 16);
      unsigned k3d = (unsigned)(L_U + (2 * (l & 1) + ((l >> 1) & 1)) * U_CH + 512 + (1536 + 128 * pw + 8 * (l >> 2)) * 2);
      unsigned xfo = (unsigned)(((4 * (l >> 5) + pw) * 4 + ((l >> 3) & 3)) * 160 + quad) * 128u + (unsigned)(l & 7) * 16u;
      u32x4 tg4 = {tag, tag, tag, tag};
      asm volatile("" : "+v"(up), "+v"(lfo), "+v"(lfr), "+v"(yr), "+v"(yo), "+v"(mo), "+v"(k3o), "+v"(k3d), "+v"(xfo), "+v"(tg4));
      unsigned so = WS_ST1 + (unsigned)xcd * 16384u + (unsigned)pw * 1024u;
      asm volatile("" : "+s"(so));
      auto pf_set = [&](int i) { asm volatile("" ::: "memory"); *(volatile LAS unsigned*)(lds + lfo + 4 * i) = tg4[0]; };
      auto pf_wait = [&](int i0) {
        _Pragma("unroll 1") for (unsigned spins = 0; spins < (SPIN_CAP << 4); ++spins) {
          if (__all(*(volatile LAS unsigned*)(lds + lfr + 4 * i0) == tag)) break;
          __builtin_amdgcn_s_sleep(1);
        }
        asm volatile("" ::: "memory");
      };
      unsigned hl[2][3];
      auto halo = [&](int s) {
#pragma unroll
        for (int i = 0; i < 6; ++i) { hl[i & 1][i >> 1] = (unsigned)__builtin_amdgcn_readlane(kh[i & 1], 4 * s + (i >> 1)); asm volatile("" : "+v"(hl[i & 1][i >> 1])); }
      };
      auto kv = [&](auto S__, auto N__) {
        constexpr int s = decltype(S__)::value;
        u32x2* x = g[s];
        SCHED();
        asm volatile("s_waitcnt vmcnt(%5)" : "+v"(x[0]), "+v"(x[1]), "+v"(x[2]), "+v"(x[3]), "+v"(kh) : "n"(decltype(N__)::value));
        if (s == 2) STAMP(27);
        if (s == 0) halo(0);
        if (s < NROT) {
          if (idx & 1) { asm volatile(""); const u32x2 t = x[0], u = x[2]; x[0] = x[1]; x[1] = t; x[2] = x[3]; x[3] = u; }
          asm volatile("" : "+v"(x[0]), "+v"(x[1]), "+v"(x[2]), "+v"(x[3]));
          if (idx & 2) { asm volatile(""); const u32x2 t = x[0], u = x[1]; x[0] = x[2]; x[1] = x[3]; x[2] = t; x[3] = u; }
          asm volatile("" : "+v"(x[0]), "+v"(x[1]), "+v"(x[2]), "+v"(x[3]));
        }
        f32x2 kf[2][5], o[2][2];
#pragma unroll
        for (int d = 0; d < 2; ++d) {
          unsigned kk[5];
          kk[3] = x[0][d]; kk[4] = x[1][d];
          kk[2] = __builtin_amdgcn_update_dpp(hl[d][2], kk[4], 0x138, 0xf, 0xf, false);
          kk[1] = __builtin_amdgcn_update_dpp(hl[d][1], kk[3], 0x138, 0xf, 0xf, false);
          kk[0] = __builtin_amdgcn_update_dpp(hl[d][0], kk[2], 0x138, 0xf, 0xf, false);
#pragma unroll
          for (int j = 0; j < 5; ++j) kf[d][j] = (f32x2){bflo(kk[j]), bfhi(kk[j])};
        }
#pragma unroll
        for (int t = 0; t < 5; ++t) {
#pragma unroll
          for (int i = 0; i < 4; ++i) {
            const int d = i >> 1, r = i & 1;
            if (t == 4) o[d][r] *= (f32x2){bflo(x[2 + r][d]), bfhi(x[2 + r][d])};
            else if (t == 0) o[d][r] = kf[d][r + 3] * wt[d][0];
            else o[d][r] = __builtin_elementwise_fma(kf[d][r + 3 - t], wt[d][t], o[d][r]);
          }
          asm volatile("" : "+v"(o[0][0]), "+v"(o[0][1]), "+v"(o[1][0]), "+v"(o[1][1]));
        }
#pragma unroll
        for (int i = 0; i < 4; ++i) *(unsigned*)(lds + up + i * U_CH + s * 1024) = pack2(o[i >> 1][1][i & 1], o[i >> 1][0][i & 1]);
        pf_set(4 * s);
        SCHED();
        if (s < 2) halo(s + 1);
        SCHED();
      };
      u32x4 sv[4], qv[4];
      u32x2 mv[2];
      auto ld_e = [&]() {
#pragma unroll
        for (int s = 0; s < 4; ++s) sv[s] = __builtin_amdgcn_raw_buffer_load_b128(rs, l16, so + (unsigned)s * 4096u, SC1);
        SCHED();
      };
      auto s_bad = [&]() { return __any(((ne2(sv[0][1], sv[0][3]) | ne2(sv[1][1], sv[1][3]) | ne2(sv[2][1], sv[2][3])) | ne2(sv[3][1], sv[3][3])) != 0u); };
      kv(IC<0>(), IC<5>());
      STAMP(23);
#define G2A 4
      gl(2, 0, G2A);
      kv(IC<1>(), IC<G2A>());
      gl(2, G2A, 4);
      STAMPF(9);
      u32x4 k3[2];
      auto ld_k3 = [&]() {
        k3[0] = __builtin_amdgcn_raw_buffer_load_b128(rs, k3o, WS_KV3, SC1);
        k3[1] = __builtin_amdgcn_raw_buffer_load_b128(rs, k3o, WS_KV3 + 5120u, SC1);
      };
      auto ld_m = [&]() {
        mv[0] = __builtin_amdgcn_raw_buffer_load_b64(rs, mo, WS_MODQ, SC1);
        mv[1] = __builtin_amdgcn_raw_buffer_load_b64(rs, mo, WS_MODQ + 20480u, SC1);
      };
      auto ld_q = [&]() {
        asm volatile("" ::: "memory");
        ld_m();
        SCHED();
#pragma unroll
        for (int s = 0; s < 4; ++s) qv[s] = __builtin_amdgcn_raw_buffer_load_b128(rs, yo, WS_QB + (unsigned)s * (32u * 20480u), BULK_AUX);
        SCHED();
      };
      auto k_bad = [&]() { return __any((ne2(k3[0][1], k3[0][3]) | ne2(k3[1][1], k3[1][3])) != 0u); };
      auto k_put = [&]() {
        *(u32x4*)(lds + k3d) = (u32x4){k3[0][0], k3[0][2], k3[1][0], k3[1][2]};
        pf_set(12);
        SCHED();
      };
#define KE_T 0
      const bool early = (unsigned)__builtin_amdgcn_s_memrealtime() - t_start < KE_T;
      if (!early) {
        hold(K_T);
        ld_k3();
        SCHED();
        ld_e();
      } else {
        DRAIN();
      }
      kv(IC<2>(), IC<6>());
      if (KE_T && early) {
        hold(K_T);
        ld_k3();
        SCHED();
        ld_e();
      }
      STAMPF(11);
      const bool kb = k_bad();
      if (!kb) k_put();
      const bool sb = s_bad();
      if (kb) ld_k3();
      if (sb) ld_e(); else ld_q();
      if (kb) {
        _Pragma("unroll 1") for (unsigned spins = 0; spins < SPIN_CAP; ++spins) {
          if (!k_bad()) break;
          __builtin_amdgcn_s_sleep(4);
          ld_k3();
        }
        k_put();
      }
      STAMPF(18);
      if (sb) {
        _Pragma("unroll 1") for (unsigned spins = 0; spins < SPIN_CAP; ++spins) {
          if (!s_bad()) break;
          __builtin_amdgcn_s_sleep(4);
          ld_e();
        }
        ld_q();
      }
      STAMPF(17);
      STAMP(2);
      u32x2 yv[4][2];
      auto ld_y = [&](int s) {
#pragma unroll
        for (int e = 0; e < 2; ++e) yv[s][e] = trd(lds + yr + 1024 * s + 32 * e);
      };
      float r1[4][2], m1[4][2];
#pragma unroll
      for (int i = 0; i < 8; ++i) {
        const unsigned x = sv[i >> 1][2 * (i & 1)];
        r1[i >> 1][i & 1] = __builtin_amdgcn_rcpf(bfhi(x));
        m1[i >> 1][i & 1] = -bflo(x) * r1[i >> 1][i & 1];
        asm volatile("" : "+v"(r1[i >> 1][i & 1]), "+v"(m1[i >> 1][i & 1]));
      }
      pf_wait(20);
      ld_y(0); ld_y(1);
      SCHED();
      if (__any(ne2(mv[0][0], mv[1][0]) != 0u)) {
        _Pragma("unroll 1") for (unsigned spins = 0; spins < SPIN_CAP; ++spins) {
          __builtin_amdgcn_s_sleep(4);
          ld_m();
          if (!__any(ne2(mv[0][0], mv[1][0]) != 0u)) break;
        }
      }
      STAMPF(13);
      f32x4 al, be;
      {
        const float mgf = rbf(sum_rows(__uint_as_float(mv[0][1]) + __uint_as_float(mv[1][1])));
        auto rl = [&](int i) { return __uint_as_float(__builtin_amdgcn_readlane(__float_as_uint(mgf), i)); };
        const f32x4 sa = {rl(0), rl(1), rl(2), rl(3)}, ca = {rl(4), rl(5), rl(6), rl(7)}, ga = {rl(8), rl(9), rl(10), rl(11)};
        al = rbf4(ca + 1.0f) * ga;
        be = sa * ga;
        asm volatile("" : "+v"(al), "+v"(be));
      }
      f32x4 qf[2][2], P[2][2];
      auto xpre = [&](int s) {
#pragma unroll
        for (int e = 0; e < 2; ++e) {
          const float r = r1[s][e], m = m1[s][e];
          const f32x4 qq = {bflo(qv[s][2 * e]), bfhi(qv[s][2 * e]), bflo(qv[s][2 * e + 1]), bfhi(qv[s][2 * e + 1])};
          qf[s & 1][e] = qq;
          P[s & 1][e] = __builtin_elementwise_fma(__builtin_elementwise_fma(qq, (f32x4){r, r, r, r}, (f32x4){m, m, m, m}), al, be);
        }
        asm volatile("" : "+v"(qf[s & 1][0]), "+v"(qf[s & 1][1]), "+v"(P[s & 1][0]), "+v"(P[s & 1][1]));
      };
      auto xfin = [&](int s) {
        u32x4 o;
#pragma unroll
        for (int e = 0; e < 2; ++e) {
          const u32x2 y = yv[s][e];
          const f32x4 yf = {bflo(y[0]), bfhi(y[0]), bflo(y[1]), bfhi(y[1])};
          const f32x4 x = __builtin_elementwise_fma(P[s & 1][e], yf, qf[s & 1][e]);
          o[2 * e] = pack2(x[0], x[1]);
          o[2 * e + 1] = pack2(x[2], x[3]);
        }
        ST16S(o, rs, yo, WS_X1B + (unsigned)s * (32u * 20480u), SC1);
      };
      xpre(0); xfin(0);
      xpre(1); xfin(1);
      SCHED();
      xpre(2); xpre(3);
#define FVR 5
#define MSPLIT 0
      if (MSPLIT) {
        pf_wait(24);
        ld_y(2);
        xfin(2);
        SCHED();
        pf_wait(28);
        ld_y(3);
        xfin(3);
      } else {
        pf_wait(28);
        ld_y(2); ld_y(3);
        xfin(2); xfin(3);
      }
      STAMP(28);
      if (BG == 1 && l == 0) lf[40 + pw] = tag;
      asm volatile("s_waitcnt vmcnt(2)" ::: "memory");
      ST16S(tg4, rs, xfo, WS_FX, SC1);
      SCHED();
      asm volatile("s_waitcnt vmcnt(1)" ::: "memory");
      ST16S(tg4, rs, xfo, WS_FX + 32u * 160u * 128u, SC1);
      if (BG == 2 && l == 0) lf[40 + pw] = tag;
      STAMP(5);
    } else {
      const int ch = w;
      char* U = lds + L_U + ch * U_CH;
      char* G = lds + ch * (4 * G_COPY);
      {
        const rsrc_t rf = __builtin_amdgcn_make_buffer_rsrc((void*)(p.ssm_k + (size_t)(quad * 4 + ch) * S_), 0, S_ * 2, 0x00020000);
        u32x4 fa[4];
        u32x2 fb[4];
        __builtin_amdgcn_s_setprio(3);
#pragma unroll
        for (int k = 0; k < 4; ++k) {
          fa[k] = __builtin_amdgcn_raw_buffer_load_b128(rf, l16 + (unsigned)k * 1024u, 0, NT);
        }
        __builtin_amdgcn_s_setprio(1);
        TAG_READY();
#pragma unroll
        for (int k = 0; k < 4; ++k) {
#pragma unroll
          for (int d = 0; d < 2; ++d)
            fb[k][d] = __builtin_amdgcn_update_dpp(k < 3 ? (unsigned)__builtin_amdgcn_readfirstlane(fa[k < 3 ? k + 1 : k][d]) : 0u, fa[k][d], 0x130, 0xf, 0xf, false);
          const unsigned x[6] = {fa[k][0], fa[k][1], fa[k][2], fa[k][3], fb[k][0], fb[k][1]};
          unsigned y[5];
#pragma unroll
          for (int d = 0; d < 5; ++d) y[d] = __builtin_amdgcn_alignbit(x[d + 1], x[d], 16);
          char* gp = G + 32 + k * 1024 + l16;
          *(u32x4*)(gp) = fa[k];
          *(u32x4*)(gp + G_COPY) = (u32x4){y[0], y[1], y[2], y[3]};
          *(u32x4*)(gp + 2 * G_COPY) = (u32x4){x[1], x[2], x[3], x[4]};
          *(u32x4*)(gp + 3 * G_COPY) = (u32x4){y[1], y[2], y[3], y[4]};
          if (k == 0) {
            const unsigned f0 = __builtin_amdgcn_readfirstlane(x[0]), f1 = __builtin_amdgcn_readfirstlane(x[1]);
            const unsigned long long f64 = (unsigned long long)f0 | ((unsigned long long)f1 << 32);
            const unsigned long long hi = l ? (f64 << (64 - 16 * (l & 3))) : 0ull;
            if (l < 4) {
              *(u32x4*)(G + l * G_COPY) = (u32x4){0u, 0u, 0u, 0u};
              *(u32x4*)(G + l * G_COPY + 16) = (u32x4){0u, 0u, (unsigned)hi, (unsigned)(hi >> 32)};
            }
            if (l >= 32) *(u32x4*)(U + (l - 32) * 16) = (u32x4){0u, 0u, 0u, 0u};
          }
        }
        asm volatile("s_waitcnt lgkmcnt(0)" ::: "memory");
        STAMP(1);
      }
      const int a = n16, h = h4, jj = h >> 1, e2 = h & 1;
      const char* ap = G + ((a + 1) & 3) * G_COPY + 2 * (16 * jj + a - 8 * e2 + 9 - ((a + 1) & 3));
      const char* bp = U + 512 + 32 * (a - jj) + 16 * e2;
      auto lda = [&](u32x4* A, int r, int p0, int np) {
#pragma unroll
        for (int pp = 0; pp < np; ++pp) {
          u32x2 lo = *(const volatile LAS u32x2*)(ap + 512 * (p0 + pp) + 64 * r);
          u32x2 hi = *(const volatile LAS u32x2*)(ap + 512 * (p0 + pp) + 64 * r + 8);
          A[pp] = (u32x4){lo[0], lo[1], hi[0], hi[1]};
        }
      };
      u32x4 AD;
      {
        const unsigned dDr = (*(cptr_t)(p.ssm_D + quad * 4 + (ch & 2)) >> (16 * (ch & 1))) & 0xffffu;
        const unsigned dv = h == (a >> 3) ? ((a & 1) ? dDr : dDr << 16) : 0u;
        const int dw = (7 - (a & 7)) >> 1;
#pragma unroll
        for (int d = 0; d < 4; ++d) AD[d] = dw == d ? dv : 0u;
      }
      u32x4 A0[8][4];
      f32x4 acc[8];
#pragma unroll
      for (int n = 0; n < 8; ++n) acc[n] = (f32x4){0.f, 0.f, 0.f, 0.f};
#pragma unroll
      for (int r = 0; r < 8; ++r) lda(A0[r], r, 0, 4);
      auto yput = [&](int s) {
#pragma unroll
        for (int e = 0; e < 2; ++e)
          *(u32x2*)(U + 1024 * s + 512 * e + (16 * a + 4 * h) * 2) = (u32x2){pack2(acc[2 * s + e][0], acc[2 * s + e][1]), pack2(acc[2 * s + e][2], acc[2 * s + e][3])};
        asm volatile("" ::: "memory");
        if (l == 0) lf[16 + 4 * s + ch] = tag;
      };
      auto stage = [&](auto S__) {
        constexpr int s = decltype(S__)::value, NH = s < 2 ? 4 - 2 * s : 0, NP = 8 - 2 * s, NB = s < 2 ? 2 : 8;
        u32x4 A1[3][NH > 0 ? NH : 1], B[NB][2];
        auto ldb = [&](int r) {
#pragma unroll
          for (int e = 0; e < 2; ++e) B[r % NB][e] = *(const u32x4*)(bp + 512 * (2 * s + e) - 64 * r);
        };
        if (NH) { lda(A1[0], 0, 4, NH); lda(A1[1], 1, 4, NH); }
        SCHED();
        if (s > 0) yput(s - 1);
        lf_wait(4 * s, 4);
        if (s == 0) STAMP(3);
#pragma unroll
        for (int r = 0; r < NB; ++r) ldb(r);
        SCHED();
#pragma unroll
        for (int e = 0; e < 2; ++e) acc[2 * s + e] = MFMA16(AD, B[0][e], acc[2 * s + e]);
#pragma unroll
        for (int r = 0; r < 8; ++r) {
          if (NH && r + 2 < 8) lda(A1[(r + 2) % 3], r + 2, 4, NH);
          SCHED();
#pragma unroll
          for (int e = 0; e < 2; ++e)
#pragma unroll
            for (int pp = 0; pp < NP - e; ++pp) acc[2 * s + e + pp] = MFMA16(pp < 4 ? A0[r][pp] : A1[r % 3][pp < 4 ? 0 : pp - 4], B[r % NB][e], acc[2 * s + e + pp]);
          SCHED();
          if (r + NB < 8) ldb(r + NB);
        }
      };
      stage(IC<0>());
      STAMP(4);
      stage(IC<1>());
      STAMPF(9);
      u32x2 fv;
      if (MSPLIT) {
        u32x4 B[8][2];
        auto ldb = [&](int r, int m0) {
#pragma unroll
          for (int m = 0; m < 2; ++m) B[r][m] = *(const u32x4*)(bp + 512 * (m0 + m) - 64 * r);
        };
        yput(1);
        if (w == 3) wo_ld();
        __builtin_amdgcn_s_setprio(3);
        lf_wait(8, 4);
        STAMP(22);
#pragma unroll
        for (int r = 0; r < 8; ++r) ldb(r, 4);
        SCHED();
#pragma unroll
        for (int m = 0; m < 2; ++m) acc[4 + m] = MFMA16(AD, B[0][m], acc[4 + m]);
#pragma unroll
        for (int r = 0; r < 8; ++r) {
          acc[4] = MFMA16(A0[r][0], B[r][0], acc[4]);
          acc[5] = MFMA16(A0[r][1], B[r][0], acc[5]);
          acc[5] = MFMA16(A0[r][0], B[r][1], acc[5]);
          SCHED();
        }
#pragma unroll
        for (int r = 0; r < 8; ++r) {
          acc[6] = MFMA16(A0[r][2], B[r][0], acc[6]);
          acc[7] = MFMA16(A0[r][3], B[r][0], acc[7]);
          acc[6] = MFMA16(A0[r][1], B[r][1], acc[6]);
          acc[7] = MFMA16(A0[r][2], B[r][1], acc[7]);
          SCHED();
          if (r == 0) yput(2);
          if (r == FVR) fv = ld8(rs, b2o);
          if (r == 3) {
            lf_wait(12, 4);
#pragma unroll
            for (int x = 0; x < 4; ++x) ldb(x, 6);
          }
          if (r > 3) ldb(r, 6);
          SCHED();
        }
#pragma unroll
        for (int m = 0; m < 2; ++m) acc[6 + m] = MFMA16(AD, B[0][m], acc[6 + m]);
#pragma unroll
        for (int r = 0; r < 8; ++r) {
          acc[6] = MFMA16(A0[r][0], B[r][0], acc[6]);
          acc[7] = MFMA16(A0[r][1], B[r][0], acc[7]);
          acc[7] = MFMA16(A0[r][0], B[r][1], acc[7]);
          SCHED();
        }
        yput(3);
      } else {
        u32x4 B[4][4];
        auto ldb = [&](int r) {
#pragma unroll
          for (int m = 0; m < 4; ++m) B[r & 3][m] = *(const u32x4*)(bp + 512 * (4 + m) - 64 * r);
        };
        yput(1);
        if (w == 3) wo_ld();
        __builtin_amdgcn_s_setprio(3);
        lf_wait(8, 8);
        STAMP(22);
#pragma unroll
        for (int r = 0; r < 4; ++r) ldb(r);
        SCHED();
#pragma unroll
        for (int m = 0; m < 4; ++m) acc[4 + m] = MFMA16(AD, B[0][m], acc[4 + m]);
#pragma unroll
        for (int r = 0; r < 8; ++r) {
#pragma unroll
          for (int m = 0; m < 4; ++m)
#pragma unroll
            for (int pp = 0; pp < 4 - m; ++pp) acc[4 + m + pp] = MFMA16(A0[r][pp], B[r & 3][m], acc[4 + m + pp]);
          SCHED();
          if (r + 4 < 8) ldb(r + 4);
          if (r == 1) fv = ld8(rs, b2o);
          SCHED();
        }
#pragma unroll
        for (int e = 0; e < 4; ++e)
          *(u32x2*)(U + 2048 + 512 * e + (16 * a + 4 * h) * 2) = (u32x2){pack2(acc[4 + e][0], acc[4 + e][1]), pack2(acc[4 + e][2], acc[4 + e][3])};
        lf_set(28 + ch);
      }
      STAMP(28);
      __builtin_amdgcn_s_setprio(0);
      b2_wait(fv, 0);
      lf_wait(20, 4);
      DRAIN();
    }
    __builtin_amdgcn_s_setprio(0);
  } else {
    const int at = idx - 20;
    char* const scr = lds + WBLK + 14848;
    auto gv_blk = [&](int j) { return j < 160 ? 30 + (j >> 3) : j < 400 ? (j >> 3) - 20 : j >> 3; };
    auto gv_fin = [&](const u32x4* pre, int j) {
      float acc[8];
      gemv_fin(acc, pre, 10, scr);
      gemv_put(acc, j < 160 ? WS_MODP + (unsigned)((j & 7) * 1280 + (j >> 3) * 64) * 8u : WS_MODQ + (unsigned)((j & 7) * 2560 + ((j >> 3) - 20) * 64) * 8u);
    };
    const int ja = xcd * 12 + at, ai = xcd * 8 + at, bi = xcd * 4 + at - 8, g = w >> 2, wj = w & 3;
    const int rj = w == 0 ? ja : w == 1 ? (at >= 4 ? 92 + ai : -1) : w == 4 ? 160 + ja : w == 5 && at < 8 ? 256 + ai : -1;
    char* const rst = lds + (g ? WBLK : 65536) + wj * 7424;
    u32x4 pre[11];
    if (at >= 8 || g == 0) {
      TOUCH_AT(2);
      const int T = at >= 8 ? 64 + 2 * bi + g : ai;
      unsigned* mds = (unsigned*)(lds + 2048) + 16 * g;
      char* qt = lds + L_RT + g * (16 * 1312);
      u32x4 ld[5];
      const bool e1 = at >= 8 && w == 3;
      if (e1) gemv_load(pre, gv_blk(128 + bi), bi & 7, 10);
      else __builtin_amdgcn_s_sleep(8);
#pragma unroll
      for (int j = 0; j < 5; ++j)
        ld[j] = __builtin_amdgcn_raw_buffer_load_b128(rq, (unsigned)(h4 * D_ * 2 + n16 * 16 + j * 256), (unsigned)((32 * (T >> 1) + 8 * wj + 4 * (T & 1)) * D_ * 2), NT);
      TAG_READY();
      {
        const bf2_t ONE2 = {(__bf16)1.0f, (__bf16)1.0f};
        char* dst = qt + (4 * wj + h4) * 1312 + n16 * 16;
        float sa = 0.f, va = 0.f;
#pragma unroll
        for (int j = 0; j < 5; ++j) {
          *(u32x4*)(dst + j * 256) = ld[j];
#pragma unroll
          for (int d = 0; d < 4; ++d) {
            const unsigned a0 = ld[j][d];
            const bf2_t xa = __builtin_bit_cast(bf2_t, a0);
            sa = __builtin_amdgcn_fdot2_f32_bf16(xa, ONE2, sa, false); va = __builtin_amdgcn_fdot2_f32_bf16(xa, xa, va, false);
          }
        }
        lf_set(8 + w);
        const float ma = sum16(sa) * (1.0f / D_);
        va = __builtin_fmaxf(sum16(va) * (1.0f / D_) - ma * ma, 0.f);
        if (n16 == 0) mds[4 * wj + h4] = pack2(ma, __builtin_amdgcn_sqrtf(rbf(rbf(va) + 1e-5f)));
      }
      if (e1) gv_fin(pre, 128 + bi);
      lf_wait(8 + 4 * g, 4);
      STAMP(23);
      {
        const char* src = qt + (l & 7) * 1312 + (40 * wj + (l >> 3)) * 8;
#pragma unroll
        for (int j = 0; j < 5; ++j) {
          const u32x2 x = *(const u32x2*)(src + j * 64), y = *(const u32x2*)(src + j * 64 + 8 * 1312);
          st16(rs, WS_QB + (unsigned)T * 20480u + (unsigned)(5 * wj + j) * 1024u + l16, (u32x4){x[0], x[1], y[0], y[1]});
        }
      }
      SCHED();
      const int g2 = at < 8 || w < 3 || w == 4 ? 400 : w == 3 ? 384 + bi : 256 + wj * 32 + bi;
      if (g2 < 400) {
        gemv_load(pre, gv_blk(g2), g2 & 7, 10);
        asm volatile("s_waitcnt vmcnt(10)" ::: "memory");
      } else if (w < 2 && rj >= 0) {
        rt_ld(rj, rst);
        asm volatile("s_waitcnt vmcnt(7)" ::: "memory");
      } else {
        DRAIN();
      }
      lf_set(16 + w);
      if (wj == 2) {
        lf_wait(16 + 4 * g, 4);
        st16(rs, WS_ST1 + (unsigned)(l >> 3) * 16384u + (unsigned)T * 128u + (unsigned)(l & 7) * 16u, (u32x4){mds[l & 7], tag, mds[8 + (l & 7)], tag});
        STAMP(22);
      }
      if (w >= 4 && w < 7) lf_set(w);
      if (g2 < 400) gv_fin(pre, g2);
      STAMP(25);
    } else {
      constexpr int KB = L_RT + 16 * 1312;
      TOUCH_AT(3);
      const int g1 = w < 6 ? 2 * ai + wj : 32 + wj * 64 + ai;
      if (w >= 6) {
        const rsrc_t rk = __builtin_amdgcn_make_buffer_rsrc((void*)(w == 6 ? p.k : p.v), 0, S_ * D_ * 2, 0x00020000);
        const unsigned ko = (unsigned)((1536 + 8 * ai) * (D_ * 2) - (w == 6 ? 4096 : 0));
        char* dst = lds + KB + (w == 6 ? 0 : 14336);
        __builtin_amdgcn_s_sleep(3);
#pragma unroll
        for (int n = 0; n < 10; ++n) __builtin_amdgcn_raw_ptr_buffer_load_lds(rk, (LAS void*)(dst + n * 1024), 16, l16, ko + (unsigned)n * 1024u, 0, NT);
        if (w == 6) {
#pragma unroll
          for (int n = 10; n < 14; ++n) __builtin_amdgcn_raw_ptr_buffer_load_lds(rk, (LAS void*)(dst + n * 1024), 16, l16, ko + (unsigned)n * 1024u, 0, NT);
        }
      }
      SCHED();
      u32x4 swt[2];
      swt[0] = *(const u32x4*)(p.shift_k + (tid - 256) * 8);
      swt[1] = *(const u32x4*)(p.shift_k + (256 + l) * 8);
      SCHED();
      if (w >= 6) __builtin_amdgcn_s_sleep(8);
      gemv_load(pre, gv_blk(g1), g1 & 7, 10);
      TAG_READY();
      asm volatile("s_waitcnt vmcnt(11)" : "+v"(swt[0]), "+v"(swt[1]) :: "memory");
      lf_set(8 + w);
      if (w < 6) gv_fin(pre, g1);
      STAMP(25);
      lf_wait(12, 4);
      STAMP(23);
#pragma unroll 1
      for (int n = 0; n < (w == 7 ? 2 : 1); ++n) {
        {
          const int pr = n ? 256 + l : tid - 256;
          const char* kp = lds + KB + 4096 + pr * 4;
          unsigned kk[11], vv[8];
#pragma unroll
          for (int i = 0; i < 11; ++i) kk[i] = *(const unsigned*)(kp + (i - 3) * 1280);
#pragma unroll
          for (int i = 0; i < 8; ++i) vv[i] = *(const unsigned*)(kp + 10240 + i * 1280);
          const u32x4 sw = n ? swt[1] : swt[0];
          const f32x2 wt[4] = {{bflo(sw[0]), bflo(sw[2])}, {bfhi(sw[0]), bfhi(sw[2])}, {bflo(sw[1]), bflo(sw[3])}, {bfhi(sw[1]), bfhi(sw[3])}};
          f32x2 o[8];
#pragma unroll
          for (int i = 0; i < 8; ++i) {
            f32x2 t = (f32x2){bflo(kk[i + 3]), bfhi(kk[i + 3])} * wt[0];
#pragma unroll
            for (int j = 1; j < 4; ++j) t = __builtin_elementwise_fma((f32x2){bflo(kk[i + 3 - j]), bfhi(kk[i + 3 - j])}, wt[j], t);
            o[i] = t * (f32x2){bflo(vv[i]), bfhi(vv[i])};
          }
#pragma unroll
          for (int e = 0; e < 4; ++e) {
            const int i = 7 - 4 * (e & 1);
            st16(rs, WS_KV3 + (unsigned)ai * 20480u + (unsigned)(e * 5120 + pr * 16),
                 (u32x4){pack2(o[i][e >> 1], o[i - 1][e >> 1]), tag, pack2(o[i - 2][e >> 1], o[i - 3][e >> 1]), tag});
          }
        }
      }
      STAMP(22);
      if (w < 7) lf_set(w);
      if (w >= 6) gv_fin(pre, g1);
      STAMPF(30);
    }
    if (w == 6 && ja < 80) {
      hold(AUX_T);
      gemv_load(pre, gv_blk(400 + ja), ja & 7, 10);
      gv_fin(pre, 400 + ja);
    }
    if (rj >= 0) {
      if (w >= 2) { hold(RT2_T); rt_ld(rj, rst); }
      rt_job(rj, rst);
    }
    STAMP(26);
    lf_set(w);
    if (w == 3) { hold(WA_T); wo_ld(); }
    STAMP(3);
    if (w < 4) {
      hold(W_T);
      b2_wait((u32x2){~tag, 0u}, 1);
      lf_wait(0, 8);
      STAMP(4);
    }
    DRAIN();
  }

  STAMPF(16);
  u32x4 wox[5];
  if (w == 3) {
    const char* bp = wo_st + (4 * h4 + (n16 >> 2)) * 160 + (n16 & 3) * 8;
#pragma unroll
    for (int dj = 0; dj < 5; ++dj) wox[dj] = trfrag(bp + dj * 32, 160);
    asm volatile("s_waitcnt lgkmcnt(0)" : "+v"(wox[0]), "+v"(wox[1]), "+v"(wox[2]), "+v"(wox[3]), "+v"(wox[4]) :: "memory");
  }
  auto wo_put = [&]() {
    if (w == 3 && R < 28) {
      const unsigned base = WS_WOUTF + (unsigned)c * WBLK + (unsigned)((R >> 1) * 10240 + (R & 1) * 1024);
#pragma unroll
      for (int dj = 0; dj < 5; ++dj) st16(rs, base + (unsigned)((R & 1) ? dj : (dj + 2) % 5) * 2048u + l16, wox[dj]);
    }
  };
  if (cs2) {
    cs_ld(csv, (8 + c) * 7 + R - 20);
    SCHED();
    load_b2(0, 12);
    SCHED();
    cs_fin(csv, (8 + c) * 7 + R - 20);
  } else if (w < 4) {
    asm volatile("" ::: "memory");
    load_b2(0, 12);
  }
  if (w < 4) {
    load_b2(12, BK);
    if (BG && is_conv) lf_wait(40, 4);
    load_b2(BK, 35);
  }
  wo_put();
  STAMPF(29);
  const unsigned xfb = WS_FX + (unsigned)(((R >> 1) * 4 + (R & 1) * 2 + (c >> 2)) * 160 + l) * 128u;
  unsigned xf3[3];
  if (w == 7) {
    lf_wait(36, 4);
    int dt = (int)(lf[44 + ((l & 31) < 20 ? l & 31 : 0)] - t_start);
    dt = l < 32 ? dt : -dt;
    dt = max(dt, __builtin_amdgcn_update_dpp(0, dt, 0xB1, 0xf, 0xf, true));
    dt = max(dt, __builtin_amdgcn_update_dpp(0, dt, 0x4E, 0xf, 0xf, true));
    dt = max(dt, __builtin_amdgcn_update_dpp(0, dt, 0x124, 0xf, 0xf, true));
    dt = max(dt, __builtin_amdgcn_update_dpp(0, dt, 0x128, 0xf, 0xf, true));
    const int d1 = min(max(max(__builtin_amdgcn_readlane(dt, 0), __builtin_amdgcn_readlane(dt, 16)), 0), 300);
    const int d0 = min(max(max(__builtin_amdgcn_readlane(dt, 32), __builtin_amdgcn_readlane(dt, 48)), 0), 300);
    dt = ((d1 + d0) * YW >> 3) - d0;
    __builtin_amdgcn_s_setprio(YPR);
    STAMPF(24);
    hold((unsigned)(YG_T + dt));
    STAMPF(30);
    xf3[0] = ld4(rs, xfb); xf3[1] = ld4(rs, xfb + 8192u); xf3[2] = ld4(rs, l < 32 ? xfb + 16384u : ~0u);
    __builtin_amdgcn_s_setprio(0);
  }
  SCHED();
  u32x4 k_c;
  unsigned kc_o = w < 2 ? (unsigned)(tid < 112 ? tid : 0) * 16u : 0x80000000u, kc_d = (unsigned)(L_CONST + (tid & 127) * 8);
  auto kc_ld = [&](int z2) { k_c = __builtin_amdgcn_raw_buffer_load_b128(rs, kc_o, WS_CS + (unsigned)(8 * z2 + c) * 2048u, SC1); };
  auto consts_put = [&](int z2) {
    if (w < 2) {
      if (__any(ne2(k_c[0], k_c[2]) != 0u)) {
        _Pragma("unroll 1") for (unsigned spins = 0; spins < SPIN_CAP; ++spins) {
          __builtin_amdgcn_s_sleep(8);
          kc_ld(z2);
          if (!__any(ne2(k_c[0], k_c[2]) != 0u)) break;
        }
      }
      if (z2) *(u32x2*)(lds + kc_d + 1024) = (u32x2){k_c[1], k_c[3]};
      else { const unsigned d = kc_d - ((kc_d >> 1) & 4u); *(unsigned*)(lds + d) = k_c[1]; *(unsigned*)(lds + d + 8) = k_c[3]; }
    }
  };
  const int wq = w ^ rotr, rt1 = wq & 3, kh1 = w >> 2;
  unsigned bo0 = (unsigned)(kh1 * 71680) + l16, bo1 = bo0 + 35840u;
  asm volatile("" : "+v"(bo0), "+v"(bo1));
  unsigned pso[3], pqo[3], oto = (unsigned)(((w ^ 4) * 7 * 64 + l) * 8), rdo = (unsigned)((w * 7 * 64 + l) * 8), cso = (unsigned)(L_CONST + (128 + n16) * 8);
#pragma unroll
  for (int e = 0; e < 3; ++e) {
    const int row = rt1 * 16 + (e < 2 ? 4 * h4 + 2 * kh1 + e : n16);
    pso[e] = (unsigned)(L_PST + row * 4); pqo[e] = (unsigned)(L_PST + 512 + row * 16 + (row & 3) * 4);
  }
  unsigned spo = (unsigned)(L_Z + tid * 16), p6o = (unsigned)(L_PST + (wq * 16 + 4 * h4) * 4), pgo = (unsigned)(L_PST + 512 + (wq * 16 + n16) * 16);
  asm volatile("" : "+v"(spo), "+v"(p6o), "+v"(pgo));
  p6o = n16 == 12 ? p6o : spo;
  pgo = h4 == (n16 >> 2) ? pgo : spo;
  unsigned bsrc = (unsigned)(((l >> 1) & 3) * 64 + (l & 1) * 4), ppo = l < 8 ? l16 : 0x80000000u;
  asm volatile("" : "+v"(pso[0]), "+v"(pso[2]), "+v"(pqo[0]), "+v"(pqo[2]), "+v"(oto), "+v"(rdo), "+v"(cso), "+v"(p6o), "+v"(pgo), "+v"(bsrc), "+v"(ppo), "+v"(kc_o), "+v"(kc_d));
  pso[1] = pso[0] + 4u; pqo[1] = pqo[0] + 20u;
#define BFRAG(q, j) (*(const u32x4*)(lds + ((q) < 5 ? bo0 : bo1) + ((q) % 5) * 7168 + (j) * 1024))
  u32x4 h2[10];
  unsigned xv = (unsigned)((4 * ((l >> 3) & 1) + h4) * 128 + (l & 7) * 16), xs0 = WS_X1B + (unsigned)(R * 4 + rt1) * 20480u + (unsigned)(10 * kh1) * 1024u;
  unsigned wvo = wsp_vo, bso = (unsigned)tau(rt1, n16) * 2u, fgo = l < 32 ? (unsigned)(l >> 3) * F4S + (unsigned)(l & 7) * 16u : 0x80000000u;
  asm volatile("" : "+v"(xv), "+v"(wvo), "+v"(bso), "+v"(fgo));
#define WO3 (-92)
#define PINS(x) do { x = (unsigned)__builtin_amdgcn_readfirstlane((int)x); asm volatile("" : "+s"(x)); } while (0)
  PINS(xs0);
  unsigned zs[8], wx8, wx4, wgb, fzs, uo, ui, xs, wl2, os, zos, pos, s1, s3;
#define LDX(q) h2[q] = __builtin_amdgcn_raw_buffer_load_b128(rs, xv + (unsigned)((q) & 3) * 1024u, xs0 + (unsigned)((q) >> 2) * 4096u, BULK_AUX)
  auto flag4 = [&](unsigned off) { ST16S(((u32x4){tag, tag, tag, tag}), rs, fgo, WS_F4 - WS_FWI + off, SC1); };
  unsigned bs_raw, fwo[1];
  f32x4 acc[7];
  float mu_n, rs_n;
  {
    unsigned f1o[1] = {w == 1 && l < 20 ? fwi + (unsigned)(c * 20 + l) * 128u : ~0u}, f1v;
    fwo[0] = w == 3 && l < 28 ? fwi + (WS_FWO - WS_FWI) + (unsigned)(c * 32 + l) * 128u : ~0u;
    asm volatile("" : "+v"(f1o[0]), "+v"(fwo[0]));
    if (w == 7) {
      asm volatile("" : "+v"(xf3[0]), "+v"(xf3[1]), "+v"(xf3[2]));
      if (__any(ne2(xf3[0], xf3[1]) != 0u) || __builtin_amdgcn_ballot_w64(xf3[2] == tag) != 0xffffffffull) {
        const unsigned fo[3] = {xf3[0] != tag ? xfb : ~0u, xf3[1] != tag ? xfb + 8192u : ~0u, l < 32 && xf3[2] != tag ? xfb + 16384u : ~0u};
        wait_lines<3, YSL>(rs, fo, tag);
      }
      if (l == 0) lf[33] = tag;
    }
    lf_wait(33, 1);
    STAMP(7);
    SCHED();
#pragma unroll
    for (int q = 0; q < 3; ++q) LDX(q);
    SCHED();
    __builtin_amdgcn_s_waitcnt(0x0F73);
    BAR();
    STAMP(8);
#pragma unroll
    for (int j = 0; j < 7; ++j) acc[j] = (f32x4){0.f, 0.f, 0.f, 0.f};
    f32x4 accg = {0.f, 0.f, 0.f, 0.f};
    u32x4 bf[7];
#pragma unroll
    for (int j = 0; j < 7; ++j) bf[j] = BFRAG(0, j);
    SCHED();
#pragma unroll
    for (int q = 3; q < 6; ++q) LDX(q);
    if (w == 3) flag4(WS_FWO + (unsigned)(c * 32 + R) * 128u);
    SCHED();
#pragma unroll
    for (int t = 0; t < 8; ++t) { zs[t] = WS_Z2F + (unsigned)(c * 64 + 8 * w + 4 * ((t >> 2) ^ rotg) + ((t + rotb) & 3)) * 7168u; PINS(zs[t]); }
    wx8 = (unsigned)((w + (idx >> 2)) & 7) * 1024u; wx4 = wx8 & 3072u; wgb = WS_WOUTF + (unsigned)c * WBLK; fzs = fz + (unsigned)(c * 32 + 4 * w) * 128u;
    uo = WS_UF + (unsigned)((R * 4 + rt1) * 8 + c) * 4096u + (unsigned)kh1 * 2048u; ui = WS_UF + (unsigned)((R * 4 + rt1) * 8 + 4 * kh1) * 4096u;
    xs = WS_X1B + (unsigned)(R * 4 + rt1) * 20480u + (unsigned)(5 * c + 2 * kh1) * 512u; wl2 = (unsigned)(120 + WO3 + (w < 4 ? w + 16 : w)) * 1024u;
    os = (unsigned)(((R * 64 + tau(rt1, 0)) * D_ + 80 * c + 32 * kh1) * 2);
    zos = WS_Z2F + (unsigned)((c * 64 + 4 * (R >> 1)) * 7 + (w < 6 ? w + 1 : 0)) * 1024u + (unsigned)(R & 1) * 512u; pos = WS_PART + (unsigned)(R * 64 + wq) * 128u;
    s1 = (unsigned)((w ^ 4) * 3072 + 1024); s3 = (unsigned)(w * 3072 + 1024);
    PINS(wx8); PINS(wx4); PINS(wgb); PINS(fzs); PINS(uo); PINS(ui); PINS(xs); PINS(wl2); PINS(os); PINS(zos); PINS(pos); PINS(s1); PINS(s3);
    SCHED();
    auto xswap = [&](int q, int e) {
#pragma unroll
      for (int d = 0; d < 2; ++d) {
        if (e == 1) h2[q][2 + d] ^= (unsigned)__builtin_amdgcn_update_dpp(0, (int)h2[q][d], 0x128, 0xf, 0x3, false);
        else h2[q][d] ^= (unsigned)__builtin_amdgcn_update_dpp(0, (int)h2[q][2 + d], 0x128, 0xf, 0xC, false);
      }
      asm volatile("" : "+v"(h2[q]));
    };
#define XSW 1
#pragma unroll
    for (int q = 0; q < 10; ++q) {
      if (q < XSW) { xswap(q, 0); xswap(q, 1); xswap(q, 2); }
      if (q == 5 && w == 1 && __builtin_amdgcn_ballot_w64(f1v == tag) != 0xfffffull) wait_lines<1, 4>(rs, f1o, tag);
      if (q == 5) { __builtin_amdgcn_s_barrier(); asm volatile("" ::: "memory"); }
      SCHED();
      accg = MFMA16(h2[q], h2[q], accg);
      SCHED();
#pragma unroll
      for (int j = 0; j < 7; ++j) {
        acc[j] = MFMA16(h2[q], bf[j], acc[j]);
        if (q == 9 && j == 1) *(f32x4*)(lds + pgo) = accg;
        if (q < 9) bf[j] = BFRAG(q + 1, j);
        if (q < 9 && q + 1 >= XSW && j >= 3 && j < 6) xswap(q + 1, j - 3);
        if (q == 1 && j == 6) f1v = ld4(rs, f1o[0]);
        if (q == 2 && j == 0) bs_raw = __builtin_amdgcn_raw_buffer_load_b16(__builtin_amdgcn_make_buffer_rsrc((void*)p.b_sp, 0, S_ * 2, 0x00020000), bso, (unsigned)R * 128u, 0);
#define KCQ 4
#define WA 8
#define WB 12
#define WQ0 2
        if (WQ0 == 2 && q >= 2 && j == 2) LDW(q - 2);
        if (WQ0 == 6 && q >= 6 && (j == 2 || j == 4)) LDW(2 * (q - 6) + (j >> 2));
        if (WQ0 == 3 && q >= 2 && q < 5 && (j & 1) && 3 * (q - 2) + (j >> 1) < WA) LDW(3 * (q - 2) + (j >> 1));
        if (WQ0 == 4 && q >= 2 && !(q & 1) && j == 2) LDW((q - 2) >> 1);
        if ((j == 0 || j == 4) && 6 + 2 * q + (j >> 2) < 10) LDX(6 + 2 * q + (j >> 2));
        if (q == KCQ && j == 0) kc_ld(1);
        if (q == 2 && j == 1 && cs1) cs_ld(csv, c * 7 + R - 25);
        if (q >= 5 && j == 5) {
          const int i = (5 + q) * 7 + (w < 7 ? w : 0);
          dma1k(rs, lds + i * 1024, l16, WS_WINF + (unsigned)c * WBLK + (unsigned)i * 1024u);
          if (q == 9) { const int i2 = 28 + (w < 7 ? w : 0); dma1k(rs, lds + i2 * 1024, l16, WS_WINF + (unsigned)c * WBLK + (unsigned)i2 * 1024u); }
        }
        SCHED();
      }
    }
    STAMP(31);
  }
  unsigned sro, swo, zo, zro, po;
  auto gelu_n = [&](f32x2* x, auto N_) {
    constexpr int N = decltype(N_)::value;
    const float c0 = -2.0f * 0.7978845608028654f * 1.4426950408889634f, c1 = c0 * 0.044715f;
    f32x2 k0 = {c0, c0}, k1 = {c1, c1}, k2 = {1.0f, 1.0f};
    asm volatile("" : "+v"(k0), "+v"(k1), "+v"(k2));
    f32x2 t[N];
#define GST(e) do { _Pragma("unroll") for (int i = 0; i < N; ++i) { t[i] = (e); asm volatile("" : "+v"(t[i])); } } while (0)
    GST(x[i] * x[i]);
    GST(__builtin_elementwise_fma(t[i], k1, k0));
    GST(t[i] * x[i]);
    GST(((f32x2){__builtin_amdgcn_exp2f(t[i][0]), __builtin_amdgcn_exp2f(t[i][1])}));
    GST(t[i] + k2);
    GST(((f32x2){__builtin_amdgcn_rcpf(t[i][0]), __builtin_amdgcn_rcpf(t[i][1])}));
#pragma unroll
    for (int i = 0; i < N; ++i) { x[i] *= t[i]; asm volatile("" : "+v"(x[i])); }
  };
  {
    f32x2 mine[7];
    f32x2 csb[7];
    if (kh1) {
#pragma unroll
      for (int j = 0; j < 7; ++j) { *(f32x2*)(lds + rdo + j * 512) = (f32x2){acc[j][0], acc[j][1]}; mine[j] = (f32x2){acc[j][2], acc[j][3]}; }
      *(f32x4*)(lds + p6o) = acc[6];
    } else {
#pragma unroll
      for (int j = 0; j < 7; ++j) { *(f32x2*)(lds + rdo + j * 512) = (f32x2){acc[j][2], acc[j][3]}; mine[j] = (f32x2){acc[j][0], acc[j][1]}; }
      *(f32x4*)(lds + p6o) = acc[6];
      consts_put(1);
      if (cs1) cs_fin(csv, c * 7 + R - 25);
#pragma unroll
      for (int k = WA; k < WB; ++k) LDW(k);
    }
    BAR();
#define BY 0
    auto b1p = [&](int k) {
      const int i = 4 * k + (w & 3) + (k < 8 ? 35 : k > 8 ? 70 : (w & 3) < 3 ? 35 : 70) - (k == 17 ? (w & 2) : 0);
      dma1k(rs, lds + i * 1024, l16, WS_WINF + (unsigned)c * WBLK + (unsigned)i * 1024u);
    };
    if (kh1) {
#pragma unroll
      for (int k = 0; k < BY; ++k) b1p(k);
#pragma unroll
      for (int k = WA; k < WB; ++k) LDW(k);
    }
    __builtin_amdgcn_s_setprio(3);
    STAMPT(9);
    f32x2 oth[7];
    f32x2 pa[3], pb[3];
#pragma unroll
    for (int e = 0; e < 3; ++e) {
      pa[e] = (f32x2){*(const float*)(lds + pso[e]), *(const float*)(lds + pqo[e])};
      pb[e] = (f32x2){*(const float*)(lds + pso[e] + 256), *(const float*)(lds + pqo[e] + 1024)};
    }
#pragma unroll
    for (int j = 0; j < 7; ++j) { oth[j] = *(const f32x2*)(lds + oto + j * 512); csb[j] = *(const f32x2*)(lds + cso + 128 * j); }
    SCHED();
    float mu2[2], rs2[2];
#pragma unroll
    for (int e = 0; e < 3; ++e) {
      const float mean = (pa[e][0] + pb[e][0]) * (1.0f / D_);
      const float var = __builtin_fmaxf((pa[e][1] + pb[e][1]) * (1.0f / D_) - mean * mean, 0.f);
      const float rstd = __builtin_amdgcn_rsqf(var + 1e-5f);
      if (e < 2) { mu2[e] = mean; rs2[e] = rstd; } else { mu_n = mean; rs_n = rstd; }
    }
    f32x2 zz[7];
    const f32x2 nmu = {-mu2[0], -mu2[1]}, rsv = {rs2[0], rs2[1]};
#pragma unroll
    for (int j = 0; j < 7; ++j)
      zz[j] = __builtin_elementwise_fma(__builtin_elementwise_fma(nmu, (f32x2){csb[j][0], csb[j][0]}, mine[j] + oth[j]), rsv, (f32x2){csb[j][1], csb[j][1]});
    gelu_n(zz, IC<7>());
    if (n16 >= 12) zz[6] = (f32x2){0.f, 0.f};
    f32x2 s1 = ((zz[0] + zz[1]) + (zz[2] + zz[3])) + ((zz[4] + zz[5]) + zz[6]);
    f32x2 s2 = zz[0] * zz[0], s3 = zz[1] * zz[1];
#pragma unroll
    for (int j = 2; j < 7; ++j) { if (j & 1) s3 = __builtin_elementwise_fma(zz[j], zz[j], s3); else s2 = __builtin_elementwise_fma(zz[j], zz[j], s2); }
    s2 += s3;
    asm("" : "+v"(s1), "+v"(s2));
    float a0 = s1[0] + dpp_f<0xB1>(s1[0]), a1 = s1[1] + dpp_f<0xB1>(s1[1]), b0 = s2[0] + dpp_f<0xB1>(s2[0]);
    a0 = (l & 1) ? a1 : a0;
    const float b1 = s2[1] + dpp_f<0xB1>(s2[1]);
    b0 = (l & 1) ? b1 : b0;
    a0 += dpp_f<0x4E>(a0); b0 += dpp_f<0x4E>(b0);
    a0 += dpp_f<0x124>(a0); b0 += dpp_f<0x124>(b0);
    a0 += dpp_f<0x128>(a0); b0 += dpp_f<0x128>(b0);
    {
      const unsigned pa0 = __builtin_amdgcn_ds_bpermute(bsrc, __float_as_uint(a0)), pb0 = __builtin_amdgcn_ds_bpermute(bsrc, __float_as_uint(b0));
      ST16S(((u32x4){tag, pa0, tag, pb0}), rs, ppo, pos + (unsigned)c * 1024u, SC1);
    }
    SCHED();
    __builtin_amdgcn_s_setprio(0);
    STAMP(10);
#define BO 14
    if (kh1) {
#pragma unroll
      for (int k = BY + BO; k < 18; ++k) b1p(k);
    } else {
#pragma unroll
      for (int k = BY; k < BY + BO; ++k) b1p(k);
    }
#pragma unroll
    for (int k = WB; k < 16; ++k) LDW(k);
#pragma unroll
    for (int j = 0; j < 7; ++j)
      *(unsigned*)(lds + L_Z + (((rt1 >> 1) * 7 + j) * 64 + l) * 16 + (4 * (rt1 & 1) + 2 * kh1) * 2) = pack2(zz[j][0], zz[j][1]);
    SCHED();
#define TR0 2
#define TR1 TR0
#define TR2 TR1
#define TR3 5
#pragma unroll
    for (int g = 0; g < TR0; ++g) WSP_TR(g);
    sro = (unsigned)(L_ST2 + h4 * 16);
    swo = (unsigned)(L_ST2 + (16 * rt1 + 4 * ((l >> 1) & 3) + 2 * kh1 + (l & 1)) * 4);
    zo = w < 7 ? (unsigned)(h4 * 7168 + n16 * 16) : 0x80000000u;
    zro = (unsigned)(L_Z + (w < 7 ? w : 0) * 1024) + l16;
    po = (unsigned)((l >> 3) * 1024 + (l & 7) * 16);
    asm volatile("" : "+v"(sro), "+v"(swo), "+v"(zo), "+v"(zro), "+v"(po));
    asm volatile("s_waitcnt vmcnt(%0)" :: "n"(16 - WB) : "memory");
    BAR();
    STAMPT(11);
  }
  unsigned z1d[7];
  constexpr int L_RED = 28672;
#define WOA(i) (((i) < 28 ? (i) : (i) < 108 ? (i) + 32 : (i) < 120 ? (i) + 37 : (i) - 92) * 1024)
#define RBAR() do { __builtin_amdgcn_s_barrier(); asm volatile("" ::: "memory"); } while (0)
#define ZAUX 16
#define NW 14
#define WOD(i0, x) dma1k(rs, lds + WOA(i0) + (x), l16, wgb + (unsigned)(i0) * 1024u + (x))
  auto wo_dma = [&](int k) {};
  u32x4 zw[NW];
#define LDZ(f) do { if (((f) >> 3) == 4) { zs[(f) & 7] += 4096u; asm volatile("" : "+s"(zs[(f) & 7])); } \
    zw[(f) % NW] = __builtin_amdgcn_raw_buffer_load_b128(rs, l16 + (unsigned)(((f) >> 3) & 3) * 1024u, zs[(f) & 7], ZAUX); } while (0)
#define WD 24
#define WE 32
#define QS 5
#define PQ 3
#define NW0 NW
#define VMW(n) asm volatile("s_waitcnt vmcnt(%0)" ::"n"(n) : "memory")
#define FQ 10
#define PP 1
#define WZ 2
#define WY 8
  static_assert(QS >= 4 && QS <= 6 && PQ >= 2 && PQ < QS && (WZ < 4 || QS >= 5), "");
  {
    f32x4 acc[7];
#pragma unroll
    for (int j = 0; j < 7; ++j) acc[j] = (f32x4){0.f, 0.f, 0.f, 0.f};
    u32x4 bf[7];
#pragma unroll
    for (int j = 0; j < 7; ++j) bf[j] = BFRAG(4, j);
    u32x4 pv, zf[2];
    unsigned fwv;
    auto ld_pv = [&]() { pv = __builtin_amdgcn_raw_buffer_load_b128(rs, po, pos, SC1); };
    auto z1 = [&](auto Q0_, auto Q1_) __attribute__((always_inline)) {
#pragma unroll
      for (int qi = decltype(Q0_)::value; qi < decltype(Q1_)::value; ++qi) {
        const int q = (qi + 4) % 10, qn = (qi + 5) % 10;
        if (qi == FQ) {
          if (kh1) VMW(WE - WD + WZ); else VMW(0);
          RBAR();
          if (w == 3) flag4(WS_FZ + (unsigned)(c * 32 + R) * 128u);
        }
        SCHED();
#pragma unroll
        for (int j = 0; j < 7; ++j) {
          acc[j] = MFMA16(bf[j], h2[q], acc[j]);
          if (qi < 9 && qi != QS - 1) bf[j] = BFRAG(qn, j);
          if (qi < 2 && (j == 1 || j == 4)) { const int i = (2 * qi + (j >> 2)) * 7 + (w < 7 ? w : 0); dma1k(rs, lds + i * 1024, l16, WS_WINF + (unsigned)c * WBLK + (unsigned)i * 1024u); }
          if (qi == 2 && j == 0) kc_ld(0);
          if (qi == 2 && j == 3) fwv = ld4(rs, fwo[0]);
          if (qi == PQ && j == 1) ld_pv();
          if (qi == QS - 1 && j < 2) zf[j] = *(const u32x4*)(lds + zro + j * 7168);
          if (!PP && qi >= QS && (j & 1) && WD + 3 * (qi - QS) + (j >> 1) < WE) LDW((WD + 3 * (qi - QS) + (j >> 1)) & 31);
          SCHED();
        }
      }
    };
    auto req1 = [&]() {
#pragma unroll
      for (int k = 16; k < WD; ++k) { LDW(k); SCHED(); }
    };
    auto req2 = [&]() {
#pragma unroll
      for (int k = WD; k < (PP ? WE : WD); ++k) { LDW(k); SCHED(); }
      sfor<0, WZ>([&](auto G_) __attribute__((always_inline)) { WOD(68 + 8 * decltype(G_)::value, wx8); SCHED(); });
    };
    if (kh1) req1();
    z1(IC<0>(), IC<QS>());
    STAMPT(29);
    f32x2 zv[2][4];
#pragma unroll
    for (int i = 0; i < 8; ++i) { zv[i >> 2][i & 3] = (f32x2){bflo(zf[i >> 2][i & 3]), bfhi(zf[i >> 2][i & 3])}; asm volatile("" : "+v"(zv[i >> 2][i & 3])); }
#pragma unroll
    for (int g = TR0; g < TR1; ++g) WSP_TR(g);
    __builtin_amdgcn_s_setprio(3);
    if (__any(ne2(pv[0], pv[2]) != 0u)) {
      _Pragma("unroll 1") for (unsigned spins = 0; spins < SPIN_CAP; ++spins) {
        ld_pv();
        if (!__any(ne2(pv[0], pv[2]) != 0u)) break;
        __builtin_amdgcn_s_sleep(1);
      }
    }
    {
      float t1 = __uint_as_float(pv[1]), t2 = __uint_as_float(pv[3]);
      t1 += dpp_f<0x128>(t1); t2 += dpp_f<0x128>(t2);
      auto x = __builtin_amdgcn_permlane16_swap(__float_as_uint(t1), __float_as_uint(t2), false, false);
      const unsigned y = __float_as_uint(__uint_as_float(x[0]) + __uint_as_float(x[1]));
      auto z = __builtin_amdgcn_permlane32_swap(y, y, false, false);
      const unsigned u = __float_as_uint(__uint_as_float(z[0]) + __uint_as_float(z[1]));
      auto v = __builtin_amdgcn_permlane16_swap(u, u, false, false);
      const float mean = __uint_as_float(v[0]) * (1.0f / FH);
      const float var = __builtin_fmaxf(__uint_as_float(v[1]) * (1.0f / FH) - mean * mean, 0.f);
      const float rstd = __builtin_amdgcn_rsqf(var + 1e-5f);
      *(float*)(lds + swo) = rstd;
      *(float*)(lds + swo + 256) = -mean * rstd;
    }
    SCHED();
    consts_put(0);
    if (w == 3 && __builtin_amdgcn_ballot_w64(fwv == tag) != 0xfffffffull) wait_lines<1, 1>(rs, fwo, tag);
    if (!kh1) req1();
    BAR();
    STAMP(12);
    {
      f32x4 sa[2][2], sb[2][2];
#pragma unroll
      for (int i = 0; i < 4; ++i) { sa[i >> 1][i & 1] = *(const f32x4*)(lds + sro + i * 64); sb[i >> 1][i & 1] = *(const f32x4*)(lds + sro + i * 64 + 256); }
      SCHED();
#pragma unroll
      for (int m = 0; m < 2; ++m) {
        u32x4 o;
#pragma unroll
        for (int e = 0; e < 4; ++e) {
          const f32x4 a = sa[m][e >> 1], b = sb[m][e >> 1];
          const f32x2 x = __builtin_elementwise_fma(zv[m][e], (f32x2){a[2 * (e & 1)], a[2 * (e & 1) + 1]}, (f32x2){b[2 * (e & 1)], b[2 * (e & 1) + 1]});
          o[e] = pack2(x[0], x[1]);
        }
        ST16S(o, rs, zo + (unsigned)m * 256u, zos, SC1);
      }
    }
    SCHED();
    __builtin_amdgcn_s_setprio(0);
    STAMPT(13);
#pragma unroll
    for (int j = 0; j < 7; ++j) bf[j] = BFRAG((QS + 4) % 10, j);
    if (kh1) req2();
    z1(IC<QS>(), IC<10>());
    f32x2 mine[7];
    if (kh1) {
#pragma unroll
      for (int j = 0; j < 7; ++j) { *(f32x2*)(lds + rdo + L_RED + j * 512) = (f32x2){acc[j][0], acc[j][1]}; mine[j] = (f32x2){acc[j][2], acc[j][3]}; }
    } else {
#pragma unroll
      for (int j = 0; j < 7; ++j) { *(f32x2*)(lds + rdo + L_RED + j * 512) = (f32x2){acc[j][2], acc[j][3]}; mine[j] = (f32x2){acc[j][0], acc[j][1]}; }
    }
    if (!kh1) req2();
#pragma unroll
    for (int g = TR1; g < TR2; ++g) WSP_TR(g);
    if (FQ > 9) VMW(WE - WD + WZ);
    BAR();
    if (FQ > 9 && w == 3) flag4(WS_FZ + (unsigned)(c * 32 + R) * 128u);
    STAMP(6);
    auto req = [&](auto G0_, auto G1_) __attribute__((always_inline)) {
#pragma unroll
      for (int k = WE; k < 32; ++k) LDW(k);
      SCHED();
      sfor<decltype(G0_)::value, decltype(G1_)::value>([&](auto G_) __attribute__((always_inline)) {
        constexpr int i0 = 4 * decltype(G_)::value;
        WOD(i0 < 68 ? i0 : i0 + 8 * WZ, wx4); SCHED();
      });
    };
    if (kh1) req(IC<0>(), IC<WY>());
    u32x4 gv[8];
    auto gb_ld = [&]() {
#pragma unroll
      for (int k = 0; k < 8; ++k) gv[k] = __builtin_amdgcn_raw_buffer_load_b128(rs, l < 40 ? l16 : 0u, WS_MODQ + (unsigned)(k * 2560 + 1920 + 80 * c) * 8u, SC1);
    };
    if (w == 2) gb_ld();
    f32x2 z[7];
    {
      f32x2 oth[7];
      f32x4 cc[7];
#pragma unroll
      for (int j = 0; j < 7; ++j) {
        oth[j] = *(const f32x2*)(lds + oto + L_RED + j * 512);
        cc[j] = *(const f32x4*)(lds + L_CONST + (16 * j + 4 * h4 + 2 * kh1) * 8);
      }
      SCHED();
#pragma unroll
      for (int j = 0; j < 7; ++j)
        z[j] = __builtin_elementwise_fma(__builtin_elementwise_fma((f32x2){-mu_n, -mu_n}, (f32x2){cc[j][0], cc[j][1]}, oth[j] + mine[j]),
                                         (f32x2){rs_n, rs_n}, (f32x2){cc[j][2], cc[j][3]});
    }
    gelu_n(z, IC<7>());
#pragma unroll
    for (int j = 0; j < 7; ++j) z1d[j] = pack2(z[j][0], z[j][1]);
    asm volatile("" : "+v"(z1d[0]), "+v"(z1d[1]), "+v"(z1d[2]), "+v"(z1d[3]), "+v"(z1d[4]), "+v"(z1d[5]), "+v"(z1d[6]));
    SCHED();
    STAMPT(30);
    if (w == 2) {
      auto bad = [&]() {
        unsigned d = 0u;
#pragma unroll
        for (int k = 0; k < 8; ++k) d |= ne2(gv[k][0], gv[k][2]);
        return __any(d != 0u);
      };
      if (bad()) {
        _Pragma("unroll 1") for (unsigned spins = 0; spins < SPIN_CAP; ++spins) {
          __builtin_amdgcn_s_sleep(8);
          gb_ld();
          if (!bad()) break;
        }
      }
      f32x2 t[4];
#pragma unroll
      for (int k = 0; k < 4; ++k) t[k] = (f32x2){__uint_as_float(gv[2 * k][1]) + __uint_as_float(gv[2 * k + 1][1]), __uint_as_float(gv[2 * k][3]) + __uint_as_float(gv[2 * k + 1][3])};
      *(f32x2*)(lds + L_MISC + l * 8) = rbf2((t[0] + t[1]) + (t[2] + t[3]));
    }
    if (!kh1) req(IC<WY>(), IC<30 - 2 * WZ>());
    const unsigned fo[1] = {l < 4 ? fzs + (unsigned)l * 128u : ~0u};
#define FPY 1
#define FPO FPY
#define FSL 1
    unsigned fv;
#pragma unroll
    for (int g = TR2; g <= TR3; ++g) {
      if (FPY == FPO) { if (g == TR2 + FPY) fv = ld4(rs, fo[0]); }
      else if (g == TR2 + FPY) { if (kh1) fv = ld4(rs, fo[0]); }
      else if (g == TR2 + FPO) { if (!kh1) fv = ld4(rs, fo[0]); }
      SCHED();
      if (g < TR3) WSP_TR(g);
      SCHED();
    }
    STAMP(14);
    if (__builtin_amdgcn_ballot_w64(fv == tag) != 15ull) wait_lines<1, FSL>(rs, fo, tag);
    STAMPT(23);
#pragma unroll
    for (int f = 0; f < NW; ++f) { LDZ(f); SCHED(); }
  }

#define WO2 15
#define TRC TR3
  {
    SCHED();
#pragma unroll
    for (int g = TRC; g < 8; ++g) WSP_TR(g);
    SCHED();
    STAMP(20);
    const int rt = rt1, kh = kh1;
    const unsigned rw = (unsigned)(L_RED + w * 512);
    unsigned rr = (unsigned)(L_RED + w * 4096 + l * 4);
    asm volatile("; m0 is set here, not in front of the first store (one wait state)" : "+v"(rr) : "{m0}"(rw));
    const float bs = bflo(bs_raw);
    unsigned long long hm = 0xffff000000000000ull;
    asm volatile("" : "+s"(hm));
    unsigned l8;
    f32x4 acc2[2][4];
    float rv[8][2];
    u32x4 ud;
    u32x4 P[2][8];
    u32x2 xp[3];
    auto ld_p = [&](int pt, int i) { P[pt][i] = __builtin_amdgcn_raw_buffer_load_b128(rs, l16 + (unsigned)pt * 1024u, ui + (unsigned)i * 2048u, SC1); };
#define RED_PUT1(j, i) asm volatile("ds_write_addtid_b32 %0 offset:%2" :: "v"(acc2[(j) & 1][((i) >> 2) & 3][(i) & 3]), "{m0}"(rw), "n"((((i) >> 2) & 3) * 4096 + (((i) >> 1) & 1) * 16384 + ((i) & 1) * 256) : "memory")
#define RED_PUT(j, r) do { RED_PUT1(j, 4 * (r)); RED_PUT1(j, 4 * (r) + 1); RED_PUT1(j, 4 * (r) + 2); RED_PUT1(j, 4 * (r) + 3); } while (0)
#define RED_GET(i) do { f32x2 x_; asm volatile("ds_read2st64_b32 %0, %1 offset0:%2 offset1:%3" : "=v"(x_) : "v"(rr), "n"(2 * (i)), "n"(2 * (i) + 1) : "memory"); rv[i][0] = x_[0]; rv[i][1] = x_[1]; } while (0)
    float zz[2], gg[2];
    unsigned cs;
    auto sum_rows_u = [&](unsigned v) {
      auto a = __builtin_amdgcn_permlane16_swap(v, v, false, false);
      v = a[0] + a[1];
      auto b = __builtin_amdgcn_permlane32_swap(v, v, false, false);
      return b[0] + b[1];
    };
    auto red_fin = [&](int j, int k, bool fuse = false) {
      const int e = k & 1, i = k >> 1;
#define VADD(d, x, y) asm volatile("v_add_f32 %0, %1, %2" : "=v"(d) : "v"(x), "v"(y))
      if (i < 4) VADD(rv[2 * i][e], rv[2 * i][e], rv[2 * i + 1][e]);
      if (i == 4 || i == 5) VADD(rv[4 * (i & 1)][e], rv[4 * (i & 1)][e], rv[4 * (i & 1) + 2][e]);
      if (i == 6) VADD(rv[0][e], rv[0][e], bs);
      if (i == 7) VADD(gg[e], rv[0][e], rv[4][e]);
      if (i == (fuse ? 9 : 8)) zz[e] = e ? bfhi(z1d[(j + 6) % 7]) : bflo(z1d[(j + 6) % 7]);
      if (i == 9) zz[e] *= gg[e];
      if (i == 9 || (i == 8 && !fuse)) asm volatile("" : "+v"(zz[e]));
      if (k == 20) { ud[j & 3] = pack2(zz[0], zz[1]); asm volatile("" : "+v"(ud[j & 3])); }
      if (k == 21) {
        if (j == 0) asm volatile("v_cndmask_b32 %0, %0, 0, %1" : "+v"(ud[0]) : "s"(hm));
        if (j == 2) asm volatile("v_add3_u32 %0, %1, %2, %3" : "=v"(cs) : "v"(ud[0]), "v"(ud[1]), "v"(ud[2]));
        if (j == 3) {
          cs = tag - sum_rows_u(cs + ud[3]);
          asm volatile("v_cndmask_b32 %0, %0, %1, %2" : "+v"(ud[0]) : "v"(cs), "s"(hm));
        }
        if (j >= 4) { ud[3] = (j == 4 ? tag : ud[3]) - ud[j & 3]; asm volatile("" : "+v"(ud[3])); }
        if (j % 3 == 0 && j) ST16S(ud, rs, l16 + (unsigned)(j / 6) * 1024u, uo, SC1);
      }
    };
    struct Sch { int b1, b2, wp[16], rp[8], vp[22], lp[8], pr[32]; };
    constexpr Sch S = {18, 28, {0, 1, 2, 3, 4, 5, 6, 7, 8, 9, 10, 11, 12, 12, 15, 15}, {19, 19, 20, 22, 23, 23, 24, 24}, {29, 29, 30, 30, 31, 31, 32, 33, 35, 36, 37, 38, 39, 40, 41, 42, 43, 46, 43, 46, 48, 48},
                       {1, 1, 1, 1, 1, 1, 1, 1}, {0, 0, 2, 1, 0, 0, 0, 0, 0, 0, 2, 2, 0, 0, 0, 0, 0, 0, 0, 1, 0, 0, 0, 1, 0, 0, 2, 1, 0, 0, 0, 0}};
    constexpr Sch F = {15, 23, {0, 1, 1, 2, 3, 4, 4, 5, 6, 7, 8, 9, 10, 11, 12, 12}, {15, 16, 16, 17, 17, 18, 18, 19}, {23, 23, 24, 24, 25, 26, 26, 27, 27, 28, 28, 29, 30, 30, 31, 31, 22, 22, 32, 33, 34, 35},
                       {1, 2, 1, 1, 1, 1, 1, 1}, {0, 2, 1, 0, 0, 1, 1, 0, 0, 2, 1, 0, 0, 2, 1, 0, 0, 2, 1, 0, 0, 2, 1, 0, 0, 2, 1, 0, 0, 2, 1, 0}};
#define VM 3
#define JF 4
#define RO 3
#define XE 1
#define CE 0
#define AP0 2
#define P1B 2
#define P1S 3
    unsigned wb[3], xo;
    const unsigned wl[3] = {(unsigned)(120 + WO3 + w) * 1024u, (unsigned)(128 + WO3 + w) * 1024u, wl2}, wg = wgb - (unsigned)(WO3 * 1024);
#define NAF 32
#define NA0 16
#define NA1 NA0
    u32x4 af[NAF];
#define LDA(m) af[(m) % NAF] = *(const u32x4*)(lds + wb[WOA(2 * (m)) >> 16] + (WOA(2 * (m)) & 65535))
    unsigned ck[2];
    const unsigned tag4 = tag * 4u;
    auto p_chk = [&](int pt, int pr, int k) {
      u32x4* q = P[pt] + 4 * pr;
      if (k < 2) ck[k & 1] = q[k & 1][0] + q[k & 1][1];
      else if (k < 8) ck[k & 1] = ck[k & 1] + q[(k & 1) + 2 * ((k >> 2) & 1)][k & 2] + q[(k & 1) + 2 * ((k >> 2) & 1)][(k & 2) + 1];
      else if (k == 8) ck[0] = pt ? ck[0] + ck[1] : sum_rows_u(ck[0] + ck[1]);
      if (k < 9) asm volatile("" : "+v"(ck[k & 1]));
      if (k == 9) {
        if (__builtin_expect(__any(ck[0] != tag4), 0)) {
          _Pragma("unroll 1") for (unsigned spins = 0; spins < SPIN_CAP; ++spins) {
            unsigned d = 0u;
#pragma unroll
            for (int i = 0; i < 4; ++i) ld_p(pt, 4 * pr + i);
#pragma unroll
            for (int i = 0; i < 4; ++i) d += q[i][0] + q[i][1] + q[i][2] + q[i][3];
            if (!pt) d = sum_rows_u(d);
            if (!__any(d != tag4)) break;
            __builtin_amdgcn_s_sleep(1);
          }
        }
      }
      if (k >= 10 && !pt) { unsigned x = q[(k - 10) & 3][0]; asm volatile("v_cndmask_b32 %0, %0, 0, %1" : "+v"(x) : "s"(hm)); q[(k - 10) & 3][0] = x; }
      if (k >= 10 && k < 13 && pt) { q[(k - 10) % 3][3] = q[3][(k - 10) % 3]; asm volatile("" : "+v"(q[(k - 10) % 3])); }
    };
    sfor<0, 224>([&](auto M_) __attribute__((always_inline)) {
      constexpr int mm = decltype(M_)::value, j = mm >> 5, m = mm & 31, y = m - (32 - 4 * RO);
      constexpr int t = j == 6 && y >= 0 ? 8 - RO + (y % (2 * RO + !RO)) / 2 : m >> 2, r = j == 6 && y >= 0 ? (y & 1) + 2 * (y / (2 * RO + !RO)) : m & 3, f = 8 * j + t;
      constexpr Sch C = j < JF ? S : F, B = j <= JF ? S : F;
      if (t == 0) asm volatile("v_mfma_f32_16x16x32_bf16 %0, %1, %2, 0" : "=&v"(acc2[j & 1][r]) : "v"(zw[f % NW]), "v"(wsp[r][t]), "v"(zw[(f | (f < 8 ? 1 : VM)) % NW]));
      else asm volatile("v_mfma_f32_16x16x32_bf16 %0, %1, %2, %0" : "+v"(acc2[j & 1][r]) : "v"(zw[f % NW]), "v"(wsp[r][t]), "v"(zw[(f | (f < 8 ? 1 : VM)) % NW]));
      SCHED();
      if (C.pr[m] == 1) __builtin_amdgcn_s_setprio(1);
      if ((m == C.b1 && j > 0) || (m == C.b2 && j > 0) || mm == 30) { asm volatile("" ::: "memory"); __builtin_amdgcn_s_waitcnt(0xC07F); RBAR(); SCHED(); }
      sfor<0, 16>([&](auto I_) __attribute__((always_inline)) {
        constexpr int i = decltype(I_)::value;
        if (j > 0 && C.wp[i] == m) { RED_PUT1(j + 1, i); SCHED(); }
        if (j > 0 && i < 8 && C.rp[i & 7] == m) { RED_GET(i & 7); SCHED(); }
      });
      if (j == 5 && m == 20) {
#pragma unroll
        for (int i = 0; i < 3; ++i) { wb[i] = (unsigned)(kh * 1024 + i * 65536) + l16; asm volatile("" : "+v"(wb[i])); }
      }
      if (XE && j == 5 && m == 13) {
        asm volatile("v_lshrrev_b32 %0, 1, %1" : "=v"(l8) : "v"(l16));
        xo = (l8 & 0x180u) + (unsigned)psi((l8 >> 3) & 15) * 8u;
        asm volatile("" : "+v"(xo));
      }
      if (RO && j == 6 && m >= 28) { RED_PUT1(6, 2 * (m - 28)); RED_PUT1(6, 2 * (m - 28) + 1); SCHED(); }
      if (CE && j == 6 && m >= 19 && m < 22) sfor<0, 3>([&](auto K_) __attribute__((always_inline)) { p_chk(0, 0, 3 * (m - 19) + decltype(K_)::value); SCHED(); });
      constexpr int ap[20] = {7, 8, 9, 10, 11, 13, 13, 14, 14, 19, 20, 21, 22, 23, 24, 25, 26, 27, 28, 29};
      sfor<0, NA0>([&](auto N_) __attribute__((always_inline)) { if (j == 6 && ap[AP0 + decltype(N_)::value] == m) { LDA(decltype(N_)::value); SCHED(); } });
      sfor<0, 22>([&](auto K_) __attribute__((always_inline)) {
        constexpr int k = decltype(K_)::value;
        if (j > 1 && B.vp[k] == m + 32) { red_fin(j > 1 ? j - 2 : 0, k, B.vp[16] == B.vp[18]); SCHED(); }
        if (j > 0 && C.vp[k] == m) { red_fin(j > 0 ? j - 1 : 0, k); SCHED(); }
      });
      if (C.pr[m] == 2) __builtin_amdgcn_s_setprio(0);
      if ((m & 3) == C.lp[m >> 2]) {
        if (f > 0 && f - 1 + NW < 56) LDZ(f - 1 + NW);
        if (j == 6 && (m >> 2) >= P1S) ld_p(0, ((m >> 2) - P1S) & 7);
        if (XE && j == 5 && m >= 20) xp[((m >> 2) - 5) % 3] = __builtin_amdgcn_raw_buffer_load_b64(rs, xo + (unsigned)((m >> 2) - 5) * 512u, xs, BULK_AUX);
        SCHED();
      }
    });
    STAMP(15);
#define NM0 0
#define GM 19
#define MB 24
    f32x4 acc[5];
    f32x4 xf[3], gf[3];
    unsigned go, oo, eo, ho, tw, tr, o1, ta, tb;
    const unsigned s2 = (unsigned)(L_MISC + 128 * kh), s4 = s1 - (unsigned)kh * 1024u;
    unsigned long long om = kh ? ~0ull : 0xccccccccccccccccull;
#define PIN2(a, b) asm volatile("" : "+v"(a), "+v"(b))
    auto glue = [&](int k) {
      if (k == 0) { ta = l8 >> 5; tb = l16 & 48u; PIN2(ta, tb); }
      if (k == 1) { tr = __umul24(ta, 112u) + tb; oo = ta & 12u; PIN2(tr, oo); }
      if (k == 2) { tr += s1; oo += ta; PIN2(tr, oo); }
      if (k == 3) { oo = __umul24(oo, 1280u) + tb; ta = l8 >> 4; PIN2(oo, ta); }
      if (k == 4) { ta &= 24u; tb = l16 & 0xf0u; PIN2(ta, tb); }
      if (k == 5) { tw = __umul24(tb, 7u) + ta; go = (ta << 1) + s2; PIN2(tw, go); }
      if (k == 6) { tw += s1; asm volatile("v_cndmask_b32 %0, %1, -2.0, %2" : "=v"(o1) : "v"(oo), "s"(om)); asm volatile("" : "+v"(tw)); }
      if (k == 7) { eo = l16 + s3; ho = l16 + s4; PIN2(eo, ho); }
      if (k >= 8 && k < 14) {
        const unsigned x = xp[((k - 8) >> 1) % 3][k & 1];
        float lo = bflo(x), hi = bfhi(x);
        PIN2(lo, hi);
        xf[((k - 8) >> 1) % 3][2 * (k & 1)] = lo; xf[((k - 8) >> 1) % 3][2 * (k & 1) + 1] = hi;
      }
    };
    auto g3 = [&](auto M0_, auto M1_) __attribute__((always_inline)) {
      sfor<decltype(M0_)::value, decltype(M1_)::value>([&](auto M_) __attribute__((always_inline)) {
        constexpr int mi = decltype(M_)::value;
        constexpr int m = mi < 55 ? mi : mi < 64 ? 55 + 5 * ((mi - 55) / 3) + (mi - 55) % 3 : 58 + 5 * ((mi - 64) / 2) + (mi & 1), u2 = m / 5;
        const u32x4 fb = u2 < 8 ? P[0][u2 & 7] : P[1][u2 < 11 ? (u2 - 8) & 3 : (u2 - 7) & 7];
        if (m < 5) asm volatile("v_mfma_f32_16x16x32_bf16 %0, %1, %2, 0" : "=&v"(acc[m % 5]) : "v"(af[m % NAF]), "v"(fb));
        else asm volatile("v_mfma_f32_16x16x32_bf16 %0, %1, %2, %0" : "+v"(acc[m % 5]) : "v"(af[m % NAF]), "v"(fb));
        SCHED();
        if (mi == NM0) RBAR();
        if (mi >= NM0 && mi < NM0 + 3) dma1k(rs, lds + wl[(mi - NM0) % 3], l16, wg + wl[(mi - NM0) % 3]);
        if (mi >= 5 && mi < 19) p_chk(0, 1, mi - 5);
        if (mi >= GM && mi < GM + 14) glue(mi - GM);
        if (mi >= 47 && mi < 54) { p_chk(1, 1, 2 * (mi - 47)); SCHED(); p_chk(1, 1, 2 * (mi - 47) + 1); }
        if (!XE && mi == 3) { xo = (l8 & 0x180u) + (unsigned)psi((l8 >> 3) & 15) * 8u; asm volatile("" : "+v"(xo)); }
        if (!XE && mi >= 4 && mi < 7) xp[(mi - 4) % 3] = __builtin_amdgcn_raw_buffer_load_b64(rs, xo + (unsigned)(mi - 4) * 512u, xs, BULK_AUX);
        if (mi >= NM0 && mi < 40 && NA1 + mi - NM0 < 60) LDA(NA1 + mi - NM0);
        if (mi >= 66 && mi < 69) *(f32x4*)(lds + eo + (mi - 66) * 1024) = acc[(mi - 66) % 3];
        SCHED();
      });
    };
    __builtin_amdgcn_s_setprio(3);
#pragma unroll
    for (int k = 18; k < 22; ++k) red_fin(5, k);
    if (!XE) asm volatile("v_lshrrev_b32 %0, 1, %1" : "=v"(l8) : "v"(l16));
    if (RO) asm volatile("s_nop 2"); else { RED_PUT(6, 0); RED_PUT(6, 1); }
    RED_PUT(6, 2); RED_PUT(6, 3);
    red_fin(6, 16); red_fin(6, 17);
    SCHED();
#pragma unroll
    for (int i = 8 - P1S; i < 8 - P1S + P1B; ++i) ld_p(0, i);
    SCHED();
    BAR();
    sfor<0, 8>([&](auto I_) __attribute__((always_inline)) { RED_GET(decltype(I_)::value); });
    SCHED();
#pragma unroll
    for (int n = NA0; n < NA1; ++n) LDA(n);
#pragma unroll
    for (int i = 8 - P1S + P1B; i < 8; ++i) ld_p(0, i);
    SCHED();
    sfor<0, 20>([&](auto K_) __attribute__((always_inline)) {
      constexpr int k = decltype(K_)::value;
      if (k < 8 && !(k & 1)) asm volatile("s_waitcnt lgkmcnt(%0)" :: "n"(6 - k + NA1 - NA0) : "memory");
      red_fin(6, k < 16 ? k : k + 2);
    });
    SCHED();
    __builtin_amdgcn_s_setprio(0);
    STAMPT(16);
#pragma unroll
    for (int k = CE ? 9 : 0; k < 14; ++k) p_chk(0, 0, k);
    SCHED();
    g3(IC<0>(), IC<MB>());
    STAMPT(22);
    DRAIN();
    RBAR();
    STAMPT(27);
#define MY 12
    if (MY == 0 || !kh) {
#pragma unroll
      for (int i = 0; i < 8; ++i) { ld_p(1, i); SCHED(); }
    }
    g3(IC<MB>(), IC<MB + MY>());
    if (MY > 0 && kh) {
#pragma unroll
      for (int i = 0; i < 8; ++i) { ld_p(1, i); SCHED(); }
    }
    STAMPT(26);
    g3(IC<MB + MY>(), IC<40>());
    STAMPT(24);
#pragma unroll
    for (int m = NA1 + 40 - NM0; m < 70; ++m) LDA(m);
#pragma unroll
    for (int x = 0; x < 3; ++x) gf[x] = *(const f32x4*)(lds + go + x * 64);
    SCHED();
    const rsrc_t ro = __builtin_amdgcn_make_buffer_rsrc((void*)p.out, 0, S_ * D_ * 2, 0x00020000);
    STAMPT(17);
#pragma unroll
    for (int k = 0; k < 14; ++k) p_chk(1, 0, k);
    asm volatile("s_nop 1");
    STAMPT(25);
    g3(IC<40>(), IC<70>());
    STAMPT(18);
    BAR();
    STAMPT(28);
    f32x4 o4[3];
    u32x4 ov[2];
#pragma unroll
    for (int j = 2; j >= 0; --j) o4[j] = *(const volatile LAS f32x4*)(lds + ho + j * 1024);
    asm volatile("" : "+v"(acc[2]), "+v"(acc[3]), "+v"(acc[4]));
    SCHED();
#pragma unroll
    for (int j = 2; j >= 0; --j) {
      const f32x4 v = __builtin_elementwise_fma(gf[j], o4[j] + acc[2 + j], xf[j]);
      *(volatile LAS u32x2*)(lds + tw + j * 32) = (u32x2){pack2(v[0], v[1]), pack2(v[2], v[3])};
      if (j < 2) ov[j] = *(const volatile LAS u32x4*)(lds + tr + j * 32);
    }
    ST16S(ov[1], ro, oo + 32u, os, 0);
    ST16S(ov[0], ro, o1, os, 0);
    if (bid == 0 && w == 5 && l8 == 0u) st4(rs, WS_EPOCH, tag);
  }
  STAMP(19);
}

extern "C" __device__ __attribute__((used, noinline, visibility("hidden"))) void mega_tail() {}

extern "C" void kernel_launch(void* const* d_in, const int* in_sizes, int n_in, void* d_out, int out_size,
                              void* d_ws, size_t ws_size, hipStream_t stream) {
  Params p;
  p.q = (const u16*)d_in[0];
  p.k = (const u16*)d_in[1];
  p.v = (const u16*)d_in[2];
  p.shift_k = (const u16*)d_in[3];
  p.ssm_k = (const u16*)d_in[4];
  p.ssm_D = (const u16*)d_in[5];
  p.w_in = (const u16*)d_in[7];
  p.w_sp = (const u16*)d_in[8];
  p.b_sp = (const u16*)d_in[9];
  p.w_out = (const u16*)d_in[10];
  p.cvec = (const u16*)d_in[11];
  p.w_mod = (const u16*)d_in[12];
  p.out = (u16*)d_out;
  p.ws = (char*)d_ws;
  mega<<<256, 512, 0, stream>>>(p);
}
```

```cpp
#define LATE_TAG
#define PROBE 10
#include <hip/hip_runtime.h>
#include <stdint.h>

typedef unsigned short u16;
typedef __attribute__((ext_vector_type(8))) short bf16x8;
typedef __attribute__((ext_vector_type(4))) short bf16x4;
typedef __attribute__((ext_vector_type(4))) float f32x4;
typedef __attribute__((ext_vector_type(2))) float f32x2;
typedef __attribute__((ext_vector_type(4))) unsigned int u32x4;
typedef __attribute__((ext_vector_type(2))) unsigned int u32x2;
#define LAS __attribute__((address_space(3)))
typedef __amdgpu_buffer_rsrc_t rsrc_t;
typedef __attribute__((ext_vector_type(4))) short s16x4;

constexpr int S_ = 2048, D_ = 640, F_ = 1728, FH = 864, CB = 108;

constexpr unsigned WS_EPOCH = 0;
constexpr unsigned WS_FWI = 4096;
constexpr unsigned WS_FWO = WS_FWI + 320 * 128;
constexpr unsigned WS_FX = WS_FWO + 64 * 128;
constexpr unsigned WS_FZ = WS_FX + 160 * 128;
constexpr unsigned WS_CS = WS_FZ + 256 * 128;
constexpr unsigned WS_FX8 = 262144;
constexpr unsigned WS_PART = 524288;
constexpr unsigned WS_MOD = WS_PART + 262144;
constexpr unsigned WS_MODP = WS_MOD + 32768;
constexpr unsigned WS_CSP = WS_MODP + 204800;
constexpr unsigned WS_ST1 = WS_CSP + 655360;
constexpr unsigned WS_KVG = WS_ST1 + 8 * 32768;
constexpr unsigned WS_QB = WS_KVG;
constexpr unsigned WS_X1B = WS_QB + 5242880;
constexpr unsigned WBLK = 143360;
constexpr unsigned WS_WINF = WS_X1B + 2621440;
constexpr unsigned WS_WOUTF = WS_WINF + 16 * WBLK;
constexpr unsigned WS_Z2F = WS_WOUTF + 8 * WBLK;
constexpr unsigned WS_UF = WS_Z2F + 8 * 64 * 7168;
constexpr unsigned WS_END = WS_UF + 32 * 4 * 8 * 3584;
constexpr unsigned WS_STAMP = 200u << 20;

constexpr int U_BYTES = (256 + S_) * 2;
constexpr int G_COPY = 4160;
constexpr int CH_BYTES = U_BYTES + 4 * G_COPY + 32;
constexpr int L_X = 4 * CH_BYTES;
constexpr int L_Y = L_X + 32768;
constexpr int L_RT = 16384;
constexpr int L_CONST = WBLK;
constexpr int L_PST = L_CONST + 2048;
constexpr int L_Z = L_CONST + 5120;
constexpr int L_FLG = L_Z;
constexpr int L_ST2 = L_Z + 14336;
constexpr int L_MISC = L_ST2 + 512;
constexpr int LDS_TOTAL = 163840;

#define FX_SLEEP 2
#define AUX_T 250
#define U_AT 17
#define RT2_T 450
#define CB_T 460
#define W_T 720
#define G_DELAY 3
#define CS_T 860
#define B_GO 0
#define EMU 1
#define PF_IDX 31
#define NB2A 4
#define FLAG_GAP 4
#define QPRIO 3
#ifndef M_DELAY
#define M_DELAY 6
#endif
#define TCH_OFF
#define TCH 0
#define TCH_N 2
#define TCH_T 500
constexpr int SC1 = 16;
constexpr unsigned SPIN_CAP = 1u << 16;

__device__ __forceinline__ float bflo(unsigned x) { return __uint_as_float(x << 16); }
__device__ __forceinline__ float bfhi(unsigned x) { return __uint_as_float(x & 0xffff0000u); }
__device__ __forceinline__ unsigned f2bf(float x) {
  __bf16 b = (__bf16)x;
  return (unsigned)__builtin_bit_cast(unsigned short, b);
}
__device__ __forceinline__ float rbf(float x) { return bflo(f2bf(x)); }
typedef __attribute__((ext_vector_type(2))) __bf16 bf2_t;
__device__ __forceinline__ unsigned pack2(float lo, float hi) {
  f32x2 v = {lo, hi};
  return __builtin_bit_cast(unsigned, __builtin_convertvector(v, bf2_t));
}
__device__ __forceinline__ f32x2 rbf2(f32x2 v) {
  const unsigned p = __builtin_bit_cast(unsigned, __builtin_convertvector(v, bf2_t));
  return (f32x2){__uint_as_float(p << 16), __uint_as_float(p & 0xffff0000u)};
}
__device__ __forceinline__ f32x4 rbf4(f32x4 v) {
  const f32x2 a = rbf2((f32x2){v[0], v[1]}), b = rbf2((f32x2){v[2], v[3]});
  return (f32x4){a[0], a[1], b[0], b[1]};
}
template <int CTRL>
__device__ __forceinline__ float dpp_f(float v) {
  return __int_as_float(__builtin_amdgcn_update_dpp(0, __float_as_int(v), CTRL, 0xf, 0xf, true));
}
__device__ __forceinline__ float sum8(float v) {
  v += dpp_f<0xB1>(v);
  v += dpp_f<0x4E>(v);
  v += dpp_f<0x141>(v);
  return v;
}
__device__ __forceinline__ float sum16(float v) {
  v = sum8(v);
  v += dpp_f<0x140>(v);
  return v;
}
__device__ __forceinline__ float sum_rows(float v) {
  auto a = __builtin_amdgcn_permlane16_swap(__float_as_uint(v), __float_as_uint(v), false, false);
  v = __uint_as_float(a[0]) + __uint_as_float(a[1]);
  auto b = __builtin_amdgcn_permlane32_swap(__float_as_uint(v), __float_as_uint(v), false, false);
  return __uint_as_float(b[0]) + __uint_as_float(b[1]);
}
__device__ __forceinline__ float wave_sum(float v) { return sum_rows(sum16(v)); }
__device__ __forceinline__ float gelu_tanh(float x) {
  const float c0 = -2.0f * 0.7978845608028654f * 1.4426950408889634f;
  const float c1 = c0 * 0.044715f;
  float p = __builtin_fmaf(x * x, c1, c0);
  float e = __builtin_amdgcn_exp2f(x * p);
  return x * __builtin_amdgcn_rcpf(1.0f + e);
}
#define MFMA16(a, b, c) __builtin_amdgcn_mfma_f32_16x16x32_bf16(__builtin_bit_cast(bf16x8, a), __builtin_bit_cast(bf16x8, b), c, 0, 0, 0)
#define MFMA16K16(a, b, c) __builtin_amdgcn_mfma_f32_16x16x16bf16_1k(__builtin_bit_cast(bf16x4, a), __builtin_bit_cast(bf16x4, b), c, 0, 0, 0)
__device__ __forceinline__ u32x2 trd(const char* p) {
  return __builtin_bit_cast(u32x2, __builtin_amdgcn_ds_read_tr16_b64_v4i16((LAS s16x4*)p));
}
__device__ __forceinline__ u32x4 trfrag(const char* p, int pitch) {
  u32x2 a = trd(p), b = trd(p + 16 * pitch);
  return (u32x4){a[0], a[1], b[0], b[1]};
}
#define BAR() asm volatile("s_waitcnt lgkmcnt(0)\n\ts_barrier" ::: "memory")
#define DRAIN() do { __builtin_amdgcn_s_waitcnt(0x0F70); asm volatile("" ::: "memory"); } while (0)
#define SCHED() __builtin_amdgcn_sched_barrier(0)
template <int N> struct IC { static constexpr int value = N; };

__device__ __forceinline__ u32x4 ld16(rsrc_t r, unsigned off) { return __builtin_amdgcn_raw_buffer_load_b128(r, off, 0, SC1); }
__device__ __forceinline__ u32x2 ld8(rsrc_t r, unsigned off) { return __builtin_amdgcn_raw_buffer_load_b64(r, off, 0, SC1); }
__device__ __forceinline__ unsigned ld4(rsrc_t r, unsigned off) { return __builtin_amdgcn_raw_buffer_load_b32(r, off, 0, SC1); }
#define ST_AUX 16
__device__ __forceinline__ void st16(rsrc_t r, unsigned off, u32x4 v) { __builtin_amdgcn_raw_buffer_store_b128(v, r, off, 0, ST_AUX); }
__device__ __forceinline__ void st8(rsrc_t r, unsigned off, u32x2 v) { __builtin_amdgcn_raw_buffer_store_b64(v, r, off, 0, SC1); }
__device__ __forceinline__ void st_flag(rsrc_t r, unsigned off, unsigned tag, int l) { if (l < 8) st16(r, off + (unsigned)l * 16u, (u32x4){tag, tag, tag, tag}); }
__device__ __forceinline__ void st4(rsrc_t r, unsigned off, unsigned v) { __builtin_amdgcn_raw_buffer_store_b32(v, r, off, 0, SC1); }
#define BULK_AUX 16
__device__ __forceinline__ u32x4 ld16p(rsrc_t r, unsigned off) { return __builtin_amdgcn_raw_buffer_load_b128(r, off, 0, BULK_AUX); }
__device__ __forceinline__ u32x2 ld8p(rsrc_t r, unsigned off) { return __builtin_amdgcn_raw_buffer_load_b64(r, off, 0, BULK_AUX); }
__device__ __forceinline__ void dma1k(rsrc_t r, char* lds_dst, unsigned l16, unsigned soff) {
  __builtin_amdgcn_raw_ptr_buffer_load_lds(r, (LAS void*)lds_dst, 16, l16, soff, 0, BULK_AUX);
}

#define STAMP(i)

template <int N, int SLP>
__device__ __forceinline__ void wait_lines(rsrc_t r, const unsigned (&off)[N], unsigned tag) {
  bool need[N];
#pragma unroll
  for (int i = 0; i < N; ++i) need[i] = off[i] != ~0u;
  _Pragma("unroll 1") for (unsigned spins = 0; spins < SPIN_CAP; ++spins) {
    unsigned v[N];
#pragma unroll
    for (int i = 0; i < N; ++i) {
      v[i] = tag;
      if (need[i]) v[i] = ld4(r, off[i]);
    }
    bool any = false;
#pragma unroll
    for (int i = 0; i < N; ++i) { need[i] = v[i] != tag; any = any || need[i]; }
    if (!__any(any)) break;
    __builtin_amdgcn_s_sleep(SLP);
  }
  asm volatile("" ::: "memory");
}

__device__ __forceinline__ int tau(int rt, int n) { return 32 * (rt >> 1) + 8 * (n >> 2) + 4 * (rt & 1) + (n & 3); }
__device__ __forceinline__ int psi(int n) { return 2 * (n & 7) + (n >> 3); }
#define QX_CM(d, s0, s1, P) "v_cndmask_b32_dpp " d ", " s0 ", " s1 ", vcc quad_perm:" P " row_mask:0xf bank_mask:0xf\n\t"
#define QX_BODY(M, P) \
  asm("s_mov_b32 vcc_lo, " M "\n\ts_mov_b32 vcc_hi, " M "\n\t" \
      QX_CM("%0", "%12", "%8", P) "v_xor_b32 %4, %8, %0\n\t" QX_CM("%1", "%13", "%9", P) "v_xor_b32 %5, %9, %1\n\t" \
      QX_CM("%2", "%14", "%10", P) "v_xor_b32 %6, %10, %2\n\t" QX_CM("%3", "%15", "%11", P) "v_xor_b32 %7, %11, %3" \
      : "=&v"(t[0]), "=&v"(t[1]), "=&v"(t[2]), "=&v"(t[3]), "=&v"(u[0]), "=&v"(u[1]), "=&v"(u[2]), "=&v"(u[3]) \
      : "v"(a[0]), "v"(a[1]), "v"(a[2]), "v"(a[3]), "v"(b[0]), "v"(b[1]), "v"(b[2]), "v"(b[3]) : "vcc")
template <int BIT>
__device__ __forceinline__ void quad_xchg3(u32x4& a, u32x4& b) {
  unsigned t[4], u[4];
  if (BIT == 0) QX_BODY("0x55555555", "[1,0,3,2]");
  else QX_BODY("0x33333333", "[2,3,0,1]");
  a = (u32x4){t[0], t[1], t[2], t[3]};
#pragma unroll
  for (int d = 0; d < 4; ++d) b[d] ^= (unsigned)__builtin_amdgcn_update_dpp(0, (int)u[d], BIT == 0 ? 0xB1 : 0x4E, 0xf, 0xf, true);
}
__device__ __forceinline__ void quad_transpose(u32x4& x0, u32x4& x1, u32x4& x2, u32x4& x3) {
  quad_xchg3<0>(x0, x1);
  quad_xchg3<0>(x2, x3);
  quad_xchg3<1>(x0, x2);
  quad_xchg3<1>(x1, x3);
  asm volatile("" : "+v"(x0), "+v"(x1), "+v"(x2), "+v"(x3));
}

struct Params {
  const u16 *q, *k, *v, *shift_k, *ssm_k, *ssm_D, *w_in, *w_sp, *b_sp, *w_out, *cvec, *w_mod;
  u16* out;
  char* ws;
};

__global__ __launch_bounds__(512, 2) void mega(Params p) {
  __shared__ __attribute__((aligned(16))) char lds[LDS_TOTAL];
  unsigned long long pr0;
  asm volatile("s_memrealtime %0" : "=s"(pr0));
  SCHED();
  const int tid = threadIdx.x, l = tid & 63;
  const int w = __builtin_amdgcn_readfirstlane(tid >> 6);
  const int bid = blockIdx.x, xcd = bid & 7, idx = bid >> 3;
  char* const ws = p.ws;
  asm volatile("" ::"s"(p.q), "s"(p.k), "s"(p.v), "s"(p.shift_k), "s"(p.ssm_k), "s"(p.ssm_D), "s"(p.w_in), "s"(p.w_sp), "s"(p.b_sp),
               "s"(p.w_out), "s"(p.cvec), "s"(p.w_mod), "s"(p.out), "s"(p.ws));
  unsigned epoch;
  asm volatile("s_load_dword %0, %1, 0x0" : "=s"(epoch) : "s"(ws));
  const rsrc_t rs = __builtin_amdgcn_make_buffer_rsrc(ws, 0, WS_END, 0x00020000);
  const unsigned l16 = (unsigned)l * 16u;
  STAMP(0);
  const unsigned t_start = (unsigned)__builtin_amdgcn_s_memrealtime();
  const unsigned long long prc = __builtin_amdgcn_s_memtime();
  unsigned long long pc0;
  unsigned clen;
  asm volatile("s_getpc_b64 %0\n.Lmega_here:\n\ts_mov_b32 %1, mega_tail-.Lmega_here" : "=s"(pc0), "=s"(clen));
  auto code_pf = [&](auto K0, auto K1) {
    const rsrc_t rc = __builtin_amdgcn_make_buffer_rsrc((void*)(pc0 & ~15ull), 0, clen & ~15u, 0x00020000);
#pragma unroll
    for (int k = decltype(K0)::value; k < decltype(K1)::value; ++k) {
      unsigned o = (k < 3 ? (unsigned)k * 4096u : k < 7 ? (clen & ~15u) - (unsigned)(k - 2) * 4096u : (unsigned)(k - 4) * 4096u) + (unsigned)(w * 1024 + l * 16);
      __builtin_amdgcn_raw_ptr_buffer_load_lds(rc, (LAS void*)(lds + 8192), 16, o, 0, 0, 0);
    }
  };
  if (w < 4 && idx == PF_IDX) code_pf(IC<0>(), IC<15>());
  unsigned tag;
#define TAG_READY() do { SCHED(); asm volatile("s_waitcnt lgkmcnt(0)\n\ts_mul_i32 %0, %1, 0x9e3779b1\n\ts_add_i32 %0, %0, 0x9e3779b9" : "=s"(tag) : "s"(epoch) : "scc"); SCHED(); } while (0)

  const unsigned pr3 = 0;
  const bool is_conv = idx < 20;
  const int quad = xcd * 20 + idx;
  const int R = (xcd & 3) * 8 + (idx & 7);
  const int c = (xcd >> 2) * 4 + (idx >> 3);
  const int n16 = l & 15, h4 = l >> 4;
  const int rp = w & 1, kq = w >> 1;
  u32x4 wsp[2][16];
  const int rot = 4 * (idx & 3);
  const rsrc_t rsp = __builtin_amdgcn_make_buffer_rsrc((void*)p.w_sp, 0, S_ * S_ * 2, 0x00020000);
  const unsigned wsp_vo = (unsigned)(((l >> 2) & 3) * 8 * 4096 + h4 * 64 + (l & 3) * 16);
#define LDW(k) wsp[((k) >> 2) & 1][((k) >> 3) * 4 + ((k) & 3)] = __builtin_amdgcn_raw_buffer_load_b128(rsp, wsp_vo, \
    (unsigned)((R * 64 + 32 * rp + 4 * (((k) >> 2) & 1) + ((k) & 3)) * 4096 + kq * 1024 + (((((k) >> 3) * 4 + rot) & 15) * 64)), 0)
#define WSP_TR(g) quad_transpose(wsp[(g) & 1][((g) >> 1) * 4], wsp[(g) & 1][((g) >> 1) * 4 + 1], wsp[(g) & 1][((g) >> 1) * 4 + 2], wsp[(g) & 1][((g) >> 1) * 4 + 3])
#define TOUCH()
#define TOUCH_END()
  const rsrc_t rwi = __builtin_amdgcn_make_buffer_rsrc((void*)p.w_in, 0, D_ * F_ * 2, 0x00020000);
  const rsrc_t rwo = __builtin_amdgcn_make_buffer_rsrc((void*)p.w_out, 0, FH * D_ * 2, 0x00020000);
  auto dma_b2 = [&](int k0, int k1) {
#pragma unroll
    for (int k = k0; k < k1; ++k) {
      int m = k + (k1 - k0 >= 8 ? (idx & 7) : 0);
      m = m >= k1 ? m - (k1 - k0) : m;
      dma1k(rs, lds + (m * 7 + w) * 1024, l16, WS_WINF + (unsigned)(8 + c) * WBLK + (unsigned)(m * 7 + w) * 1024u);
    }
  };
  auto poison_u = [&]() {
#pragma unroll
    for (int k = 0; k < 7; ++k) {
      const int i = w * 7 + k;
      st16(rs, WS_UF + (unsigned)((R * 4 + i / 14) * 8 + c - 3) * 3584u + (unsigned)(i % 14) * 1024u + l16, (u32x4){~0u, ~0u, ~0u, ~0u});
    }
  };
  auto wait_weights = [&]() {
    const unsigned fo[1] = {l < 20 ? WS_FWI + (unsigned)((8 + c) * 20 + l) * 128u : ~0u};
    wait_lines<1, 8>(rs, fo, tag);
  };
  auto rt_job = [&](int i0) {
    char* st = lds + L_RT + w * 18432;
    if (i0 < 320) {
      const int cb = (i0 < 160 ? 8 : 0) + (i0 & 7), Q = (i0 < 160 ? i0 : i0 - 160) >> 3;
#pragma unroll
      for (int r = 0; r < 7; ++r) {
        const int g = r * 64 + l;
        __builtin_amdgcn_raw_ptr_buffer_load_lds(rwi, (LAS void*)(st + r * 1024), 16, (unsigned)((g / 14) * (F_ * 2) + (g % 14) * 16 + Q * 32 * F_ * 2 + cb * CB * 2),
                                                 0, 0, 0);
      }
      const int col = 1920 + 640 * (l >> 5) + 32 * Q + (l & 31);
      const unsigned po = WS_MODP + (unsigned)col * 8u;
      u32x2 pp8[8];
      while ((unsigned)__builtin_amdgcn_s_memrealtime() - t_start < CB_T) __builtin_amdgcn_s_sleep(2);
      _Pragma("unroll 1") for (unsigned spins = 0; spins < SPIN_CAP; ++spins) {
        unsigned dd = 0u;
#pragma unroll
        for (int i = 0; i < 8; ++i) pp8[i] = ld8(rs, po + (unsigned)i * 25600u);
#pragma unroll
        for (int i = 0; i < 8; ++i) dd |= pp8[i][0] ^ tag;
        if (__all(dd == 0u)) break;
        __builtin_amdgcn_s_sleep(4);
      }
      {
        float t = 0.f;
#pragma unroll
        for (int i = 0; i < 8; ++i) t += __uint_as_float(pp8[i][1]);
        if (l >= 32) *(float*)(st + 8192 + (l - 32) * 4) = rbf(1.0f + rbf(t));
        else *(u16*)(st + 8320 + l * 2) = (u16)f2bf(t);
      }
      DRAIN();
      asm volatile("s_waitcnt lgkmcnt(0)" ::: "memory");
      const f32x4 ca = *(const f32x4*)(st + 8192 + h4 * 16), cc = *(const f32x4*)(st + 8192 + 64 + h4 * 16);
      const char* bp = st + (4 * h4 + (n16 >> 2)) * 224 + (n16 & 3) * 8;
      u32x4 x[7], o[7];
#pragma unroll
      for (int j = 0; j < 7; ++j) {
        x[j] = trfrag(bp + j * 32, 224);
        o[j] = (u32x4){pack2(bflo(x[j][0]) * ca[0], bfhi(x[j][0]) * ca[1]), pack2(bflo(x[j][1]) * ca[2], bfhi(x[j][1]) * ca[3]),
                       pack2(bflo(x[j][2]) * cc[0], bfhi(x[j][2]) * cc[1]), pack2(bflo(x[j][3]) * cc[2], bfhi(x[j][3]) * cc[3])};
        if (j == 6 && n16 == 12) o[j] = (u32x4){0x3F803F80u, 0x3F803F80u, 0x3F803F80u, 0x3F803F80u};
        st16(rs, WS_WINF + (unsigned)cb * WBLK + (unsigned)(Q * 7 + j) * 1024u + l16, o[j]);
      }
      const u32x2 e0 = *(const u32x2*)(st + 8320 + h4 * 8), e1 = *(const u32x2*)(st + 8352 + h4 * 8);
      f32x4 d[2] = {{0.f, 0.f, 0.f, 0.f}, {0.f, 0.f, 0.f, 0.f}};
#pragma unroll
      for (int j = 0; j < 7; ++j) {
        const unsigned one = n16 == 4 * (j & 3) ? 0x3F803F80u : 0u;
        const bool sr = n16 == 4 * (j & 3) + 1;
        d[j >> 2] = MFMA16(((u32x4){one, one, one, one}), o[j], d[j >> 2]);
        d[j >> 2] = MFMA16(((u32x4){sr ? e0[0] : 0u, sr ? e0[1] : 0u, sr ? e1[0] : 0u, sr ? e1[1] : 0u}), x[j], d[j >> 2]);
      }
#pragma unroll
      for (int m = 0; m < 2; ++m)
        st16(rs, WS_CSP + (unsigned)((cb * 20 + Q) * 2 + m) * 1024u + l16, (u32x4){tag, __float_as_uint(d[m][0]), tag, __float_as_uint(d[m][1])});
      asm volatile("s_waitcnt vmcnt(2)" ::: "memory");
      st_flag(rs, WS_FWI + (unsigned)(cb * 20 + Q) * 128u, tag, l);
    } else if (i0 < 384) {
      const int v = i0 - 320, cc = v >> 3, c2 = v & 7;
#pragma unroll
      for (int r = 0; r < 18; ++r) {
        const int g = r * 64 + l;
        __builtin_amdgcn_raw_ptr_buffer_load_lds(rwo, (LAS void*)(st + r * 1024), 16, (unsigned)((g / 10) * (D_ * 2) + (g % 10) * 16 + c2 * CB * D_ * 2 + cc * 160),
                                                 0, 0, 0);
      }
      DRAIN();
      const char* bp = st + (4 * h4 + (n16 >> 2)) * 160 + (n16 & 3) * 8;
      const unsigned base = WS_WOUTF + (unsigned)cc * WBLK + (unsigned)c2 * 17920u;
#pragma unroll
      for (int qq = 0; qq < 3; ++qq)
#pragma unroll
        for (int dj = 0; dj < 5; ++dj) st16(rs, base + (unsigned)(qq * 5 + dj) * 1024u + l16, trfrag(bp + qq * 5120 + dj * 32, 160));
#pragma unroll
      for (int dj = 0; dj < 5; ++dj) st8(rs, base + 15360u + (unsigned)dj * 512u + (unsigned)l * 8u, trd(bp + 15360 + dj * 32));
      DRAIN();
      st_flag(rs, WS_FWO + (unsigned)v * 128u, tag, l);
    }
  };

  auto red8 = [&](float v) { v += dpp_f<0x128>(v); return sum_rows(v); };
  const rsrc_t rwm = __builtin_amdgcn_make_buffer_rsrc((void*)p.w_mod, 0, D_ * 3840 * 2, 0x00020000);
  auto gemv_load = [&](u32x4* pre, int blk, int sl) {
    pre[10][0] = *(const unsigned*)(p.cvec + 80 * sl + 2 * (l < 40 ? l : 0));
#pragma unroll
    for (int pp = 0; pp < 10; ++pp)
      pre[pp] = __builtin_amdgcn_raw_buffer_load_b128(rwm, (unsigned)((l >> 3) * 7680 + (l & 7) * 16), (unsigned)((80 * sl + pp * 8) * 7680 + blk * 128), 0);
  };
  auto gemv_fin = [&](float* acc, const u32x4* pre) {
    unsigned* svt = (unsigned*)(lds + 4096) + w * 96;
#pragma unroll
    for (int e = 0; e < 2; ++e) {
      const int row = 2 * l + e;
      const float cv = e ? bfhi(pre[10][0]) : bflo(pre[10][0]);
      if (l < 40) svt[(row & 7) * 12 + (row >> 3)] = __float_as_uint(rbf(cv * __builtin_amdgcn_rcpf(1.0f + __expf(-cv))));
    }
    asm volatile("s_waitcnt lgkmcnt(0)" ::: "memory");
    u32x4 sv[3];
#pragma unroll
    for (int i = 0; i < 3; ++i) sv[i] = *(const u32x4*)(svt + (l >> 3) * 12 + 4 * i);
    SCHED();
#pragma unroll
    for (int e = 0; e < 8; ++e) acc[e] = 0.f;
#pragma unroll
    for (int pp = 0; pp < 10; ++pp) {
      const unsigned sh = sv[pp >> 2][pp & 3], sl = sh >> 16;
#pragma unroll
      for (int e = 0; e < 4; ++e) {
        const unsigned x = pre[pp][e];
        acc[2 * e] = __builtin_amdgcn_fdot2_f32_bf16(__builtin_bit_cast(bf2_t, x), __builtin_bit_cast(bf2_t, sl), acc[2 * e], false);
        acc[2 * e + 1] = __builtin_amdgcn_fdot2_f32_bf16(__builtin_bit_cast(bf2_t, x), __builtin_bit_cast(bf2_t, sh), acc[2 * e + 1], false);
      }
    }
#pragma unroll
    for (int e = 0; e < 8; ++e) acc[e] = red8(acc[e]);
  };
  auto gemv_put = [&](const u32x4* pre, int blk, int sl) {
    float acc[8];
    gemv_fin(acc, pre);
    float v = acc[0];
#pragma unroll
    for (int e = 1; e < 8; ++e) v = (l >> 3) == e ? acc[e] : v;
    st8(rs, WS_MODP + (unsigned)(sl * 3200 + blk * 64 + l) * 8u,
        (u32x2){tag, (unsigned)__builtin_amdgcn_ds_bpermute(((l & 7) * 8 + (l >> 3)) * 4, (int)__float_as_uint(v))});
  };
  const int a1 = xcd * 10 + idx - 20;
  typedef const __attribute__((address_space(4))) unsigned* cptr_t;
  const bf2_t ONE2 = {(__bf16)1.0f, (__bf16)1.0f};

  if (__builtin_expect(is_conv, 1)) {
    const int ch = w & 3, hf = w >> 2;
    char* U = lds + ch * CH_BYTES;
    char* G = U + U_BYTES;
    volatile LAS unsigned* lf = (volatile LAS unsigned*)(lds + L_FLG);
    auto lf_set = [&](int i) { asm volatile("s_waitcnt lgkmcnt(0)" ::: "memory"); if (l == 0) lf[i] = tag; };
    auto lf_wait = [&](int i0, int n) {
      _Pragma("unroll 1") for (unsigned spins = 0; spins < (SPIN_CAP << 4); ++spins) {
        const unsigned v = lf[i0 + (l < n ? l : 0)];
        if (__all(v == tag)) break;
        __builtin_amdgcn_s_sleep(1);
      }
      asm volatile("" ::: "memory");
    };
    const rsrc_t rk = __builtin_amdgcn_make_buffer_rsrc((void*)p.k, 0, S_ * D_ * 2, 0x00020000);
    const rsrc_t rv = __builtin_amdgcn_make_buffer_rsrc((void*)p.v, 0, S_ * D_ * 2, 0x00020000);
    if (hf) {
      const rsrc_t rf = __builtin_amdgcn_make_buffer_rsrc((void*)(p.ssm_k + (size_t)(quad * 4 + ch) * S_), 0, S_ * 2, 0x00020000);
      u32x4 fa[4];
      u32x2 fb[4];
      __builtin_amdgcn_s_setprio(3);
#pragma unroll
      for (int k = 0; k < 4; ++k) {
        fa[k] = __builtin_amdgcn_raw_buffer_load_b128(rf, l16 + (unsigned)k * 1024u, 0, 0);
        fb[k] = __builtin_amdgcn_raw_buffer_load_b64(rf, l16 + (unsigned)k * 1024u + 16u, 0, 0);
      }
      __builtin_amdgcn_s_setprio(0);
      TAG_READY();
      SCHED();
#pragma unroll
      for (int k = 0; k < 4; ++k) {
        const unsigned x[6] = {fa[k][0], fa[k][1], fa[k][2], fa[k][3], fb[k][0], fb[k][1]};
        unsigned y[5];
#pragma unroll
        for (int d = 0; d < 5; ++d) y[d] = __builtin_amdgcn_alignbit(x[d + 1], x[d], 16);
        char* gp = G + 32 + k * 1024 + l16;
        *(u32x4*)(gp) = fa[k];
        *(u32x4*)(gp + G_COPY) = (u32x4){y[0], y[1], y[2], y[3]};
        *(u32x4*)(gp + 2 * G_COPY) = (u32x4){x[1], x[2], x[3], x[4]};
        *(u32x4*)(gp + 3 * G_COPY) = (u32x4){y[1], y[2], y[3], y[4]};
        if (k == 0) {
          const unsigned f0 = __builtin_amdgcn_readfirstlane(x[0]), f1 = __builtin_amdgcn_readfirstlane(x[1]);
          const unsigned long long f64 = (unsigned long long)f0 | ((unsigned long long)f1 << 32);
          const unsigned long long hi = l ? (f64 << (64 - 16 * (l & 3))) : 0ull;
          if (l < 4) {
            *(u32x4*)(G + l * G_COPY) = (u32x4){0u, 0u, 0u, 0u};
            *(u32x4*)(G + l * G_COPY + 16) = (u32x4){0u, 0u, (unsigned)hi, (unsigned)(hi >> 32)};
          }
          if (l >= 32) *(u32x4*)(U + (l - 32) * 16) = (u32x4){0u, 0u, 0u, 0u};
        }
      }
      lf_set(ch);
      STAMP(1);
    } else {
      __builtin_amdgcn_s_sleep(G_DELAY);
      __builtin_amdgcn_s_setprio(2);
    }
    u32x2 kk[7], vv[4], kh;
    auto gather_all = [&](bool first) {
      kh = __builtin_amdgcn_raw_buffer_load_b64(rk, w == 0 || l > 2 ? 0x80000000u : (unsigned)((256 * w - 3 + l) * D_ * 2 + quad * 8), 0, 0);
#pragma unroll
      for (int x = 0; x < 8; ++x) {
        if (x == 4 + B_GO) { SCHED(); if (first) TAG_READY(); if (l == 0) lf[24 + w] = tag; }
        SCHED();
        const unsigned so = (unsigned)(((x + idx) & 3) * D_ * 2 + quad * 8);
        if (x < 4) kk[3 + x] = __builtin_amdgcn_raw_buffer_load_b64(rk, (unsigned)(tid * 4 * D_ * 2), so, 0);
        else vv[x - 4] = __builtin_amdgcn_raw_buffer_load_b64(rv, (unsigned)(tid * 4 * D_ * 2), so, 0);
      }
      SCHED();
      STAMP(21);
    };
    auto unrot = [&](u32x2* x) {
      if (idx & 1) { asm volatile(""); const u32x2 t = x[3]; x[3] = x[2]; x[2] = x[1]; x[1] = x[0]; x[0] = t; }
      asm volatile("" : "+v"(x[0]), "+v"(x[1]), "+v"(x[2]), "+v"(x[3]));
      if (idx & 2) { const u32x2 t = x[0], u = x[1]; x[0] = x[2]; x[1] = x[3]; x[2] = t; x[3] = u; }
      asm volatile("" : "+v"(x[0]), "+v"(x[1]), "+v"(x[2]), "+v"(x[3]));
    };
    if (hf == 0) gather_all(true);
    const int ip = (tid & 255) + 512 * hf;
    const int ra = 32 * (ip >> 4) + 8 * ((ip >> 2) & 1) + 4 * ((ip >> 3) & 1) + (ip & 3);
    u32x4 qv[4], sv[4];
    u32x2 mp[2];
    const unsigned eo_s = (unsigned)(ra * 16), eo_q = (unsigned)((ra >> 3) * 20480 + (ra & 7) * 16);
    unsigned eo_m;
    {
      const int col = (n16 < 12 ? n16 >> 2 : 0) * D_ + quad * 4 + (l & 3);
      eo_m = (unsigned)col * 8u + (unsigned)h4 * 51200u;
    }
    auto ld_e = [&]() {
#pragma unroll
      for (int j = 0; j < 4; ++j) {
        const unsigned tb = 2 * (j & 1) + 64 * (j >> 1);
        qv[j] = __builtin_amdgcn_raw_buffer_load_b128(rs, eo_q, WS_QB + tb * 20480u + (unsigned)quad * 128u, SC1);
        sv[j] = __builtin_amdgcn_raw_buffer_load_b128(rs, eo_s, WS_ST1 + (unsigned)xcd * 32768u + tb * 128u, SC1);
        if (j < 2) mp[j] = __builtin_amdgcn_raw_buffer_load_b64(rs, eo_m, WS_MODP + (unsigned)j * 25600u, SC1);
      }
    };
    auto e_ok = [&]() {
      unsigned d = 0u;
#pragma unroll
      for (int j = 0; j < 4; ++j) {
        asm volatile("" : "+v"(qv[j]), "+v"(sv[j]), "+v"(mp[j & 1]));
        d |= ((sv[j][1] ^ tag) | (mp[j & 1][0] ^ tag)) | ((qv[j][1] ^ tag) | (qv[j][3] ^ tag));
        asm volatile("" : "+v"(d));
      }
      return __all(d == 0u);
    };
    f32x2 hh[8], qf[8], gam[2];
#define PIN8(x) asm volatile("" : "+v"(x[0]), "+v"(x[1]), "+v"(x[2]), "+v"(x[3]), "+v"(x[4]), "+v"(x[5]), "+v"(x[6]), "+v"(x[7]))
    auto epi_pre = [&]() {
      _Pragma("unroll 1") for (unsigned spins = 0; spins < SPIN_CAP; ++spins) {
        if (e_ok()) break;
        __builtin_amdgcn_s_sleep(4);
        ld_e();
      }
      float mgf = rbf(sum_rows(__uint_as_float(mp[0][1]) + __uint_as_float(mp[1][1])));
      mgf = (n16 >> 2) == 1 ? rbf(1.0f + mgf) : mgf;
      auto rl = [&](int i) { return __uint_as_float(__builtin_amdgcn_readlane(__float_as_uint(mgf), i)); };
      const f32x2 sa[2] = {{rl(0), rl(1)}, {rl(2), rl(3)}}, c1[2] = {{rl(4), rl(5)}, {rl(6), rl(7)}};
      gam[0] = (f32x2){rl(8), rl(9)}; gam[1] = (f32x2){rl(10), rl(11)};
#pragma unroll
      for (int i = 0; i < 8; ++i) hh[i] = rbf2((f32x2){bflo(qv[i >> 1][2 * (i & 1)]), bfhi(qv[i >> 1][2 * (i & 1)])} - bflo(sv[i >> 1][0]));
      PIN8(hh);
#pragma unroll
      for (int i = 0; i < 8; ++i) hh[i] = rbf2(hh[i] * __builtin_amdgcn_rcpf(bfhi(sv[i >> 1][0])));
      PIN8(hh);
#pragma unroll
      for (int i = 0; i < 8; ++i) hh[i] = rbf2(hh[i] * c1[i & 1]);
      PIN8(hh);
#pragma unroll
      for (int i = 0; i < 8; ++i) hh[i] = rbf2(hh[i] + sa[i & 1]);
      PIN8(hh);
    };
    const int a = l & 15, h = l >> 4, jj = h >> 1, e2 = h & 1;
    const char* ap = G + ((a + 1) & 3) * G_COPY + 2 * (16 * jj + a - 8 * e2 + 9 - ((a + 1) & 3));
    const char* bp = U + 512 + 32 * (a - jj) + 16 * e2 + 2048 * hf;
    auto lda = [&](u32x4* A, int r, int np) {
#pragma unroll
      for (int pp = 0; pp < np; ++pp) {
        u32x2 lo = *(const volatile LAS u32x2*)(ap + 512 * pp + 64 * r);
        u32x2 hi = *(const volatile LAS u32x2*)(ap + 512 * pp + 64 * r + 8);
        A[pp] = (u32x4){lo[0], lo[1], hi[0], hi[1]};
      }
    };
    auto ldb = [&](u32x4* B, int r) {
#pragma unroll
      for (int m = 0; m < 4; ++m) B[m] = *(const u32x4*)(bp + 512 * m - 64 * r);
    };
    auto kv_put = [&]() {
      __builtin_amdgcn_s_waitcnt(0x0F74);
      unrot(kk + 3);
#pragma unroll
      for (int j = 0; j < 3; ++j)
#pragma unroll
        for (int d = 0; d < 2; ++d)
          kk[j][d] = __builtin_amdgcn_update_dpp((unsigned)__builtin_amdgcn_readlane(kh[d], j), kk[4 + j][d], 0x138, 0xf, 0xf, false);
      const cptr_t sp = (cptr_t)(p.shift_k + quad * 16);
      char* up = lds + 512 + (tid >> 1) * 16 + (1 - (tid & 1)) * 8;
      STAMP(23);
      f32x2 oo[2][4];
#pragma unroll
      for (int d = 0; d < 2; ++d) {
        f32x2 kf[7], wt[4];
        f32x2* o = oo[d];
#pragma unroll
        for (int j = 0; j < 7; ++j) kf[j] = (f32x2){bflo(kk[j][d]), bfhi(kk[j][d])};
        wt[0] = (f32x2){bflo(sp[4 * d]), bflo(sp[4 * d + 2])}; wt[1] = (f32x2){bfhi(sp[4 * d]), bfhi(sp[4 * d + 2])};
        wt[2] = (f32x2){bflo(sp[4 * d + 1]), bflo(sp[4 * d + 3])}; wt[3] = (f32x2){bfhi(sp[4 * d + 1]), bfhi(sp[4 * d + 3])};
#pragma unroll
        for (int i = 0; i < 4; ++i) o[i] = rbf2(kf[i] * wt[3]);
#pragma unroll
        for (int t = 1; t < 4; ++t) {
          f32x2 pr[4];
#pragma unroll
          for (int i = 0; i < 4; ++i) pr[i] = rbf2(kf[i + t] * wt[3 - t]);
          asm volatile("" : "+v"(pr[0]), "+v"(pr[1]), "+v"(pr[2]), "+v"(pr[3]));
#pragma unroll
          for (int i = 0; i < 4; ++i) o[i] = rbf2(o[i] + pr[i]);
          asm volatile("" : "+v"(o[0]), "+v"(o[1]), "+v"(o[2]), "+v"(o[3]));
        }
      }
      SCHED();
      __builtin_amdgcn_s_waitcnt(0x0F70);
      unrot(vv);
#pragma unroll
      for (int d = 0; d < 2; ++d) {
        f32x2* o = oo[d];
#pragma unroll
        for (int i = 0; i < 4; ++i) o[i] *= (f32x2){bflo(vv[i][d]), bfhi(vv[i][d])};
#pragma unroll
        for (int e = 0; e < 2; ++e) *(u32x2*)(up + (2 * d + e) * CH_BYTES) = (u32x2){pack2(o[3][e], o[2][e]), pack2(o[1][e], o[0][e])};
      }
      lf_set(4 + w);
    };
    u32x4 AD;
    {
      const unsigned dDr = (*(cptr_t)(p.ssm_D + quad * 4 + (ch & 2)) >> (16 * (ch & 1))) & 0xffffu;
      const unsigned dv = h == (a >> 3) ? ((a & 1) ? dDr : dDr << 16) : 0u;
      const int dw = (7 - (a & 7)) >> 1;
#pragma unroll
      for (int d = 0; d < 4; ++d) AD[d] = dw == d ? dv : 0u;
    }
    f32x4* X = (f32x4*)(lds + L_X + ch * 4096) + l;
    char* Y = lds + L_Y + ch * 4096 + (16 * a + 4 * h) * 2 + 2048 * hf;
    if (hf == 0) {
      u32x4 A[3][8], B[2][4];
      f32x4 acc[8];
      lf_wait(ch, 1);
      lda(A[0], 0, 8);
      lda(A[1], 1, 8);
      SCHED();
      kv_put();
      STAMP(2);
      lf_wait(4, 4);
      STAMP(3);
      if (TCH & 2) { SCHED(); TOUCH(); SCHED(); }
      ldb(B[0], 0);
      ldb(B[1], 1);
      SCHED();
#pragma unroll
      for (int n = 0; n < 8; ++n) acc[n] = n < 4 ? MFMA16(AD, B[0][n], ((f32x4){0.f, 0.f, 0.f, 0.f})) : (f32x4){0.f, 0.f, 0.f, 0.f};
#pragma unroll
      for (int r = 0; r < 8; ++r) {
        if (r + 2 < 8) lda(A[(r + 2) % 3], r + 2, 8);
        SCHED();
#pragma unroll
        for (int m = 0; m < 4; ++m)
#pragma unroll
          for (int pp = 0; pp < 8 - m; ++pp) acc[m + pp] = MFMA16(A[r % 3][pp], B[r & 1][m], acc[m + pp]);
        SCHED();
        if (r + 2 < 8) ldb(B[r & 1], r + 2);
      }
      __builtin_amdgcn_s_setprio(0);
      ld_e();
      if (TCH & 1) { SCHED(); TOUCH(); SCHED(); }
#pragma unroll
      for (int n = 0; n < 4; ++n) X[n * 64] = acc[4 + n];
      lf_set(12 + ch);
#pragma unroll
      for (int n = 0; n < 4; ++n) *(u32x2*)(Y + n * 512) = (u32x2){pack2(acc[n][0], acc[n][1]), pack2(acc[n][2], acc[n][3])};
      lf_set(16 + w);
      STAMP(4);
      epi_pre();
    } else {
      u32x4 A[8][4], B[2][4];
      f32x4 acc[4];
      lf_wait(24, 4);
      gather_all(false);
      __builtin_amdgcn_s_setprio(3);
#pragma unroll
      for (int r = 0; r < 8; ++r) lda(A[r], r, 4);
      SCHED();
      kv_put();
      ld_e();
      if (TCH & 3) { SCHED(); TOUCH(); SCHED(); }
      STAMP(2);
      lf_wait(7, 5);
      STAMP(3);
      ldb(B[0], 0);
      ldb(B[1], 1);
      SCHED();
#pragma unroll
      for (int n = 0; n < 4; ++n) acc[n] = MFMA16(AD, B[0][n], ((f32x4){0.f, 0.f, 0.f, 0.f}));
#pragma unroll
      for (int r = 0; r < 8; ++r) {
        SCHED();
#pragma unroll
        for (int m = 0; m < 4; ++m)
#pragma unroll
          for (int pp = 0; pp < 4 - m; ++pp) acc[m + pp] = MFMA16(A[r][pp], B[r & 1][m], acc[m + pp]);
        SCHED();
        if (r + 2 < 8) ldb(B[r & 1], r + 2);
      }
      STAMP(4);
      lf_wait(12 + ch, 1);
      f32x4 o4[4];
#pragma unroll
      for (int n = 0; n < 4; ++n) o4[n] = X[n * 64];
      SCHED();
#pragma unroll
      for (int n = 0; n < 4; ++n)
        *(u32x2*)(Y + n * 512) = (u32x2){pack2(o4[n][0] + acc[n][0], o4[n][1] + acc[n][1]), pack2(o4[n][2] + acc[n][2], o4[n][3] + acc[n][3])};
      lf_set(16 + w);
      epi_pre();
    }
    if (w == 3) wait_weights();
    STAMP(24);
    lf_wait(16 + 4 * hf, 4);
    STAMP(26);
    {
      unsigned yy[16];
#pragma unroll
      for (int i = 0; i < 16; ++i) yy[i] = *(const u16*)(lds + L_Y + (i & 3) * 4096 + (ra + 16 * ((i >> 2) & 1) + 512 * (i >> 3)) * 2);
      SCHED();
#pragma unroll
      for (int i = 0; i < 8; ++i) hh[i] = rbf2(hh[i] * (f32x2){bflo(yy[2 * i]), bflo(yy[2 * i + 1])});
      PIN8(hh);
#pragma unroll
      for (int i = 0; i < 8; ++i) hh[i] = rbf2(hh[i] * gam[i & 1]);
      PIN8(hh);
#pragma unroll
      for (int i = 0; i < 8; ++i) hh[i] += (f32x2){bflo(qv[i >> 1][2 * (i & 1)]), bfhi(qv[i >> 1][2 * (i & 1)])};
#pragma unroll
      for (int j = 0; j < 2; ++j) {
        u32x4 o;
#pragma unroll
        for (int d = 0; d < 4; ++d) o[d] = pack2(hh[4 * j + d][0], hh[4 * j + d][1]);
        const int i = ip + 256 * j;
        st16(rs, WS_X1B + (unsigned)(i >> 3) * 20480u + (unsigned)quad * 128u + (unsigned)(i & 7) * 16u, o);
      }
    }
    __builtin_amdgcn_s_setprio(0);
    SCHED();
    STAMP(28);
    BAR();
    if (w < 7) dma_b2(0, NB2A);
    asm volatile("s_waitcnt vmcnt(%0)" ::"n"(NB2A) : "memory");
    if (w == 7) DRAIN();
    TOUCH_END();
    __builtin_amdgcn_s_barrier();
    asm volatile("" ::: "memory");
    if (w == 7) st16(rs, WS_FX8 + (unsigned)((l >> 3) * 160 + quad) * 128u + (unsigned)(l & 7) * 16u, (u32x4){tag, tag, tag, tag});
    else __builtin_amdgcn_s_sleep(FLAG_GAP);
    STAMP(5);
    if (w < 7) dma_b2(NB2A, 20);
  } else {
    float* red = (float*)lds;
    auto hold = [&](unsigned tt = AUX_T) { while ((unsigned)__builtin_amdgcn_s_memrealtime() - t_start < tt) __builtin_amdgcn_s_sleep(2); };
    const int ag = xcd * 2 + idx - 30;
    const int jb = idx < 31 ? ((idx - 20) * 8 + xcd) * 3 + w - 5 : 256;
    if (w >= 5 && jb < 256) {
      const rsrc_t rq = __builtin_amdgcn_make_buffer_rsrc((void*)p.q, 0, S_ * D_ * 2, 0x00020000);
      char* qt = lds + L_RT + w * 18432;
      u32x4 ld[10];
      __builtin_amdgcn_s_setprio(QPRIO);
#pragma unroll
      for (int j = 0; j < 10; ++j)
        ld[j] = __builtin_amdgcn_raw_buffer_load_b128(rq, (unsigned)((l >> 3) * D_ * 2 + (l & 7) * 16 + j * 128), (unsigned)(jb * 8 * D_ * 2), 0);
      STAMP(21);
      TAG_READY();
#pragma unroll
      for (int j = 0; j < 10; ++j) *(u32x4*)(qt + (l >> 3) * 1312 + (l & 7) * 16 + j * 128) = ld[j];
      float sa = 0.f, va = 0.f;
#pragma unroll
      for (int j = 0; j < 10; ++j)
#pragma unroll
        for (int d = 0; d < 4; ++d) {
          const unsigned a0 = ld[j][d];
          const bf2_t xa = __builtin_bit_cast(bf2_t, a0);
          sa = __builtin_amdgcn_fdot2_f32_bf16(xa, ONE2, sa, false); va = __builtin_amdgcn_fdot2_f32_bf16(xa, xa, va, false);
        }
      const float ma = sum8(sa) * (1.0f / D_);
      va = __builtin_fmaxf(sum8(va) * (1.0f / D_) - ma * ma, 0.f);
      const unsigned md = __builtin_amdgcn_ds_bpermute((l & 7) * 32, pack2(ma, __builtin_amdgcn_sqrtf(rbf(rbf(va) + 1e-5f))));
      st16(rs, WS_ST1 + (unsigned)(l >> 3) * 32768u + (unsigned)jb * 128u + (unsigned)(l & 7) * 16u, (u32x4){md, tag, md, tag});
      asm volatile("s_waitcnt lgkmcnt(0)" ::: "memory");
      STAMP(23);
#pragma unroll
      for (int j = 0; j < 20; ++j) {
        const u32x2 x = *(const u32x2*)(qt + (l & 7) * 1312 + (l >> 3) * 8 + j * 64);
        __builtin_amdgcn_raw_buffer_store_b128((u32x4){x[0], tag, x[1], tag}, rs, WS_QB + (unsigned)jb * 20480u + (unsigned)j * 1024u + l16, 0, SC1);
      }
      __builtin_amdgcn_s_setprio(0);
      STAMP(22);
    } else if (idx >= 30 || w >= 5) TAG_READY();

    if (idx < 30) {
      if (w < 5) {
        u32x4 pg[11];
        __builtin_amdgcn_s_sleep(M_DELAY);
        const int blk = (w < 2 ? 4 - w : w - 2) * 10 + (a1 >> 3);
        gemv_load(pg, blk, a1 & 7);
        TAG_READY();
        gemv_put(pg, blk, a1 & 7);
        STAMP(25);
        if (w >= 2) hold(RT2_T);
        rt_job(w < 4 ? (w >> 1) * 160 + a1 * 2 + (w & 1) : 320 + a1);
        STAMP(26);
      }
    } else if (ag < 10) {
      float acc[8];
      u32x4 pg[11];
      hold();
      gemv_load(pg, 50 + ag, w);
      gemv_fin(acc, pg);
      if (l < 8) {
        *(f32x4*)(red + w * 64 + l * 8) = (f32x4){acc[0], acc[1], acc[2], acc[3]};
        *(f32x4*)(red + w * 64 + l * 8 + 4) = (f32x4){acc[4], acc[5], acc[6], acc[7]};
      }
      BAR();
      if (w == 4) {
        float t = 0.f;
#pragma unroll
        for (int r = 0; r < 8; ++r) t += red[r * 64 + l];
        st8(rs, WS_MOD + (unsigned)((50 + ag) * 64 + l) * 8u, (u32x2){tag, __float_as_uint(rbf(t))});
      }
      STAMP(2);
    }
    STAMP(3);
    if (TCH & 4) { hold(TCH_T); TOUCH(); }
    hold(W_T);
    if (w == 7) wait_weights();
    TOUCH_END();
    BAR();
    STAMP(4);
    if (idx >= 30 && (w == 5 || w == 6)) {
      u32x4 v[20];
      hold(CS_T);
      _Pragma("unroll 1") for (unsigned spins = 0; spins < SPIN_CAP; ++spins) {
        unsigned dd = 0u;
#pragma unroll
        for (int k = 0; k < 20; ++k) v[k] = ld16(rs, WS_CSP + (unsigned)((ag * 20 + k) * 2 + w - 5) * 1024u + l16);
#pragma unroll
        for (int k = 0; k < 20; ++k) dd |= (v[k][0] ^ tag) | (v[k][2] ^ tag);
        if (__all(dd == 0u)) break;
        __builtin_amdgcn_s_sleep(16);
      }
      float t0 = 0.f, t1 = 0.f;
#pragma unroll
      for (int k = 0; k < 20; ++k) { t0 += __uint_as_float(v[k][1]); t1 += __uint_as_float(v[k][3]); }
      st16(rs, WS_CS + (unsigned)(ag * 2 + w - 5) * 1024u + l16, (u32x4){tag, __float_as_uint(t0), tag, __float_as_uint(t1)});
      STAMP(24);
    }
    if (w < 7) dma_b2(0, 20);
    if (idx >= 24) poison_u();
  }

  u32x4 k_c;
  unsigned kc_o;
  {
    const int i = tid < 224 ? tid : 0;
    const int col = i >= 112 ? (8 + c) * 128 + i - 112 : c * 128 + i;
    kc_o = w < 4 ? WS_CS + (unsigned)col * 16u : WS_MOD + (unsigned)(5 * D_ + 80 * c + 2 * (tid >= 256 && tid < 296 ? tid - 256 : 0)) * 8u;
  }
  auto consts_put = [&]() {
    if (!__all(k_c[0] == tag && k_c[2] == tag)) {
      _Pragma("unroll 1") for (unsigned spins = 0; spins < SPIN_CAP; ++spins) {
        __builtin_amdgcn_s_sleep(8);
        k_c = ld16(rs, kc_o);
        if (__all(k_c[0] == tag && k_c[2] == tag)) break;
      }
    }
    if (tid < 224) *(u32x2*)(lds + L_CONST + tid * 8) = (u32x2){k_c[1], k_c[3]};
    if (tid >= 256 && tid < 296) *(u32x2*)(lds + L_MISC + (tid - 256) * 8) = (u32x2){k_c[1], k_c[3]};
  };
  const int rt1 = w & 3, kh1 = w >> 2;
  unsigned bo0 = (unsigned)(kh1 * 71680) + l16, bo1 = bo0 + 35840u;
  asm volatile("" : "+v"(bo0), "+v"(bo1));
#define BFRAG(q, j) (*(const u32x4*)(lds + ((q) < 5 ? bo0 : bo1) + ((q) % 5) * 7168 + (j) * 1024))
  unsigned long long prc2;
  unsigned prt2;
  u32x4 h2[10];
  f32x4 acc[7];
  float mu_n, rs_n;
  {
    if (w == 6) {
      const unsigned fo[1] = {l < 20 ? WS_FWI + (unsigned)(c * 20 + l) * 128u : l < 28 ? WS_FWO + (unsigned)(c * 8 + l - 20) * 128u : ~0u};
      wait_lines<1, 8>(rs, fo, tag);
    }
    if (w == 7) {
      const unsigned fb = WS_FX8 + (unsigned)(xcd * 160 + l) * 128u;
      const unsigned fo[3] = {fb, fb + 8192u, l < 32 ? fb + 16384u : ~0u};
      wait_lines<3, FX_SLEEP>(rs, fo, tag);
    }
    BAR();
    prc2 = __builtin_amdgcn_s_memtime();
    prt2 = (unsigned)__builtin_amdgcn_s_memrealtime();
    STAMP(7);
    const unsigned xo = (unsigned)(R * 4 + rt1) * 20480u + (unsigned)(10 * kh1) * 1024u +
                        (unsigned)((4 * ((l >> 3) & 1) + h4) * 128 + (l & 7) * 16);
#pragma unroll
    for (int q = 0; q < 10; ++q) h2[q] = ld16p(rs, WS_X1B + xo + (unsigned)q * 1024u);
    SCHED();
    __builtin_amdgcn_s_waitcnt(0x0F7A);
    BAR();
    STAMP(8);
#pragma unroll
    for (int j = 0; j < 7; ++j) acc[j] = (f32x4){0.f, 0.f, 0.f, 0.f};
    f32x4 accg = {0.f, 0.f, 0.f, 0.f};
    u32x4 bf[7];
#pragma unroll
    for (int j = 0; j < 7; ++j) bf[j] = BFRAG(0, j);
#pragma unroll
    for (int q = 0; q < 10; ++q) {
      {
        const u32x4 x = h2[q];
        h2[q][0] = __builtin_amdgcn_update_dpp(x[0], x[2], 0x128, 0xf, 0xC, false);
        h2[q][1] = __builtin_amdgcn_update_dpp(x[1], x[3], 0x128, 0xf, 0xC, false);
        h2[q][2] = __builtin_amdgcn_update_dpp(x[2], x[0], 0x128, 0xf, 0x3, false);
        h2[q][3] = __builtin_amdgcn_update_dpp(x[3], x[1], 0x128, 0xf, 0x3, false);
      }
      if (q == 5) { __builtin_amdgcn_s_barrier(); asm volatile("" ::: "memory"); }
      SCHED();
      accg = MFMA16(h2[q], h2[q], accg);
#pragma unroll
      for (int j = 0; j < 7; ++j) {
        acc[j] = MFMA16(h2[q], bf[j], acc[j]);
        if (q < 9) bf[j] = BFRAG(q + 1, j);
        if (q >= 2 && j == 1) LDW(2 * q - 4);
        if (q >= 2 && j == 3) LDW(2 * q - 3);
        if (q == 7 && j == 0) k_c = ld16(rs, kc_o);
        if (q >= 5 && j == 5) {
          const int i = (5 + q) * 7 + (w < 7 ? w : 0);
          dma1k(rs, lds + i * 1024, l16, WS_WINF + (unsigned)c * WBLK + (unsigned)i * 1024u);
          if (q == 9) { const int i2 = 28 + (w < 7 ? w : 0); dma1k(rs, lds + i2 * 1024, l16, WS_WINF + (unsigned)c * WBLK + (unsigned)i2 * 1024u); }
        }
        SCHED();
      }
    }
    STAMP(31);
    {
      if (n16 == 12) *(f32x4*)(lds + L_PST + ((kh1 * 4 + rt1) * 16 + 4 * h4) * 4) = acc[6];
      const float dg = (n16 & 2) ? ((n16 & 1) ? accg[3] : accg[2]) : ((n16 & 1) ? accg[1] : accg[0]);
      if (h4 == (n16 >> 2)) *(float*)(lds + L_PST + 512 + ((kh1 * 4 + rt1) * 16 + n16) * 4) = dg;
      consts_put();
    }
  }
  auto gelu_n = [&](f32x2* x, auto N_) {
    constexpr int N = decltype(N_)::value;
    const float c0 = -2.0f * 0.7978845608028654f * 1.4426950408889634f, c1 = c0 * 0.044715f;
    f32x2 k0 = {c0, c0}, k1 = {c1, c1}, k2 = {1.0f, 1.0f};
    asm volatile("" : "+v"(k0), "+v"(k1), "+v"(k2));
#pragma unroll
    for (int i = 0; i < N; ++i) {
      f32x2 t = __builtin_elementwise_fma(x[i] * x[i], k1, k0) * x[i];
      asm("" : "+v"(t));
      t = (f32x2){__builtin_amdgcn_exp2f(t[0]), __builtin_amdgcn_exp2f(t[1])} + k2;
      asm("" : "+v"(t));
      x[i] *= (f32x2){__builtin_amdgcn_rcpf(t[0]), __builtin_amdgcn_rcpf(t[1])};
    }
  };
  {
    f32x2* red = (f32x2*)lds;
    f32x2 mine[7];
    if (kh1) {
#pragma unroll
      for (int j = 0; j < 7; ++j) { red[(w * 7 + j) * 64 + l] = (f32x2){acc[j][0], acc[j][1]}; mine[j] = (f32x2){acc[j][2], acc[j][3]}; }
    } else {
#pragma unroll
      for (int j = 0; j < 7; ++j) { red[(w * 7 + j) * 64 + l] = (f32x2){acc[j][2], acc[j][3]}; mine[j] = (f32x2){acc[j][0], acc[j][1]}; }
    }
#pragma unroll
    for (int k = 16; k < 24; ++k) LDW(k);
    BAR();
    __builtin_amdgcn_s_setprio(3);
    STAMP(9);
    f32x2 oth[7];
    f32x2 csb[7];
    f32x2 pa[3], pb[3];
    {
      const float* ps = (const float*)(lds + L_PST);
#pragma unroll
      for (int e = 0; e < 3; ++e) {
        const int row = rt1 * 16 + (e < 2 ? 4 * h4 + 2 * kh1 + e : n16);
        pa[e] = (f32x2){ps[row], ps[128 + row]}; pb[e] = (f32x2){ps[64 + row], ps[192 + row]};
      }
    }
#pragma unroll
    for (int j = 0; j < 7; ++j) { oth[j] = red[((w ^ 4) * 7 + j) * 64 + l]; csb[j] = *(const f32x2*)(lds + L_CONST + (112 + 16 * j + n16) * 8); }
    SCHED();
    float mu2[2], rs2[2];
#pragma unroll
    for (int e = 0; e < 3; ++e) {
      const float mean = (pa[e][0] + pb[e][0]) * (1.0f / D_);
      const float var = __builtin_fmaxf((pa[e][1] + pb[e][1]) * (1.0f / D_) - mean * mean, 0.f);
      const float rstd = __builtin_amdgcn_rsqf(var + 1e-5f);
      if (e < 2) { mu2[e] = mean; rs2[e] = rstd; } else { mu_n = mean; rs_n = rstd; }
    }
    f32x2 zz[7];
    const f32x2 nmu = {-mu2[0], -mu2[1]}, rsv = {rs2[0], rs2[1]};
#pragma unroll
    for (int j = 0; j < 7; ++j)
      zz[j] = __builtin_elementwise_fma(__builtin_elementwise_fma(nmu, (f32x2){csb[j][0], csb[j][0]}, mine[j] + oth[j]), rsv, (f32x2){csb[j][1], csb[j][1]});
    gelu_n(zz, IC<7>());
    if (n16 >= 12) zz[6] = (f32x2){0.f, 0.f};
    f32x2 s1 = zz[0], s2 = zz[0] * zz[0];
#pragma unroll
    for (int j = 1; j < 7; ++j) { s1 += zz[j]; s2 = __builtin_elementwise_fma(zz[j], zz[j], s2); }
    asm("" : "+v"(s1), "+v"(s2));
    float a0 = (l & 1) ? s1[1] : s1[0], a1 = (l & 1) ? s1[0] : s1[1], b0 = (l & 1) ? s2[1] : s2[0], b1 = (l & 1) ? s2[0] : s2[1];
    a0 += dpp_f<0xB1>(a1); b0 += dpp_f<0xB1>(b1);
    a0 += dpp_f<0x4E>(a0); b0 += dpp_f<0x4E>(b0);
    a0 += dpp_f<0x124>(a0); b0 += dpp_f<0x124>(b0);
    a0 += dpp_f<0x128>(a0); b0 += dpp_f<0x128>(b0);
    {
      const int src = ((l >> 1) & 3) * 64 + (l & 1) * 4;
      const unsigned pa0 = __builtin_amdgcn_ds_bpermute(src, __float_as_uint(a0)), pb0 = __builtin_amdgcn_ds_bpermute(src, __float_as_uint(b0));
      if (l < 8) st16(rs, WS_PART + (unsigned)((R * 8 + c) * 8 + w) * 128u + l16, (u32x4){tag, pa0, tag, pb0});
    }
    SCHED();
    __builtin_amdgcn_s_setprio(0);
    STAMP(10);
#pragma unroll
    for (int k = 0; k < 10; ++k) {
      const int i = (k < 5 ? 5 + k : 10 + k) * 7 + w;
      if (w < 7) dma1k(rs, lds + i * 1024, l16, WS_WINF + (unsigned)c * WBLK + (unsigned)i * 1024u);
    }
#pragma unroll
    for (int j = 0; j < 7; ++j)
      *(unsigned*)(lds + L_Z + (((rt1 >> 1) * 7 + j) * 64 + l) * 16 + (4 * (rt1 & 1) + 2 * kh1) * 2) = pack2(zz[j][0], zz[j][1]);
    SCHED();
#pragma unroll
    for (int g = 0; g < 4; ++g) WSP_TR(g);
    if (w == 3) {
      __builtin_amdgcn_s_setprio(3);
      u32x4 v[8];
      const unsigned po = WS_PART + (unsigned)(R * 8) * 1024u + l16;
      unsigned need = 0xffu;
      _Pragma("unroll 1") for (unsigned spins = 0; spins < SPIN_CAP; ++spins) {
#pragma unroll
        for (int k = 0; k < 8; ++k)
          if (need & (1u << k)) v[k] = ld16(rs, po + (unsigned)k * 1024u);
#pragma unroll
        for (int k = 0; k < 8; ++k)
          if (__all(v[k][0] == tag && v[k][2] == tag)) need &= ~(1u << k);
        if (need == 0u) break;
        __builtin_amdgcn_s_sleep(1);
      }
      float t1 = 0.f, t2 = 0.f;
#pragma unroll
      for (int k = 0; k < 8; ++k) { t1 += __uint_as_float(v[k][1]); t2 += __uint_as_float(v[k][3]); }
      const float mean = t1 * (1.0f / FH);
      const float var = __builtin_fmaxf(t2 * (1.0f / FH) - mean * mean, 0.f);
      const float rstd = __builtin_amdgcn_rsqf(var + 1e-5f);
      const int sg = 16 * ((l >> 3) & 3) + 4 * ((l >> 1) & 3) + 2 * (l >> 5) + (l & 1);
      *(f32x2*)(lds + L_ST2 + sg * 8) = (f32x2){rstd, -mean * rstd};
    }
    STAMP(11);
    DRAIN();
    BAR();
    __builtin_amdgcn_s_setprio(3);
    STAMP(12);
    u32x4 zf[2];
#pragma unroll
    for (int m = 0; m < 2; ++m) zf[m] = *(const u32x4*)(lds + L_Z + ((m * 7 + (w < 7 ? w : 0)) * 64 + l) * 16);
    if (w < 7) {
#pragma unroll
      for (int m = 0; m < 2; ++m) {
        const f32x4* sp = (const f32x4*)(lds + L_ST2 + (32 * m + 4 * h4) * 8);
        u32x4 o;
#pragma unroll
        for (int e = 0; e < 4; ++e) {
          f32x4 st = sp[(e & 1) + 8 * (e >> 1)];
          o[e] = pack2(__builtin_fmaf(bflo(zf[m][e]), st[0], st[1]), __builtin_fmaf(bfhi(zf[m][e]), st[2], st[3]));
        }
        st16(rs, WS_Z2F + (unsigned)((c * 64 + 4 * (R >> 1) + h4) * 7 + w) * 1024u + (unsigned)((2 * (R & 1) + m) * 256 + n16 * 16), o);
      }
    }
    SCHED();
    __builtin_amdgcn_s_setprio(0);
#pragma unroll
    for (int k = 0; k < 4; ++k)
      if (w < 7) dma1k(rs, lds + (k * 7 + w) * 1024, l16, WS_WINF + (unsigned)c * WBLK + (unsigned)(k * 7 + w) * 1024u);
    SCHED();
    STAMP(13);
  }
  unsigned bs_raw;
  u32x2 xp[3];
  constexpr int NST = 5, STG = 28672;
#define SLOT(t) (((t) % NST) * STG)
  const unsigned sZ = WS_Z2F + (unsigned)(c * 64) * 7168u + (unsigned)w * 1024u;
  auto issue = [&](int t, int k) { dma1k(rs, lds + SLOT(t) + k * 7168 + w * 1024, l16, sZ + (unsigned)(16 * k + ((t + rot) & 15)) * 7168u); };
  {
    f32x4 acc[7];
#pragma unroll
    for (int j = 0; j < 7; ++j) acc[j] = (f32x4){0.f, 0.f, 0.f, 0.f};
    u32x4 bf[7];
#pragma unroll
    for (int j = 0; j < 7; ++j) bf[j] = BFRAG(4, j);
#pragma unroll
    for (int qi = 0; qi < 10; ++qi) {
      const int q = (qi + 4) % 10, qn = (qi + 5) % 10;
      if (qi == 5) {
        DRAIN();
        __builtin_amdgcn_s_barrier();
        asm volatile("" ::: "memory");
        if (w == 7) st_flag(rs, WS_FZ + (unsigned)(c * 32 + R) * 128u, tag, l);
        STAMP(6);
#pragma unroll
        for (int k = 24; k < 32; ++k) LDW(k);
        bs_raw = p.b_sp[R * 64 + tau(2 * rp + (kq & 1), n16)];
#pragma unroll
        for (int j = 0; j < 3; ++j) {
          const int dj = (w >> 2) ? (3 + (j < 2 ? j : 1)) : j;
          const int d0 = 80 * c + 16 * dj + 4 * h4;
          xp[j] = ld8p(rs, WS_X1B + (unsigned)(R * 4 + (w & 3)) * 20480u + (unsigned)(d0 >> 2) * 128u + (unsigned)psi(n16) * 8u);
        }
      }
      SCHED();
#pragma unroll
      for (int j = 0; j < 7; ++j) {
        acc[j] = MFMA16(bf[j], h2[q], acc[j]);
        if (qi < 9) bf[j] = BFRAG(qn, j);
        SCHED();
      }
    }
    STAMP(29);
    f32x4* red = (f32x4*)(lds + 100352);
    f32x4 m4[4];
    if (kh1 == 0) {
#pragma unroll
      for (int j = 0; j < 3; ++j) red[(w * 4 + j) * 64 + l] = acc[4 + j];
#pragma unroll
      for (int j = 0; j < 4; ++j) m4[j] = acc[j];
    } else {
#pragma unroll
      for (int j = 0; j < 4; ++j) red[(w * 4 + j) * 64 + l] = acc[j];
#pragma unroll
      for (int j = 0; j < 4; ++j) m4[j] = acc[4 + (j < 3 ? j : 2)];
    }
    WSP_TR(4);
    WSP_TR(5);
    if (w == 3) {
      const unsigned fo[1] = {l < 32 ? WS_FZ + (unsigned)(c * 32 + l) * 128u : ~0u};
      wait_lines<1, 1>(rs, fo, tag);
    }
    DRAIN();
    BAR();
    STAMP(14);
    auto issue_q = [&](int t) {
#pragma unroll
      for (int j = 0; j < 7; ++j)
        dma1k(rs, lds + SLOT(t) + (w & 3) * 7168 + j * 1024, l16, WS_Z2F + (unsigned)(c * 64 + 16 * (w & 3) + ((t + rot) & 15)) * 7168u + (unsigned)j * 1024u);
    };
    if (w >= 4) { issue_q(0); issue_q(1); }
    SCHED();
    f32x4 o4[4], c01[4], c23[4];
#pragma unroll
    for (int j = 0; j < 4; ++j) {
      const int jj = (kh1 && j == 3) ? 2 : j, jt = 4 * kh1 + jj;
      o4[j] = red[((w ^ 4) * 4 + jj) * 64 + l];
      c01[j] = *(const f32x4*)(lds + L_CONST + (16 * jt + 4 * h4) * 8);
      c23[j] = *(const f32x4*)(lds + L_CONST + (16 * jt + 4 * h4 + 2) * 8);
    }
    SCHED();
    f32x2 z[8];
#pragma unroll
    for (int i = 0; i < 8; ++i) {
      const int j = i >> 1, r = 2 * (i & 1);
      const f32x4 cc = r ? c23[j] : c01[j];
      z[i] = __builtin_elementwise_fma(__builtin_elementwise_fma((f32x2){-mu_n, -mu_n}, (f32x2){cc[0], cc[2]}, (f32x2){o4[j][r], o4[j][r + 1]} + (f32x2){m4[j][r], m4[j][r + 1]}),
                                       (f32x2){rs_n, rs_n}, (f32x2){cc[1], cc[3]});
    }
    SCHED();
    gelu_n(z, IC<6>());
    if (kh1 == 0) gelu_n(z + 6, IC<2>());
#pragma unroll
    for (int j = 0; j < 4; ++j)
      if (kh1 == 0 || j < 3) *(u32x2*)(lds + L_Z + (((kh1 ? 4 + j : j) * 4 + rt1) * 64 + l) * 8) = (u32x2){pack2(z[2 * j][0], z[2 * j][1]), pack2(z[2 * j + 1][0], z[2 * j + 1][1])};
    SCHED();
    if (w < 4) issue_q(2);
    STAMP(30);
  }

  f32x4 acc2[2][7];
  {
    SCHED();
    WSP_TR(6);
    WSP_TR(7);
#pragma unroll
    for (int i = 0; i < 2; ++i)
#pragma unroll
      for (int j = 0; j < 7; ++j) acc2[i][j] = (f32x4){0.f, 0.f, 0.f, 0.f};
    const char* rd = lds + kq * 7168 + l16;
#define VMW(n) asm volatile("s_waitcnt vmcnt(%0)" ::"n"((n) * 4) : "memory")
#define RBAR() do { __builtin_amdgcn_s_barrier(); asm volatile("" ::: "memory"); } while (0)
    SCHED();
    if (w >= 4) DRAIN();
    BAR();
    if (w < 7) {
#pragma unroll
      for (int k = 0; k < 4; ++k) issue(3, k);
    }
    STAMP(20);
    u32x4 fr[7];
#pragma unroll
    for (int j = 0; j < 7; ++j) fr[j] = *(const u32x4*)(rd + j * 1024);
    u32x4 wo[18];
#define LDO(k) wo[k] = ld16p(rs, WS_WOUTF + (unsigned)c * WBLK + (unsigned)((k) < 17 || w < 4 ? w + 8 * (k) : w) * 1024u + l16)
#pragma unroll
    for (int t = 0; t < 15; ++t) {
      SCHED();
      if (t < 13) VMW(2);
      else if (t == 13) asm volatile("s_waitcnt vmcnt(10)" ::: "memory");
      else asm volatile("s_waitcnt vmcnt(12)" ::: "memory");
      RBAR();
      SCHED();
#pragma unroll
      for (int j = 0; j < 7; ++j) {
        acc2[0][j] = MFMA16(fr[j], wsp[0][t], acc2[0][j]);
        acc2[1][j] = MFMA16(fr[j], wsp[1][t], acc2[1][j]);
        fr[j] = *(const u32x4*)(rd + SLOT(t + 1) + j * 1024);
        if (t + 4 < 16 && j < 4 && w < 7) issue(t + 4, j);
        if (t >= 12 && j < 6) LDO(6 * (t - 12) + j);
        SCHED();
      }
    }
    f32x4* red = (f32x4*)(lds + STG) + rp * (42 * 64) + l;
    f32x4 own[4];
    auto last = [&](auto KQ_) {
      constexpr int KQ = decltype(KQ_)::value;
      constexpr int ord[7] = {KQ < 2 ? 2 : 0, KQ < 2 ? 3 : 1, KQ < 2 ? 6 : 4, KQ < 2 ? 0 : 5, KQ < 2 ? 1 : 2, KQ < 2 ? 4 : 3, KQ < 2 ? 5 : 6};
#pragma unroll
      for (int x = 0; x < 9; ++x) {
        if (x < 7) {
          const int j = ord[x];
          acc2[1 - (KQ & 1)][j] = MFMA16(fr[j], wsp[1 - (KQ & 1)][15], acc2[1 - (KQ & 1)][j]);
          acc2[KQ & 1][j] = MFMA16(fr[j], wsp[KQ & 1][15], acc2[KQ & 1][j]);
        }
        if (x >= 2) {
          const int j = ord[x - 2];
#pragma unroll
          for (int i = 0; i < 2; ++i) {
            const int ko = i + 2 * ((j >> 1) & 1);
            if (KQ != ko) red[((i * 7 + j) * 3 + (KQ > ko ? KQ - 1 : KQ)) * 64] = acc2[i][j];
          }
        }
        SCHED();
      }
#pragma unroll
      for (int x = 0; x < 4; ++x) {
        const int j = 2 * (KQ >> 1) + 4 * (x >> 1) + (x & 1);
        own[x] = acc2[KQ & 1][j < 7 ? j : 6];
      }
    };
    if (kq == 0) last(IC<0>());
    else if (kq == 1) last(IC<1>());
    else if (kq == 2) last(IC<2>());
    else last(IC<3>());
    STAMP(15);
    const int io = kq & 1, jb = kq >> 1;
    const int rto = 2 * rp + io;
    u32x2 z[4];
#pragma unroll
    for (int x = 0; x < 4; ++x) {
      const int j = 2 * jb + 4 * (x >> 1) + (x & 1);
      z[x] = *(const u32x2*)(lds + L_Z + (((j < 7 ? j : 6) * 4 + rto) * 64 + l) * 8);
    }
    f32x4 gbv[3];
#pragma unroll
    for (int j = 0; j < 3; ++j) gbv[j] = *(const f32x4*)(lds + L_MISC + (16 * ((w >> 2) ? 3 + (j < 2 ? j : 1) : j) + 4 * h4) * 4);
    BAR();
    {
      f32x4 r3[4][3];
#pragma unroll
      for (int x = 0; x < 4; ++x) {
        const int j = 2 * jb + 4 * (x >> 1) + (x & 1);
#pragma unroll
        for (int k = 0; k < 3; ++k) r3[x][k] = red[((io * 7 + (j < 7 ? j : 6)) * 3 + k) * 64];
      }
      SCHED();
      const float bs = bflo(bs_raw);
      const unsigned ub = WS_UF + (unsigned)((R * 4 + rto) * 8 + c) * 3584u;
      u32x4 o[2];
#pragma unroll
      for (int x = 0; x < 4; ++x) {
        const f32x4 g = (own[x] + r3[x][0]) + (r3[x][1] + r3[x][2]) + bs;
        o[x >> 1][2 * (x & 1)] = pack2(bflo(z[x][0]) * g[0], bfhi(z[x][0]) * g[1]);
        o[x >> 1][2 * (x & 1) + 1] = pack2(bflo(z[x][1]) * g[2], bfhi(z[x][1]) * g[3]);
      }
      st16(rs, ub + (unsigned)jb * 1024u + l16, o[0]);
      if (jb == 0) st16(rs, ub + 2048u + l16, o[1]);
      else st8(rs, ub + 3072u + (unsigned)l * 8u, h4 == 3 ? (u32x2){0u, 0u} : (u32x2){o[1][0], o[1][1]});
    }
    SCHED();
    BAR();
    STAMP(16);

    const int rt = w & 3, kh = w >> 2;
    u32x4 uf[4][3];
    u32x2 uh[4];
    const unsigned ub = WS_UF + (unsigned)((R * 4 + rt) * 8 + kh) * 3584u;
    auto ld_u = [&](int cc) {
#pragma unroll
      for (int qq = 0; qq < 3; ++qq) uf[cc][qq] = ld16(rs, ub + (unsigned)cc * 7168u + (unsigned)qq * 1024u + l16);
      uh[cc] = ld8(rs, ub + (unsigned)cc * 7168u + 3072u + (unsigned)l * 8u);
    };
    auto u_ok = [&](int cc) {
      unsigned m = __builtin_elementwise_max(uh[cc][0], uh[cc][1]);
#pragma unroll
      for (int qq = 0; qq < 3; ++qq) {
        m = __builtin_elementwise_max(m, __builtin_elementwise_max(uf[cc][qq][0], uf[cc][qq][1]));
        m = __builtin_elementwise_max(m, __builtin_elementwise_max(uf[cc][qq][2], uf[cc][qq][3]));
      }
      return __all(m != ~0u);
    };
#pragma unroll
    for (int k = 0; k < 18; ++k) {
      if (k == U_AT) {
        SCHED();
        ld_u(0);
        ld_u(1);
        SCHED();
      }
      if (k < 17 || w < 4) *(u32x4*)(lds + (w + 8 * k) * 1024 + l16) = wo[k];
    }
    BAR();
    STAMP(17);
    f32x4 acc[5];
#pragma unroll
    for (int j = 0; j < 5; ++j) acc[j] = (f32x4){0.f, 0.f, 0.f, 0.f};
    unsigned wb0 = (unsigned)(kh * 17920) + l16, wb1 = wb0 + 71680u, wh0 = (unsigned)(kh * 17920 + 15360 + l * 8), wh1 = wh0 + 71680u;
    asm volatile("" : "+v"(wb0), "+v"(wb1), "+v"(wh0), "+v"(wh1));
    u32x4 af[2][15];
    u32x2 ah[2][5];
    auto g3ld = [&](int cc) {
#pragma unroll
      for (int i = 0; i < 15; ++i) af[cc & 1][i] = *(const u32x4*)(lds + (cc < 2 ? wb0 : wb1) + (cc & 1) * 35840 + i * 1024);
#pragma unroll
      for (int dj = 0; dj < 5; ++dj) ah[cc & 1][dj] = *(const u32x2*)(lds + (cc < 2 ? wh0 : wh1) + (cc & 1) * 35840 + dj * 512);
    };
    g3ld(0);
    SCHED();
    ld_u(2);
    ld_u(3);
    SCHED();
    g3ld(1);
#pragma unroll
    for (int cc = 0; cc < 4; ++cc) {
      SCHED();
      if (!u_ok(cc)) {
        _Pragma("unroll 1") for (unsigned spins = 0; spins < SPIN_CAP; ++spins) {
          __builtin_amdgcn_s_sleep(1);
          ld_u(cc);
          if (u_ok(cc)) break;
        }
      }
      SCHED();
#pragma unroll
      for (int i = 0; i < 15; ++i) {
        acc[i % 5] = MFMA16(af[cc & 1][i], uf[cc][i / 5], acc[i % 5]);
        if (cc < 2) af[cc & 1][i] = *(const u32x4*)(lds + wb1 + (cc & 1) * 35840 + i * 1024);
        SCHED();
      }
#pragma unroll
      for (int dj = 0; dj < 5; ++dj) {
        acc[dj] = MFMA16K16(ah[cc & 1][dj], uh[cc], acc[dj]);
        if (cc < 2) ah[cc & 1][dj] = *(const u32x2*)(lds + wh1 + (cc & 1) * 35840 + dj * 512);
        SCHED();
      }
    }
    STAMP(18);
    f32x4* X = (f32x4*)(lds + WBLK) + rt * (5 * 64) + l;
    f32x4 m4[3];
    if (kh == 0) {
      X[3 * 64] = acc[3]; X[4 * 64] = acc[4];
      m4[0] = acc[0]; m4[1] = acc[1]; m4[2] = acc[2];
    } else {
      X[0] = acc[0]; X[64] = acc[1]; X[128] = acc[2];
      m4[0] = acc[3]; m4[1] = acc[4]; m4[2] = acc[4];
    }
    BAR();
    if (bid == 0 && tid == 320) st4(rs, WS_EPOCH, tag);
    const int t = R * 64 + tau(rt, n16);
    f32x4 o4[3];
#pragma unroll
    for (int j = 0; j < 3; ++j) o4[j] = X[(kh ? 3 + (j < 2 ? j : 1) : j) * 64];
    SCHED();
#pragma unroll
    for (int j = 0; j < 3; ++j) {
      const f32x4 v = rbf4(gbv[j] * rbf4(o4[j] + m4[j]));
      const u32x2 o = {pack2(bflo(xp[j][0]) + v[0], bfhi(xp[j][0]) + v[1]), pack2(bflo(xp[j][1]) + v[2], bfhi(xp[j][1]) + v[3])};
      if (j < 2 || kh == 0)
        __builtin_amdgcn_raw_buffer_store_b64(o, __builtin_amdgcn_make_buffer_rsrc((void*)p.out, 0, S_ * D_ * 2, 0x00020000),
                                              (unsigned)(t * D_ + 80 * c + 16 * (kh ? 3 + j : j) + 4 * h4) * 2u, 0, 0);
    }
  }
  if (w == 0) {
    unsigned ref = PROBE == 2 ? t_start : PROBE == 3 ? pr3 : (unsigned)pr0;
    unsigned dl = 6000u;
    if (PROBE == 7) { ref = t_start; dl = (unsigned)(__builtin_amdgcn_s_memtime() - prc) / 10u; }
    if (PROBE == 8) { ref = (unsigned)__builtin_amdgcn_s_memrealtime(); }
    if (PROBE == 9) { ref = t_start; dl = 4000u + (unsigned)(prc2 - prc) / 10u; }
    if (PROBE == 10) { ref = prt2; }
    if (PROBE == 4) { ref = ld4(rs, 2048u); ref = __builtin_amdgcn_readfirstlane(ref); dl = 8000u; }
    if (PROBE == 6) { if (bid == 0 && l == 0) st4(rs, 2048u, ref); }
    else while ((unsigned)__builtin_amdgcn_s_memrealtime() - ref < dl) __builtin_amdgcn_s_sleep(1);
  }
  STAMP(19);
}

__global__ void k1(char* ws) {
  unsigned t = (unsigned)__builtin_amdgcn_s_memrealtime();
  if (PROBE == 6) {
    const unsigned ref = *(volatile unsigned*)(ws + 2048);
    while ((unsigned)__builtin_amdgcn_s_memrealtime() - ref < 8000u) __builtin_amdgcn_s_sleep(1);
  } else if (threadIdx.x == 0) *(volatile unsigned*)(ws + 2048) = t;
}
extern "C" __device__ __attribute__((used, noinline, visibility("hidden"))) void mega_tail() {}

extern "C" void kernel_launch(void* const* d_in, const int* in_sizes, int n_in, void* d_out, int out_size,
                              void* d_ws, size_t ws_size, hipStream_t stream) {
  Params p;
  p.q = (const u16*)d_in[0];
  p.k = (const u16*)d_in[1];
  p.v = (const u16*)d_in[2];
  p.shift_k = (const u16*)d_in[3];
  p.ssm_k = (const u16*)d_in[4];
  p.ssm_D = (const u16*)d_in[5];
  p.w_in = (const u16*)d_in[7];
  p.w_sp = (const u16*)d_in[8];
  p.b_sp = (const u16*)d_in[9];
  p.w_out = (const u16*)d_in[10];
  p.cvec = (const u16*)d_in[11];
  p.w_mod = (const u16*)d_in[12];
  p.out = (u16*)d_out;
  p.ws = (char*)d_ws;
  if (PROBE == 4 || PROBE == 5) k1<<<1, 64, 0, stream>>>(p.ws);
  mega<<<256, 512, 0, stream>>>(p);
  if (PROBE == 6) k1<<<1, 64, 0, stream>>>(p.ws);
}
```

```cpp
#define YG_T 940
#define BG 2
#include <hip/hip_runtime.h>
#include <stdint.h>

typedef unsigned short u16;
typedef __attribute__((ext_vector_type(8))) short bf16x8;
typedef __attribute__((ext_vector_type(4))) short bf16x4;
typedef __attribute__((ext_vector_type(4))) float f32x4;
typedef __attribute__((ext_vector_type(2))) float f32x2;
typedef __attribute__((ext_vector_type(4))) unsigned int u32x4;
typedef __attribute__((ext_vector_type(2))) unsigned int u32x2;
#define LAS __attribute__((address_space(3)))
typedef __amdgpu_buffer_rsrc_t rsrc_t;
typedef __attribute__((ext_vector_type(4))) short s16x4;

constexpr int S_ = 2048, D_ = 640, F_ = 1728, FH = 864, CB = 108;

constexpr unsigned WS_EPOCH = 0;
constexpr unsigned WS_FWI = 4096;
constexpr unsigned WS_FZ = WS_FWI + 384 * 128;
constexpr unsigned WS_FWO = WS_FZ + 256 * 128;
constexpr unsigned F4S = 896 * 128;
constexpr unsigned WS_CS = WS_FZ + 256 * 128;
constexpr unsigned WS_MOD = 131072;
constexpr unsigned WS_MODP = WS_MOD + 81920;
constexpr unsigned WS_ST1 = 393216;
constexpr unsigned WS_PART = 524288;
constexpr unsigned WS_CSP = WS_PART + 262144;
constexpr unsigned WS_FX = WS_CSP + 655360;
constexpr unsigned WS_X1B = WS_FX + 16 * 4 * 160 * 128;
constexpr unsigned WS_QB = WS_X1B + 2621440;
constexpr unsigned WS_KV3 = WS_QB + 2621440;
constexpr unsigned WS_MODQ = WS_KV3 + 1310720;
constexpr unsigned WBLK = 143360;
constexpr unsigned WS_WINF = WS_MODQ + 245760;
constexpr unsigned WS_WOUTF = WS_WINF + 16 * WBLK;
constexpr unsigned WS_Z2F = WS_WOUTF + 8 * WBLK;
constexpr unsigned WS_UF = WS_Z2F + 8 * 64 * 7168;
constexpr unsigned WS_F4 = WS_UF + 32 * 4 * 8 * 4096;
constexpr unsigned WS_END = WS_F4 + 4 * F4S;
constexpr unsigned WS_STAMP = 200u << 20;

constexpr int G_COPY = 4160;
constexpr int L_U = WBLK;
constexpr int U_CH = (256 + S_) * 2 + 32;
constexpr int L_FLG = L_U + 4 * U_CH;
constexpr int L_RT = 16384;
constexpr int L_CONST = WBLK;
constexpr int L_PST = L_CONST + 2048;
constexpr int L_Z = L_CONST + 5120;
constexpr int L_ST2 = L_Z + 14336;
constexpr int L_MISC = L_ST2 + 512;
constexpr int LDS_TOTAL = 163840;

#define AUX_T 610
#define RT2_T 360
#define CB_T 380
#define W_T 700
#define BK 12
#define YW 4
#define YPR 0
#define YSL 2
#define K_T 460
#ifndef M_T
#define M_T 520
#endif
#define WA_T 580
constexpr int SC1 = 16, NT = 2;
constexpr unsigned SPIN_CAP = 1u << 16;

__device__ __forceinline__ float bflo(unsigned x) { return __uint_as_float(x << 16); }
__device__ __forceinline__ float bfhi(unsigned x) { return __uint_as_float(x & 0xffff0000u); }
__device__ __forceinline__ unsigned f2bf(float x) {
  __bf16 b = (__bf16)x;
  return (unsigned)__builtin_bit_cast(unsigned short, b);
}
__device__ __forceinline__ float rbf(float x) { return bflo(f2bf(x)); }
typedef __attribute__((ext_vector_type(2))) __bf16 bf2_t;
__device__ __forceinline__ unsigned pack2(float lo, float hi) {
  f32x2 v = {lo, hi};
  return __builtin_bit_cast(unsigned, __builtin_convertvector(v, bf2_t));
}
__device__ __forceinline__ f32x2 rbf2(f32x2 v) {
  const unsigned p = __builtin_bit_cast(unsigned, __builtin_convertvector(v, bf2_t));
  return (f32x2){__uint_as_float(p << 16), __uint_as_float(p & 0xffff0000u)};
}
__device__ __forceinline__ f32x4 rbf4(f32x4 v) {
  const f32x2 a = rbf2((f32x2){v[0], v[1]}), b = rbf2((f32x2){v[2], v[3]});
  return (f32x4){a[0], a[1], b[0], b[1]};
}
template <int CTRL>
__device__ __forceinline__ float dpp_f(float v) {
  return __int_as_float(__builtin_amdgcn_update_dpp(0, __float_as_int(v), CTRL, 0xf, 0xf, true));
}
__device__ __forceinline__ float sum8(float v) {
  v += dpp_f<0xB1>(v);
  v += dpp_f<0x4E>(v);
  v += dpp_f<0x141>(v);
  return v;
}
__device__ __forceinline__ float sum16(float v) {
  v = sum8(v);
  v += dpp_f<0x140>(v);
  return v;
}
__device__ __forceinline__ float sum_rows(float v) {
  auto a = __builtin_amdgcn_permlane16_swap(__float_as_uint(v), __float_as_uint(v), false, false);
  v = __uint_as_float(a[0]) + __uint_as_float(a[1]);
  auto b = __builtin_amdgcn_permlane32_swap(__float_as_uint(v), __float_as_uint(v), false, false);
  return __uint_as_float(b[0]) + __uint_as_float(b[1]);
}
#define MFMA16(a, b, c) __builtin_amdgcn_mfma_f32_16x16x32_bf16(__builtin_bit_cast(bf16x8, a), __builtin_bit_cast(bf16x8, b), c, 0, 0, 0)
#define MFMA16K16(a, b, c) __builtin_amdgcn_mfma_f32_16x16x16bf16_1k(__builtin_bit_cast(bf16x4, a), __builtin_bit_cast(bf16x4, b), c, 0, 0, 0)
__device__ __forceinline__ u32x2 trd(const char* p) {
  return __builtin_bit_cast(u32x2, __builtin_amdgcn_ds_read_tr16_b64_v4i16((LAS s16x4*)p));
}
__device__ __forceinline__ u32x4 trfrag(const char* p, int pitch) {
  u32x2 a = trd(p), b = trd(p + 16 * pitch);
  return (u32x4){a[0], a[1], b[0], b[1]};
}
#define BAR() asm volatile("s_waitcnt lgkmcnt(0)\n\ts_barrier" ::: "memory")
#define DRAIN() do { __builtin_amdgcn_s_waitcnt(0x0F70); asm volatile("" ::: "memory"); } while (0)
#define SCHED() __builtin_amdgcn_sched_barrier(0)
template <int N> struct IC { static constexpr int value = N; };
template <int I, int N, class F>
__device__ __forceinline__ void sfor(F&& f) {
  if constexpr (I < N) { f(IC<I>()); sfor<I + 1, N>(f); }
}

__device__ __forceinline__ u32x4 ld16(rsrc_t r, unsigned off) { return __builtin_amdgcn_raw_buffer_load_b128(r, off, 0, SC1); }
__device__ __forceinline__ u32x2 ld8(rsrc_t r, unsigned off) { return __builtin_amdgcn_raw_buffer_load_b64(r, off, 0, SC1); }
__device__ __forceinline__ unsigned ld4(rsrc_t r, unsigned off) { return __builtin_amdgcn_raw_buffer_load_b32(r, off, 0, SC1); }
#define ST_AUX 16
__device__ __forceinline__ void st16(rsrc_t r, unsigned off, u32x4 v) { __builtin_amdgcn_raw_buffer_store_b128(v, r, off, 0, ST_AUX); }
#define ST16S(v, r, voff, soff, aux) do { const u32x4 v_ = (v); __builtin_amdgcn_raw_buffer_store_b128(v_, r, voff, soff, aux); asm volatile("s_nop 1" :: "v"(v_)); } while (0)
__device__ __forceinline__ void st8(rsrc_t r, unsigned off, u32x2 v) { __builtin_amdgcn_raw_buffer_store_b64(v, r, off, 0, SC1); }
__device__ __forceinline__ void st_flag4(rsrc_t r, unsigned off, unsigned tag, int l) {
  if (l < 32) st16(r, WS_F4 - WS_FWI + off + (unsigned)(l >> 3) * F4S + (unsigned)(l & 7) * 16u, (u32x4){tag, tag, tag, tag});
}
__device__ __forceinline__ void st4(rsrc_t r, unsigned off, unsigned v) { __builtin_amdgcn_raw_buffer_store_b32(v, r, off, 0, SC1); }
#define BULK_AUX 16
__device__ __forceinline__ u32x4 ld16p(rsrc_t r, unsigned off) { return __builtin_amdgcn_raw_buffer_load_b128(r, off, 0, BULK_AUX); }
__device__ __forceinline__ u32x2 ld8p(rsrc_t r, unsigned off) { return __builtin_amdgcn_raw_buffer_load_b64(r, off, 0, BULK_AUX); }
__device__ __forceinline__ void dma1k(rsrc_t r, char* lds_dst, unsigned l16, unsigned soff) {
  __builtin_amdgcn_raw_ptr_buffer_load_lds(r, (LAS void*)lds_dst, 16, l16, soff, 0, BULK_AUX);
}

#define STAMP(i)
#define STAMPF(i)
#define STAMPT(i) STAMP(i)

template <int N, int SLP>
__device__ __forceinline__ void wait_lines(rsrc_t r, const unsigned (&off)[N], unsigned tag) {
  bool need[N];
#pragma unroll
  for (int i = 0; i < N; ++i) need[i] = off[i] != ~0u;
  _Pragma("unroll 1") for (unsigned spins = 0; spins < SPIN_CAP; ++spins) {
    unsigned v[N];
#pragma unroll
    for (int i = 0; i < N; ++i) {
      v[i] = tag;
      if (need[i]) v[i] = ld4(r, off[i]);
    }
    bool any = false;
#pragma unroll
    for (int i = 0; i < N; ++i) { need[i] = v[i] != tag; any = any || need[i]; }
    if (!__any(any)) break;
    __builtin_amdgcn_s_sleep(SLP);
  }
  asm volatile("" ::: "memory");
}

__device__ __forceinline__ int tau(int rt, int n) { return 32 * (rt >> 1) + 8 * (n >> 2) + 4 * (rt & 1) + (n & 3); }
__device__ __forceinline__ int psi(int n) { return 2 * (n & 7) + (n >> 3); }
#define QX_CM(d, s0, s1, P) "v_cndmask_b32_dpp " d ", " s0 ", " s1 ", vcc quad_perm:" P " row_mask:0xf bank_mask:0xf\n\t"
#define QX_BODY(M, P) \
  asm("s_mov_b32 vcc_lo, " M "\n\ts_mov_b32 vcc_hi, " M "\n\t" \
      QX_CM("%0", "%12", "%8", P) "v_xor_b32 %4, %8, %0\n\t" QX_CM("%1", "%13", "%9", P) "v_xor_b32 %5, %9, %1\n\t" \
      QX_CM("%2", "%14", "%10", P) "v_xor_b32 %6, %10, %2\n\t" QX_CM("%3", "%15", "%11", P) "v_xor_b32 %7, %11, %3" \
      : "=&v"(t[0]), "=&v"(t[1]), "=&v"(t[2]), "=&v"(t[3]), "=&v"(u[0]), "=&v"(u[1]), "=&v"(u[2]), "=&v"(u[3]) \
      : "v"(a[0]), "v"(a[1]), "v"(a[2]), "v"(a[3]), "v"(b[0]), "v"(b[1]), "v"(b[2]), "v"(b[3]) : "vcc")
template <int BIT>
__device__ __forceinline__ void quad_xchg3(u32x4& a, u32x4& b) {
  unsigned t[4], u[4];
  if (BIT == 0) QX_BODY("0x55555555", "[1,0,3,2]");
  else QX_BODY("0x33333333", "[2,3,0,1]");
  a = (u32x4){t[0], t[1], t[2], t[3]};
#pragma unroll
  for (int d = 0; d < 4; ++d) b[d] ^= (unsigned)__builtin_amdgcn_update_dpp(0, (int)u[d], BIT == 0 ? 0xB1 : 0x4E, 0xf, 0xf, true);
}
__device__ __forceinline__ void quad_transpose(u32x4& x0, u32x4& x1, u32x4& x2, u32x4& x3) {
  quad_xchg3<0>(x0, x1);
  quad_xchg3<0>(x2, x3);
  quad_xchg3<1>(x0, x2);
  quad_xchg3<1>(x1, x3);
  asm volatile("" : "+v"(x0), "+v"(x1), "+v"(x2), "+v"(x3));
}

struct Params {
  const u16 *q, *k, *v, *shift_k, *ssm_k, *ssm_D, *w_in, *w_sp, *b_sp, *w_out, *cvec, *w_mod;
  u16* out;
  char* ws;
};

__global__ __launch_bounds__(512, 2) void mega(Params p) {
  __shared__ __attribute__((aligned(16))) char lds[LDS_TOTAL];
  {
    unsigned long long pc0;
    unsigned vt;
    asm volatile("v_readfirstlane_b32 vcc_lo, %3\n\ts_cmpk_lt_u32 %2, 192\n\ts_cbranch_scc1 4f\n\ts_cmpk_lt_u32 vcc_lo, 64\n\ts_cbranch_scc0 4f\n\t"
                 "s_lshr_b32 vcc_hi, %2, 3\n\ts_sub_u32 vcc_hi, vcc_hi, 24\n\ts_lshl_b32 vcc_hi, vcc_hi, 13\n\t"
                 "v_lshlrev_b32 %1, 7, %3\n\tv_add_u32 %1, vcc_hi, %1\n\ts_getpc_b64 %0\n.Lmega_here:\n\t"
                 "v_min_u32 %1, mega_tail-.Lmega_here-4, %1\n\ts_movk_i32 m0, 0x2000\n\ts_nop 1\n\tglobal_load_lds_dword %1, %0\n4:\n\t"
                 "s_cmpk_lt_u32 %2, 160\n\ts_cbranch_scc0 1f\n\ts_cmpk_lt_u32 vcc_lo, 256\n\ts_cbranch_scc1 3f\n\t"
                 "s_call_b64 vcc, .Ltouch1\n\ts_branch 3f\n1:\n\t"
                 "s_cmpk_lt_u32 vcc_lo, 256\n\ts_cbranch_scc1 2f\n\ts_cmpk_lt_u32 %2, 224\n\ts_cbranch_scc0 2f\n\ts_call_b64 vcc, .Ltouch3\n\ts_branch 3f\n"
                 "2:\n\ts_call_b64 vcc, .Ltouch2\n3:"
                 : "=&s"(pc0), "=&v"(vt) : "s"(blockIdx.x), "v"(threadIdx.x) : "vcc", "scc", "memory");
  }
  SCHED();
  const int tid = threadIdx.x, l = tid & 63;
  const int w = __builtin_amdgcn_readfirstlane(tid >> 6);
  const int bid = blockIdx.x, xcd = bid & 7, idx = bid >> 3;
#define TOUCH_AT(n) asm volatile("s_branch 1f\n.Ltouch" #n ":\n\ts_setpc_b64 vcc\n1:")
  char* const ws = p.ws;
  asm volatile("" ::"s"(p.q), "s"(p.k), "s"(p.v), "s"(p.shift_k), "s"(p.ssm_k), "s"(p.ssm_D), "s"(p.w_in), "s"(p.w_sp), "s"(p.b_sp),
               "s"(p.w_out), "s"(p.cvec), "s"(p.w_mod), "s"(p.out), "s"(p.ws));
  unsigned epoch, tag, t_start;
  unsigned long long t64;
  asm volatile("s_load_dword %0, %2, 0x0\n\ts_memrealtime %1" : "=&s"(epoch), "=&s"(t64) : "s"(ws));
#define TAG_READY() do { SCHED(); \
    asm volatile("s_waitcnt lgkmcnt(0)\n\ts_mul_i32 %0, %2, 0x9e3779b1\n\ts_add_i32 %0, %0, 0x9e3779b9\n\ts_mov_b64 %1, %3" \
                 : "=&s"(tag), "=&s"(t64r) : "s"(epoch), "s"(t64) : "scc"); t_start = (unsigned)t64r; SCHED(); } while (0)
  unsigned long long t64r;
  const rsrc_t rs = __builtin_amdgcn_make_buffer_rsrc(ws, 0, WS_END, 0x00020000);
  const unsigned l16 = (unsigned)l * 16u;
  STAMP(0);
  const bool is_conv = idx < 20;
  const int quad = xcd * 20 + idx;
  const int R = is_conv ? (xcd & 3) * 5 + (idx >> 2) : (xcd & 3) * 3 + 15 + (idx >> 2);
  const int c = (xcd >> 2) * 4 + (idx & 3);
  const unsigned fwi = WS_F4 + (unsigned)(xcd & 3) * F4S, fz = fwi + (WS_FZ - WS_FWI);
  const int n16 = l & 15, h4 = l >> 4;
  u32x4 wsp[4][8];
#define ROTC 2
#define WAUX 0
  const int rotg = ((idx >> 4) ^ ((ROTC & 2) ? idx >> 1 : 0)) & 1, rotb = (idx >> 2) & 3, rotr = (ROTC & 1) ? (idx & 1) * 2 : 0;
  const rsrc_t rsp = __builtin_amdgcn_make_buffer_rsrc((void*)p.w_sp, 0, S_ * S_ * 2, 0x00020000);
  const unsigned wsp_vo = (unsigned)(((l >> 2) & 3) * 8 * 4096 + h4 * 64 + ((l + rotb) & 3) * 16);
#define LDW(k) LDW_(k)
#define WSP_TR(g) WSP_TR_(g)
#define WROW R
#define LDW_(k) wsp[((k) >> 2) & 3][((k) >> 4) * 4 + ((k) & 3)] = __builtin_amdgcn_raw_buffer_load_b128(rsp, wvo, \
    (unsigned)((WROW * 64 + 16 * ((((k) >> 2) & 2) ^ rotr) + 4 * (((k) >> 2) & 1) + ((k) & 3)) * 4096 + w * 512 + ((((k) >> 4) ^ rotg) * 256)), WAUX)
#define WSP_TR_(g) quad_transpose(wsp[(g) & 3][((g) >> 2) * 4], wsp[(g) & 3][((g) >> 2) * 4 + 1], wsp[(g) & 3][((g) >> 2) * 4 + 2], wsp[(g) & 3][((g) >> 2) * 4 + 3])
  const rsrc_t rwi = __builtin_amdgcn_make_buffer_rsrc((void*)p.w_in, 0, D_ * F_ * 2, 0x00020000);
  const rsrc_t rwo = __builtin_amdgcn_make_buffer_rsrc((void*)p.w_out, 0, FH * D_ * 2, 0x00020000);
  const rsrc_t rq = __builtin_amdgcn_make_buffer_rsrc((void*)p.q, 0, S_ * D_ * 2, 0x00020000);
  auto hold = [&](unsigned tt) { while ((unsigned)__builtin_amdgcn_s_memrealtime() - t_start < tt) __builtin_amdgcn_s_sleep(2); };
  auto ne2 = [&](unsigned x, unsigned y) { unsigned d; asm("v_bitop3_b32 %0, %1, %2, %3 bitop3:0x7e" : "=v"(d) : "v"(x), "v"(y), "s"(tag)); return d; };
  volatile LAS unsigned* lf = (volatile LAS unsigned*)(lds + L_FLG);
  auto lf_set = [&](int i) { asm volatile("s_waitcnt lgkmcnt(0)" ::: "memory"); if (l == 0) lf[i] = tag; };
  auto lf_wait = [&](int i0, int n) {
    _Pragma("unroll 1") for (unsigned spins = 0; spins < (SPIN_CAP << 4); ++spins) {
      const unsigned v = lf[i0 + (l & (n - 1))];
      if (__all(v == tag)) break;
      __builtin_amdgcn_s_sleep(1);
    }
    asm volatile("" ::: "memory");
  };
  const unsigned b2o = fwi + (unsigned)((8 + c) * 20 + 5 * (w & 3) + (l < 5 ? l : 0)) * 128u;
  auto b2_wait = [&](u32x2 fv, int slp) {
    if (!__all(fv[0] == tag)) {
      _Pragma("unroll 1") for (unsigned spins = 0; spins < SPIN_CAP; ++spins) {
        if (fv[0] != tag) fv = ld8(rs, b2o);
        if (__all(fv[0] == tag)) break;
        if (slp) __builtin_amdgcn_s_sleep(8); else __builtin_amdgcn_s_sleep(4);
      }
    }
    asm volatile("" ::: "memory");
    if (l < 5) lf[44 + 5 * w + l] = fv[1];
    if (l == 0) lf[36 + w] = tag;
  };
  auto load_b2 = [&](int k0, int k1) {
    const unsigned a0 = (unsigned)(35 * w + (idx >> 2) * 4) * 1024u, gb = WS_WINF + (unsigned)(8 + c) * WBLK;
    unsigned a = a0, g = gb + a0;
#pragma unroll
    for (int k = k0; k < k1; ++k) {
      if (k == k0 || (k & 3) == 3) {
        a = k + (idx >> 2) * 4 >= 35 ? a0 - 35840u : a0;
        g = gb + a;
        asm volatile("" : "+s"(a), "+s"(g));
      }
      dma1k(rs, lds + a + k * 1024, l16, g + (unsigned)k * 1024u);
    }
  };
  char* const wo_st = lds + 122880;
  auto wo_ld = [&]() {
    const int U = R < 28 ? R : 0, u2 = U >> 1, fi = (u2 + 1) % 3;
    const int c0 = 4 * (U & 1) + (u2 < 8 ? u2 >> 1 : u2 < 11 ? 0 : 2);
    const int b0 = u2 < 8 ? CB * c0 + 2 * (u2 & 1) + 96 : CB * (c0 + (fi >> 1)) + 48 + 2 * (fi & 1), d1 = u2 < 8 ? -96 : 16, d2 = d1 + 16;
    const int b3 = u2 < 8 ? b0 - 64 : CB * (c0 + 1) + 50 + 16 * fi;
#pragma unroll
    for (int r = 0; r < 5; ++r) {
      const int g = r * 64 + l, rho = g / 10;
      const int fb = (rho & 16) ? ((rho & 2) ? b3 : b0 + d2) : ((rho & 2) ? b0 + d1 : b0);
      __builtin_amdgcn_raw_ptr_buffer_load_lds(rwo, (LAS void*)(wo_st + r * 1024), 16, (unsigned)((fb + (rho & 13)) * (D_ * 2) + (g % 10) * 16 + c * 160), 0, 0, 0);
    }
  };
  auto rt_ld = [&](int i0, char* st) {
    const int cb = (i0 < 160 ? 8 : 0) + (i0 & 7), Q = (i0 < 160 ? i0 : i0 - 160) >> 3;
#pragma unroll
    for (int r = 0; r < 7; ++r) {
      const int g = r * 64 + l;
      __builtin_amdgcn_raw_ptr_buffer_load_lds(rwi, (LAS void*)(st + r * 1024), 16, (unsigned)((g / 14) * (F_ * 2) + (g % 14) * 16 + Q * 32 * F_ * 2 + cb * CB * 2),
                                               0, 0, NT);
    }
  };
  auto rt_job = [&](int i0, char* st) {
    const int cb = (i0 < 160 ? 8 : 0) + (i0 & 7), Q = (i0 < 160 ? i0 : i0 - 160) >> 3;
    const int col = 640 * (l >> 5) + 32 * Q + (l & 31);
    DRAIN();
    u32x4 x[7], o[7];
    f32x2 xu[7][4];
    {
      const char* bp = st + (4 * h4 + (n16 >> 2)) * 224 + (n16 & 3) * 8;
#pragma unroll
      for (int j = 0; j < 7; ++j) x[j] = trfrag(bp + j * 32, 224);
#pragma unroll
      for (int j = 0; j < 7; ++j)
#pragma unroll
        for (int e = 0; e < 4; ++e) { xu[j][e] = (f32x2){bflo(x[j][e]), bfhi(x[j][e])}; asm volatile("" : "+v"(xu[j][e])); }
    }
    unsigned mk[4][2], pv = (unsigned)col * 8u, cw = l >= 32 ? (unsigned)(7168 + (l - 32) * 4) : (unsigned)(7298 + l * 2 - 4), m12 = n16 == 12 ? ~0u : 0u;
    unsigned fgo = l < 32 ? (unsigned)(l >> 3) * F4S + (unsigned)(l & 7) * 16u : 0x80000000u;
#pragma unroll
    for (int i = 0; i < 4; ++i) {
      mk[i][0] = n16 == 4 * i ? 0x3F803F80u : 0u; mk[i][1] = n16 == 4 * i + 1 ? ~0u : 0u;
      asm volatile("" : "+v"(mk[i][0]), "+v"(mk[i][1]));
    }
    asm volatile("" : "+v"(pv), "+v"(cw), "+v"(m12), "+v"(fgo));
    unsigned ob = WS_WINF + (unsigned)cb * WBLK + (unsigned)(Q * 7) * 1024u;
    asm volatile("" : "+s"(ob));
    u32x2 pp8[8];
    hold(CB_T);
    _Pragma("unroll 1") for (unsigned spins = 0; spins < SPIN_CAP; ++spins) {
#pragma unroll
      for (int i = 0; i < 8; ++i) pp8[i] = __builtin_amdgcn_raw_buffer_load_b64(rs, pv, WS_MODP + (unsigned)i * 10240u, SC1);
      if (!__any(((ne2(pp8[0][0], pp8[1][0]) | ne2(pp8[2][0], pp8[3][0]) | ne2(pp8[4][0], pp8[5][0])) | ne2(pp8[6][0], pp8[7][0])) != 0u)) break;
      __builtin_amdgcn_s_sleep(4);
    }
    STAMPF(9);
    __builtin_amdgcn_s_setprio(3);
    {
      auto pf = [&](int i) { return __uint_as_float(pp8[i][1]); };
      const float t = rbf(((pf(0) + pf(1)) + (pf(2) + pf(3))) + ((pf(4) + pf(5)) + (pf(6) + pf(7))));
      if (l >= 32) *(float*)(st + cw) = rbf(1.0f + t);
      else *(u16*)(st + cw + 2) = (u16)(__float_as_uint(t) >> 16);
    }
    asm volatile("" ::: "memory");
    const f32x4 ca = *(const volatile LAS f32x4*)(st + 7168 + h4 * 16), cc = *(const volatile LAS f32x4*)(st + 7168 + 64 + h4 * 16);
    const u32x2 e0 = *(const volatile LAS u32x2*)(st + 7296 + h4 * 8), e1 = *(const volatile LAS u32x2*)(st + 7328 + h4 * 8);
    SCHED();
#pragma unroll
    for (int j = 0; j < 7; ++j) {
      const f32x2 y0 = xu[j][0] * (f32x2){ca[0], ca[1]}, y1 = xu[j][1] * (f32x2){ca[2], ca[3]}, y2 = xu[j][2] * (f32x2){cc[0], cc[1]}, y3 = xu[j][3] * (f32x2){cc[2], cc[3]};
      o[j] = (u32x4){pack2(y0[0], y0[1]), pack2(y1[0], y1[1]), pack2(y2[0], y2[1]), pack2(y3[0], y3[1])};
      if (j == 6) {
#pragma unroll
        for (int e = 0; e < 4; ++e) o[j][e] = (o[j][e] & ~m12) | mk[3][0];
      }
      ST16S(o[j], rs, l16 + (unsigned)(j & 3) * 1024u, ob + (unsigned)(j >> 2) * 4096u, SC1);
      SCHED();
    }
    __builtin_amdgcn_s_setprio(0);
    STAMPF(11);
    f32x4 d[2] = {{0.f, 0.f, 0.f, 0.f}, {0.f, 0.f, 0.f, 0.f}};
#pragma unroll
    for (int j = 0; j < 7; ++j) {
      const unsigned one = mk[j & 3][0], sr = mk[j & 3][1];
      d[j >> 2] = MFMA16(((u32x4){one, one, one, one}), o[j], d[j >> 2]);
      d[j >> 2] = MFMA16(((u32x4){sr & e0[0], sr & e0[1], sr & e1[0], sr & e1[1]}), x[j], d[j >> 2]);
    }
#pragma unroll
    for (int m = 0; m < 2; ++m)
      st16(rs, WS_CSP + (unsigned)((cb * 20 + Q) * 2 + m) * 1024u + l16, (u32x4){tag, __float_as_uint(d[m][0]), tag, __float_as_uint(d[m][1])});
    STAMPF(13);
    u32x4 tg4 = {tag, t_start, tag, tag};
    asm volatile("s_waitcnt vmcnt(2)" : "+v"(tg4) :: "memory");
    ST16S(tg4, rs, fgo, WS_F4 + (unsigned)(cb * 20 + Q) * 128u, SC1);
  };

  auto red8 = [&](float v) { v += dpp_f<0x128>(v); return sum_rows(v); };
  const rsrc_t rwm = __builtin_amdgcn_make_buffer_rsrc((void*)p.w_mod, 0, D_ * 3840 * 2, 0x00020000);
  auto gemv_load = [&](u32x4* pre, int blk, int sl, int np) {
    pre[10][0] = *(const unsigned*)(p.cvec + 8 * np * sl + 2 * (l < 4 * np ? l : 0));
#pragma unroll
    for (int pp = 0; pp < np; ++pp)
      pre[pp] = __builtin_amdgcn_raw_buffer_load_b128(rwm, (unsigned)((l >> 3) * 7680 + (l & 7) * 16), (unsigned)((8 * np * sl + pp * 8) * 7680 + blk * 128), NT);
  };
  auto gemv_fin = [&](float* acc, const u32x4* pre, int np, char* scr) {
    const unsigned cv2 = pre[10][0];
    unsigned* svt = (unsigned*)scr + w * 96;
#pragma unroll
    for (int e = 0; e < 2; ++e) {
      const int row = 2 * l + e;
      const float cv = e ? bfhi(cv2) : bflo(cv2);
      if (l < 4 * np) svt[(row & 7) * 12 + (row >> 3)] = __float_as_uint(rbf(cv * __builtin_amdgcn_rcpf(1.0f + __expf(-cv))));
    }
    asm volatile("s_waitcnt lgkmcnt(0)" ::: "memory");
    u32x4 sv[3];
#pragma unroll
    for (int i = 0; i < (np + 3) / 4; ++i) sv[i] = *(const u32x4*)(svt + (l >> 3) * 12 + 4 * i);
    SCHED();
#pragma unroll
    for (int e = 0; e < 8; ++e) acc[e] = 0.f;
#pragma unroll
    for (int pp = 0; pp < np; ++pp) {
      const unsigned sh = sv[pp >> 2][pp & 3], sl2 = sh >> 16;
#pragma unroll
      for (int e = 0; e < 4; ++e) {
        const unsigned x = pre[pp][e];
        acc[2 * e] = __builtin_amdgcn_fdot2_f32_bf16(__builtin_bit_cast(bf2_t, x), __builtin_bit_cast(bf2_t, sl2), acc[2 * e], false);
        acc[2 * e + 1] = __builtin_amdgcn_fdot2_f32_bf16(__builtin_bit_cast(bf2_t, x), __builtin_bit_cast(bf2_t, sh), acc[2 * e + 1], false);
      }
    }
#pragma unroll
    for (int e = 0; e < 8; ++e) acc[e] = red8(acc[e]);
  };
  auto gemv_put = [&](const float* acc, unsigned off) {
    float v = acc[0];
#pragma unroll
    for (int e = 1; e < 8; ++e) { v = (l >> 3) == e ? acc[e] : v; asm("" : "+v"(v)); }
    st8(rs, off + (unsigned)l * 8u, (u32x2){tag, (unsigned)__builtin_amdgcn_ds_bpermute(((l & 7) * 8 + (l >> 3)) * 4, (int)__float_as_uint(v))});
  };
  auto cs_ld = [&](u32x4* v, int cj) {
    const int cb = cj / 7, j = cj % 7;
#pragma unroll
    for (int k = 0; k < 5; ++k)
      v[k] = __builtin_amdgcn_raw_buffer_load_b128(rs, (unsigned)(h4 * 10240 + n16 * 16), WS_CSP + (unsigned)((cb * 40 + (j >> 2) + 2 * k) * 1024 + (j & 3) * 256), SC1);
  };
  auto cs_fin = [&](u32x4* v, int cj) {
    auto bad = [&]() { return __any(((ne2(v[0][0], v[0][2]) | ne2(v[1][0], v[1][2]) | ne2(v[2][0], v[2][2])) | ne2(v[3][0], v[3][2]) | ne2(v[4][0], v[4][2])) != 0u); };
    if (bad()) {
      _Pragma("unroll 1") for (unsigned spins = 0; spins < SPIN_CAP; ++spins) {
        __builtin_amdgcn_s_sleep(8);
        cs_ld(v, cj);
        if (!bad()) break;
      }
    }
    float t0 = 0.f, t1 = 0.f;
#pragma unroll
    for (int k = 0; k < 5; ++k) { t0 += __uint_as_float(v[k][1]); t1 += __uint_as_float(v[k][3]); }
    t0 = sum_rows(t0); t1 = sum_rows(t1);
    if (l < 16) st16(rs, WS_CS + (unsigned)((cj / 7) * 2048 + (cj % 7) * 256) + l16, (u32x4){tag, __float_as_uint(t0), tag, __float_as_uint(t1)});
  };
  const bool cs2 = w == 2 && R >= 20 && R < 27, cs1 = w == 2 && R >= 25;
  u32x4 csv[5];
  typedef const __attribute__((address_space(4))) unsigned* cptr_t;

  if (__builtin_expect(is_conv, 1)) {
    if (w >= 4) {
      TOUCH_AT(1);
      const int pw = w - 4;
      const rsrc_t ra = __builtin_amdgcn_make_buffer_rsrc((void*)((idx & 2) ? p.v : p.k), 0, S_ * D_ * 2, 0x00020000);
      const rsrc_t rb = __builtin_amdgcn_make_buffer_rsrc((void*)((idx & 2) ? p.k : p.v), 0, S_ * D_ * 2, 0x00020000);
      const rsrc_t rk = __builtin_amdgcn_make_buffer_rsrc((void*)p.k, 0, S_ * D_ * 2, 0x00020000);
      const rsrc_t rv = __builtin_amdgcn_make_buffer_rsrc((void*)p.v, 0, S_ * D_ * 2, 0x00020000);
#define NROT 2
      const unsigned vo = (unsigned)((128 * pw + 2 * l) * (D_ * 2));
      const unsigned so0 = (unsigned)((idx & 1) * (D_ * 2) + quad * 8), so1 = (unsigned)((1 - (idx & 1)) * (D_ * 2) + quad * 8);
      u32x2 g[3][4], kh;
      auto gl = [&](int s, int j0, int j1) {
        __builtin_amdgcn_s_setprio(0);
#pragma unroll
        for (int j = j0; j < j1; ++j) {
          if (s < NROT) g[s][j] = __builtin_amdgcn_raw_buffer_load_b64(j < 2 ? ra : rb, vo, ((j & 1) ? so1 : so0) + (unsigned)(s * 512 * D_ * 2), 0);
          else g[s][j] = __builtin_amdgcn_raw_buffer_load_b64(j < 2 ? rk : rv, vo + (unsigned)((j & 1) * D_ * 2), (unsigned)(quad * 8 + s * 512 * D_ * 2), 0);
          SCHED();
        }
        __builtin_amdgcn_s_setprio(3);
      };
      const cptr_t sp = (cptr_t)(p.shift_k + quad * 16);
      unsigned spv[8];
#pragma unroll
      for (int i = 0; i < 8; ++i) spv[i] = sp[i];
      __builtin_amdgcn_s_sleep(3);
      gl(0, 0, 4);
      {
        const int row = 512 * (l >> 2) + 128 * pw - 3 + (l & 3);
        kh = __builtin_amdgcn_raw_buffer_load_b64(rk, (l >= 12 || (l & 3) == 3 || row < 0) ? 0x80000000u : (unsigned)(row * D_ * 2 + quad * 8), 0, 0);
      }
      TAG_READY();
      f32x2 wt[2][4];
#pragma unroll
      for (int d = 0; d < 2; ++d) {
        wt[d][0] = (f32x2){bflo(spv[4 * d]), bflo(spv[4 * d + 2])}; wt[d][1] = (f32x2){bfhi(spv[4 * d]), bfhi(spv[4 * d + 2])};
        wt[d][2] = (f32x2){bflo(spv[4 * d + 1]), bflo(spv[4 * d + 3])}; wt[d][3] = (f32x2){bfhi(spv[4 * d + 1]), bfhi(spv[4 * d + 3])};
        asm volatile("" : "+s"(wt[d][0]), "+s"(wt[d][1]), "+s"(wt[d][2]), "+s"(wt[d][3]));
      }
      SCHED();
      {
        const int j = idx * 4 + pw;
        const unsigned o = (unsigned)((512 + 64 * (j >> 2) + l) * (D_ * 2) + (((160 * xcd) >> 7) + ((j >> 1) & 1)) * 128);
        __builtin_amdgcn_raw_ptr_buffer_load_lds((j & 1) ? ra : rb, (LAS void*)(lds + 98304), 4, j < 64 ? o : 0x80000000u, 0, 0, 0);
      }
      SCHED();
      gl(1, 0, 4);
      STAMP(21);
      unsigned up = (unsigned)(L_U + 512 + 256 * pw + (l >> 2) * 16 + (3 - (l & 3)) * 4);
      unsigned lfo = l == 0 ? (unsigned)(L_FLG + 4 * pw) : (unsigned)(100352 + tid * 4), lfr = (unsigned)(L_FLG + (l & 3) * 4);
      unsigned yr = (unsigned)(L_U + (n16 >> 2) * U_CH + (128 * pw + 32 * h4 + 8 * (n16 & 1) + 4 * ((n16 >> 1) & 1)) * 2);
      unsigned yo = (unsigned)(8 * pw + (l >> 3)) * 20480u + (unsigned)quad * 128u + (unsigned)(l & 7) * 16u;
      unsigned mo = (unsigned)((n16 < 12 ? n16 >> 2 : 0) * D_ + quad * 4 + (l & 3)) * 8u + (unsigned)h4 * 40960u;
      unsigned k3o = (unsigned)(16 * pw + (l >> 2)) * 20480u + (unsigned)(((l >> 1) & 1) * 10240 + (2 * quad + (l & 1)) * 16);
      unsigned k3d = (unsigned)(L_U + (2 * (l & 1) + ((l >> 1) & 1)) * U_CH + 512 + (1536 + 128 * pw + 8 * (l >> 2)) * 2);
      unsigned xfo = (unsigned)(((4 * (l >> 5) + pw) * 4 + ((l >> 3) & 3)) * 160 + quad) * 128u + (unsigned)(l & 7) * 16u;
      u32x4 tg4 = {tag, tag, tag, tag};
      asm volatile("" : "+v"(up), "+v"(lfo), "+v"(lfr), "+v"(yr), "+v"(yo), "+v"(mo), "+v"(k3o), "+v"(k3d), "+v"(xfo), "+v"(tg4));
      unsigned so = WS_ST1 + (unsigned)xcd * 16384u + (unsigned)pw * 1024u;
      asm volatile("" : "+s"(so));
      auto pf_set = [&](int i) { asm volatile("" ::: "memory"); *(volatile LAS unsigned*)(lds + lfo + 4 * i) = tg4[0]; };
      auto pf_wait = [&](int i0) {
        _Pragma("unroll 1") for (unsigned spins = 0; spins < (SPIN_CAP << 4); ++spins) {
          if (__all(*(volatile LAS unsigned*)(lds + lfr + 4 * i0) == tag)) break;
          __builtin_amdgcn_s_sleep(1);
        }
        asm volatile("" ::: "memory");
      };
      unsigned hl[2][3];
      auto halo = [&](int s) {
#pragma unroll
        for (int i = 0; i < 6; ++i) { hl[i & 1][i >> 1] = (unsigned)__builtin_amdgcn_readlane(kh[i & 1], 4 * s + (i >> 1)); asm volatile("" : "+v"(hl[i & 1][i >> 1])); }
      };
      auto kv = [&](auto S__, auto N__) {
        constexpr int s = decltype(S__)::value;
        u32x2* x = g[s];
        SCHED();
        asm volatile("s_waitcnt vmcnt(%5)" : "+v"(x[0]), "+v"(x[1]), "+v"(x[2]), "+v"(x[3]), "+v"(kh) : "n"(decltype(N__)::value));
        if (s == 2) STAMP(27);
        if (s == 0) halo(0);
        if (s < NROT) {
          if (idx & 1) { asm volatile(""); const u32x2 t = x[0], u = x[2]; x[0] = x[1]; x[1] = t; x[2] = x[3]; x[3] = u; }
          asm volatile("" : "+v"(x[0]), "+v"(x[1]), "+v"(x[2]), "+v"(x[3]));
          if (idx & 2) { asm volatile(""); const u32x2 t = x[0], u = x[1]; x[0] = x[2]; x[1] = x[3]; x[2] = t; x[3] = u; }
          asm volatile("" : "+v"(x[0]), "+v"(x[1]), "+v"(x[2]), "+v"(x[3]));
        }
        f32x2 kf[2][5], o[2][2];
#pragma unroll
        for (int d = 0; d < 2; ++d) {
          unsigned kk[5];
          kk[3] = x[0][d]; kk[4] = x[1][d];
          kk[2] = __builtin_amdgcn_update_dpp(hl[d][2], kk[4], 0x138, 0xf, 0xf, false);
          kk[1] = __builtin_amdgcn_update_dpp(hl[d][1], kk[3], 0x138, 0xf, 0xf, false);
          kk[0] = __builtin_amdgcn_update_dpp(hl[d][0], kk[2], 0x138, 0xf, 0xf, false);
#pragma unroll
          for (int j = 0; j < 5; ++j) kf[d][j] = (f32x2){bflo(kk[j]), bfhi(kk[j])};
        }
#pragma unroll
        for (int t = 0; t < 5; ++t) {
#pragma unroll
          for (int i = 0; i < 4; ++i) {
            const int d = i >> 1, r = i & 1;
            if (t == 4) o[d][r] *= (f32x2){bflo(x[2 + r][d]), bfhi(x[2 + r][d])};
            else if (t == 0) o[d][r] = kf[d][r + 3] * wt[d][0];
            else o[d][r] = __builtin_elementwise_fma(kf[d][r + 3 - t], wt[d][t], o[d][r]);
          }
          asm volatile("" : "+v"(o[0][0]), "+v"(o[0][1]), "+v"(o[1][0]), "+v"(o[1][1]));
        }
#pragma unroll
        for (int i = 0; i < 4; ++i) *(unsigned*)(lds + up + i * U_CH + s * 1024) = pack2(o[i >> 1][1][i & 1], o[i >> 1][0][i & 1]);
        pf_set(4 * s);
        SCHED();
        if (s < 2) halo(s + 1);
        SCHED();
      };
      u32x4 sv[4], qv[4];
      u32x2 mv[2];
      auto ld_e = [&]() {
#pragma unroll
        for (int s = 0; s < 4; ++s) sv[s] = __builtin_amdgcn_raw_buffer_load_b128(rs, l16, so + (unsigned)s * 4096u, SC1);
        SCHED();
      };
      auto s_bad = [&]() { return __any(((ne2(sv[0][1], sv[0][3]) | ne2(sv[1][1], sv[1][3]) | ne2(sv[2][1], sv[2][3])) | ne2(sv[3][1], sv[3][3])) != 0u); };
      kv(IC<0>(), IC<5>());
      STAMP(23);
#define G2A 4
      gl(2, 0, G2A);
      kv(IC<1>(), IC<G2A>());
      gl(2, G2A, 4);
      STAMPF(9);
      u32x4 k3[2];
      auto ld_k3 = [&]() {
        k3[0] = __builtin_amdgcn_raw_buffer_load_b128(rs, k3o, WS_KV3, SC1);
        k3[1] = __builtin_amdgcn_raw_buffer_load_b128(rs, k3o, WS_KV3 + 5120u, SC1);
      };
      auto ld_m = [&]() {
        mv[0] = __builtin_amdgcn_raw_buffer_load_b64(rs, mo, WS_MODQ, SC1);
        mv[1] = __builtin_amdgcn_raw_buffer_load_b64(rs, mo, WS_MODQ + 20480u, SC1);
      };
      auto ld_q = [&]() {
        asm volatile("" ::: "memory");
        ld_m();
        SCHED();
#pragma unroll
        for (int s = 0; s < 4; ++s) qv[s] = __builtin_amdgcn_raw_buffer_load_b128(rs, yo, WS_QB + (unsigned)s * (32u * 20480u), BULK_AUX);
        SCHED();
      };
      auto k_bad = [&]() { return __any((ne2(k3[0][1], k3[0][3]) | ne2(k3[1][1], k3[1][3])) != 0u); };
      auto k_put = [&]() {
        *(u32x4*)(lds + k3d) = (u32x4){k3[0][0], k3[0][2], k3[1][0], k3[1][2]};
        pf_set(12);
        SCHED();
      };
#define KE_T 0
      const bool early = (unsigned)__builtin_amdgcn_s_memrealtime() - t_start < KE_T;
      if (!early) {
        hold(K_T);
        ld_k3();
        SCHED();
        ld_e();
      } else {
        DRAIN();
      }
      kv(IC<2>(), IC<6>());
      if (KE_T && early) {
        hold(K_T);
        ld_k3();
        SCHED();
        ld_e();
      }
      STAMPF(11);
      const bool kb = k_bad();
      if (!kb) k_put();
      const bool sb = s_bad();
      if (kb) ld_k3();
      if (sb) ld_e(); else ld_q();
      if (kb) {
        _Pragma("unroll 1") for (unsigned spins = 0; spins < SPIN_CAP; ++spins) {
          if (!k_bad()) break;
          __builtin_amdgcn_s_sleep(4);
          ld_k3();
        }
        k_put();
      }
      STAMPF(18);
      if (sb) {
        _Pragma("unroll 1") for (unsigned spins = 0; spins < SPIN_CAP; ++spins) {
          if (!s_bad()) break;
          __builtin_amdgcn_s_sleep(4);
          ld_e();
        }
        ld_q();
      }
      STAMPF(17);
      STAMP(2);
      u32x2 yv[4][2];
      auto ld_y = [&](int s) {
#pragma unroll
        for (int e = 0; e < 2; ++e) yv[s][e] = trd(lds + yr + 1024 * s + 32 * e);
      };
      float r1[4][2], m1[4][2];
#pragma unroll
      for (int i = 0; i < 8; ++i) {
        const unsigned x = sv[i >> 1][2 * (i & 1)];
        r1[i >> 1][i & 1] = __builtin_amdgcn_rcpf(bfhi(x));
        m1[i >> 1][i & 1] = -bflo(x) * r1[i >> 1][i & 1];
        asm volatile("" : "+v"(r1[i >> 1][i & 1]), "+v"(m1[i >> 1][i & 1]));
      }
      pf_wait(20);
      ld_y(0); ld_y(1);
      SCHED();
      if (__any(ne2(mv[0][0], mv[1][0]) != 0u)) {
        _Pragma("unroll 1") for (unsigned spins = 0; spins < SPIN_CAP; ++spins) {
          __builtin_amdgcn_s_sleep(4);
          ld_m();
          if (!__any(ne2(mv[0][0], mv[1][0]) != 0u)) break;
        }
      }
      STAMPF(13);
      f32x4 al, be;
      {
        const float mgf = rbf(sum_rows(__uint_as_float(mv[0][1]) + __uint_as_float(mv[1][1])));
        auto rl = [&](int i) { return __uint_as_float(__builtin_amdgcn_readlane(__float_as_uint(mgf), i)); };
        const f32x4 sa = {rl(0), rl(1), rl(2), rl(3)}, ca = {rl(4), rl(5), rl(6), rl(7)}, ga = {rl(8), rl(9), rl(10), rl(11)};
        al = rbf4(ca + 1.0f) * ga;
        be = sa * ga;
        asm volatile("" : "+v"(al), "+v"(be));
      }
      f32x4 qf[2][2], P[2][2];
      auto xpre = [&](int s) {
#pragma unroll
        for (int e = 0; e < 2; ++e) {
          const float r = r1[s][e], m = m1[s][e];
          const f32x4 qq = {bflo(qv[s][2 * e]), bfhi(qv[s][2 * e]), bflo(qv[s][2 * e + 1]), bfhi(qv[s][2 * e + 1])};
          qf[s & 1][e] = qq;
          P[s & 1][e] = __builtin_elementwise_fma(__builtin_elementwise_fma(qq, (f32x4){r, r, r, r}, (f32x4){m, m, m, m}), al, be);
        }
        asm volatile("" : "+v"(qf[s & 1][0]), "+v"(qf[s & 1][1]), "+v"(P[s & 1][0]), "+v"(P[s & 1][1]));
      };
      auto xfin = [&](int s) {
        u32x4 o;
#pragma unroll
        for (int e = 0; e < 2; ++e) {
          const u32x2 y = yv[s][e];
          const f32x4 yf = {bflo(y[0]), bfhi(y[0]), bflo(y[1]), bfhi(y[1])};
          const f32x4 x = __builtin_elementwise_fma(P[s & 1][e], yf, qf[s & 1][e]);
          o[2 * e] = pack2(x[0], x[1]);
          o[2 * e + 1] = pack2(x[2], x[3]);
        }
        ST16S(o, rs, yo, WS_X1B + (unsigned)s * (32u * 20480u), SC1);
      };
      xpre(0); xfin(0);
      xpre(1); xfin(1);
      SCHED();
      xpre(2); xpre(3);
#define FVR 5
#define MSPLIT 0
      if (MSPLIT) {
        pf_wait(24);
        ld_y(2);
        xfin(2);
        SCHED();
        pf_wait(28);
        ld_y(3);
        xfin(3);
      } else {
        pf_wait(28);
        ld_y(2); ld_y(3);
        xfin(2); xfin(3);
      }
      STAMP(28);
      if (BG == 1 && l == 0) lf[40 + pw] = tag;
      asm volatile("s_waitcnt vmcnt(2)" ::: "memory");
      ST16S(tg4, rs, xfo, WS_FX, SC1);
      SCHED();
      asm volatile("s_waitcnt vmcnt(1)" ::: "memory");
      ST16S(tg4, rs, xfo, WS_FX + 32u * 160u * 128u, SC1);
      if (BG == 2 && l == 0) lf[40 + pw] = tag;
      STAMP(5);
    } else {
      const int ch = w;
      char* U = lds + L_U + ch * U_CH;
      char* G = lds + ch * (4 * G_COPY);
      {
        const rsrc_t rf = __builtin_amdgcn_make_buffer_rsrc((void*)(p.ssm_k + (size_t)(quad * 4 + ch) * S_), 0, S_ * 2, 0x00020000);
        u32x4 fa[4];
        u32x2 fb[4];
        __builtin_amdgcn_s_setprio(3);
#pragma unroll
        for (int k = 0; k < 4; ++k) {
          fa[k] = __builtin_amdgcn_raw_buffer_load_b128(rf, l16 + (unsigned)k * 1024u, 0, NT);
        }
        __builtin_amdgcn_s_setprio(1);
        TAG_READY();
#pragma unroll
        for (int k = 0; k < 4; ++k) {
#pragma unroll
          for (int d = 0; d < 2; ++d)
            fb[k][d] = __builtin_amdgcn_update_dpp(k < 3 ? (unsigned)__builtin_amdgcn_readfirstlane(fa[k < 3 ? k + 1 : k][d]) : 0u, fa[k][d], 0x130, 0xf, 0xf, false);
          const unsigned x[6] = {fa[k][0], fa[k][1], fa[k][2], fa[k][3], fb[k][0], fb[k][1]};
          unsigned y[5];
#pragma unroll
          for (int d = 0; d < 5; ++d) y[d] = __builtin_amdgcn_alignbit(x[d + 1], x[d], 16);
          char* gp = G + 32 + k * 1024 + l16;
          *(u32x4*)(gp) = fa[k];
          *(u32x4*)(gp + G_COPY) = (u32x4){y[0], y[1], y[2], y[3]};
          *(u32x4*)(gp + 2 * G_COPY) = (u32x4){x[1], x[2], x[3], x[4]};
          *(u32x4*)(gp + 3 * G_COPY) = (u32x4){y[1], y[2], y[3], y[4]};
          if (k == 0) {
            const unsigned f0 = __builtin_amdgcn_readfirstlane(x[0]), f1 = __builtin_amdgcn_readfirstlane(x[1]);
            const unsigned long long f64 = (unsigned long long)f0 | ((unsigned long long)f1 << 32);
            const unsigned long long hi = l ? (f64 << (64 - 16 * (l & 3))) : 0ull;
            if (l < 4) {
              *(u32x4*)(G + l * G_COPY) = (u32x4){0u, 0u, 0u, 0u};
              *(u32x4*)(G + l * G_COPY + 16) = (u32x4){0u, 0u, (unsigned)hi, (unsigned)(hi >> 32)};
            }
            if (l >= 32) *(u32x4*)(U + (l - 32) * 16) = (u32x4){0u, 0u, 0u, 0u};
          }
        }
        asm volatile("s_waitcnt lgkmcnt(0)" ::: "memory");
        STAMP(1);
      }
      const int a = n16, h = h4, jj = h >> 1, e2 = h & 1;
      const char* ap = G + ((a + 1) & 3) * G_COPY + 2 * (16 * jj + a - 8 * e2 + 9 - ((a + 1) & 3));
      const char* bp = U + 512 + 32 * (a - jj) + 16 * e2;
      auto lda = [&](u32x4* A, int r, int p0, int np) {
#pragma unroll
        for (int pp = 0; pp < np; ++pp) {
          u32x2 lo = *(const volatile LAS u32x2*)(ap + 512 * (p0 + pp) + 64 * r);
          u32x2 hi = *(const volatile LAS u32x2*)(ap + 512 * (p0 + pp) + 64 * r + 8);
          A[pp] = (u32x4){lo[0], lo[1], hi[0], hi[1]};
        }
      };
      u32x4 AD;
      {
        const unsigned dDr = (*(cptr_t)(p.ssm_D + quad * 4 + (ch & 2)) >> (16 * (ch & 1))) & 0xffffu;
        const unsigned dv = h == (a >> 3) ? ((a & 1) ? dDr : dDr << 16) : 0u;
        const int dw = (7 - (a & 7)) >> 1;
#pragma unroll
        for (int d = 0; d < 4; ++d) AD[d] = dw == d ? dv : 0u;
      }
      u32x4 A0[8][4];
      f32x4 acc[8];
#pragma unroll
      for (int n = 0; n < 8; ++n) acc[n] = (f32x4){0.f, 0.f, 0.f, 0.f};
#pragma unroll
      for (int r = 0; r < 8; ++r) lda(A0[r], r, 0, 4);
      auto yput = [&](int s) {
#pragma unroll
        for (int e = 0; e < 2; ++e)
          *(u32x2*)(U + 1024 * s + 512 * e + (16 * a + 4 * h) * 2) = (u32x2){pack2(acc[2 * s + e][0], acc[2 * s + e][1]), pack2(acc[2 * s + e][2], acc[2 * s + e][3])};
        asm volatile("" ::: "memory");
        if (l == 0) lf[16 + 4 * s + ch] = tag;
      };
      auto stage = [&](auto S__) {
        constexpr int s = decltype(S__)::value, NH = s < 2 ? 4 - 2 * s : 0, NP = 8 - 2 * s, NB = s < 2 ? 2 : 8;
        u32x4 A1[3][NH > 0 ? NH : 1], B[NB][2];
        auto ldb = [&](int r) {
#pragma unroll
          for (int e = 0; e < 2; ++e) B[r % NB][e] = *(const u32x4*)(bp + 512 * (2 * s + e) - 64 * r);
        };
        if (NH) { lda(A1[0], 0, 4, NH); lda(A1[1], 1, 4, NH); }
        SCHED();
        if (s > 0) yput(s - 1);
        lf_wait(4 * s, 4);
        if (s == 0) STAMP(3);
#pragma unroll
        for (int r = 0; r < NB; ++r) ldb(r);
        SCHED();
#pragma unroll
        for (int e = 0; e < 2; ++e) acc[2 * s + e] = MFMA16(AD, B[0][e], acc[2 * s + e]);
#pragma unroll
        for (int r = 0; r < 8; ++r) {
          if (NH && r + 2 < 8) lda(A1[(r + 2) % 3], r + 2, 4, NH);
          SCHED();
#pragma unroll
          for (int e = 0; e < 2; ++e)
#pragma unroll
            for (int pp = 0; pp < NP - e; ++pp) acc[2 * s + e + pp] = MFMA16(pp < 4 ? A0[r][pp] : A1[r % 3][pp < 4 ? 0 : pp - 4], B[r % NB][e], acc[2 * s + e + pp]);
          SCHED();
          if (r + NB < 8) ldb(r + NB);
        }
      };
      stage(IC<0>());
      STAMP(4);
      stage(IC<1>());
      STAMPF(9);
      u32x2 fv;
      if (MSPLIT) {
        u32x4 B[8][2];
        auto ldb = [&](int r, int m0) {
#pragma unroll
          for (int m = 0; m < 2; ++m) B[r][m] = *(const u32x4*)(bp + 512 * (m0 + m) - 64 * r);
        };
        yput(1);
        if (w == 3) wo_ld();
        __builtin_amdgcn_s_setprio(3);
        lf_wait(8, 4);
        STAMP(22);
#pragma unroll
        for (int r = 0; r < 8; ++r) ldb(r, 4);
        SCHED();
#pragma unroll
        for (int m = 0; m < 2; ++m) acc[4 + m] = MFMA16(AD, B[0][m], acc[4 + m]);
#pragma unroll
        for (int r = 0; r < 8; ++r) {
          acc[4] = MFMA16(A0[r][0], B[r][0], acc[4]);
          acc[5] = MFMA16(A0[r][1], B[r][0], acc[5]);
          acc[5] = MFMA16(A0[r][0], B[r][1], acc[5]);
          SCHED();
        }
#pragma unroll
        for (int r = 0; r < 8; ++r) {
          acc[6] = MFMA16(A0[r][2], B[r][0], acc[6]);
          acc[7] = MFMA16(A0[r][3], B[r][0], acc[7]);
          acc[6] = MFMA16(A0[r][1], B[r][1], acc[6]);
          acc[7] = MFMA16(A0[r][2], B[r][1], acc[7]);
          SCHED();
          if (r == 0) yput(2);
          if (r == FVR) fv = ld8(rs, b2o);
          if (r == 3) {
            lf_wait(12, 4);
#pragma unroll
            for (int x = 0; x < 4; ++x) ldb(x, 6);
          }
          if (r > 3) ldb(r, 6);
          SCHED();
        }
#pragma unroll
        for (int m = 0; m < 2; ++m) acc[6 + m] = MFMA16(AD, B[0][m], acc[6 + m]);
#pragma unroll
        for (int r = 0; r < 8; ++r) {
          acc[6] = MFMA16(A0[r][0], B[r][0], acc[6]);
          acc[7] = MFMA16(A0[r][1], B[r][0], acc[7]);
          acc[7] = MFMA16(A0[r][0], B[r][1], acc[7]);
          SCHED();
        }
        yput(3);
      } else {
        u32x4 B[4][4];
        auto ldb = [&](int r) {
#pragma unroll
          for (int m = 0; m < 4; ++m) B[r & 3][m] = *(const u32x4*)(bp + 512 * (4 + m) - 64 * r);
        };
        yput(1);
        if (w == 3) wo_ld();
        __builtin_amdgcn_s_setprio(3);
        lf_wait(8, 8);
        STAMP(22);
#pragma unroll
        for (int r = 0; r < 4; ++r) ldb(r);
        SCHED();
#pragma unroll
        for (int m = 0; m < 4; ++m) acc[4 + m] = MFMA16(AD, B[0][m], acc[4 + m]);
#pragma unroll
        for (int r = 0; r < 8; ++r) {
#pragma unroll
          for (int m = 0; m < 4; ++m)
#pragma unroll
            for (int pp = 0; pp < 4 - m; ++pp) acc[4 + m + pp] = MFMA16(A0[r][pp], B[r & 3][m], acc[4 + m + pp]);
          SCHED();
          if (r + 4 < 8) ldb(r + 4);
          if (r == 1) fv = ld8(rs, b2o);
          SCHED();
        }
#pragma unroll
        for (int e = 0; e < 4; ++e)
          *(u32x2*)(U + 2048 + 512 * e + (16 * a + 4 * h) * 2) = (u32x2){pack2(acc[4 + e][0], acc[4 + e][1]), pack2(acc[4 + e][2], acc[4 + e][3])};
        lf_set(28 + ch);
      }
      STAMP(28);
      __builtin_amdgcn_s_setprio(0);
      b2_wait(fv, 0);
      lf_wait(20, 4);
      DRAIN();
    }
    __builtin_amdgcn_s_setprio(0);
  } else {
    const int at = idx - 20;
    char* const scr = lds + WBLK + 14848;
    auto gv_blk = [&](int j) { return j < 160 ? 30 + (j >> 3) : j < 400 ? (j >> 3) - 20 : j >> 3; };
    auto gv_fin = [&](const u32x4* pre, int j) {
      float acc[8];
      gemv_fin(acc, pre, 10, scr);
      gemv_put(acc, j < 160 ? WS_MODP + (unsigned)((j & 7) * 1280 + (j >> 3) * 64) * 8u : WS_MODQ + (unsigned)((j & 7) * 2560 + ((j >> 3) - 20) * 64) * 8u);
    };
    const int ja = xcd * 12 + at, ai = xcd * 8 + at, bi = xcd * 4 + at - 8, g = w >> 2, wj = w & 3;
    const int rj = w == 0 ? ja : w == 1 ? (at >= 4 ? 92 + ai : -1) : w == 4 ? 160 + ja : w == 5 && at < 8 ? 256 + ai : -1;
    char* const rst = lds + (g ? WBLK : 65536) + wj * 7424;
    u32x4 pre[11];
    if (at >= 8 || g == 0) {
      TOUCH_AT(2);
      const int T = at >= 8 ? 64 + 2 * bi + g : ai;
      unsigned* mds = (unsigned*)(lds + 2048) + 16 * g;
      char* qt = lds + L_RT + g * (16 * 1312);
      u32x4 ld[5];
      const bool e1 = at >= 8 && w == 3;
      if (e1) gemv_load(pre, gv_blk(128 + bi), bi & 7, 10);
      else __builtin_amdgcn_s_sleep(8);
#pragma unroll
      for (int j = 0; j < 5; ++j)
        ld[j] = __builtin_amdgcn_raw_buffer_load_b128(rq, (unsigned)(h4 * D_ * 2 + n16 * 16 + j * 256), (unsigned)((32 * (T >> 1) + 8 * wj + 4 * (T & 1)) * D_ * 2), NT);
      TAG_READY();
      {
        const bf2_t ONE2 = {(__bf16)1.0f, (__bf16)1.0f};
        char* dst = qt + (4 * wj + h4) * 1312 + n16 * 16;
        float sa = 0.f, va = 0.f;
#pragma unroll
        for (int j = 0; j < 5; ++j) {
          *(u32x4*)(dst + j * 256) = ld[j];
#pragma unroll
          for (int d = 0; d < 4; ++d) {
            const unsigned a0 = ld[j][d];
            const bf2_t xa = __builtin_bit_cast(bf2_t, a0);
            sa = __builtin_amdgcn_fdot2_f32_bf16(xa, ONE2, sa, false); va = __builtin_amdgcn_fdot2_f32_bf16(xa, xa, va, false);
          }
        }
        lf_set(8 + w);
        const float ma = sum16(sa) * (1.0f / D_);
        va = __builtin_fmaxf(sum16(va) * (1.0f / D_) - ma * ma, 0.f);
        if (n16 == 0) mds[4 * wj + h4] = pack2(ma, __builtin_amdgcn_sqrtf(rbf(rbf(va) + 1e-5f)));
      }
      if (e1) gv_fin(pre, 128 + bi);
      lf_wait(8 + 4 * g, 4);
      STAMP(23);
      {
        const char* src = qt + (l & 7) * 1312 + (40 * wj + (l >> 3)) * 8;
#pragma unroll
        for (int j = 0; j < 5; ++j) {
          const u32x2 x = *(const u32x2*)(src + j * 64), y = *(const u32x2*)(src + j * 64 + 8 * 1312);
          st16(rs, WS_QB + (unsigned)T * 20480u + (unsigned)(5 * wj + j) * 1024u + l16, (u32x4){x[0], x[1], y[0], y[1]});
        }
      }
      SCHED();
      const int g2 = at < 8 || w < 3 || w == 4 ? 400 : w == 3 ? 384 + bi : 256 + wj * 32 + bi;
      if (g2 < 400) {
        gemv_load(pre, gv_blk(g2), g2 & 7, 10);
        asm volatile("s_waitcnt vmcnt(10)" ::: "memory");
      } else if (w < 2 && rj >= 0) {
        rt_ld(rj, rst);
        asm volatile("s_waitcnt vmcnt(7)" ::: "memory");
      } else {
        DRAIN();
      }
      lf_set(16 + w);
      if (wj == 2) {
        lf_wait(16 + 4 * g, 4);
        st16(rs, WS_ST1 + (unsigned)(l >> 3) * 16384u + (unsigned)T * 128u + (unsigned)(l & 7) * 16u, (u32x4){mds[l & 7], tag, mds[8 + (l & 7)], tag});
        STAMP(22);
      }
      if (w >= 4 && w < 7) lf_set(w);
      if (g2 < 400) gv_fin(pre, g2);
      STAMP(25);
    } else {
      constexpr int KB = L_RT + 16 * 1312;
      TOUCH_AT(3);
      const int g1 = w < 6 ? 2 * ai + wj : 32 + wj * 64 + ai;
      if (w >= 6) {
        const rsrc_t rk = __builtin_amdgcn_make_buffer_rsrc((void*)(w == 6 ? p.k : p.v), 0, S_ * D_ * 2, 0x00020000);
        const unsigned ko = (unsigned)((1536 + 8 * ai) * (D_ * 2) - (w == 6 ? 4096 : 0));
        char* dst = lds + KB + (w == 6 ? 0 : 14336);
        __builtin_amdgcn_s_sleep(3);
#pragma unroll
        for (int n = 0; n < 10; ++n) __builtin_amdgcn_raw_ptr_buffer_load_lds(rk, (LAS void*)(dst + n * 1024), 16, l16, ko + (unsigned)n * 1024u, 0, NT);
        if (w == 6) {
#pragma unroll
          for (int n = 10; n < 14; ++n) __builtin_amdgcn_raw_ptr_buffer_load_lds(rk, (LAS void*)(dst + n * 1024), 16, l16, ko + (unsigned)n * 1024u, 0, NT);
        }
      }
      SCHED();
      u32x4 swt[2];
      swt[0] = *(const u32x4*)(p.shift_k + (tid - 256) * 8);
      swt[1] = *(const u32x4*)(p.shift_k + (256 + l) * 8);
      SCHED();
      if (w >= 6) __builtin_amdgcn_s_sleep(8);
      gemv_load(pre, gv_blk(g1), g1 & 7, 10);
      TAG_READY();
      asm volatile("s_waitcnt vmcnt(11)" : "+v"(swt[0]), "+v"(swt[1]) :: "memory");
      lf_set(8 + w);
      if (w < 6) gv_fin(pre, g1);
      STAMP(25);
      lf_wait(12, 4);
      STAMP(23);
#pragma unroll 1
      for (int n = 0; n < (w == 7 ? 2 : 1); ++n) {
        {
          const int pr = n ? 256 + l : tid - 256;
          const char* kp = lds + KB + 4096 + pr * 4;
          unsigned kk[11], vv[8];
#pragma unroll
          for (int i = 0; i < 11; ++i) kk[i] = *(const unsigned*)(kp + (i - 3) * 1280);
#pragma unroll
          for (int i = 0; i < 8; ++i) vv[i] = *(const unsigned*)(kp + 10240 + i * 1280);
          const u32x4 sw = n ? swt[1] : swt[0];
          const f32x2 wt[4] = {{bflo(sw[0]), bflo(sw[2])}, {bfhi(sw[0]), bfhi(sw[2])}, {bflo(sw[1]), bflo(sw[3])}, {bfhi(sw[1]), bfhi(sw[3])}};
          f32x2 o[8];
#pragma unroll
          for (int i = 0; i < 8; ++i) {
            f32x2 t = (f32x2){bflo(kk[i + 3]), bfhi(kk[i + 3])} * wt[0];
#pragma unroll
            for (int j = 1; j < 4; ++j) t = __builtin_elementwise_fma((f32x2){bflo(kk[i + 3 - j]), bfhi(kk[i + 3 - j])}, wt[j], t);
            o[i] = t * (f32x2){bflo(vv[i]), bfhi(vv[i])};
          }
#pragma unroll
          for (int e = 0; e < 4; ++e) {
            const int i = 7 - 4 * (e & 1);
            st16(rs, WS_KV3 + (unsigned)ai * 20480u + (unsigned)(e * 5120 + pr * 16),
                 (u32x4){pack2(o[i][e >> 1], o[i - 1][e >> 1]), tag, pack2(o[i - 2][e >> 1], o[i - 3][e >> 1]), tag});
          }
        }
      }
      STAMP(22);
      if (w < 7) lf_set(w);
      if (w >= 6) gv_fin(pre, g1);
      STAMPF(30);
    }
    if (w == 6 && ja < 80) {
      hold(AUX_T);
      gemv_load(pre, gv_blk(400 + ja), ja & 7, 10);
      gv_fin(pre, 400 + ja);
    }
    if (rj >= 0) {
      if (w >= 2) { hold(RT2_T); rt_ld(rj, rst); }
      rt_job(rj, rst);
    }
    STAMP(26);
    lf_set(w);
    if (w == 3) { hold(WA_T); wo_ld(); }
    STAMP(3);
    if (w < 4) {
      hold(W_T);
      b2_wait((u32x2){~tag, 0u}, 1);
      lf_wait(0, 8);
      STAMP(4);
    }
    DRAIN();
  }

  STAMPF(16);
  u32x4 wox[5];
  if (w == 3) {
    const char* bp = wo_st + (4 * h4 + (n16 >> 2)) * 160 + (n16 & 3) * 8;
#pragma unroll
    for (int dj = 0; dj < 5; ++dj) wox[dj] = trfrag(bp + dj * 32, 160);
    asm volatile("s_waitcnt lgkmcnt(0)" : "+v"(wox[0]), "+v"(wox[1]), "+v"(wox[2]), "+v"(wox[3]), "+v"(wox[4]) :: "memory");
  }
  auto wo_put = [&]() {
    if (w == 3 && R < 28) {
      const unsigned base = WS_WOUTF + (unsigned)c * WBLK + (unsigned)((R >> 1) * 10240 + (R & 1) * 1024);
#pragma unroll
      for (int dj = 0; dj < 5; ++dj) st16(rs, base + (unsigned)((R & 1) ? dj : (dj + 2) % 5) * 2048u + l16, wox[dj]);
    }
  };
  if (cs2) {
    cs_ld(csv, (8 + c) * 7 + R - 20);
    SCHED();
    load_b2(0, 12);
    SCHED();
    cs_fin(csv, (8 + c) * 7 + R - 20);
  } else if (w < 4) {
    asm volatile("" ::: "memory");
    load_b2(0, 12);
  }
  if (w < 4) {
    load_b2(12, BK);
    if (BG && is_conv) lf_wait(40, 4);
    load_b2(BK, 35);
  }
  wo_put();
  STAMPF(29);
  const unsigned xfb = WS_FX + (unsigned)(((R >> 1) * 4 + (R & 1) * 2 + (c >> 2)) * 160 + l) * 128u;
  unsigned xf3[3];
  if (w == 7) {
    lf_wait(36, 4);
    int dt = (int)(lf[44 + ((l & 31) < 20 ? l & 31 : 0)] - t_start);
    dt = l < 32 ? dt : -dt;
    dt = max(dt, __builtin_amdgcn_update_dpp(0, dt, 0xB1, 0xf, 0xf, true));
    dt = max(dt, __builtin_amdgcn_update_dpp(0, dt, 0x4E, 0xf, 0xf, true));
    dt = max(dt, __builtin_amdgcn_update_dpp(0, dt, 0x124, 0xf, 0xf, true));
    dt = max(dt, __builtin_amdgcn_update_dpp(0, dt, 0x128, 0xf, 0xf, true));
    const int d1 = min(max(max(__builtin_amdgcn_readlane(dt, 0), __builtin_amdgcn_readlane(dt, 16)), 0), 300);
    const int d0 = min(max(max(__builtin_amdgcn_readlane(dt, 32), __builtin_amdgcn_readlane(dt, 48)), 0), 300);
    dt = ((d1 + d0) * YW >> 3) - d0;
    __builtin_amdgcn_s_setprio(YPR);
    STAMPF(24);
    hold((unsigned)(YG_T + dt));
    STAMPF(30);
    xf3[0] = ld4(rs, xfb); xf3[1] = ld4(rs, xfb + 8192u); xf3[2] = ld4(rs, l < 32 ? xfb + 16384u : ~0u);
    __builtin_amdgcn_s_setprio(0);
  }
  SCHED();
  u32x4 k_c;
  unsigned kc_o = w < 2 ? (unsigned)(tid < 112 ? tid : 0) * 16u : 0x80000000u, kc_d = (unsigned)(L_CONST + (tid & 127) * 8);
  auto kc_ld = [&](int z2) { k_c = __builtin_amdgcn_raw_buffer_load_b128(rs, kc_o, WS_CS + (unsigned)(8 * z2 + c) * 2048u, SC1); };
  auto consts_put = [&](int z2) {
    if (w < 2) {
      if (__any(ne2(k_c[0], k_c[2]) != 0u)) {
        _Pragma("unroll 1") for (unsigned spins = 0; spins < SPIN_CAP; ++spins) {
          __builtin_amdgcn_s_sleep(8);
          kc_ld(z2);
          if (!__any(ne2(k_c[0], k_c[2]) != 0u)) break;
        }
      }
      if (z2) *(u32x2*)(lds + kc_d + 1024) = (u32x2){k_c[1], k_c[3]};
      else { const unsigned d = kc_d - ((kc_d >> 1) & 4u); *(unsigned*)(lds + d) = k_c[1]; *(unsigned*)(lds + d + 8) = k_c[3]; }
    }
  };
  const int wq = w ^ rotr, rt1 = wq & 3, kh1 = w >> 2;
  unsigned bo0 = (unsigned)(kh1 * 71680) + l16, bo1 = bo0 + 35840u;
  asm volatile("" : "+v"(bo0), "+v"(bo1));
  unsigned pso[3], pqo[3], oto = (unsigned)(((w ^ 4) * 7 * 64 + l) * 8), rdo = (unsigned)((w * 7 * 64 + l) * 8), cso = (unsigned)(L_CONST + (128 + n16) * 8);
#pragma unroll
  for (int e = 0; e < 3; ++e) {
    const int row = rt1 * 16 + (e < 2 ? 4 * h4 + 2 * kh1 + e : n16);
    pso[e] = (unsigned)(L_PST + row * 4); pqo[e] = (unsigned)(L_PST + 512 + row * 16 + (row & 3) * 4);
  }
  unsigned spo = (unsigned)(L_Z + tid * 16), p6o = (unsigned)(L_PST + (wq * 16 + 4 * h4) * 4), pgo = (unsigned)(L_PST + 512 + (wq * 16 + n16) * 16);
  asm volatile("" : "+v"(spo), "+v"(p6o), "+v"(pgo));
  p6o = n16 == 12 ? p6o : spo;
  pgo = h4 == (n16 >> 2) ? pgo : spo;
  unsigned bsrc = (unsigned)(((l >> 1) & 3) * 64 + (l & 1) * 4), ppo = l < 8 ? l16 : 0x80000000u;
  asm volatile("" : "+v"(pso[0]), "+v"(pso[2]), "+v"(pqo[0]), "+v"(pqo[2]), "+v"(oto), "+v"(rdo), "+v"(cso), "+v"(p6o), "+v"(pgo), "+v"(bsrc), "+v"(ppo), "+v"(kc_o), "+v"(kc_d));
  pso[1] = pso[0] + 4u; pqo[1] = pqo[0] + 20u;
#define BFRAG(q, j) (*(const u32x4*)(lds + ((q) < 5 ? bo0 : bo1) + ((q) % 5) * 7168 + (j) * 1024))
  u32x4 h2[10];
  unsigned xv = (unsigned)((4 * ((l >> 3) & 1) + h4) * 128 + (l & 7) * 16), xs0 = WS_X1B + (unsigned)(R * 4 + rt1) * 20480u + (unsigned)(10 * kh1) * 1024u;
  unsigned wvo = wsp_vo, bso = (unsigned)tau(rt1, n16) * 2u, fgo = l < 32 ? (unsigned)(l >> 3) * F4S + (unsigned)(l & 7) * 16u : 0x80000000u;
  asm volatile("" : "+v"(xv), "+v"(wvo), "+v"(bso), "+v"(fgo));
#define WO3 (-92)
#define PINS(x) do { x = (unsigned)__builtin_amdgcn_readfirstlane((int)x); asm volatile("" : "+s"(x)); } while (0)
  PINS(xs0);
  unsigned zs[8], wx8, wx4, wgb, fzs, uo, ui, xs, wl2, os, zos, pos, s1, s3;
#define LDX(q) h2[q] = __builtin_amdgcn_raw_buffer_load_b128(rs, xv + (unsigned)((q) & 3) * 1024u, xs0 + (unsigned)((q) >> 2) * 4096u, BULK_AUX)
  auto flag4 = [&](unsigned off) { ST16S(((u32x4){tag, tag, tag, tag}), rs, fgo, WS_F4 - WS_FWI + off, SC1); };
  unsigned bs_raw, fwo[1];
  f32x4 acc[7];
  float mu_n, rs_n;
  {
    unsigned f1o[1] = {w == 1 && l < 20 ? fwi + (unsigned)(c * 20 + l) * 128u : ~0u}, f1v;
    fwo[0] = w == 3 && l < 28 ? fwi + (WS_FWO - WS_FWI) + (unsigned)(c * 32 + l) * 128u : ~0u;
    asm volatile("" : "+v"(f1o[0]), "+v"(fwo[0]));
    if (w == 7) {
      asm volatile("" : "+v"(xf3[0]), "+v"(xf3[1]), "+v"(xf3[2]));
      if (__any(ne2(xf3[0], xf3[1]) != 0u) || __builtin_amdgcn_ballot_w64(xf3[2] == tag) != 0xffffffffull) {
        const unsigned fo[3] = {xf3[0] != tag ? xfb : ~0u, xf3[1] != tag ? xfb + 8192u : ~0u, l < 32 && xf3[2] != tag ? xfb + 16384u : ~0u};
        wait_lines<3, YSL>(rs, fo, tag);
      }
      if (l == 0) lf[33] = tag;
    }
    lf_wait(33, 1);
    STAMP(7);
    SCHED();
#pragma unroll
    for (int q = 0; q < 3; ++q) LDX(q);
    SCHED();
    __builtin_amdgcn_s_waitcnt(0x0F73);
    BAR();
    STAMP(8);
#pragma unroll
    for (int j = 0; j < 7; ++j) acc[j] = (f32x4){0.f, 0.f, 0.f, 0.f};
    f32x4 accg = {0.f, 0.f, 0.f, 0.f};
    u32x4 bf[7];
#pragma unroll
    for (int j = 0; j < 7; ++j) bf[j] = BFRAG(0, j);
    SCHED();
#pragma unroll
    for (int q = 3; q < 6; ++q) LDX(q);
    if (w == 3) flag4(WS_FWO + (unsigned)(c * 32 + R) * 128u);
    SCHED();
#pragma unroll
    for (int t = 0; t < 8; ++t) { zs[t] = WS_Z2F + (unsigned)(c * 64 + 8 * w + 4 * ((t >> 2) ^ rotg) + ((t + rotb) & 3)) * 7168u; PINS(zs[t]); }
    wx8 = (unsigned)((w + (idx >> 2)) & 7) * 1024u; wx4 = wx8 & 3072u; wgb = WS_WOUTF + (unsigned)c * WBLK; fzs = fz + (unsigned)(c * 32 + 4 * w) * 128u;
    uo = WS_UF + (unsigned)((R * 4 + rt1) * 8 + c) * 4096u + (unsigned)kh1 * 2048u; ui = WS_UF + (unsigned)((R * 4 + rt1) * 8 + 4 * kh1) * 4096u;
    xs = WS_X1B + (unsigned)(R * 4 + rt1) * 20480u + (unsigned)(5 * c + 2 * kh1) * 512u; wl2 = (unsigned)(120 + WO3 + (w < 4 ? w + 16 : w)) * 1024u;
    os = (unsigned)(((R * 64 + tau(rt1, 0)) * D_ + 80 * c + 32 * kh1) * 2);
    zos = WS_Z2F + (unsigned)((c * 64 + 4 * (R >> 1)) * 7 + (w < 6 ? w + 1 : 0)) * 1024u + (unsigned)(R & 1) * 512u; pos = WS_PART + (unsigned)(R * 64 + wq) * 128u;
    s1 = (unsigned)((w ^ 4) * 3072 + 1024); s3 = (unsigned)(w * 3072 + 1024);
    PINS(wx8); PINS(wx4); PINS(wgb); PINS(fzs); PINS(uo); PINS(ui); PINS(xs); PINS(wl2); PINS(os); PINS(zos); PINS(pos); PINS(s1); PINS(s3);
    SCHED();
    auto xswap = [&](int q, int e) {
#pragma unroll
      for (int d = 0; d < 2; ++d) {
        if (e == 1) h2[q][2 + d] ^= (unsigned)__builtin_amdgcn_update_dpp(0, (int)h2[q][d], 0x128, 0xf, 0x3, false);
        else h2[q][d] ^= (unsigned)__builtin_amdgcn_update_dpp(0, (int)h2[q][2 + d], 0x128, 0xf, 0xC, false);
      }
      asm volatile("" : "+v"(h2[q]));
    };
#define XSW 1
#pragma unroll
    for (int q = 0; q < 10; ++q) {
      if (q < XSW) { xswap(q, 0); xswap(q, 1); xswap(q, 2); }
      if (q == 5 && w == 1 && __builtin_amdgcn_ballot_w64(f1v == tag) != 0xfffffull) wait_lines<1, 4>(rs, f1o, tag);
      if (q == 5) { __builtin_amdgcn_s_barrier(); asm volatile("" ::: "memory"); }
      SCHED();
      accg = MFMA16(h2[q], h2[q], accg);
      SCHED();
#pragma unroll
      for (int j = 0; j < 7; ++j) {
        acc[j] = MFMA16(h2[q], bf[j], acc[j]);
        if (q == 9 && j == 1) *(f32x4*)(lds + pgo) = accg;
        if (q < 9) bf[j] = BFRAG(q + 1, j);
        if (q < 9 && q + 1 >= XSW && j >= 3 && j < 6) xswap(q + 1, j - 3);
        if (q == 1 && j == 6) f1v = ld4(rs, f1o[0]);
        if (q == 2 && j == 0) bs_raw = __builtin_amdgcn_raw_buffer_load_b16(__builtin_amdgcn_make_buffer_rsrc((void*)p.b_sp, 0, S_ * 2, 0x00020000), bso, (unsigned)R * 128u, 0);
#define KCQ 4
#define WA 8
#define WB 12
#define WQ0 2
        if (WQ0 == 2 && q >= 2 && j == 2) LDW(q - 2);
        if (WQ0 == 6 && q >= 6 && (j == 2 || j == 4)) LDW(2 * (q - 6) + (j >> 2));
        if (WQ0 == 3 && q >= 2 && q < 5 && (j & 1) && 3 * (q - 2) + (j >> 1) < WA) LDW(3 * (q - 2) + (j >> 1));
        if (WQ0 == 4 && q >= 2 && !(q & 1) && j == 2) LDW((q - 2) >> 1);
        if ((j == 0 || j == 4) && 6 + 2 * q + (j >> 2) < 10) LDX(6 + 2 * q + (j >> 2));
        if (q == KCQ && j == 0) kc_ld(1);
        if (q == 2 && j == 1 && cs1) cs_ld(csv, c * 7 + R - 25);
        if (q >= 5 && j == 5) {
          const int i = (5 + q) * 7 + (w < 7 ? w : 0);
          dma1k(rs, lds + i * 1024, l16, WS_WINF + (unsigned)c * WBLK + (unsigned)i * 1024u);
          if (q == 9) { const int i2 = 28 + (w < 7 ? w : 0); dma1k(rs, lds + i2 * 1024, l16, WS_WINF + (unsigned)c * WBLK + (unsigned)i2 * 1024u); }
        }
        SCHED();
      }
    }
    STAMP(31);
  }
  unsigned sro, swo, zo, zro, po;
  auto gelu_n = [&](f32x2* x, auto N_) {
    constexpr int N = decltype(N_)::value;
    const float c0 = -2.0f * 0.7978845608028654f * 1.4426950408889634f, c1 = c0 * 0.044715f;
    f32x2 k0 = {c0, c0}, k1 = {c1, c1}, k2 = {1.0f, 1.0f};
    asm volatile("" : "+v"(k0), "+v"(k1), "+v"(k2));
    f32x2 t[N];
#define GST(e) do { _Pragma("unroll") for (int i = 0; i < N; ++i) { t[i] = (e); asm volatile("" : "+v"(t[i])); } } while (0)
    GST(x[i] * x[i]);
    GST(__builtin_elementwise_fma(t[i], k1, k0));
    GST(t[i] * x[i]);
    GST(((f32x2){__builtin_amdgcn_exp2f(t[i][0]), __builtin_amdgcn_exp2f(t[i][1])}));
    GST(t[i] + k2);
    GST(((f32x2){__builtin_amdgcn_rcpf(t[i][0]), __builtin_amdgcn_rcpf(t[i][1])}));
#pragma unroll
    for (int i = 0; i < N; ++i) { x[i] *= t[i]; asm volatile("" : "+v"(x[i])); }
  };
  {
    f32x2 mine[7];
    f32x2 csb[7];
    if (kh1) {
#pragma unroll
      for (int j = 0; j < 7; ++j) { *(f32x2*)(lds + rdo + j * 512) = (f32x2){acc[j][0], acc[j][1]}; mine[j] = (f32x2){acc[j][2], acc[j][3]}; }
      *(f32x4*)(lds + p6o) = acc[6];
    } else {
#pragma unroll
      for (int j = 0; j < 7; ++j) { *(f32x2*)(lds + rdo + j * 512) = (f32x2){acc[j][2], acc[j][3]}; mine[j] = (f32x2){acc[j][0], acc[j][1]}; }
      *(f32x4*)(lds + p6o) = acc[6];
      consts_put(1);
      if (cs1) cs_fin(csv, c * 7 + R - 25);
#pragma unroll
      for (int k = WA; k < WB; ++k) LDW(k);
    }
    BAR();
#define BY 0
    auto b1p = [&](int k) {
      const int i = 4 * k + (w & 3) + (k < 8 ? 35 : k > 8 ? 70 : (w & 3) < 3 ? 35 : 70) - (k == 17 ? (w & 2) : 0);
      dma1k(rs, lds + i * 1024, l16, WS_WINF + (unsigned)c * WBLK + (unsigned)i * 1024u);
    };
    if (kh1) {
#pragma unroll
      for (int k = 0; k < BY; ++k) b1p(k);
#pragma unroll
      for (int k = WA; k < WB; ++k) LDW(k);
    }
    __builtin_amdgcn_s_setprio(3);
    STAMPT(9);
    f32x2 oth[7];
    f32x2 pa[3], pb[3];
#pragma unroll
    for (int e = 0; e < 3; ++e) {
      pa[e] = (f32x2){*(const float*)(lds + pso[e]), *(const float*)(lds + pqo[e])};
      pb[e] = (f32x2){*(const float*)(lds + pso[e] + 256), *(const float*)(lds + pqo[e] + 1024)};
    }
#pragma unroll
    for (int j = 0; j < 7; ++j) { oth[j] = *(const f32x2*)(lds + oto + j * 512); csb[j] = *(const f32x2*)(lds + cso + 128 * j); }
    SCHED();
    float mu2[2], rs2[2];
#pragma unroll
    for (int e = 0; e < 3; ++e) {
      const float mean = (pa[e][0] + pb[e][0]) * (1.0f / D_);
      const float var = __builtin_fmaxf((pa[e][1] + pb[e][1]) * (1.0f / D_) - mean * mean, 0.f);
      const float rstd = __builtin_amdgcn_rsqf(var + 1e-5f);
      if (e < 2) { mu2[e] = mean; rs2[e] = rstd; } else { mu_n = mean; rs_n = rstd; }
    }
    f32x2 zz[7];
    const f32x2 nmu = {-mu2[0], -mu2[1]}, rsv = {rs2[0], rs2[1]};
#pragma unroll
    for (int j = 0; j < 7; ++j)
      zz[j] = __builtin_elementwise_fma(__builtin_elementwise_fma(nmu, (f32x2){csb[j][0], csb[j][0]}, mine[j] + oth[j]), rsv, (f32x2){csb[j][1], csb[j][1]});
    gelu_n(zz, IC<7>());
    if (n16 >= 12) zz[6] = (f32x2){0.f, 0.f};
    f32x2 s1 = ((zz[0] + zz[1]) + (zz[2] + zz[3])) + ((zz[4] + zz[5]) + zz[6]);
    f32x2 s2 = zz[0] * zz[0], s3 = zz[1] * zz[1];
#pragma unroll
    for (int j = 2; j < 7; ++j) { if (j & 1) s3 = __builtin_elementwise_fma(zz[j], zz[j], s3); else s2 = __builtin_elementwise_fma(zz[j], zz[j], s2); }
    s2 += s3;
    asm("" : "+v"(s1), "+v"(s2));
    float a0 = s1[0] + dpp_f<0xB1>(s1[0]), a1 = s1[1] + dpp_f<0xB1>(s1[1]), b0 = s2[0] + dpp_f<0xB1>(s2[0]);
    a0 = (l & 1) ? a1 : a0;
    const float b1 = s2[1] + dpp_f<0xB1>(s2[1]);
    b0 = (l & 1) ? b1 : b0;
    a0 += dpp_f<0x4E>(a0); b0 += dpp_f<0x4E>(b0);
    a0 += dpp_f<0x124>(a0); b0 += dpp_f<0x124>(b0);
    a0 += dpp_f<0x128>(a0); b0 += dpp_f<0x128>(b0);
    {
      const unsigned pa0 = __builtin_amdgcn_ds_bpermute(bsrc, __float_as_uint(a0)), pb0 = __builtin_amdgcn_ds_bpermute(bsrc, __float_as_uint(b0));
      ST16S(((u32x4){tag, pa0, tag, pb0}), rs, ppo, pos + (unsigned)c * 1024u, SC1);
    }
    SCHED();
    __builtin_amdgcn_s_setprio(0);
    STAMP(10);
#define BO 14
    if (kh1) {
#pragma unroll
      for (int k = BY + BO; k < 18; ++k) b1p(k);
    } else {
#pragma unroll
      for (int k = BY; k < BY + BO; ++k) b1p(k);
    }
#pragma unroll
    for (int k = WB; k < 16; ++k) LDW(k);
#pragma unroll
    for (int j = 0; j < 7; ++j)
      *(unsigned*)(lds + L_Z + (((rt1 >> 1) * 7 + j) * 64 + l) * 16 + (4 * (rt1 & 1) + 2 * kh1) * 2) = pack2(zz[j][0], zz[j][1]);
    SCHED();
#define TR0 2
#define TR1 TR0
#define TR2 TR1
#define TR3 5
#pragma unroll
    for (int g = 0; g < TR0; ++g) WSP_TR(g);
    sro = (unsigned)(L_ST2 + h4 * 16);
    swo = (unsigned)(L_ST2 + (16 * rt1 + 4 * ((l >> 1) & 3) + 2 * kh1 + (l & 1)) * 4);
    zo = w < 7 ? (unsigned)(h4 * 7168 + n16 * 16) : 0x80000000u;
    zro = (unsigned)(L_Z + (w < 7 ? w : 0) * 1024) + l16;
    po = (unsigned)((l >> 3) * 1024 + (l & 7) * 16);
    asm volatile("" : "+v"(sro), "+v"(swo), "+v"(zo), "+v"(zro), "+v"(po));
    asm volatile("s_waitcnt vmcnt(%0)" :: "n"(16 - WB) : "memory");
    BAR();
    STAMPT(11);
  }
  unsigned z1d[7];
  constexpr int L_RED = 28672;
#define WOA(i) (((i) < 28 ? (i) : (i) < 108 ? (i) + 32 : (i) < 120 ? (i) + 37 : (i) - 92) * 1024)
#define RBAR() do { __builtin_amdgcn_s_barrier(); asm volatile("" ::: "memory"); } while (0)
#define ZAUX 16
#define NW 14
#define WOD(i0, x) dma1k(rs, lds + WOA(i0) + (x), l16, wgb + (unsigned)(i0) * 1024u + (x))
  auto wo_dma = [&](int k) {};
  u32x4 zw[NW];
#define LDZ(f) do { if (((f) >> 3) == 4) { zs[(f) & 7] += 4096u; asm volatile("" : "+s"(zs[(f) & 7])); } \
    zw[(f) % NW] = __builtin_amdgcn_raw_buffer_load_b128(rs, l16 + (unsigned)(((f) >> 3) & 3) * 1024u, zs[(f) & 7], ZAUX); } while (0)
#define WD 24
#define WE 32
#define QS 5
#define PQ 3
#define NW0 NW
#define VMW(n) asm volatile("s_waitcnt vmcnt(%0)" ::"n"(n) : "memory")
#define FQ 10
#define PP 1
#define WZ 2
#define WY 8
  static_assert(QS >= 4 && QS <= 6 && PQ >= 2 && PQ < QS && (WZ < 4 || QS >= 5), "");
  {
    f32x4 acc[7];
#pragma unroll
    for (int j = 0; j < 7; ++j) acc[j] = (f32x4){0.f, 0.f, 0.f, 0.f};
    u32x4 bf[7];
#pragma unroll
    for (int j = 0; j < 7; ++j) bf[j] = BFRAG(4, j);
    u32x4 pv, zf[2];
    unsigned fwv;
    auto ld_pv = [&]() { pv = __builtin_amdgcn_raw_buffer_load_b128(rs, po, pos, SC1); };
    auto z1 = [&](auto Q0_, auto Q1_) __attribute__((always_inline)) {
#pragma unroll
      for (int qi = decltype(Q0_)::value; qi < decltype(Q1_)::value; ++qi) {
        const int q = (qi + 4) % 10, qn = (qi + 5) % 10;
        if (qi == FQ) {
          if (kh1) VMW(WE - WD + WZ); else VMW(0);
          RBAR();
          if (w == 3) flag4(WS_FZ + (unsigned)(c * 32 + R) * 128u);
        }
        SCHED();
#pragma unroll
        for (int j = 0; j < 7; ++j) {
          acc[j] = MFMA16(bf[j], h2[q], acc[j]);
          if (qi < 9 && qi != QS - 1) bf[j] = BFRAG(qn, j);
          if (qi < 2 && (j == 1 || j == 4)) { const int i = (2 * qi + (j >> 2)) * 7 + (w < 7 ? w : 0); dma1k(rs, lds + i * 1024, l16, WS_WINF + (unsigned)c * WBLK + (unsigned)i * 1024u); }
          if (qi == 2 && j == 0) kc_ld(0);
          if (qi == 2 && j == 3) fwv = ld4(rs, fwo[0]);
          if (qi == PQ && j == 1) ld_pv();
          if (qi == QS - 1 && j < 2) zf[j] = *(const u32x4*)(lds + zro + j * 7168);
          if (!PP && qi >= QS && (j & 1) && WD + 3 * (qi - QS) + (j >> 1) < WE) LDW((WD + 3 * (qi - QS) + (j >> 1)) & 31);
          SCHED();
        }
      }
    };
    auto req1 = [&]() {
#pragma unroll
      for (int k = 16; k < WD; ++k) { LDW(k); SCHED(); }
    };
    auto req2 = [&]() {
#pragma unroll
      for (int k = WD; k < (PP ? WE : WD); ++k) { LDW(k); SCHED(); }
      sfor<0, WZ>([&](auto G_) __attribute__((always_inline)) { WOD(68 + 8 * decltype(G_)::value, wx8); SCHED(); });
    };
    if (kh1) req1();
    z1(IC<0>(), IC<QS>());
    STAMPT(29);
    f32x2 zv[2][4];
#pragma unroll
    for (int i = 0; i < 8; ++i) { zv[i >> 2][i & 3] = (f32x2){bflo(zf[i >> 2][i & 3]), bfhi(zf[i >> 2][i & 3])}; asm volatile("" : "+v"(zv[i >> 2][i & 3])); }
#pragma unroll
    for (int g = TR0; g < TR1; ++g) WSP_TR(g);
    __builtin_amdgcn_s_setprio(3);
    if (__any(ne2(pv[0], pv[2]) != 0u)) {
      _Pragma("unroll 1") for (unsigned spins = 0; spins < SPIN_CAP; ++spins) {
        ld_pv();
        if (!__any(ne2(pv[0], pv[2]) != 0u)) break;
        __builtin_amdgcn_s_sleep(1);
      }
    }
    {
      float t1 = __uint_as_float(pv[1]), t2 = __uint_as_float(pv[3]);
      t1 += dpp_f<0x128>(t1); t2 += dpp_f<0x128>(t2);
      auto x = __builtin_amdgcn_permlane16_swap(__float_as_uint(t1), __float_as_uint(t2), false, false);
      const unsigned y = __float_as_uint(__uint_as_float(x[0]) + __uint_as_float(x[1]));
      auto z = __builtin_amdgcn_permlane32_swap(y, y, false, false);
      const unsigned u = __float_as_uint(__uint_as_float(z[0]) + __uint_as_float(z[1]));
      auto v = __builtin_amdgcn_permlane16_swap(u, u, false, false);
      const float mean = __uint_as_float(v[0]) * (1.0f / FH);
      const float var = __builtin_fmaxf(__uint_as_float(v[1]) * (1.0f / FH) - mean * mean, 0.f);
      const float rstd = __builtin_amdgcn_rsqf(var + 1e-5f);
      *(float*)(lds + swo) = rstd;
      *(float*)(lds + swo + 256) = -mean * rstd;
    }
    SCHED();
    consts_put(0);
    if (w == 3 && __builtin_amdgcn_ballot_w64(fwv == tag) != 0xfffffffull) wait_lines<1, 1>(rs, fwo, tag);
    if (!kh1) req1();
    BAR();
    STAMP(12);
    {
      f32x4 sa[2][2], sb[2][2];
#pragma unroll
      for (int i = 0; i < 4; ++i) { sa[i >> 1][i & 1] = *(const f32x4*)(lds + sro + i * 64); sb[i >> 1][i & 1] = *(const f32x4*)(lds + sro + i * 64 + 256); }
      SCHED();
#pragma unroll
      for (int m = 0; m < 2; ++m) {
        u32x4 o;
#pragma unroll
        for (int e = 0; e < 4; ++e) {
          const f32x4 a = sa[m][e >> 1], b = sb[m][e >> 1];
          const f32x2 x = __builtin_elementwise_fma(zv[m][e], (f32x2){a[2 * (e & 1)], a[2 * (e & 1) + 1]}, (f32x2){b[2 * (e & 1)], b[2 * (e & 1) + 1]});
          o[e] = pack2(x[0], x[1]);
        }
        ST16S(o, rs, zo + (unsigned)m * 256u, zos, SC1);
      }
    }
    SCHED();
    __builtin_amdgcn_s_setprio(0);
    STAMPT(13);
#pragma unroll
    for (int j = 0; j < 7; ++j) bf[j] = BFRAG((QS + 4) % 10, j);
    if (kh1) req2();
    z1(IC<QS>(), IC<10>());
    f32x2 mine[7];
    if (kh1) {
#pragma unroll
      for (int j = 0; j < 7; ++j) { *(f32x2*)(lds + rdo + L_RED + j * 512) = (f32x2){acc[j][0], acc[j][1]}; mine[j] = (f32x2){acc[j][2], acc[j][3]}; }
    } else {
#pragma unroll
      for (int j = 0; j < 7; ++j) { *(f32x2*)(lds + rdo + L_RED + j * 512) = (f32x2){acc[j][2], acc[j][3]}; mine[j] = (f32x2){acc[j][0], acc[j][1]}; }
    }
    if (!kh1) req2();
#pragma unroll
    for (int g = TR1; g < TR2; ++g) WSP_TR(g);
    if (FQ > 9) VMW(WE - WD + WZ);
    BAR();
    if (FQ > 9 && w == 3) flag4(WS_FZ + (unsigned)(c * 32 + R) * 128u);
    STAMP(6);
    auto req = [&](auto G0_, auto G1_) __attribute__((always_inline)) {
#pragma unroll
      for (int k = WE; k < 32; ++k) LDW(k);
      SCHED();
      sfor<decltype(G0_)::value, decltype(G1_)::value>([&](auto G_) __attribute__((always_inline)) {
        constexpr int i0 = 4 * decltype(G_)::value;
        WOD(i0 < 68 ? i0 : i0 + 8 * WZ, wx4); SCHED();
      });
    };
    if (kh1) req(IC<0>(), IC<WY>());
    u32x4 gv[8];
    auto gb_ld = [&]() {
#pragma unroll
      for (int k = 0; k < 8; ++k) gv[k] = __builtin_amdgcn_raw_buffer_load_b128(rs, l < 40 ? l16 : 0u, WS_MODQ + (unsigned)(k * 2560 + 1920 + 80 * c) * 8u, SC1);
    };
    if (w == 2) gb_ld();
    f32x2 z[7];
    {
      f32x2 oth[7];
      f32x4 cc[7];
#pragma unroll
      for (int j = 0; j < 7; ++j) {
        oth[j] = *(const f32x2*)(lds + oto + L_RED + j * 512);
        cc[j] = *(const f32x4*)(lds + L_CONST + (16 * j + 4 * h4 + 2 * kh1) * 8);
      }
      SCHED();
#pragma unroll
      for (int j = 0; j < 7; ++j)
        z[j] = __builtin_elementwise_fma(__builtin_elementwise_fma((f32x2){-mu_n, -mu_n}, (f32x2){cc[j][0], cc[j][1]}, oth[j] + mine[j]),
                                         (f32x2){rs_n, rs_n}, (f32x2){cc[j][2], cc[j][3]});
    }
    gelu_n(z, IC<7>());
#pragma unroll
    for (int j = 0; j < 7; ++j) z1d[j] = pack2(z[j][0], z[j][1]);
    asm volatile("" : "+v"(z1d[0]), "+v"(z1d[1]), "+v"(z1d[2]), "+v"(z1d[3]), "+v"(z1d[4]), "+v"(z1d[5]), "+v"(z1d[6]));
    SCHED();
    STAMPT(30);
    if (w == 2) {
      auto bad = [&]() {
        unsigned d = 0u;
#pragma unroll
        for (int k = 0; k < 8; ++k) d |= ne2(gv[k][0], gv[k][2]);
        return __any(d != 0u);
      };
      if (bad()) {
        _Pragma("unroll 1") for (unsigned spins = 0; spins < SPIN_CAP; ++spins) {
          __builtin_amdgcn_s_sleep(8);
          gb_ld();
          if (!bad()) break;
        }
      }
      f32x2 t[4];
#pragma unroll
      for (int k = 0; k < 4; ++k) t[k] = (f32x2){__uint_as_float(gv[2 * k][1]) + __uint_as_float(gv[2 * k + 1][1]), __uint_as_float(gv[2 * k][3]) + __uint_as_float(gv[2 * k + 1][3])};
      *(f32x2*)(lds + L_MISC + l * 8) = rbf2((t[0] + t[1]) + (t[2] + t[3]));
    }
    if (!kh1) req(IC<WY>(), IC<30 - 2 * WZ>());
    const unsigned fo[1] = {l < 4 ? fzs + (unsigned)l * 128u : ~0u};
#define FPY 1
#define FPO FPY
#define FSL 1
    unsigned fv;
#pragma unroll
    for (int g = TR2; g <= TR3; ++g) {
      if (FPY == FPO) { if (g == TR2 + FPY) fv = ld4(rs, fo[0]); }
      else if (g == TR2 + FPY) { if (kh1) fv = ld4(rs, fo[0]); }
      else if (g == TR2 + FPO) { if (!kh1) fv = ld4(rs, fo[0]); }
      SCHED();
      if (g < TR3) WSP_TR(g);
      SCHED();
    }
    STAMP(14);
    if (__builtin_amdgcn_ballot_w64(fv == tag) != 15ull) wait_lines<1, FSL>(rs, fo, tag);
    STAMPT(23);
#pragma unroll
    for (int f = 0; f < NW; ++f) { LDZ(f); SCHED(); }
  }

#define WO2 15
#define TRC TR3
  {
    SCHED();
#pragma unroll
    for (int g = TRC; g < 8; ++g) WSP_TR(g);
    SCHED();
    STAMP(20);
    const int rt = rt1, kh = kh1;
    const unsigned rw = (unsigned)(L_RED + w * 512);
    unsigned rr = (unsigned)(L_RED + w * 4096 + l * 4);
    asm volatile("; m0 is set here, not in front of the first store (one wait state)" : "+v"(rr) : "{m0}"(rw));
    const float bs = bflo(bs_raw);
    unsigned long long hm = 0xffff000000000000ull;
    asm volatile("" : "+s"(hm));
    unsigned l8;
    f32x4 acc2[2][4];
    float rv[8][2];
    u32x4 ud;
    u32x4 P[2][8];
    u32x2 xp[3];
    auto ld_p = [&](int pt, int i) { P[pt][i] = __builtin_amdgcn_raw_buffer_load_b128(rs, l16 + (unsigned)pt * 1024u, ui + (unsigned)i * 2048u, SC1); };
#define RED_PUT1(j, i) asm volatile("ds_write_addtid_b32 %0 offset:%2" :: "v"(acc2[(j) & 1][((i) >> 2) & 3][(i) & 3]), "{m0}"(rw), "n"((((i) >> 2) & 3) * 4096 + (((i) >> 1) & 1) * 16384 + ((i) & 1) * 256) : "memory")
#define RED_PUT(j, r) do { RED_PUT1(j, 4 * (r)); RED_PUT1(j, 4 * (r) + 1); RED_PUT1(j, 4 * (r) + 2); RED_PUT1(j, 4 * (r) + 3); } while (0)
#define RED_GET(i) do { f32x2 x_; asm volatile("ds_read2st64_b32 %0, %1 offset0:%2 offset1:%3" : "=v"(x_) : "v"(rr), "n"(2 * (i)), "n"(2 * (i) + 1) : "memory"); rv[i][0] = x_[0]; rv[i][1] = x_[1]; } while (0)
    float zz[2], gg[2];
    unsigned cs;
    auto sum_rows_u = [&](unsigned v) {
      auto a = __builtin_amdgcn_permlane16_swap(v, v, false, false);
      v = a[0] + a[1];
      auto b = __builtin_amdgcn_permlane32_swap(v, v, false, false);
      return b[0] + b[1];
    };
    auto red_fin = [&](int j, int k, bool fuse = false) {
      const int e = k & 1, i = k >> 1;
#define VADD(d, x, y) asm volatile("v_add_f32 %0, %1, %2" : "=v"(d) : "v"(x), "v"(y))
      if (i < 4) VADD(rv[2 * i][e], rv[2 * i][e], rv[2 * i + 1][e]);
      if (i == 4 || i == 5) VADD(rv[4 * (i & 1)][e], rv[4 * (i & 1)][e], rv[4 * (i & 1) + 2][e]);
      if (i == 6) VADD(rv[0][e], rv[0][e], bs);
      if (i == 7) VADD(gg[e], rv[0][e], rv[4][e]);
      if (i == (fuse ? 9 : 8)) zz[e] = e ? bfhi(z1d[(j + 6) % 7]) : bflo(z1d[(j + 6) % 7]);
      if (i == 9) zz[e] *= gg[e];
      if (i == 9 || (i == 8 && !fuse)) asm volatile("" : "+v"(zz[e]));
      if (k == 20) { ud[j & 3] = pack2(zz[0], zz[1]); asm volatile("" : "+v"(ud[j & 3])); }
      if (k == 21) {
        if (j == 0) asm volatile("v_cndmask_b32 %0, %0, 0, %1" : "+v"(ud[0]) : "s"(hm));
        if (j == 2) asm volatile("v_add3_u32 %0, %1, %2, %3" : "=v"(cs) : "v"(ud[0]), "v"(ud[1]), "v"(ud[2]));
        if (j == 3) {
          cs = tag - sum_rows_u(cs + ud[3]);
          asm volatile("v_cndmask_b32 %0, %0, %1, %2" : "+v"(ud[0]) : "v"(cs), "s"(hm));
        }
        if (j >= 4) { ud[3] = (j == 4 ? tag : ud[3]) - ud[j & 3]; asm volatile("" : "+v"(ud[3])); }
        if (j % 3 == 0 && j) ST16S(ud, rs, l16 + (unsigned)(j / 6) * 1024u, uo, SC1);
      }
    };
    struct Sch { int b1, b2, wp[16], rp[8], vp[22], lp[8], pr[32]; };
    constexpr Sch S = {18, 28, {0, 1, 2, 3, 4, 5, 6, 7, 8, 9, 10, 11, 12, 12, 15, 15}, {19, 19, 20, 22, 23, 23, 24, 24}, {29, 29, 30, 30, 31, 31, 32, 33, 35, 36, 37, 38, 39, 40, 41, 42, 43, 46, 43, 46, 48, 48},
                       {1, 1, 1, 1, 1, 1, 1, 1}, {0, 0, 2, 1, 0, 0, 0, 0, 0, 0, 2, 2, 0, 0, 0, 0, 0, 0, 0, 1, 0, 0, 0, 1, 0, 0, 2, 1, 0, 0, 0, 0}};
    constexpr Sch F = {15, 23, {0, 1, 1, 2, 3, 4, 4, 5, 6, 7, 8, 9, 10, 11, 12, 12}, {15, 16, 16, 17, 17, 18, 18, 19}, {23, 23, 24, 24, 25, 26, 26, 27, 27, 28, 28, 29, 30, 30, 31, 31, 22, 22, 32, 33, 34, 35},
                       {1, 2, 1, 1, 1, 1, 1, 1}, {0, 2, 1, 0, 0, 1, 1, 0, 0, 2, 1, 0, 0, 2, 1, 0, 0, 2, 1, 0, 0, 2, 1, 0, 0, 2, 1, 0, 0, 2, 1, 0}};
#define VM 3
#define JF 4
#define RO 3
#define XE 1
#define CE 0
#define AP0 2
#define P1B 2
#define P1S 3
    unsigned wb[3], xo;
    const unsigned wl[3] = {(unsigned)(120 + WO3 + w) * 1024u, (unsigned)(128 + WO3 + w) * 1024u, wl2}, wg = wgb - (unsigned)(WO3 * 1024);
#define NAF 32
#define NA0 16
#define NA1 NA0
    u32x4 af[NAF];
#define LDA(m) af[(m) % NAF] = *(const u32x4*)(lds + wb[WOA(2 * (m)) >> 16] + (WOA(2 * (m)) & 65535))
    unsigned ck[2];
    const unsigned tag4 = tag * 4u;
    auto p_chk = [&](int pt, int pr, int k) {
      u32x4* q = P[pt] + 4 * pr;
      if (k < 2) ck[k & 1] = q[k & 1][0] + q[k & 1][1];
      else if (k < 8) ck[k & 1] = ck[k & 1] + q[(k & 1) + 2 * ((k >> 2) & 1)][k & 2] + q[(k & 1) + 2 * ((k >> 2) & 1)][(k & 2) + 1];
      else if (k == 8) ck[0] = pt ? ck[0] + ck[1] : sum_rows_u(ck[0] + ck[1]);
      if (k < 9) asm volatile("" : "+v"(ck[k & 1]));
      if (k == 9) {
        if (__builtin_expect(__any(ck[0] != tag4), 0)) {
          _Pragma("unroll 1") for (unsigned spins = 0; spins < SPIN_CAP; ++spins) {
            unsigned d = 0u;
#pragma unroll
            for (int i = 0; i < 4; ++i) ld_p(pt, 4 * pr + i);
#pragma unroll
            for (int i = 0; i < 4; ++i) d += q[i][0] + q[i][1] + q[i][2] + q[i][3];
            if (!pt) d = sum_rows_u(d);
            if (!__any(d != tag4)) break;
            __builtin_amdgcn_s_sleep(1);
          }
        }
      }
      if (k >= 10 && !pt) { unsigned x = q[(k - 10) & 3][0]; asm volatile("v_cndmask_b32 %0, %0, 0, %1" : "+v"(x) : "s"(hm)); q[(k - 10) & 3][0] = x; }
      if (k >= 10 && k < 13 && pt) { q[(k - 10) % 3][3] = q[3][(k - 10) % 3]; asm volatile("" : "+v"(q[(k - 10) % 3])); }
    };
    sfor<0, 224>([&](auto M_) __attribute__((always_inline)) {
      constexpr int mm = decltype(M_)::value, j = mm >> 5, m = mm & 31, y = m - (32 - 4 * RO);
      constexpr int t = j == 6 && y >= 0 ? 8 - RO + (y % (2 * RO + !RO)) / 2 : m >> 2, r = j == 6 && y >= 0 ? (y & 1) + 2 * (y / (2 * RO + !RO)) : m & 3, f = 8 * j + t;
      constexpr Sch C = j < JF ? S : F, B = j <= JF ? S : F;
      if (t == 0) asm volatile("v_mfma_f32_16x16x32_bf16 %0, %1, %2, 0" : "=&v"(acc2[j & 1][r]) : "v"(zw[f % NW]), "v"(wsp[r][t]), "v"(zw[(f | (f < 8 ? 1 : VM)) % NW]));
      else asm volatile("v_mfma_f32_16x16x32_bf16 %0, %1, %2, %0" : "+v"(acc2[j & 1][r]) : "v"(zw[f % NW]), "v"(wsp[r][t]), "v"(zw[(f | (f < 8 ? 1 : VM)) % NW]));
      SCHED();
      if (C.pr[m] == 1) __builtin_amdgcn_s_setprio(1);
      if ((m == C.b1 && j > 0) || (m == C.b2 && j > 0) || mm == 30) { asm volatile("" ::: "memory"); __builtin_amdgcn_s_waitcnt(0xC07F); RBAR(); SCHED(); }
      sfor<0, 16>([&](auto I_) __attribute__((always_inline)) {
        constexpr int i = decltype(I_)::value;
        if (j > 0 && C.wp[i] == m) { RED_PUT1(j + 1, i); SCHED(); }
        if (j > 0 && i < 8 && C.rp[i & 7] == m) { RED_GET(i & 7); SCHED(); }
      });
      if (j == 5 && m == 20) {
#pragma unroll
        for (int i = 0; i < 3; ++i) { wb[i] = (unsigned)(kh * 1024 + i * 65536) + l16; asm volatile("" : "+v"(wb[i])); }
      }
      if (XE && j == 5 && m == 13) {
        asm volatile("v_lshrrev_b32 %0, 1, %1" : "=v"(l8) : "v"(l16));
        xo = (l8 & 0x180u) + (unsigned)psi((l8 >> 3) & 15) * 8u;
        asm volatile("" : "+v"(xo));
      }
      if (RO && j == 6 && m >= 28) { RED_PUT1(6, 2 * (m - 28)); RED_PUT1(6, 2 * (m - 28) + 1); SCHED(); }
      if (CE && j == 6 && m >= 19 && m < 22) sfor<0, 3>([&](auto K_) __attribute__((always_inline)) { p_chk(0, 0, 3 * (m - 19) + decltype(K_)::value); SCHED(); });
      constexpr int ap[20] = {7, 8, 9, 10, 11, 13, 13, 14, 14, 19, 20, 21, 22, 23, 24, 25, 26, 27, 28, 29};
      sfor<0, NA0>([&](auto N_) __attribute__((always_inline)) { if (j == 6 && ap[AP0 + decltype(N_)::value] == m) { LDA(decltype(N_)::value); SCHED(); } });
      sfor<0, 22>([&](auto K_) __attribute__((always_inline)) {
        constexpr int k = decltype(K_)::value;
        if (j > 1 && B.vp[k] == m + 32) { red_fin(j > 1 ? j - 2 : 0, k, B.vp[16] == B.vp[18]); SCHED(); }
        if (j > 0 && C.vp[k] == m) { red_fin(j > 0 ? j - 1 : 0, k); SCHED(); }
      });
      if (C.pr[m] == 2) __builtin_amdgcn_s_setprio(0);
      if ((m & 3) == C.lp[m >> 2]) {
        if (f > 0 && f - 1 + NW < 56) LDZ(f - 1 + NW);
        if (j == 6 && (m >> 2) >= P1S) ld_p(0, ((m >> 2) - P1S) & 7);
        if (XE && j == 5 && m >= 20) xp[((m >> 2) - 5) % 3] = __builtin_amdgcn_raw_buffer_load_b64(rs, xo + (unsigned)((m >> 2) - 5) * 512u, xs, BULK_AUX);
        SCHED();
      }
    });
    STAMP(15);
#define NM0 0
#define GM 19
#define MB 24
    f32x4 acc[5];
    f32x4 xf[3], gf[3];
    unsigned go, oo, eo, ho, tw, tr, o1, ta, tb;
    const unsigned s2 = (unsigned)(L_MISC + 128 * kh), s4 = s1 - (unsigned)kh * 1024u;
    unsigned long long om = kh ? ~0ull : 0xccccccccccccccccull;
#define PIN2(a, b) asm volatile("" : "+v"(a), "+v"(b))
    auto glue = [&](int k) {
      if (k == 0) { ta = l8 >> 5; tb = l16 & 48u; PIN2(ta, tb); }
      if (k == 1) { tr = __umul24(ta, 112u) + tb; oo = ta & 12u; PIN2(tr, oo); }
      if (k == 2) { tr += s1; oo += ta; PIN2(tr, oo); }
      if (k == 3) { oo = __umul24(oo, 1280u) + tb; ta = l8 >> 4; PIN2(oo, ta); }
      if (k == 4) { ta &= 24u; tb = l16 & 0xf0u; PIN2(ta, tb); }
      if (k == 5) { tw = __umul24(tb, 7u) + ta; go = (ta << 1) + s2; PIN2(tw, go); }
      if (k == 6) { tw += s1; asm volatile("v_cndmask_b32 %0, %1, -2.0, %2" : "=v"(o1) : "v"(oo), "s"(om)); asm volatile("" : "+v"(tw)); }
      if (k == 7) { eo = l16 + s3; ho = l16 + s4; PIN2(eo, ho); }
      if (k >= 8 && k < 14) {
        const unsigned x = xp[((k - 8) >> 1) % 3][k & 1];
        float lo = bflo(x), hi = bfhi(x);
        PIN2(lo, hi);
        xf[((k - 8) >> 1) % 3][2 * (k & 1)] = lo; xf[((k - 8) >> 1) % 3][2 * (k & 1) + 1] = hi;
      }
    };
    auto g3 = [&](auto M0_, auto M1_) __attribute__((always_inline)) {
      sfor<decltype(M0_)::value, decltype(M1_)::value>([&](auto M_) __attribute__((always_inline)) {
        constexpr int mi = decltype(M_)::value;
        constexpr int m = mi < 55 ? mi : mi < 64 ? 55 + 5 * ((mi - 55) / 3) + (mi - 55) % 3 : 58 + 5 * ((mi - 64) / 2) + (mi & 1), u2 = m / 5;
        const u32x4 fb = u2 < 8 ? P[0][u2 & 7] : P[1][u2 < 11 ? (u2 - 8) & 3 : (u2 - 7) & 7];
        if (m < 5) asm volatile("v_mfma_f32_16x16x32_bf16 %0, %1, %2, 0" : "=&v"(acc[m % 5]) : "v"(af[m % NAF]), "v"(fb));
        else asm volatile("v_mfma_f32_16x16x32_bf16 %0, %1, %2, %0" : "+v"(acc[m % 5]) : "v"(af[m % NAF]), "v"(fb));
        SCHED();
        if (mi == NM0) RBAR();
        if (mi >= NM0 && mi < NM0 + 3) dma1k(rs, lds + wl[(mi - NM0) % 3], l16, wg + wl[(mi - NM0) % 3]);
        if (mi >= 5 && mi < 19) p_chk(0, 1, mi - 5);
        if (mi >= GM && mi < GM + 14) glue(mi - GM);
        if (mi >= 47 && mi < 54) { p_chk(1, 1, 2 * (mi - 47)); SCHED(); p_chk(1, 1, 2 * (mi - 47) + 1); }
        if (!XE && mi == 3) { xo = (l8 & 0x180u) + (unsigned)psi((l8 >> 3) & 15) * 8u; asm volatile("" : "+v"(xo)); }
        if (!XE && mi >= 4 && mi < 7) xp[(mi - 4) % 3] = __builtin_amdgcn_raw_buffer_load_b64(rs, xo + (unsigned)(mi - 4) * 512u, xs, BULK_AUX);
        if (mi >= NM0 && mi < 40 && NA1 + mi - NM0 < 60) LDA(NA1 + mi - NM0);
        if (mi >= 66 && mi < 69) *(f32x4*)(lds + eo + (mi - 66) * 1024) = acc[(mi - 66) % 3];
        SCHED();
      });
    };
    __builtin_amdgcn_s_setprio(3);
#pragma unroll
    for (int k = 18; k < 22; ++k) red_fin(5, k);
    if (!XE) asm volatile("v_lshrrev_b32 %0, 1, %1" : "=v"(l8) : "v"(l16));
    if (RO) asm volatile("s_nop 2"); else { RED_PUT(6, 0); RED_PUT(6, 1); }
    RED_PUT(6, 2); RED_PUT(6, 3);
    red_fin(6, 16); red_fin(6, 17);
    SCHED();
#pragma unroll
    for (int i = 8 - P1S; i < 8 - P1S + P1B; ++i) ld_p(0, i);
    SCHED();
    BAR();
    sfor<0, 8>([&](auto I_) __attribute__((always_inline)) { RED_GET(decltype(I_)::value); });
    SCHED();
#pragma unroll
    for (int n = NA0; n < NA1; ++n) LDA(n);
#pragma unroll
    for (int i = 8 - P1S + P1B; i < 8; ++i) ld_p(0, i);
    SCHED();
    sfor<0, 20>([&](auto K_) __attribute__((always_inline)) {
      constexpr int k = decltype(K_)::value;
      if (k < 8 && !(k & 1)) asm volatile("s_waitcnt lgkmcnt(%0)" :: "n"(6 - k + NA1 - NA0) : "memory");
      red_fin(6, k < 16 ? k : k + 2);
    });
    SCHED();
    __builtin_amdgcn_s_setprio(0);
    STAMPT(16);
#pragma unroll
    for (int k = CE ? 9 : 0; k < 14; ++k) p_chk(0, 0, k);
    SCHED();
    g3(IC<0>(), IC<MB>());
    STAMPT(22);
    DRAIN();
    RBAR();
    STAMPT(27);
#define MY 12
    if (MY == 0 || !kh) {
#pragma unroll
      for (int i = 0; i < 8; ++i) { ld_p(1, i); SCHED(); }
    }
    g3(IC<MB>(), IC<MB + MY>());
    if (MY > 0 && kh) {
#pragma unroll
      for (int i = 0; i < 8; ++i) { ld_p(1, i); SCHED(); }
    }
    STAMPT(26);
    g3(IC<MB + MY>(), IC<40>());
    STAMPT(24);
#pragma unroll
    for (int m = NA1 + 40 - NM0; m < 70; ++m) LDA(m);
#pragma unroll
    for (int x = 0; x < 3; ++x) gf[x] = *(const f32x4*)(lds + go + x * 64);
    SCHED();
    const rsrc_t ro = __builtin_amdgcn_make_buffer_rsrc((void*)p.out, 0, S_ * D_ * 2, 0x00020000);
    STAMPT(17);
#pragma unroll
    for (int k = 0; k < 14; ++k) p_chk(1, 0, k);
    asm volatile("s_nop 1");
    STAMPT(25);
    g3(IC<40>(), IC<70>());
    STAMPT(18);
    BAR();
    STAMPT(28);
    f32x4 o4[3];
    u32x4 ov[2];
#pragma unroll
    for (int j = 2; j >= 0; --j) o4[j] = *(const volatile LAS f32x4*)(lds + ho + j * 1024);
    asm volatile("" : "+v"(acc[2]), "+v"(acc[3]), "+v"(acc[4]));
    SCHED();
#pragma unroll
    for (int j = 2; j >= 0; --j) {
      const f32x4 v = __builtin_elementwise_fma(gf[j], o4[j] + acc[2 + j], xf[j]);
      *(volatile LAS u32x2*)(lds + tw + j * 32) = (u32x2){pack2(v[0], v[1]), pack2(v[2], v[3])};
      if (j < 2) ov[j] = *(const volatile LAS u32x4*)(lds + tr + j * 32);
    }
    ST16S(ov[1], ro, oo + 32u, os, 0);
    ST16S(ov[0], ro, o1, os, 0);
    if (bid == 0 && w == 5 && l8 == 0u) st4(rs, WS_EPOCH, tag);
  }
  STAMP(19);
}

extern "C" __device__ __attribute__((used, noinline, visibility("hidden"))) void mega_tail() {}

extern "C" void kernel_launch(void* const* d_in, const int* in_sizes, int n_in, void* d_out, int out_size,
                              void* d_ws, size_t ws_size, hipStream_t stream) {
  Params p;
  p.q = (const u16*)d_in[0];
  p.k = (const u16*)d_in[1];
  p.v = (const u16*)d_in[2];
  p.shift_k = (const u16*)d_in[3];
  p.ssm_k = (const u16*)d_in[4];
  p.ssm_D = (const u16*)d_in[5];
  p.w_in = (const u16*)d_in[7];
  p.w_sp = (const u16*)d_in[8];
  p.b_sp = (const u16*)d_in[9];
  p.w_out = (const u16*)d_in[10];
  p.cvec = (const u16*)d_in[11];
  p.w_mod = (const u16*)d_in[12];
  p.out = (u16*)d_out;
  p.ws = (char*)d_ws;
  mega<<<256, 512, 0, stream>>>(p);
}
```

```cpp
#include <hip/hip_runtime.h>
#include <stdint.h>

typedef unsigned short u16;
typedef __attribute__((ext_vector_type(8))) short bf16x8;
typedef __attribute__((ext_vector_type(4))) short bf16x4;
typedef __attribute__((ext_vector_type(4))) float f32x4;
typedef __attribute__((ext_vector_type(2))) float f32x2;
typedef __attribute__((ext_vector_type(4))) unsigned int u32x4;
typedef __attribute__((ext_vector_type(2))) unsigned int u32x2;
#define LAS __attribute__((address_space(3)))
typedef __amdgpu_buffer_rsrc_t rsrc_t;
typedef __attribute__((ext_vector_type(4))) short s16x4;

constexpr int S_ = 2048, D_ = 640, F_ = 1728, FH = 864, CB = 108;

constexpr unsigned WS_EPOCH = 0;
constexpr unsigned WS_FWI = 4096;
constexpr unsigned WS_FZ = WS_FWI + 384 * 128;
constexpr unsigned WS_FWO = WS_FZ + 256 * 128;
constexpr unsigned F4S = 896 * 128;
constexpr unsigned WS_CS = WS_FZ + 256 * 128;
constexpr unsigned WS_MOD = 131072;
constexpr unsigned WS_MODP = WS_MOD + 81920;
constexpr unsigned WS_ST1 = 393216;
constexpr unsigned WS_PART = 524288;
constexpr unsigned WS_CSP = WS_PART + 262144;
constexpr unsigned WS_FX = WS_CSP + 655360;
constexpr unsigned WS_X1B = WS_FX + 16 * 4 * 160 * 128;
constexpr unsigned WS_QB = WS_X1B + 2621440;
constexpr unsigned WS_KV3 = WS_QB + 2621440;
constexpr unsigned WS_MODQ = WS_KV3 + 1310720;
constexpr unsigned WBLK = 143360;
constexpr unsigned WS_WINF = WS_MODQ + 245760;
constexpr unsigned WS_WOUTF = WS_WINF + 16 * WBLK;
constexpr unsigned WS_Z2F = WS_WOUTF + 8 * WBLK;
constexpr unsigned WS_UF = WS_Z2F + 8 * 64 * 7168;
constexpr unsigned WS_F4 = WS_UF + 32 * 4 * 8 * 4096;
constexpr unsigned WS_END = WS_F4 + 4 * F4S;
constexpr unsigned WS_STAMP = 200u << 20;

constexpr int G_COPY = 4160;
constexpr int L_U = WBLK;
constexpr int U_CH = (256 + S_) * 2 + 32;
constexpr int L_FLG = L_U + 4 * U_CH;
constexpr int L_RT = 16384;
constexpr int L_CONST = WBLK;
constexpr int L_PST = L_CONST + 2048;
constexpr int L_Z = L_CONST + 5120;
constexpr int L_ST2 = L_Z + 14336;
constexpr int L_MISC = L_ST2 + 512;
constexpr int LDS_TOTAL = 163840;

#define AUX_T 610
#define RT2_T 360
#define CB_T 380
#define W_T 700
#define YG_T 920
#define BG 2
#define BK 12
#define YW 4
#define QG 0
#define QA 4
#define QM 6
#define YPR 0
#define YSL 2
#define K_T 460
#ifndef M_T
#define M_T 0
#endif
#define WA_T 580
constexpr int SC1 = 16, NT = 2;
constexpr unsigned SPIN_CAP = 1u << 16;

__device__ __forceinline__ float bflo(unsigned x) { return __uint_as_float(x << 16); }
__device__ __forceinline__ float bfhi(unsigned x) { return __uint_as_float(x & 0xffff0000u); }
__device__ __forceinline__ unsigned f2bf(float x) {
  __bf16 b = (__bf16)x;
  return (unsigned)__builtin_bit_cast(unsigned short, b);
}
__device__ __forceinline__ float rbf(float x) { return bflo(f2bf(x)); }
typedef __attribute__((ext_vector_type(2))) __bf16 bf2_t;
__device__ __forceinline__ unsigned pack2(float lo, float hi) {
  f32x2 v = {lo, hi};
  return __builtin_bit_cast(unsigned, __builtin_convertvector(v, bf2_t));
}
__device__ __forceinline__ f32x2 rbf2(f32x2 v) {
  const unsigned p = __builtin_bit_cast(unsigned, __builtin_convertvector(v, bf2_t));
  return (f32x2){__uint_as_float(p << 16), __uint_as_float(p & 0xffff0000u)};
}
__device__ __forceinline__ f32x4 rbf4(f32x4 v) {
  const f32x2 a = rbf2((f32x2){v[0], v[1]}), b = rbf2((f32x2){v[2], v[3]});
  return (f32x4){a[0], a[1], b[0], b[1]};
}
template <int CTRL>
__device__ __forceinline__ float dpp_f(float v) {
  return __int_as_float(__builtin_amdgcn_update_dpp(0, __float_as_int(v), CTRL, 0xf, 0xf, true));
}
__device__ __forceinline__ float sum8(float v) {
  v += dpp_f<0xB1>(v);
  v += dpp_f<0x4E>(v);
  v += dpp_f<0x141>(v);
  return v;
}
__device__ __forceinline__ float sum16(float v) {
  v = sum8(v);
  v += dpp_f<0x140>(v);
  return v;
}
__device__ __forceinline__ float sum_rows(float v) {
  auto a = __builtin_amdgcn_permlane16_swap(__float_as_uint(v), __float_as_uint(v), false, false);
  v = __uint_as_float(a[0]) + __uint_as_float(a[1]);
  auto b = __builtin_amdgcn_permlane32_swap(__float_as_uint(v), __float_as_uint(v), false, false);
  return __uint_as_float(b[0]) + __uint_as_float(b[1]);
}
#define MFMA16(a, b, c) __builtin_amdgcn_mfma_f32_16x16x32_bf16(__builtin_bit_cast(bf16x8, a), __builtin_bit_cast(bf16x8, b), c, 0, 0, 0)
#define MFMA_IP(c, a, b) asm volatile("v_mfma_f32_16x16x32_bf16 %0, %1, %2, %0" : "+v"(c) : "v"(a), "v"(b))
#define MFMA_0(c, a, b) asm volatile("v_mfma_f32_16x16x32_bf16 %0, %1, %2, 0" : "=&v"(c) : "v"(a), "v"(b))
#define MFMA16K16(a, b, c) __builtin_amdgcn_mfma_f32_16x16x16bf16_1k(__builtin_bit_cast(bf16x4, a), __builtin_bit_cast(bf16x4, b), c, 0, 0, 0)
__device__ __forceinline__ u32x2 trd(const char* p) {
  return __builtin_bit_cast(u32x2, __builtin_amdgcn_ds_read_tr16_b64_v4i16((LAS s16x4*)p));
}
__device__ __forceinline__ u32x4 trfrag(const char* p, int pitch) {
  u32x2 a = trd(p), b = trd(p + 16 * pitch);
  return (u32x4){a[0], a[1], b[0], b[1]};
}
#define BAR() asm volatile("s_waitcnt lgkmcnt(0)\n\ts_barrier" ::: "memory")
#define DRAIN() do { __builtin_amdgcn_s_waitcnt(0x0F70); asm volatile("" ::: "memory"); } while (0)
#define SCHED() __builtin_amdgcn_sched_barrier(0)
template <int N> struct IC { static constexpr int value = N; };
template <int I, int N, class F>
__device__ __forceinline__ void sfor(F&& f) {
  if constexpr (I < N) { f(IC<I>()); sfor<I + 1, N>(f); }
}

__device__ __forceinline__ u32x4 ld16(rsrc_t r, unsigned off) { return __builtin_amdgcn_raw_buffer_load_b128(r, off, 0, SC1); }
__device__ __forceinline__ u32x2 ld8(rsrc_t r, unsigned off) { return __builtin_amdgcn_raw_buffer_load_b64(r, off, 0, SC1); }
__device__ __forceinline__ unsigned ld4(rsrc_t r, unsigned off) { return __builtin_amdgcn_raw_buffer_load_b32(r, off, 0, SC1); }
#define ST_AUX 16
__device__ __forceinline__ void st16(rsrc_t r, unsigned off, u32x4 v) { __builtin_amdgcn_raw_buffer_store_b128(v, r, off, 0, ST_AUX); }
#define ST16S(v, r, voff, soff, aux) do { const u32x4 v_ = (v); __builtin_amdgcn_raw_buffer_store_b128(v_, r, voff, soff, aux); asm volatile("s_nop 1" :: "v"(v_)); } while (0)
__device__ __forceinline__ void st8(rsrc_t r, unsigned off, u32x2 v) { __builtin_amdgcn_raw_buffer_store_b64(v, r, off, 0, SC1); }
__device__ __forceinline__ void st_flag4(rsrc_t r, unsigned off, unsigned tag, int l) {
  if (l < 32) st16(r, WS_F4 - WS_FWI + off + (unsigned)(l >> 3) * F4S + (unsigned)(l & 7) * 16u, (u32x4){tag, tag, tag, tag});
}
__device__ __forceinline__ void st4(rsrc_t r, unsigned off, unsigned v) { __builtin_amdgcn_raw_buffer_store_b32(v, r, off, 0, SC1); }
#define BULK_AUX 16
__device__ __forceinline__ u32x4 ld16p(rsrc_t r, unsigned off) { return __builtin_amdgcn_raw_buffer_load_b128(r, off, 0, BULK_AUX); }
__device__ __forceinline__ u32x2 ld8p(rsrc_t r, unsigned off) { return __builtin_amdgcn_raw_buffer_load_b64(r, off, 0, BULK_AUX); }
__device__ __forceinline__ void dma1k(rsrc_t r, char* lds_dst, unsigned l16, unsigned soff) {
  __builtin_amdgcn_raw_ptr_buffer_load_lds(r, (LAS void*)lds_dst, 16, l16, soff, 0, BULK_AUX);
}

#define STAMP(i)
#define STAMPF(i)
#define STAMPT(i) STAMP(i)

template <int N, int SLP>
__device__ __forceinline__ void wait_lines(rsrc_t r, const unsigned (&off)[N], unsigned tag) {
  bool need[N];
#pragma unroll
  for (int i = 0; i < N; ++i) need[i] = off[i] != ~0u;
  _Pragma("unroll 1") for (unsigned spins = 0; spins < SPIN_CAP; ++spins) {
    unsigned v[N];
#pragma unroll
    for (int i = 0; i < N; ++i) {
      v[i] = tag;
      if (need[i]) v[i] = ld4(r, off[i]);
    }
    bool any = false;
#pragma unroll
    for (int i = 0; i < N; ++i) { need[i] = v[i] != tag; any = any || need[i]; }
    if (!__any(any)) break;
    __builtin_amdgcn_s_sleep(SLP);
  }
  asm volatile("" ::: "memory");
}

__device__ __forceinline__ int tau(int rt, int n) { return 32 * (rt >> 1) + 8 * (n >> 2) + 4 * (rt & 1) + (n & 3); }
__device__ __forceinline__ int psi(int n) { return 2 * (n & 7) + (n >> 3); }
#define QX_CM(d, s0, s1, P) "v_cndmask_b32_dpp " d ", " s0 ", " s1 ", vcc quad_perm:" P " row_mask:0xf bank_mask:0xf\n\t"
#define QX_BODY(M, P) \
  asm("s_mov_b32 vcc_lo, " M "\n\ts_mov_b32 vcc_hi, " M "\n\t" \
      QX_CM("%0", "%12", "%8", P) "v_xor_b32 %4, %8, %0\n\t" QX_CM("%1", "%13", "%9", P) "v_xor_b32 %5, %9, %1\n\t" \
      QX_CM("%2", "%14", "%10", P) "v_xor_b32 %6, %10, %2\n\t" QX_CM("%3", "%15", "%11", P) "v_xor_b32 %7, %11, %3" \
      : "=&v"(t[0]), "=&v"(t[1]), "=&v"(t[2]), "=&v"(t[3]), "=&v"(u[0]), "=&v"(u[1]), "=&v"(u[2]), "=&v"(u[3]) \
      : "v"(a[0]), "v"(a[1]), "v"(a[2]), "v"(a[3]), "v"(b[0]), "v"(b[1]), "v"(b[2]), "v"(b[3]) : "vcc")
template <int BIT>
__device__ __forceinline__ void quad_xchg3(u32x4& a, u32x4& b) {
  unsigned t[4], u[4];
  if (BIT == 0) QX_BODY("0x55555555", "[1,0,3,2]");
  else QX_BODY("0x33333333", "[2,3,0,1]");
  a = (u32x4){t[0], t[1], t[2], t[3]};
#pragma unroll
  for (int d = 0; d < 4; ++d) b[d] ^= (unsigned)__builtin_amdgcn_update_dpp(0, (int)u[d], BIT == 0 ? 0xB1 : 0x4E, 0xf, 0xf, true);
}
__device__ __forceinline__ void quad_transpose(u32x4& x0, u32x4& x1, u32x4& x2, u32x4& x3) {
  quad_xchg3<0>(x0, x1);
  quad_xchg3<0>(x2, x3);
  quad_xchg3<1>(x0, x2);
  quad_xchg3<1>(x1, x3);
  asm volatile("" : "+v"(x0), "+v"(x1), "+v"(x2), "+v"(x3));
}

struct Params {
  const u16 *q, *k, *v, *shift_k, *ssm_k, *ssm_D, *w_in, *w_sp, *b_sp, *w_out, *cvec, *w_mod;
  u16* out;
  char* ws;
};

__global__ __launch_bounds__(512, 2) void mega(Params p) {
  __shared__ __attribute__((aligned(16))) char lds[LDS_TOTAL];
  {
    unsigned long long pc0;
    unsigned vt;
    asm volatile("v_readfirstlane_b32 vcc_lo, %3\n\ts_cmpk_lt_u32 %2, 192\n\ts_cbranch_scc1 4f\n\ts_cmpk_lt_u32 vcc_lo, 64\n\ts_cbranch_scc0 4f\n\t"
                 "s_lshr_b32 vcc_hi, %2, 3\n\ts_sub_u32 vcc_hi, vcc_hi, 24\n\ts_lshl_b32 vcc_hi, vcc_hi, 13\n\t"
                 "v_lshlrev_b32 %1, 7, %3\n\tv_add_u32 %1, vcc_hi, %1\n\ts_getpc_b64 %0\n.Lmega_here:\n\t"
                 "v_min_u32 %1, mega_tail-.Lmega_here-4, %1\n\ts_movk_i32 m0, 0x2000\n\ts_nop 1\n\tglobal_load_lds_dword %1, %0\n4:\n\t"
                 "s_cmpk_lt_u32 %2, 160\n\ts_cbranch_scc0 1f\n\ts_cmpk_lt_u32 vcc_lo, 256\n\ts_cbranch_scc1 3f\n\t"
                 "s_call_b64 vcc, .Ltouch1\n\ts_branch 3f\n1:\n\t"
                 "s_cmpk_lt_u32 vcc_lo, 256\n\ts_cbranch_scc1 2f\n\ts_cmpk_lt_u32 %2, 224\n\ts_cbranch_scc0 2f\n\ts_call_b64 vcc, .Ltouch3\n\ts_branch 3f\n"
                 "2:\n\ts_call_b64 vcc, .Ltouch2\n3:"
                 : "=&s"(pc0), "=&v"(vt) : "s"(blockIdx.x), "v"(threadIdx.x) : "vcc", "scc", "memory");
  }
  SCHED();
  const int tid = threadIdx.x, l = tid & 63;
  const int w = __builtin_amdgcn_readfirstlane(tid >> 6);
  const int bid = blockIdx.x, xcd = bid & 7, idx = bid >> 3;
#define TOUCH_AT(n) asm volatile("s_branch 1f\n.Ltouch" #n ":\n\ts_setpc_b64 vcc\n1:")
  char* const ws = p.ws;
  asm volatile("" ::"s"(p.q), "s"(p.k), "s"(p.v), "s"(p.shift_k), "s"(p.ssm_k), "s"(p.ssm_D), "s"(p.w_in), "s"(p.w_sp), "s"(p.b_sp),
               "s"(p.w_out), "s"(p.cvec), "s"(p.w_mod), "s"(p.out), "s"(p.ws));
  unsigned epoch, tag, t_start;
  unsigned long long t64;
  asm volatile("s_load_dword %0, %2, 0x0\n\ts_memrealtime %1" : "=&s"(epoch), "=&s"(t64) : "s"(ws));
#define TAG_READY() do { SCHED(); \
    asm volatile("s_waitcnt lgkmcnt(0)\n\ts_mul_i32 %0, %2, 0x9e3779b1\n\ts_add_i32 %0, %0, 0x9e3779b9\n\ts_mov_b64 %1, %3" \
                 : "=&s"(tag), "=&s"(t64r) : "s"(epoch), "s"(t64) : "scc"); t_start = (unsigned)t64r; SCHED(); } while (0)
  unsigned long long t64r;
  const rsrc_t rs = __builtin_amdgcn_make_buffer_rsrc(ws, 0, WS_END, 0x00020000);
  const unsigned l16 = (unsigned)l * 16u;
  STAMP(0);
  const bool is_conv = idx < 20;
  const int quad = xcd * 20 + idx;
  const int R = is_conv ? (xcd & 3) * 5 + (idx >> 2) : (xcd & 3) * 3 + 15 + (idx >> 2);
  const int c = (xcd >> 2) * 4 + (idx & 3);
  const unsigned fwi = WS_F4 + (unsigned)(xcd & 3) * F4S, fz = fwi + (WS_FZ - WS_FWI);
  const int n16 = l & 15, h4 = l >> 4;
  u32x4 wsp[4][8];
#define ROTC 2
#define WAUX 0
  const int rotg = ((idx >> 4) ^ ((ROTC & 2) ? idx >> 1 : 0)) & 1, rotb = (idx >> 2) & 3, rotr = (ROTC & 1) ? (idx & 1) * 2 : 0;
  const rsrc_t rsp = __builtin_amdgcn_make_buffer_rsrc((void*)p.w_sp, 0, S_ * S_ * 2, 0x00020000);
  const unsigned wsp_vo = (unsigned)(((l >> 2) & 3) * 8 * 4096 + h4 * 64 + ((l + rotb) & 3) * 16);
#define LDW(k) LDW_(k)
#define WSP_TR(g) WSP_TR_(g)
#define WROW R
#define LDW_(k) wsp[((k) >> 2) & 3][((k) >> 4) * 4 + ((k) & 3)] = __builtin_amdgcn_raw_buffer_load_b128(rsp, wvo, \
    (unsigned)((WROW * 64 + 16 * ((((k) >> 2) & 2) ^ rotr) + 4 * (((k) >> 2) & 1) + ((k) & 3)) * 4096 + w * 512 + ((((k) >> 4) ^ rotg) * 256)), WAUX)
#define WSP_TR_(g) quad_transpose(wsp[(g) & 3][((g) >> 2) * 4], wsp[(g) & 3][((g) >> 2) * 4 + 1], wsp[(g) & 3][((g) >> 2) * 4 + 2], wsp[(g) & 3][((g) >> 2) * 4 + 3])
  const rsrc_t rwi = __builtin_amdgcn_make_buffer_rsrc((void*)p.w_in, 0, D_ * F_ * 2, 0x00020000);
  const rsrc_t rwo = __builtin_amdgcn_make_buffer_rsrc((void*)p.w_out, 0, FH * D_ * 2, 0x00020000);
  const rsrc_t rq = __builtin_amdgcn_make_buffer_rsrc((void*)p.q, 0, S_ * D_ * 2, 0x00020000);
  auto hold = [&](unsigned tt) { while ((unsigned)__builtin_amdgcn_s_memrealtime() - t_start < tt) __builtin_amdgcn_s_sleep(2); };
  auto ne2 = [&](unsigned x, unsigned y) { unsigned d; asm("v_bitop3_b32 %0, %1, %2, %3 bitop3:0x7e" : "=v"(d) : "v"(x), "v"(y), "s"(tag)); return d; };
  volatile LAS unsigned* lf = (volatile LAS unsigned*)(lds + L_FLG);
  auto lf_set = [&](int i) { asm volatile("s_waitcnt lgkmcnt(0)" ::: "memory"); if (l == 0) lf[i] = tag; };
  auto lf_wait = [&](int i0, int n) {
    _Pragma("unroll 1") for (unsigned spins = 0; spins < (SPIN_CAP << 4); ++spins) {
      const unsigned v = lf[i0 + (l & (n - 1))];
      if (__all(v == tag)) break;
      __builtin_amdgcn_s_sleep(1);
    }
    asm volatile("" ::: "memory");
  };
  const unsigned b2o = fwi + (unsigned)((8 + c) * 20 + 5 * (w & 3) + (l < 5 ? l : 0)) * 128u;
  auto b2_wait = [&](u32x2 fv, int slp) {
    if (!__all(fv[0] == tag)) {
      _Pragma("unroll 1") for (unsigned spins = 0; spins < SPIN_CAP; ++spins) {
        if (fv[0] != tag) fv = ld8(rs, b2o);
        if (__all(fv[0] == tag)) break;
        if (slp) __builtin_amdgcn_s_sleep(8); else __builtin_amdgcn_s_sleep(4);
      }
    }
    asm volatile("" ::: "memory");
    if (l < 5) lf[44 + 5 * w + l] = fv[1];
    if (l == 0) lf[36 + w] = tag;
  };
  auto load_b2 = [&](int k0, int k1) {
    const unsigned a0 = (unsigned)(35 * w + (idx >> 2) * 4) * 1024u, gb = WS_WINF + (unsigned)(8 + c) * WBLK;
    unsigned a = a0, g = gb + a0;
#pragma unroll
    for (int k = k0; k < k1; ++k) {
      if (k == k0 || (k & 3) == 3) {
        a = k + (idx >> 2) * 4 >= 35 ? a0 - 35840u : a0;
        g = gb + a;
        asm volatile("" : "+s"(a), "+s"(g));
      }
      dma1k(rs, lds + a + k * 1024, l16, g + (unsigned)k * 1024u);
    }
  };
  char* const wo_st = lds + 122880;
  auto wo_ld = [&]() {
    const int U = R < 28 ? R : 0, u2 = U >> 1, fi = (u2 + 1) % 3;
    const int c0 = 4 * (U & 1) + (u2 < 8 ? u2 >> 1 : u2 < 11 ? 0 : 2);
    const int b0 = u2 < 8 ? CB * c0 + 2 * (u2 & 1) + 96 : CB * (c0 + (fi >> 1)) + 48 + 2 * (fi & 1), d1 = u2 < 8 ? -96 : 16, d2 = d1 + 16;
    const int b3 = u2 < 8 ? b0 - 64 : CB * (c0 + 1) + 50 + 16 * fi;
#pragma unroll
    for (int r = 0; r < 5; ++r) {
      const int g = r * 64 + l, rho = g / 10;
      const int fb = (rho & 16) ? ((rho & 2) ? b3 : b0 + d2) : ((rho & 2) ? b0 + d1 : b0);
      __builtin_amdgcn_raw_ptr_buffer_load_lds(rwo, (LAS void*)(wo_st + r * 1024), 16, (unsigned)((fb + (rho & 13)) * (D_ * 2) + (g % 10) * 16 + c * 160), 0, 0, 0);
    }
  };
  auto rt_ld = [&](int i0, char* st) {
    const int cb = (i0 < 160 ? 8 : 0) + (i0 & 7), Q = (i0 < 160 ? i0 : i0 - 160) >> 3;
#pragma unroll
    for (int r = 0; r < 7; ++r) {
      const int g = r * 64 + l;
      __builtin_amdgcn_raw_ptr_buffer_load_lds(rwi, (LAS void*)(st + r * 1024), 16, (unsigned)((g / 14) * (F_ * 2) + (g % 14) * 16 + Q * 32 * F_ * 2 + cb * CB * 2),
                                               0, 0, NT);
    }
  };
  auto rt_job = [&](int i0, char* st) {
    const int cb = (i0 < 160 ? 8 : 0) + (i0 & 7), Q = (i0 < 160 ? i0 : i0 - 160) >> 3;
    const int col = 640 * (l >> 5) + 32 * Q + (l & 31);
    DRAIN();
    u32x4 x[7], o[7];
    f32x2 xu[7][4];
    {
      const char* bp = st + (4 * h4 + (n16 >> 2)) * 224 + (n16 & 3) * 8;
#pragma unroll
      for (int j = 0; j < 7; ++j) x[j] = trfrag(bp + j * 32, 224);
#pragma unroll
      for (int j = 0; j < 7; ++j)
#pragma unroll
        for (int e = 0; e < 4; ++e) { xu[j][e] = (f32x2){bflo(x[j][e]), bfhi(x[j][e])}; asm volatile("" : "+v"(xu[j][e])); }
    }
    unsigned mk[4][2], pv = (unsigned)col * 8u, cw = l >= 32 ? (unsigned)(7168 + (l - 32) * 4) : (unsigned)(7298 + l * 2 - 4), m12 = n16 == 12 ? ~0u : 0u;
    unsigned fgo = l < 32 ? (unsigned)(l >> 3) * F4S + (unsigned)(l & 7) * 16u : 0x80000000u;
#pragma unroll
    for (int i = 0; i < 4; ++i) {
      mk[i][0] = n16 == 4 * i ? 0x3F803F80u : 0u; mk[i][1] = n16 == 4 * i + 1 ? ~0u : 0u;
      asm volatile("" : "+v"(mk[i][0]), "+v"(mk[i][1]));
    }
    asm volatile("" : "+v"(pv), "+v"(cw), "+v"(m12), "+v"(fgo));
    unsigned ob = WS_WINF + (unsigned)cb * WBLK + (unsigned)(Q * 7) * 1024u;
    asm volatile("" : "+s"(ob));
    u32x2 pp8[8];
    hold(CB_T);
    _Pragma("unroll 1") for (unsigned spins = 0; spins < SPIN_CAP; ++spins) {
#pragma unroll
      for (int i = 0; i < 8; ++i) pp8[i] = __builtin_amdgcn_raw_buffer_load_b64(rs, pv, WS_MODP + (unsigned)i * 10240u, SC1);
      if (!__any(((ne2(pp8[0][0], pp8[1][0]) | ne2(pp8[2][0], pp8[3][0]) | ne2(pp8[4][0], pp8[5][0])) | ne2(pp8[6][0], pp8[7][0])) != 0u)) break;
      __builtin_amdgcn_s_sleep(4);
    }
    STAMPF(9);
    __builtin_amdgcn_s_setprio(3);
    {
      auto pf = [&](int i) { return __uint_as_float(pp8[i][1]); };
      const float t = rbf(((pf(0) + pf(1)) + (pf(2) + pf(3))) + ((pf(4) + pf(5)) + (pf(6) + pf(7))));
      if (l >= 32) *(float*)(st + cw) = rbf(1.0f + t);
      else *(u16*)(st + cw + 2) = (u16)(__float_as_uint(t) >> 16);
    }
    asm volatile("" ::: "memory");
    const f32x4 ca = *(const volatile LAS f32x4*)(st + 7168 + h4 * 16), cc = *(const volatile LAS f32x4*)(st + 7168 + 64 + h4 * 16);
    const u32x2 e0 = *(const volatile LAS u32x2*)(st + 7296 + h4 * 8), e1 = *(const volatile LAS u32x2*)(st + 7328 + h4 * 8);
    SCHED();
#pragma unroll
    for (int j = 0; j < 7; ++j) {
      const f32x2 y0 = xu[j][0] * (f32x2){ca[0], ca[1]}, y1 = xu[j][1] * (f32x2){ca[2], ca[3]}, y2 = xu[j][2] * (f32x2){cc[0], cc[1]}, y3 = xu[j][3] * (f32x2){cc[2], cc[3]};
      o[j] = (u32x4){pack2(y0[0], y0[1]), pack2(y1[0], y1[1]), pack2(y2[0], y2[1]), pack2(y3[0], y3[1])};
      if (j == 6) {
#pragma unroll
        for (int e = 0; e < 4; ++e) o[j][e] = (o[j][e] & ~m12) | mk[3][0];
      }
      ST16S(o[j], rs, l16 + (unsigned)(j & 3) * 1024u, ob + (unsigned)(j >> 2) * 4096u, SC1);
      SCHED();
    }
    __builtin_amdgcn_s_setprio(0);
    STAMPF(11);
    f32x4 d[2] = {{0.f, 0.f, 0.f, 0.f}, {0.f, 0.f, 0.f, 0.f}};
#pragma unroll
    for (int j = 0; j < 7; ++j) {
      const unsigned one = mk[j & 3][0], sr = mk[j & 3][1];
      d[j >> 2] = MFMA16(((u32x4){one, one, one, one}), o[j], d[j >> 2]);
      d[j >> 2] = MFMA16(((u32x4){sr & e0[0], sr & e0[1], sr & e1[0], sr & e1[1]}), x[j], d[j >> 2]);
    }
#pragma unroll
    for (int m = 0; m < 2; ++m)
      st16(rs, WS_CSP + (unsigned)((cb * 20 + Q) * 2 + m) * 1024u + l16, (u32x4){tag, __float_as_uint(d[m][0]), tag, __float_as_uint(d[m][1])});
    STAMPF(13);
    u32x4 tg4 = {tag, t_start, tag, tag};
    asm volatile("s_waitcnt vmcnt(2)" : "+v"(tg4) :: "memory");
    ST16S(tg4, rs, fgo, WS_F4 + (unsigned)(cb * 20 + Q) * 128u, SC1);
  };

  auto red8 = [&](float v) { v += dpp_f<0x128>(v); return sum_rows(v); };
  const rsrc_t rwm = __builtin_amdgcn_make_buffer_rsrc((void*)p.w_mod, 0, D_ * 3840 * 2, 0x00020000);
  auto gemv_load = [&](u32x4* pre, int blk, int sl, int np) {
    pre[10][0] = *(const unsigned*)(p.cvec + 8 * np * sl + 2 * (l < 4 * np ? l : 0));
#pragma unroll
    for (int pp = 0; pp < np; ++pp)
      pre[pp] = __builtin_amdgcn_raw_buffer_load_b128(rwm, (unsigned)((l >> 3) * 7680 + (l & 7) * 16), (unsigned)((8 * np * sl + pp * 8) * 7680 + blk * 128), NT);
  };
  auto gemv_fin = [&](float* acc, const u32x4* pre, int np, char* scr) {
    const unsigned cv2 = pre[10][0];
    unsigned* svt = (unsigned*)scr + w * 96;
#pragma unroll
    for (int e = 0; e < 2; ++e) {
      const int row = 2 * l + e;
      const float cv = e ? bfhi(cv2) : bflo(cv2);
      if (l < 4 * np) svt[(row & 7) * 12 + (row >> 3)] = __float_as_uint(rbf(cv * __builtin_amdgcn_rcpf(1.0f + __expf(-cv))));
    }
    asm volatile("s_waitcnt lgkmcnt(0)" ::: "memory");
    u32x4 sv[3];
#pragma unroll
    for (int i = 0; i < (np + 3) / 4; ++i) sv[i] = *(const u32x4*)(svt + (l >> 3) * 12 + 4 * i);
    SCHED();
#pragma unroll
    for (int e = 0; e < 8; ++e) acc[e] = 0.f;
#pragma unroll
    for (int pp = 0; pp < np; ++pp) {
      const unsigned sh = sv[pp >> 2][pp & 3], sl2 = sh >> 16;
#pragma unroll
      for (int e = 0; e < 4; ++e) {
        const unsigned x = pre[pp][e];
        acc[2 * e] = __builtin_amdgcn_fdot2_f32_bf16(__builtin_bit_cast(bf2_t, x), __builtin_bit_cast(bf2_t, sl2), acc[2 * e], false);
        acc[2 * e + 1] = __builtin_amdgcn_fdot2_f32_bf16(__builtin_bit_cast(bf2_t, x), __builtin_bit_cast(bf2_t, sh), acc[2 * e + 1], false);
      }
    }
#pragma unroll
    for (int e = 0; e < 8; ++e) acc[e] = red8(acc[e]);
  };
  auto gemv_put = [&](const float* acc, unsigned off) {
    float v = acc[0];
#pragma unroll
    for (int e = 1; e < 8; ++e) { v = (l >> 3) == e ? acc[e] : v; asm("" : "+v"(v)); }
    st8(rs, off + (unsigned)l * 8u, (u32x2){tag, (unsigned)__builtin_amdgcn_ds_bpermute(((l & 7) * 8 + (l >> 3)) * 4, (int)__float_as_uint(v))});
  };
  auto cs_ld = [&](u32x4* v, int cj) {
    const int cb = cj / 7, j = cj % 7;
#pragma unroll
    for (int k = 0; k < 5; ++k)
      v[k] = __builtin_amdgcn_raw_buffer_load_b128(rs, (unsigned)(h4 * 10240 + n16 * 16), WS_CSP + (unsigned)((cb * 40 + (j >> 2) + 2 * k) * 1024 + (j & 3) * 256), SC1);
  };
  auto cs_fin = [&](u32x4* v, int cj) {
    auto bad = [&]() { return __any(((ne2(v[0][0], v[0][2]) | ne2(v[1][0], v[1][2]) | ne2(v[2][0], v[2][2])) | ne2(v[3][0], v[3][2]) | ne2(v[4][0], v[4][2])) != 0u); };
    if (bad()) {
      _Pragma("unroll 1") for (unsigned spins = 0; spins < SPIN_CAP; ++spins) {
        __builtin_amdgcn_s_sleep(8);
        cs_ld(v, cj);
        if (!bad()) break;
      }
    }
    float t0 = 0.f, t1 = 0.f;
#pragma unroll
    for (int k = 0; k < 5; ++k) { t0 += __uint_as_float(v[k][1]); t1 += __uint_as_float(v[k][3]); }
    t0 = sum_rows(t0); t1 = sum_rows(t1);
    if (l < 16) st16(rs, WS_CS + (unsigned)((cj / 7) * 2048 + (cj % 7) * 256) + l16, (u32x4){tag, __float_as_uint(t0), tag, __float_as_uint(t1)});
  };
  const bool cs2 = w == 2 && R >= 20 && R < 27, cs1 = w == 2 && R >= 25;
  u32x4 csv[5];
  typedef const __attribute__((address_space(4))) unsigned* cptr_t;

  if (__builtin_expect(is_conv, 1)) {
    if (w >= 4) {
      TOUCH_AT(1);
      const int pw = w - 4;
      const rsrc_t ra = __builtin_amdgcn_make_buffer_rsrc((void*)((idx & 2) ? p.v : p.k), 0, S_ * D_ * 2, 0x00020000);
      const rsrc_t rb = __builtin_amdgcn_make_buffer_rsrc((void*)((idx & 2) ? p.k : p.v), 0, S_ * D_ * 2, 0x00020000);
      const rsrc_t rk = __builtin_amdgcn_make_buffer_rsrc((void*)p.k, 0, S_ * D_ * 2, 0x00020000);
      const rsrc_t rv = __builtin_amdgcn_make_buffer_rsrc((void*)p.v, 0, S_ * D_ * 2, 0x00020000);
#define NROT 2
      const unsigned vo = (unsigned)((128 * pw + 2 * l) * (D_ * 2));
      const unsigned so0 = (unsigned)((idx & 1) * (D_ * 2) + quad * 8), so1 = (unsigned)((1 - (idx & 1)) * (D_ * 2) + quad * 8);
      u32x2 g[3][4], kh;
      auto gl = [&](int s, int j0, int j1) {
        __builtin_amdgcn_s_setprio(0);
#pragma unroll
        for (int j = j0; j < j1; ++j) {
          if (s < NROT) g[s][j] = __builtin_amdgcn_raw_buffer_load_b64(j < 2 ? ra : rb, vo, ((j & 1) ? so1 : so0) + (unsigned)(s * 512 * D_ * 2), 0);
          else g[s][j] = __builtin_amdgcn_raw_buffer_load_b64(j < 2 ? rk : rv, vo + (unsigned)((j & 1) * D_ * 2), (unsigned)(quad * 8 + s * 512 * D_ * 2), 0);
          SCHED();
        }
        __builtin_amdgcn_s_setprio(3);
      };
      const cptr_t sp = (cptr_t)(p.shift_k + quad * 16);
      unsigned spv[8];
#pragma unroll
      for (int i = 0; i < 8; ++i) spv[i] = sp[i];
      __builtin_amdgcn_s_sleep(3);
      gl(0, 0, 4);
      {
        const int row = 512 * (l >> 2) + 128 * pw - 3 + (l & 3);
        kh = __builtin_amdgcn_raw_buffer_load_b64(rk, (l >= 12 || (l & 3) == 3 || row < 0) ? 0x80000000u : (unsigned)(row * D_ * 2 + quad * 8), 0, 0);
      }
      TAG_READY();
      f32x2 wt[2][4];
#pragma unroll
      for (int d = 0; d < 2; ++d) {
        wt[d][0] = (f32x2){bflo(spv[4 * d]), bflo(spv[4 * d + 2])}; wt[d][1] = (f32x2){bfhi(spv[4 * d]), bfhi(spv[4 * d + 2])};
        wt[d][2] = (f32x2){bflo(spv[4 * d + 1]), bflo(spv[4 * d + 3])}; wt[d][3] = (f32x2){bfhi(spv[4 * d + 1]), bfhi(spv[4 * d + 3])};
        asm volatile("" : "+s"(wt[d][0]), "+s"(wt[d][1]), "+s"(wt[d][2]), "+s"(wt[d][3]));
      }
      SCHED();
      {
        const int j = idx * 4 + pw;
        const unsigned o = (unsigned)((512 + 64 * (j >> 2) + l) * (D_ * 2) + (((160 * xcd) >> 7) + ((j >> 1) & 1)) * 128);
        __builtin_amdgcn_raw_ptr_buffer_load_lds((j & 1) ? ra : rb, (LAS void*)(lds + 98304), 4, j < 64 ? o : 0x80000000u, 0, 0, 0);
      }
      SCHED();
      gl(1, 0, 4);
      STAMP(21);
      unsigned up = (unsigned)(L_U + 512 + 256 * pw + (l >> 2) * 16 + (3 - (l & 3)) * 4);
      unsigned lfo = l == 0 ? (unsigned)(L_FLG + 4 * pw) : (unsigned)(100352 + tid * 4), lfr = (unsigned)(L_FLG + (l & 3) * 4);
      unsigned yr = (unsigned)(L_U + (n16 >> 2) * U_CH + (128 * pw + 32 * h4 + 8 * (n16 & 1) + 4 * ((n16 >> 1) & 1)) * 2);
      unsigned yo = (unsigned)(8 * pw + (l >> 3)) * 20480u + (unsigned)quad * 128u + (unsigned)(l & 7) * 16u;
      unsigned mo = (unsigned)((n16 < 12 ? n16 >> 2 : 0) * D_ + quad * 4 + (l & 3)) * 8u + (unsigned)h4 * 40960u;
      unsigned k3o = (unsigned)(16 * pw + (l >> 2)) * 20480u + (unsigned)(((l >> 1) & 1) * 10240 + (2 * quad + (l & 1)) * 16);
      unsigned k3d = (unsigned)(L_U + (2 * (l & 1) + ((l >> 1) & 1)) * U_CH + 512 + (1536 + 128 * pw + 8 * (l >> 2)) * 2);
      unsigned xfo = (unsigned)(((4 * (l >> 5) + pw) * 4 + ((l >> 3) & 3)) * 160 + quad) * 128u + (unsigned)(l & 7) * 16u;
      u32x4 tg4 = {tag, tag, tag, tag};
      asm volatile("" : "+v"(up), "+v"(lfo), "+v"(lfr), "+v"(yr), "+v"(yo), "+v"(mo), "+v"(k3o), "+v"(k3d), "+v"(xfo), "+v"(tg4));
      unsigned so = WS_ST1 + (unsigned)xcd * 16384u + (unsigned)pw * 1024u;
      asm volatile("" : "+s"(so));
      auto pf_set = [&](int i) { asm volatile("" ::: "memory"); *(volatile LAS unsigned*)(lds + lfo + 4 * i) = tg4[0]; };
      auto pf_wait = [&](int i0) {
        _Pragma("unroll 1") for (unsigned spins = 0; spins < (SPIN_CAP << 4); ++spins) {
          if (__all(*(volatile LAS unsigned*)(lds + lfr + 4 * i0) == tag)) break;
          __builtin_amdgcn_s_sleep(1);
        }
        asm volatile("" ::: "memory");
      };
      unsigned hl[2][3];
      auto halo = [&](int s) {
#pragma unroll
        for (int i = 0; i < 6; ++i) { hl[i & 1][i >> 1] = (unsigned)__builtin_amdgcn_readlane(kh[i & 1], 4 * s + (i >> 1)); asm volatile("" : "+v"(hl[i & 1][i >> 1])); }
      };
      auto kv = [&](auto S__, auto N__) {
        constexpr int s = decltype(S__)::value;
        u32x2* x = g[s];
        SCHED();
        asm volatile("s_waitcnt vmcnt(%5)" : "+v"(x[0]), "+v"(x[1]), "+v"(x[2]), "+v"(x[3]), "+v"(kh) : "n"(decltype(N__)::value));
        if (s == 2) STAMP(27);
        if (s == 0) halo(0);
        if (s < NROT) {
          if (idx & 1) { asm volatile(""); const u32x2 t = x[0], u = x[2]; x[0] = x[1]; x[1] = t; x[2] = x[3]; x[3] = u; }
          asm volatile("" : "+v"(x[0]), "+v"(x[1]), "+v"(x[2]), "+v"(x[3]));
          if (idx & 2) { asm volatile(""); const u32x2 t = x[0], u = x[1]; x[0] = x[2]; x[1] = x[3]; x[2] = t; x[3] = u; }
          asm volatile("" : "+v"(x[0]), "+v"(x[1]), "+v"(x[2]), "+v"(x[3]));
        }
        f32x2 kf[2][5], o[2][2];
#pragma unroll
        for (int d = 0; d < 2; ++d) {
          unsigned kk[5];
          kk[3] = x[0][d]; kk[4] = x[1][d];
          kk[2] = __builtin_amdgcn_update_dpp(hl[d][2], kk[4], 0x138, 0xf, 0xf, false);
          kk[1] = __builtin_amdgcn_update_dpp(hl[d][1], kk[3], 0x138, 0xf, 0xf, false);
          kk[0] = __builtin_amdgcn_update_dpp(hl[d][0], kk[2], 0x138, 0xf, 0xf, false);
#pragma unroll
          for (int j = 0; j < 5; ++j) kf[d][j] = (f32x2){bflo(kk[j]), bfhi(kk[j])};
        }
#pragma unroll
        for (int t = 0; t < 5; ++t) {
#pragma unroll
          for (int i = 0; i < 4; ++i) {
            const int d = i >> 1, r = i & 1;
            if (t == 4) o[d][r] *= (f32x2){bflo(x[2 + r][d]), bfhi(x[2 + r][d])};
            else if (t == 0) o[d][r] = kf[d][r + 3] * wt[d][0];
            else o[d][r] = __builtin_elementwise_fma(kf[d][r + 3 - t], wt[d][t], o[d][r]);
          }
          asm volatile("" : "+v"(o[0][0]), "+v"(o[0][1]), "+v"(o[1][0]), "+v"(o[1][1]));
        }
#pragma unroll
        for (int i = 0; i < 4; ++i) *(unsigned*)(lds + up + i * U_CH + s * 1024) = pack2(o[i >> 1][1][i & 1], o[i >> 1][0][i & 1]);
        pf_set(4 * s);
        SCHED();
        if (s < 2) halo(s + 1);
        SCHED();
      };
      u32x4 sv[4], qv[4];
      u32x2 mv[2];
      auto ld_e = [&]() {
#pragma unroll
        for (int s = 0; s < 4; ++s) sv[s] = __builtin_amdgcn_raw_buffer_load_b128(rs, l16, so + (unsigned)s * 4096u, SC1);
        SCHED();
      };
      auto s_bad = [&]() { return __any(((ne2(sv[0][1], sv[0][3]) | ne2(sv[1][1], sv[1][3]) | ne2(sv[2][1], sv[2][3])) | ne2(sv[3][1], sv[3][3])) != 0u); };
      unsigned qo = (unsigned)((128 * pw + 32 * h4 + 4 * ((l >> 3) & 1) + 8 * ((l >> 2) & 1) + (l & 3)) * (D_ * 2));
      asm volatile("" : "+v"(qo));
      auto glq = [&](int j0, int j1) {
        __builtin_amdgcn_s_setprio(0);
#pragma unroll
        for (int j = j0; j < j1; ++j) {
          const u32x2 x = __builtin_amdgcn_raw_buffer_load_b64(rq, qo, (unsigned)(quad * 8 + (512 * (j >> 1) + 16 * (j & 1)) * D_ * 2), 0);
          qv[j >> 1][2 * (j & 1)] = x[0]; qv[j >> 1][2 * (j & 1) + 1] = x[1];
          SCHED();
        }
        __builtin_amdgcn_s_setprio(3);
      };
      kv(IC<0>(), IC<5>());
      STAMP(23);
      if (QG) {
        const int j = idx * 4 + pw;
        const unsigned o = (unsigned)((64 * (j >> 1) + l) * (D_ * 2) + (((160 * xcd) >> 7) + (j & 1)) * 128);
        __builtin_amdgcn_raw_ptr_buffer_load_lds(rq, (LAS void*)(lds + 98304), 4, j < 64 ? o : 0x80000000u, 0, 0, 0);
        SCHED();
      }
#define G2A 4
      gl(2, 0, G2A);
      kv(IC<1>(), IC<G2A + QG>());
      gl(2, G2A, 4);
      STAMPF(9);
      u32x4 k3[2];
      auto ld_k3 = [&]() {
        k3[0] = __builtin_amdgcn_raw_buffer_load_b128(rs, k3o, WS_KV3, SC1);
        k3[1] = __builtin_amdgcn_raw_buffer_load_b128(rs, k3o, WS_KV3 + 5120u, SC1);
      };
      auto ld_m = [&]() {
        mv[0] = __builtin_amdgcn_raw_buffer_load_b64(rs, mo, WS_MODQ, SC1);
        mv[1] = __builtin_amdgcn_raw_buffer_load_b64(rs, mo, WS_MODQ + 20480u, SC1);
      };
      auto ld_q = [&]() {
        asm volatile("" ::: "memory");
        if (!M_T) ld_m();
        SCHED();
#pragma unroll
        for (int s = 0; s < 4; ++s) qv[s] = __builtin_amdgcn_raw_buffer_load_b128(rs, yo, WS_QB + (unsigned)s * (32u * 20480u), BULK_AUX);
        SCHED();
      };
      auto k_bad = [&]() { return __any((ne2(k3[0][1], k3[0][3]) | ne2(k3[1][1], k3[1][3])) != 0u); };
      auto k_put = [&]() {
        *(u32x4*)(lds + k3d) = (u32x4){k3[0][0], k3[0][2], k3[1][0], k3[1][2]};
        pf_set(12);
        SCHED();
      };
#define KE_T 0
      const bool early = (unsigned)__builtin_amdgcn_s_memrealtime() - t_start < KE_T;
      if (!early) {
        hold(K_T);
        ld_k3();
        SCHED();
        ld_e();
      } else {
        DRAIN();
      }
      kv(IC<2>(), IC<6>());
      if (KE_T && early) {
        hold(K_T);
        ld_k3();
        SCHED();
        ld_e();
      }
      STAMPF(11);
      const bool kb = k_bad();
      if (!kb) k_put();
      const bool sb = s_bad();
      if (kb) ld_k3();
      if (sb) ld_e(); else ld_q();
      if (kb) {
        _Pragma("unroll 1") for (unsigned spins = 0; spins < SPIN_CAP; ++spins) {
          if (!k_bad()) break;
          __builtin_amdgcn_s_sleep(4);
          ld_k3();
        }
        k_put();
      }
      STAMPF(18);
      if (sb) {
        _Pragma("unroll 1") for (unsigned spins = 0; spins < SPIN_CAP; ++spins) {
          if (!s_bad()) break;
          __builtin_amdgcn_s_sleep(4);
          ld_e();
        }
        ld_q();
      }
      STAMPF(17);
      if (M_T) { hold(M_T); ld_m(); SCHED(); }
      STAMP(2);
      u32x2 yv[4][2];
      auto ld_y = [&](int s) {
#pragma unroll
        for (int e = 0; e < 2; ++e) yv[s][e] = trd(lds + yr + 1024 * s + 32 * e);
      };
      float r1[4][2], m1[4][2];
#pragma unroll
      for (int i = 0; i < 8; ++i) {
        const unsigned x = sv[i >> 1][2 * (i & 1)];
        r1[i >> 1][i & 1] = __builtin_amdgcn_rcpf(bfhi(x));
        m1[i >> 1][i & 1] = -bflo(x) * r1[i >> 1][i & 1];
        asm volatile("" : "+v"(r1[i >> 1][i & 1]), "+v"(m1[i >> 1][i & 1]));
      }
      pf_wait(20);
      ld_y(0); ld_y(1);
      SCHED();
      if (__any(ne2(mv[0][0], mv[1][0]) != 0u)) {
        _Pragma("unroll 1") for (unsigned spins = 0; spins < SPIN_CAP; ++spins) {
          __builtin_amdgcn_s_sleep(4);
          ld_m();
          if (!__any(ne2(mv[0][0], mv[1][0]) != 0u)) break;
        }
      }
      STAMPF(13);
      f32x4 al, be;
      {
        const float mgf = rbf(sum_rows(__uint_as_float(mv[0][1]) + __uint_as_float(mv[1][1])));
        auto rl = [&](int i) { return __uint_as_float(__builtin_amdgcn_readlane(__float_as_uint(mgf), i)); };
        const f32x4 sa = {rl(0), rl(1), rl(2), rl(3)}, ca = {rl(4), rl(5), rl(6), rl(7)}, ga = {rl(8), rl(9), rl(10), rl(11)};
        al = rbf4(ca + 1.0f) * ga;
        be = sa * ga;
        asm volatile("" : "+v"(al), "+v"(be));
      }
      f32x4 qf[2][2], P[2][2];
      auto xpre = [&](int s) {
#pragma unroll
        for (int e = 0; e < 2; ++e) {
          const float r = r1[s][e], m = m1[s][e];
          const f32x4 qq = {bflo(qv[s][2 * e]), bfhi(qv[s][2 * e]), bflo(qv[s][2 * e + 1]), bfhi(qv[s][2 * e + 1])};
          qf[s & 1][e] = qq;
          P[s & 1][e] = __builtin_elementwise_fma(__builtin_elementwise_fma(qq, (f32x4){r, r, r, r}, (f32x4){m, m, m, m}), al, be);
        }
        asm volatile("" : "+v"(qf[s & 1][0]), "+v"(qf[s & 1][1]), "+v"(P[s & 1][0]), "+v"(P[s & 1][1]));
      };
      auto xfin = [&](int s) {
        u32x4 o;
#pragma unroll
        for (int e = 0; e < 2; ++e) {
          const u32x2 y = yv[s][e];
          const f32x4 yf = {bflo(y[0]), bfhi(y[0]), bflo(y[1]), bfhi(y[1])};
          const f32x4 x = __builtin_elementwise_fma(P[s & 1][e], yf, qf[s & 1][e]);
          o[2 * e] = pack2(x[0], x[1]);
          o[2 * e + 1] = pack2(x[2], x[3]);
        }
        ST16S(o, rs, yo, WS_X1B + (unsigned)s * (32u * 20480u), SC1);
      };
      xpre(0); xfin(0);
      xpre(1); xfin(1);
      SCHED();
      xpre(2); xpre(3);
#define FVR 5
#define MSPLIT 0
      if (MSPLIT) {
        pf_wait(24);
        ld_y(2);
        xfin(2);
        SCHED();
        pf_wait(28);
        ld_y(3);
        xfin(3);
      } else {
        pf_wait(28);
        ld_y(2); ld_y(3);
        xfin(2); xfin(3);
      }
      STAMP(28);
      if (BG == 1 && l == 0) lf[40 + pw] = tag;
      asm volatile("s_waitcnt vmcnt(2)" ::: "memory");
      ST16S(tg4, rs, xfo, WS_FX, SC1);
      SCHED();
      asm volatile("s_waitcnt vmcnt(1)" ::: "memory");
      ST16S(tg4, rs, xfo, WS_FX + 32u * 160u * 128u, SC1);
      if (BG == 2 && l == 0) lf[40 + pw] = tag;
      STAMP(5);
    } else {
      const int ch = w;
      char* U = lds + L_U + ch * U_CH;
      char* G = lds + ch * (4 * G_COPY);
      {
        const rsrc_t rf = __builtin_amdgcn_make_buffer_rsrc((void*)(p.ssm_k + (size_t)(quad * 4 + ch) * S_), 0, S_ * 2, 0x00020000);
        u32x4 fa[4];
        u32x2 fb[4];
        __builtin_amdgcn_s_setprio(3);
#pragma unroll
        for (int k = 0; k < 4; ++k) {
          fa[k] = __builtin_amdgcn_raw_buffer_load_b128(rf, l16 + (unsigned)k * 1024u, 0, NT);
        }
        __builtin_amdgcn_s_setprio(1);
        TAG_READY();
#pragma unroll
        for (int k = 0; k < 4; ++k) {
#pragma unroll
          for (int d = 0; d < 2; ++d)
            fb[k][d] = __builtin_amdgcn_update_dpp(k < 3 ? (unsigned)__builtin_amdgcn_readfirstlane(fa[k < 3 ? k + 1 : k][d]) : 0u, fa[k][d], 0x130, 0xf, 0xf, false);
          const unsigned x[6] = {fa[k][0], fa[k][1], fa[k][2], fa[k][3], fb[k][0], fb[k][1]};
          unsigned y[5];
#pragma unroll
          for (int d = 0; d < 5; ++d) y[d] = __builtin_amdgcn_alignbit(x[d + 1], x[d], 16);
          char* gp = G + 32 + k * 1024 + l16;
          *(u32x4*)(gp) = fa[k];
          *(u32x4*)(gp + G_COPY) = (u32x4){y[0], y[1], y[2], y[3]};
          *(u32x4*)(gp + 2 * G_COPY) = (u32x4){x[1], x[2], x[3], x[4]};
          *(u32x4*)(gp + 3 * G_COPY) = (u32x4){y[1], y[2], y[3], y[4]};
          if (k == 0) {
            const unsigned f0 = __builtin_amdgcn_readfirstlane(x[0]), f1 = __builtin_amdgcn_readfirstlane(x[1]);
            const unsigned long long f64 = (unsigned long long)f0 | ((unsigned long long)f1 << 32);
            const unsigned long long hi = l ? (f64 << (64 - 16 * (l & 3))) : 0ull;
            if (l < 4) {
              *(u32x4*)(G + l * G_COPY) = (u32x4){0u, 0u, 0u, 0u};
              *(u32x4*)(G + l * G_COPY + 16) = (u32x4){0u, 0u, (unsigned)hi, (unsigned)(hi >> 32)};
            }
            if (l >= 32) *(u32x4*)(U + (l - 32) * 16) = (u32x4){0u, 0u, 0u, 0u};
          }
        }
        asm volatile("s_waitcnt lgkmcnt(0)" ::: "memory");
        STAMP(1);
      }
      const int a = n16, h = h4, jj = h >> 1, e2 = h & 1;
      const char* ap = G + ((a + 1) & 3) * G_COPY + 2 * (16 * jj + a - 8 * e2 + 9 - ((a + 1) & 3));
      unsigned bp = (unsigned)(L_U + ch * U_CH + 32 * (a - jj) + 16 * e2);
      auto lda = [&](u32x4* A, int r, int p0, int np) {
#pragma unroll
        for (int pp = 0; pp < np; ++pp) {
          u32x2 lo = *(const volatile LAS u32x2*)(ap + 512 * (p0 + pp) + 64 * r);
          u32x2 hi = *(const volatile LAS u32x2*)(ap + 512 * (p0 + pp) + 64 * r + 8);
          A[pp] = (u32x4){lo[0], lo[1], hi[0], hi[1]};
        }
      };
      u32x4 AD;
      {
        const unsigned dDr = (*(cptr_t)(p.ssm_D + quad * 4 + (ch & 2)) >> (16 * (ch & 1))) & 0xffffu;
        const unsigned dv = h == (a >> 3) ? ((a & 1) ? dDr : dDr << 16) : 0u;
        const int dw = (7 - (a & 7)) >> 1;
#pragma unroll
        for (int d = 0; d < 4; ++d) AD[d] = dw == d ? dv : 0u;
      }
      u32x4 A0[8][4];
      f32x4 acc[8];
#pragma unroll
      for (int n = 0; n < 8; ++n) acc[n] = (f32x4){0.f, 0.f, 0.f, 0.f};
#pragma unroll
      for (int r = 0; r < 8; ++r) lda(A0[r], r, 0, 4);
      auto yput = [&](int s) {
#pragma unroll
        for (int e = 0; e < 2; ++e)
          *(u32x2*)(U + 1024 * s + 512 * e + (16 * a + 4 * h) * 2) = (u32x2){pack2(acc[2 * s + e][0], acc[2 * s + e][1]), pack2(acc[2 * s + e][2], acc[2 * s + e][3])};
        asm volatile("" ::: "memory");
        if (l == 0) lf[16 + 4 * s + ch] = tag;
      };
      asm volatile("" : "+v"(AD), "+v"(bp), "+v"(acc[0]), "+v"(acc[1]), "+v"(acc[2]), "+v"(acc[3]), "+v"(acc[4]), "+v"(acc[5]), "+v"(acc[6]), "+v"(acc[7]));
      auto stage = [&](auto S__) {
        constexpr int s = decltype(S__)::value, NH = 4 - 2 * s, NP = 8 - 2 * s;
        u32x2 A1[3][NH][2];
        u32x4 B[3][2];
        auto lda1 = [&](int r, int i) { A1[r % 3][i >> 1][i & 1] = *(const volatile LAS u32x2*)(ap + 512 * (4 + (i >> 1)) + 64 * r + 8 * (i & 1)); };
        auto ldb = [&](int r, int e) { B[r % 3][e] = *(const u32x4*)(lds + bp + 512 * (2 * s + e + 1) - 64 * r); };
#pragma unroll
        for (int i = 0; i < 4 * NH; ++i) lda1(i / (2 * NH), i % (2 * NH));
        SCHED();
        if (s > 0) yput(s - 1);
        lf_wait(4 * s, 4);
        if (s == 0) STAMP(3);
#pragma unroll
        for (int i = 0; i < 4; ++i) ldb(i >> 1, i & 1);
        SCHED();
#pragma unroll
        for (int e = 0; e < 2; ++e) MFMA_IP(acc[2 * s + e], AD, B[0][e]);
        sfor<0, 8 * (2 * NP - 1)>([&](auto I_) __attribute__((always_inline)) {
          constexpr int r = decltype(I_)::value / (2 * NP - 1), i = decltype(I_)::value % (2 * NP - 1), e = i >= NP, pp = i - e * NP, h1 = pp < 4 ? 0 : pp - 4;
          const u32x4 a1 = {A1[r % 3][h1][0][0], A1[r % 3][h1][0][1], A1[r % 3][h1][1][0], A1[r % 3][h1][1][1]};
          const u32x4 fa = pp < 4 ? A0[r][pp & 3] : a1;
          MFMA_IP(acc[2 * s + e + pp], fa, B[r % 3][e]);
          SCHED();
          if (r + 2 < 8 && i < 2) ldb(r + 2, i & 1);
          if (r + 2 < 8 && i >= 2 && i < 2 + 2 * NH) lda1(r + 2, (i - 2) % (2 * NH));
          SCHED();
        });
      };
      stage(IC<0>());
      STAMP(4);
      stage(IC<1>());
      STAMPF(9);
      u32x2 fv;
      if (MSPLIT) {
        u32x4 B[8][2];
        auto ldb = [&](int r, int m0) {
#pragma unroll
          for (int m = 0; m < 2; ++m) B[r][m] = *(const u32x4*)(lds + bp + 512 * (m0 + m + 1) - 64 * r);
        };
        asm volatile("s_nop 1" : "+v"(acc[2]), "+v"(acc[3]));
        yput(1);
        if (w == 3) wo_ld();
        __builtin_amdgcn_s_setprio(3);
        lf_wait(8, 4);
        STAMP(22);
        auto ldb1 = [&](int i, int m0) { B[i >> 1][i & 1] = *(const u32x4*)(lds + bp + 512 * (m0 + (i & 1) + 1) - 64 * (i >> 1)); };
#pragma unroll
        for (int r = 0; r < 3; ++r) ldb(r, 4);
        SCHED();
#pragma unroll
        for (int m = 0; m < 2; ++m) MFMA_IP(acc[4 + m], AD, B[0][m]);
        sfor<0, 24>([&](auto I_) __attribute__((always_inline)) {
          constexpr int i = decltype(I_)::value, r = i / 3, k = i % 3;
          MFMA_IP(acc[4 + (k != 1)], A0[r][k == 0], B[r][k == 2]);
          SCHED();
          if (i < 10) ldb1(6 + i, 4);
          SCHED();
        });
        sfor<0, 32>([&](auto I_) __attribute__((always_inline)) {
          constexpr int i = decltype(I_)::value, r = i >> 2, k = i & 3;
          MFMA_IP(acc[6 + (k & 1)], A0[r][2 + (k & 1) - (k >> 1)], B[r][k >> 1]);
          SCHED();
          if (i == 7) yput(2);
          if (i == 4 * FVR) fv = ld8(rs, b2o);
          if (i == 19) lf_wait(12, 4);
          if (i >= 19 && i < 29) ldb1(i - 19, 6);
          SCHED();
        });
#pragma unroll
        for (int m = 0; m < 2; ++m) MFMA_IP(acc[6 + m], AD, B[0][m]);
        sfor<0, 24>([&](auto I_) __attribute__((always_inline)) {
          constexpr int i = decltype(I_)::value, r = i / 3, k = i % 3;
          MFMA_IP(acc[6 + (k != 1)], A0[r][k == 0], B[r][k == 2]);
          SCHED();
          if (i < 6) ldb1(10 + i, 6);
          SCHED();
        });
        asm volatile("s_nop 7" : "+v"(acc[6]), "+v"(acc[7]));
        yput(3);
      } else {
        u32x4 B[3][4];
        auto ldb = [&](int r, int m) { B[r % 3][m] = *(const u32x4*)(lds + bp + 512 * (5 + m) - 64 * r); };
        asm volatile("s_nop 1" : "+v"(acc[2]), "+v"(acc[3]));
        yput(1);
        if (w == 3) wo_ld();
        __builtin_amdgcn_s_setprio(3);
        lf_wait(8, 8);
        STAMP(22);
#pragma unroll
        for (int i = 0; i < 8; ++i) ldb(i >> 2, i & 3);
        SCHED();
#pragma unroll
        for (int m = 0; m < 4; ++m) MFMA_IP(acc[4 + m], AD, B[0][m]);
        sfor<0, 80>([&](auto I_) __attribute__((always_inline)) {
          constexpr int r = decltype(I_)::value / 10, i = decltype(I_)::value % 10, m = i < 4 ? 0 : i < 7 ? 1 : i < 9 ? 2 : 3, pp = i - (m * (9 - m)) / 2;
          MFMA_IP(acc[4 + m + pp], A0[r][pp], B[r % 3][m]);
          SCHED();
          if (r + 2 < 8 && i < 4) ldb(r + 2, i & 3);
          if (r == 1 && i == 9) fv = ld8(rs, b2o);
          SCHED();
        });
#pragma unroll
        for (int e = 0; e < 4; ++e) {
          if (e == 1) asm volatile("s_nop 4" : "+v"(acc[5]), "+v"(acc[6]), "+v"(acc[7]));
          *(u32x2*)(U + 2048 + 512 * e + (16 * a + 4 * h) * 2) = (u32x2){pack2(acc[4 + e][0], acc[4 + e][1]), pack2(acc[4 + e][2], acc[4 + e][3])};
          SCHED();
        }
        asm volatile("" ::: "memory");
        if (l == 0) lf[28 + ch] = tag;
      }
      STAMP(28);
      __builtin_amdgcn_s_setprio(0);
      b2_wait(fv, 0);
      lf_wait(20, 4);
      DRAIN();
    }
    __builtin_amdgcn_s_setprio(0);
  } else {
    const int at = idx - 20;
    char* const scr = lds + WBLK + 14848;
    auto gv_blk = [&](int j) { return j < 160 ? 30 + (j >> 3) : j < 400 ? (j >> 3) - 20 : j >> 3; };
    auto gv_fin = [&](const u32x4* pre, int j) {
      float acc[8];
      gemv_fin(acc, pre, 10, scr);
      gemv_put(acc, j < 160 ? WS_MODP + (unsigned)((j & 7) * 1280 + (j >> 3) * 64) * 8u : WS_MODQ + (unsigned)((j & 7) * 2560 + ((j >> 3) - 20) * 64) * 8u);
    };
    const int ja = xcd * 12 + at, ai = xcd * 8 + at, bi = xcd * 4 + at - 8, g = w >> 2, wj = w & 3;
    const int rj = w == 0 ? ja : w == 1 ? (at >= 4 ? 92 + ai : -1) : w == 4 ? 160 + ja : w == 5 && at < 8 ? 256 + ai : -1;
    char* const rst = lds + (g ? WBLK : 65536) + wj * 7424;
    u32x4 pre[11];
    if (at >= 8 || g == 0) {
      TOUCH_AT(2);
      const int T = at >= 8 ? 64 + 2 * bi + g : ai;
      unsigned* mds = (unsigned*)(lds + 2048) + 16 * g;
      char* qt = lds + L_RT + g * (16 * 1312);
      u32x4 ld[5];
      const bool e1 = at >= 8 && w == 3;
      if (e1) gemv_load(pre, gv_blk(128 + bi), bi & 7, 10);
      else __builtin_amdgcn_s_sleep(8);
#pragma unroll
      for (int j = 0; j < 5; ++j)
        ld[j] = __builtin_amdgcn_raw_buffer_load_b128(rq, (unsigned)(h4 * D_ * 2 + n16 * 16 + j * 256), (unsigned)((32 * (T >> 1) + 8 * wj + 4 * (T & 1)) * D_ * 2), QG ? 0 : NT);
      TAG_READY();
      {
        const bf2_t ONE2 = {(__bf16)1.0f, (__bf16)1.0f};
        char* dst = qt + (4 * wj + h4) * 1312 + n16 * 16;
        float sa = 0.f, va = 0.f;
#pragma unroll
        for (int j = 0; j < 5; ++j) {
          if (!QG) *(u32x4*)(dst + j * 256) = ld[j];
#pragma unroll
          for (int d = 0; d < 4; ++d) {
            const unsigned a0 = ld[j][d];
            const bf2_t xa = __builtin_bit_cast(bf2_t, a0);
            sa = __builtin_amdgcn_fdot2_f32_bf16(xa, ONE2, sa, false); va = __builtin_amdgcn_fdot2_f32_bf16(xa, xa, va, false);
          }
        }
        if (!QG) lf_set(8 + w);
        const float ma = sum16(sa) * (1.0f / D_);
        va = __builtin_fmaxf(sum16(va) * (1.0f / D_) - ma * ma, 0.f);
        if (n16 == 0) mds[4 * wj + h4] = pack2(ma, __builtin_amdgcn_sqrtf(rbf(rbf(va) + 1e-5f)));
      }
      if (QG) lf_set(16 + w);
      if (e1) gv_fin(pre, 128 + bi);
      if (!QG) lf_wait(8 + 4 * g, 4);
      STAMP(23);
      if (QG) {
        if (wj == 2) {
          lf_wait(16 + 4 * g, 4);
          st16(rs, WS_ST1 + (unsigned)(l >> 3) * 16384u + (unsigned)T * 128u + (unsigned)(l & 7) * 16u, (u32x4){mds[l & 7], tag, mds[8 + (l & 7)], tag});
          STAMP(22);
        }
      } else {
        const char* src = qt + (l & 7) * 1312 + (40 * wj + (l >> 3)) * 8;
#pragma unroll
        for (int j = 0; j < 5; ++j) {
          const u32x2 x = *(const u32x2*)(src + j * 64), y = *(const u32x2*)(src + j * 64 + 8 * 1312);
          st16(rs, WS_QB + (unsigned)T * 20480u + (unsigned)(5 * wj + j) * 1024u + l16, (u32x4){x[0], x[1], y[0], y[1]});
        }
      }
      SCHED();
      const int g2 = at < 8 || w < 3 || w == 4 ? 400 : w == 3 ? 384 + bi : 256 + wj * 32 + bi;
      if (g2 < 400) {
        gemv_load(pre, gv_blk(g2), g2 & 7, 10);
        if (!QG) asm volatile("s_waitcnt vmcnt(10)" ::: "memory");
      } else if (w < 2 && rj >= 0) {
        rt_ld(rj, rst);
        if (!QG) asm volatile("s_waitcnt vmcnt(7)" ::: "memory");
      } else if (!QG) {
        DRAIN();
      }
      if (!QG) lf_set(16 + w);
      if (!QG && wj == 2) {
        lf_wait(16 + 4 * g, 4);
        st16(rs, WS_ST1 + (unsigned)(l >> 3) * 16384u + (unsigned)T * 128u + (unsigned)(l & 7) * 16u, (u32x4){mds[l & 7], tag, mds[8 + (l & 7)], tag});
        STAMP(22);
      }
      if (w >= 4 && w < 7) lf_set(w);
      if (g2 < 400) gv_fin(pre, g2);
      STAMP(25);
    } else {
      constexpr int KB = L_RT + 16 * 1312;
      TOUCH_AT(3);
      const int g1 = w < 6 ? 2 * ai + wj : 32 + wj * 64 + ai;
      if (w >= 6) {
        const rsrc_t rk = __builtin_amdgcn_make_buffer_rsrc((void*)(w == 6 ? p.k : p.v), 0, S_ * D_ * 2, 0x00020000);
        const unsigned ko = (unsigned)((1536 + 8 * ai) * (D_ * 2) - (w == 6 ? 4096 : 0));
        char* dst = lds + KB + (w == 6 ? 0 : 14336);
        __builtin_amdgcn_s_sleep(3);
#pragma unroll
        for (int n = 0; n < 10; ++n) __builtin_amdgcn_raw_ptr_buffer_load_lds(rk, (LAS void*)(dst + n * 1024), 16, l16, ko + (unsigned)n * 1024u, 0, NT);
        if (w == 6) {
#pragma unroll
          for (int n = 10; n < 14; ++n) __builtin_amdgcn_raw_ptr_buffer_load_lds(rk, (LAS void*)(dst + n * 1024), 16, l16, ko + (unsigned)n * 1024u, 0, NT);
        }
      }
      SCHED();
      u32x4 swt[2];
      swt[0] = *(const u32x4*)(p.shift_k + (tid - 256) * 8);
      swt[1] = *(const u32x4*)(p.shift_k + (256 + l) * 8);
      SCHED();
      if (w >= 6) __builtin_amdgcn_s_sleep(8);
      gemv_load(pre, gv_blk(g1), g1 & 7, 10);
      TAG_READY();
      asm volatile("s_waitcnt vmcnt(11)" : "+v"(swt[0]), "+v"(swt[1]) :: "memory");
      lf_set(8 + w);
      if (w < 6) gv_fin(pre, g1);
      STAMP(25);
      lf_wait(12, 4);
      STAMP(23);
#pragma unroll 1
      for (int n = 0; n < (w == 7 ? 2 : 1); ++n) {
        {
          const int pr = n ? 256 + l : tid - 256;
          const char* kp = lds + KB + 4096 + pr * 4;
          unsigned kk[11], vv[8];
#pragma unroll
          for (int i = 0; i < 11; ++i) kk[i] = *(const unsigned*)(kp + (i - 3) * 1280);
#pragma unroll
          for (int i = 0; i < 8; ++i) vv[i] = *(const unsigned*)(kp + 10240 + i * 1280);
          const u32x4 sw = n ? swt[1] : swt[0];
          const f32x2 wt[4] = {{bflo(sw[0]), bflo(sw[2])}, {bfhi(sw[0]), bfhi(sw[2])}, {bflo(sw[1]), bflo(sw[3])}, {bfhi(sw[1]), bfhi(sw[3])}};
          f32x2 o[8];
#pragma unroll
          for (int i = 0; i < 8; ++i) {
            f32x2 t = (f32x2){bflo(kk[i + 3]), bfhi(kk[i + 3])} * wt[0];
#pragma unroll
            for (int j = 1; j < 4; ++j) t = __builtin_elementwise_fma((f32x2){bflo(kk[i + 3 - j]), bfhi(kk[i + 3 - j])}, wt[j], t);
            o[i] = t * (f32x2){bflo(vv[i]), bfhi(vv[i])};
          }
#pragma unroll
          for (int e = 0; e < 4; ++e) {
            const int i = 7 - 4 * (e & 1);
            st16(rs, WS_KV3 + (unsigned)ai * 20480u + (unsigned)(e * 5120 + pr * 16),
                 (u32x4){pack2(o[i][e >> 1], o[i - 1][e >> 1]), tag, pack2(o[i - 2][e >> 1], o[i - 3][e >> 1]), tag});
          }
        }
      }
      STAMP(22);
      if (w < 7) lf_set(w);
      if (w >= 6) gv_fin(pre, g1);
      STAMPF(30);
    }
    if (w == 6 && ja < 80) {
      hold(AUX_T);
      gemv_load(pre, gv_blk(400 + ja), ja & 7, 10);
      gv_fin(pre, 400 + ja);
    }
    if (rj >= 0) {
      if (w >= 2) { hold(RT2_T); rt_ld(rj, rst); }
      rt_job(rj, rst);
    }
    STAMP(26);
    lf_set(w);
    if (w == 3) { hold(WA_T); wo_ld(); }
    STAMP(3);
    if (w < 4) {
      hold(W_T);
      b2_wait((u32x2){~tag, 0u}, 1);
      lf_wait(0, 8);
      STAMP(4);
    }
    DRAIN();
  }

  STAMPF(16);
  u32x4 wox[5];
  if (w == 3) {
    const char* bp = wo_st + (4 * h4 + (n16 >> 2)) * 160 + (n16 & 3) * 8;
#pragma unroll
    for (int dj = 0; dj < 5; ++dj) wox[dj] = trfrag(bp + dj * 32, 160);
    asm volatile("s_waitcnt lgkmcnt(0)" : "+v"(wox[0]), "+v"(wox[1]), "+v"(wox[2]), "+v"(wox[3]), "+v"(wox[4]) :: "memory");
  }
  auto wo_put = [&]() {
    if (w == 3 && R < 28) {
      const unsigned base = WS_WOUTF + (unsigned)c * WBLK + (unsigned)((R >> 1) * 10240 + (R & 1) * 1024);
#pragma unroll
      for (int dj = 0; dj < 5; ++dj) st16(rs, base + (unsigned)((R & 1) ? dj : (dj + 2) % 5) * 2048u + l16, wox[dj]);
    }
  };
  if (cs2) {
    cs_ld(csv, (8 + c) * 7 + R - 20);
    SCHED();
    load_b2(0, 12);
    SCHED();
    cs_fin(csv, (8 + c) * 7 + R - 20);
  } else if (w < 4) {
    asm volatile("" ::: "memory");
    load_b2(0, 12);
  }
  if (w < 4) {
    load_b2(12, BK);
    if (BG && is_conv) lf_wait(40, 4);
    load_b2(BK, 35);
  }
  wo_put();
  STAMPF(29);
  const unsigned xfb = WS_FX + (unsigned)(((R >> 1) * 4 + (R & 1) * 2 + (c >> 2)) * 160 + l) * 128u;
  unsigned xf3[3];
  if (w == 7) {
    lf_wait(36, 4);
    int dt = (int)(lf[44 + ((l & 31) < 20 ? l & 31 : 0)] - t_start);
    dt = l < 32 ? dt : -dt;
    dt = max(dt, __builtin_amdgcn_update_dpp(0, dt, 0xB1, 0xf, 0xf, true));
    dt = max(dt, __builtin_amdgcn_update_dpp(0, dt, 0x4E, 0xf, 0xf, true));
    dt = max(dt, __builtin_amdgcn_update_dpp(0, dt, 0x124, 0xf, 0xf, true));
    dt = max(dt, __builtin_amdgcn_update_dpp(0, dt, 0x128, 0xf, 0xf, true));
    const int d1 = min(max(max(__builtin_amdgcn_readlane(dt, 0), __builtin_amdgcn_readlane(dt, 16)), 0), 300);
    const int d0 = min(max(max(__builtin_amdgcn_readlane(dt, 32), __builtin_amdgcn_readlane(dt, 48)), 0), 300);
    dt = ((d1 + d0) * YW >> 3) - d0;
    __builtin_amdgcn_s_setprio(YPR);
    STAMPF(24);
    hold((unsigned)(YG_T + dt));
    STAMPF(30);
    xf3[0] = ld4(rs, xfb); xf3[1] = ld4(rs, xfb + 8192u); xf3[2] = ld4(rs, l < 32 ? xfb + 16384u : ~0u);
    __builtin_amdgcn_s_setprio(0);
  }
  SCHED();
  u32x4 k_c;
  unsigned kc_o = w < 2 ? (unsigned)(tid < 112 ? tid : 0) * 16u : 0x80000000u, kc_d = (unsigned)(L_CONST + (tid & 127) * 8);
  auto kc_ld = [&](int z2) { k_c = __builtin_amdgcn_raw_buffer_load_b128(rs, kc_o, WS_CS + (unsigned)(8 * z2 + c) * 2048u, SC1); };
  auto consts_put = [&](int z2) {
    if (w < 2) {
      if (__any(ne2(k_c[0], k_c[2]) != 0u)) {
        _Pragma("unroll 1") for (unsigned spins = 0; spins < SPIN_CAP; ++spins) {
          __builtin_amdgcn_s_sleep(8);
          kc_ld(z2);
          if (!__any(ne2(k_c[0], k_c[2]) != 0u)) break;
        }
      }
      if (z2) *(u32x2*)(lds + kc_d + 1024) = (u32x2){k_c[1], k_c[3]};
      else { const unsigned d = kc_d - ((kc_d >> 1) & 4u); *(unsigned*)(lds + d) = k_c[1]; *(unsigned*)(lds + d + 8) = k_c[3]; }
    }
  };
  const int wq = w ^ rotr, rt1 = wq & 3, kh1 = w >> 2;
  unsigned bo0 = (unsigned)(kh1 * 71680) + l16, bo1 = bo0 + 35840u;
  asm volatile("" : "+v"(bo0), "+v"(bo1));
  unsigned pso[3], pqo[3], oto = (unsigned)(((w ^ 4) * 7 * 64 + l) * 8), rdo = (unsigned)((w * 7 * 64 + l) * 8), cso = (unsigned)(L_CONST + (128 + n16) * 8);
#pragma unroll
  for (int e = 0; e < 3; ++e) {
    const int row = rt1 * 16 + (e < 2 ? 4 * h4 + 2 * kh1 + e : n16);
    pso[e] = (unsigned)(L_PST + row * 4); pqo[e] = (unsigned)(L_PST + 512 + row * 16 + (row & 3) * 4);
  }
  unsigned spo = (unsigned)(L_Z + tid * 16), p6o = (unsigned)(L_PST + (wq * 16 + 4 * h4) * 4), pgo = (unsigned)(L_PST + 512 + (wq * 16 + n16) * 16);
  asm volatile("" : "+v"(spo), "+v"(p6o), "+v"(pgo));
  p6o = n16 == 12 ? p6o : spo;
  pgo = h4 == (n16 >> 2) ? pgo : spo;
  unsigned bsrc = (unsigned)(((l >> 1) & 3) * 64 + (l & 1) * 4), ppo = l < 8 ? l16 : 0x80000000u;
  asm volatile("" : "+v"(pso[0]), "+v"(pso[2]), "+v"(pqo[0]), "+v"(pqo[2]), "+v"(oto), "+v"(rdo), "+v"(cso), "+v"(p6o), "+v"(pgo), "+v"(bsrc), "+v"(ppo), "+v"(kc_o), "+v"(kc_d));
  pso[1] = pso[0] + 4u; pqo[1] = pqo[0] + 20u;
#define BFRAG(q, j) (*(const u32x4*)(lds + ((q) < 5 ? bo0 : bo1) + ((q) % 5) * 7168 + (j) * 1024))
  u32x4 h2[10];
  unsigned xv = (unsigned)((4 * ((l >> 3) & 1) + h4) * 128 + (l & 7) * 16), xs0 = WS_X1B + (unsigned)(R * 4 + rt1) * 20480u + (unsigned)(10 * kh1) * 1024u;
  unsigned wvo = wsp_vo, bso = (unsigned)tau(rt1, n16) * 2u, fgo = l < 32 ? (unsigned)(l >> 3) * F4S + (unsigned)(l & 7) * 16u : 0x80000000u;
  asm volatile("" : "+v"(xv), "+v"(wvo), "+v"(bso), "+v"(fgo));
#define WO3 (-92)
#define PINS(x) do { x = (unsigned)__builtin_amdgcn_readfirstlane((int)x); asm volatile("" : "+s"(x)); } while (0)
  PINS(xs0);
  unsigned zs[8], wx8, wx4, wgb, fzs, uo, ui, xs, wl2, os, zos, pos, s1, s3;
#define LDX(q) h2[q] = __builtin_amdgcn_raw_buffer_load_b128(rs, xv + (unsigned)((q) & 3) * 1024u, xs0 + (unsigned)((q) >> 2) * 4096u, BULK_AUX)
  auto flag4 = [&](unsigned off) { ST16S(((u32x4){tag, tag, tag, tag}), rs, fgo, WS_F4 - WS_FWI + off, SC1); };
  unsigned bs_raw, fwo[1];
  f32x4 acc[7];
  float mu_n, rs_n;
  {
    unsigned f1o[1] = {w == 1 && l < 20 ? fwi + (unsigned)(c * 20 + l) * 128u : ~0u}, f1v;
    fwo[0] = w == 3 && l < 28 ? fwi + (WS_FWO - WS_FWI) + (unsigned)(c * 32 + l) * 128u : ~0u;
    asm volatile("" : "+v"(f1o[0]), "+v"(fwo[0]));
    if (w == 7) {
      asm volatile("" : "+v"(xf3[0]), "+v"(xf3[1]), "+v"(xf3[2]));
      if (__any(ne2(xf3[0], xf3[1]) != 0u) || __builtin_amdgcn_ballot_w64(xf3[2] == tag) != 0xffffffffull) {
        const unsigned fo[3] = {xf3[0] != tag ? xfb : ~0u, xf3[1] != tag ? xfb + 8192u : ~0u, l < 32 && xf3[2] != tag ? xfb + 16384u : ~0u};
        wait_lines<3, YSL>(rs, fo, tag);
      }
      if (l == 0) lf[33] = tag;
    }
    lf_wait(33, 1);
    STAMP(7);
    SCHED();
#pragma unroll
    for (int q = 0; q < 3; ++q) LDX(q);
    SCHED();
    __builtin_amdgcn_s_waitcnt(0x0F73);
    BAR();
    STAMP(8);
#pragma unroll
    for (int j = 0; j < 7; ++j) acc[j] = (f32x4){0.f, 0.f, 0.f, 0.f};
    f32x4 accg = {0.f, 0.f, 0.f, 0.f};
    u32x4 bf[7];
#pragma unroll
    for (int j = 0; j < 7; ++j) bf[j] = BFRAG(0, j);
    SCHED();
#pragma unroll
    for (int q = 3; q < 6; ++q) LDX(q);
    if (w == 3) flag4(WS_FWO + (unsigned)(c * 32 + R) * 128u);
    SCHED();
#pragma unroll
    for (int t = 0; t < 8; ++t) { zs[t] = WS_Z2F + (unsigned)(c * 64 + 8 * w + 4 * ((t >> 2) ^ rotg) + ((t + rotb) & 3)) * 7168u; PINS(zs[t]); }
    wx8 = (unsigned)((w + (idx >> 2)) & 7) * 1024u; wx4 = wx8 & 3072u; wgb = WS_WOUTF + (unsigned)c * WBLK; fzs = fz + (unsigned)(c * 32 + 4 * w) * 128u;
    uo = WS_UF + (unsigned)((R * 4 + rt1) * 8 + c) * 4096u + (unsigned)kh1 * 2048u; ui = WS_UF + (unsigned)((R * 4 + rt1) * 8 + 4 * kh1) * 4096u;
    xs = WS_X1B + (unsigned)(R * 4 + rt1) * 20480u + (unsigned)(5 * c + 2 * kh1) * 512u; wl2 = (unsigned)(120 + WO3 + (w < 4 ? w + 16 : w)) * 1024u;
    os = (unsigned)(((R * 64 + tau(rt1, 0)) * D_ + 80 * c + 32 * kh1) * 2);
    zos = WS_Z2F + (unsigned)((c * 64 + 4 * (R >> 1)) * 7 + (w < 6 ? w + 1 : 0)) * 1024u + (unsigned)(R & 1) * 512u; pos = WS_PART + (unsigned)(R * 64 + wq) * 128u;
    s1 = (unsigned)((w ^ 4) * 3072 + 1024); s3 = (unsigned)(w * 3072 + 1024);
    PINS(wx8); PINS(wx4); PINS(wgb); PINS(fzs); PINS(uo); PINS(ui); PINS(xs); PINS(wl2); PINS(os); PINS(zos); PINS(pos); PINS(s1); PINS(s3);
    SCHED();
    auto xswap = [&](int q, int e) {
#pragma unroll
      for (int d = 0; d < 2; ++d) {
        if (e == 1) h2[q][2 + d] ^= (unsigned)__builtin_amdgcn_update_dpp(0, (int)h2[q][d], 0x128, 0xf, 0x3, false);
        else h2[q][d] ^= (unsigned)__builtin_amdgcn_update_dpp(0, (int)h2[q][2 + d], 0x128, 0xf, 0xC, false);
      }
      asm volatile("" : "+v"(h2[q]));
    };
#define XSW 1
#pragma unroll
    for (int q = 0; q < 10; ++q) {
      if (q < XSW) { xswap(q, 0); xswap(q, 1); xswap(q, 2); }
      if (q == 5 && w == 1 && __builtin_amdgcn_ballot_w64(f1v == tag) != 0xfffffull) wait_lines<1, 4>(rs, f1o, tag);
      if (q == 5) { __builtin_amdgcn_s_barrier(); asm volatile("" ::: "memory"); }
      SCHED();
      accg = MFMA16(h2[q], h2[q], accg);
      SCHED();
#pragma unroll
      for (int j = 0; j < 7; ++j) {
        acc[j] = MFMA16(h2[q], bf[j], acc[j]);
        if (q == 9 && j == 1) *(f32x4*)(lds + pgo) = accg;
        if (q < 9) bf[j] = BFRAG(q + 1, j);
        if (q < 9 && q + 1 >= XSW && j >= 3 && j < 6) xswap(q + 1, j - 3);
        if (q == 1 && j == 6) f1v = ld4(rs, f1o[0]);
        if (q == 2 && j == 0) bs_raw = __builtin_amdgcn_raw_buffer_load_b16(__builtin_amdgcn_make_buffer_rsrc((void*)p.b_sp, 0, S_ * 2, 0x00020000), bso, (unsigned)R * 128u, 0);
#define KCQ 4
#define WA 8
#define WB 12
#define WQ0 2
        if (WQ0 == 2 && q >= 2 && j == 2) LDW(q - 2);
        if (WQ0 == 6 && q >= 6 && (j == 2 || j == 4)) LDW(2 * (q - 6) + (j >> 2));
        if (WQ0 == 3 && q >= 2 && q < 5 && (j & 1) && 3 * (q - 2) + (j >> 1) < WA) LDW(3 * (q - 2) + (j >> 1));
        if (WQ0 == 4 && q >= 2 && !(q & 1) && j == 2) LDW((q - 2) >> 1);
        if ((j == 0 || j == 4) && 6 + 2 * q + (j >> 2) < 10) LDX(6 + 2 * q + (j >> 2));
        if (q == KCQ && j == 0) kc_ld(1);
        if (q == 2 && j == 1 && cs1) cs_ld(csv, c * 7 + R - 25);
        if (q >= 5 && j == 5) {
          const int i = (5 + q) * 7 + (w < 7 ? w : 0);
          dma1k(rs, lds + i * 1024, l16, WS_WINF + (unsigned)c * WBLK + (unsigned)i * 1024u);
          if (q == 9) { const int i2 = 28 + (w < 7 ? w : 0); dma1k(rs, lds + i2 * 1024, l16, WS_WINF + (unsigned)c * WBLK + (unsigned)i2 * 1024u); }
        }
        SCHED();
      }
    }
    STAMP(31);
  }
  unsigned sro, swo, zo, zro, po;
  auto gelu_n = [&](f32x2* x, auto N_) {
    constexpr int N = decltype(N_)::value;
    const float c0 = -2.0f * 0.7978845608028654f * 1.4426950408889634f, c1 = c0 * 0.044715f;
    f32x2 k0 = {c0, c0}, k1 = {c1, c1}, k2 = {1.0f, 1.0f};
    asm volatile("" : "+v"(k0), "+v"(k1), "+v"(k2));
    f32x2 t[N];
#define GST(e) do { _Pragma("unroll") for (int i = 0; i < N; ++i) { t[i] = (e); asm volatile("" : "+v"(t[i])); } } while (0)
    GST(x[i] * x[i]);
    GST(__builtin_elementwise_fma(t[i], k1, k0));
    GST(t[i] * x[i]);
    GST(((f32x2){__builtin_amdgcn_exp2f(t[i][0]), __builtin_amdgcn_exp2f(t[i][1])}));
    GST(t[i] + k2);
    GST(((f32x2){__builtin_amdgcn_rcpf(t[i][0]), __builtin_amdgcn_rcpf(t[i][1])}));
#pragma unroll
    for (int i = 0; i < N; ++i) { x[i] *= t[i]; asm volatile("" : "+v"(x[i])); }
  };
  {
    f32x2 mine[7];
    f32x2 csb[7];
    if (kh1) {
#pragma unroll
      for (int j = 0; j < 7; ++j) { *(f32x2*)(lds + rdo + j * 512) = (f32x2){acc[j][0], acc[j][1]}; mine[j] = (f32x2){acc[j][2], acc[j][3]}; }
      *(f32x4*)(lds + p6o) = acc[6];
    } else {
#pragma unroll
      for (int j = 0; j < 7; ++j) { *(f32x2*)(lds + rdo + j * 512) = (f32x2){acc[j][2], acc[j][3]}; mine[j] = (f32x2){acc[j][0], acc[j][1]}; }
      *(f32x4*)(lds + p6o) = acc[6];
      consts_put(1);
      if (cs1) cs_fin(csv, c * 7 + R - 25);
#pragma unroll
      for (int k = WA; k < WB; ++k) LDW(k);
    }
    BAR();
#define BY 0
    auto b1p = [&](int k) {
      const int i = 4 * k + (w & 3) + (k < 8 ? 35 : k > 8 ? 70 : (w & 3) < 3 ? 35 : 70) - (k == 17 ? (w & 2) : 0);
      dma1k(rs, lds + i * 1024, l16, WS_WINF + (unsigned)c * WBLK + (unsigned)i * 1024u);
    };
    if (kh1) {
#pragma unroll
      for (int k = 0; k < BY; ++k) b1p(k);
#pragma unroll
      for (int k = WA; k < WB; ++k) LDW(k);
    }
    __builtin_amdgcn_s_setprio(3);
    STAMPT(9);
    f32x2 oth[7];
    f32x2 pa[3], pb[3];
#pragma unroll
    for (int e = 0; e < 3; ++e) {
      pa[e] = (f32x2){*(const float*)(lds + pso[e]), *(const float*)(lds + pqo[e])};
      pb[e] = (f32x2){*(const float*)(lds + pso[e] + 256), *(const float*)(lds + pqo[e] + 1024)};
    }
#pragma unroll
    for (int j = 0; j < 7; ++j) { oth[j] = *(const f32x2*)(lds + oto + j * 512); csb[j] = *(const f32x2*)(lds + cso + 128 * j); }
    SCHED();
    float mu2[2], rs2[2];
#pragma unroll
    for (int e = 0; e < 3; ++e) {
      const float mean = (pa[e][0] + pb[e][0]) * (1.0f / D_);
      const float var = __builtin_fmaxf((pa[e][1] + pb[e][1]) * (1.0f / D_) - mean * mean, 0.f);
      const float rstd = __builtin_amdgcn_rsqf(var + 1e-5f);
      if (e < 2) { mu2[e] = mean; rs2[e] = rstd; } else { mu_n = mean; rs_n = rstd; }
    }
    f32x2 zz[7];
    const f32x2 nmu = {-mu2[0], -mu2[1]}, rsv = {rs2[0], rs2[1]};
#pragma unroll
    for (int j = 0; j < 7; ++j)
      zz[j] = __builtin_elementwise_fma(__builtin_elementwise_fma(nmu, (f32x2){csb[j][0], csb[j][0]}, mine[j] + oth[j]), rsv, (f32x2){csb[j][1], csb[j][1]});
    gelu_n(zz, IC<7>());
    if (n16 >= 12) zz[6] = (f32x2){0.f, 0.f};
    f32x2 s1 = ((zz[0] + zz[1]) + (zz[2] + zz[3])) + ((zz[4] + zz[5]) + zz[6]);
    f32x2 s2 = zz[0] * zz[0], s3 = zz[1] * zz[1];
#pragma unroll
    for (int j = 2; j < 7; ++j) { if (j & 1) s3 = __builtin_elementwise_fma(zz[j], zz[j], s3); else s2 = __builtin_elementwise_fma(zz[j], zz[j], s2); }
    s2 += s3;
    asm("" : "+v"(s1), "+v"(s2));
    float a0 = s1[0] + dpp_f<0xB1>(s1[0]), a1 = s1[1] + dpp_f<0xB1>(s1[1]), b0 = s2[0] + dpp_f<0xB1>(s2[0]);
    a0 = (l & 1) ? a1 : a0;
    const float b1 = s2[1] + dpp_f<0xB1>(s2[1]);
    b0 = (l & 1) ? b1 : b0;
    a0 += dpp_f<0x4E>(a0); b0 += dpp_f<0x4E>(b0);
    a0 += dpp_f<0x124>(a0); b0 += dpp_f<0x124>(b0);
    a0 += dpp_f<0x128>(a0); b0 += dpp_f<0x128>(b0);
    {
      const unsigned pa0 = __builtin_amdgcn_ds_bpermute(bsrc, __float_as_uint(a0)), pb0 = __builtin_amdgcn_ds_bpermute(bsrc, __float_as_uint(b0));
      ST16S(((u32x4){tag, pa0, tag, pb0}), rs, ppo, pos + (unsigned)c * 1024u, SC1);
    }
    SCHED();
    __builtin_amdgcn_s_setprio(0);
    STAMP(10);
#define BO 14
    if (kh1) {
#pragma unroll
      for (int k = BY + BO; k < 18; ++k) b1p(k);
    } else {
#pragma unroll
      for (int k = BY; k < BY + BO; ++k) b1p(k);
    }
#pragma unroll
    for (int k = WB; k < 16; ++k) LDW(k);
#pragma unroll
    for (int j = 0; j < 7; ++j)
      *(unsigned*)(lds + L_Z + (((rt1 >> 1) * 7 + j) * 64 + l) * 16 + (4 * (rt1 & 1) + 2 * kh1) * 2) = pack2(zz[j][0], zz[j][1]);
    SCHED();
#define TR0 2
#define TR1 TR0
#define TR2 TR1
#define TR3 5
#pragma unroll
    for (int g = 0; g < TR0; ++g) WSP_TR(g);
    sro = (unsigned)(L_ST2 + h4 * 16);
    swo = (unsigned)(L_ST2 + (16 * rt1 + 4 * ((l >> 1) & 3) + 2 * kh1 + (l & 1)) * 4);
    zo = w < 7 ? (unsigned)(h4 * 7168 + n16 * 16) : 0x80000000u;
    zro = (unsigned)(L_Z + (w < 7 ? w : 0) * 1024) + l16;
    po = (unsigned)((l >> 3) * 1024 + (l & 7) * 16);
    asm volatile("" : "+v"(sro), "+v"(swo), "+v"(zo), "+v"(zro), "+v"(po));
    asm volatile("s_waitcnt vmcnt(%0)" :: "n"(16 - WB) : "memory");
    BAR();
    STAMPT(11);
  }
  unsigned z1d[7];
  constexpr int L_RED = 28672;
#define WOA(i) (((i) < 28 ? (i) : (i) < 108 ? (i) + 32 : (i) < 120 ? (i) + 37 : (i) - 92) * 1024)
#define RBAR() do { __builtin_amdgcn_s_barrier(); asm volatile("" ::: "memory"); } while (0)
#define ZAUX 16
#define NW 14
#define WOD(i0, x) dma1k(rs, lds + WOA(i0) + (x), l16, wgb + (unsigned)(i0) * 1024u + (x))
  auto wo_dma = [&](int k) {};
  u32x4 zw[NW];
#define LDZ(f) do { if (((f) >> 3) == 4) { zs[(f) & 7] += 4096u; asm volatile("" : "+s"(zs[(f) & 7])); } \
    zw[(f) % NW] = __builtin_amdgcn_raw_buffer_load_b128(rs, l16 + (unsigned)(((f) >> 3) & 3) * 1024u, zs[(f) & 7], ZAUX); } while (0)
#define WD 24
#define WE 32
#define QS 5
#define PQ 3
#define NW0 NW
#define VMW(n) asm volatile("s_waitcnt vmcnt(%0)" ::"n"(n) : "memory")
#define FQ 10
#define PP 1
#define WZ 2
#define WY 8
  static_assert(QS >= 4 && QS <= 6 && PQ >= 2 && PQ < QS && (WZ < 4 || QS >= 5), "");
  {
    f32x4 acc[7];
#pragma unroll
    for (int j = 0; j < 7; ++j) acc[j] = (f32x4){0.f, 0.f, 0.f, 0.f};
    u32x4 bf[7];
#pragma unroll
    for (int j = 0; j < 7; ++j) bf[j] = BFRAG(4, j);
    u32x4 pv, zf[2];
    unsigned fwv;
    auto ld_pv = [&]() { pv = __builtin_amdgcn_raw_buffer_load_b128(rs, po, pos, SC1); };
    auto z1 = [&](auto Q0_, auto Q1_) __attribute__((always_inline)) {
#pragma unroll
      for (int qi = decltype(Q0_)::value; qi < decltype(Q1_)::value; ++qi) {
        const int q = (qi + 4) % 10, qn = (qi + 5) % 10;
        if (qi == FQ) {
          if (kh1) VMW(WE - WD + WZ); else VMW(0);
          RBAR();
          if (w == 3) flag4(WS_FZ + (unsigned)(c * 32 + R) * 128u);
        }
        SCHED();
#pragma unroll
        for (int j = 0; j < 7; ++j) {
          acc[j] = MFMA16(bf[j], h2[q], acc[j]);
          if (qi < 9 && qi != QS - 1) bf[j] = BFRAG(qn, j);
          if (qi < 2 && (j == 1 || j == 4)) { const int i = (2 * qi + (j >> 2)) * 7 + (w < 7 ? w : 0); dma1k(rs, lds + i * 1024, l16, WS_WINF + (unsigned)c * WBLK + (unsigned)i * 1024u); }
          if (qi == 2 && j == 0) kc_ld(0);
          if (qi == 2 && j == 3) fwv = ld4(rs, fwo[0]);
          if (qi == PQ && j == 1) ld_pv();
          if (qi == QS - 1 && j < 2) zf[j] = *(const u32x4*)(lds + zro + j * 7168);
          if (!PP && qi >= QS && (j & 1) && WD + 3 * (qi - QS) + (j >> 1) < WE) LDW((WD + 3 * (qi - QS) + (j >> 1)) & 31);
          SCHED();
        }
      }
    };
    auto req1 = [&]() {
#pragma unroll
      for (int k = 16; k < WD; ++k) { LDW(k); SCHED(); }
    };
    auto req2 = [&]() {
#pragma unroll
      for (int k = WD; k < (PP ? WE : WD); ++k) { LDW(k); SCHED(); }
      sfor<0, WZ>([&](auto G_) __attribute__((always_inline)) { WOD(68 + 8 * decltype(G_)::value, wx8); SCHED(); });
    };
    if (kh1) req1();
    z1(IC<0>(), IC<QS>());
    STAMPT(29);
    f32x2 zv[2][4];
#pragma unroll
    for (int i = 0; i < 8; ++i) { zv[i >> 2][i & 3] = (f32x2){bflo(zf[i >> 2][i & 3]), bfhi(zf[i >> 2][i & 3])}; asm volatile("" : "+v"(zv[i >> 2][i & 3])); }
#pragma unroll
    for (int g = TR0; g < TR1; ++g) WSP_TR(g);
    __builtin_amdgcn_s_setprio(3);
    if (__any(ne2(pv[0], pv[2]) != 0u)) {
      _Pragma("unroll 1") for (unsigned spins = 0; spins < SPIN_CAP; ++spins) {
        ld_pv();
        if (!__any(ne2(pv[0], pv[2]) != 0u)) break;
        __builtin_amdgcn_s_sleep(1);
      }
    }
    {
      float t1 = __uint_as_float(pv[1]), t2 = __uint_as_float(pv[3]);
      t1 += dpp_f<0x128>(t1); t2 += dpp_f<0x128>(t2);
      auto x = __builtin_amdgcn_permlane16_swap(__float_as_uint(t1), __float_as_uint(t2), false, false);
      const unsigned y = __float_as_uint(__uint_as_float(x[0]) + __uint_as_float(x[1]));
      auto z = __builtin_amdgcn_permlane32_swap(y, y, false, false);
      const unsigned u = __float_as_uint(__uint_as_float(z[0]) + __uint_as_float(z[1]));
      auto v = __builtin_amdgcn_permlane16_swap(u, u, false, false);
      const float mean = __uint_as_float(v[0]) * (1.0f / FH);
      const float var = __builtin_fmaxf(__uint_as_float(v[1]) * (1.0f / FH) - mean * mean, 0.f);
      const float rstd = __builtin_amdgcn_rsqf(var + 1e-5f);
      *(float*)(lds + swo) = rstd;
      *(float*)(lds + swo + 256) = -mean * rstd;
    }
    SCHED();
    consts_put(0);
    if (w == 3 && __builtin_amdgcn_ballot_w64(fwv == tag) != 0xfffffffull) wait_lines<1, 1>(rs, fwo, tag);
    if (!kh1) req1();
    BAR();
    STAMP(12);
    {
      f32x4 sa[2][2], sb[2][2];
#pragma unroll
      for (int i = 0; i < 4; ++i) { sa[i >> 1][i & 1] = *(const f32x4*)(lds + sro + i * 64); sb[i >> 1][i & 1] = *(const f32x4*)(lds + sro + i * 64 + 256); }
      SCHED();
#pragma unroll
      for (int m = 0; m < 2; ++m) {
        u32x4 o;
#pragma unroll
        for (int e = 0; e < 4; ++e) {
          const f32x4 a = sa[m][e >> 1], b = sb[m][e >> 1];
          const f32x2 x = __builtin_elementwise_fma(zv[m][e], (f32x2){a[2 * (e & 1)], a[2 * (e & 1) + 1]}, (f32x2){b[2 * (e & 1)], b[2 * (e & 1) + 1]});
          o[e] = pack2(x[0], x[1]);
        }
        ST16S(o, rs, zo + (unsigned)m * 256u, zos, SC1);
      }
    }
    SCHED();
    __builtin_amdgcn_s_setprio(0);
    STAMPT(13);
#pragma unroll
    for (int j = 0; j < 7; ++j) bf[j] = BFRAG((QS + 4) % 10, j);
    if (kh1) req2();
    z1(IC<QS>(), IC<10>());
    f32x2 mine[7];
    if (kh1) {
#pragma unroll
      for (int j = 0; j < 7; ++j) { *(f32x2*)(lds + rdo + L_RED + j * 512) = (f32x2){acc[j][0], acc[j][1]}; mine[j] = (f32x2){acc[j][2], acc[j][3]}; }
    } else {
#pragma unroll
      for (int j = 0; j < 7; ++j) { *(f32x2*)(lds + rdo + L_RED + j * 512) = (f32x2){acc[j][2], acc[j][3]}; mine[j] = (f32x2){acc[j][0], acc[j][1]}; }
    }
    if (!kh1) req2();
#pragma unroll
    for (int g = TR1; g < TR2; ++g) WSP_TR(g);
    if (FQ > 9) VMW(WE - WD + WZ);
    BAR();
    if (FQ > 9 && w == 3) flag4(WS_FZ + (unsigned)(c * 32 + R) * 128u);
    STAMP(6);
    auto req = [&](auto G0_, auto G1_) __attribute__((always_inline)) {
#pragma unroll
      for (int k = WE; k < 32; ++k) LDW(k);
      SCHED();
      sfor<decltype(G0_)::value, decltype(G1_)::value>([&](auto G_) __attribute__((always_inline)) {
        constexpr int i0 = 4 * decltype(G_)::value;
        WOD(i0 < 68 ? i0 : i0 + 8 * WZ, wx4); SCHED();
      });
    };
    if (kh1) req(IC<0>(), IC<WY>());
    u32x4 gv[8];
    auto gb_ld = [&]() {
#pragma unroll
      for (int k = 0; k < 8; ++k) gv[k] = __builtin_amdgcn_raw_buffer_load_b128(rs, l < 40 ? l16 : 0u, WS_MODQ + (unsigned)(k * 2560 + 1920 + 80 * c) * 8u, SC1);
    };
    if (w == 2) gb_ld();
    f32x2 z[7];
    {
      f32x2 oth[7];
      f32x4 cc[7];
#pragma unroll
      for (int j = 0; j < 7; ++j) {
        oth[j] = *(const f32x2*)(lds + oto + L_RED + j * 512);
        cc[j] = *(const f32x4*)(lds + L_CONST + (16 * j + 4 * h4 + 2 * kh1) * 8);
      }
      SCHED();
#pragma unroll
      for (int j = 0; j < 7; ++j)
        z[j] = __builtin_elementwise_fma(__builtin_elementwise_fma((f32x2){-mu_n, -mu_n}, (f32x2){cc[j][0], cc[j][1]}, oth[j] + mine[j]),
                                         (f32x2){rs_n, rs_n}, (f32x2){cc[j][2], cc[j][3]});
    }
    gelu_n(z, IC<7>());
#pragma unroll
    for (int j = 0; j < 7; ++j) z1d[j] = pack2(z[j][0], z[j][1]);
    asm volatile("" : "+v"(z1d[0]), "+v"(z1d[1]), "+v"(z1d[2]), "+v"(z1d[3]), "+v"(z1d[4]), "+v"(z1d[5]), "+v"(z1d[6]));
    SCHED();
    STAMPT(30);
    if (w == 2) {
      auto bad = [&]() {
        unsigned d = 0u;
#pragma unroll
        for (int k = 0; k < 8; ++k) d |= ne2(gv[k][0], gv[k][2]);
        return __any(d != 0u);
      };
      if (bad()) {
        _Pragma("unroll 1") for (unsigned spins = 0; spins < SPIN_CAP; ++spins) {
          __builtin_amdgcn_s_sleep(8);
          gb_ld();
          if (!bad()) break;
        }
      }
      f32x2 t[4];
#pragma unroll
      for (int k = 0; k < 4; ++k) t[k] = (f32x2){__uint_as_float(gv[2 * k][1]) + __uint_as_float(gv[2 * k + 1][1]), __uint_as_float(gv[2 * k][3]) + __uint_as_float(gv[2 * k + 1][3])};
      *(f32x2*)(lds + L_MISC + l * 8) = rbf2((t[0] + t[1]) + (t[2] + t[3]));
    }
    if (!kh1) req(IC<WY>(), IC<30 - 2 * WZ>());
    const unsigned fo[1] = {l < 4 ? fzs + (unsigned)l * 128u : ~0u};
#define FPY 1
#define FPO FPY
#define FSL 1
    unsigned fv;
#pragma unroll
    for (int g = TR2; g <= TR3; ++g) {
      if (FPY == FPO) { if (g == TR2 + FPY) fv = ld4(rs, fo[0]); }
      else if (g == TR2 + FPY) { if (kh1) fv = ld4(rs, fo[0]); }
      else if (g == TR2 + FPO) { if (!kh1) fv = ld4(rs, fo[0]); }
      SCHED();
      if (g < TR3) WSP_TR(g);
      SCHED();
    }
    STAMP(14);
    if (__builtin_amdgcn_ballot_w64(fv == tag) != 15ull) wait_lines<1, FSL>(rs, fo, tag);
    STAMPT(23);
#pragma unroll
    for (int f = 0; f < NW; ++f) { LDZ(f); SCHED(); }
  }

#define WO2 15
#define TRC TR3
  {
    SCHED();
#pragma unroll
    for (int g = TRC; g < 8; ++g) WSP_TR(g);
    SCHED();
    STAMP(20);
    const int rt = rt1, kh = kh1;
    const unsigned rw = (unsigned)(L_RED + w * 512);
    unsigned rr = (unsigned)(L_RED + w * 4096 + l * 4);
    asm volatile("; m0 is set here, not in front of the first store (one wait state)" : "+v"(rr) : "{m0}"(rw));
    const float bs = bflo(bs_raw);
    unsigned long long hm = 0xffff000000000000ull;
    asm volatile("" : "+s"(hm));
    unsigned l8;
    f32x4 acc2[2][4];
    float rv[8][2];
    u32x4 ud;
    u32x4 P[2][8];
    u32x2 xp[3];
    auto ld_p = [&](int pt, int i) { P[pt][i] = __builtin_amdgcn_raw_buffer_load_b128(rs, l16 + (unsigned)pt * 1024u, ui + (unsigned)i * 2048u, SC1); };
#define RED_PUT1(j, i) asm volatile("ds_write_addtid_b32 %0 offset:%2" :: "v"(acc2[(j) & 1][((i) >> 2) & 3][(i) & 3]), "{m0}"(rw), "n"((((i) >> 2) & 3) * 4096 + (((i) >> 1) & 1) * 16384 + ((i) & 1) * 256) : "memory")
#define RED_PUT(j, r) do { RED_PUT1(j, 4 * (r)); RED_PUT1(j, 4 * (r) + 1); RED_PUT1(j, 4 * (r) + 2); RED_PUT1(j, 4 * (r) + 3); } while (0)
#define RED_GET(i) do { f32x2 x_; asm volatile("ds_read2st64_b32 %0, %1 offset0:%2 offset1:%3" : "=v"(x_) : "v"(rr), "n"(2 * (i)), "n"(2 * (i) + 1) : "memory"); rv[i][0] = x_[0]; rv[i][1] = x_[1]; } while (0)
    float zz[2], gg[2];
    unsigned cs;
    auto sum_rows_u = [&](unsigned v) {
      auto a = __builtin_amdgcn_permlane16_swap(v, v, false, false);
      v = a[0] + a[1];
      auto b = __builtin_amdgcn_permlane32_swap(v, v, false, false);
      return b[0] + b[1];
    };
    auto red_fin = [&](int j, int k, bool fuse = false) {
      const int e = k & 1, i = k >> 1;
#define VADD(d, x, y) asm volatile("v_add_f32 %0, %1, %2" : "=v"(d) : "v"(x), "v"(y))
      if (i < 4) VADD(rv[2 * i][e], rv[2 * i][e], rv[2 * i + 1][e]);
      if (i == 4 || i == 5) VADD(rv[4 * (i & 1)][e], rv[4 * (i & 1)][e], rv[4 * (i & 1) + 2][e]);
      if (i == 6) VADD(rv[0][e], rv[0][e], bs);
      if (i == 7) VADD(gg[e], rv[0][e], rv[4][e]);
      if (i == (fuse ? 9 : 8)) zz[e] = e ? bfhi(z1d[(j + 6) % 7]) : bflo(z1d[(j + 6) % 7]);
      if (i == 9) zz[e] *= gg[e];
      if (i == 9 || (i == 8 && !fuse)) asm volatile("" : "+v"(zz[e]));
      if (k == 20) { ud[j & 3] = pack2(zz[0], zz[1]); asm volatile("" : "+v"(ud[j & 3])); }
      if (k == 21) {
        if (j == 0) asm volatile("v_cndmask_b32 %0, %0, 0, %1" : "+v"(ud[0]) : "s"(hm));
        if (j == 2) asm volatile("v_add3_u32 %0, %1, %2, %3" : "=v"(cs) : "v"(ud[0]), "v"(ud[1]), "v"(ud[2]));
        if (j == 3) {
          cs = tag - sum_rows_u(cs + ud[3]);
          asm volatile("v_cndmask_b32 %0, %0, %1, %2" : "+v"(ud[0]) : "v"(cs), "s"(hm));
        }
        if (j >= 4) { ud[3] = (j == 4 ? tag : ud[3]) - ud[j & 3]; asm volatile("" : "+v"(ud[3])); }
        if (j % 3 == 0 && j) ST16S(ud, rs, l16 + (unsigned)(j / 6) * 1024u, uo, SC1);
      }
    };
    struct Sch { int b1, b2, wp[16], rp[8], vp[22], lp[8], pr[32]; };
    constexpr Sch S = {18, 28, {0, 1, 2, 3, 4, 5, 6, 7, 8, 9, 10, 11, 12, 12, 15, 15}, {19, 19, 20, 22, 23, 23, 24, 24}, {29, 29, 30, 30, 31, 31, 32, 33, 35, 36, 37, 38, 39, 40, 41, 42, 43, 46, 43, 46, 48, 48},
                       {1, 1, 1, 1, 1, 1, 1, 1}, {0, 0, 2, 1, 0, 0, 0, 0, 0, 0, 2, 2, 0, 0, 0, 0, 0, 0, 0, 1, 0, 0, 0, 1, 0, 0, 2, 1, 0, 0, 0, 0}};
    constexpr Sch F = {15, 23, {0, 1, 1, 2, 3, 4, 4, 5, 6, 7, 8, 9, 10, 11, 12, 12}, {15, 16, 16, 17, 17, 18, 18, 19}, {23, 23, 24, 24, 25, 26, 26, 27, 27, 28, 28, 29, 30, 30, 31, 31, 22, 22, 32, 33, 34, 35},
                       {1, 2, 1, 1, 1, 1, 1, 1}, {0, 2, 1, 0, 0, 1, 1, 0, 0, 2, 1, 0, 0, 2, 1, 0, 0, 2, 1, 0, 0, 2, 1, 0, 0, 2, 1, 0, 0, 2, 1, 0}};
#define VM 3
#define JF 4
#define RO 3
#define XE 1
#define CE 0
#define AP0 2
#define P1B 2
#define P1S 3
    unsigned wb[3], xo;
    const unsigned wl[3] = {(unsigned)(120 + WO3 + w) * 1024u, (unsigned)(128 + WO3 + w) * 1024u, wl2}, wg = wgb - (unsigned)(WO3 * 1024);
#define NAF 32
#define NA0 16
#define NA1 NA0
    u32x4 af[NAF];
#define LDA(m) af[(m) % NAF] = *(const u32x4*)(lds + wb[WOA(2 * (m)) >> 16] + (WOA(2 * (m)) & 65535))
    unsigned ck[2];
    const unsigned tag4 = tag * 4u;
    auto p_chk = [&](int pt, int pr, int k) {
      u32x4* q = P[pt] + 4 * pr;
      if (k < 2) ck[k & 1] = q[k & 1][0] + q[k & 1][1];
      else if (k < 8) ck[k & 1] = ck[k & 1] + q[(k & 1) + 2 * ((k >> 2) & 1)][k & 2] + q[(k & 1) + 2 * ((k >> 2) & 1)][(k & 2) + 1];
      else if (k == 8) ck[0] = pt ? ck[0] + ck[1] : sum_rows_u(ck[0] + ck[1]);
      if (k < 9) asm volatile("" : "+v"(ck[k & 1]));
      if (k == 9) {
        if (__builtin_expect(__any(ck[0] != tag4), 0)) {
          _Pragma("unroll 1") for (unsigned spins = 0; spins < SPIN_CAP; ++spins) {
            unsigned d = 0u;
#pragma unroll
            for (int i = 0; i < 4; ++i) ld_p(pt, 4 * pr + i);
#pragma unroll
            for (int i = 0; i < 4; ++i) d += q[i][0] + q[i][1] + q[i][2] + q[i][3];
            if (!pt) d = sum_rows_u(d);
            if (!__any(d != tag4)) break;
            __builtin_amdgcn_s_sleep(1);
          }
        }
      }
      if (k >= 10 && !pt) { unsigned x = q[(k - 10) & 3][0]; asm volatile("v_cndmask_b32 %0, %0, 0, %1" : "+v"(x) : "s"(hm)); q[(k - 10) & 3][0] = x; }
      if (k >= 10 && k < 13 && pt) { q[(k - 10) % 3][3] = q[3][(k - 10) % 3]; asm volatile("" : "+v"(q[(k - 10) % 3])); }
    };
    sfor<0, 224>([&](auto M_) __attribute__((always_inline)) {
      constexpr int mm = decltype(M_)::value, j = mm >> 5, m = mm & 31, y = m - (32 - 4 * RO);
      constexpr int t = j == 6 && y >= 0 ? 8 - RO + (y % (2 * RO + !RO)) / 2 : m >> 2, r = j == 6 && y >= 0 ? (y & 1) + 2 * (y / (2 * RO + !RO)) : m & 3, f = 8 * j + t;
      constexpr Sch C = j < JF ? S : F, B = j <= JF ? S : F;
      if (t == 0) asm volatile("v_mfma_f32_16x16x32_bf16 %0, %1, %2, 0" : "=&v"(acc2[j & 1][r]) : "v"(zw[f % NW]), "v"(wsp[r][t]), "v"(zw[(f | (f < 8 ? 1 : VM)) % NW]));
      else asm volatile("v_mfma_f32_16x16x32_bf16 %0, %1, %2, %0" : "+v"(acc2[j & 1][r]) : "v"(zw[f % NW]), "v"(wsp[r][t]), "v"(zw[(f | (f < 8 ? 1 : VM)) % NW]));
      SCHED();
      if (C.pr[m] == 1) __builtin_amdgcn_s_setprio(1);
      if ((m == C.b1 && j > 0) || (m == C.b2 && j > 0) || mm == 30) { asm volatile("" ::: "memory"); __builtin_amdgcn_s_waitcnt(0xC07F); RBAR(); SCHED(); }
      sfor<0, 16>([&](auto I_) __attribute__((always_inline)) {
        constexpr int i = decltype(I_)::value;
        if (j > 0 && C.wp[i] == m) { RED_PUT1(j + 1, i); SCHED(); }
        if (j > 0 && i < 8 && C.rp[i & 7] == m) { RED_GET(i & 7); SCHED(); }
      });
      if (j == 5 && m == 20) {
#pragma unroll
        for (int i = 0; i < 3; ++i) { wb[i] = (unsigned)(kh * 1024 + i * 65536) + l16; asm volatile("" : "+v"(wb[i])); }
      }
      if (XE && j == 5 && m == 13) {
        asm volatile("v_lshrrev_b32 %0, 1, %1" : "=v"(l8) : "v"(l16));
        xo = (l8 & 0x180u) + (unsigned)psi((l8 >> 3) & 15) * 8u;
        asm volatile("" : "+v"(xo));
      }
      if (RO && j == 6 && m >= 28) { RED_PUT1(6, 2 * (m - 28)); RED_PUT1(6, 2 * (m - 28) + 1); SCHED(); }
      if (CE && j == 6 && m >= 19 && m < 22) sfor<0, 3>([&](auto K_) __attribute__((always_inline)) { p_chk(0, 0, 3 * (m - 19) + decltype(K_)::value); SCHED(); });
      constexpr int ap[20] = {7, 8, 9, 10, 11, 13, 13, 14, 14, 19, 20, 21, 22, 23, 24, 25, 26, 27, 28, 29};
      sfor<0, NA0>([&](auto N_) __attribute__((always_inline)) { if (j == 6 && ap[AP0 + decltype(N_)::value] == m) { LDA(decltype(N_)::value); SCHED(); } });
      sfor<0, 22>([&](auto K_) __attribute__((always_inline)) {
        constexpr int k = decltype(K_)::value;
        if (j > 1 && B.vp[k] == m + 32) { red_fin(j > 1 ? j - 2 : 0, k, B.vp[16] == B.vp[18]); SCHED(); }
        if (j > 0 && C.vp[k] == m) { red_fin(j > 0 ? j - 1 : 0, k); SCHED(); }
      });
      if (C.pr[m] == 2) __builtin_amdgcn_s_setprio(0);
      if ((m & 3) == C.lp[m >> 2]) {
        if (f > 0 && f - 1 + NW < 56) LDZ(f - 1 + NW);
        if (j == 6 && (m >> 2) >= P1S) ld_p(0, ((m >> 2) - P1S) & 7);
        if (XE && j == 5 && m >= 20) xp[((m >> 2) - 5) % 3] = __builtin_amdgcn_raw_buffer_load_b64(rs, xo + (unsigned)((m >> 2) - 5) * 512u, xs, BULK_AUX);
        SCHED();
      }
    });
    STAMP(15);
#define NM0 0
#define OAUX 0
#define GM 19
#define MB 24
    f32x4 acc[5];
    f32x4 xf[3], gf[3];
    unsigned go, oo, eo, ho, tw, tr, o1, ta, tb;
    const unsigned s2 = (unsigned)(L_MISC + 128 * kh), s4 = s1 - (unsigned)kh * 1024u;
    unsigned long long om = kh ? ~0ull : 0xccccccccccccccccull;
#define PIN2(a, b) asm volatile("" : "+v"(a), "+v"(b))
    auto glue = [&](int k) {
      if (k == 0) { ta = l8 >> 5; tb = l16 & 48u; PIN2(ta, tb); }
      if (k == 1) { tr = __umul24(ta, 112u) + tb; oo = ta & 12u; PIN2(tr, oo); }
      if (k == 2) { tr += s1; oo += ta; PIN2(tr, oo); }
      if (k == 3) { oo = __umul24(oo, 1280u) + tb; ta = l8 >> 4; PIN2(oo, ta); }
      if (k == 4) { ta &= 24u; tb = l16 & 0xf0u; PIN2(ta, tb); }
      if (k == 5) { tw = __umul24(tb, 7u) + ta; go = (ta << 1) + s2; PIN2(tw, go); }
      if (k == 6) { tw += s1; asm volatile("v_cndmask_b32 %0, %1, -2.0, %2" : "=v"(o1) : "v"(oo), "s"(om)); asm volatile("" : "+v"(tw)); }
      if (k == 7) { eo = l16 + s3; ho = l16 + s4; PIN2(eo, ho); }
      if (k >= 8 && k < 14) {
        const unsigned x = xp[((k - 8) >> 1) % 3][k & 1];
        float lo = bflo(x), hi = bfhi(x);
        PIN2(lo, hi);
        xf[((k - 8) >> 1) % 3][2 * (k & 1)] = lo; xf[((k - 8) >> 1) % 3][2 * (k & 1) + 1] = hi;
      }
    };
    auto g3 = [&](auto M0_, auto M1_) __attribute__((always_inline)) {
      sfor<decltype(M0_)::value, decltype(M1_)::value>([&](auto M_) __attribute__((always_inline)) {
        constexpr int mi = decltype(M_)::value;
        constexpr int m = mi < 55 ? mi : mi < 64 ? 55 + 5 * ((mi - 55) / 3) + (mi - 55) % 3 : 58 + 5 * ((mi - 64) / 2) + (mi & 1), u2 = m / 5;
        const u32x4 fb = u2 < 8 ? P[0][u2 & 7] : P[1][u2 < 11 ? (u2 - 8) & 3 : (u2 - 7) & 7];
        if (m < 5) asm volatile("v_mfma_f32_16x16x32_bf16 %0, %1, %2, 0" : "=&v"(acc[m % 5]) : "v"(af[m % NAF]), "v"(fb));
        else asm volatile("v_mfma_f32_16x16x32_bf16 %0, %1, %2, %0" : "+v"(acc[m % 5]) : "v"(af[m % NAF]), "v"(fb));
        SCHED();
        if (mi == NM0) RBAR();
        if (mi >= NM0 && mi < NM0 + 3) dma1k(rs, lds + wl[(mi - NM0) % 3], l16, wg + wl[(mi - NM0) % 3]);
        if (mi >= 5 && mi < 19) p_chk(0, 1, mi - 5);
        if (mi >= GM && mi < GM + 14) glue(mi - GM);
        if (mi >= 47 && mi < 54) { p_chk(1, 1, 2 * (mi - 47)); SCHED(); p_chk(1, 1, 2 * (mi - 47) + 1); }
        if (!XE && mi == 3) { xo = (l8 & 0x180u) + (unsigned)psi((l8 >> 3) & 15) * 8u; asm volatile("" : "+v"(xo)); }
        if (!XE && mi >= 4 && mi < 7) xp[(mi - 4) % 3] = __builtin_amdgcn_raw_buffer_load_b64(rs, xo + (unsigned)(mi - 4) * 512u, xs, BULK_AUX);
        if (mi >= NM0 && mi < 40 && NA1 + mi - NM0 < 60) LDA(NA1 + mi - NM0);
        if (mi >= 66 && mi < 69) *(f32x4*)(lds + eo + (mi - 66) * 1024) = acc[(mi - 66) % 3];
        SCHED();
      });
    };
    __builtin_amdgcn_s_setprio(3);
#pragma unroll
    for (int k = 18; k < 22; ++k) red_fin(5, k);
    if (!XE) asm volatile("v_lshrrev_b32 %0, 1, %1" : "=v"(l8) : "v"(l16));
    if (RO) asm volatile("s_nop 2"); else { RED_PUT(6, 0); RED_PUT(6, 1); }
    RED_PUT(6, 2); RED_PUT(6, 3);
    red_fin(6, 16); red_fin(6, 17);
    SCHED();
#pragma unroll
    for (int i = 8 - P1S; i < 8 - P1S + P1B; ++i) ld_p(0, i);
    SCHED();
    BAR();
    sfor<0, 8>([&](auto I_) __attribute__((always_inline)) { RED_GET(decltype(I_)::value); });
    SCHED();
#pragma unroll
    for (int n = NA0; n < NA1; ++n) LDA(n);
#pragma unroll
    for (int i = 8 - P1S + P1B; i < 8; ++i) ld_p(0, i);
    SCHED();
    sfor<0, 20>([&](auto K_) __attribute__((always_inline)) {
      constexpr int k = decltype(K_)::value;
      if (k < 8 && !(k & 1)) asm volatile("s_waitcnt lgkmcnt(%0)" :: "n"(6 - k + NA1 - NA0) : "memory");
      red_fin(6, k < 16 ? k : k + 2);
    });
    SCHED();
    __builtin_amdgcn_s_setprio(0);
    STAMPT(16);
#pragma unroll
    for (int k = CE ? 9 : 0; k < 14; ++k) p_chk(0, 0, k);
    SCHED();
    g3(IC<0>(), IC<MB>());
    STAMPT(22);
    DRAIN();
    RBAR();
    STAMPT(27);
#define MY 12
    if (MY == 0 || !kh) {
#pragma unroll
      for (int i = 0; i < 8; ++i) { ld_p(1, i); SCHED(); }
    }
    g3(IC<MB>(), IC<MB + MY>());
    if (MY > 0 && kh) {
#pragma unroll
      for (int i = 0; i < 8; ++i) { ld_p(1, i); SCHED(); }
    }
    STAMPT(26);
    g3(IC<MB + MY>(), IC<40>());
    STAMPT(24);
#pragma unroll
    for (int m = NA1 + 40 - NM0; m < 70; ++m) LDA(m);
#pragma unroll
    for (int x = 0; x < 3; ++x) gf[x] = *(const f32x4*)(lds + go + x * 64);
    SCHED();
    const rsrc_t ro = __builtin_amdgcn_make_buffer_rsrc((void*)p.out, 0, S_ * D_ * 2, 0x00020000);
    STAMPT(17);
#pragma unroll
    for (int k = 0; k < 14; ++k) p_chk(1, 0, k);
    asm volatile("s_nop 1");
    STAMPT(25);
    g3(IC<40>(), IC<70>());
    STAMPT(18);
    BAR();
    STAMPT(28);
    f32x4 o4[3];
    u32x4 ov[2];
#pragma unroll
    for (int j = 2; j >= 0; --j) o4[j] = *(const volatile LAS f32x4*)(lds + ho + j * 1024);
    asm volatile("" : "+v"(acc[2]), "+v"(acc[3]), "+v"(acc[4]));
    SCHED();
#pragma unroll
    for (int j = 2; j >= 0; --j) {
      const f32x4 v = __builtin_elementwise_fma(gf[j], o4[j] + acc[2 + j], xf[j]);
      *(volatile LAS u32x2*)(lds + tw + j * 32) = (u32x2){pack2(v[0], v[1]), pack2(v[2], v[3])};
      if (j < 2) ov[j] = *(const volatile LAS u32x4*)(lds + tr + j * 32);
    }
    ST16S(ov[1], ro, oo + 32u, os, OAUX);
    ST16S(ov[0], ro, o1, os, OAUX);
    if (bid == 0 && w == 5 && l8 == 0u) st4(rs, WS_EPOCH, tag);
  }
  STAMP(19);
}

extern "C" __device__ __attribute__((used, noinline, visibility("hidden"))) void mega_tail() {}

extern "C" void kernel_launch(void* const* d_in, const int* in_sizes, int n_in, void* d_out, int out_size,
                              void* d_ws, size_t ws_size, hipStream_t stream) {
  Params p;
  p.q = (const u16*)d_in[0];
  p.k = (const u16*)d_in[1];
  p.v = (const u16*)d_in[2];
  p.shift_k = (const u16*)d_in[3];
  p.ssm_k = (const u16*)d_in[4];
  p.ssm_D = (const u16*)d_in[5];
  p.w_in = (const u16*)d_in[7];
  p.w_sp = (const u16*)d_in[8];
  p.b_sp = (const u16*)d_in[9];
  p.w_out = (const u16*)d_in[10];
  p.cvec = (const u16*)d_in[11];
  p.w_mod = (const u16*)d_in[12];
  p.out = (u16*)d_out;
  p.ws = (char*)d_ws;
  mega<<<256, 512, 0, stream>>>(p);
}
```

```cpp
#include <hip/hip_runtime.h>
#include <stdint.h>

typedef unsigned short u16;
typedef __attribute__((ext_vector_type(8))) short bf16x8;
typedef __attribute__((ext_vector_type(4))) short bf16x4;
typedef __attribute__((ext_vector_type(4))) float f32x4;
typedef __attribute__((ext_vector_type(2))) float f32x2;
typedef __attribute__((ext_vector_type(4))) unsigned int u32x4;
typedef __attribute__((ext_vector_type(2))) unsigned int u32x2;
#define LAS __attribute__((address_space(3)))
typedef __amdgpu_buffer_rsrc_t rsrc_t;
typedef __attribute__((ext_vector_type(4))) short s16x4;

constexpr int S_ = 2048, D_ = 640, F_ = 1728, FH = 864, CB = 108;

constexpr unsigned WS_EPOCH = 0;
constexpr unsigned WS_FWI = 4096;
constexpr unsigned WS_FZ = WS_FWI + 384 * 128;
constexpr unsigned WS_FWO = WS_FZ + 256 * 128;
constexpr unsigned F4S = 896 * 128;
constexpr unsigned WS_CS = WS_FZ + 256 * 128;
constexpr unsigned WS_MOD = 131072;
constexpr unsigned WS_MODP = WS_MOD + 81920;
constexpr unsigned WS_ST1 = 393216;
constexpr unsigned WS_PART = 524288;
constexpr unsigned WS_CSP = WS_PART + 262144;
constexpr unsigned WS_FX = WS_CSP + 655360;
constexpr unsigned WS_X1B = WS_FX + 16 * 4 * 160 * 128;
constexpr unsigned WS_QB = WS_X1B + 2621440;
constexpr unsigned WS_KV3 = WS_QB + 2621440;
constexpr unsigned WS_MODQ = WS_KV3 + 1310720;
constexpr unsigned WBLK = 143360;
constexpr unsigned WS_WINF = WS_MODQ + 245760;
constexpr unsigned WS_WOUTF = WS_WINF + 16 * WBLK;
constexpr unsigned WS_Z2F = WS_WOUTF + 8 * WBLK;
constexpr unsigned WS_UF = WS_Z2F + 8 * 64 * 7168;
constexpr unsigned WS_F4 = WS_UF + 32 * 4 * 8 * 4096;
constexpr unsigned WS_END = WS_F4 + 4 * F4S;
constexpr unsigned WS_STAMP = 200u << 20;

constexpr int G_COPY = 4160;
constexpr int L_U = WBLK;
constexpr int U_CH = (256 + S_) * 2 + 32;
constexpr int L_FLG = L_U + 4 * U_CH;
constexpr int L_RT = 16384;
constexpr int L_CONST = WBLK;
constexpr int L_PST = L_CONST + 2048;
constexpr int L_Z = L_CONST + 5120;
constexpr int L_ST2 = L_Z + 14336;
constexpr int L_MISC = L_ST2 + 512;
constexpr int LDS_TOTAL = 163840;

#define AUX_T 610
#define RT2_T 360
#define CB_T 380
#define W_T 700
#define YG_T 920
#define BG 2
#define BK 12
#define YW 4
#define QG 0
#define QA 4
#define QM 6
#define GE 0
#define YPR 0
#define YSL 2
#define K_T 460
#ifndef M_T
#define M_T 0
#endif
#define WA_T 580
constexpr int SC1 = 16, NT = 2;
constexpr unsigned SPIN_CAP = 1u << 16;

__device__ __forceinline__ float bflo(unsigned x) { return __uint_as_float(x << 16); }
__device__ __forceinline__ float bfhi(unsigned x) { return __uint_as_float(x & 0xffff0000u); }
__device__ __forceinline__ unsigned f2bf(float x) {
  __bf16 b = (__bf16)x;
  return (unsigned)__builtin_bit_cast(unsigned short, b);
}
__device__ __forceinline__ float rbf(float x) { return bflo(f2bf(x)); }
typedef __attribute__((ext_vector_type(2))) __bf16 bf2_t;
__device__ __forceinline__ unsigned pack2(float lo, float hi) {
  f32x2 v = {lo, hi};
  return __builtin_bit_cast(unsigned, __builtin_convertvector(v, bf2_t));
}
__device__ __forceinline__ f32x2 rbf2(f32x2 v) {
  const unsigned p = __builtin_bit_cast(unsigned, __builtin_convertvector(v, bf2_t));
  return (f32x2){__uint_as_float(p << 16), __uint_as_float(p & 0xffff0000u)};
}
__device__ __forceinline__ f32x4 rbf4(f32x4 v) {
  const f32x2 a = rbf2((f32x2){v[0], v[1]}), b = rbf2((f32x2){v[2], v[3]});
  return (f32x4){a[0], a[1], b[0], b[1]};
}
template <int CTRL>
__device__ __forceinline__ float dpp_f(float v) {
  return __int_as_float(__builtin_amdgcn_update_dpp(0, __float_as_int(v), CTRL, 0xf, 0xf, true));
}
__device__ __forceinline__ float sum8(float v) {
  v += dpp_f<0xB1>(v);
  v += dpp_f<0x4E>(v);
  v += dpp_f<0x141>(v);
  return v;
}
__device__ __forceinline__ float sum16(float v) {
  v = sum8(v);
  v += dpp_f<0x140>(v);
  return v;
}
__device__ __forceinline__ float sum_rows(float v) {
  auto a = __builtin_amdgcn_permlane16_swap(__float_as_uint(v), __float_as_uint(v), false, false);
  v = __uint_as_float(a[0]) + __uint_as_float(a[1]);
  auto b = __builtin_amdgcn_permlane32_swap(__float_as_uint(v), __float_as_uint(v), false, false);
  return __uint_as_float(b[0]) + __uint_as_float(b[1]);
}
#define MFMA16(a, b, c) __builtin_amdgcn_mfma_f32_16x16x32_bf16(__builtin_bit_cast(bf16x8, a), __builtin_bit_cast(bf16x8, b), c, 0, 0, 0)
#define MFMA_IP(c, a, b) asm volatile("v_mfma_f32_16x16x32_bf16 %0, %1, %2, %0" : "+v"(c) : "v"(a), "v"(b))
#define MFMA_0(c, a, b) asm volatile("v_mfma_f32_16x16x32_bf16 %0, %1, %2, 0" : "=&v"(c) : "v"(a), "v"(b))
#define MFMA16K16(a, b, c) __builtin_amdgcn_mfma_f32_16x16x16bf16_1k(__builtin_bit_cast(bf16x4, a), __builtin_bit_cast(bf16x4, b), c, 0, 0, 0)
__device__ __forceinline__ u32x2 trd(const char* p) {
  return __builtin_bit_cast(u32x2, __builtin_amdgcn_ds_read_tr16_b64_v4i16((LAS s16x4*)p));
}
__device__ __forceinline__ u32x4 trfrag(const char* p, int pitch) {
  u32x2 a = trd(p), b = trd(p + 16 * pitch);
  return (u32x4){a[0], a[1], b[0], b[1]};
}
#define BAR() asm volatile("s_waitcnt lgkmcnt(0)\n\ts_barrier" ::: "memory")
#define DRAIN() do { __builtin_amdgcn_s_waitcnt(0x0F70); asm volatile("" ::: "memory"); } while (0)
#define SCHED() __builtin_amdgcn_sched_barrier(0)
template <int N> struct IC { static constexpr int value = N; };
template <int I, int N, class F>
__device__ __forceinline__ void sfor(F&& f) {
  if constexpr (I < N) { f(IC<I>()); sfor<I + 1, N>(f); }
}

__device__ __forceinline__ u32x4 ld16(rsrc_t r, unsigned off) { return __builtin_amdgcn_raw_buffer_load_b128(r, off, 0, SC1); }
__device__ __forceinline__ u32x2 ld8(rsrc_t r, unsigned off) { return __builtin_amdgcn_raw_buffer_load_b64(r, off, 0, SC1); }
__device__ __forceinline__ unsigned ld4(rsrc_t r, unsigned off) { return __builtin_amdgcn_raw_buffer_load_b32(r, off, 0, SC1); }
#define ST_AUX 16
__device__ __forceinline__ void st16(rsrc_t r, unsigned off, u32x4 v) { __builtin_amdgcn_raw_buffer_store_b128(v, r, off, 0, ST_AUX); }
#define ST16S(v, r, voff, soff, aux) do { const u32x4 v_ = (v); __builtin_amdgcn_raw_buffer_store_b128(v_, r, voff, soff, aux); asm volatile("s_nop 1" :: "v"(v_)); } while (0)
__device__ __forceinline__ void st8(rsrc_t r, unsigned off, u32x2 v) { __builtin_amdgcn_raw_buffer_store_b64(v, r, off, 0, SC1); }
__device__ __forceinline__ void st_flag4(rsrc_t r, unsigned off, unsigned tag, int l) {
  if (l < 32) st16(r, WS_F4 - WS_FWI + off + (unsigned)(l >> 3) * F4S + (unsigned)(l & 7) * 16u, (u32x4){tag, tag, tag, tag});
}
__device__ __forceinline__ void st4(rsrc_t r, unsigned off, unsigned v) { __builtin_amdgcn_raw_buffer_store_b32(v, r, off, 0, SC1); }
#define BULK_AUX 16
__device__ __forceinline__ u32x4 ld16p(rsrc_t r, unsigned off) { return __builtin_amdgcn_raw_buffer_load_b128(r, off, 0, BULK_AUX); }
__device__ __forceinline__ u32x2 ld8p(rsrc_t r, unsigned off) { return __builtin_amdgcn_raw_buffer_load_b64(r, off, 0, BULK_AUX); }
__device__ __forceinline__ void dma1k(rsrc_t r, char* lds_dst, unsigned l16, unsigned soff) {
  __builtin_amdgcn_raw_ptr_buffer_load_lds(r, (LAS void*)lds_dst, 16, l16, soff, 0, BULK_AUX);
}

#define STAMP(i)
#define STAMPF(i)
#define STAMPT(i) STAMP(i)

template <int N, int SLP>
__device__ __forceinline__ void wait_lines(rsrc_t r, const unsigned (&off)[N], unsigned tag) {
  bool need[N];
#pragma unroll
  for (int i = 0; i < N; ++i) need[i] = off[i] != ~0u;
  _Pragma("unroll 1") for (unsigned spins = 0; spins < SPIN_CAP; ++spins) {
    unsigned v[N];
#pragma unroll
    for (int i = 0; i < N; ++i) {
      v[i] = tag;
      if (need[i]) v[i] = ld4(r, off[i]);
    }
    bool any = false;
#pragma unroll
    for (int i = 0; i < N; ++i) { need[i] = v[i] != tag; any = any || need[i]; }
    if (!__any(any)) break;
    __builtin_amdgcn_s_sleep(SLP);
  }
  asm volatile("" ::: "memory");
}

__device__ __forceinline__ int tau(int rt, int n) { return 32 * (rt >> 1) + 8 * (n >> 2) + 4 * (rt & 1) + (n & 3); }
__device__ __forceinline__ int psi(int n) { return 2 * (n & 7) + (n >> 3); }
#define QX_CM(d, s0, s1, P) "v_cndmask_b32_dpp " d ", " s0 ", " s1 ", vcc quad_perm:" P " row_mask:0xf bank_mask:0xf\n\t"
#define QX_BODY(M, P) \
  asm("s_mov_b32 vcc_lo, " M "\n\ts_mov_b32 vcc_hi, " M "\n\t" \
      QX_CM("%0", "%12", "%8", P) "v_xor_b32 %4, %8, %0\n\t" QX_CM("%1", "%13", "%9", P) "v_xor_b32 %5, %9, %1\n\t" \
      QX_CM("%2", "%14", "%10", P) "v_xor_b32 %6, %10, %2\n\t" QX_CM("%3", "%15", "%11", P) "v_xor_b32 %7, %11, %3" \
      : "=&v"(t[0]), "=&v"(t[1]), "=&v"(t[2]), "=&v"(t[3]), "=&v"(u[0]), "=&v"(u[1]), "=&v"(u[2]), "=&v"(u[3]) \
      : "v"(a[0]), "v"(a[1]), "v"(a[2]), "v"(a[3]), "v"(b[0]), "v"(b[1]), "v"(b[2]), "v"(b[3]) : "vcc")
template <int BIT>
__device__ __forceinline__ void quad_xchg3(u32x4& a, u32x4& b) {
  unsigned t[4], u[4];
  if (BIT == 0) QX_BODY("0x55555555", "[1,0,3,2]");
  else QX_BODY("0x33333333", "[2,3,0,1]");
  a = (u32x4){t[0], t[1], t[2], t[3]};
#pragma unroll
  for (int d = 0; d < 4; ++d) b[d] ^= (unsigned)__builtin_amdgcn_update_dpp(0, (int)u[d], BIT == 0 ? 0xB1 : 0x4E, 0xf, 0xf, true);
}
__device__ __forceinline__ void quad_transpose(u32x4& x0, u32x4& x1, u32x4& x2, u32x4& x3) {
  quad_xchg3<0>(x0, x1);
  quad_xchg3<0>(x2, x3);
  quad_xchg3<1>(x0, x2);
  quad_xchg3<1>(x1, x3);
  asm volatile("" : "+v"(x0), "+v"(x1), "+v"(x2), "+v"(x3));
}

struct Params {
  const u16 *q, *k, *v, *shift_k, *ssm_k, *ssm_D, *w_in, *w_sp, *b_sp, *w_out, *cvec, *w_mod;
  u16* out;
  char* ws;
};

__global__ __launch_bounds__(512, 2) void mega(Params p) {
  __shared__ __attribute__((aligned(16))) char lds[LDS_TOTAL];
  {
    unsigned long long pc0;
    unsigned vt;
    asm volatile("v_readfirstlane_b32 vcc_lo, %3\n\ts_cmpk_lt_u32 %2, 192\n\ts_cbranch_scc1 4f\n\ts_cmpk_lt_u32 vcc_lo, 64\n\ts_cbranch_scc0 4f\n\t"
                 "s_lshr_b32 vcc_hi, %2, 3\n\ts_sub_u32 vcc_hi, vcc_hi, 24\n\ts_lshl_b32 vcc_hi, vcc_hi, 13\n\t"
                 "v_lshlrev_b32 %1, 7, %3\n\tv_add_u32 %1, vcc_hi, %1\n\ts_getpc_b64 %0\n.Lmega_here:\n\t"
                 "v_min_u32 %1, mega_tail-.Lmega_here-4, %1\n\ts_movk_i32 m0, 0x2000\n\ts_nop 1\n\tglobal_load_lds_dword %1, %0\n4:\n\t"
                 "s_cmpk_lt_u32 %2, 160\n\ts_cbranch_scc0 1f\n\ts_cmpk_lt_u32 vcc_lo, 256\n\ts_cbranch_scc1 3f\n\t"
                 "s_call_b64 vcc, .Ltouch1\n\ts_branch 3f\n1:\n\t"
                 "s_cmpk_lt_u32 vcc_lo, 256\n\ts_cbranch_scc1 2f\n\ts_cmpk_lt_u32 %2, 224\n\ts_cbranch_scc0 2f\n\ts_call_b64 vcc, .Ltouch3\n\ts_branch 3f\n"
                 "2:\n\ts_call_b64 vcc, .Ltouch2\n3:"
                 : "=&s"(pc0), "=&v"(vt) : "s"(blockIdx.x), "v"(threadIdx.x) : "vcc", "scc", "memory");
  }
  SCHED();
  const int tid = threadIdx.x, l = tid & 63;
  const int w = __builtin_amdgcn_readfirstlane(tid >> 6);
  const int bid = blockIdx.x, xcd = bid & 7, idx = bid >> 3;
#define TOUCH_AT(n) asm volatile("s_branch 1f\n.Ltouch" #n ":\n\ts_setpc_b64 vcc\n1:")
  char* const ws = p.ws;
  asm volatile("" ::"s"(p.q), "s"(p.k), "s"(p.v), "s"(p.shift_k), "s"(p.ssm_k), "s"(p.ssm_D), "s"(p.w_in), "s"(p.w_sp), "s"(p.b_sp),
               "s"(p.w_out), "s"(p.cvec), "s"(p.w_mod), "s"(p.out), "s"(p.ws));
  unsigned epoch, tag, t_start;
  unsigned long long t64;
  asm volatile("s_load_dword %0, %2, 0x0\n\ts_memrealtime %1" : "=&s"(epoch), "=&s"(t64) : "s"(ws));
#define TAG_READY() do { SCHED(); \
    asm volatile("s_waitcnt lgkmcnt(0)\n\ts_mul_i32 %0, %2, 0x9e3779b1\n\ts_add_i32 %0, %0, 0x9e3779b9\n\ts_mov_b64 %1, %3" \
                 : "=&s"(tag), "=&s"(t64r) : "s"(epoch), "s"(t64) : "scc"); t_start = (unsigned)t64r; SCHED(); } while (0)
  unsigned long long t64r;
  const rsrc_t rs = __builtin_amdgcn_make_buffer_rsrc(ws, 0, WS_END, 0x00020000);
  const unsigned l16 = (unsigned)l * 16u;
  STAMP(0);
  const bool is_conv = idx < 20;
  const int quad = xcd * 20 + idx;
  const int R = is_conv ? (xcd & 3) * 5 + (idx >> 2) : (xcd & 3) * 3 + 15 + (idx >> 2);
  const int c = (xcd >> 2) * 4 + (idx & 3);
  const unsigned fwi = WS_F4 + (unsigned)(xcd & 3) * F4S, fz = fwi + (WS_FZ - WS_FWI);
  const int n16 = l & 15, h4 = l >> 4;
  u32x4 wsp[4][8];
#define ROTC 2
#define WAUX 0
  const int rotg = ((idx >> 4) ^ ((ROTC & 2) ? idx >> 1 : 0)) & 1, rotb = (idx >> 2) & 3, rotr = (ROTC & 1) ? (idx & 1) * 2 : 0;
  const rsrc_t rsp = __builtin_amdgcn_make_buffer_rsrc((void*)p.w_sp, 0, S_ * S_ * 2, 0x00020000);
  const unsigned wsp_vo = (unsigned)(((l >> 2) & 3) * 8 * 4096 + h4 * 64 + ((l + rotb) & 3) * 16);
#define LDW(k) LDW_(k)
#define WSP_TR(g) WSP_TR_(g)
#define WROW R
#define LDW_(k) wsp[((k) >> 2) & 3][((k) >> 4) * 4 + ((k) & 3)] = __builtin_amdgcn_raw_buffer_load_b128(rsp, wvo, \
    (unsigned)((WROW * 64 + 16 * ((((k) >> 2) & 2) ^ rotr) + 4 * (((k) >> 2) & 1) + ((k) & 3)) * 4096 + w * 512 + ((((k) >> 4) ^ rotg) * 256)), WAUX)
#define WSP_TR_(g) quad_transpose(wsp[(g) & 3][((g) >> 2) * 4], wsp[(g) & 3][((g) >> 2) * 4 + 1], wsp[(g) & 3][((g) >> 2) * 4 + 2], wsp[(g) & 3][((g) >> 2) * 4 + 3])
#define WSP_Q(g, k) wsp[(g) & 3][(((g) >> 2) & 1) * 4 + (k)]
#define WSP_TX(x) do { if (((x) & 2) == 0) quad_xchg3<0>(WSP_Q((x) >> 2, 2 * ((x) & 1)), WSP_Q((x) >> 2, 2 * ((x) & 1) + 1)); \
    else quad_xchg3<1>(WSP_Q((x) >> 2, (x) & 1), WSP_Q((x) >> 2, ((x) & 1) + 2)); \
    asm volatile("" : "+v"(WSP_Q((x) >> 2, 0)), "+v"(WSP_Q((x) >> 2, 1)), "+v"(WSP_Q((x) >> 2, 2)), "+v"(WSP_Q((x) >> 2, 3))); } while (0)
  const rsrc_t rwi = __builtin_amdgcn_make_buffer_rsrc((void*)p.w_in, 0, D_ * F_ * 2, 0x00020000);
  const rsrc_t rwo = __builtin_amdgcn_make_buffer_rsrc((void*)p.w_out, 0, FH * D_ * 2, 0x00020000);
  const rsrc_t rq = __builtin_amdgcn_make_buffer_rsrc((void*)p.q, 0, S_ * D_ * 2, 0x00020000);
  auto hold = [&](unsigned tt) { while ((unsigned)__builtin_amdgcn_s_memrealtime() - t_start < tt) __builtin_amdgcn_s_sleep(2); };
  auto ne2 = [&](unsigned x, unsigned y) { unsigned d; asm("v_bitop3_b32 %0, %1, %2, %3 bitop3:0x7e" : "=v"(d) : "v"(x), "v"(y), "s"(tag)); return d; };
  volatile LAS unsigned* lf = (volatile LAS unsigned*)(lds + L_FLG);
  auto lf_set = [&](int i) { asm volatile("s_waitcnt lgkmcnt(0)" ::: "memory"); if (l == 0) lf[i] = tag; };
  auto lf_wait = [&](int i0, int n) {
    _Pragma("unroll 1") for (unsigned spins = 0; spins < (SPIN_CAP << 4); ++spins) {
      const unsigned v = lf[i0 + (l & (n - 1))];
      if (__all(v == tag)) break;
      __builtin_amdgcn_s_sleep(1);
    }
    asm volatile("" ::: "memory");
  };
  const unsigned b2o = fwi + (unsigned)((8 + c) * 20 + 5 * (w & 3) + (l < 5 ? l : 0)) * 128u;
  auto b2_wait = [&](u32x2 fv, int slp) {
    if (!__all(fv[0] == tag)) {
      _Pragma("unroll 1") for (unsigned spins = 0; spins < SPIN_CAP; ++spins) {
        if (fv[0] != tag) fv = ld8(rs, b2o);
        if (__all(fv[0] == tag)) break;
        if (slp) __builtin_amdgcn_s_sleep(8); else __builtin_amdgcn_s_sleep(4);
      }
    }
    asm volatile("" ::: "memory");
    if (l < 5) lf[44 + 5 * w + l] = fv[1];
    if (l == 0) lf[36 + w] = tag;
  };
  auto load_b2 = [&](int k0, int k1) {
    const unsigned a0 = (unsigned)(35 * w + (idx >> 2) * 4) * 1024u, gb = WS_WINF + (unsigned)(8 + c) * WBLK;
    unsigned a = a0, g = gb + a0;
#pragma unroll
    for (int k = k0; k < k1; ++k) {
      if (k == k0 || (k & 3) == 3) {
        a = k + (idx >> 2) * 4 >= 35 ? a0 - 35840u : a0;
        g = gb + a;
        asm volatile("" : "+s"(a), "+s"(g));
      }
      dma1k(rs, lds + a + k * 1024, l16, g + (unsigned)k * 1024u);
    }
  };
  char* const wo_st = lds + 122880;
  auto wo_ld = [&]() {
    const int U = R < 28 ? R : 0, u2 = U >> 1, fi = (u2 + 1) % 3;
    const int c0 = 4 * (U & 1) + (u2 < 8 ? u2 >> 1 : u2 < 11 ? 0 : 2);
    const int b0 = u2 < 8 ? CB * c0 + 2 * (u2 & 1) + 96 : CB * (c0 + (fi >> 1)) + 48 + 2 * (fi & 1), d1 = u2 < 8 ? -96 : 16, d2 = d1 + 16;
    const int b3 = u2 < 8 ? b0 - 64 : CB * (c0 + 1) + 50 + 16 * fi;
#pragma unroll
    for (int r = 0; r < 5; ++r) {
      const int g = r * 64 + l, rho = g / 10;
      const int fb = (rho & 16) ? ((rho & 2) ? b3 : b0 + d2) : ((rho & 2) ? b0 + d1 : b0);
      __builtin_amdgcn_raw_ptr_buffer_load_lds(rwo, (LAS void*)(wo_st + r * 1024), 16, (unsigned)((fb + (rho & 13)) * (D_ * 2) + (g % 10) * 16 + c * 160), 0, 0, 0);
    }
  };
  auto rt_ld = [&](int i0, char* st) {
    const int cb = (i0 < 160 ? 8 : 0) + (i0 & 7), Q = (i0 < 160 ? i0 : i0 - 160) >> 3;
#pragma unroll
    for (int r = 0; r < 7; ++r) {
      const int g = r * 64 + l;
      __builtin_amdgcn_raw_ptr_buffer_load_lds(rwi, (LAS void*)(st + r * 1024), 16, (unsigned)((g / 14) * (F_ * 2) + (g % 14) * 16 + Q * 32 * F_ * 2 + cb * CB * 2),
                                               0, 0, NT);
    }
  };
  auto rt_job = [&](int i0, char* st) {
    const int cb = (i0 < 160 ? 8 : 0) + (i0 & 7), Q = (i0 < 160 ? i0 : i0 - 160) >> 3;
    const int col = 640 * (l >> 5) + 32 * Q + (l & 31);
    DRAIN();
    u32x4 x[7], o[7];
    f32x2 xu[7][4];
    {
      const char* bp = st + (4 * h4 + (n16 >> 2)) * 224 + (n16 & 3) * 8;
#pragma unroll
      for (int j = 0; j < 7; ++j) x[j] = trfrag(bp + j * 32, 224);
#pragma unroll
      for (int j = 0; j < 7; ++j)
#pragma unroll
        for (int e = 0; e < 4; ++e) { xu[j][e] = (f32x2){bflo(x[j][e]), bfhi(x[j][e])}; asm volatile("" : "+v"(xu[j][e])); }
    }
    unsigned mk[4][2], pv = (unsigned)col * 8u, cw = l >= 32 ? (unsigned)(7168 + (l - 32) * 4) : (unsigned)(7298 + l * 2 - 4), m12 = n16 == 12 ? ~0u : 0u;
    unsigned fgo = l < 32 ? (unsigned)(l >> 3) * F4S + (unsigned)(l & 7) * 16u : 0x80000000u;
#pragma unroll
    for (int i = 0; i < 4; ++i) {
      mk[i][0] = n16 == 4 * i ? 0x3F803F80u : 0u; mk[i][1] = n16 == 4 * i + 1 ? ~0u : 0u;
      asm volatile("" : "+v"(mk[i][0]), "+v"(mk[i][1]));
    }
    asm volatile("" : "+v"(pv), "+v"(cw), "+v"(m12), "+v"(fgo));
    unsigned ob = WS_WINF + (unsigned)cb * WBLK + (unsigned)(Q * 7) * 1024u;
    asm volatile("" : "+s"(ob));
    u32x2 pp8[8];
    hold(CB_T);
    _Pragma("unroll 1") for (unsigned spins = 0; spins < SPIN_CAP; ++spins) {
#pragma unroll
      for (int i = 0; i < 8; ++i) pp8[i] = __builtin_amdgcn_raw_buffer_load_b64(rs, pv, WS_MODP + (unsigned)i * 10240u, SC1);
      if (!__any(((ne2(pp8[0][0], pp8[1][0]) | ne2(pp8[2][0], pp8[3][0]) | ne2(pp8[4][0], pp8[5][0])) | ne2(pp8[6][0], pp8[7][0])) != 0u)) break;
      __builtin_amdgcn_s_sleep(4);
    }
    STAMPF(9);
    __builtin_amdgcn_s_setprio(3);
    {
      auto pf = [&](int i) { return __uint_as_float(pp8[i][1]); };
      const float t = rbf(((pf(0) + pf(1)) + (pf(2) + pf(3))) + ((pf(4) + pf(5)) + (pf(6) + pf(7))));
      if (l >= 32) *(float*)(st + cw) = rbf(1.0f + t);
      else *(u16*)(st + cw + 2) = (u16)(__float_as_uint(t) >> 16);
    }
    asm volatile("" ::: "memory");
    const f32x4 ca = *(const volatile LAS f32x4*)(st + 7168 + h4 * 16), cc = *(const volatile LAS f32x4*)(st + 7168 + 64 + h4 * 16);
    const u32x2 e0 = *(const volatile LAS u32x2*)(st + 7296 + h4 * 8), e1 = *(const volatile LAS u32x2*)(st + 7328 + h4 * 8);
    SCHED();
#pragma unroll
    for (int j = 0; j < 7; ++j) {
      const f32x2 y0 = xu[j][0] * (f32x2){ca[0], ca[1]}, y1 = xu[j][1] * (f32x2){ca[2], ca[3]}, y2 = xu[j][2] * (f32x2){cc[0], cc[1]}, y3 = xu[j][3] * (f32x2){cc[2], cc[3]};
      o[j] = (u32x4){pack2(y0[0], y0[1]), pack2(y1[0], y1[1]), pack2(y2[0], y2[1]), pack2(y3[0], y3[1])};
      if (j == 6) {
#pragma unroll
        for (int e = 0; e < 4; ++e) o[j][e] = (o[j][e] & ~m12) | mk[3][0];
      }
      ST16S(o[j], rs, l16 + (unsigned)(j & 3) * 1024u, ob + (unsigned)(j >> 2) * 4096u, SC1);
      SCHED();
    }
    __builtin_amdgcn_s_setprio(0);
    STAMPF(11);
    f32x4 d[2] = {{0.f, 0.f, 0.f, 0.f}, {0.f, 0.f, 0.f, 0.f}};
#pragma unroll
    for (int j = 0; j < 7; ++j) {
      const unsigned one = mk[j & 3][0], sr = mk[j & 3][1];
      d[j >> 2] = MFMA16(((u32x4){one, one, one, one}), o[j], d[j >> 2]);
      d[j >> 2] = MFMA16(((u32x4){sr & e0[0], sr & e0[1], sr & e1[0], sr & e1[1]}), x[j], d[j >> 2]);
    }
#pragma unroll
    for (int m = 0; m < 2; ++m)
      st16(rs, WS_CSP + (unsigned)((cb * 20 + Q) * 2 + m) * 1024u + l16, (u32x4){tag, __float_as_uint(d[m][0]), tag, __float_as_uint(d[m][1])});
    STAMPF(13);
    u32x4 tg4 = {tag, t_start, tag, tag};
    asm volatile("s_waitcnt vmcnt(2)" : "+v"(tg4) :: "memory");
    ST16S(tg4, rs, fgo, WS_F4 + (unsigned)(cb * 20 + Q) * 128u, SC1);
  };

  auto red8 = [&](float v) { v += dpp_f<0x128>(v); return sum_rows(v); };
  const rsrc_t rwm = __builtin_amdgcn_make_buffer_rsrc((void*)p.w_mod, 0, D_ * 3840 * 2, 0x00020000);
  auto gemv_load = [&](u32x4* pre, int blk, int sl, int np) {
    pre[10][0] = *(const unsigned*)(p.cvec + 8 * np * sl + 2 * (l < 4 * np ? l : 0));
#pragma unroll
    for (int pp = 0; pp < np; ++pp)
      pre[pp] = __builtin_amdgcn_raw_buffer_load_b128(rwm, (unsigned)((l >> 3) * 7680 + (l & 7) * 16), (unsigned)((8 * np * sl + pp * 8) * 7680 + blk * 128), NT);
  };
  auto gemv_fin = [&](float* acc, const u32x4* pre, int np, char* scr) {
    const unsigned cv2 = pre[10][0];
    unsigned* svt = (unsigned*)scr + w * 96;
#pragma unroll
    for (int e = 0; e < 2; ++e) {
      const int row = 2 * l + e;
      const float cv = e ? bfhi(cv2) : bflo(cv2);
      if (l < 4 * np) svt[(row & 7) * 12 + (row >> 3)] = __float_as_uint(rbf(cv * __builtin_amdgcn_rcpf(1.0f + __expf(-cv))));
    }
    asm volatile("s_waitcnt lgkmcnt(0)" ::: "memory");
    u32x4 sv[3];
#pragma unroll
    for (int i = 0; i < (np + 3) / 4; ++i) sv[i] = *(const u32x4*)(svt + (l >> 3) * 12 + 4 * i);
    SCHED();
#pragma unroll
    for (int e = 0; e < 8; ++e) acc[e] = 0.f;
#pragma unroll
    for (int pp = 0; pp < np; ++pp) {
      const unsigned sh = sv[pp >> 2][pp & 3], sl2 = sh >> 16;
#pragma unroll
      for (int e = 0; e < 4; ++e) {
        const unsigned x = pre[pp][e];
        acc[2 * e] = __builtin_amdgcn_fdot2_f32_bf16(__builtin_bit_cast(bf2_t, x), __builtin_bit_cast(bf2_t, sl2), acc[2 * e], false);
        acc[2 * e + 1] = __builtin_amdgcn_fdot2_f32_bf16(__builtin_bit_cast(bf2_t, x), __builtin_bit_cast(bf2_t, sh), acc[2 * e + 1], false);
      }
    }
#pragma unroll
    for (int e = 0; e < 8; ++e) acc[e] = red8(acc[e]);
  };
  auto gemv_put = [&](const float* acc, unsigned off) {
    float v = acc[0];
#pragma unroll
    for (int e = 1; e < 8; ++e) { v = (l >> 3) == e ? acc[e] : v; asm("" : "+v"(v)); }
    st8(rs, off + (unsigned)l * 8u, (u32x2){tag, (unsigned)__builtin_amdgcn_ds_bpermute(((l & 7) * 8 + (l >> 3)) * 4, (int)__float_as_uint(v))});
  };
  auto cs_ld = [&](u32x4* v, int cj) {
    const int cb = cj / 7, j = cj % 7;
#pragma unroll
    for (int k = 0; k < 5; ++k)
      v[k] = __builtin_amdgcn_raw_buffer_load_b128(rs, (unsigned)(h4 * 10240 + n16 * 16), WS_CSP + (unsigned)((cb * 40 + (j >> 2) + 2 * k) * 1024 + (j & 3) * 256), SC1);
  };
  auto cs_fin = [&](u32x4* v, int cj) {
    auto bad = [&]() { return __any(((ne2(v[0][0], v[0][2]) | ne2(v[1][0], v[1][2]) | ne2(v[2][0], v[2][2])) | ne2(v[3][0], v[3][2]) | ne2(v[4][0], v[4][2])) != 0u); };
    if (bad()) {
      _Pragma("unroll 1") for (unsigned spins = 0; spins < SPIN_CAP; ++spins) {
        __builtin_amdgcn_s_sleep(8);
        cs_ld(v, cj);
        if (!bad()) break;
      }
    }
    float t0 = 0.f, t1 = 0.f;
#pragma unroll
    for (int k = 0; k < 5; ++k) { t0 += __uint_as_float(v[k][1]); t1 += __uint_as_float(v[k][3]); }
    t0 = sum_rows(t0); t1 = sum_rows(t1);
    if (l < 16) st16(rs, WS_CS + (unsigned)((cj / 7) * 2048 + (cj % 7) * 256) + l16, (u32x4){tag, __float_as_uint(t0), tag, __float_as_uint(t1)});
  };
  const bool cs2 = w == 2 && R >= 20 && R < 27, cs1 = w == 2 && R >= 25;
  u32x4 csv[5];
  typedef const __attribute__((address_space(4))) unsigned* cptr_t;

  if (__builtin_expect(is_conv, 1)) {
    if (w >= 4) {
      TOUCH_AT(1);
      const int pw = w - 4;
      const rsrc_t ra = __builtin_amdgcn_make_buffer_rsrc((void*)((idx & 2) ? p.v : p.k), 0, S_ * D_ * 2, 0x00020000);
      const rsrc_t rb = __builtin_amdgcn_make_buffer_rsrc((void*)((idx & 2) ? p.k : p.v), 0, S_ * D_ * 2, 0x00020000);
      const rsrc_t rk = __builtin_amdgcn_make_buffer_rsrc((void*)p.k, 0, S_ * D_ * 2, 0x00020000);
      const rsrc_t rv = __builtin_amdgcn_make_buffer_rsrc((void*)p.v, 0, S_ * D_ * 2, 0x00020000);
#define NROT 2
      const unsigned vo = (unsigned)((128 * pw + 2 * l) * (D_ * 2));
      const unsigned so0 = (unsigned)((idx & 1) * (D_ * 2) + quad * 8), so1 = (unsigned)((1 - (idx & 1)) * (D_ * 2) + quad * 8);
      u32x2 g[3][4], kh;
      auto gl = [&](int s, int j0, int j1) {
        __builtin_amdgcn_s_setprio(0);
#pragma unroll
        for (int j = j0; j < j1; ++j) {
          if (s < NROT) g[s][j] = __builtin_amdgcn_raw_buffer_load_b64(j < 2 ? ra : rb, vo, ((j & 1) ? so1 : so0) + (unsigned)(s * 512 * D_ * 2), 0);
          else g[s][j] = __builtin_amdgcn_raw_buffer_load_b64(j < 2 ? rk : rv, vo + (unsigned)((j & 1) * D_ * 2), (unsigned)(quad * 8 + s * 512 * D_ * 2), 0);
          SCHED();
        }
        __builtin_amdgcn_s_setprio(3);
      };
      const cptr_t sp = (cptr_t)(p.shift_k + quad * 16);
      unsigned spv[8];
#pragma unroll
      for (int i = 0; i < 8; ++i) spv[i] = sp[i];
      __builtin_amdgcn_s_sleep(3);
      gl(0, 0, 4);
      {
        const int row = 512 * (l >> 2) + 128 * pw - 3 + (l & 3);
        kh = __builtin_amdgcn_raw_buffer_load_b64(rk, (l >= 12 || (l & 3) == 3 || row < 0) ? 0x80000000u : (unsigned)(row * D_ * 2 + quad * 8), 0, 0);
      }
      TAG_READY();
      f32x2 wt[2][4];
#pragma unroll
      for (int d = 0; d < 2; ++d) {
        wt[d][0] = (f32x2){bflo(spv[4 * d]), bflo(spv[4 * d + 2])}; wt[d][1] = (f32x2){bfhi(spv[4 * d]), bfhi(spv[4 * d + 2])};
        wt[d][2] = (f32x2){bflo(spv[4 * d + 1]), bflo(spv[4 * d + 3])}; wt[d][3] = (f32x2){bfhi(spv[4 * d + 1]), bfhi(spv[4 * d + 3])};
        asm volatile("" : "+s"(wt[d][0]), "+s"(wt[d][1]), "+s"(wt[d][2]), "+s"(wt[d][3]));
      }
      SCHED();
      {
        const int j = idx * 4 + pw;
        const unsigned o = (unsigned)((512 + 64 * (j >> 2) + l) * (D_ * 2) + (((160 * xcd) >> 7) + ((j >> 1) & 1)) * 128);
        __builtin_amdgcn_raw_ptr_buffer_load_lds((j & 1) ? ra : rb, (LAS void*)(lds + 98304), 4, j < 64 ? o : 0x80000000u, 0, 0, 0);
      }
      SCHED();
      gl(1, 0, 4);
      STAMP(21);
      unsigned up = (unsigned)(L_U + 512 + 256 * pw + (l >> 2) * 16 + (3 - (l & 3)) * 4);
      unsigned lfo = l == 0 ? (unsigned)(L_FLG + 4 * pw) : (unsigned)(100352 + tid * 4), lfr = (unsigned)(L_FLG + (l & 3) * 4);
      unsigned yr = (unsigned)(L_U + (n16 >> 2) * U_CH + (128 * pw + 32 * h4 + 8 * (n16 & 1) + 4 * ((n16 >> 1) & 1)) * 2);
      unsigned yo = (unsigned)(8 * pw + (l >> 3)) * 20480u + (unsigned)quad * 128u + (unsigned)(l & 7) * 16u;
      unsigned mo = (unsigned)((n16 < 12 ? n16 >> 2 : 0) * D_ + quad * 4 + (l & 3)) * 8u + (unsigned)h4 * 40960u;
      unsigned k3o = (unsigned)(16 * pw + (l >> 2)) * 20480u + (unsigned)(((l >> 1) & 1) * 10240 + (2 * quad + (l & 1)) * 16);
      unsigned k3d = (unsigned)(L_U + (2 * (l & 1) + ((l >> 1) & 1)) * U_CH + 512 + (1536 + 128 * pw + 8 * (l >> 2)) * 2);
      unsigned xfo = (unsigned)(((4 * (l >> 5) + pw) * 4 + ((l >> 3) & 3)) * 160 + quad) * 128u + (unsigned)(l & 7) * 16u;
      u32x4 tg4 = {tag, tag, tag, tag};
      asm volatile("" : "+v"(up), "+v"(lfo), "+v"(lfr), "+v"(yr), "+v"(yo), "+v"(mo), "+v"(k3o), "+v"(k3d), "+v"(xfo), "+v"(tg4));
      unsigned so = WS_ST1 + (unsigned)xcd * 16384u + (unsigned)pw * 1024u;
      asm volatile("" : "+s"(so));
      auto pf_set = [&](int i) { asm volatile("" ::: "memory"); *(volatile LAS unsigned*)(lds + lfo + 4 * i) = tg4[0]; };
      auto pf_wait = [&](int i0) {
        _Pragma("unroll 1") for (unsigned spins = 0; spins < (SPIN_CAP << 4); ++spins) {
          if (__all(*(volatile LAS unsigned*)(lds + lfr + 4 * i0) == tag)) break;
          __builtin_amdgcn_s_sleep(1);
        }
        asm volatile("" ::: "memory");
      };
      unsigned hl[2][3];
      auto halo = [&](int s) {
#pragma unroll
        for (int i = 0; i < 6; ++i) { hl[i & 1][i >> 1] = (unsigned)__builtin_amdgcn_readlane(kh[i & 1], 4 * s + (i >> 1)); asm volatile("" : "+v"(hl[i & 1][i >> 1])); }
      };
      auto kv = [&](auto S__, auto N__) {
        constexpr int s = decltype(S__)::value;
        u32x2* x = g[s];
        SCHED();
        asm volatile("s_waitcnt vmcnt(%5)" : "+v"(x[0]), "+v"(x[1]), "+v"(x[2]), "+v"(x[3]), "+v"(kh) : "n"(decltype(N__)::value));
        if (s == 2) STAMP(27);
        if (s == 0) halo(0);
        if (s < NROT) {
          if (idx & 1) { asm volatile(""); const u32x2 t = x[0], u = x[2]; x[0] = x[1]; x[1] = t; x[2] = x[3]; x[3] = u; }
          asm volatile("" : "+v"(x[0]), "+v"(x[1]), "+v"(x[2]), "+v"(x[3]));
          if (idx & 2) { asm volatile(""); const u32x2 t = x[0], u = x[1]; x[0] = x[2]; x[1] = x[3]; x[2] = t; x[3] = u; }
          asm volatile("" : "+v"(x[0]), "+v"(x[1]), "+v"(x[2]), "+v"(x[3]));
        }
        f32x2 kf[2][5], o[2][2];
#pragma unroll
        for (int d = 0; d < 2; ++d) {
          unsigned kk[5];
          kk[3] = x[0][d]; kk[4] = x[1][d];
          kk[2] = __builtin_amdgcn_update_dpp(hl[d][2], kk[4], 0x138, 0xf, 0xf, false);
          kk[1] = __builtin_amdgcn_update_dpp(hl[d][1], kk[3], 0x138, 0xf, 0xf, false);
          kk[0] = __builtin_amdgcn_update_dpp(hl[d][0], kk[2], 0x138, 0xf, 0xf, false);
#pragma unroll
          for (int j = 0; j < 5; ++j) kf[d][j] = (f32x2){bflo(kk[j]), bfhi(kk[j])};
        }
#pragma unroll
        for (int t = 0; t < 5; ++t) {
#pragma unroll
          for (int i = 0; i < 4; ++i) {
            const int d = i >> 1, r = i & 1;
            if (t == 4) o[d][r] *= (f32x2){bflo(x[2 + r][d]), bfhi(x[2 + r][d])};
            else if (t == 0) o[d][r] = kf[d][r + 3] * wt[d][0];
            else o[d][r] = __builtin_elementwise_fma(kf[d][r + 3 - t], wt[d][t], o[d][r]);
          }
          asm volatile("" : "+v"(o[0][0]), "+v"(o[0][1]), "+v"(o[1][0]), "+v"(o[1][1]));
        }
#pragma unroll
        for (int i = 0; i < 4; ++i) *(unsigned*)(lds + up + i * U_CH + s * 1024) = pack2(o[i >> 1][1][i & 1], o[i >> 1][0][i & 1]);
        pf_set(4 * s);
        SCHED();
        if (s < 2) halo(s + 1);
        SCHED();
      };
      u32x4 sv[4], qv[4];
      u32x2 mv[2];
      auto ld_e = [&]() {
#pragma unroll
        for (int s = 0; s < 4; ++s) sv[s] = __builtin_amdgcn_raw_buffer_load_b128(rs, l16, so + (unsigned)s * 4096u, SC1);
        SCHED();
      };
      auto s_bad = [&]() { return __any(((ne2(sv[0][1], sv[0][3]) | ne2(sv[1][1], sv[1][3]) | ne2(sv[2][1], sv[2][3])) | ne2(sv[3][1], sv[3][3])) != 0u); };
      unsigned qo = (unsigned)((128 * pw + 32 * h4 + 4 * ((l >> 3) & 1) + 8 * ((l >> 2) & 1) + (l & 3)) * (D_ * 2));
      asm volatile("" : "+v"(qo));
      auto glq = [&](int j0, int j1) {
        __builtin_amdgcn_s_setprio(0);
#pragma unroll
        for (int j = j0; j < j1; ++j) {
          const u32x2 x = __builtin_amdgcn_raw_buffer_load_b64(rq, qo, (unsigned)(quad * 8 + (512 * (j >> 1) + 16 * (j & 1)) * D_ * 2), 0);
          qv[j >> 1][2 * (j & 1)] = x[0]; qv[j >> 1][2 * (j & 1) + 1] = x[1];
          SCHED();
        }
        __builtin_amdgcn_s_setprio(3);
      };
      kv(IC<0>(), IC<5>());
      STAMP(23);
      if (QG) {
        const int j = idx * 4 + pw;
        const unsigned o = (unsigned)((64 * (j >> 1) + l) * (D_ * 2) + (((160 * xcd) >> 7) + (j & 1)) * 128);
        __builtin_amdgcn_raw_ptr_buffer_load_lds(rq, (LAS void*)(lds + 98304), 4, j < 64 ? o : 0x80000000u, 0, 0, 0);
        SCHED();
      }
#define G2A 4
      gl(2, 0, G2A);
      kv(IC<1>(), IC<G2A + QG>());
      gl(2, G2A, 4);
      STAMPF(9);
      u32x4 k3[2];
      auto ld_k3 = [&]() {
        k3[0] = __builtin_amdgcn_raw_buffer_load_b128(rs, k3o, WS_KV3, SC1);
        k3[1] = __builtin_amdgcn_raw_buffer_load_b128(rs, k3o, WS_KV3 + 5120u, SC1);
      };
      auto ld_m = [&]() {
        mv[0] = __builtin_amdgcn_raw_buffer_load_b64(rs, mo, WS_MODQ, SC1);
        mv[1] = __builtin_amdgcn_raw_buffer_load_b64(rs, mo, WS_MODQ + 20480u, SC1);
      };
      auto ld_q = [&]() {
        asm volatile("" ::: "memory");
        if (!M_T) ld_m();
        SCHED();
#pragma unroll
        for (int s = 0; s < 4; ++s) qv[s] = __builtin_amdgcn_raw_buffer_load_b128(rs, yo, WS_QB + (unsigned)s * (32u * 20480u), BULK_AUX);
        SCHED();
      };
      auto k_bad = [&]() { return __any((ne2(k3[0][1], k3[0][3]) | ne2(k3[1][1], k3[1][3])) != 0u); };
      auto k_put = [&]() {
        *(u32x4*)(lds + k3d) = (u32x4){k3[0][0], k3[0][2], k3[1][0], k3[1][2]};
        pf_set(12);
        SCHED();
      };
#define KE_T 0
      const bool early = (unsigned)__builtin_amdgcn_s_memrealtime() - t_start < KE_T;
      if (!early) {
        hold(K_T);
        ld_k3();
        SCHED();
        ld_e();
      } else {
        DRAIN();
      }
      kv(IC<2>(), IC<6>());
      if (KE_T && early) {
        hold(K_T);
        ld_k3();
        SCHED();
        ld_e();
      }
      STAMPF(11);
      const bool kb = k_bad();
      if (!kb) k_put();
      const bool sb = s_bad();
      if (kb) ld_k3();
      if (sb) ld_e(); else ld_q();
      if (kb) {
        _Pragma("unroll 1") for (unsigned spins = 0; spins < SPIN_CAP; ++spins) {
          if (!k_bad()) break;
          __builtin_amdgcn_s_sleep(4);
          ld_k3();
        }
        k_put();
      }
      STAMPF(18);
      if (sb) {
        _Pragma("unroll 1") for (unsigned spins = 0; spins < SPIN_CAP; ++spins) {
          if (!s_bad()) break;
          __builtin_amdgcn_s_sleep(4);
          ld_e();
        }
        ld_q();
      }
      STAMPF(17);
      if (M_T) { hold(M_T); ld_m(); SCHED(); }
      STAMP(2);
      u32x2 yv[4][2];
      auto ld_y = [&](int s) {
#pragma unroll
        for (int e = 0; e < 2; ++e) yv[s][e] = trd(lds + yr + 1024 * s + 32 * e);
      };
      float r1[4][2], m1[4][2];
#pragma unroll
      for (int i = 0; i < 8; ++i) {
        const unsigned x = sv[i >> 1][2 * (i & 1)];
        r1[i >> 1][i & 1] = __builtin_amdgcn_rcpf(bfhi(x));
        m1[i >> 1][i & 1] = -bflo(x) * r1[i >> 1][i & 1];
        asm volatile("" : "+v"(r1[i >> 1][i & 1]), "+v"(m1[i >> 1][i & 1]));
      }
      pf_wait(20);
      ld_y(0); ld_y(1);
      SCHED();
      if (__any(ne2(mv[0][0], mv[1][0]) != 0u)) {
        _Pragma("unroll 1") for (unsigned spins = 0; spins < SPIN_CAP; ++spins) {
          __builtin_amdgcn_s_sleep(4);
          ld_m();
          if (!__any(ne2(mv[0][0], mv[1][0]) != 0u)) break;
        }
      }
      STAMPF(13);
      f32x4 al, be;
      {
        const float mgf = rbf(sum_rows(__uint_as_float(mv[0][1]) + __uint_as_float(mv[1][1])));
        auto rl = [&](int i) { return __uint_as_float(__builtin_amdgcn_readlane(__float_as_uint(mgf), i)); };
        const f32x4 sa = {rl(0), rl(1), rl(2), rl(3)}, ca = {rl(4), rl(5), rl(6), rl(7)}, ga = {rl(8), rl(9), rl(10), rl(11)};
        al = rbf4(ca + 1.0f) * ga;
        be = sa * ga;
        asm volatile("" : "+v"(al), "+v"(be));
      }
      f32x4 qf[2][2], P[2][2];
      auto xpre = [&](int s) {
#pragma unroll
        for (int e = 0; e < 2; ++e) {
          const float r = r1[s][e], m = m1[s][e];
          const f32x4 qq = {bflo(qv[s][2 * e]), bfhi(qv[s][2 * e]), bflo(qv[s][2 * e + 1]), bfhi(qv[s][2 * e + 1])};
          qf[s & 1][e] = qq;
          P[s & 1][e] = __builtin_elementwise_fma(__builtin_elementwise_fma(qq, (f32x4){r, r, r, r}, (f32x4){m, m, m, m}), al, be);
        }
        asm volatile("" : "+v"(qf[s & 1][0]), "+v"(qf[s & 1][1]), "+v"(P[s & 1][0]), "+v"(P[s & 1][1]));
      };
      auto xfin = [&](int s) {
        u32x4 o;
#pragma unroll
        for (int e = 0; e < 2; ++e) {
          const u32x2 y = yv[s][e];
          const f32x4 yf = {bflo(y[0]), bfhi(y[0]), bflo(y[1]), bfhi(y[1])};
          const f32x4 x = __builtin_elementwise_fma(P[s & 1][e], yf, qf[s & 1][e]);
          o[2 * e] = pack2(x[0], x[1]);
          o[2 * e + 1] = pack2(x[2], x[3]);
        }
        ST16S(o, rs, yo, WS_X1B + (unsigned)s * (32u * 20480u), SC1);
      };
      xpre(0); xfin(0);
      xpre(1); xfin(1);
      SCHED();
      xpre(2); xpre(3);
#define FVR 5
#define MSPLIT 0
      if (MSPLIT) {
        pf_wait(24);
        ld_y(2);
        xfin(2);
        SCHED();
        pf_wait(28);
        ld_y(3);
        xfin(3);
      } else {
        pf_wait(28);
        ld_y(2); ld_y(3);
        xfin(2); xfin(3);
      }
      STAMP(28);
      if (BG == 1 && l == 0) lf[40 + pw] = tag;
      asm volatile("s_waitcnt vmcnt(2)" ::: "memory");
      ST16S(tg4, rs, xfo, WS_FX, SC1);
      SCHED();
      asm volatile("s_waitcnt vmcnt(1)" ::: "memory");
      ST16S(tg4, rs, xfo, WS_FX + 32u * 160u * 128u, SC1);
      if (BG == 2 && l == 0) lf[40 + pw] = tag;
      STAMP(5);
    } else {
      const int ch = w;
      char* U = lds + L_U + ch * U_CH;
      char* G = lds + ch * (4 * G_COPY);
      {
        const rsrc_t rf = __builtin_amdgcn_make_buffer_rsrc((void*)(p.ssm_k + (size_t)(quad * 4 + ch) * S_), 0, S_ * 2, 0x00020000);
        u32x4 fa[4];
        u32x2 fb[4];
        __builtin_amdgcn_s_setprio(3);
#pragma unroll
        for (int k = 0; k < 4; ++k) {
          fa[k] = __builtin_amdgcn_raw_buffer_load_b128(rf, l16 + (unsigned)k * 1024u, 0, NT);
        }
        __builtin_amdgcn_s_setprio(1);
        TAG_READY();
#pragma unroll
        for (int k = 0; k < 4; ++k) {
#pragma unroll
          for (int d = 0; d < 2; ++d)
            fb[k][d] = __builtin_amdgcn_update_dpp(k < 3 ? (unsigned)__builtin_amdgcn_readfirstlane(fa[k < 3 ? k + 1 : k][d]) : 0u, fa[k][d], 0x130, 0xf, 0xf, false);
          const unsigned x[6] = {fa[k][0], fa[k][1], fa[k][2], fa[k][3], fb[k][0], fb[k][1]};
          unsigned y[5];
#pragma unroll
          for (int d = 0; d < 5; ++d) y[d] = __builtin_amdgcn_alignbit(x[d + 1], x[d], 16);
          char* gp = G + 32 + k * 1024 + l16;
          *(u32x4*)(gp) = fa[k];
          *(u32x4*)(gp + G_COPY) = (u32x4){y[0], y[1], y[2], y[3]};
          *(u32x4*)(gp + 2 * G_COPY) = (u32x4){x[1], x[2], x[3], x[4]};
          *(u32x4*)(gp + 3 * G_COPY) = (u32x4){y[1], y[2], y[3], y[4]};
          if (k == 0) {
            const unsigned f0 = __builtin_amdgcn_readfirstlane(x[0]), f1 = __builtin_amdgcn_readfirstlane(x[1]);
            const unsigned long long f64 = (unsigned long long)f0 | ((unsigned long long)f1 << 32);
            const unsigned long long hi = l ? (f64 << (64 - 16 * (l & 3))) : 0ull;
            if (l < 4) {
              *(u32x4*)(G + l * G_COPY) = (u32x4){0u, 0u, 0u, 0u};
              *(u32x4*)(G + l * G_COPY + 16) = (u32x4){0u, 0u, (unsigned)hi, (unsigned)(hi >> 32)};
            }
            if (l >= 32) *(u32x4*)(U + (l - 32) * 16) = (u32x4){0u, 0u, 0u, 0u};
          }
        }
        asm volatile("s_waitcnt lgkmcnt(0)" ::: "memory");
        STAMP(1);
      }
      const int a = n16, h = h4, jj = h >> 1, e2 = h & 1;
      const char* ap = G + ((a + 1) & 3) * G_COPY + 2 * (16 * jj + a - 8 * e2 + 9 - ((a + 1) & 3));
      unsigned bp = (unsigned)(L_U + ch * U_CH + 32 * (a - jj) + 16 * e2);
      auto lda = [&](u32x4* A, int r, int p0, int np) {
#pragma unroll
        for (int pp = 0; pp < np; ++pp) {
          u32x2 lo = *(const volatile LAS u32x2*)(ap + 512 * (p0 + pp) + 64 * r);
          u32x2 hi = *(const volatile LAS u32x2*)(ap + 512 * (p0 + pp) + 64 * r + 8);
          A[pp] = (u32x4){lo[0], lo[1], hi[0], hi[1]};
        }
      };
      u32x4 AD;
      {
        const unsigned dDr = (*(cptr_t)(p.ssm_D + quad * 4 + (ch & 2)) >> (16 * (ch & 1))) & 0xffffu;
        const unsigned dv = h == (a >> 3) ? ((a & 1) ? dDr : dDr << 16) : 0u;
        const int dw = (7 - (a & 7)) >> 1;
#pragma unroll
        for (int d = 0; d < 4; ++d) AD[d] = dw == d ? dv : 0u;
      }
      u32x4 A0[8][4];
      f32x4 acc[8];
#pragma unroll
      for (int n = 0; n < 8; ++n) acc[n] = (f32x4){0.f, 0.f, 0.f, 0.f};
#pragma unroll
      for (int r = 0; r < 8; ++r) lda(A0[r], r, 0, 4);
      auto yput = [&](int s) {
#pragma unroll
        for (int e = 0; e < 2; ++e)
          *(u32x2*)(U + 1024 * s + 512 * e + (16 * a + 4 * h) * 2) = (u32x2){pack2(acc[2 * s + e][0], acc[2 * s + e][1]), pack2(acc[2 * s + e][2], acc[2 * s + e][3])};
        asm volatile("" ::: "memory");
        if (l == 0) lf[16 + 4 * s + ch] = tag;
      };
      asm volatile("" : "+v"(AD), "+v"(bp), "+v"(acc[0]), "+v"(acc[1]), "+v"(acc[2]), "+v"(acc[3]), "+v"(acc[4]), "+v"(acc[5]), "+v"(acc[6]), "+v"(acc[7]));
      auto stage = [&](auto S__) {
        constexpr int s = decltype(S__)::value, NH = 4 - 2 * s, NP = 8 - 2 * s;
        u32x2 A1[3][NH][2];
        u32x4 B[3][2];
        auto lda1 = [&](int r, int i) { A1[r % 3][i >> 1][i & 1] = *(const volatile LAS u32x2*)(ap + 512 * (4 + (i >> 1)) + 64 * r + 8 * (i & 1)); };
        auto ldb = [&](int r, int e) { B[r % 3][e] = *(const u32x4*)(lds + bp + 512 * (2 * s + e + 1) - 64 * r); };
#pragma unroll
        for (int i = 0; i < 4 * NH; ++i) lda1(i / (2 * NH), i % (2 * NH));
        SCHED();
        if (s > 0) yput(s - 1);
        lf_wait(4 * s, 4);
        if (s == 0) STAMP(3);
#pragma unroll
        for (int i = 0; i < 4; ++i) ldb(i >> 1, i & 1);
        SCHED();
#pragma unroll
        for (int e = 0; e < 2; ++e) MFMA_IP(acc[2 * s + e], AD, B[0][e]);
        sfor<0, 8 * (2 * NP - 1)>([&](auto I_) __attribute__((always_inline)) {
          constexpr int r = decltype(I_)::value / (2 * NP - 1), i = decltype(I_)::value % (2 * NP - 1), e = i >= NP, pp = i - e * NP, h1 = pp < 4 ? 0 : pp - 4;
          const u32x4 a1 = {A1[r % 3][h1][0][0], A1[r % 3][h1][0][1], A1[r % 3][h1][1][0], A1[r % 3][h1][1][1]};
          const u32x4 fa = pp < 4 ? A0[r][pp & 3] : a1;
          MFMA_IP(acc[2 * s + e + pp], fa, B[r % 3][e]);
          SCHED();
          if (r + 2 < 8 && i < 2) ldb(r + 2, i & 1);
          if (r + 2 < 8 && i >= 2 && i < 2 + 2 * NH) lda1(r + 2, (i - 2) % (2 * NH));
          SCHED();
        });
      };
      stage(IC<0>());
      STAMP(4);
      stage(IC<1>());
      STAMPF(9);
      u32x2 fv;
      if (MSPLIT) {
        u32x4 B[8][2];
        auto ldb = [&](int r, int m0) {
#pragma unroll
          for (int m = 0; m < 2; ++m) B[r][m] = *(const u32x4*)(lds + bp + 512 * (m0 + m + 1) - 64 * r);
        };
        asm volatile("s_nop 1" : "+v"(acc[2]), "+v"(acc[3]));
        yput(1);
        if (w == 3) wo_ld();
        __builtin_amdgcn_s_setprio(3);
        lf_wait(8, 4);
        STAMP(22);
        auto ldb1 = [&](int i, int m0) { B[i >> 1][i & 1] = *(const u32x4*)(lds + bp + 512 * (m0 + (i & 1) + 1) - 64 * (i >> 1)); };
#pragma unroll
        for (int r = 0; r < 3; ++r) ldb(r, 4);
        SCHED();
#pragma unroll
        for (int m = 0; m < 2; ++m) MFMA_IP(acc[4 + m], AD, B[0][m]);
        sfor<0, 24>([&](auto I_) __attribute__((always_inline)) {
          constexpr int i = decltype(I_)::value, r = i / 3, k = i % 3;
          MFMA_IP(acc[4 + (k != 1)], A0[r][k == 0], B[r][k == 2]);
          SCHED();
          if (i < 10) ldb1(6 + i, 4);
          SCHED();
        });
        sfor<0, 32>([&](auto I_) __attribute__((always_inline)) {
          constexpr int i = decltype(I_)::value, r = i >> 2, k = i & 3;
          MFMA_IP(acc[6 + (k & 1)], A0[r][2 + (k & 1) - (k >> 1)], B[r][k >> 1]);
          SCHED();
          if (i == 7) yput(2);
          if (i == 4 * FVR) fv = ld8(rs, b2o);
          if (i == 19) lf_wait(12, 4);
          if (i >= 19 && i < 29) ldb1(i - 19, 6);
          SCHED();
        });
#pragma unroll
        for (int m = 0; m < 2; ++m) MFMA_IP(acc[6 + m], AD, B[0][m]);
        sfor<0, 24>([&](auto I_) __attribute__((always_inline)) {
          constexpr int i = decltype(I_)::value, r = i / 3, k = i % 3;
          MFMA_IP(acc[6 + (k != 1)], A0[r][k == 0], B[r][k == 2]);
          SCHED();
          if (i < 6) ldb1(10 + i, 6);
          SCHED();
        });
        asm volatile("s_nop 7" : "+v"(acc[6]), "+v"(acc[7]));
        yput(3);
      } else {
        u32x4 B[3][4];
        auto ldb = [&](int r, int m) { B[r % 3][m] = *(const u32x4*)(lds + bp + 512 * (5 + m) - 64 * r); };
        asm volatile("s_nop 1" : "+v"(acc[2]), "+v"(acc[3]));
        yput(1);
        if (w == 3) wo_ld();
        __builtin_amdgcn_s_setprio(3);
        lf_wait(8, 8);
        STAMP(22);
#pragma unroll
        for (int i = 0; i < 8; ++i) ldb(i >> 2, i & 3);
        SCHED();
#pragma unroll
        for (int m = 0; m < 4; ++m) MFMA_IP(acc[4 + m], AD, B[0][m]);
        sfor<0, 80>([&](auto I_) __attribute__((always_inline)) {
          constexpr int r = decltype(I_)::value / 10, i = decltype(I_)::value % 10, m = i < 4 ? 0 : i < 7 ? 1 : i < 9 ? 2 : 3, pp = i - (m * (9 - m)) / 2;
          MFMA_IP(acc[4 + m + pp], A0[r][pp], B[r % 3][m]);
          SCHED();
          if (r + 2 < 8 && i < 4) ldb(r + 2, i & 3);
          if (r == 1 && i == 9) fv = ld8(rs, b2o);
          SCHED();
        });
#pragma unroll
        for (int e = 0; e < 4; ++e) {
          if (e == 1) asm volatile("s_nop 4" : "+v"(acc[5]), "+v"(acc[6]), "+v"(acc[7]));
          *(u32x2*)(U + 2048 + 512 * e + (16 * a + 4 * h) * 2) = (u32x2){pack2(acc[4 + e][0], acc[4 + e][1]), pack2(acc[4 + e][2], acc[4 + e][3])};
          SCHED();
        }
        asm volatile("" ::: "memory");
        if (l == 0) lf[28 + ch] = tag;
      }
      STAMP(28);
      __builtin_amdgcn_s_setprio(0);
      b2_wait(fv, 0);
      lf_wait(20, 4);
      DRAIN();
    }
    __builtin_amdgcn_s_setprio(0);
  } else {
    const int at = idx - 20;
    char* const scr = lds + WBLK + 14848;
    auto gv_blk = [&](int j) { return j < 160 ? 30 + (j >> 3) : j < 400 ? (j >> 3) - 20 : j >> 3; };
    auto gv_fin = [&](const u32x4* pre, int j) {
      float acc[8];
      gemv_fin(acc, pre, 10, scr);
      gemv_put(acc, j < 160 ? WS_MODP + (unsigned)((j & 7) * 1280 + (j >> 3) * 64) * 8u : WS_MODQ + (unsigned)((j & 7) * 2560 + ((j >> 3) - 20) * 64) * 8u);
    };
    const int ja = xcd * 12 + at, ai = xcd * 8 + at, bi = xcd * 4 + at - 8, g = w >> 2, wj = w & 3;
    const int rj = w == 0 ? ja : w == 1 ? (at >= 4 ? 92 + ai : -1) : w == 4 ? 160 + ja : w == 5 && at < 8 ? 256 + ai : -1;
    char* const rst = lds + (g ? WBLK : 65536) + wj * 7424;
    u32x4 pre[11];
    if (at >= 8 || g == 0) {
      TOUCH_AT(2);
      const int T = at >= 8 ? 64 + 2 * bi + g : ai;
      unsigned* mds = (unsigned*)(lds + 2048) + 16 * g;
      char* qt = lds + L_RT + g * (16 * 1312);
      u32x4 ld[5];
      const bool e1 = at >= 8 && w == 3;
      if (e1) gemv_load(pre, gv_blk(128 + bi), bi & 7, 10);
      else __builtin_amdgcn_s_sleep(8);
#pragma unroll
      for (int j = 0; j < 5; ++j)
        ld[j] = __builtin_amdgcn_raw_buffer_load_b128(rq, (unsigned)(h4 * D_ * 2 + n16 * 16 + j * 256), (unsigned)((32 * (T >> 1) + 8 * wj + 4 * (T & 1)) * D_ * 2), QG ? 0 : NT);
      TAG_READY();
      {
        const bf2_t ONE2 = {(__bf16)1.0f, (__bf16)1.0f};
        char* dst = qt + (4 * wj + h4) * 1312 + n16 * 16;
        float sa = 0.f, va = 0.f;
#pragma unroll
        for (int j = 0; j < 5; ++j) {
          if (!QG) *(u32x4*)(dst + j * 256) = ld[j];
#pragma unroll
          for (int d = 0; d < 4; ++d) {
            const unsigned a0 = ld[j][d];
            const bf2_t xa = __builtin_bit_cast(bf2_t, a0);
            sa = __builtin_amdgcn_fdot2_f32_bf16(xa, ONE2, sa, false); va = __builtin_amdgcn_fdot2_f32_bf16(xa, xa, va, false);
          }
        }
        if (!QG) lf_set(8 + w);
        const float ma = sum16(sa) * (1.0f / D_);
        va = __builtin_fmaxf(sum16(va) * (1.0f / D_) - ma * ma, 0.f);
        if (n16 == 0) mds[4 * wj + h4] = pack2(ma, __builtin_amdgcn_sqrtf(rbf(rbf(va) + 1e-5f)));
      }
      if (QG) lf_set(16 + w);
      if (e1) gv_fin(pre, 128 + bi);
      if (!QG) lf_wait(8 + 4 * g, 4);
      STAMP(23);
      if (QG) {
        if (wj == 2) {
          lf_wait(16 + 4 * g, 4);
          st16(rs, WS_ST1 + (unsigned)(l >> 3) * 16384u + (unsigned)T * 128u + (unsigned)(l & 7) * 16u, (u32x4){mds[l & 7], tag, mds[8 + (l & 7)], tag});
          STAMP(22);
        }
      } else {
        const char* src = qt + (l & 7) * 1312 + (40 * wj + (l >> 3)) * 8;
#pragma unroll
        for (int j = 0; j < 5; ++j) {
          const u32x2 x = *(const u32x2*)(src + j * 64), y = *(const u32x2*)(src + j * 64 + 8 * 1312);
          st16(rs, WS_QB + (unsigned)T * 20480u + (unsigned)(5 * wj + j) * 1024u + l16, (u32x4){x[0], x[1], y[0], y[1]});
        }
      }
      SCHED();
      const int g2 = at < 8 || w < 3 || w == 4 ? 400 : w == 3 ? 384 + bi : 256 + wj * 32 + bi;
      if (g2 < 400) {
        gemv_load(pre, gv_blk(g2), g2 & 7, 10);
        if (!QG) asm volatile("s_waitcnt vmcnt(10)" ::: "memory");
      } else if (w < 2 && rj >= 0) {
        rt_ld(rj, rst);
        if (!QG) asm volatile("s_waitcnt vmcnt(7)" ::: "memory");
      } else if (!QG) {
        DRAIN();
      }
      if (!QG) lf_set(16 + w);
      if (!QG && wj == 2) {
        lf_wait(16 + 4 * g, 4);
        st16(rs, WS_ST1 + (unsigned)(l >> 3) * 16384u + (unsigned)T * 128u + (unsigned)(l & 7) * 16u, (u32x4){mds[l & 7], tag, mds[8 + (l & 7)], tag});
        STAMP(22);
      }
      if (w >= 4 && w < 7) lf_set(w);
      if (g2 < 400) gv_fin(pre, g2);
      STAMP(25);
    } else {
      constexpr int KB = L_RT + 16 * 1312;
      TOUCH_AT(3);
      const int g1 = w < 6 ? 2 * ai + wj : 32 + wj * 64 + ai;
      if (w >= 6) {
        const rsrc_t rk = __builtin_amdgcn_make_buffer_rsrc((void*)(w == 6 ? p.k : p.v), 0, S_ * D_ * 2, 0x00020000);
        const unsigned ko = (unsigned)((1536 + 8 * ai) * (D_ * 2) - (w == 6 ? 4096 : 0));
        char* dst = lds + KB + (w == 6 ? 0 : 14336);
        __builtin_amdgcn_s_sleep(3);
#pragma unroll
        for (int n = 0; n < 10; ++n) __builtin_amdgcn_raw_ptr_buffer_load_lds(rk, (LAS void*)(dst + n * 1024), 16, l16, ko + (unsigned)n * 1024u, 0, NT);
        if (w == 6) {
#pragma unroll
          for (int n = 10; n < 14; ++n) __builtin_amdgcn_raw_ptr_buffer_load_lds(rk, (LAS void*)(dst + n * 1024), 16, l16, ko + (unsigned)n * 1024u, 0, NT);
        }
      }
      SCHED();
      u32x4 swt[2];
      swt[0] = *(const u32x4*)(p.shift_k + (tid - 256) * 8);
      swt[1] = *(const u32x4*)(p.shift_k + (256 + l) * 8);
      SCHED();
      if (w >= 6) __builtin_amdgcn_s_sleep(8);
      gemv_load(pre, gv_blk(g1), g1 & 7, 10);
      TAG_READY();
      asm volatile("s_waitcnt vmcnt(11)" : "+v"(swt[0]), "+v"(swt[1]) :: "memory");
      lf_set(8 + w);
      if (w < 6) gv_fin(pre, g1);
      STAMP(25);
      lf_wait(12, 4);
      STAMP(23);
#pragma unroll 1
      for (int n = 0; n < (w == 7 ? 2 : 1); ++n) {
        {
          const int pr = n ? 256 + l : tid - 256;
          const char* kp = lds + KB + 4096 + pr * 4;
          unsigned kk[11], vv[8];
#pragma unroll
          for (int i = 0; i < 11; ++i) kk[i] = *(const unsigned*)(kp + (i - 3) * 1280);
#pragma unroll
          for (int i = 0; i < 8; ++i) vv[i] = *(const unsigned*)(kp + 10240 + i * 1280);
          const u32x4 sw = n ? swt[1] : swt[0];
          const f32x2 wt[4] = {{bflo(sw[0]), bflo(sw[2])}, {bfhi(sw[0]), bfhi(sw[2])}, {bflo(sw[1]), bflo(sw[3])}, {bfhi(sw[1]), bfhi(sw[3])}};
          f32x2 o[8];
#pragma unroll
          for (int i = 0; i < 8; ++i) {
            f32x2 t = (f32x2){bflo(kk[i + 3]), bfhi(kk[i + 3])} * wt[0];
#pragma unroll
            for (int j = 1; j < 4; ++j) t = __builtin_elementwise_fma((f32x2){bflo(kk[i + 3 - j]), bfhi(kk[i + 3 - j])}, wt[j], t);
            o[i] = t * (f32x2){bflo(vv[i]), bfhi(vv[i])};
          }
#pragma unroll
          for (int e = 0; e < 4; ++e) {
            const int i = 7 - 4 * (e & 1);
            st16(rs, WS_KV3 + (unsigned)ai * 20480u + (unsigned)(e * 5120 + pr * 16),
                 (u32x4){pack2(o[i][e >> 1], o[i - 1][e >> 1]), tag, pack2(o[i - 2][e >> 1], o[i - 3][e >> 1]), tag});
          }
        }
      }
      STAMP(22);
      if (w < 7) lf_set(w);
      if (w >= 6) gv_fin(pre, g1);
      STAMPF(30);
    }
    if (w == 6 && ja < 80) {
      hold(AUX_T);
      gemv_load(pre, gv_blk(400 + ja), ja & 7, 10);
      gv_fin(pre, 400 + ja);
    }
    if (rj >= 0) {
      if (w >= 2) { hold(RT2_T); rt_ld(rj, rst); }
      rt_job(rj, rst);
    }
    STAMP(26);
    lf_set(w);
    if (w == 3) { hold(WA_T); wo_ld(); }
    STAMP(3);
    if (w < 4) {
      hold(W_T);
      b2_wait((u32x2){~tag, 0u}, 1);
      lf_wait(0, 8);
      STAMP(4);
    }
    DRAIN();
  }

  STAMPF(16);
  u32x4 wox[5];
  if (w == 3) {
    const char* bp = wo_st + (4 * h4 + (n16 >> 2)) * 160 + (n16 & 3) * 8;
#pragma unroll
    for (int dj = 0; dj < 5; ++dj) wox[dj] = trfrag(bp + dj * 32, 160);
    asm volatile("s_waitcnt lgkmcnt(0)" : "+v"(wox[0]), "+v"(wox[1]), "+v"(wox[2]), "+v"(wox[3]), "+v"(wox[4]) :: "memory");
  }
  auto wo_put = [&]() {
    if (w == 3 && R < 28) {
      const unsigned base = WS_WOUTF + (unsigned)c * WBLK + (unsigned)((R >> 1) * 10240 + (R & 1) * 1024);
#pragma unroll
      for (int dj = 0; dj < 5; ++dj) st16(rs, base + (unsigned)((R & 1) ? dj : (dj + 2) % 5) * 2048u + l16, wox[dj]);
    }
  };
  if (cs2) {
    cs_ld(csv, (8 + c) * 7 + R - 20);
    SCHED();
    load_b2(0, 12);
    SCHED();
    cs_fin(csv, (8 + c) * 7 + R - 20);
  } else if (w < 4) {
    asm volatile("" ::: "memory");
    load_b2(0, 12);
  }
  const unsigned xfb = WS_FX + (unsigned)(((R >> 1) * 4 + (R & 1) * 2 + (c >> 2)) * 160 + l) * 128u;
  unsigned xf3[3];
  if (w == 7) {
    lf_wait(36, 4);
    int dt = (int)(lf[44 + ((l & 31) < 20 ? l & 31 : 0)] - t_start);
    dt = l < 32 ? dt : -dt;
    dt = max(dt, __builtin_amdgcn_update_dpp(0, dt, 0xB1, 0xf, 0xf, true));
    dt = max(dt, __builtin_amdgcn_update_dpp(0, dt, 0x4E, 0xf, 0xf, true));
    dt = max(dt, __builtin_amdgcn_update_dpp(0, dt, 0x124, 0xf, 0xf, true));
    dt = max(dt, __builtin_amdgcn_update_dpp(0, dt, 0x128, 0xf, 0xf, true));
    const int d1 = min(max(max(__builtin_amdgcn_readlane(dt, 0), __builtin_amdgcn_readlane(dt, 16)), 0), 300);
    const int d0 = min(max(max(__builtin_amdgcn_readlane(dt, 32), __builtin_amdgcn_readlane(dt, 48)), 0), 300);
    dt = ((d1 + d0) * YW >> 3) - d0;
    __builtin_amdgcn_s_setprio(YPR);
    STAMPF(24);
    hold((unsigned)(YG_T + dt));
    STAMPF(30);
    xf3[0] = ld4(rs, xfb); xf3[1] = ld4(rs, xfb + 8192u); xf3[2] = ld4(rs, l < 32 ? xfb + 16384u : ~0u);
    __builtin_amdgcn_s_setprio(0);
  }
  SCHED();
  u32x4 k_c;
  unsigned kc_o = w < 2 ? (unsigned)(tid < 112 ? tid : 0) * 16u : 0x80000000u, kc_d = (unsigned)(L_CONST + (tid & 127) * 8);
  auto kc_ld = [&](int z2) { k_c = __builtin_amdgcn_raw_buffer_load_b128(rs, kc_o, WS_CS + (unsigned)(8 * z2 + c) * 2048u, SC1); };
  auto consts_put = [&](int z2) {
    if (w < 2) {
      if (__any(ne2(k_c[0], k_c[2]) != 0u)) {
        _Pragma("unroll 1") for (unsigned spins = 0; spins < SPIN_CAP; ++spins) {
          __builtin_amdgcn_s_sleep(8);
          kc_ld(z2);
          if (!__any(ne2(k_c[0], k_c[2]) != 0u)) break;
        }
      }
      if (z2) *(u32x2*)(lds + kc_d + 1024) = (u32x2){k_c[1], k_c[3]};
      else { const unsigned d = kc_d - ((kc_d >> 1) & 4u); *(unsigned*)(lds + d) = k_c[1]; *(unsigned*)(lds + d + 8) = k_c[3]; }
    }
  };
  const int wq = w ^ rotr, rt1 = wq & 3, kh1 = w >> 2;
  unsigned bo0 = (unsigned)(kh1 * 71680) + l16, bo1 = bo0 + 35840u;
  asm volatile("" : "+v"(bo0), "+v"(bo1));
  unsigned pso[3], pqo[3], oto = (unsigned)(((w ^ 4) * 7 * 64 + l) * 8), rdo = (unsigned)((w * 7 * 64 + l) * 8), cso = (unsigned)(L_CONST + (128 + n16) * 8);
#pragma unroll
  for (int e = 0; e < 3; ++e) {
    const int row = rt1 * 16 + (e < 2 ? 4 * h4 + 2 * kh1 + e : n16);
    pso[e] = (unsigned)(L_PST + row * 4); pqo[e] = (unsigned)(L_PST + 512 + row * 16 + (row & 3) * 4);
  }
  unsigned spo = (unsigned)(L_Z + tid * 16), p6o = (unsigned)(L_PST + (wq * 16 + 4 * h4) * 4), pgo = (unsigned)(L_PST + 512 + (wq * 16 + n16) * 16);
  asm volatile("" : "+v"(spo), "+v"(p6o), "+v"(pgo));
  p6o = n16 == 12 ? p6o : spo;
  pgo = h4 == (n16 >> 2) ? pgo : spo;
  unsigned bsrc = (unsigned)(((l >> 1) & 3) * 64 + (l & 1) * 4), ppo = l < 8 ? l16 : 0x80000000u;
  asm volatile("" : "+v"(pso[0]), "+v"(pso[2]), "+v"(pqo[0]), "+v"(pqo[2]), "+v"(oto), "+v"(rdo), "+v"(cso), "+v"(p6o), "+v"(pgo), "+v"(bsrc), "+v"(ppo), "+v"(kc_o), "+v"(kc_d));
  pso[1] = pso[0] + 4u; pqo[1] = pqo[0] + 20u;
#define BFRAG(q, j) (*(const u32x4*)(lds + ((q) < 5 ? bo0 : bo1) + ((q) % 5) * 7168 + (j) * 1024))
  u32x4 h2[10];
  unsigned xv = (unsigned)((4 * ((l >> 3) & 1) + h4) * 128 + (l & 7) * 16), xs0 = WS_X1B + (unsigned)(R * 4 + rt1) * 20480u + (unsigned)(10 * kh1) * 1024u;
  unsigned wvo = wsp_vo, bso = (unsigned)tau(rt1, n16) * 2u, fgo = l < 32 ? (unsigned)(l >> 3) * F4S + (unsigned)(l & 7) * 16u : 0x80000000u;
  asm volatile("" : "+v"(xv), "+v"(wvo), "+v"(bso), "+v"(fgo));
#define WO3 (-92)
#define PINS(x) do { x = (unsigned)__builtin_amdgcn_readfirstlane((int)x); asm volatile("" : "+s"(x)); } while (0)
  PINS(xs0);
  unsigned zs[8], wx8, wx4, wgb, fzs, uo, ui, xs, wl2, os, zos, pos, s1, s3;
#define LDX(q) h2[q] = __builtin_amdgcn_raw_buffer_load_b128(rs, xv + (unsigned)((q) & 3) * 1024u, xs0 + (unsigned)((q) >> 2) * 4096u, BULK_AUX)
  auto flag4 = [&](unsigned off) { ST16S(((u32x4){tag, tag, tag, tag}), rs, fgo, WS_F4 - WS_FWI + off, SC1); };
  unsigned bs_raw, fwo[1];
  f32x4 acc[7];
  float mu_n, rs_n;
  {
    unsigned f1o[1] = {w == 1 && l < 20 ? fwi + (unsigned)(c * 20 + l) * 128u : ~0u}, f1v;
    fwo[0] = w == 3 && l < 28 ? fwi + (WS_FWO - WS_FWI) + (unsigned)(c * 32 + l) * 128u : ~0u;
    asm volatile("" : "+v"(f1o[0]), "+v"(fwo[0]));
    auto late_scalars = [&]() {
#pragma unroll
    for (int t = 0; t < 8; ++t) { zs[t] = WS_Z2F + (unsigned)(c * 64 + 8 * w + 4 * ((t >> 2) ^ rotg) + ((t + rotb) & 3)) * 7168u; PINS(zs[t]); }
    wx8 = (unsigned)((w + (idx >> 2)) & 7) * 1024u; wx4 = wx8 & 3072u; wgb = WS_WOUTF + (unsigned)c * WBLK; fzs = fz + (unsigned)(c * 32 + 4 * w) * 128u;
    uo = WS_UF + (unsigned)((R * 4 + rt1) * 8 + c) * 4096u + (unsigned)kh1 * 2048u; ui = WS_UF + (unsigned)((R * 4 + rt1) * 8 + 4 * kh1) * 4096u;
    xs = WS_X1B + (unsigned)(R * 4 + rt1) * 20480u + (unsigned)(5 * c + 2 * kh1) * 512u; wl2 = (unsigned)(120 + WO3 + (w < 4 ? w + 16 : w)) * 1024u;
    os = (unsigned)(((R * 64 + tau(rt1, 0)) * D_ + 80 * c + 32 * kh1) * 2);
    zos = WS_Z2F + (unsigned)((c * 64 + 4 * (R >> 1)) * 7 + (w < 6 ? w + 1 : 0)) * 1024u + (unsigned)(R & 1) * 512u; pos = WS_PART + (unsigned)(R * 64 + wq) * 128u;
    s1 = (unsigned)((w ^ 4) * 3072 + 1024); s3 = (unsigned)(w * 3072 + 1024);
    PINS(wx8); PINS(wx4); PINS(wgb); PINS(fzs); PINS(uo); PINS(ui); PINS(xs); PINS(wl2); PINS(os); PINS(zos); PINS(pos); PINS(s1); PINS(s3);
    SCHED();
    };
    if (GE) late_scalars();
  if (w < 4) {
    load_b2(12, BK);
    if (BG && is_conv) lf_wait(40, 4);
    load_b2(BK, 35);
  }
  wo_put();
  STAMPF(29);
    if (w == 7) {
      asm volatile("" : "+v"(xf3[0]), "+v"(xf3[1]), "+v"(xf3[2]));
      if (__any(ne2(xf3[0], xf3[1]) != 0u) || __builtin_amdgcn_ballot_w64(xf3[2] == tag) != 0xffffffffull) {
        const unsigned fo[3] = {xf3[0] != tag ? xfb : ~0u, xf3[1] != tag ? xfb + 8192u : ~0u, l < 32 && xf3[2] != tag ? xfb + 16384u : ~0u};
        wait_lines<3, YSL>(rs, fo, tag);
      }
      if (l == 0) lf[33] = tag;
    }
    lf_wait(33, 1);
    STAMP(7);
    SCHED();
#pragma unroll
    for (int q = 0; q < 3; ++q) LDX(q);
    SCHED();
    __builtin_amdgcn_s_waitcnt(0x0F73);
    BAR();
    STAMP(8);
#pragma unroll
    for (int j = 0; j < 7; ++j) acc[j] = (f32x4){0.f, 0.f, 0.f, 0.f};
    f32x4 accg = {0.f, 0.f, 0.f, 0.f};
    u32x4 bf[7];
#pragma unroll
    for (int j = 0; j < 7; ++j) bf[j] = BFRAG(0, j);
    SCHED();
#pragma unroll
    for (int q = 3; q < 6; ++q) LDX(q);
    if (w == 3) flag4(WS_FWO + (unsigned)(c * 32 + R) * 128u);
    SCHED();
    if (!GE) late_scalars();
    auto xswap = [&](int q, int e) {
#pragma unroll
      for (int d = 0; d < 2; ++d) {
        if (e == 1) h2[q][2 + d] ^= (unsigned)__builtin_amdgcn_update_dpp(0, (int)h2[q][d], 0x128, 0xf, 0x3, false);
        else h2[q][d] ^= (unsigned)__builtin_amdgcn_update_dpp(0, (int)h2[q][2 + d], 0x128, 0xf, 0xC, false);
      }
      asm volatile("" : "+v"(h2[q]));
    };
#define XSW 1
#pragma unroll
    for (int q = 0; q < 10; ++q) {
      if (q < XSW) { xswap(q, 0); xswap(q, 1); xswap(q, 2); }
      if (q == 5 && w == 1 && __builtin_amdgcn_ballot_w64(f1v == tag) != 0xfffffull) wait_lines<1, 4>(rs, f1o, tag);
      if (q == 5) { __builtin_amdgcn_s_barrier(); asm volatile("" ::: "memory"); }
      SCHED();
      accg = MFMA16(h2[q], h2[q], accg);
      SCHED();
#pragma unroll
      for (int j = 0; j < 7; ++j) {
        acc[j] = MFMA16(h2[q], bf[j], acc[j]);
        if (q == 9 && j == 1) *(f32x4*)(lds + pgo) = accg;
        if (q < 9) bf[j] = BFRAG(q + 1, j);
        if (q < 9 && q + 1 >= XSW && j >= 3 && j < 6) xswap(q + 1, j - 3);
        if (q == 1 && j == 6) f1v = ld4(rs, f1o[0]);
        if (q == 2 && j == 0) bs_raw = __builtin_amdgcn_raw_buffer_load_b16(__builtin_amdgcn_make_buffer_rsrc((void*)p.b_sp, 0, S_ * 2, 0x00020000), bso, (unsigned)R * 128u, 0);
#define KCQ 4
#define WA 8
#define WB 12
#define WQ0 2
        if (WQ0 == 2 && q >= 2 && j == 2) LDW(q - 2);
        if (WQ0 == 6 && q >= 6 && (j == 2 || j == 4)) LDW(2 * (q - 6) + (j >> 2));
        if (WQ0 == 3 && q >= 2 && q < 5 && (j & 1) && 3 * (q - 2) + (j >> 1) < WA) LDW(3 * (q - 2) + (j >> 1));
        if (WQ0 == 4 && q >= 2 && !(q & 1) && j == 2) LDW((q - 2) >> 1);
        if ((j == 0 || j == 4) && 6 + 2 * q + (j >> 2) < 10) LDX(6 + 2 * q + (j >> 2));
        if (q == KCQ && j == 0) kc_ld(1);
        if (q == 2 && j == 1 && cs1) cs_ld(csv, c * 7 + R - 25);
        if (q >= 5 && j == 5) {
          const int i = (5 + q) * 7 + (w < 7 ? w : 0);
          dma1k(rs, lds + i * 1024, l16, WS_WINF + (unsigned)c * WBLK + (unsigned)i * 1024u);
          if (q == 9) { const int i2 = 28 + (w < 7 ? w : 0); dma1k(rs, lds + i2 * 1024, l16, WS_WINF + (unsigned)c * WBLK + (unsigned)i2 * 1024u); }
        }
        SCHED();
      }
    }
    STAMP(31);
  }
  unsigned sro, swo, zo, zro, po;
  auto gelu_n = [&](f32x2* x, auto N_, auto&& fill) {
    constexpr int N = decltype(N_)::value;
    const float c0 = -2.0f * 0.7978845608028654f * 1.4426950408889634f, c1 = c0 * 0.044715f;
    f32x2 k0 = {c0, c0}, k1 = {c1, c1}, k2 = {1.0f, 1.0f};
    asm volatile("" : "+v"(k0), "+v"(k1), "+v"(k2));
    f32x2 t[N];
#define GST(e) do { _Pragma("unroll") for (int i = 0; i < N; ++i) { t[i] = (e); asm volatile("" : "+v"(t[i])); } } while (0)
    GST(x[i] * x[i]);
    GST(__builtin_elementwise_fma(t[i], k1, k0));
    fill(IC<0>());
    GST(t[i] * x[i]);
    fill(IC<1>());
    GST(((f32x2){__builtin_amdgcn_exp2f(t[i][0]), __builtin_amdgcn_exp2f(t[i][1])}));
    fill(IC<2>());
    GST(t[i] + k2);
    fill(IC<3>());
    GST(((f32x2){__builtin_amdgcn_rcpf(t[i][0]), __builtin_amdgcn_rcpf(t[i][1])}));
    fill(IC<4>());
#pragma unroll
    for (int i = 0; i < N; ++i) { x[i] *= t[i]; asm volatile("" : "+v"(x[i])); }
  };
  {
    f32x2 mine[7];
    f32x2 csb[7];
    if (kh1) {
#pragma unroll
      for (int j = 0; j < 7; ++j) { *(f32x2*)(lds + rdo + j * 512) = (f32x2){acc[j][0], acc[j][1]}; mine[j] = (f32x2){acc[j][2], acc[j][3]}; }
      *(f32x4*)(lds + p6o) = acc[6];
    } else {
#pragma unroll
      for (int j = 0; j < 7; ++j) { *(f32x2*)(lds + rdo + j * 512) = (f32x2){acc[j][2], acc[j][3]}; mine[j] = (f32x2){acc[j][0], acc[j][1]}; }
      *(f32x4*)(lds + p6o) = acc[6];
      consts_put(1);
      if (cs1) cs_fin(csv, c * 7 + R - 25);
#pragma unroll
      for (int k = WA; k < WB; ++k) LDW(k);
    }
    BAR();
#define BY 0
    auto b1p = [&](int k) {
      const int i = 4 * k + (w & 3) + (k < 8 ? 35 : k > 8 ? 70 : (w & 3) < 3 ? 35 : 70) - (k == 17 ? (w & 2) : 0);
      dma1k(rs, lds + i * 1024, l16, WS_WINF + (unsigned)c * WBLK + (unsigned)i * 1024u);
    };
#define IB 5
    auto b18 = [&](int k) {
      const int i = 8 * k + w + (k < 4 ? 35 : k > 4 ? 70 : w < 3 ? 35 : 70) - (k == 8 && w > 5 ? 2 : 0);
      dma1k(rs, lds + i * 1024, l16, WS_WINF + (unsigned)c * WBLK + (unsigned)i * 1024u);
      SCHED();
    };
    if (kh1) {
#pragma unroll
      for (int k = 0; k < BY; ++k) b1p(k);
#pragma unroll
      for (int k = WA; k < WB; ++k) LDW(k);
    }
    __builtin_amdgcn_s_setprio(3);
    STAMPT(9);
    f32x2 oth[7];
    f32x2 pa[3], pb[3];
#pragma unroll
    for (int e = 0; e < 3; ++e) {
      pa[e] = (f32x2){*(const float*)(lds + pso[e]), *(const float*)(lds + pqo[e])};
      pb[e] = (f32x2){*(const float*)(lds + pso[e] + 256), *(const float*)(lds + pqo[e] + 1024)};
    }
#pragma unroll
    for (int j = 0; j < 7; ++j) { oth[j] = *(const f32x2*)(lds + oto + j * 512); csb[j] = *(const f32x2*)(lds + cso + 128 * j); }
    SCHED();
    if (IB > 6) b18(IB - 7);
    float mu2[2], rs2[2];
#pragma unroll
    for (int e = 0; e < 3; ++e) {
      const float mean = (pa[e][0] + pb[e][0]) * (1.0f / D_);
      const float var = __builtin_fmaxf((pa[e][1] + pb[e][1]) * (1.0f / D_) - mean * mean, 0.f);
      const float rstd = __builtin_amdgcn_rsqf(var + 1e-5f);
      if (e < 2) { mu2[e] = mean; rs2[e] = rstd; } else { mu_n = mean; rs_n = rstd; }
    }
    f32x2 zz[7];
    const f32x2 nmu = {-mu2[0], -mu2[1]}, rsv = {rs2[0], rs2[1]};
#pragma unroll
    for (int j = 0; j < 7; ++j)
      zz[j] = __builtin_elementwise_fma(__builtin_elementwise_fma(nmu, (f32x2){csb[j][0], csb[j][0]}, mine[j] + oth[j]), rsv, (f32x2){csb[j][1], csb[j][1]});
    if (IB > 5) {
#pragma unroll
      for (int j = 0; j < 7; ++j) asm volatile("" : "+v"(zz[j]));
      b18(IB - 6);
    }
    gelu_n(zz, IC<7>(), [&](auto K_) __attribute__((always_inline)) { if (IB - 5 + decltype(K_)::value >= 0 && IB) b18(IB - 5 + decltype(K_)::value); });
    if (n16 >= 12) zz[6] = (f32x2){0.f, 0.f};
    f32x2 s1 = ((zz[0] + zz[1]) + (zz[2] + zz[3])) + ((zz[4] + zz[5]) + zz[6]);
    f32x2 s2 = zz[0] * zz[0], s3 = zz[1] * zz[1];
#pragma unroll
    for (int j = 2; j < 7; ++j) { if (j & 1) s3 = __builtin_elementwise_fma(zz[j], zz[j], s3); else s2 = __builtin_elementwise_fma(zz[j], zz[j], s2); }
    s2 += s3;
    asm("" : "+v"(s1), "+v"(s2));
    float a0 = s1[0] + dpp_f<0xB1>(s1[0]), a1 = s1[1] + dpp_f<0xB1>(s1[1]), b0 = s2[0] + dpp_f<0xB1>(s2[0]);
    a0 = (l & 1) ? a1 : a0;
    const float b1 = s2[1] + dpp_f<0xB1>(s2[1]);
    b0 = (l & 1) ? b1 : b0;
    a0 += dpp_f<0x4E>(a0); b0 += dpp_f<0x4E>(b0);
    a0 += dpp_f<0x124>(a0); b0 += dpp_f<0x124>(b0);
    a0 += dpp_f<0x128>(a0); b0 += dpp_f<0x128>(b0);
    {
      const unsigned pa0 = __builtin_amdgcn_ds_bpermute(bsrc, __float_as_uint(a0)), pb0 = __builtin_amdgcn_ds_bpermute(bsrc, __float_as_uint(b0));
      ST16S(((u32x4){tag, pa0, tag, pb0}), rs, ppo, pos + (unsigned)c * 1024u, SC1);
    }
    SCHED();
    __builtin_amdgcn_s_setprio(0);
    STAMP(10);
#define BO 14
    if (kh1) {
#pragma unroll
      for (int k = 2 * IB + BY + BO; k < 18; ++k) b1p(k);
    } else {
#pragma unroll
      for (int k = 2 * IB + BY; k < 2 * IB + BY + BO && k < 18; ++k) b1p(k);
    }
#define WC 16
#define IT 0
#pragma unroll
    for (int k = WB; k < (IT ? WB : WC); ++k) LDW(k);
#pragma unroll
    for (int j = 0; j < 7; ++j)
      *(unsigned*)(lds + L_Z + (((rt1 >> 1) * 7 + j) * 64 + l) * 16 + (4 * (rt1 & 1) + 2 * kh1) * 2) = pack2(zz[j][0], zz[j][1]);
    SCHED();
#define TR0 2
#define TR1 TR0
#define TR2 TR1
#define TR3 5
    sfor<0, 4 * TR0>([&](auto X_) __attribute__((always_inline)) {
      constexpr int x = decltype(X_)::value, n = WC - WB, k0 = x * n / (4 * TR0), k1 = (x + 1) * n / (4 * TR0);
      if (IT) sfor<k0, k1>([&](auto K_) __attribute__((always_inline)) { LDW((WB + decltype(K_)::value) & 31); SCHED(); });
      WSP_TX(x);
      SCHED();
    });
    sro = (unsigned)(L_ST2 + h4 * 16);
    swo = (unsigned)(L_ST2 + (16 * rt1 + 4 * ((l >> 1) & 3) + 2 * kh1 + (l & 1)) * 4);
    zo = w < 7 ? (unsigned)(h4 * 7168 + n16 * 16) : 0x80000000u;
    zro = (unsigned)(L_Z + (w < 7 ? w : 0) * 1024) + l16;
    po = (unsigned)((l >> 3) * 1024 + (l & 7) * 16);
    asm volatile("" : "+v"(sro), "+v"(swo), "+v"(zo), "+v"(zro), "+v"(po));
    asm volatile("s_waitcnt vmcnt(%0)" :: "n"(WC - WB) : "memory");
    BAR();
    STAMPT(11);
  }
  unsigned z1d[7];
  constexpr int L_RED = 28672;
#define WOA(i) (((i) < 28 ? (i) : (i) < 108 ? (i) + 32 : (i) < 120 ? (i) + 37 : (i) - 92) * 1024)
#define RBAR() do { __builtin_amdgcn_s_barrier(); asm volatile("" ::: "memory"); } while (0)
#define ZAUX 16
#define NW 14
#define WOD(i0, x) dma1k(rs, lds + WOA(i0) + (x), l16, wgb + (unsigned)(i0) * 1024u + (x))
  auto wo_dma = [&](int k) {};
  u32x4 zw[NW];
#define LDZ(f) do { if (((f) >> 3) == 4) { zs[(f) & 7] += 4096u; asm volatile("" : "+s"(zs[(f) & 7])); } \
    zw[(f) % NW] = __builtin_amdgcn_raw_buffer_load_b128(rs, l16 + (unsigned)(((f) >> 3) & 3) * 1024u, zs[(f) & 7], ZAUX); } while (0)
#define WD 24
#define WE 32
#define QS 5
#define PQ 3
#define NW0 NW
#define VMW(n) asm volatile("s_waitcnt vmcnt(%0)" ::"n"(n) : "memory")
#define FQ 10
#define BFK 0
#define BFN 0
#define PP 1
#define WZ 2
#define WY 8
  static_assert((BFK == 0 || QS < 6) && QS >= 4 && QS <= 6 && PQ >= 2 && PQ < QS && (WZ < 4 || QS >= 5), "");
  {
    f32x4 acc[7];
#pragma unroll
    for (int j = 0; j < 7; ++j) acc[j] = (f32x4){0.f, 0.f, 0.f, 0.f};
    u32x4 bf[7];
#pragma unroll
    for (int j = 0; j < 7; ++j) bf[j] = BFRAG(4, j);
    u32x4 pv, zf[2];
    unsigned fwv;
    auto ld_pv = [&]() { pv = __builtin_amdgcn_raw_buffer_load_b128(rs, po, pos, SC1); };
    auto z1 = [&](auto Q0_, auto Q1_) __attribute__((always_inline)) {
#pragma unroll
      for (int qi = decltype(Q0_)::value; qi < decltype(Q1_)::value; ++qi) {
        const int q = (qi + 4) % 10, qn = (qi + 5) % 10;
        if (qi == FQ) {
          if (kh1) VMW(WE - WD + WZ); else VMW(0);
          RBAR();
          if (w == 3) flag4(WS_FZ + (unsigned)(c * 32 + R) * 128u);
        }
        SCHED();
#pragma unroll
        for (int j = 0; j < 7; ++j) {
          acc[j] = MFMA16(bf[j], h2[q], acc[j]);
          if (qi < 9 && (qi != QS - 1 || j < BFK)) bf[j] = BFRAG(qn, j);
          if (qi < 2 && (j == 1 || j == 4)) { const int i = (2 * qi + (j >> 2)) * 7 + (w < 7 ? w : 0); dma1k(rs, lds + i * 1024, l16, WS_WINF + (unsigned)c * WBLK + (unsigned)i * 1024u); }
          if (qi == 2 && j == 0) kc_ld(0);
          if (qi == 2 && j == 3) fwv = ld4(rs, fwo[0]);
          if (qi == PQ && j == 1) ld_pv();
          if (qi == QS - 1 && j < 2) zf[j] = *(const u32x4*)(lds + zro + j * 7168);
          if (!PP && qi >= QS && (j & 1) && WD + 3 * (qi - QS) + (j >> 1) < WE) LDW((WD + 3 * (qi - QS) + (j >> 1)) & 31);
          SCHED();
        }
      }
    };
    auto req1 = [&]() {
#pragma unroll
      for (int k = WC; k < WD; ++k) { LDW(k); SCHED(); }
    };
    auto req2 = [&]() {
#pragma unroll
      for (int k = WD; k < (PP ? WE : WD); ++k) { LDW(k); SCHED(); }
      sfor<0, WZ>([&](auto G_) __attribute__((always_inline)) { WOD(68 + 8 * decltype(G_)::value, wx8); SCHED(); });
    };
    if (kh1) req1();
    z1(IC<0>(), IC<QS>());
    STAMPT(29);
    f32x2 zv[2][4];
#pragma unroll
    for (int i = 0; i < 8; ++i) { zv[i >> 2][i & 3] = (f32x2){bflo(zf[i >> 2][i & 3]), bfhi(zf[i >> 2][i & 3])}; asm volatile("" : "+v"(zv[i >> 2][i & 3])); }
#pragma unroll
    for (int g = TR0; g < TR1; ++g) WSP_TR(g);
    __builtin_amdgcn_s_setprio(3);
    if (__any(ne2(pv[0], pv[2]) != 0u)) {
      _Pragma("unroll 1") for (unsigned spins = 0; spins < SPIN_CAP; ++spins) {
        ld_pv();
        if (!__any(ne2(pv[0], pv[2]) != 0u)) break;
        __builtin_amdgcn_s_sleep(1);
      }
    }
    {
      float t1 = __uint_as_float(pv[1]), t2 = __uint_as_float(pv[3]);
      t1 += dpp_f<0x128>(t1); t2 += dpp_f<0x128>(t2);
      auto x = __builtin_amdgcn_permlane16_swap(__float_as_uint(t1), __float_as_uint(t2), false, false);
      const unsigned y = __float_as_uint(__uint_as_float(x[0]) + __uint_as_float(x[1]));
      auto z = __builtin_amdgcn_permlane32_swap(y, y, false, false);
      const unsigned u = __float_as_uint(__uint_as_float(z[0]) + __uint_as_float(z[1]));
      auto v = __builtin_amdgcn_permlane16_swap(u, u, false, false);
      const float mean = __uint_as_float(v[0]) * (1.0f / FH);
      const float var = __builtin_fmaxf(__uint_as_float(v[1]) * (1.0f / FH) - mean * mean, 0.f);
      const float rstd = __builtin_amdgcn_rsqf(var + 1e-5f);
      *(float*)(lds + swo) = rstd;
      *(float*)(lds + swo + 256) = -mean * rstd;
    }
    SCHED();
    consts_put(0);
    if (w == 3 && __builtin_amdgcn_ballot_w64(fwv == tag) != 0xfffffffull) wait_lines<1, 1>(rs, fwo, tag);
    if (!kh1) req1();
    BAR();
    STAMP(12);
    {
      f32x4 sa[2][2], sb[2][2];
#pragma unroll
      for (int i = 0; i < 4; ++i) { sa[i >> 1][i & 1] = *(const f32x4*)(lds + sro + i * 64); sb[i >> 1][i & 1] = *(const f32x4*)(lds + sro + i * 64 + 256); }
      SCHED();
#pragma unroll
      for (int j = BFK; j < (BFN ? 7 : BFK); ++j) bf[j] = BFRAG((QS + 4) % 10, j);
      SCHED();
#pragma unroll
      for (int m = 0; m < 2; ++m) {
        u32x4 o;
#pragma unroll
        for (int e = 0; e < 4; ++e) {
          const f32x4 a = sa[m][e >> 1], b = sb[m][e >> 1];
          const f32x2 x = __builtin_elementwise_fma(zv[m][e], (f32x2){a[2 * (e & 1)], a[2 * (e & 1) + 1]}, (f32x2){b[2 * (e & 1)], b[2 * (e & 1) + 1]});
          o[e] = pack2(x[0], x[1]);
        }
        ST16S(o, rs, zo + (unsigned)m * 256u, zos, SC1);
      }
    }
    SCHED();
    __builtin_amdgcn_s_setprio(0);
    STAMPT(13);
#pragma unroll
    for (int j = BFK; j < (BFN ? BFK : 7); ++j) bf[j] = BFRAG((QS + 4) % 10, j);
    if (kh1) req2();
    z1(IC<QS>(), IC<10>());
    f32x2 mine[7];
    if (kh1) {
#pragma unroll
      for (int j = 0; j < 7; ++j) { *(f32x2*)(lds + rdo + L_RED + j * 512) = (f32x2){acc[j][0], acc[j][1]}; mine[j] = (f32x2){acc[j][2], acc[j][3]}; }
    } else {
#pragma unroll
      for (int j = 0; j < 7; ++j) { *(f32x2*)(lds + rdo + L_RED + j * 512) = (f32x2){acc[j][2], acc[j][3]}; mine[j] = (f32x2){acc[j][0], acc[j][1]}; }
    }
    if (!kh1) req2();
#pragma unroll
    for (int g = TR1; g < TR2; ++g) WSP_TR(g);
    if (FQ > 9) VMW(WE - WD + WZ);
    BAR();
    if (FQ > 9 && w == 3) flag4(WS_FZ + (unsigned)(c * 32 + R) * 128u);
    STAMP(6);
    auto req = [&](auto G0_, auto G1_) __attribute__((always_inline)) {
#pragma unroll
      for (int k = WE; k < 32; ++k) LDW(k);
      SCHED();
      sfor<decltype(G0_)::value, decltype(G1_)::value>([&](auto G_) __attribute__((always_inline)) {
        constexpr int i0 = 4 * decltype(G_)::value;
        WOD(i0 < 68 ? i0 : i0 + 8 * WZ, wx4); SCHED();
      });
    };
#define IL 1
#define XF 12
#define XC 18
    static_assert(!IL || (WZ == 2 && WE == 32), "");
    auto wo8 = [&](auto K_) __attribute__((always_inline)) {
      constexpr int k = decltype(K_)::value;
      if (k < 12) WOD(k < 3 ? 8 * k : k < 8 ? 8 * k + 4 : 8 * k + 20, wx8);
      else if (k == 12) dma1k(rs, lds + (kh1 ? WOA(116) : WOA(24)) + wx4, l16, wgb + (kh1 ? 116u : 24u) * 1024u + wx4);
      SCHED();
    };
    if (IL) { wo8(IC<0>()); wo8(IC<1>()); }
    if (!IL && kh1) req(IC<0>(), IC<WY>());
    u32x4 gv[8];
    auto gb_ld = [&]() {
#pragma unroll
      for (int k = 0; k < 8; ++k) gv[k] = __builtin_amdgcn_raw_buffer_load_b128(rs, l < 40 ? l16 : 0u, WS_MODQ + (unsigned)(k * 2560 + 1920 + 80 * c) * 8u, SC1);
    };
    if (w == 2) gb_ld();
    f32x2 z[7];
    {
      f32x2 oth[7];
      f32x4 cc[7];
#pragma unroll
      for (int j = 0; j < 7; ++j) {
        oth[j] = *(const f32x2*)(lds + oto + L_RED + j * 512);
        cc[j] = *(const f32x4*)(lds + L_CONST + (16 * j + 4 * h4 + 2 * kh1) * 8);
      }
      SCHED();
#pragma unroll
      for (int j = 0; j < 7; ++j)
        z[j] = __builtin_elementwise_fma(__builtin_elementwise_fma((f32x2){-mu_n, -mu_n}, (f32x2){cc[j][0], cc[j][1]}, oth[j] + mine[j]),
                                         (f32x2){rs_n, rs_n}, (f32x2){cc[j][2], cc[j][3]});
    }
    if (IL) {
#pragma unroll
      for (int j = 0; j < 7; ++j) asm volatile("" : "+v"(z[j]));
      wo8(IC<2>());
    }
    gelu_n(z, IC<7>(), [&](auto K_) __attribute__((always_inline)) { if (IL) wo8(IC<3 + decltype(K_)::value>()); });
#pragma unroll
    for (int j = 0; j < 7; ++j) z1d[j] = pack2(z[j][0], z[j][1]);
    asm volatile("" : "+v"(z1d[0]), "+v"(z1d[1]), "+v"(z1d[2]), "+v"(z1d[3]), "+v"(z1d[4]), "+v"(z1d[5]), "+v"(z1d[6]));
    SCHED();
    STAMPT(30);
    if (w == 2) {
      auto bad = [&]() {
        unsigned d = 0u;
#pragma unroll
        for (int k = 0; k < 8; ++k) d |= ne2(gv[k][0], gv[k][2]);
        return __any(d != 0u);
      };
      if (bad()) {
        _Pragma("unroll 1") for (unsigned spins = 0; spins < SPIN_CAP; ++spins) {
          __builtin_amdgcn_s_sleep(8);
          gb_ld();
          if (!bad()) break;
        }
      }
      f32x2 t[4];
#pragma unroll
      for (int k = 0; k < 4; ++k) t[k] = (f32x2){__uint_as_float(gv[2 * k][1]) + __uint_as_float(gv[2 * k + 1][1]), __uint_as_float(gv[2 * k][3]) + __uint_as_float(gv[2 * k + 1][3])};
      *(f32x2*)(lds + L_MISC + l * 8) = rbf2((t[0] + t[1]) + (t[2] + t[3]));
    }
    if (!IL && !kh1) req(IC<WY>(), IC<30 - 2 * WZ>());
    const unsigned fo[1] = {l < 4 ? fzs + (unsigned)l * 128u : ~0u};
#define FPY 1
#define FPO FPY
#define FSL 1
    unsigned fv;
    sfor<4 * TR2, 32>([&](auto X_) __attribute__((always_inline)) {
      constexpr int x = decltype(X_)::value;
      wo8(IC<8 + x - 4 * TR2>());
      if (x == XF) { fv = ld4(rs, fo[0]); SCHED(); }
      if (x == XC) {
        STAMP(14);
        if (__builtin_amdgcn_ballot_w64(fv == tag) != 15ull) wait_lines<1, FSL>(rs, fo, tag);
        STAMPT(23);
      }
      if (x >= XC && x - XC < NW) { LDZ((x - XC) & 15); SCHED(); }
      WSP_TX(x);
      SCHED();
    });
#pragma unroll
    for (int f = 32 - XC; f < NW; ++f) { LDZ(f); SCHED(); }
  }

#define WO2 15
#define TRC (IL ? 8 : TR3)
  {
    SCHED();
#pragma unroll
    for (int g = TRC; g < 8; ++g) WSP_TR(g);
    SCHED();
    STAMP(20);
    const int rt = rt1, kh = kh1;
    const unsigned rw = (unsigned)(L_RED + w * 512);
    unsigned rr = (unsigned)(L_RED + w * 4096 + l * 4);
    asm volatile("; m0 is set here, not in front of the first store (one wait state)" : "+v"(rr) : "{m0}"(rw));
    const float bs = bflo(bs_raw);
    unsigned long long hm = 0xffff000000000000ull;
    asm volatile("" : "+s"(hm));
    unsigned l8;
    f32x4 acc2[2][4];
    float rv[8][2];
    u32x4 ud;
    u32x4 P[2][8];
    u32x2 xp[3];
    auto ld_p = [&](int pt, int i) { P[pt][i] = __builtin_amdgcn_raw_buffer_load_b128(rs, l16 + (unsigned)pt * 1024u, ui + (unsigned)i * 2048u, SC1); };
#define RED_PUT1(j, i) asm volatile("ds_write_addtid_b32 %0 offset:%2" :: "v"(acc2[(j) & 1][((i) >> 2) & 3][(i) & 3]), "{m0}"(rw), "n"((((i) >> 2) & 3) * 4096 + (((i) >> 1) & 1) * 16384 + ((i) & 1) * 256) : "memory")
#define RED_PUT(j, r) do { RED_PUT1(j, 4 * (r)); RED_PUT1(j, 4 * (r) + 1); RED_PUT1(j, 4 * (r) + 2); RED_PUT1(j, 4 * (r) + 3); } while (0)
#define RED_GET(i) do { f32x2 x_; asm volatile("ds_read2st64_b32 %0, %1 offset0:%2 offset1:%3" : "=v"(x_) : "v"(rr), "n"(2 * (i)), "n"(2 * (i) + 1) : "memory"); rv[i][0] = x_[0]; rv[i][1] = x_[1]; } while (0)
    float zz[2], gg[2];
    unsigned cs;
    auto sum_rows_u = [&](unsigned v) {
      auto a = __builtin_amdgcn_permlane16_swap(v, v, false, false);
      v = a[0] + a[1];
      auto b = __builtin_amdgcn_permlane32_swap(v, v, false, false);
      return b[0] + b[1];
    };
    auto red_fin = [&](int j, int k, bool fuse = false) {
      const int e = k & 1, i = k >> 1;
#define VADD(d, x, y) asm volatile("v_add_f32 %0, %1, %2" : "=v"(d) : "v"(x), "v"(y))
      if (i < 4) VADD(rv[2 * i][e], rv[2 * i][e], rv[2 * i + 1][e]);
      if (i == 4 || i == 5) VADD(rv[4 * (i & 1)][e], rv[4 * (i & 1)][e], rv[4 * (i & 1) + 2][e]);
      if (i == 6) VADD(rv[0][e], rv[0][e], bs);
      if (i == 7) VADD(gg[e], rv[0][e], rv[4][e]);
      if (i == (fuse ? 9 : 8)) zz[e] = e ? bfhi(z1d[(j + 6) % 7]) : bflo(z1d[(j + 6) % 7]);
      if (i == 9) zz[e] *= gg[e];
      if (i == 9 || (i == 8 && !fuse)) asm volatile("" : "+v"(zz[e]));
      if (k == 20) { ud[j & 3] = pack2(zz[0], zz[1]); asm volatile("" : "+v"(ud[j & 3])); }
      if (k == 21) {
        if (j == 0) asm volatile("v_cndmask_b32 %0, %0, 0, %1" : "+v"(ud[0]) : "s"(hm));
        if (j == 2) asm volatile("v_add3_u32 %0, %1, %2, %3" : "=v"(cs) : "v"(ud[0]), "v"(ud[1]), "v"(ud[2]));
        if (j == 3) {
          cs = tag - sum_rows_u(cs + ud[3]);
          asm volatile("v_cndmask_b32 %0, %0, %1, %2" : "+v"(ud[0]) : "v"(cs), "s"(hm));
        }
        if (j >= 4) { ud[3] = (j == 4 ? tag : ud[3]) - ud[j & 3]; asm volatile("" : "+v"(ud[3])); }
        if (j % 3 == 0 && j) ST16S(ud, rs, l16 + (unsigned)(j / 6) * 1024u, uo, SC1);
      }
    };
    struct Sch { int b1, b2, wp[16], rp[8], vp[22], lp[8], pr[32]; };
    constexpr Sch S = {18, 28, {0, 1, 2, 3, 4, 5, 6, 7, 8, 9, 10, 11, 12, 12, 15, 15}, {19, 19, 20, 22, 23, 23, 24, 24}, {29, 29, 30, 30, 31, 31, 32, 33, 35, 36, 37, 38, 39, 40, 41, 42, 43, 46, 43, 46, 48, 48},
                       {1, 1, 1, 1, 1, 1, 1, 1}, {0, 0, 2, 1, 0, 0, 0, 0, 0, 0, 2, 2, 0, 0, 0, 0, 0, 0, 0, 1, 0, 0, 0, 1, 0, 0, 2, 1, 0, 0, 0, 0}};
    constexpr Sch F = {15, 23, {0, 1, 1, 2, 3, 4, 4, 5, 6, 7, 8, 9, 10, 11, 12, 12}, {15, 16, 16, 17, 17, 18, 18, 19}, {23, 23, 24, 24, 25, 26, 26, 27, 27, 28, 28, 29, 30, 30, 31, 31, 22, 22, 32, 33, 34, 35},
                       {1, 2, 1, 1, 1, 1, 1, 1}, {0, 2, 1, 0, 0, 1, 1, 0, 0, 2, 1, 0, 0, 2, 1, 0, 0, 2, 1, 0, 0, 2, 1, 0, 0, 2, 1, 0, 0, 2, 1, 0}};
#define VM 3
#define JF 4
#define RO 3
#define XE 1
#define CE 0
#define AP0 2
#define P1B 2
#define P1S 3
    unsigned wb[3], xo;
    const unsigned wl[3] = {(unsigned)(120 + WO3 + w) * 1024u, (unsigned)(128 + WO3 + w) * 1024u, wl2}, wg = wgb - (unsigned)(WO3 * 1024);
#define NAF 32
#define NA0 16
#define NA1 NA0
    u32x4 af[NAF];
#define LDA(m) af[(m) % NAF] = *(const u32x4*)(lds + wb[WOA(2 * (m)) >> 16] + (WOA(2 * (m)) & 65535))
    unsigned ck[2];
    const unsigned tag4 = tag * 4u;
    auto p_chk = [&](int pt, int pr, int k) {
      u32x4* q = P[pt] + 4 * pr;
      if (k < 2) ck[k & 1] = q[k & 1][0] + q[k & 1][1];
      else if (k < 8) ck[k & 1] = ck[k & 1] + q[(k & 1) + 2 * ((k >> 2) & 1)][k & 2] + q[(k & 1) + 2 * ((k >> 2) & 1)][(k & 2) + 1];
      else if (k == 8) ck[0] = pt ? ck[0] + ck[1] : sum_rows_u(ck[0] + ck[1]);
      if (k < 9) asm volatile("" : "+v"(ck[k & 1]));
      if (k == 9) {
        if (__builtin_expect(__any(ck[0] != tag4), 0)) {
          _Pragma("unroll 1") for (unsigned spins = 0; spins < SPIN_CAP; ++spins) {
            unsigned d = 0u;
#pragma unroll
            for (int i = 0; i < 4; ++i) ld_p(pt, 4 * pr + i);
#pragma unroll
            for (int i = 0; i < 4; ++i) d += q[i][0] + q[i][1] + q[i][2] + q[i][3];
            if (!pt) d = sum_rows_u(d);
            if (!__any(d != tag4)) break;
            __builtin_amdgcn_s_sleep(1);
          }
        }
      }
      if (k >= 10 && !pt) { unsigned x = q[(k - 10) & 3][0]; asm volatile("v_cndmask_b32 %0, %0, 0, %1" : "+v"(x) : "s"(hm)); q[(k - 10) & 3][0] = x; }
      if (k >= 10 && k < 13 && pt) { q[(k - 10) % 3][3] = q[3][(k - 10) % 3]; asm volatile("" : "+v"(q[(k - 10) % 3])); }
    };
    sfor<0, 224>([&](auto M_) __attribute__((always_inline)) {
      constexpr int mm = decltype(M_)::value, j = mm >> 5, m = mm & 31, y = m - (32 - 4 * RO);
      constexpr int t = j == 6 && y >= 0 ? 8 - RO + (y % (2 * RO + !RO)) / 2 : m >> 2, r = j == 6 && y >= 0 ? (y & 1) + 2 * (y / (2 * RO + !RO)) : m & 3, f = 8 * j + t;
      constexpr Sch C = j < JF ? S : F, B = j <= JF ? S : F;
      if (t == 0) asm volatile("v_mfma_f32_16x16x32_bf16 %0, %1, %2, 0" : "=&v"(acc2[j & 1][r]) : "v"(zw[f % NW]), "v"(wsp[r][t]), "v"(zw[(f | (f < 8 ? 1 : VM)) % NW]));
      else asm volatile("v_mfma_f32_16x16x32_bf16 %0, %1, %2, %0" : "+v"(acc2[j & 1][r]) : "v"(zw[f % NW]), "v"(wsp[r][t]), "v"(zw[(f | (f < 8 ? 1 : VM)) % NW]));
      SCHED();
      if (C.pr[m] == 1) __builtin_amdgcn_s_setprio(1);
      if ((m == C.b1 && j > 0) || (m == C.b2 && j > 0) || mm == 30) { asm volatile("" ::: "memory"); __builtin_amdgcn_s_waitcnt(0xC07F); RBAR(); SCHED(); }
      sfor<0, 16>([&](auto I_) __attribute__((always_inline)) {
        constexpr int i = decltype(I_)::value;
        if (j > 0 && C.wp[i] == m) { RED_PUT1(j + 1, i); SCHED(); }
        if (j > 0 && i < 8 && C.rp[i & 7] == m) { RED_GET(i & 7); SCHED(); }
      });
      if (j == 5 && m == 20) {
#pragma unroll
        for (int i = 0; i < 3; ++i) { wb[i] = (unsigned)(kh * 1024 + i * 65536) + l16; asm volatile("" : "+v"(wb[i])); }
      }
      if (XE && j == 5 && m == 13) {
        asm volatile("v_lshrrev_b32 %0, 1, %1" : "=v"(l8) : "v"(l16));
        xo = (l8 & 0x180u) + (unsigned)psi((l8 >> 3) & 15) * 8u;
        asm volatile("" : "+v"(xo));
      }
      if (RO && j == 6 && m >= 28) { RED_PUT1(6, 2 * (m - 28)); RED_PUT1(6, 2 * (m - 28) + 1); SCHED(); }
      if (CE && j == 6 && m >= 19 && m < 22) sfor<0, 3>([&](auto K_) __attribute__((always_inline)) { p_chk(0, 0, 3 * (m - 19) + decltype(K_)::value); SCHED(); });
      constexpr int ap[20] = {7, 8, 9, 10, 11, 13, 13, 14, 14, 19, 20, 21, 22, 23, 24, 25, 26, 27, 28, 29};
      sfor<0, NA0>([&](auto N_) __attribute__((always_inline)) { if (j == 6 && ap[AP0 + decltype(N_)::value] == m) { LDA(decltype(N_)::value); SCHED(); } });
      sfor<0, 22>([&](auto K_) __attribute__((always_inline)) {
        constexpr int k = decltype(K_)::value;
        if (j > 1 && B.vp[k] == m + 32) { red_fin(j > 1 ? j - 2 : 0, k, B.vp[16] == B.vp[18]); SCHED(); }
        if (j > 0 && C.vp[k] == m) { red_fin(j > 0 ? j - 1 : 0, k); SCHED(); }
      });
      if (C.pr[m] == 2) __builtin_amdgcn_s_setprio(0);
      if ((m & 3) == C.lp[m >> 2]) {
        if (f > 0 && f - 1 + NW < 56) LDZ(f - 1 + NW);
        if (j == 6 && (m >> 2) >= P1S) ld_p(0, ((m >> 2) - P1S) & 7);
        if (XE && j == 5 && m >= 20) xp[((m >> 2) - 5) % 3] = __builtin_amdgcn_raw_buffer_load_b64(rs, xo + (unsigned)((m >> 2) - 5) * 512u, xs, BULK_AUX);
        SCHED();
      }
    });
    STAMP(15);
#define NM0 0
#define OAUX 0
#define GM 19
#define MB 24
    f32x4 acc[5];
    f32x4 xf[3], gf[3];
    unsigned go, oo, eo, ho, tw, tr, o1, ta, tb;
    const unsigned s2 = (unsigned)(L_MISC + 128 * kh), s4 = s1 - (unsigned)kh * 1024u;
    unsigned long long om = kh ? ~0ull : 0xccccccccccccccccull;
#define PIN2(a, b) asm volatile("" : "+v"(a), "+v"(b))
    auto glue = [&](int k) {
      if (k == 0) { ta = l8 >> 5; tb = l16 & 48u; PIN2(ta, tb); }
      if (k == 1) { tr = __umul24(ta, 112u) + tb; oo = ta & 12u; PIN2(tr, oo); }
      if (k == 2) { tr += s1; oo += ta; PIN2(tr, oo); }
      if (k == 3) { oo = __umul24(oo, 1280u) + tb; ta = l8 >> 4; PIN2(oo, ta); }
      if (k == 4) { ta &= 24u; tb = l16 & 0xf0u; PIN2(ta, tb); }
      if (k == 5) { tw = __umul24(tb, 7u) + ta; go = (ta << 1) + s2; PIN2(tw, go); }
      if (k == 6) { tw += s1; asm volatile("v_cndmask_b32 %0, %1, -2.0, %2" : "=v"(o1) : "v"(oo), "s"(om)); asm volatile("" : "+v"(tw)); }
      if (k == 7) { eo = l16 + s3; ho = l16 + s4; PIN2(eo, ho); }
      if (k >= 8 && k < 14) {
        const unsigned x = xp[((k - 8) >> 1) % 3][k & 1];
        float lo = bflo(x), hi = bfhi(x);
        PIN2(lo, hi);
        xf[((k - 8) >> 1) % 3][2 * (k & 1)] = lo; xf[((k - 8) >> 1) % 3][2 * (k & 1) + 1] = hi;
      }
    };
    auto g3 = [&](auto M0_, auto M1_) __attribute__((always_inline)) {
      sfor<decltype(M0_)::value, decltype(M1_)::value>([&](auto M_) __attribute__((always_inline)) {
        constexpr int mi = decltype(M_)::value;
        constexpr int m = mi < 55 ? mi : mi < 64 ? 55 + 5 * ((mi - 55) / 3) + (mi - 55) % 3 : 58 + 5 * ((mi - 64) / 2) + (mi & 1), u2 = m / 5;
        const u32x4 fb = u2 < 8 ? P[0][u2 & 7] : P[1][u2 < 11 ? (u2 - 8) & 3 : (u2 - 7) & 7];
        if (m < 5) asm volatile("v_mfma_f32_16x16x32_bf16 %0, %1, %2, 0" : "=&v"(acc[m % 5]) : "v"(af[m % NAF]), "v"(fb));
        else asm volatile("v_mfma_f32_16x16x32_bf16 %0, %1, %2, %0" : "+v"(acc[m % 5]) : "v"(af[m % NAF]), "v"(fb));
        SCHED();
        if (mi == NM0) RBAR();
        if (mi >= NM0 && mi < NM0 + 3) dma1k(rs, lds + wl[(mi - NM0) % 3], l16, wg + wl[(mi - NM0) % 3]);
        if (mi >= 5 && mi < 19) p_chk(0, 1, mi - 5);
        if (mi >= GM && mi < GM + 14) glue(mi - GM);
        if (mi >= 47 && mi < 54) { p_chk(1, 1, 2 * (mi - 47)); SCHED(); p_chk(1, 1, 2 * (mi - 47) + 1); }
        if (!XE && mi == 3) { xo = (l8 & 0x180u) + (unsigned)psi((l8 >> 3) & 15) * 8u; asm volatile("" : "+v"(xo)); }
        if (!XE && mi >= 4 && mi < 7) xp[(mi - 4) % 3] = __builtin_amdgcn_raw_buffer_load_b64(rs, xo + (unsigned)(mi - 4) * 512u, xs, BULK_AUX);
        if (mi >= NM0 && mi < 40 && NA1 + mi - NM0 < 60) LDA(NA1 + mi - NM0);
        if (mi >= 66 && mi < 69) *(f32x4*)(lds + eo + (mi - 66) * 1024) = acc[(mi - 66) % 3];
        SCHED();
      });
    };
    __builtin_amdgcn_s_setprio(3);
#pragma unroll
    for (int k = 18; k < 22; ++k) red_fin(5, k);
    if (!XE) asm volatile("v_lshrrev_b32 %0, 1, %1" : "=v"(l8) : "v"(l16));
    if (RO) asm volatile("s_nop 2"); else { RED_PUT(6, 0); RED_PUT(6, 1); }
    RED_PUT(6, 2); RED_PUT(6, 3);
    red_fin(6, 16); red_fin(6, 17);
    SCHED();
#pragma unroll
    for (int i = 8 - P1S; i < 8 - P1S + P1B; ++i) ld_p(0, i);
    SCHED();
    BAR();
    sfor<0, 8>([&](auto I_) __attribute__((always_inline)) { RED_GET(decltype(I_)::value); });
    SCHED();
#pragma unroll
    for (int n = NA0; n < NA1; ++n) LDA(n);
#pragma unroll
    for (int i = 8 - P1S + P1B; i < 8; ++i) ld_p(0, i);
    SCHED();
    sfor<0, 20>([&](auto K_) __attribute__((always_inline)) {
      constexpr int k = decltype(K_)::value;
      if (k < 8 && !(k & 1)) asm volatile("s_waitcnt lgkmcnt(%0)" :: "n"(6 - k + NA1 - NA0) : "memory");
      red_fin(6, k < 16 ? k : k + 2);
    });
    SCHED();
    __builtin_amdgcn_s_setprio(0);
    STAMPT(16);
#pragma unroll
    for (int k = CE ? 9 : 0; k < 14; ++k) p_chk(0, 0, k);
    SCHED();
    g3(IC<0>(), IC<MB>());
    STAMPT(22);
    DRAIN();
    RBAR();
    STAMPT(27);
#define MY 12
    if (MY == 0 || !kh) {
#pragma unroll
      for (int i = 0; i < 8; ++i) { ld_p(1, i); SCHED(); }
    }
    g3(IC<MB>(), IC<MB + MY>());
    if (MY > 0 && kh) {
#pragma unroll
      for (int i = 0; i < 8; ++i) { ld_p(1, i); SCHED(); }
    }
    STAMPT(26);
    g3(IC<MB + MY>(), IC<40>());
    STAMPT(24);
#pragma unroll
    for (int m = NA1 + 40 - NM0; m < 70; ++m) LDA(m);
#pragma unroll
    for (int x = 0; x < 3; ++x) gf[x] = *(const f32x4*)(lds + go + x * 64);
    SCHED();
    const rsrc_t ro = __builtin_amdgcn_make_buffer_rsrc((void*)p.out, 0, S_ * D_ * 2, 0x00020000);
    STAMPT(17);
#pragma unroll
    for (int k = 0; k < 14; ++k) p_chk(1, 0, k);
    asm volatile("s_nop 1");
    STAMPT(25);
    g3(IC<40>(), IC<70>());
    STAMPT(18);
    BAR();
    STAMPT(28);
    f32x4 o4[3];
    u32x4 ov[2];
#pragma unroll
    for (int j = 2; j >= 0; --j) o4[j] = *(const volatile LAS f32x4*)(lds + ho + j * 1024);
    asm volatile("" : "+v"(acc[2]), "+v"(acc[3]), "+v"(acc[4]));
    SCHED();
#pragma unroll
    for (int j = 2; j >= 0; --j) {
      const f32x4 v = __builtin_elementwise_fma(gf[j], o4[j] + acc[2 + j], xf[j]);
      *(volatile LAS u32x2*)(lds + tw + j * 32) = (u32x2){pack2(v[0], v[1]), pack2(v[2], v[3])};
      if (j < 2) ov[j] = *(const volatile LAS u32x4*)(lds + tr + j * 32);
    }
    ST16S(ov[1], ro, oo + 32u, os, OAUX);
    ST16S(ov[0], ro, o1, os, OAUX);
    if (bid == 0 && w == 5 && l8 == 0u) st4(rs, WS_EPOCH, tag);
  }
  STAMP(19);
}

extern "C" __device__ __attribute__((used, noinline, visibility("hidden"))) void mega_tail() {}

extern "C" void kernel_launch(void* const* d_in, const int* in_sizes, int n_in, void* d_out, int out_size,
                              void* d_ws, size_t ws_size, hipStream_t stream) {
  Params p;
  p.q = (const u16*)d_in[0];
  p.k = (const u16*)d_in[1];
  p.v = (const u16*)d_in[2];
  p.shift_k = (const u16*)d_in[3];
  p.ssm_k = (const u16*)d_in[4];
  p.ssm_D = (const u16*)d_in[5];
  p.w_in = (const u16*)d_in[7];
  p.w_sp = (const u16*)d_in[8];
  p.b_sp = (const u16*)d_in[9];
  p.w_out = (const u16*)d_in[10];
  p.cvec = (const u16*)d_in[11];
  p.w_mod = (const u16*)d_in[12];
  p.out = (u16*)d_out;
  p.ws = (char*)d_ws;
  mega<<<256, 512, 0, stream>>>(p);
}
```

```cpp
#define LATE_TAG
#define PROBE 8
#include <hip/hip_runtime.h>
#include <stdint.h>

typedef unsigned short u16;
typedef __attribute__((ext_vector_type(8))) short bf16x8;
typedef __attribute__((ext_vector_type(4))) short bf16x4;
typedef __attribute__((ext_vector_type(4))) float f32x4;
typedef __attribute__((ext_vector_type(2))) float f32x2;
typedef __attribute__((ext_vector_type(4))) unsigned int u32x4;
typedef __attribute__((ext_vector_type(2))) unsigned int u32x2;
#define LAS __attribute__((address_space(3)))
typedef __amdgpu_buffer_rsrc_t rsrc_t;
typedef __attribute__((ext_vector_type(4))) short s16x4;

constexpr int S_ = 2048, D_ = 640, F_ = 1728, FH = 864, CB = 108;

constexpr unsigned WS_EPOCH = 0;
constexpr unsigned WS_FWI = 4096;
constexpr unsigned WS_FWO = WS_FWI + 320 * 128;
constexpr unsigned WS_FX = WS_FWO + 64 * 128;
constexpr unsigned WS_FZ = WS_FX + 160 * 128;
constexpr unsigned WS_CS = WS_FZ + 256 * 128;
constexpr unsigned WS_FX8 = 262144;
constexpr unsigned WS_PART = 524288;
constexpr unsigned WS_MOD = WS_PART + 262144;
constexpr unsigned WS_MODP = WS_MOD + 32768;
constexpr unsigned WS_CSP = WS_MODP + 204800;
constexpr unsigned WS_ST1 = WS_CSP + 655360;
constexpr unsigned WS_KVG = WS_ST1 + 8 * 32768;
constexpr unsigned WS_QB = WS_KVG;
constexpr unsigned WS_X1B = WS_QB + 5242880;
constexpr unsigned WBLK = 143360;
constexpr unsigned WS_WINF = WS_X1B + 2621440;
constexpr unsigned WS_WOUTF = WS_WINF + 16 * WBLK;
constexpr unsigned WS_Z2F = WS_WOUTF + 8 * WBLK;
constexpr unsigned WS_UF = WS_Z2F + 8 * 64 * 7168;
constexpr unsigned WS_END = WS_UF + 32 * 4 * 8 * 3584;
constexpr unsigned WS_STAMP = 200u << 20;

constexpr int U_BYTES = (256 + S_) * 2;
constexpr int G_COPY = 4160;
constexpr int CH_BYTES = U_BYTES + 4 * G_COPY + 32;
constexpr int L_X = 4 * CH_BYTES;
constexpr int L_Y = L_X + 32768;
constexpr int L_RT = 16384;
constexpr int L_CONST = WBLK;
constexpr int L_PST = L_CONST + 2048;
constexpr int L_Z = L_CONST + 5120;
constexpr int L_FLG = L_Z;
constexpr int L_ST2 = L_Z + 14336;
constexpr int L_MISC = L_ST2 + 512;
constexpr int LDS_TOTAL = 163840;

#define FX_SLEEP 2
#define AUX_T 250
#define U_AT 17
#define RT2_T 450
#define CB_T 460
#define W_T 720
#define G_DELAY 3
#define CS_T 860
#define B_GO 0
#define EMU 1
#define PF_IDX 31
#define NB2A 4
#define FLAG_GAP 4
#define QPRIO 3
#ifndef M_DELAY
#define M_DELAY 6
#endif
#define TCH_OFF
#define TCH 0
#define TCH_N 2
#define TCH_T 500
constexpr int SC1 = 16;
constexpr unsigned SPIN_CAP = 1u << 16;

__device__ __forceinline__ float bflo(unsigned x) { return __uint_as_float(x << 16); }
__device__ __forceinline__ float bfhi(unsigned x) { return __uint_as_float(x & 0xffff0000u); }
__device__ __forceinline__ unsigned f2bf(float x) {
  __bf16 b = (__bf16)x;
  return (unsigned)__builtin_bit_cast(unsigned short, b);
}
__device__ __forceinline__ float rbf(float x) { return bflo(f2bf(x)); }
typedef __attribute__((ext_vector_type(2))) __bf16 bf2_t;
__device__ __forceinline__ unsigned pack2(float lo, float hi) {
  f32x2 v = {lo, hi};
  return __builtin_bit_cast(unsigned, __builtin_convertvector(v, bf2_t));
}
__device__ __forceinline__ f32x2 rbf2(f32x2 v) {
  const unsigned p = __builtin_bit_cast(unsigned, __builtin_convertvector(v, bf2_t));
  return (f32x2){__uint_as_float(p << 16), __uint_as_float(p & 0xffff0000u)};
}
__device__ __forceinline__ f32x4 rbf4(f32x4 v) {
  const f32x2 a = rbf2((f32x2){v[0], v[1]}), b = rbf2((f32x2){v[2], v[3]});
  return (f32x4){a[0], a[1], b[0], b[1]};
}
template <int CTRL>
__device__ __forceinline__ float dpp_f(float v) {
  return __int_as_float(__builtin_amdgcn_update_dpp(0, __float_as_int(v), CTRL, 0xf, 0xf, true));
}
__device__ __forceinline__ float sum8(float v) {
  v += dpp_f<0xB1>(v);
  v += dpp_f<0x4E>(v);
  v += dpp_f<0x141>(v);
  return v;
}
__device__ __forceinline__ float sum16(float v) {
  v = sum8(v);
  v += dpp_f<0x140>(v);
  return v;
}
__device__ __forceinline__ float sum_rows(float v) {
  auto a = __builtin_amdgcn_permlane16_swap(__float_as_uint(v), __float_as_uint(v), false, false);
  v = __uint_as_float(a[0]) + __uint_as_float(a[1]);
  auto b = __builtin_amdgcn_permlane32_swap(__float_as_uint(v), __float_as_uint(v), false, false);
  return __uint_as_float(b[0]) + __uint_as_float(b[1]);
}
__device__ __forceinline__ float wave_sum(float v) { return sum_rows(sum16(v)); }
__device__ __forceinline__ float gelu_tanh(float x) {
  const float c0 = -2.0f * 0.7978845608028654f * 1.4426950408889634f;
  const float c1 = c0 * 0.044715f;
  float p = __builtin_fmaf(x * x, c1, c0);
  float e = __builtin_amdgcn_exp2f(x * p);
  return x * __builtin_amdgcn_rcpf(1.0f + e);
}
#define MFMA16(a, b, c) __builtin_amdgcn_mfma_f32_16x16x32_bf16(__builtin_bit_cast(bf16x8, a), __builtin_bit_cast(bf16x8, b), c, 0, 0, 0)
#define MFMA16K16(a, b, c) __builtin_amdgcn_mfma_f32_16x16x16bf16_1k(__builtin_bit_cast(bf16x4, a), __builtin_bit_cast(bf16x4, b), c, 0, 0, 0)
__device__ __forceinline__ u32x2 trd(const char* p) {
  return __builtin_bit_cast(u32x2, __builtin_amdgcn_ds_read_tr16_b64_v4i16((LAS s16x4*)p));
}
__device__ __forceinline__ u32x4 trfrag(const char* p, int pitch) {
  u32x2 a = trd(p), b = trd(p + 16 * pitch);
  return (u32x4){a[0], a[1], b[0], b[1]};
}
#define BAR() asm volatile("s_waitcnt lgkmcnt(0)\n\ts_barrier" ::: "memory")
#define DRAIN() do { __builtin_amdgcn_s_waitcnt(0x0F70); asm volatile("" ::: "memory"); } while (0)
#define SCHED() __builtin_amdgcn_sched_barrier(0)
template <int N> struct IC { static constexpr int value = N; };

__device__ __forceinline__ u32x4 ld16(rsrc_t r, unsigned off) { return __builtin_amdgcn_raw_buffer_load_b128(r, off, 0, SC1); }
__device__ __forceinline__ u32x2 ld8(rsrc_t r, unsigned off) { return __builtin_amdgcn_raw_buffer_load_b64(r, off, 0, SC1); }
__device__ __forceinline__ unsigned ld4(rsrc_t r, unsigned off) { return __builtin_amdgcn_raw_buffer_load_b32(r, off, 0, SC1); }
#define ST_AUX 16
__device__ __forceinline__ void st16(rsrc_t r, unsigned off, u32x4 v) { __builtin_amdgcn_raw_buffer_store_b128(v, r, off, 0, ST_AUX); }
__device__ __forceinline__ void st8(rsrc_t r, unsigned off, u32x2 v) { __builtin_amdgcn_raw_buffer_store_b64(v, r, off, 0, SC1); }
__device__ __forceinline__ void st_flag(rsrc_t r, unsigned off, unsigned tag, int l) { if (l < 8) st16(r, off + (unsigned)l * 16u, (u32x4){tag, tag, tag, tag}); }
__device__ __forceinline__ void st4(rsrc_t r, unsigned off, unsigned v) { __builtin_amdgcn_raw_buffer_store_b32(v, r, off, 0, SC1); }
#define BULK_AUX 16
__device__ __forceinline__ u32x4 ld16p(rsrc_t r, unsigned off) { return __builtin_amdgcn_raw_buffer_load_b128(r, off, 0, BULK_AUX); }
__device__ __forceinline__ u32x2 ld8p(rsrc_t r, unsigned off) { return __builtin_amdgcn_raw_buffer_load_b64(r, off, 0, BULK_AUX); }
__device__ __forceinline__ void dma1k(rsrc_t r, char* lds_dst, unsigned l16, unsigned soff) {
  __builtin_amdgcn_raw_ptr_buffer_load_lds(r, (LAS void*)lds_dst, 16, l16, soff, 0, BULK_AUX);
}

#define STAMP(i)

template <int N, int SLP>
__device__ __forceinline__ void wait_lines(rsrc_t r, const unsigned (&off)[N], unsigned tag) {
  bool need[N];
#pragma unroll
  for (int i = 0; i < N; ++i) need[i] = off[i] != ~0u;
  _Pragma("unroll 1") for (unsigned spins = 0; spins < SPIN_CAP; ++spins) {
    unsigned v[N];
#pragma unroll
    for (int i = 0; i < N; ++i) {
      v[i] = tag;
      if (need[i]) v[i] = ld4(r, off[i]);
    }
    bool any = false;
#pragma unroll
    for (int i = 0; i < N; ++i) { need[i] = v[i] != tag; any = any || need[i]; }
    if (!__any(any)) break;
    __builtin_amdgcn_s_sleep(SLP);
  }
  asm volatile("" ::: "memory");
}

__device__ __forceinline__ int tau(int rt, int n) { return 32 * (rt >> 1) + 8 * (n >> 2) + 4 * (rt & 1) + (n & 3); }
__device__ __forceinline__ int psi(int n) { return 2 * (n & 7) + (n >> 3); }
#define QX_CM(d, s0, s1, P) "v_cndmask_b32_dpp " d ", " s0 ", " s1 ", vcc quad_perm:" P " row_mask:0xf bank_mask:0xf\n\t"
#define QX_BODY(M, P) \
  asm("s_mov_b32 vcc_lo, " M "\n\ts_mov_b32 vcc_hi, " M "\n\t" \
      QX_CM("%0", "%12", "%8", P) "v_xor_b32 %4, %8, %0\n\t" QX_CM("%1", "%13", "%9", P) "v_xor_b32 %5, %9, %1\n\t" \
      QX_CM("%2", "%14", "%10", P) "v_xor_b32 %6, %10, %2\n\t" QX_CM("%3", "%15", "%11", P) "v_xor_b32 %7, %11, %3" \
      : "=&v"(t[0]), "=&v"(t[1]), "=&v"(t[2]), "=&v"(t[3]), "=&v"(u[0]), "=&v"(u[1]), "=&v"(u[2]), "=&v"(u[3]) \
      : "v"(a[0]), "v"(a[1]), "v"(a[2]), "v"(a[3]), "v"(b[0]), "v"(b[1]), "v"(b[2]), "v"(b[3]) : "vcc")
template <int BIT>
__device__ __forceinline__ void quad_xchg3(u32x4& a, u32x4& b) {
  unsigned t[4], u[4];
  if (BIT == 0) QX_BODY("0x55555555", "[1,0,3,2]");
  else QX_BODY("0x33333333", "[2,3,0,1]");
  a = (u32x4){t[0], t[1], t[2], t[3]};
#pragma unroll
  for (int d = 0; d < 4; ++d) b[d] ^= (unsigned)__builtin_amdgcn_update_dpp(0, (int)u[d], BIT == 0 ? 0xB1 : 0x4E, 0xf, 0xf, true);
}
__device__ __forceinline__ void quad_transpose(u32x4& x0, u32x4& x1, u32x4& x2, u32x4& x3) {
  quad_xchg3<0>(x0, x1);
  quad_xchg3<0>(x2, x3);
  quad_xchg3<1>(x0, x2);
  quad_xchg3<1>(x1, x3);
  asm volatile("" : "+v"(x0), "+v"(x1), "+v"(x2), "+v"(x3));
}

struct Params {
  const u16 *q, *k, *v, *shift_k, *ssm_k, *ssm_D, *w_in, *w_sp, *b_sp, *w_out, *cvec, *w_mod;
  u16* out;
  char* ws;
};

__global__ __launch_bounds__(512, 2) void mega(Params p) {
  __shared__ __attribute__((aligned(16))) char lds[LDS_TOTAL];
  unsigned long long pr0;
  asm volatile("s_memrealtime %0" : "=s"(pr0));
  SCHED();
  const int tid = threadIdx.x, l = tid & 63;
  const int w = __builtin_amdgcn_readfirstlane(tid >> 6);
  const int bid = blockIdx.x, xcd = bid & 7, idx = bid >> 3;
  char* const ws = p.ws;
  asm volatile("" ::"s"(p.q), "s"(p.k), "s"(p.v), "s"(p.shift_k), "s"(p.ssm_k), "s"(p.ssm_D), "s"(p.w_in), "s"(p.w_sp), "s"(p.b_sp),
               "s"(p.w_out), "s"(p.cvec), "s"(p.w_mod), "s"(p.out), "s"(p.ws));
  unsigned epoch;
  asm volatile("s_load_dword %0, %1, 0x0" : "=s"(epoch) : "s"(ws));
  const rsrc_t rs = __builtin_amdgcn_make_buffer_rsrc(ws, 0, WS_END, 0x00020000);
  const unsigned l16 = (unsigned)l * 16u;
  STAMP(0);
  const unsigned t_start = (unsigned)__builtin_amdgcn_s_memrealtime();
  const unsigned long long prc = __builtin_amdgcn_s_memtime();
  unsigned long long pc0;
  unsigned clen;
  asm volatile("s_getpc_b64 %0\n.Lmega_here:\n\ts_mov_b32 %1, mega_tail-.Lmega_here" : "=s"(pc0), "=s"(clen));
  auto code_pf = [&](auto K0, auto K1) {
    const rsrc_t rc = __builtin_amdgcn_make_buffer_rsrc((void*)(pc0 & ~15ull), 0, clen & ~15u, 0x00020000);
#pragma unroll
    for (int k = decltype(K0)::value; k < decltype(K1)::value; ++k) {
      unsigned o = (k < 3 ? (unsigned)k * 4096u : k < 7 ? (clen & ~15u) - (unsigned)(k - 2) * 4096u : (unsigned)(k - 4) * 4096u) + (unsigned)(w * 1024 + l * 16);
      __builtin_amdgcn_raw_ptr_buffer_load_lds(rc, (LAS void*)(lds + 8192), 16, o, 0, 0, 0);
    }
  };
  if (w < 4 && idx == PF_IDX) code_pf(IC<0>(), IC<15>());
  unsigned tag;
#define TAG_READY() do { SCHED(); asm volatile("s_waitcnt lgkmcnt(0)\n\ts_mul_i32 %0, %1, 0x9e3779b1\n\ts_add_i32 %0, %0, 0x9e3779b9" : "=s"(tag) : "s"(epoch) : "scc"); SCHED(); } while (0)

  const unsigned pr3 = 0;
  const bool is_conv = idx < 20;
  const int quad = xcd * 20 + idx;
  const int R = (xcd & 3) * 8 + (idx & 7);
  const int c = (xcd >> 2) * 4 + (idx >> 3);
  const int n16 = l & 15, h4 = l >> 4;
  const int rp = w & 1, kq = w >> 1;
  u32x4 wsp[2][16];
  const int rot = 4 * (idx & 3);
  const rsrc_t rsp = __builtin_amdgcn_make_buffer_rsrc((void*)p.w_sp, 0, S_ * S_ * 2, 0x00020000);
  const unsigned wsp_vo = (unsigned)(((l >> 2) & 3) * 8 * 4096 + h4 * 64 + (l & 3) * 16);
#define LDW(k) wsp[((k) >> 2) & 1][((k) >> 3) * 4 + ((k) & 3)] = __builtin_amdgcn_raw_buffer_load_b128(rsp, wsp_vo, \
    (unsigned)((R * 64 + 32 * rp + 4 * (((k) >> 2) & 1) + ((k) & 3)) * 4096 + kq * 1024 + (((((k) >> 3) * 4 + rot) & 15) * 64)), 0)
#define WSP_TR(g) quad_transpose(wsp[(g) & 1][((g) >> 1) * 4], wsp[(g) & 1][((g) >> 1) * 4 + 1], wsp[(g) & 1][((g) >> 1) * 4 + 2], wsp[(g) & 1][((g) >> 1) * 4 + 3])
#define TOUCH()
#define TOUCH_END()
  const rsrc_t rwi = __builtin_amdgcn_make_buffer_rsrc((void*)p.w_in, 0, D_ * F_ * 2, 0x00020000);
  const rsrc_t rwo = __builtin_amdgcn_make_buffer_rsrc((void*)p.w_out, 0, FH * D_ * 2, 0x00020000);
  auto dma_b2 = [&](int k0, int k1) {
#pragma unroll
    for (int k = k0; k < k1; ++k) {
      int m = k + (k1 - k0 >= 8 ? (idx & 7) : 0);
      m = m >= k1 ? m - (k1 - k0) : m;
      dma1k(rs, lds + (m * 7 + w) * 1024, l16, WS_WINF + (unsigned)(8 + c) * WBLK + (unsigned)(m * 7 + w) * 1024u);
    }
  };
  auto poison_u = [&]() {
#pragma unroll
    for (int k = 0; k < 7; ++k) {
      const int i = w * 7 + k;
      st16(rs, WS_UF + (unsigned)((R * 4 + i / 14) * 8 + c - 3) * 3584u + (unsigned)(i % 14) * 1024u + l16, (u32x4){~0u, ~0u, ~0u, ~0u});
    }
  };
  auto wait_weights = [&]() {
    const unsigned fo[1] = {l < 20 ? WS_FWI + (unsigned)((8 + c) * 20 + l) * 128u : ~0u};
    wait_lines<1, 8>(rs, fo, tag);
  };
  auto rt_job = [&](int i0) {
    char* st = lds + L_RT + w * 18432;
    if (i0 < 320) {
      const int cb = (i0 < 160 ? 8 : 0) + (i0 & 7), Q = (i0 < 160 ? i0 : i0 - 160) >> 3;
#pragma unroll
      for (int r = 0; r < 7; ++r) {
        const int g = r * 64 + l;
        __builtin_amdgcn_raw_ptr_buffer_load_lds(rwi, (LAS void*)(st + r * 1024), 16, (unsigned)((g / 14) * (F_ * 2) + (g % 14) * 16 + Q * 32 * F_ * 2 + cb * CB * 2),
                                                 0, 0, 0);
      }
      const int col = 1920 + 640 * (l >> 5) + 32 * Q + (l & 31);
      const unsigned po = WS_MODP + (unsigned)col * 8u;
      u32x2 pp8[8];
      while ((unsigned)__builtin_amdgcn_s_memrealtime() - t_start < CB_T) __builtin_amdgcn_s_sleep(2);
      _Pragma("unroll 1") for (unsigned spins = 0; spins < SPIN_CAP; ++spins) {
        unsigned dd = 0u;
#pragma unroll
        for (int i = 0; i < 8; ++i) pp8[i] = ld8(rs, po + (unsigned)i * 25600u);
#pragma unroll
        for (int i = 0; i < 8; ++i) dd |= pp8[i][0] ^ tag;
        if (__all(dd == 0u)) break;
        __builtin_amdgcn_s_sleep(4);
      }
      {
        float t = 0.f;
#pragma unroll
        for (int i = 0; i < 8; ++i) t += __uint_as_float(pp8[i][1]);
        if (l >= 32) *(float*)(st + 8192 + (l - 32) * 4) = rbf(1.0f + rbf(t));
        else *(u16*)(st + 8320 + l * 2) = (u16)f2bf(t);
      }
      DRAIN();
      asm volatile("s_waitcnt lgkmcnt(0)" ::: "memory");
      const f32x4 ca = *(const f32x4*)(st + 8192 + h4 * 16), cc = *(const f32x4*)(st + 8192 + 64 + h4 * 16);
      const char* bp = st + (4 * h4 + (n16 >> 2)) * 224 + (n16 & 3) * 8;
      u32x4 x[7], o[7];
#pragma unroll
      for (int j = 0; j < 7; ++j) {
        x[j] = trfrag(bp + j * 32, 224);
        o[j] = (u32x4){pack2(bflo(x[j][0]) * ca[0], bfhi(x[j][0]) * ca[1]), pack2(bflo(x[j][1]) * ca[2], bfhi(x[j][1]) * ca[3]),
                       pack2(bflo(x[j][2]) * cc[0], bfhi(x[j][2]) * cc[1]), pack2(bflo(x[j][3]) * cc[2], bfhi(x[j][3]) * cc[3])};
        if (j == 6 && n16 == 12) o[j] = (u32x4){0x3F803F80u, 0x3F803F80u, 0x3F803F80u, 0x3F803F80u};
        st16(rs, WS_WINF + (unsigned)cb * WBLK + (unsigned)(Q * 7 + j) * 1024u + l16, o[j]);
      }
      const u32x2 e0 = *(const u32x2*)(st + 8320 + h4 * 8), e1 = *(const u32x2*)(st + 8352 + h4 * 8);
      f32x4 d[2] = {{0.f, 0.f, 0.f, 0.f}, {0.f, 0.f, 0.f, 0.f}};
#pragma unroll
      for (int j = 0; j < 7; ++j) {
        const unsigned one = n16 == 4 * (j & 3) ? 0x3F803F80u : 0u;
        const bool sr = n16 == 4 * (j & 3) + 1;
        d[j >> 2] = MFMA16(((u32x4){one, one, one, one}), o[j], d[j >> 2]);
        d[j >> 2] = MFMA16(((u32x4){sr ? e0[0] : 0u, sr ? e0[1] : 0u, sr ? e1[0] : 0u, sr ? e1[1] : 0u}), x[j], d[j >> 2]);
      }
#pragma unroll
      for (int m = 0; m < 2; ++m)
        st16(rs, WS_CSP + (unsigned)((cb * 20 + Q) * 2 + m) * 1024u + l16, (u32x4){tag, __float_as_uint(d[m][0]), tag, __float_as_uint(d[m][1])});
      asm volatile("s_waitcnt vmcnt(2)" ::: "memory");
      st_flag(rs, WS_FWI + (unsigned)(cb * 20 + Q) * 128u, tag, l);
    } else if (i0 < 384) {
      const int v = i0 - 320, cc = v >> 3, c2 = v & 7;
#pragma unroll
      for (int r = 0; r < 18; ++r) {
        const int g = r * 64 + l;
        __builtin_amdgcn_raw_ptr_buffer_load_lds(rwo, (LAS void*)(st + r * 1024), 16, (unsigned)((g / 10) * (D_ * 2) + (g % 10) * 16 + c2 * CB * D_ * 2 + cc * 160),
                                                 0, 0, 0);
      }
      DRAIN();
      const char* bp = st + (4 * h4 + (n16 >> 2)) * 160 + (n16 & 3) * 8;
      const unsigned base = WS_WOUTF + (unsigned)cc * WBLK + (unsigned)c2 * 17920u;
#pragma unroll
      for (int qq = 0; qq < 3; ++qq)
#pragma unroll
        for (int dj = 0; dj < 5; ++dj) st16(rs, base + (unsigned)(qq * 5 + dj) * 1024u + l16, trfrag(bp + qq * 5120 + dj * 32, 160));
#pragma unroll
      for (int dj = 0; dj < 5; ++dj) st8(rs, base + 15360u + (unsigned)dj * 512u + (unsigned)l * 8u, trd(bp + 15360 + dj * 32));
      DRAIN();
      st_flag(rs, WS_FWO + (unsigned)v * 128u, tag, l);
    }
  };

  auto red8 = [&](float v) { v += dpp_f<0x128>(v); return sum_rows(v); };
  const rsrc_t rwm = __builtin_amdgcn_make_buffer_rsrc((void*)p.w_mod, 0, D_ * 3840 * 2, 0x00020000);
  auto gemv_load = [&](u32x4* pre, int blk, int sl) {
    pre[10][0] = *(const unsigned*)(p.cvec + 80 * sl + 2 * (l < 40 ? l : 0));
#pragma unroll
    for (int pp = 0; pp < 10; ++pp)
      pre[pp] = __builtin_amdgcn_raw_buffer_load_b128(rwm, (unsigned)((l >> 3) * 7680 + (l & 7) * 16), (unsigned)((80 * sl + pp * 8) * 7680 + blk * 128), 0);
  };
  auto gemv_fin = [&](float* acc, const u32x4* pre) {
    unsigned* svt = (unsigned*)(lds + 4096) + w * 96;
#pragma unroll
    for (int e = 0; e < 2; ++e) {
      const int row = 2 * l + e;
      const float cv = e ? bfhi(pre[10][0]) : bflo(pre[10][0]);
      if (l < 40) svt[(row & 7) * 12 + (row >> 3)] = __float_as_uint(rbf(cv * __builtin_amdgcn_rcpf(1.0f + __expf(-cv))));
    }
    asm volatile("s_waitcnt lgkmcnt(0)" ::: "memory");
    u32x4 sv[3];
#pragma unroll
    for (int i = 0; i < 3; ++i) sv[i] = *(const u32x4*)(svt + (l >> 3) * 12 + 4 * i);
    SCHED();
#pragma unroll
    for (int e = 0; e < 8; ++e) acc[e] = 0.f;
#pragma unroll
    for (int pp = 0; pp < 10; ++pp) {
      const unsigned sh = sv[pp >> 2][pp & 3], sl = sh >> 16;
#pragma unroll
      for (int e = 0; e < 4; ++e) {
        const unsigned x = pre[pp][e];
        acc[2 * e] = __builtin_amdgcn_fdot2_f32_bf16(__builtin_bit_cast(bf2_t, x), __builtin_bit_cast(bf2_t, sl), acc[2 * e], false);
        acc[2 * e + 1] = __builtin_amdgcn_fdot2_f32_bf16(__builtin_bit_cast(bf2_t, x), __builtin_bit_cast(bf2_t, sh), acc[2 * e + 1], false);
      }
    }
#pragma unroll
    for (int e = 0; e < 8; ++e) acc[e] = red8(acc[e]);
  };
  auto gemv_put = [&](const u32x4* pre, int blk, int sl) {
    float acc[8];
    gemv_fin(acc, pre);
    float v = acc[0];
#pragma unroll
    for (int e = 1; e < 8; ++e) v = (l >> 3) == e ? acc[e] : v;
    st8(rs, WS_MODP + (unsigned)(sl * 3200 + blk * 64 + l) * 8u,
        (u32x2){tag, (unsigned)__builtin_amdgcn_ds_bpermute(((l & 7) * 8 + (l >> 3)) * 4, (int)__float_as_uint(v))});
  };
  const int a1 = xcd * 10 + idx - 20;
  typedef const __attribute__((address_space(4))) unsigned* cptr_t;
  const bf2_t ONE2 = {(__bf16)1.0f, (__bf16)1.0f};

  if (__builtin_expect(is_conv, 1)) {
    const int ch = w & 3, hf = w >> 2;
    char* U = lds + ch * CH_BYTES;
    char* G = U + U_BYTES;
    volatile LAS unsigned* lf = (volatile LAS unsigned*)(lds + L_FLG);
    auto lf_set = [&](int i) { asm volatile("s_waitcnt lgkmcnt(0)" ::: "memory"); if (l == 0) lf[i] = tag; };
    auto lf_wait = [&](int i0, int n) {
      _Pragma("unroll 1") for (unsigned spins = 0; spins < (SPIN_CAP << 4); ++spins) {
        const unsigned v = lf[i0 + (l < n ? l : 0)];
        if (__all(v == tag)) break;
        __builtin_amdgcn_s_sleep(1);
      }
      asm volatile("" ::: "memory");
    };
    const rsrc_t rk = __builtin_amdgcn_make_buffer_rsrc((void*)p.k, 0, S_ * D_ * 2, 0x00020000);
    const rsrc_t rv = __builtin_amdgcn_make_buffer_rsrc((void*)p.v, 0, S_ * D_ * 2, 0x00020000);
    if (hf) {
      const rsrc_t rf = __builtin_amdgcn_make_buffer_rsrc((void*)(p.ssm_k + (size_t)(quad * 4 + ch) * S_), 0, S_ * 2, 0x00020000);
      u32x4 fa[4];
      u32x2 fb[4];
      __builtin_amdgcn_s_setprio(3);
#pragma unroll
      for (int k = 0; k < 4; ++k) {
        fa[k] = __builtin_amdgcn_raw_buffer_load_b128(rf, l16 + (unsigned)k * 1024u, 0, 0);
        fb[k] = __builtin_amdgcn_raw_buffer_load_b64(rf, l16 + (unsigned)k * 1024u + 16u, 0, 0);
      }
      __builtin_amdgcn_s_setprio(0);
      TAG_READY();
      SCHED();
#pragma unroll
      for (int k = 0; k < 4; ++k) {
        const unsigned x[6] = {fa[k][0], fa[k][1], fa[k][2], fa[k][3], fb[k][0], fb[k][1]};
        unsigned y[5];
#pragma unroll
        for (int d = 0; d < 5; ++d) y[d] = __builtin_amdgcn_alignbit(x[d + 1], x[d], 16);
        char* gp = G + 32 + k * 1024 + l16;
        *(u32x4*)(gp) = fa[k];
        *(u32x4*)(gp + G_COPY) = (u32x4){y[0], y[1], y[2], y[3]};
        *(u32x4*)(gp + 2 * G_COPY) = (u32x4){x[1], x[2], x[3], x[4]};
        *(u32x4*)(gp + 3 * G_COPY) = (u32x4){y[1], y[2], y[3], y[4]};
        if (k == 0) {
          const unsigned f0 = __builtin_amdgcn_readfirstlane(x[0]), f1 = __builtin_amdgcn_readfirstlane(x[1]);
          const unsigned long long f64 = (unsigned long long)f0 | ((unsigned long long)f1 << 32);
          const unsigned long long hi = l ? (f64 << (64 - 16 * (l & 3))) : 0ull;
          if (l < 4) {
            *(u32x4*)(G + l * G_COPY) = (u32x4){0u, 0u, 0u, 0u};
            *(u32x4*)(G + l * G_COPY + 16) = (u32x4){0u, 0u, (unsigned)hi, (unsigned)(hi >> 32)};
          }
          if (l >= 32) *(u32x4*)(U + (l - 32) * 16) = (u32x4){0u, 0u, 0u, 0u};
        }
      }
      lf_set(ch);
      STAMP(1);
    } else {
      __builtin_amdgcn_s_sleep(G_DELAY);
      __builtin_amdgcn_s_setprio(2);
    }
    u32x2 kk[7], vv[4], kh;
    auto gather_all = [&](bool first) {
      kh = __builtin_amdgcn_raw_buffer_load_b64(rk, w == 0 || l > 2 ? 0x80000000u : (unsigned)((256 * w - 3 + l) * D_ * 2 + quad * 8), 0, 0);
#pragma unroll
      for (int x = 0; x < 8; ++x) {
        if (x == 4 + B_GO) { SCHED(); if (first) TAG_READY(); if (l == 0) lf[24 + w] = tag; }
        SCHED();
        const unsigned so = (unsigned)(((x + idx) & 3) * D_ * 2 + quad * 8);
        if (x < 4) kk[3 + x] = __builtin_amdgcn_raw_buffer_load_b64(rk, (unsigned)(tid * 4 * D_ * 2), so, 0);
        else vv[x - 4] = __builtin_amdgcn_raw_buffer_load_b64(rv, (unsigned)(tid * 4 * D_ * 2), so, 0);
      }
      SCHED();
      STAMP(21);
    };
    auto unrot = [&](u32x2* x) {
      if (idx & 1) { asm volatile(""); const u32x2 t = x[3]; x[3] = x[2]; x[2] = x[1]; x[1] = x[0]; x[0] = t; }
      asm volatile("" : "+v"(x[0]), "+v"(x[1]), "+v"(x[2]), "+v"(x[3]));
      if (idx & 2) { const u32x2 t = x[0], u = x[1]; x[0] = x[2]; x[1] = x[3]; x[2] = t; x[3] = u; }
      asm volatile("" : "+v"(x[0]), "+v"(x[1]), "+v"(x[2]), "+v"(x[3]));
    };
    if (hf == 0) gather_all(true);
    const int ip = (tid & 255) + 512 * hf;
    const int ra = 32 * (ip >> 4) + 8 * ((ip >> 2) & 1) + 4 * ((ip >> 3) & 1) + (ip & 3);
    u32x4 qv[4], sv[4];
    u32x2 mp[2];
    const unsigned eo_s = (unsigned)(ra * 16), eo_q = (unsigned)((ra >> 3) * 20480 + (ra & 7) * 16);
    unsigned eo_m;
    {
      const int col = (n16 < 12 ? n16 >> 2 : 0) * D_ + quad * 4 + (l & 3);
      eo_m = (unsigned)col * 8u + (unsigned)h4 * 51200u;
    }
    auto ld_e = [&]() {
#pragma unroll
      for (int j = 0; j < 4; ++j) {
        const unsigned tb = 2 * (j & 1) + 64 * (j >> 1);
        qv[j] = __builtin_amdgcn_raw_buffer_load_b128(rs, eo_q, WS_QB + tb * 20480u + (unsigned)quad * 128u, SC1);
        sv[j] = __builtin_amdgcn_raw_buffer_load_b128(rs, eo_s, WS_ST1 + (unsigned)xcd * 32768u + tb * 128u, SC1);
        if (j < 2) mp[j] = __builtin_amdgcn_raw_buffer_load_b64(rs, eo_m, WS_MODP + (unsigned)j * 25600u, SC1);
      }
    };
    auto e_ok = [&]() {
      unsigned d = 0u;
#pragma unroll
      for (int j = 0; j < 4; ++j) {
        asm volatile("" : "+v"(qv[j]), "+v"(sv[j]), "+v"(mp[j & 1]));
        d |= ((sv[j][1] ^ tag) | (mp[j & 1][0] ^ tag)) | ((qv[j][1] ^ tag) | (qv[j][3] ^ tag));
        asm volatile("" : "+v"(d));
      }
      return __all(d == 0u);
    };
    f32x2 hh[8], qf[8], gam[2];
#define PIN8(x) asm volatile("" : "+v"(x[0]), "+v"(x[1]), "+v"(x[2]), "+v"(x[3]), "+v"(x[4]), "+v"(x[5]), "+v"(x[6]), "+v"(x[7]))
    auto epi_pre = [&]() {
      _Pragma("unroll 1") for (unsigned spins = 0; spins < SPIN_CAP; ++spins) {
        if (e_ok()) break;
        __builtin_amdgcn_s_sleep(4);
        ld_e();
      }
      float mgf = rbf(sum_rows(__uint_as_float(mp[0][1]) + __uint_as_float(mp[1][1])));
      mgf = (n16 >> 2) == 1 ? rbf(1.0f + mgf) : mgf;
      auto rl = [&](int i) { return __uint_as_float(__builtin_amdgcn_readlane(__float_as_uint(mgf), i)); };
      const f32x2 sa[2] = {{rl(0), rl(1)}, {rl(2), rl(3)}}, c1[2] = {{rl(4), rl(5)}, {rl(6), rl(7)}};
      gam[0] = (f32x2){rl(8), rl(9)}; gam[1] = (f32x2){rl(10), rl(11)};
#pragma unroll
      for (int i = 0; i < 8; ++i) hh[i] = rbf2((f32x2){bflo(qv[i >> 1][2 * (i & 1)]), bfhi(qv[i >> 1][2 * (i & 1)])} - bflo(sv[i >> 1][0]));
      PIN8(hh);
#pragma unroll
      for (int i = 0; i < 8; ++i) hh[i] = rbf2(hh[i] * __builtin_amdgcn_rcpf(bfhi(sv[i >> 1][0])));
      PIN8(hh);
#pragma unroll
      for (int i = 0; i < 8; ++i) hh[i] = rbf2(hh[i] * c1[i & 1]);
      PIN8(hh);
#pragma unroll
      for (int i = 0; i < 8; ++i) hh[i] = rbf2(hh[i] + sa[i & 1]);
      PIN8(hh);
    };
    const int a = l & 15, h = l >> 4, jj = h >> 1, e2 = h & 1;
    const char* ap = G + ((a + 1) & 3) * G_COPY + 2 * (16 * jj + a - 8 * e2 + 9 - ((a + 1) & 3));
    const char* bp = U + 512 + 32 * (a - jj) + 16 * e2 + 2048 * hf;
    auto lda = [&](u32x4* A, int r, int np) {
#pragma unroll
      for (int pp = 0; pp < np; ++pp) {
        u32x2 lo = *(const volatile LAS u32x2*)(ap + 512 * pp + 64 * r);
        u32x2 hi = *(const volatile LAS u32x2*)(ap + 512 * pp + 64 * r + 8);
        A[pp] = (u32x4){lo[0], lo[1], hi[0], hi[1]};
      }
    };
    auto ldb = [&](u32x4* B, int r) {
#pragma unroll
      for (int m = 0; m < 4; ++m) B[m] = *(const u32x4*)(bp + 512 * m - 64 * r);
    };
    auto kv_put = [&]() {
      __builtin_amdgcn_s_waitcnt(0x0F74);
      unrot(kk + 3);
#pragma unroll
      for (int j = 0; j < 3; ++j)
#pragma unroll
        for (int d = 0; d < 2; ++d)
          kk[j][d] = __builtin_amdgcn_update_dpp((unsigned)__builtin_amdgcn_readlane(kh[d], j), kk[4 + j][d], 0x138, 0xf, 0xf, false);
      const cptr_t sp = (cptr_t)(p.shift_k + quad * 16);
      char* up = lds + 512 + (tid >> 1) * 16 + (1 - (tid & 1)) * 8;
      STAMP(23);
      f32x2 oo[2][4];
#pragma unroll
      for (int d = 0; d < 2; ++d) {
        f32x2 kf[7], wt[4];
        f32x2* o = oo[d];
#pragma unroll
        for (int j = 0; j < 7; ++j) kf[j] = (f32x2){bflo(kk[j][d]), bfhi(kk[j][d])};
        wt[0] = (f32x2){bflo(sp[4 * d]), bflo(sp[4 * d + 2])}; wt[1] = (f32x2){bfhi(sp[4 * d]), bfhi(sp[4 * d + 2])};
        wt[2] = (f32x2){bflo(sp[4 * d + 1]), bflo(sp[4 * d + 3])}; wt[3] = (f32x2){bfhi(sp[4 * d + 1]), bfhi(sp[4 * d + 3])};
#pragma unroll
        for (int i = 0; i < 4; ++i) o[i] = rbf2(kf[i] * wt[3]);
#pragma unroll
        for (int t = 1; t < 4; ++t) {
          f32x2 pr[4];
#pragma unroll
          for (int i = 0; i < 4; ++i) pr[i] = rbf2(kf[i + t] * wt[3 - t]);
          asm volatile("" : "+v"(pr[0]), "+v"(pr[1]), "+v"(pr[2]), "+v"(pr[3]));
#pragma unroll
          for (int i = 0; i < 4; ++i) o[i] = rbf2(o[i] + pr[i]);
          asm volatile("" : "+v"(o[0]), "+v"(o[1]), "+v"(o[2]), "+v"(o[3]));
        }
      }
      SCHED();
      __builtin_amdgcn_s_waitcnt(0x0F70);
      unrot(vv);
#pragma unroll
      for (int d = 0; d < 2; ++d) {
        f32x2* o = oo[d];
#pragma unroll
        for (int i = 0; i < 4; ++i) o[i] *= (f32x2){bflo(vv[i][d]), bfhi(vv[i][d])};
#pragma unroll
        for (int e = 0; e < 2; ++e) *(u32x2*)(up + (2 * d + e) * CH_BYTES) = (u32x2){pack2(o[3][e], o[2][e]), pack2(o[1][e], o[0][e])};
      }
      lf_set(4 + w);
    };
    u32x4 AD;
    {
      const unsigned dDr = (*(cptr_t)(p.ssm_D + quad * 4 + (ch & 2)) >> (16 * (ch & 1))) & 0xffffu;
      const unsigned dv = h == (a >> 3) ? ((a & 1) ? dDr : dDr << 16) : 0u;
      const int dw = (7 - (a & 7)) >> 1;
#pragma unroll
      for (int d = 0; d < 4; ++d) AD[d] = dw == d ? dv : 0u;
    }
    f32x4* X = (f32x4*)(lds + L_X + ch * 4096) + l;
    char* Y = lds + L_Y + ch * 4096 + (16 * a + 4 * h) * 2 + 2048 * hf;
    if (hf == 0) {
      u32x4 A[3][8], B[2][4];
      f32x4 acc[8];
      lf_wait(ch, 1);
      lda(A[0], 0, 8);
      lda(A[1], 1, 8);
      SCHED();
      kv_put();
      STAMP(2);
      lf_wait(4, 4);
      STAMP(3);
      if (TCH & 2) { SCHED(); TOUCH(); SCHED(); }
      ldb(B[0], 0);
      ldb(B[1], 1);
      SCHED();
#pragma unroll
      for (int n = 0; n < 8; ++n) acc[n] = n < 4 ? MFMA16(AD, B[0][n], ((f32x4){0.f, 0.f, 0.f, 0.f})) : (f32x4){0.f, 0.f, 0.f, 0.f};
#pragma unroll
      for (int r = 0; r < 8; ++r) {
        if (r + 2 < 8) lda(A[(r + 2) % 3], r + 2, 8);
        SCHED();
#pragma unroll
        for (int m = 0; m < 4; ++m)
#pragma unroll
          for (int pp = 0; pp < 8 - m; ++pp) acc[m + pp] = MFMA16(A[r % 3][pp], B[r & 1][m], acc[m + pp]);
        SCHED();
        if (r + 2 < 8) ldb(B[r & 1], r + 2);
      }
      __builtin_amdgcn_s_setprio(0);
      ld_e();
      if (TCH & 1) { SCHED(); TOUCH(); SCHED(); }
#pragma unroll
      for (int n = 0; n < 4; ++n) X[n * 64] = acc[4 + n];
      lf_set(12 + ch);
#pragma unroll
      for (int n = 0; n < 4; ++n) *(u32x2*)(Y + n * 512) = (u32x2){pack2(acc[n][0], acc[n][1]), pack2(acc[n][2], acc[n][3])};
      lf_set(16 + w);
      STAMP(4);
      epi_pre();
    } else {
      u32x4 A[8][4], B[2][4];
      f32x4 acc[4];
      lf_wait(24, 4);
      gather_all(false);
      __builtin_amdgcn_s_setprio(3);
#pragma unroll
      for (int r = 0; r < 8; ++r) lda(A[r], r, 4);
      SCHED();
      kv_put();
      ld_e();
      if (TCH & 3) { SCHED(); TOUCH(); SCHED(); }
      STAMP(2);
      lf_wait(7, 5);
      STAMP(3);
      ldb(B[0], 0);
      ldb(B[1], 1);
      SCHED();
#pragma unroll
      for (int n = 0; n < 4; ++n) acc[n] = MFMA16(AD, B[0][n], ((f32x4){0.f, 0.f, 0.f, 0.f}));
#pragma unroll
      for (int r = 0; r < 8; ++r) {
        SCHED();
#pragma unroll
        for (int m = 0; m < 4; ++m)
#pragma unroll
          for (int pp = 0; pp < 4 - m; ++pp) acc[m + pp] = MFMA16(A[r][pp], B[r & 1][m], acc[m + pp]);
        SCHED();
        if (r + 2 < 8) ldb(B[r & 1], r + 2);
      }
      STAMP(4);
      lf_wait(12 + ch, 1);
      f32x4 o4[4];
#pragma unroll
      for (int n = 0; n < 4; ++n) o4[n] = X[n * 64];
      SCHED();
#pragma unroll
      for (int n = 0; n < 4; ++n)
        *(u32x2*)(Y + n * 512) = (u32x2){pack2(o4[n][0] + acc[n][0], o4[n][1] + acc[n][1]), pack2(o4[n][2] + acc[n][2], o4[n][3] + acc[n][3])};
      lf_set(16 + w);
      epi_pre();
    }
    if (w == 3) wait_weights();
    STAMP(24);
    lf_wait(16 + 4 * hf, 4);
    STAMP(26);
    {
      unsigned yy[16];
#pragma unroll
      for (int i = 0; i < 16; ++i) yy[i] = *(const u16*)(lds + L_Y + (i & 3) * 4096 + (ra + 16 * ((i >> 2) & 1) + 512 * (i >> 3)) * 2);
      SCHED();
#pragma unroll
      for (int i = 0; i < 8; ++i) hh[i] = rbf2(hh[i] * (f32x2){bflo(yy[2 * i]), bflo(yy[2 * i + 1])});
      PIN8(hh);
#pragma unroll
      for (int i = 0; i < 8; ++i) hh[i] = rbf2(hh[i] * gam[i & 1]);
      PIN8(hh);
#pragma unroll
      for (int i = 0; i < 8; ++i) hh[i] += (f32x2){bflo(qv[i >> 1][2 * (i & 1)]), bfhi(qv[i >> 1][2 * (i & 1)])};
#pragma unroll
      for (int j = 0; j < 2; ++j) {
        u32x4 o;
#pragma unroll
        for (int d = 0; d < 4; ++d) o[d] = pack2(hh[4 * j + d][0], hh[4 * j + d][1]);
        const int i = ip + 256 * j;
        st16(rs, WS_X1B + (unsigned)(i >> 3) * 20480u + (unsigned)quad * 128u + (unsigned)(i & 7) * 16u, o);
      }
    }
    __builtin_amdgcn_s_setprio(0);
    SCHED();
    STAMP(28);
    BAR();
    if (w < 7) dma_b2(0, NB2A);
    asm volatile("s_waitcnt vmcnt(%0)" ::"n"(NB2A) : "memory");
    if (w == 7) DRAIN();
    TOUCH_END();
    __builtin_amdgcn_s_barrier();
    asm volatile("" ::: "memory");
    if (w == 7) st16(rs, WS_FX8 + (unsigned)((l >> 3) * 160 + quad) * 128u + (unsigned)(l & 7) * 16u, (u32x4){tag, tag, tag, tag});
    else __builtin_amdgcn_s_sleep(FLAG_GAP);
    STAMP(5);
    if (w < 7) dma_b2(NB2A, 20);
  } else {
    float* red = (float*)lds;
    auto hold = [&](unsigned tt = AUX_T) { while ((unsigned)__builtin_amdgcn_s_memrealtime() - t_start < tt) __builtin_amdgcn_s_sleep(2); };
    const int ag = xcd * 2 + idx - 30;
    const int jb = idx < 31 ? ((idx - 20) * 8 + xcd) * 3 + w - 5 : 256;
    if (w >= 5 && jb < 256) {
      const rsrc_t rq = __builtin_amdgcn_make_buffer_rsrc((void*)p.q, 0, S_ * D_ * 2, 0x00020000);
      char* qt = lds + L_RT + w * 18432;
      u32x4 ld[10];
      __builtin_amdgcn_s_setprio(QPRIO);
#pragma unroll
      for (int j = 0; j < 10; ++j)
        ld[j] = __builtin_amdgcn_raw_buffer_load_b128(rq, (unsigned)((l >> 3) * D_ * 2 + (l & 7) * 16 + j * 128), (unsigned)(jb * 8 * D_ * 2), 0);
      STAMP(21);
      TAG_READY();
#pragma unroll
      for (int j = 0; j < 10; ++j) *(u32x4*)(qt + (l >> 3) * 1312 + (l & 7) * 16 + j * 128) = ld[j];
      float sa = 0.f, va = 0.f;
#pragma unroll
      for (int j = 0; j < 10; ++j)
#pragma unroll
        for (int d = 0; d < 4; ++d) {
          const unsigned a0 = ld[j][d];
          const bf2_t xa = __builtin_bit_cast(bf2_t, a0);
          sa = __builtin_amdgcn_fdot2_f32_bf16(xa, ONE2, sa, false); va = __builtin_amdgcn_fdot2_f32_bf16(xa, xa, va, false);
        }
      const float ma = sum8(sa) * (1.0f / D_);
      va = __builtin_fmaxf(sum8(va) * (1.0f / D_) - ma * ma, 0.f);
      const unsigned md = __builtin_amdgcn_ds_bpermute((l & 7) * 32, pack2(ma, __builtin_amdgcn_sqrtf(rbf(rbf(va) + 1e-5f))));
      st16(rs, WS_ST1 + (unsigned)(l >> 3) * 32768u + (unsigned)jb * 128u + (unsigned)(l & 7) * 16u, (u32x4){md, tag, md, tag});
      asm volatile("s_waitcnt lgkmcnt(0)" ::: "memory");
      STAMP(23);
#pragma unroll
      for (int j = 0; j < 20; ++j) {
        const u32x2 x = *(const u32x2*)(qt + (l & 7) * 1312 + (l >> 3) * 8 + j * 64);
        __builtin_amdgcn_raw_buffer_store_b128((u32x4){x[0], tag, x[1], tag}, rs, WS_QB + (unsigned)jb * 20480u + (unsigned)j * 1024u + l16, 0, SC1);
      }
      __builtin_amdgcn_s_setprio(0);
      STAMP(22);
    } else if (idx >= 30 || w >= 5) TAG_READY();

    if (idx < 30) {
      if (w < 5) {
        u32x4 pg[11];
        __builtin_amdgcn_s_sleep(M_DELAY);
        const int blk = (w < 2 ? 4 - w : w - 2) * 10 + (a1 >> 3);
        gemv_load(pg, blk, a1 & 7);
        TAG_READY();
        gemv_put(pg, blk, a1 & 7);
        STAMP(25);
        if (w >= 2) hold(RT2_T);
        rt_job(w < 4 ? (w >> 1) * 160 + a1 * 2 + (w & 1) : 320 + a1);
        STAMP(26);
      }
    } else if (ag < 10) {
      float acc[8];
      u32x4 pg[11];
      hold();
      gemv_load(pg, 50 + ag, w);
      gemv_fin(acc, pg);
      if (l < 8) {
        *(f32x4*)(red + w * 64 + l * 8) = (f32x4){acc[0], acc[1], acc[2], acc[3]};
        *(f32x4*)(red + w * 64 + l * 8 + 4) = (f32x4){acc[4], acc[5], acc[6], acc[7]};
      }
      BAR();
      if (w == 4) {
        float t = 0.f;
#pragma unroll
        for (int r = 0; r < 8; ++r) t += red[r * 64 + l];
        st8(rs, WS_MOD + (unsigned)((50 + ag) * 64 + l) * 8u, (u32x2){tag, __float_as_uint(rbf(t))});
      }
      STAMP(2);
    }
    STAMP(3);
    if (TCH & 4) { hold(TCH_T); TOUCH(); }
    hold(W_T);
    if (w == 7) wait_weights();
    TOUCH_END();
    BAR();
    STAMP(4);
    if (idx >= 30 && (w == 5 || w == 6)) {
      u32x4 v[20];
      hold(CS_T);
      _Pragma("unroll 1") for (unsigned spins = 0; spins < SPIN_CAP; ++spins) {
        unsigned dd = 0u;
#pragma unroll
        for (int k = 0; k < 20; ++k) v[k] = ld16(rs, WS_CSP + (unsigned)((ag * 20 + k) * 2 + w - 5) * 1024u + l16);
#pragma unroll
        for (int k = 0; k < 20; ++k) dd |= (v[k][0] ^ tag) | (v[k][2] ^ tag);
        if (__all(dd == 0u)) break;
        __builtin_amdgcn_s_sleep(16);
      }
      float t0 = 0.f, t1 = 0.f;
#pragma unroll
      for (int k = 0; k < 20; ++k) { t0 += __uint_as_float(v[k][1]); t1 += __uint_as_float(v[k][3]); }
      st16(rs, WS_CS + (unsigned)(ag * 2 + w - 5) * 1024u + l16, (u32x4){tag, __float_as_uint(t0), tag, __float_as_uint(t1)});
      STAMP(24);
    }
    if (w < 7) dma_b2(0, 20);
    if (idx >= 24) poison_u();
  }

  u32x4 k_c;
  unsigned kc_o;
  {
    const int i = tid < 224 ? tid : 0;
    const int col = i >= 112 ? (8 + c) * 128 + i - 112 : c * 128 + i;
    kc_o = w < 4 ? WS_CS + (unsigned)col * 16u : WS_MOD + (unsigned)(5 * D_ + 80 * c + 2 * (tid >= 256 && tid < 296 ? tid - 256 : 0)) * 8u;
  }
  auto consts_put = [&]() {
    if (!__all(k_c[0] == tag && k_c[2] == tag)) {
      _Pragma("unroll 1") for (unsigned spins = 0; spins < SPIN_CAP; ++spins) {
        __builtin_amdgcn_s_sleep(8);
        k_c = ld16(rs, kc_o);
        if (__all(k_c[0] == tag && k_c[2] == tag)) break;
      }
    }
    if (tid < 224) *(u32x2*)(lds + L_CONST + tid * 8) = (u32x2){k_c[1], k_c[3]};
    if (tid >= 256 && tid < 296) *(u32x2*)(lds + L_MISC + (tid - 256) * 8) = (u32x2){k_c[1], k_c[3]};
  };
  const int rt1 = w & 3, kh1 = w >> 2;
  unsigned bo0 = (unsigned)(kh1 * 71680) + l16, bo1 = bo0 + 35840u;
  asm volatile("" : "+v"(bo0), "+v"(bo1));
#define BFRAG(q, j) (*(const u32x4*)(lds + ((q) < 5 ? bo0 : bo1) + ((q) % 5) * 7168 + (j) * 1024))
  unsigned long long prc2;
  unsigned prt2;
  u32x4 h2[10];
  f32x4 acc[7];
  float mu_n, rs_n;
  {
    if (w == 6) {
      const unsigned fo[1] = {l < 20 ? WS_FWI + (unsigned)(c * 20 + l) * 128u : l < 28 ? WS_FWO + (unsigned)(c * 8 + l - 20) * 128u : ~0u};
      wait_lines<1, 8>(rs, fo, tag);
    }
    if (w == 7) {
      const unsigned fb = WS_FX8 + (unsigned)(xcd * 160 + l) * 128u;
      const unsigned fo[3] = {fb, fb + 8192u, l < 32 ? fb + 16384u : ~0u};
      wait_lines<3, FX_SLEEP>(rs, fo, tag);
    }
    BAR();
    prc2 = __builtin_amdgcn_s_memtime();
    prt2 = (unsigned)__builtin_amdgcn_s_memrealtime();
    STAMP(7);
    const unsigned xo = (unsigned)(R * 4 + rt1) * 20480u + (unsigned)(10 * kh1) * 1024u +
                        (unsigned)((4 * ((l >> 3) & 1) + h4) * 128 + (l & 7) * 16);
#pragma unroll
    for (int q = 0; q < 10; ++q) h2[q] = ld16p(rs, WS_X1B + xo + (unsigned)q * 1024u);
    SCHED();
    __builtin_amdgcn_s_waitcnt(0x0F7A);
    BAR();
    STAMP(8);
#pragma unroll
    for (int j = 0; j < 7; ++j) acc[j] = (f32x4){0.f, 0.f, 0.f, 0.f};
    f32x4 accg = {0.f, 0.f, 0.f, 0.f};
    u32x4 bf[7];
#pragma unroll
    for (int j = 0; j < 7; ++j) bf[j] = BFRAG(0, j);
#pragma unroll
    for (int q = 0; q < 10; ++q) {
      {
        const u32x4 x = h2[q];
        h2[q][0] = __builtin_amdgcn_update_dpp(x[0], x[2], 0x128, 0xf, 0xC, false);
        h2[q][1] = __builtin_amdgcn_update_dpp(x[1], x[3], 0x128, 0xf, 0xC, false);
        h2[q][2] = __builtin_amdgcn_update_dpp(x[2], x[0], 0x128, 0xf, 0x3, false);
        h2[q][3] = __builtin_amdgcn_update_dpp(x[3], x[1], 0x128, 0xf, 0x3, false);
      }
      if (q == 5) { __builtin_amdgcn_s_barrier(); asm volatile("" ::: "memory"); }
      SCHED();
      accg = MFMA16(h2[q], h2[q], accg);
#pragma unroll
      for (int j = 0; j < 7; ++j) {
        acc[j] = MFMA16(h2[q], bf[j], acc[j]);
        if (q < 9) bf[j] = BFRAG(q + 1, j);
        if (q >= 2 && j == 1) LDW(2 * q - 4);
        if (q >= 2 && j == 3) LDW(2 * q - 3);
        if (q == 7 && j == 0) k_c = ld16(rs, kc_o);
        if (q >= 5 && j == 5) {
          const int i = (5 + q) * 7 + (w < 7 ? w : 0);
          dma1k(rs, lds + i * 1024, l16, WS_WINF + (unsigned)c * WBLK + (unsigned)i * 1024u);
          if (q == 9) { const int i2 = 28 + (w < 7 ? w : 0); dma1k(rs, lds + i2 * 1024, l16, WS_WINF + (unsigned)c * WBLK + (unsigned)i2 * 1024u); }
        }
        SCHED();
      }
    }
    STAMP(31);
    {
      if (n16 == 12) *(f32x4*)(lds + L_PST + ((kh1 * 4 + rt1) * 16 + 4 * h4) * 4) = acc[6];
      const float dg = (n16 & 2) ? ((n16 & 1) ? accg[3] : accg[2]) : ((n16 & 1) ? accg[1] : accg[0]);
      if (h4 == (n16 >> 2)) *(float*)(lds + L_PST + 512 + ((kh1 * 4 + rt1) * 16 + n16) * 4) = dg;
      consts_put();
    }
  }
  auto gelu_n = [&](f32x2* x, auto N_) {
    constexpr int N = decltype(N_)::value;
    const float c0 = -2.0f * 0.7978845608028654f * 1.4426950408889634f, c1 = c0 * 0.044715f;
    f32x2 k0 = {c0, c0}, k1 = {c1, c1}, k2 = {1.0f, 1.0f};
    asm volatile("" : "+v"(k0), "+v"(k1), "+v"(k2));
#pragma unroll
    for (int i = 0; i < N; ++i) {
      f32x2 t = __builtin_elementwise_fma(x[i] * x[i], k1, k0) * x[i];
      asm("" : "+v"(t));
      t = (f32x2){__builtin_amdgcn_exp2f(t[0]), __builtin_amdgcn_exp2f(t[1])} + k2;
      asm("" : "+v"(t));
      x[i] *= (f32x2){__builtin_amdgcn_rcpf(t[0]), __builtin_amdgcn_rcpf(t[1])};
    }
  };
  {
    f32x2* red = (f32x2*)lds;
    f32x2 mine[7];
    if (kh1) {
#pragma unroll
      for (int j = 0; j < 7; ++j) { red[(w * 7 + j) * 64 + l] = (f32x2){acc[j][0], acc[j][1]}; mine[j] = (f32x2){acc[j][2], acc[j][3]}; }
    } else {
#pragma unroll
      for (int j = 0; j < 7; ++j) { red[(w * 7 + j) * 64 + l] = (f32x2){acc[j][2], acc[j][3]}; mine[j] = (f32x2){acc[j][0], acc[j][1]}; }
    }
#pragma unroll
    for (int k = 16; k < 24; ++k) LDW(k);
    BAR();
    __builtin_amdgcn_s_setprio(3);
    STAMP(9);
    f32x2 oth[7];
    f32x2 csb[7];
    f32x2 pa[3], pb[3];
    {
      const float* ps = (const float*)(lds + L_PST);
#pragma unroll
      for (int e = 0; e < 3; ++e) {
        const int row = rt1 * 16 + (e < 2 ? 4 * h4 + 2 * kh1 + e : n16);
        pa[e] = (f32x2){ps[row], ps[128 + row]}; pb[e] = (f32x2){ps[64 + row], ps[192 + row]};
      }
    }
#pragma unroll
    for (int j = 0; j < 7; ++j) { oth[j] = red[((w ^ 4) * 7 + j) * 64 + l]; csb[j] = *(const f32x2*)(lds + L_CONST + (112 + 16 * j + n16) * 8); }
    SCHED();
    float mu2[2], rs2[2];
#pragma unroll
    for (int e = 0; e < 3; ++e) {
      const float mean = (pa[e][0] + pb[e][0]) * (1.0f / D_);
      const float var = __builtin_fmaxf((pa[e][1] + pb[e][1]) * (1.0f / D_) - mean * mean, 0.f);
      const float rstd = __builtin_amdgcn_rsqf(var + 1e-5f);
      if (e < 2) { mu2[e] = mean; rs2[e] = rstd; } else { mu_n = mean; rs_n = rstd; }
    }
    f32x2 zz[7];
    const f32x2 nmu = {-mu2[0], -mu2[1]}, rsv = {rs2[0], rs2[1]};
#pragma unroll
    for (int j = 0; j < 7; ++j)
      zz[j] = __builtin_elementwise_fma(__builtin_elementwise_fma(nmu, (f32x2){csb[j][0], csb[j][0]}, mine[j] + oth[j]), rsv, (f32x2){csb[j][1], csb[j][1]});
    gelu_n(zz, IC<7>());
    if (n16 >= 12) zz[6] = (f32x2){0.f, 0.f};
    f32x2 s1 = zz[0], s2 = zz[0] * zz[0];
#pragma unroll
    for (int j = 1; j < 7; ++j) { s1 += zz[j]; s2 = __builtin_elementwise_fma(zz[j], zz[j], s2); }
    asm("" : "+v"(s1), "+v"(s2));
    float a0 = (l & 1) ? s1[1] : s1[0], a1 = (l & 1) ? s1[0] : s1[1], b0 = (l & 1) ? s2[1] : s2[0], b1 = (l & 1) ? s2[0] : s2[1];
    a0 += dpp_f<0xB1>(a1); b0 += dpp_f<0xB1>(b1);
    a0 += dpp_f<0x4E>(a0); b0 += dpp_f<0x4E>(b0);
    a0 += dpp_f<0x124>(a0); b0 += dpp_f<0x124>(b0);
    a0 += dpp_f<0x128>(a0); b0 += dpp_f<0x128>(b0);
    {
      const int src = ((l >> 1) & 3) * 64 + (l & 1) * 4;
      const unsigned pa0 = __builtin_amdgcn_ds_bpermute(src, __float_as_uint(a0)), pb0 = __builtin_amdgcn_ds_bpermute(src, __float_as_uint(b0));
      if (l < 8) st16(rs, WS_PART + (unsigned)((R * 8 + c) * 8 + w) * 128u + l16, (u32x4){tag, pa0, tag, pb0});
    }
    SCHED();
    __builtin_amdgcn_s_setprio(0);
    STAMP(10);
#pragma unroll
    for (int k = 0; k < 10; ++k) {
      const int i = (k < 5 ? 5 + k : 10 + k) * 7 + w;
      if (w < 7) dma1k(rs, lds + i * 1024, l16, WS_WINF + (unsigned)c * WBLK + (unsigned)i * 1024u);
    }
#pragma unroll
    for (int j = 0; j < 7; ++j)
      *(unsigned*)(lds + L_Z + (((rt1 >> 1) * 7 + j) * 64 + l) * 16 + (4 * (rt1 & 1) + 2 * kh1) * 2) = pack2(zz[j][0], zz[j][1]);
    SCHED();
#pragma unroll
    for (int g = 0; g < 4; ++g) WSP_TR(g);
    if (w == 3) {
      __builtin_amdgcn_s_setprio(3);
      u32x4 v[8];
      const unsigned po = WS_PART + (unsigned)(R * 8) * 1024u + l16;
      unsigned need = 0xffu;
      _Pragma("unroll 1") for (unsigned spins = 0; spins < SPIN_CAP; ++spins) {
#pragma unroll
        for (int k = 0; k < 8; ++k)
          if (need & (1u << k)) v[k] = ld16(rs, po + (unsigned)k * 1024u);
#pragma unroll
        for (int k = 0; k < 8; ++k)
          if (__all(v[k][0] == tag && v[k][2] == tag)) need &= ~(1u << k);
        if (need == 0u) break;
        __builtin_amdgcn_s_sleep(1);
      }
      float t1 = 0.f, t2 = 0.f;
#pragma unroll
      for (int k = 0; k < 8; ++k) { t1 += __uint_as_float(v[k][1]); t2 += __uint_as_float(v[k][3]); }
      const float mean = t1 * (1.0f / FH);
      const float var = __builtin_fmaxf(t2 * (1.0f / FH) - mean * mean, 0.f);
      const float rstd = __builtin_amdgcn_rsqf(var + 1e-5f);
      const int sg = 16 * ((l >> 3) & 3) + 4 * ((l >> 1) & 3) + 2 * (l >> 5) + (l & 1);
      *(f32x2*)(lds + L_ST2 + sg * 8) = (f32x2){rstd, -mean * rstd};
    }
    STAMP(11);
    DRAIN();
    BAR();
    __builtin_amdgcn_s_setprio(3);
    STAMP(12);
    u32x4 zf[2];
#pragma unroll
    for (int m = 0; m < 2; ++m) zf[m] = *(const u32x4*)(lds + L_Z + ((m * 7 + (w < 7 ? w : 0)) * 64 + l) * 16);
    if (w < 7) {
#pragma unroll
      for (int m = 0; m < 2; ++m) {
        const f32x4* sp = (const f32x4*)(lds + L_ST2 + (32 * m + 4 * h4) * 8);
        u32x4 o;
#pragma unroll
        for (int e = 0; e < 4; ++e) {
          f32x4 st = sp[(e & 1) + 8 * (e >> 1)];
          o[e] = pack2(__builtin_fmaf(bflo(zf[m][e]), st[0], st[1]), __builtin_fmaf(bfhi(zf[m][e]), st[2], st[3]));
        }
        st16(rs, WS_Z2F + (unsigned)((c * 64 + 4 * (R >> 1) + h4) * 7 + w) * 1024u + (unsigned)((2 * (R & 1) + m) * 256 + n16 * 16), o);
      }
    }
    SCHED();
    __builtin_amdgcn_s_setprio(0);
#pragma unroll
    for (int k = 0; k < 4; ++k)
      if (w < 7) dma1k(rs, lds + (k * 7 + w) * 1024, l16, WS_WINF + (unsigned)c * WBLK + (unsigned)(k * 7 + w) * 1024u);
    SCHED();
    STAMP(13);
  }
  unsigned bs_raw;
  u32x2 xp[3];
  constexpr int NST = 5, STG = 28672;
#define SLOT(t) (((t) % NST) * STG)
  const unsigned sZ = WS_Z2F + (unsigned)(c * 64) * 7168u + (unsigned)w * 1024u;
  auto issue = [&](int t, int k) { dma1k(rs, lds + SLOT(t) + k * 7168 + w * 1024, l16, sZ + (unsigned)(16 * k + ((t + rot) & 15)) * 7168u); };
  {
    f32x4 acc[7];
#pragma unroll
    for (int j = 0; j < 7; ++j) acc[j] = (f32x4){0.f, 0.f, 0.f, 0.f};
    u32x4 bf[7];
#pragma unroll
    for (int j = 0; j < 7; ++j) bf[j] = BFRAG(4, j);
#pragma unroll
    for (int qi = 0; qi < 10; ++qi) {
      const int q = (qi + 4) % 10, qn = (qi + 5) % 10;
      if (qi == 5) {
        DRAIN();
        __builtin_amdgcn_s_barrier();
        asm volatile("" ::: "memory");
        if (w == 7) st_flag(rs, WS_FZ + (unsigned)(c * 32 + R) * 128u, tag, l);
        STAMP(6);
#pragma unroll
        for (int k = 24; k < 32; ++k) LDW(k);
        bs_raw = p.b_sp[R * 64 + tau(2 * rp + (kq & 1), n16)];
#pragma unroll
        for (int j = 0; j < 3; ++j) {
          const int dj = (w >> 2) ? (3 + (j < 2 ? j : 1)) : j;
          const int d0 = 80 * c + 16 * dj + 4 * h4;
          xp[j] = ld8p(rs, WS_X1B + (unsigned)(R * 4 + (w & 3)) * 20480u + (unsigned)(d0 >> 2) * 128u + (unsigned)psi(n16) * 8u);
        }
      }
      SCHED();
#pragma unroll
      for (int j = 0; j < 7; ++j) {
        acc[j] = MFMA16(bf[j], h2[q], acc[j]);
        if (qi < 9) bf[j] = BFRAG(qn, j);
        SCHED();
      }
    }
    STAMP(29);
    f32x4* red = (f32x4*)(lds + 100352);
    f32x4 m4[4];
    if (kh1 == 0) {
#pragma unroll
      for (int j = 0; j < 3; ++j) red[(w * 4 + j) * 64 + l] = acc[4 + j];
#pragma unroll
      for (int j = 0; j < 4; ++j) m4[j] = acc[j];
    } else {
#pragma unroll
      for (int j = 0; j < 4; ++j) red[(w * 4 + j) * 64 + l] = acc[j];
#pragma unroll
      for (int j = 0; j < 4; ++j) m4[j] = acc[4 + (j < 3 ? j : 2)];
    }
    WSP_TR(4);
    WSP_TR(5);
    if (w == 3) {
      const unsigned fo[1] = {l < 32 ? WS_FZ + (unsigned)(c * 32 + l) * 128u : ~0u};
      wait_lines<1, 1>(rs, fo, tag);
    }
    DRAIN();
    BAR();
    STAMP(14);
    auto issue_q = [&](int t) {
#pragma unroll
      for (int j = 0; j < 7; ++j)
        dma1k(rs, lds + SLOT(t) + (w & 3) * 7168 + j * 1024, l16, WS_Z2F + (unsigned)(c * 64 + 16 * (w & 3) + ((t + rot) & 15)) * 7168u + (unsigned)j * 1024u);
    };
    if (w >= 4) { issue_q(0); issue_q(1); }
    SCHED();
    f32x4 o4[4], c01[4], c23[4];
#pragma unroll
    for (int j = 0; j < 4; ++j) {
      const int jj = (kh1 && j == 3) ? 2 : j, jt = 4 * kh1 + jj;
      o4[j] = red[((w ^ 4) * 4 + jj) * 64 + l];
      c01[j] = *(const f32x4*)(lds + L_CONST + (16 * jt + 4 * h4) * 8);
      c23[j] = *(const f32x4*)(lds + L_CONST + (16 * jt + 4 * h4 + 2) * 8);
    }
    SCHED();
    f32x2 z[8];
#pragma unroll
    for (int i = 0; i < 8; ++i) {
      const int j = i >> 1, r = 2 * (i & 1);
      const f32x4 cc = r ? c23[j] : c01[j];
      z[i] = __builtin_elementwise_fma(__builtin_elementwise_fma((f32x2){-mu_n, -mu_n}, (f32x2){cc[0], cc[2]}, (f32x2){o4[j][r], o4[j][r + 1]} + (f32x2){m4[j][r], m4[j][r + 1]}),
                                       (f32x2){rs_n, rs_n}, (f32x2){cc[1], cc[3]});
    }
    SCHED();
    gelu_n(z, IC<6>());
    if (kh1 == 0) gelu_n(z + 6, IC<2>());
#pragma unroll
    for (int j = 0; j < 4; ++j)
      if (kh1 == 0 || j < 3) *(u32x2*)(lds + L_Z + (((kh1 ? 4 + j : j) * 4 + rt1) * 64 + l) * 8) = (u32x2){pack2(z[2 * j][0], z[2 * j][1]), pack2(z[2 * j + 1][0], z[2 * j + 1][1])};
    SCHED();
    if (w < 4) issue_q(2);
    STAMP(30);
  }

  f32x4 acc2[2][7];
  {
    SCHED();
    WSP_TR(6);
    WSP_TR(7);
#pragma unroll
    for (int i = 0; i < 2; ++i)
#pragma unroll
      for (int j = 0; j < 7; ++j) acc2[i][j] = (f32x4){0.f, 0.f, 0.f, 0.f};
    const char* rd = lds + kq * 7168 + l16;
#define VMW(n) asm volatile("s_waitcnt vmcnt(%0)" ::"n"((n) * 4) : "memory")
#define RBAR() do { __builtin_amdgcn_s_barrier(); asm volatile("" ::: "memory"); } while (0)
    SCHED();
    if (w >= 4) DRAIN();
    BAR();
    if (w < 7) {
#pragma unroll
      for (int k = 0; k < 4; ++k) issue(3, k);
    }
    STAMP(20);
    u32x4 fr[7];
#pragma unroll
    for (int j = 0; j < 7; ++j) fr[j] = *(const u32x4*)(rd + j * 1024);
    u32x4 wo[18];
#define LDO(k) wo[k] = ld16p(rs, WS_WOUTF + (unsigned)c * WBLK + (unsigned)((k) < 17 || w < 4 ? w + 8 * (k) : w) * 1024u + l16)
#pragma unroll
    for (int t = 0; t < 15; ++t) {
      SCHED();
      if (t < 13) VMW(2);
      else if (t == 13) asm volatile("s_waitcnt vmcnt(10)" ::: "memory");
      else asm volatile("s_waitcnt vmcnt(12)" ::: "memory");
      RBAR();
      SCHED();
#pragma unroll
      for (int j = 0; j < 7; ++j) {
        acc2[0][j] = MFMA16(fr[j], wsp[0][t], acc2[0][j]);
        acc2[1][j] = MFMA16(fr[j], wsp[1][t], acc2[1][j]);
        fr[j] = *(const u32x4*)(rd + SLOT(t + 1) + j * 1024);
        if (t + 4 < 16 && j < 4 && w < 7) issue(t + 4, j);
        if (t >= 12 && j < 6) LDO(6 * (t - 12) + j);
        SCHED();
      }
    }
    f32x4* red = (f32x4*)(lds + STG) + rp * (42 * 64) + l;
    f32x4 own[4];
    auto last = [&](auto KQ_) {
      constexpr int KQ = decltype(KQ_)::value;
      constexpr int ord[7] = {KQ < 2 ? 2 : 0, KQ < 2 ? 3 : 1, KQ < 2 ? 6 : 4, KQ < 2 ? 0 : 5, KQ < 2 ? 1 : 2, KQ < 2 ? 4 : 3, KQ < 2 ? 5 : 6};
#pragma unroll
      for (int x = 0; x < 9; ++x) {
        if (x < 7) {
          const int j = ord[x];
          acc2[1 - (KQ & 1)][j] = MFMA16(fr[j], wsp[1 - (KQ & 1)][15], acc2[1 - (KQ & 1)][j]);
          acc2[KQ & 1][j] = MFMA16(fr[j], wsp[KQ & 1][15], acc2[KQ & 1][j]);
        }
        if (x >= 2) {
          const int j = ord[x - 2];
#pragma unroll
          for (int i = 0; i < 2; ++i) {
            const int ko = i + 2 * ((j >> 1) & 1);
            if (KQ != ko) red[((i * 7 + j) * 3 + (KQ > ko ? KQ - 1 : KQ)) * 64] = acc2[i][j];
          }
        }
        SCHED();
      }
#pragma unroll
      for (int x = 0; x < 4; ++x) {
        const int j = 2 * (KQ >> 1) + 4 * (x >> 1) + (x & 1);
        own[x] = acc2[KQ & 1][j < 7 ? j : 6];
      }
    };
    if (kq == 0) last(IC<0>());
    else if (kq == 1) last(IC<1>());
    else if (kq == 2) last(IC<2>());
    else last(IC<3>());
    STAMP(15);
    const int io = kq & 1, jb = kq >> 1;
    const int rto = 2 * rp + io;
    u32x2 z[4];
#pragma unroll
    for (int x = 0; x < 4; ++x) {
      const int j = 2 * jb + 4 * (x >> 1) + (x & 1);
      z[x] = *(const u32x2*)(lds + L_Z + (((j < 7 ? j : 6) * 4 + rto) * 64 + l) * 8);
    }
    f32x4 gbv[3];
#pragma unroll
    for (int j = 0; j < 3; ++j) gbv[j] = *(const f32x4*)(lds + L_MISC + (16 * ((w >> 2) ? 3 + (j < 2 ? j : 1) : j) + 4 * h4) * 4);
    BAR();
    {
      f32x4 r3[4][3];
#pragma unroll
      for (int x = 0; x < 4; ++x) {
        const int j = 2 * jb + 4 * (x >> 1) + (x & 1);
#pragma unroll
        for (int k = 0; k < 3; ++k) r3[x][k] = red[((io * 7 + (j < 7 ? j : 6)) * 3 + k) * 64];
      }
      SCHED();
      const float bs = bflo(bs_raw);
      const unsigned ub = WS_UF + (unsigned)((R * 4 + rto) * 8 + c) * 3584u;
      u32x4 o[2];
#pragma unroll
      for (int x = 0; x < 4; ++x) {
        const f32x4 g = (own[x] + r3[x][0]) + (r3[x][1] + r3[x][2]) + bs;
        o[x >> 1][2 * (x & 1)] = pack2(bflo(z[x][0]) * g[0], bfhi(z[x][0]) * g[1]);
        o[x >> 1][2 * (x & 1) + 1] = pack2(bflo(z[x][1]) * g[2], bfhi(z[x][1]) * g[3]);
      }
      st16(rs, ub + (unsigned)jb * 1024u + l16, o[0]);
      if (jb == 0) st16(rs, ub + 2048u + l16, o[1]);
      else st8(rs, ub + 3072u + (unsigned)l * 8u, h4 == 3 ? (u32x2){0u, 0u} : (u32x2){o[1][0], o[1][1]});
    }
    SCHED();
    BAR();
    STAMP(16);

    const int rt = w & 3, kh = w >> 2;
    u32x4 uf[4][3];
    u32x2 uh[4];
    const unsigned ub = WS_UF + (unsigned)((R * 4 + rt) * 8 + kh) * 3584u;
    auto ld_u = [&](int cc) {
#pragma unroll
      for (int qq = 0; qq < 3; ++qq) uf[cc][qq] = ld16(rs, ub + (unsigned)cc * 7168u + (unsigned)qq * 1024u + l16);
      uh[cc] = ld8(rs, ub + (unsigned)cc * 7168u + 3072u + (unsigned)l * 8u);
    };
    auto u_ok = [&](int cc) {
      unsigned m = __builtin_elementwise_max(uh[cc][0], uh[cc][1]);
#pragma unroll
      for (int qq = 0; qq < 3; ++qq) {
        m = __builtin_elementwise_max(m, __builtin_elementwise_max(uf[cc][qq][0], uf[cc][qq][1]));
        m = __builtin_elementwise_max(m, __builtin_elementwise_max(uf[cc][qq][2], uf[cc][qq][3]));
      }
      return __all(m != ~0u);
    };
#pragma unroll
    for (int k = 0; k < 18; ++k) {
      if (k == U_AT) {
        SCHED();
        ld_u(0);
        ld_u(1);
        SCHED();
      }
      if (k < 17 || w < 4) *(u32x4*)(lds + (w + 8 * k) * 1024 + l16) = wo[k];
    }
    BAR();
    STAMP(17);
    f32x4 acc[5];
#pragma unroll
    for (int j = 0; j < 5; ++j) acc[j] = (f32x4){0.f, 0.f, 0.f, 0.f};
    unsigned wb0 = (unsigned)(kh * 17920) + l16, wb1 = wb0 + 71680u, wh0 = (unsigned)(kh * 17920 + 15360 + l * 8), wh1 = wh0 + 71680u;
    asm volatile("" : "+v"(wb0), "+v"(wb1), "+v"(wh0), "+v"(wh1));
    u32x4 af[2][15];
    u32x2 ah[2][5];
    auto g3ld = [&](int cc) {
#pragma unroll
      for (int i = 0; i < 15; ++i) af[cc & 1][i] = *(const u32x4*)(lds + (cc < 2 ? wb0 : wb1) + (cc & 1) * 35840 + i * 1024);
#pragma unroll
      for (int dj = 0; dj < 5; ++dj) ah[cc & 1][dj] = *(const u32x2*)(lds + (cc < 2 ? wh0 : wh1) + (cc & 1) * 35840 + dj * 512);
    };
    g3ld(0);
    SCHED();
    ld_u(2);
    ld_u(3);
    SCHED();
    g3ld(1);
#pragma unroll
    for (int cc = 0; cc < 4; ++cc) {
      SCHED();
      if (!u_ok(cc)) {
        _Pragma("unroll 1") for (unsigned spins = 0; spins < SPIN_CAP; ++spins) {
          __builtin_amdgcn_s_sleep(1);
          ld_u(cc);
          if (u_ok(cc)) break;
        }
      }
      SCHED();
#pragma unroll
      for (int i = 0; i < 15; ++i) {
        acc[i % 5] = MFMA16(af[cc & 1][i], uf[cc][i / 5], acc[i % 5]);
        if (cc < 2) af[cc & 1][i] = *(const u32x4*)(lds + wb1 + (cc & 1) * 35840 + i * 1024);
        SCHED();
      }
#pragma unroll
      for (int dj = 0; dj < 5; ++dj) {
        acc[dj] = MFMA16K16(ah[cc & 1][dj], uh[cc], acc[dj]);
        if (cc < 2) ah[cc & 1][dj] = *(const u32x2*)(lds + wh1 + (cc & 1) * 35840 + dj * 512);
        SCHED();
      }
    }
    STAMP(18);
    f32x4* X = (f32x4*)(lds + WBLK) + rt * (5 * 64) + l;
    f32x4 m4[3];
    if (kh == 0) {
      X[3 * 64] = acc[3]; X[4 * 64] = acc[4];
      m4[0] = acc[0]; m4[1] = acc[1]; m4[2] = acc[2];
    } else {
      X[0] = acc[0]; X[64] = acc[1]; X[128] = acc[2];
      m4[0] = acc[3]; m4[1] = acc[4]; m4[2] = acc[4];
    }
    BAR();
    if (bid == 0 && tid == 320) st4(rs, WS_EPOCH, tag);
    const int t = R * 64 + tau(rt, n16);
    f32x4 o4[3];
#pragma unroll
    for (int j = 0; j < 3; ++j) o4[j] = X[(kh ? 3 + (j < 2 ? j : 1) : j) * 64];
    SCHED();
#pragma unroll
    for (int j = 0; j < 3; ++j) {
      const f32x4 v = rbf4(gbv[j] * rbf4(o4[j] + m4[j]));
      const u32x2 o = {pack2(bflo(xp[j][0]) + v[0], bfhi(xp[j][0]) + v[1]), pack2(bflo(xp[j][1]) + v[2], bfhi(xp[j][1]) + v[3])};
      if (j < 2 || kh == 0)
        __builtin_amdgcn_raw_buffer_store_b64(o, __builtin_amdgcn_make_buffer_rsrc((void*)p.out, 0, S_ * D_ * 2, 0x00020000),
                                              (unsigned)(t * D_ + 80 * c + 16 * (kh ? 3 + j : j) + 4 * h4) * 2u, 0, 0);
    }
  }
  if (w == 0) {
    unsigned ref = PROBE == 2 ? t_start : PROBE == 3 ? pr3 : (unsigned)pr0;
    unsigned dl = 6000u;
    if (PROBE == 7) { ref = t_start; dl = (unsigned)(__builtin_amdgcn_s_memtime() - prc) / 10u; }
    if (PROBE == 8) { ref = (unsigned)__builtin_amdgcn_s_memrealtime(); }
    if (PROBE == 9) { ref = t_start; dl = 4000u + (unsigned)(prc2 - prc) / 10u; }
    if (PROBE == 10) { ref = prt2; }
    if (PROBE == 4) { ref = ld4(rs, 2048u); ref = __builtin_amdgcn_readfirstlane(ref); dl = 8000u; }
    if (PROBE == 6) { if (bid == 0 && l == 0) st4(rs, 2048u, ref); }
    else while ((unsigned)__builtin_amdgcn_s_memrealtime() - ref < dl) __builtin_amdgcn_s_sleep(1);
  }
  STAMP(19);
}

__global__ void k1(char* ws) {
  unsigned t = (unsigned)__builtin_amdgcn_s_memrealtime();
  if (PROBE == 6) {
    const unsigned ref = *(volatile unsigned*)(ws + 2048);
    while ((unsigned)__builtin_amdgcn_s_memrealtime() - ref < 8000u) __builtin_amdgcn_s_sleep(1);
  } else if (threadIdx.x == 0) *(volatile unsigned*)(ws + 2048) = t;
}
extern "C" __device__ __attribute__((used, noinline, visibility("hidden"))) void mega_tail() {}

extern "C" void kernel_launch(void* const* d_in, const int* in_sizes, int n_in, void* d_out, int out_size,
                              void* d_ws, size_t ws_size, hipStream_t stream) {
  Params p;
  p.q = (const u16*)d_in[0];
  p.k = (const u16*)d_in[1];
  p.v = (const u16*)d_in[2];
  p.shift_k = (const u16*)d_in[3];
  p.ssm_k = (const u16*)d_in[4];
  p.ssm_D = (const u16*)d_in[5];
  p.w_in = (const u16*)d_in[7];
  p.w_sp = (const u16*)d_in[8];
  p.b_sp = (const u16*)d_in[9];
  p.w_out = (const u16*)d_in[10];
  p.cvec = (const u16*)d_in[11];
  p.w_mod = (const u16*)d_in[12];
  p.out = (u16*)d_out;
  p.ws = (char*)d_ws;
  if (PROBE == 4 || PROBE == 5) k1<<<1, 64, 0, stream>>>(p.ws);
  mega<<<256, 512, 0, stream>>>(p);
  if (PROBE == 6) k1<<<1, 64, 0, stream>>>(p.ws);
}
```

```cpp
#define YG_T 900
#include <hip/hip_runtime.h>
#include <stdint.h>

typedef unsigned short u16;
typedef __attribute__((ext_vector_type(8))) short bf16x8;
typedef __attribute__((ext_vector_type(4))) short bf16x4;
typedef __attribute__((ext_vector_type(4))) float f32x4;
typedef __attribute__((ext_vector_type(2))) float f32x2;
typedef __attribute__((ext_vector_type(4))) unsigned int u32x4;
typedef __attribute__((ext_vector_type(2))) unsigned int u32x2;
#define LAS __attribute__((address_space(3)))
typedef __amdgpu_buffer_rsrc_t rsrc_t;
typedef __attribute__((ext_vector_type(4))) short s16x4;

constexpr int S_ = 2048, D_ = 640, F_ = 1728, FH = 864, CB = 108;

constexpr unsigned WS_EPOCH = 0;
constexpr unsigned WS_FWI = 4096;
constexpr unsigned WS_FZ = WS_FWI + 384 * 128;
constexpr unsigned WS_FWO = WS_FZ + 256 * 128;
constexpr unsigned F4S = 896 * 128;
constexpr unsigned WS_CS = WS_FZ + 256 * 128;
constexpr unsigned WS_MOD = 131072;
constexpr unsigned WS_MODP = WS_MOD + 81920;
constexpr unsigned WS_ST1 = 393216;
constexpr unsigned WS_PART = 524288;
constexpr unsigned WS_CSP = WS_PART + 262144;
constexpr unsigned WS_FX = WS_CSP + 655360;
constexpr unsigned WS_X1B = WS_FX + 16 * 4 * 160 * 128;
constexpr unsigned WS_QB = WS_X1B + 2621440;
constexpr unsigned WS_KV3 = WS_QB + 2621440;
constexpr unsigned WS_MODQ = WS_KV3 + 1310720;
constexpr unsigned WBLK = 143360;
constexpr unsigned WS_WINF = WS_MODQ + 245760;
constexpr unsigned WS_WOUTF = WS_WINF + 16 * WBLK;
constexpr unsigned WS_Z2F = WS_WOUTF + 8 * WBLK;
constexpr unsigned WS_UF = WS_Z2F + 8 * 64 * 7168;
constexpr unsigned WS_F4 = WS_UF + 32 * 4 * 8 * 4096;
constexpr unsigned WS_QCK = WS_F4 + 4 * F4S;
constexpr unsigned WS_END = WS_QCK + 131072;
constexpr unsigned WS_STAMP = 200u << 20;

constexpr int G_COPY = 4160;
constexpr int L_U = WBLK;
constexpr int U_CH = (256 + S_) * 2 + 32;
constexpr int L_FLG = L_U + 4 * U_CH;
constexpr int L_RT = 16384;
constexpr int L_CONST = WBLK;
constexpr int L_PST = L_CONST + 2048;
constexpr int L_Z = L_CONST + 5120;
constexpr int L_ST2 = L_Z + 14336;
constexpr int L_MISC = L_ST2 + 512;
constexpr int LDS_TOTAL = 163840;

#define AUX_T 610
#define RT2_T 360
#define CB_T 380
#define W_T 700
#define BG 2
#define BK 12
#define YW 4
#define NOPK 1
#define ME 0
#define MPR 3
#define G2E 1
#define QP 0
#define B0 12
#define QC 1
#define KE_T 0
#define QG 0
#define QA 4
#define QM 6
#define GE 0
#define YPR 0
#define YSL 2
#define K_T 480
#ifndef M_T
#define M_T 0
#endif
#define WA_T 580
constexpr int SC1 = 16, NT = 2;
constexpr unsigned SPIN_CAP = 1u << 16;

__device__ __forceinline__ float bflo(unsigned x) { return __uint_as_float(x << 16); }
__device__ __forceinline__ float bfhi(unsigned x) { return __uint_as_float(x & 0xffff0000u); }
__device__ __forceinline__ unsigned f2bf(float x) {
  __bf16 b = (__bf16)x;
  return (unsigned)__builtin_bit_cast(unsigned short, b);
}
__device__ __forceinline__ float rbf(float x) { return bflo(f2bf(x)); }
typedef __attribute__((ext_vector_type(2))) __bf16 bf2_t;
__device__ __forceinline__ unsigned pack2(float lo, float hi) {
  f32x2 v = {lo, hi};
  return __builtin_bit_cast(unsigned, __builtin_convertvector(v, bf2_t));
}
__device__ __forceinline__ f32x2 rbf2(f32x2 v) {
  const unsigned p = __builtin_bit_cast(unsigned, __builtin_convertvector(v, bf2_t));
  return (f32x2){__uint_as_float(p << 16), __uint_as_float(p & 0xffff0000u)};
}
__device__ __forceinline__ f32x4 rbf4(f32x4 v) {
  const f32x2 a = rbf2((f32x2){v[0], v[1]}), b = rbf2((f32x2){v[2], v[3]});
  return (f32x4){a[0], a[1], b[0], b[1]};
}
__device__ __forceinline__ f32x4 fma4(f32x4 a, f32x4 b, f32x4 c) {
  float r[4];
  asm("v_fma_f32 %0, %4, %8, %12\n\tv_fma_f32 %1, %5, %9, %13\n\tv_fma_f32 %2, %6, %10, %14\n\tv_fma_f32 %3, %7, %11, %15"
      : "=&v"(r[0]), "=&v"(r[1]), "=&v"(r[2]), "=&v"(r[3])
      : "v"(a[0]), "v"(a[1]), "v"(a[2]), "v"(a[3]), "v"(b[0]), "v"(b[1]), "v"(b[2]), "v"(b[3]), "v"(c[0]), "v"(c[1]), "v"(c[2]), "v"(c[3]));
  return (f32x4){r[0], r[1], r[2], r[3]};
}
template <int CTRL>
__device__ __forceinline__ float dpp_f(float v) {
  return __int_as_float(__builtin_amdgcn_update_dpp(0, __float_as_int(v), CTRL, 0xf, 0xf, true));
}
__device__ __forceinline__ float sum8(float v) {
  v += dpp_f<0xB1>(v);
  v += dpp_f<0x4E>(v);
  v += dpp_f<0x141>(v);
  return v;
}
__device__ __forceinline__ float sum16(float v) {
  v = sum8(v);
  v += dpp_f<0x140>(v);
  return v;
}
__device__ __forceinline__ float sum_rows(float v) {
  auto a = __builtin_amdgcn_permlane16_swap(__float_as_uint(v), __float_as_uint(v), false, false);
  v = __uint_as_float(a[0]) + __uint_as_float(a[1]);
  auto b = __builtin_amdgcn_permlane32_swap(__float_as_uint(v), __float_as_uint(v), false, false);
  return __uint_as_float(b[0]) + __uint_as_float(b[1]);
}
#define MFMA16(a, b, c) __builtin_amdgcn_mfma_f32_16x16x32_bf16(__builtin_bit_cast(bf16x8, a), __builtin_bit_cast(bf16x8, b), c, 0, 0, 0)
#define MFMA_IP(c, a, b) asm volatile("v_mfma_f32_16x16x32_bf16 %0, %1, %2, %0" : "+v"(c) : "v"(a), "v"(b))
#define MFMA_0(c, a, b) asm volatile("v_mfma_f32_16x16x32_bf16 %0, %1, %2, 0" : "=&v"(c) : "v"(a), "v"(b))
#define MFMA16K16(a, b, c) __builtin_amdgcn_mfma_f32_16x16x16bf16_1k(__builtin_bit_cast(bf16x4, a), __builtin_bit_cast(bf16x4, b), c, 0, 0, 0)
__device__ __forceinline__ u32x2 trd(const char* p) {
  return __builtin_bit_cast(u32x2, __builtin_amdgcn_ds_read_tr16_b64_v4i16((LAS s16x4*)p));
}
__device__ __forceinline__ u32x4 trfrag(const char* p, int pitch) {
  u32x2 a = trd(p), b = trd(p + 16 * pitch);
  return (u32x4){a[0], a[1], b[0], b[1]};
}
#define BAR() asm volatile("s_waitcnt lgkmcnt(0)\n\ts_barrier" ::: "memory")
#define DRAIN() do { __builtin_amdgcn_s_waitcnt(0x0F70); asm volatile("" ::: "memory"); } while (0)
#define SCHED() __builtin_amdgcn_sched_barrier(0)
template <int N> struct IC { static constexpr int value = N; };
template <int I, int N, class F>
__device__ __forceinline__ void sfor(F&& f) {
  if constexpr (I < N) { f(IC<I>()); sfor<I + 1, N>(f); }
}

__device__ __forceinline__ u32x4 ld16(rsrc_t r, unsigned off) { return __builtin_amdgcn_raw_buffer_load_b128(r, off, 0, SC1); }
__device__ __forceinline__ u32x2 ld8(rsrc_t r, unsigned off) { return __builtin_amdgcn_raw_buffer_load_b64(r, off, 0, SC1); }
__device__ __forceinline__ unsigned ld4(rsrc_t r, unsigned off) { return __builtin_amdgcn_raw_buffer_load_b32(r, off, 0, SC1); }
#define ST_AUX 16
__device__ __forceinline__ void st16(rsrc_t r, unsigned off, u32x4 v) { __builtin_amdgcn_raw_buffer_store_b128(v, r, off, 0, ST_AUX); }
#define ST16S(v, r, voff, soff, aux) do { const u32x4 v_ = (v); __builtin_amdgcn_raw_buffer_store_b128(v_, r, voff, soff, aux); asm volatile("s_nop 1" :: "v"(v_)); } while (0)
__device__ __forceinline__ void st8(rsrc_t r, unsigned off, u32x2 v) { __builtin_amdgcn_raw_buffer_store_b64(v, r, off, 0, SC1); }
__device__ __forceinline__ void st_flag4(rsrc_t r, unsigned off, unsigned tag, int l) {
  if (l < 32) st16(r, WS_F4 - WS_FWI + off + (unsigned)(l >> 3) * F4S + (unsigned)(l & 7) * 16u, (u32x4){tag, tag, tag, tag});
}
__device__ __forceinline__ void st4(rsrc_t r, unsigned off, unsigned v) { __builtin_amdgcn_raw_buffer_store_b32(v, r, off, 0, SC1); }
#define BULK_AUX 16
__device__ __forceinline__ u32x4 ld16p(rsrc_t r, unsigned off) { return __builtin_amdgcn_raw_buffer_load_b128(r, off, 0, BULK_AUX); }
__device__ __forceinline__ u32x2 ld8p(rsrc_t r, unsigned off) { return __builtin_amdgcn_raw_buffer_load_b64(r, off, 0, BULK_AUX); }
__device__ __forceinline__ void dma1k(rsrc_t r, char* lds_dst, unsigned l16, unsigned soff) {
  __builtin_amdgcn_raw_ptr_buffer_load_lds(r, (LAS void*)lds_dst, 16, l16, soff, 0, BULK_AUX);
}

#define STAMP(i)
#define STAMPF(i)
#define STAMPT(i) STAMP(i)

template <int N, int SLP>
__device__ __forceinline__ void wait_lines(rsrc_t r, const unsigned (&off)[N], unsigned tag) {
  bool need[N];
#pragma unroll
  for (int i = 0; i < N; ++i) need[i] = off[i] != ~0u;
  _Pragma("unroll 1") for (unsigned spins = 0; spins < SPIN_CAP; ++spins) {
    unsigned v[N];
#pragma unroll
    for (int i = 0; i < N; ++i) {
      v[i] = tag;
      if (need[i]) v[i] = ld4(r, off[i]);
    }
    bool any = false;
#pragma unroll
    for (int i = 0; i < N; ++i) { need[i] = v[i] != tag; any = any || need[i]; }
    if (!__any(any)) break;
    __builtin_amdgcn_s_sleep(SLP);
  }
  asm volatile("" ::: "memory");
}

__device__ __forceinline__ int tau(int rt, int n) { return 32 * (rt >> 1) + 8 * (n >> 2) + 4 * (rt & 1) + (n & 3); }
__device__ __forceinline__ int psi(int n) { return 2 * (n & 7) + (n >> 3); }
#define QX_CM(d, s0, s1, P) "v_cndmask_b32_dpp " d ", " s0 ", " s1 ", vcc quad_perm:" P " row_mask:0xf bank_mask:0xf\n\t"
#define QX_BODY(M, P) \
  asm("s_mov_b32 vcc_lo, " M "\n\ts_mov_b32 vcc_hi, " M "\n\t" \
      QX_CM("%0", "%12", "%8", P) "v_xor_b32 %4, %8, %0\n\t" QX_CM("%1", "%13", "%9", P) "v_xor_b32 %5, %9, %1\n\t" \
      QX_CM("%2", "%14", "%10", P) "v_xor_b32 %6, %10, %2\n\t" QX_CM("%3", "%15", "%11", P) "v_xor_b32 %7, %11, %3" \
      : "=&v"(t[0]), "=&v"(t[1]), "=&v"(t[2]), "=&v"(t[3]), "=&v"(u[0]), "=&v"(u[1]), "=&v"(u[2]), "=&v"(u[3]) \
      : "v"(a[0]), "v"(a[1]), "v"(a[2]), "v"(a[3]), "v"(b[0]), "v"(b[1]), "v"(b[2]), "v"(b[3]) : "vcc")
template <int BIT>
__device__ __forceinline__ void quad_xchg3(u32x4& a, u32x4& b) {
  unsigned t[4], u[4];
  if (BIT == 0) QX_BODY("0x55555555", "[1,0,3,2]");
  else QX_BODY("0x33333333", "[2,3,0,1]");
  a = (u32x4){t[0], t[1], t[2], t[3]};
#pragma unroll
  for (int d = 0; d < 4; ++d) b[d] ^= (unsigned)__builtin_amdgcn_update_dpp(0, (int)u[d], BIT == 0 ? 0xB1 : 0x4E, 0xf, 0xf, true);
}
__device__ __forceinline__ void quad_transpose(u32x4& x0, u32x4& x1, u32x4& x2, u32x4& x3) {
  quad_xchg3<0>(x0, x1);
  quad_xchg3<0>(x2, x3);
  quad_xchg3<1>(x0, x2);
  quad_xchg3<1>(x1, x3);
  asm volatile("" : "+v"(x0), "+v"(x1), "+v"(x2), "+v"(x3));
}

struct Params {
  const u16 *q, *k, *v, *shift_k, *ssm_k, *ssm_D, *w_in, *w_sp, *b_sp, *w_out, *cvec, *w_mod;
  u16* out;
  char* ws;
};

__global__ __launch_bounds__(512, 2) void mega(Params p) {
  __shared__ __attribute__((aligned(16))) char lds[LDS_TOTAL];
  {
    unsigned long long pc0;
    unsigned vt;
    asm volatile("v_readfirstlane_b32 vcc_lo, %3\n\ts_cmpk_lt_u32 %2, 192\n\ts_cbranch_scc1 4f\n\ts_cmpk_lt_u32 vcc_lo, 64\n\ts_cbranch_scc0 4f\n\t"
                 "s_lshr_b32 vcc_hi, %2, 3\n\ts_sub_u32 vcc_hi, vcc_hi, 24\n\ts_lshl_b32 vcc_hi, vcc_hi, 13\n\t"
                 "v_lshlrev_b32 %1, 7, %3\n\tv_add_u32 %1, vcc_hi, %1\n\ts_getpc_b64 %0\n.Lmega_here:\n\t"
                 "v_min_u32 %1, mega_tail-.Lmega_here-4, %1\n\ts_movk_i32 m0, 0x2000\n\ts_nop 1\n\tglobal_load_lds_dword %1, %0\n4:\n\t"
                 "s_cmpk_lt_u32 %2, 160\n\ts_cbranch_scc0 1f\n\ts_cmpk_lt_u32 vcc_lo, 256\n\ts_cbranch_scc1 3f\n\t"
                 "s_call_b64 vcc, .Ltouch1\n\ts_branch 3f\n1:\n\t"
                 "s_cmpk_lt_u32 vcc_lo, 256\n\ts_cbranch_scc1 2f\n\ts_cmpk_lt_u32 %2, 224\n\ts_cbranch_scc0 2f\n\ts_call_b64 vcc, .Ltouch3\n\ts_branch 3f\n"
                 "2:\n\ts_call_b64 vcc, .Ltouch2\n3:"
                 : "=&s"(pc0), "=&v"(vt) : "s"(blockIdx.x), "v"(threadIdx.x) : "vcc", "scc", "memory");
  }
  SCHED();
  const int tid = threadIdx.x, l = tid & 63;
  const int w = __builtin_amdgcn_readfirstlane(tid >> 6);
  const int bid = blockIdx.x, xcd = bid & 7, idx = bid >> 3;
#define TOUCH_AT(n) asm volatile("s_branch 1f\n.Ltouch" #n ":\n\ts_setpc_b64 vcc\n1:")
  char* const ws = p.ws;
  asm volatile("" ::"s"(p.q), "s"(p.k), "s"(p.v), "s"(p.shift_k), "s"(p.ssm_k), "s"(p.ssm_D), "s"(p.w_in), "s"(p.w_sp), "s"(p.b_sp),
               "s"(p.w_out), "s"(p.cvec), "s"(p.w_mod), "s"(p.out), "s"(p.ws));
  unsigned epoch, tag, t_start;
  unsigned long long t64;
  asm volatile("s_load_dword %0, %2, 0x0\n\ts_memrealtime %1" : "=&s"(epoch), "=&s"(t64) : "s"(ws));
#define TAG_READY() do { SCHED(); \
    asm volatile("s_waitcnt lgkmcnt(0)\n\ts_mul_i32 %0, %2, 0x9e3779b1\n\ts_add_i32 %0, %0, 0x9e3779b9\n\ts_mov_b64 %1, %3" \
                 : "=&s"(tag), "=&s"(t64r) : "s"(epoch), "s"(t64) : "scc"); t_start = (unsigned)t64r; SCHED(); } while (0)
  unsigned long long t64r;
  const rsrc_t rs = __builtin_amdgcn_make_buffer_rsrc(ws, 0, WS_END, 0x00020000);
  const unsigned l16 = (unsigned)l * 16u;
  STAMP(0);
  const bool is_conv = idx < 20;
  const int quad = xcd * 20 + idx;
  const int R = is_conv ? (xcd & 3) * 5 + (idx >> 2) : (xcd & 3) * 3 + 15 + (idx >> 2);
  const int c = (xcd >> 2) * 4 + (idx & 3);
  const unsigned fwi = WS_F4 + (unsigned)(xcd & 3) * F4S, fz = fwi + (WS_FZ - WS_FWI);
  const int n16 = l & 15, h4 = l >> 4;
  u32x4 wsp[4][8];
#define ROTC 2
#define ROTZ 3
#define WAUX 0
  const int rotg = (((ROTZ & 1) ? idx >> ((ROTZ & 4) ? 2 : 4) : 0) ^ ((ROTC & 2) ? idx >> 1 : 0)) & 1, rotb = (ROTZ & 2) ? (idx >> 2) & 3 : 0, rotr = (ROTC & 1) ? (idx & 1) * 2 : 0;
  const rsrc_t rsp = __builtin_amdgcn_make_buffer_rsrc((void*)p.w_sp, 0, S_ * S_ * 2, 0x00020000);
  const unsigned wsp_vo = (unsigned)(((l >> 2) & 3) * 8 * 4096 + h4 * 64 + ((l + rotb) & 3) * 16);
#define LDW(k) LDW_(k)
#define WSP_TR(g) WSP_TR_(g)
#define WROW R
#define LDW_(k) wsp[((k) >> 2) & 3][((k) >> 4) * 4 + ((k) & 3)] = __builtin_amdgcn_raw_buffer_load_b128(rsp, wvo, \
    (unsigned)((WROW * 64 + 16 * ((((k) >> 2) & 2) ^ rotr) + 4 * (((k) >> 2) & 1) + ((k) & 3)) * 4096 + w * 512 + ((((k) >> 4) ^ rotg) * 256)), WAUX)
#define WSP_TR_(g) quad_transpose(wsp[(g) & 3][((g) >> 2) * 4], wsp[(g) & 3][((g) >> 2) * 4 + 1], wsp[(g) & 3][((g) >> 2) * 4 + 2], wsp[(g) & 3][((g) >> 2) * 4 + 3])
#define WSP_Q(g, k) wsp[(g) & 3][(((g) >> 2) & 1) * 4 + (k)]
#define WSP_TX(x) do { if (((x) & 2) == 0) quad_xchg3<0>(WSP_Q((x) >> 2, 2 * ((x) & 1)), WSP_Q((x) >> 2, 2 * ((x) & 1) + 1)); \
    else quad_xchg3<1>(WSP_Q((x) >> 2, (x) & 1), WSP_Q((x) >> 2, ((x) & 1) + 2)); \
    asm volatile("" : "+v"(WSP_Q((x) >> 2, 0)), "+v"(WSP_Q((x) >> 2, 1)), "+v"(WSP_Q((x) >> 2, 2)), "+v"(WSP_Q((x) >> 2, 3))); } while (0)
  const rsrc_t rwi = __builtin_amdgcn_make_buffer_rsrc((void*)p.w_in, 0, D_ * F_ * 2, 0x00020000);
  const rsrc_t rwo = __builtin_amdgcn_make_buffer_rsrc((void*)p.w_out, 0, FH * D_ * 2, 0x00020000);
  const rsrc_t rq = __builtin_amdgcn_make_buffer_rsrc((void*)p.q, 0, S_ * D_ * 2, 0x00020000);
  auto hold = [&](unsigned tt) { while ((unsigned)__builtin_amdgcn_s_memrealtime() - t_start < tt) __builtin_amdgcn_s_sleep(2); };
  auto ne2 = [&](unsigned x, unsigned y) { unsigned d; asm("v_bitop3_b32 %0, %1, %2, %3 bitop3:0x7e" : "=v"(d) : "v"(x), "v"(y), "s"(tag)); return d; };
  auto sum8u = [&](unsigned v) {
    v += (unsigned)__builtin_amdgcn_update_dpp(0, (int)v, 0xB1, 0xf, 0xf, true);
    v += (unsigned)__builtin_amdgcn_update_dpp(0, (int)v, 0x4E, 0xf, 0xf, true);
    return v + (unsigned)__builtin_amdgcn_update_dpp(0, (int)v, 0x141, 0xf, 0xf, true);
  };
  volatile LAS unsigned* lf = (volatile LAS unsigned*)(lds + L_FLG);
  auto lf_set = [&](int i) { asm volatile("s_waitcnt lgkmcnt(0)" ::: "memory"); if (l == 0) lf[i] = tag; };
  auto lf_wait = [&](int i0, int n) {
    _Pragma("unroll 1") for (unsigned spins = 0; spins < (SPIN_CAP << 4); ++spins) {
      const unsigned v = lf[i0 + (l & (n - 1))];
      if (__all(v == tag)) break;
      __builtin_amdgcn_s_sleep(1);
    }
    asm volatile("" ::: "memory");
  };
  const unsigned b2o = fwi + (unsigned)((8 + c) * 20 + 5 * (w & 3) + (l < 5 ? l : 0)) * 128u;
  auto b2_wait = [&](u32x2 fv, int slp) {
    if (!__all(fv[0] == tag)) {
      _Pragma("unroll 1") for (unsigned spins = 0; spins < SPIN_CAP; ++spins) {
        if (fv[0] != tag) fv = ld8(rs, b2o);
        if (__all(fv[0] == tag)) break;
        if (slp) __builtin_amdgcn_s_sleep(8); else __builtin_amdgcn_s_sleep(4);
      }
    }
    asm volatile("" ::: "memory");
    if (l < 5) lf[44 + 5 * w + l] = fv[1];
    if (l == 0) lf[36 + w] = tag;
  };
  auto load_b2 = [&](int k0, int k1) {
    const unsigned a0 = (unsigned)(35 * w + (idx >> 2) * 4) * 1024u, gb = WS_WINF + (unsigned)(8 + c) * WBLK;
    unsigned a = a0, g = gb + a0;
#pragma unroll
    for (int k = k0; k < k1; ++k) {
      if (k == k0 || (k & 3) == 3) {
        a = k + (idx >> 2) * 4 >= 35 ? a0 - 35840u : a0;
        g = gb + a;
        asm volatile("" : "+s"(a), "+s"(g));
      }
      dma1k(rs, lds + a + k * 1024, l16, g + (unsigned)k * 1024u);
    }
  };
  char* const wo_st = lds + 122880;
  auto wo_ld = [&]() {
    const int U = R < 28 ? R : 0, u2 = U >> 1, fi = (u2 + 1) % 3;
    const int c0 = 4 * (U & 1) + (u2 < 8 ? u2 >> 1 : u2 < 11 ? 0 : 2);
    const int b0 = u2 < 8 ? CB * c0 + 2 * (u2 & 1) + 96 : CB * (c0 + (fi >> 1)) + 48 + 2 * (fi & 1), d1 = u2 < 8 ? -96 : 16, d2 = d1 + 16;
    const int b3 = u2 < 8 ? b0 - 64 : CB * (c0 + 1) + 50 + 16 * fi;
#pragma unroll
    for (int r = 0; r < 5; ++r) {
      const int g = r * 64 + l, rho = g / 10;
      const int fb = (rho & 16) ? ((rho & 2) ? b3 : b0 + d2) : ((rho & 2) ? b0 + d1 : b0);
      __builtin_amdgcn_raw_ptr_buffer_load_lds(rwo, (LAS void*)(wo_st + r * 1024), 16, (unsigned)((fb + (rho & 13)) * (D_ * 2) + (g % 10) * 16 + c * 160), 0, 0, 0);
    }
  };
  auto rt_ld = [&](int i0, char* st) {
    const int cb = (i0 < 160 ? 8 : 0) + (i0 & 7), Q = (i0 < 160 ? i0 : i0 - 160) >> 3;
#pragma unroll
    for (int r = 0; r < 7; ++r) {
      const int g = r * 64 + l;
      __builtin_amdgcn_raw_ptr_buffer_load_lds(rwi, (LAS void*)(st + r * 1024), 16, (unsigned)((g / 14) * (F_ * 2) + (g % 14) * 16 + Q * 32 * F_ * 2 + cb * CB * 2),
                                               0, 0, NT);
    }
  };
  auto rt_job = [&](int i0, char* st) {
    const int cb = (i0 < 160 ? 8 : 0) + (i0 & 7), Q = (i0 < 160 ? i0 : i0 - 160) >> 3;
    const int col = 640 * (l >> 5) + 32 * Q + (l & 31);
    DRAIN();
    u32x4 x[7], o[7];
    f32x2 xu[7][4];
    {
      const char* bp = st + (4 * h4 + (n16 >> 2)) * 224 + (n16 & 3) * 8;
#pragma unroll
      for (int j = 0; j < 7; ++j) x[j] = trfrag(bp + j * 32, 224);
#pragma unroll
      for (int j = 0; j < 7; ++j)
#pragma unroll
        for (int e = 0; e < 4; ++e) { xu[j][e] = (f32x2){bflo(x[j][e]), bfhi(x[j][e])}; asm volatile("" : "+v"(xu[j][e])); }
    }
    unsigned mk[4][2], pv = (unsigned)col * 8u, cw = l >= 32 ? (unsigned)(7168 + (l - 32) * 4) : (unsigned)(7298 + l * 2 - 4), m12 = n16 == 12 ? ~0u : 0u;
    unsigned fgo = l < 32 ? (unsigned)(l >> 3) * F4S + (unsigned)(l & 7) * 16u : 0x80000000u;
#pragma unroll
    for (int i = 0; i < 4; ++i) {
      mk[i][0] = n16 == 4 * i ? 0x3F803F80u : 0u; mk[i][1] = n16 == 4 * i + 1 ? ~0u : 0u;
      asm volatile("" : "+v"(mk[i][0]), "+v"(mk[i][1]));
    }
    asm volatile("" : "+v"(pv), "+v"(cw), "+v"(m12), "+v"(fgo));
    unsigned ob = WS_WINF + (unsigned)cb * WBLK + (unsigned)(Q * 7) * 1024u;
    asm volatile("" : "+s"(ob));
    u32x2 pp8[8];
    hold(CB_T);
    _Pragma("unroll 1") for (unsigned spins = 0; spins < SPIN_CAP; ++spins) {
#pragma unroll
      for (int i = 0; i < 8; ++i) pp8[i] = __builtin_amdgcn_raw_buffer_load_b64(rs, pv, WS_MODP + (unsigned)i * 10240u, SC1);
      if (!__any(((ne2(pp8[0][0], pp8[1][0]) | ne2(pp8[2][0], pp8[3][0]) | ne2(pp8[4][0], pp8[5][0])) | ne2(pp8[6][0], pp8[7][0])) != 0u)) break;
      __builtin_amdgcn_s_sleep(4);
    }
    STAMPF(9);
    __builtin_amdgcn_s_setprio(3);
    {
      auto pf = [&](int i) { return __uint_as_float(pp8[i][1]); };
      const float t = rbf(((pf(0) + pf(1)) + (pf(2) + pf(3))) + ((pf(4) + pf(5)) + (pf(6) + pf(7))));
      if (l >= 32) *(float*)(st + cw) = rbf(1.0f + t);
      else *(u16*)(st + cw + 2) = (u16)(__float_as_uint(t) >> 16);
    }
    asm volatile("" ::: "memory");
    const f32x4 ca = *(const volatile LAS f32x4*)(st + 7168 + h4 * 16), cc = *(const volatile LAS f32x4*)(st + 7168 + 64 + h4 * 16);
    const u32x2 e0 = *(const volatile LAS u32x2*)(st + 7296 + h4 * 8), e1 = *(const volatile LAS u32x2*)(st + 7328 + h4 * 8);
    SCHED();
#pragma unroll
    for (int j = 0; j < 7; ++j) {
      const f32x2 y0 = xu[j][0] * (f32x2){ca[0], ca[1]}, y1 = xu[j][1] * (f32x2){ca[2], ca[3]}, y2 = xu[j][2] * (f32x2){cc[0], cc[1]}, y3 = xu[j][3] * (f32x2){cc[2], cc[3]};
      o[j] = (u32x4){pack2(y0[0], y0[1]), pack2(y1[0], y1[1]), pack2(y2[0], y2[1]), pack2(y3[0], y3[1])};
      if (j == 6) {
#pragma unroll
        for (int e = 0; e < 4; ++e) o[j][e] = (o[j][e] & ~m12) | mk[3][0];
      }
      ST16S(o[j], rs, l16 + (unsigned)(j & 3) * 1024u, ob + (unsigned)(j >> 2) * 4096u, SC1);
      SCHED();
    }
    __builtin_amdgcn_s_setprio(0);
    STAMPF(11);
    f32x4 d[2] = {{0.f, 0.f, 0.f, 0.f}, {0.f, 0.f, 0.f, 0.f}};
#pragma unroll
    for (int j = 0; j < 7; ++j) {
      const unsigned one = mk[j & 3][0], sr = mk[j & 3][1];
      d[j >> 2] = MFMA16(((u32x4){one, one, one, one}), o[j], d[j >> 2]);
      d[j >> 2] = MFMA16(((u32x4){sr & e0[0], sr & e0[1], sr & e1[0], sr & e1[1]}), x[j], d[j >> 2]);
    }
#pragma unroll
    for (int m = 0; m < 2; ++m)
      st16(rs, WS_CSP + (unsigned)((cb * 20 + Q) * 2 + m) * 1024u + l16, (u32x4){tag, __float_as_uint(d[m][0]), tag, __float_as_uint(d[m][1])});
    STAMPF(13);
    u32x4 tg4 = {tag, t_start, tag, tag};
    asm volatile("s_waitcnt vmcnt(2)" : "+v"(tg4) :: "memory");
    ST16S(tg4, rs, fgo, WS_F4 + (unsigned)(cb * 20 + Q) * 128u, SC1);
  };

  auto red8 = [&](float v) { v += dpp_f<0x128>(v); return sum_rows(v); };
  const rsrc_t rwm = __builtin_amdgcn_make_buffer_rsrc((void*)p.w_mod, 0, D_ * 3840 * 2, 0x00020000);
  auto gemv_load = [&](u32x4* pre, int blk, int sl, int np) {
    pre[10][0] = *(const unsigned*)(p.cvec + 8 * np * sl + 2 * (l < 4 * np ? l : 0));
#pragma unroll
    for (int pp = 0; pp < np; ++pp)
      pre[pp] = __builtin_amdgcn_raw_buffer_load_b128(rwm, (unsigned)((l >> 3) * 7680 + (l & 7) * 16), (unsigned)((8 * np * sl + pp * 8) * 7680 + blk * 128), NT);
  };
  auto gemv_fin = [&](float* acc, const u32x4* pre, int np, char* scr) {
    const unsigned cv2 = pre[10][0];
    unsigned* svt = (unsigned*)scr + w * 96;
#pragma unroll
    for (int e = 0; e < 2; ++e) {
      const int row = 2 * l + e;
      const float cv = e ? bfhi(cv2) : bflo(cv2);
      if (l < 4 * np) svt[(row & 7) * 12 + (row >> 3)] = __float_as_uint(rbf(cv * __builtin_amdgcn_rcpf(1.0f + __expf(-cv))));
    }
    asm volatile("s_waitcnt lgkmcnt(0)" ::: "memory");
    u32x4 sv[3];
#pragma unroll
    for (int i = 0; i < (np + 3) / 4; ++i) sv[i] = *(const u32x4*)(svt + (l >> 3) * 12 + 4 * i);
    SCHED();
#pragma unroll
    for (int e = 0; e < 8; ++e) acc[e] = 0.f;
#pragma unroll
    for (int pp = 0; pp < np; ++pp) {
      const unsigned sh = sv[pp >> 2][pp & 3], sl2 = sh >> 16;
#pragma unroll
      for (int e = 0; e < 4; ++e) {
        const unsigned x = pre[pp][e];
        acc[2 * e] = __builtin_amdgcn_fdot2_f32_bf16(__builtin_bit_cast(bf2_t, x), __builtin_bit_cast(bf2_t, sl2), acc[2 * e], false);
        acc[2 * e + 1] = __builtin_amdgcn_fdot2_f32_bf16(__builtin_bit_cast(bf2_t, x), __builtin_bit_cast(bf2_t, sh), acc[2 * e + 1], false);
      }
    }
#pragma unroll
    for (int e = 0; e < 8; ++e) acc[e] = red8(acc[e]);
  };
  auto gemv_put = [&](const float* acc, unsigned off) {
    float v = acc[0];
#pragma unroll
    for (int e = 1; e < 8; ++e) { v = (l >> 3) == e ? acc[e] : v; asm("" : "+v"(v)); }
    st8(rs, off + (unsigned)l * 8u, (u32x2){tag, (unsigned)__builtin_amdgcn_ds_bpermute(((l & 7) * 8 + (l >> 3)) * 4, (int)__float_as_uint(v))});
  };
  auto cs_ld = [&](u32x4* v, int cj) {
    const int cb = cj / 7, j = cj % 7;
#pragma unroll
    for (int k = 0; k < 5; ++k)
      v[k] = __builtin_amdgcn_raw_buffer_load_b128(rs, (unsigned)(h4 * 10240 + n16 * 16), WS_CSP + (unsigned)((cb * 40 + (j >> 2) + 2 * k) * 1024 + (j & 3) * 256), SC1);
  };
  auto cs_fin = [&](u32x4* v, int cj) {
    auto bad = [&]() { return __any(((ne2(v[0][0], v[0][2]) | ne2(v[1][0], v[1][2]) | ne2(v[2][0], v[2][2])) | ne2(v[3][0], v[3][2]) | ne2(v[4][0], v[4][2])) != 0u); };
    if (bad()) {
      _Pragma("unroll 1") for (unsigned spins = 0; spins < SPIN_CAP; ++spins) {
        __builtin_amdgcn_s_sleep(8);
        cs_ld(v, cj);
        if (!bad()) break;
      }
    }
    float t0 = 0.f, t1 = 0.f;
#pragma unroll
    for (int k = 0; k < 5; ++k) { t0 += __uint_as_float(v[k][1]); t1 += __uint_as_float(v[k][3]); }
    t0 = sum_rows(t0); t1 = sum_rows(t1);
    if (l < 16) st16(rs, WS_CS + (unsigned)((cj / 7) * 2048 + (cj % 7) * 256) + l16, (u32x4){tag, __float_as_uint(t0), tag, __float_as_uint(t1)});
  };
  const bool cs2 = w == 2 && R >= 20 && R < 27, cs1 = w == 2 && R >= 25;
  u32x4 csv[5];
  typedef const __attribute__((address_space(4))) unsigned* cptr_t;

  if (__builtin_expect(is_conv, 1)) {
    if (w >= 4) {
      TOUCH_AT(1);
      const int pw = w - 4;
      const rsrc_t ra = __builtin_amdgcn_make_buffer_rsrc((void*)((idx & 2) ? p.v : p.k), 0, S_ * D_ * 2, 0x00020000);
      const rsrc_t rb = __builtin_amdgcn_make_buffer_rsrc((void*)((idx & 2) ? p.k : p.v), 0, S_ * D_ * 2, 0x00020000);
      const rsrc_t rk = __builtin_amdgcn_make_buffer_rsrc((void*)p.k, 0, S_ * D_ * 2, 0x00020000);
      const rsrc_t rv = __builtin_amdgcn_make_buffer_rsrc((void*)p.v, 0, S_ * D_ * 2, 0x00020000);
#define NROT 2
      const unsigned vo = (unsigned)((128 * pw + 2 * l) * (D_ * 2));
      const unsigned so0 = (unsigned)((idx & 1) * (D_ * 2) + quad * 8), so1 = (unsigned)((1 - (idx & 1)) * (D_ * 2) + quad * 8);
      u32x2 g[3][4], kh;
      auto gl = [&](int s, int j0, int j1) {
        __builtin_amdgcn_s_setprio(0);
#pragma unroll
        for (int j = j0; j < j1; ++j) {
          if (s < NROT) g[s][j] = __builtin_amdgcn_raw_buffer_load_b64(j < 2 ? ra : rb, vo, ((j & 1) ? so1 : so0) + (unsigned)(s * 512 * D_ * 2), 0);
          else g[s][j] = __builtin_amdgcn_raw_buffer_load_b64(j < 2 ? rk : rv, vo + (unsigned)((j & 1) * D_ * 2), (unsigned)(quad * 8 + s * 512 * D_ * 2), 0);
          SCHED();
        }
        __builtin_amdgcn_s_setprio(3);
      };
      const cptr_t sp = (cptr_t)(p.shift_k + quad * 16);
      unsigned spv[8];
#pragma unroll
      for (int i = 0; i < 8; ++i) spv[i] = sp[i];
      __builtin_amdgcn_s_sleep(3);
      gl(0, 0, 4);
      {
        const int row = 512 * (l >> 2) + 128 * pw - 3 + (l & 3);
        kh = __builtin_amdgcn_raw_buffer_load_b64(rk, (l >= 12 || (l & 3) == 3 || row < 0) ? 0x80000000u : (unsigned)(row * D_ * 2 + quad * 8), 0, 0);
      }
      TAG_READY();
      f32x2 wt[2][4];
#pragma unroll
      for (int d = 0; d < 2; ++d) {
        wt[d][0] = (f32x2){bflo(spv[4 * d]), bflo(spv[4 * d + 2])}; wt[d][1] = (f32x2){bfhi(spv[4 * d]), bfhi(spv[4 * d + 2])};
        wt[d][2] = (f32x2){bflo(spv[4 * d + 1]), bflo(spv[4 * d + 3])}; wt[d][3] = (f32x2){bfhi(spv[4 * d + 1]), bfhi(spv[4 * d + 3])};
        asm volatile("" : "+s"(wt[d][0]), "+s"(wt[d][1]), "+s"(wt[d][2]), "+s"(wt[d][3]));
      }
      SCHED();
      {
        const int j = idx * 4 + pw;
        const unsigned o = (unsigned)((512 + 64 * (j >> 2) + l) * (D_ * 2) + (((160 * xcd) >> 7) + ((j >> 1) & 1)) * 128);
        __builtin_amdgcn_raw_ptr_buffer_load_lds((j & 1) ? ra : rb, (LAS void*)(lds + 98304), 4, j < 64 ? o : 0x80000000u, 0, 0, 0);
      }
      SCHED();
      gl(1, 0, 4);
      STAMP(21);
      unsigned up = (unsigned)(L_U + 512 + 256 * pw + (l >> 2) * 16 + (3 - (l & 3)) * 4);
      unsigned lfo = l == 0 ? (unsigned)(L_FLG + 4 * pw) : (unsigned)(100352 + tid * 4), lfr = (unsigned)(L_FLG + (l & 3) * 4);
      unsigned yr = (unsigned)(L_U + (n16 >> 2) * U_CH + (128 * pw + 32 * h4 + 8 * (n16 & 1) + 4 * ((n16 >> 1) & 1)) * 2);
      unsigned yo = (unsigned)(8 * pw + (l >> 3)) * 20480u + (unsigned)quad * 128u + (unsigned)(l & 7) * 16u;
      unsigned mo = (unsigned)((n16 < 12 ? n16 >> 2 : 0) * D_ + quad * 4 + (l & 3)) * 8u + (unsigned)h4 * 40960u;
      unsigned k3o = (unsigned)(16 * pw + (l >> 2)) * 20480u + (unsigned)(((l >> 1) & 1) * 10240 + (2 * quad + (l & 1)) * 16);
      unsigned k3d = (unsigned)(L_U + (2 * (l & 1) + ((l >> 1) & 1)) * U_CH + 512 + (1536 + 128 * pw + 8 * (l >> 2)) * 2);
      unsigned xfo = (unsigned)(((4 * (l >> 5) + pw) * 4 + ((l >> 3) & 3)) * 160 + quad) * 128u + (unsigned)(l & 7) * 16u;
      u32x4 tg4 = {tag, tag, tag, tag};
      asm volatile("" : "+v"(up), "+v"(lfo), "+v"(lfr), "+v"(yr), "+v"(yo), "+v"(mo), "+v"(k3o), "+v"(k3d), "+v"(xfo), "+v"(tg4));
      unsigned so = WS_ST1 + (unsigned)xcd * 16384u + (unsigned)pw * 1024u;
      asm volatile("" : "+s"(so));
      auto pf_set = [&](int i) { asm volatile("" ::: "memory"); *(volatile LAS unsigned*)(lds + lfo + 4 * i) = tg4[0]; };
      auto pf_wait = [&](int i0) {
        _Pragma("unroll 1") for (unsigned spins = 0; spins < (SPIN_CAP << 4); ++spins) {
          if (__all(*(volatile LAS unsigned*)(lds + lfr + 4 * i0) == tag)) break;
          __builtin_amdgcn_s_sleep(1);
        }
        asm volatile("" ::: "memory");
      };
      unsigned hl[2][3];
      auto halo = [&](int s) {
#pragma unroll
        for (int i = 0; i < 6; ++i) { hl[i & 1][i >> 1] = (unsigned)__builtin_amdgcn_readlane(kh[i & 1], 4 * s + (i >> 1)); asm volatile("" : "+v"(hl[i & 1][i >> 1])); }
      };
      auto kv = [&](auto S__, auto N__) {
        constexpr int s = decltype(S__)::value;
        u32x2* x = g[s];
        SCHED();
        asm volatile("s_waitcnt vmcnt(%5)" : "+v"(x[0]), "+v"(x[1]), "+v"(x[2]), "+v"(x[3]), "+v"(kh) : "n"(decltype(N__)::value));
        if (s == 2) STAMP(27);
        if (s == 0) halo(0);
        if (s < NROT) {
          if (idx & 1) { asm volatile(""); const u32x2 t = x[0], u = x[2]; x[0] = x[1]; x[1] = t; x[2] = x[3]; x[3] = u; }
          asm volatile("" : "+v"(x[0]), "+v"(x[1]), "+v"(x[2]), "+v"(x[3]));
          if (idx & 2) { asm volatile(""); const u32x2 t = x[0], u = x[1]; x[0] = x[2]; x[1] = x[3]; x[2] = t; x[3] = u; }
          asm volatile("" : "+v"(x[0]), "+v"(x[1]), "+v"(x[2]), "+v"(x[3]));
        }
        f32x2 kf[2][5], o[2][2];
#pragma unroll
        for (int d = 0; d < 2; ++d) {
          unsigned kk[5];
          kk[3] = x[0][d]; kk[4] = x[1][d];
          kk[2] = __builtin_amdgcn_update_dpp(hl[d][2], kk[4], 0x138, 0xf, 0xf, false);
          kk[1] = __builtin_amdgcn_update_dpp(hl[d][1], kk[3], 0x138, 0xf, 0xf, false);
          kk[0] = __builtin_amdgcn_update_dpp(hl[d][0], kk[2], 0x138, 0xf, 0xf, false);
#pragma unroll
          for (int j = 0; j < 5; ++j) kf[d][j] = (f32x2){bflo(kk[j]), bfhi(kk[j])};
        }
#pragma unroll
        for (int t = 0; t < 5; ++t) {
#pragma unroll
          for (int i = 0; i < 4; ++i) {
            const int d = i >> 1, r = i & 1;
            if (t == 4) o[d][r] *= (f32x2){bflo(x[2 + r][d]), bfhi(x[2 + r][d])};
            else if (t == 0) o[d][r] = kf[d][r + 3] * wt[d][0];
            else o[d][r] = __builtin_elementwise_fma(kf[d][r + 3 - t], wt[d][t], o[d][r]);
          }
          asm volatile("" : "+v"(o[0][0]), "+v"(o[0][1]), "+v"(o[1][0]), "+v"(o[1][1]));
        }
#pragma unroll
        for (int i = 0; i < 4; ++i) *(unsigned*)(lds + up + i * U_CH + s * 1024) = pack2(o[i >> 1][1][i & 1], o[i >> 1][0][i & 1]);
        pf_set(4 * s);
        SCHED();
        if (s < 2) halo(s + 1);
        SCHED();
      };
      u32x4 sv[4], qv[4];
      u32x2 mv[2];
      auto ld_e = [&]() {
#pragma unroll
        for (int s = 0; s < 4; ++s) sv[s] = __builtin_amdgcn_raw_buffer_load_b128(rs, l16, so + (unsigned)s * 4096u, SC1);
        SCHED();
      };
      auto s_bad = [&]() { return __any(((ne2(sv[0][1], sv[0][3]) | ne2(sv[1][1], sv[1][3]) | ne2(sv[2][1], sv[2][3])) | ne2(sv[3][1], sv[3][3])) != 0u); };
      unsigned qo = (unsigned)((128 * pw + 32 * h4 + 4 * ((l >> 3) & 1) + 8 * ((l >> 2) & 1) + (l & 3)) * (D_ * 2));
      asm volatile("" : "+v"(qo));
      auto glq = [&](int j0, int j1) {
        __builtin_amdgcn_s_setprio(0);
#pragma unroll
        for (int j = j0; j < j1; ++j) {
          const u32x2 x = __builtin_amdgcn_raw_buffer_load_b64(rq, qo, (unsigned)(quad * 8 + (512 * (j >> 1) + 16 * (j & 1)) * D_ * 2), 0);
          qv[j >> 1][2 * (j & 1)] = x[0]; qv[j >> 1][2 * (j & 1) + 1] = x[1];
          SCHED();
        }
        __builtin_amdgcn_s_setprio(3);
      };
      kv(IC<0>(), IC<5>());
      STAMP(23);
      if (QG) {
        const int j = idx * 4 + pw;
        const unsigned o = (unsigned)((64 * (j >> 1) + l) * (D_ * 2) + (((160 * xcd) >> 7) + (j & 1)) * 128);
        __builtin_amdgcn_raw_ptr_buffer_load_lds(rq, (LAS void*)(lds + 98304), 4, j < 64 ? o : 0x80000000u, 0, 0, 0);
        SCHED();
      }
#define G2A 4
      gl(2, 0, G2A);
      kv(IC<1>(), IC<G2A + QG>());
      gl(2, G2A, 4);
      STAMPF(9);
      u32x4 k3[2];
      auto ld_k3 = [&]() {
        k3[0] = __builtin_amdgcn_raw_buffer_load_b128(rs, k3o, WS_KV3, SC1);
        k3[1] = __builtin_amdgcn_raw_buffer_load_b128(rs, k3o, WS_KV3 + 5120u, SC1);
      };
      auto ld_m = [&]() {
        mv[0] = __builtin_amdgcn_raw_buffer_load_b64(rs, mo, WS_MODQ, SC1);
        mv[1] = __builtin_amdgcn_raw_buffer_load_b64(rs, mo, WS_MODQ + 20480u, SC1);
      };
      auto ld_q = [&]() {
        asm volatile("" ::: "memory");
        if (!M_T) ld_m();
        SCHED();
#pragma unroll
        for (int s = 0; s < 4; ++s) qv[s] = __builtin_amdgcn_raw_buffer_load_b128(rs, yo, WS_QB + (unsigned)s * (32u * 20480u), BULK_AUX);
        SCHED();
      };
      auto k_bad = [&]() { return __any((ne2(k3[0][1], k3[0][3]) | ne2(k3[1][1], k3[1][3])) != 0u); };
      auto k_put = [&]() {
        *(u32x4*)(lds + k3d) = (u32x4){k3[0][0], k3[0][2], k3[1][0], k3[1][2]};
        pf_set(12);
        SCHED();
      };
      unsigned qck[4], qco = (unsigned)((8 * pw + (l >> 3)) * 1024 + (quad / 40) * 256 + (quad % 40) * 4);
      asm volatile("" : "+v"(qco));
      auto ld_qc = [&]() {
#pragma unroll
        for (int s = 0; s < 4; ++s) {
          qv[s] = __builtin_amdgcn_raw_buffer_load_b128(rs, yo, WS_QB + (unsigned)s * (32u * 20480u), SC1);
          qck[s] = __builtin_amdgcn_raw_buffer_load_b32(rs, qco, WS_QCK + (unsigned)s * 32768u, SC1);
        }
        SCHED();
      };
      auto q_bad = [&]() {
        unsigned d = 0u;
#pragma unroll
        for (int s = 0; s < 4; ++s) d += (qv[s][0] + qv[s][1]) + (qv[s][2] + qv[s][3]);
        return __any(sum8u(d) + ((qck[0] + qck[1]) + (qck[2] + qck[3])) != tag * 4u);
      };
      hold(K_T);
      ld_k3();
      SCHED();
      ld_e();
      if (QP == 0) ld_qc();
      kv(IC<2>(), IC<QP ? 6 : 14>());
      if (QP == 1) { ld_qc(); if (ME) { ld_m(); SCHED(); } }
      STAMPF(11);
      if (k_bad()) {
        _Pragma("unroll 1") for (unsigned spins = 0; spins < SPIN_CAP; ++spins) {
          __builtin_amdgcn_s_sleep(4);
          ld_k3();
          if (!k_bad()) break;
        }
      }
      k_put();
      STAMPF(18);
      if (QP == 2) ld_qc();
      if (ME && QP != 1) { if (M_T) hold(M_T); ld_m(); SCHED(); }
      if (s_bad() || q_bad()) {
        _Pragma("unroll 1") for (unsigned spins = 0; spins < SPIN_CAP; ++spins) {
          __builtin_amdgcn_s_sleep(4);
          ld_e();
          ld_qc();
          if (!(s_bad() || q_bad())) break;
        }
      }
      STAMPF(17);
      if (!ME) { if (M_T) hold(M_T); ld_m(); SCHED(); }
      STAMP(2);
      u32x2 yv[4][2];
      auto ld_y = [&](int s) {
#pragma unroll
        for (int e = 0; e < 2; ++e) yv[s][e] = trd(lds + yr + 1024 * s + 32 * e);
      };
      float r1[4][2], m1[4][2];
#pragma unroll
      for (int i = 0; i < 8; ++i) {
        const unsigned x = sv[i >> 1][2 * (i & 1)];
        r1[i >> 1][i & 1] = __builtin_amdgcn_rcpf(bfhi(x));
        m1[i >> 1][i & 1] = -bflo(x) * r1[i >> 1][i & 1];
        asm volatile("" : "+v"(r1[i >> 1][i & 1]), "+v"(m1[i >> 1][i & 1]));
      }
      f32x4 qf[4][2], P[4][2];
      auto xln = [&](int s) {
#pragma unroll
        for (int e = 0; e < 2; ++e) {
          const float r = r1[s][e], m = m1[s][e];
          const f32x4 qq = {bflo(qv[s][2 * e]), bfhi(qv[s][2 * e]), bflo(qv[s][2 * e + 1]), bfhi(qv[s][2 * e + 1])};
          qf[s][e] = qq;
          P[s][e] = (NOPK & 1) ? fma4(qq, (f32x4){r, r, r, r}, (f32x4){m, m, m, m}) : __builtin_elementwise_fma(qq, (f32x4){r, r, r, r}, (f32x4){m, m, m, m});
        }
        asm volatile("" : "+v"(qf[s][0]), "+v"(qf[s][1]), "+v"(P[s][0]), "+v"(P[s][1]));
      };
      if (QC) { xln(0); xln(1); xln(2); xln(3); }
      pf_wait(20);
      ld_y(0); ld_y(1);
      SCHED();
      if (__any(ne2(mv[0][0], mv[1][0]) != 0u)) {
        _Pragma("unroll 1") for (unsigned spins = 0; spins < SPIN_CAP; ++spins) {
          __builtin_amdgcn_s_sleep(4);
          ld_m();
          if (!__any(ne2(mv[0][0], mv[1][0]) != 0u)) break;
        }
      }
      STAMPF(13);
      f32x4 al, be;
      {
        const float mgf = rbf(sum_rows(__uint_as_float(mv[0][1]) + __uint_as_float(mv[1][1])));
        auto rl = [&](int i) { return __uint_as_float(__builtin_amdgcn_readlane(__float_as_uint(mgf), i)); };
        const f32x4 sa = {rl(0), rl(1), rl(2), rl(3)}, ca = {rl(4), rl(5), rl(6), rl(7)}, ga = {rl(8), rl(9), rl(10), rl(11)};
        al = rbf4(ca + 1.0f) * ga;
        be = sa * ga;
        asm volatile("" : "+v"(al), "+v"(be));
      }
      auto xpre = [&](int s) {
        if (!QC) xln(s);
#pragma unroll
        for (int e = 0; e < 2; ++e) P[s][e] = (NOPK & 2) ? fma4(P[s][e], al, be) : __builtin_elementwise_fma(P[s][e], al, be);
        asm volatile("" : "+v"(P[s][0]), "+v"(P[s][1]));
      };
      auto xfin = [&](int s) {
        u32x4 o;
#pragma unroll
        for (int e = 0; e < 2; ++e) {
          const u32x2 y = yv[s][e];
          const f32x4 yf = {bflo(y[0]), bfhi(y[0]), bflo(y[1]), bfhi(y[1])};
          const f32x4 x = ((NOPK & 2) && s < 2) ? fma4(P[s][e], yf, qf[s][e]) : __builtin_elementwise_fma(P[s][e], yf, qf[s][e]);
          o[2 * e] = pack2(x[0], x[1]);
          o[2 * e + 1] = pack2(x[2], x[3]);
        }
        ST16S(o, rs, yo, WS_X1B + (unsigned)s * (32u * 20480u), SC1);
      };
      xpre(0); xfin(0);
      xpre(1); xfin(1);
      SCHED();
      xpre(2); xpre(3);
#define FVR 5
#define MSPLIT 1
      if (MSPLIT) {
        pf_wait(24);
        ld_y(2);
        xfin(2);
        SCHED();
        pf_wait(28);
        ld_y(3);
        xfin(3);
      } else {
        pf_wait(28);
        ld_y(2); ld_y(3);
        xfin(2); xfin(3);
      }
      STAMP(28);
      if (BG == 1 && l == 0) lf[40 + pw] = tag;
      asm volatile("s_waitcnt vmcnt(2)" ::: "memory");
      ST16S(tg4, rs, xfo, WS_FX, SC1);
      SCHED();
      asm volatile("s_waitcnt vmcnt(1)" ::: "memory");
      ST16S(tg4, rs, xfo, WS_FX + 32u * 160u * 128u, SC1);
      if (BG == 2 && l == 0) lf[40 + pw] = tag;
      STAMP(5);
    } else {
      const int ch = w;
      char* U = lds + L_U + ch * U_CH;
      char* G = lds + ch * (4 * G_COPY);
      {
        const rsrc_t rf = __builtin_amdgcn_make_buffer_rsrc((void*)(p.ssm_k + (size_t)(quad * 4 + ch) * S_), 0, S_ * 2, 0x00020000);
        u32x4 fa[4];
        u32x2 fb[4];
        __builtin_amdgcn_s_setprio(3);
#pragma unroll
        for (int k = 0; k < 4; ++k) {
          fa[k] = __builtin_amdgcn_raw_buffer_load_b128(rf, l16 + (unsigned)k * 1024u, 0, NT);
        }
        __builtin_amdgcn_s_setprio(1);
        TAG_READY();
#pragma unroll
        for (int k = 0; k < 4; ++k) {
#pragma unroll
          for (int d = 0; d < 2; ++d)
            fb[k][d] = __builtin_amdgcn_update_dpp(k < 3 ? (unsigned)__builtin_amdgcn_readfirstlane(fa[k < 3 ? k + 1 : k][d]) : 0u, fa[k][d], 0x130, 0xf, 0xf, false);
          const unsigned x[6] = {fa[k][0], fa[k][1], fa[k][2], fa[k][3], fb[k][0], fb[k][1]};
          unsigned y[5];
#pragma unroll
          for (int d = 0; d < 5; ++d) y[d] = __builtin_amdgcn_alignbit(x[d + 1], x[d], 16);
          char* gp = G + 32 + k * 1024 + l16;
          *(u32x4*)(gp) = fa[k];
          *(u32x4*)(gp + G_COPY) = (u32x4){y[0], y[1], y[2], y[3]};
          *(u32x4*)(gp + 2 * G_COPY) = (u32x4){x[1], x[2], x[3], x[4]};
          *(u32x4*)(gp + 3 * G_COPY) = (u32x4){y[1], y[2], y[3], y[4]};
          if (k == 0) {
            const unsigned f0 = __builtin_amdgcn_readfirstlane(x[0]), f1 = __builtin_amdgcn_readfirstlane(x[1]);
            const unsigned long long f64 = (unsigned long long)f0 | ((unsigned long long)f1 << 32);
            const unsigned long long hi = l ? (f64 << (64 - 16 * (l & 3))) : 0ull;
            if (l < 4) {
              *(u32x4*)(G + l * G_COPY) = (u32x4){0u, 0u, 0u, 0u};
              *(u32x4*)(G + l * G_COPY + 16) = (u32x4){0u, 0u, (unsigned)hi, (unsigned)(hi >> 32)};
            }
            if (l >= 32) *(u32x4*)(U + (l - 32) * 16) = (u32x4){0u, 0u, 0u, 0u};
          }
        }
        asm volatile("s_waitcnt lgkmcnt(0)" ::: "memory");
        STAMP(1);
      }
      const int a = n16, h = h4, jj = h >> 1, e2 = h & 1;
      const char* ap = G + ((a + 1) & 3) * G_COPY + 2 * (16 * jj + a - 8 * e2 + 9 - ((a + 1) & 3));
      unsigned bp = (unsigned)(L_U + ch * U_CH + 32 * (a - jj) + 16 * e2);
      auto lda = [&](u32x4* A, int r, int p0, int np) {
#pragma unroll
        for (int pp = 0; pp < np; ++pp) {
          u32x2 lo = *(const volatile LAS u32x2*)(ap + 512 * (p0 + pp) + 64 * r);
          u32x2 hi = *(const volatile LAS u32x2*)(ap + 512 * (p0 + pp) + 64 * r + 8);
          A[pp] = (u32x4){lo[0], lo[1], hi[0], hi[1]};
        }
      };
      u32x4 AD;
      {
        const unsigned dDr = (*(cptr_t)(p.ssm_D + quad * 4 + (ch & 2)) >> (16 * (ch & 1))) & 0xffffu;
        const unsigned dv = h == (a >> 3) ? ((a & 1) ? dDr : dDr << 16) : 0u;
        const int dw = (7 - (a & 7)) >> 1;
#pragma unroll
        for (int d = 0; d < 4; ++d) AD[d] = dw == d ? dv : 0u;
      }
      u32x4 A0[8][4];
      f32x4 acc[8];
#pragma unroll
      for (int n = 0; n < 8; ++n) acc[n] = (f32x4){0.f, 0.f, 0.f, 0.f};
#pragma unroll
      for (int r = 0; r < 8; ++r) lda(A0[r], r, 0, 4);
      auto yput = [&](int s) {
#pragma unroll
        for (int e = 0; e < 2; ++e)
          *(u32x2*)(U + 1024 * s + 512 * e + (16 * a + 4 * h) * 2) = (u32x2){pack2(acc[2 * s + e][0], acc[2 * s + e][1]), pack2(acc[2 * s + e][2], acc[2 * s + e][3])};
        asm volatile("" ::: "memory");
        if (l == 0) lf[16 + 4 * s + ch] = tag;
      };
      asm volatile("" : "+v"(AD), "+v"(bp), "+v"(acc[0]), "+v"(acc[1]), "+v"(acc[2]), "+v"(acc[3]), "+v"(acc[4]), "+v"(acc[5]), "+v"(acc[6]), "+v"(acc[7]));
      auto stage = [&](auto S__) {
        constexpr int s = decltype(S__)::value, NH = 4 - 2 * s, NP = 8 - 2 * s;
        u32x2 A1[3][NH][2];
        u32x4 B[2][2];
        auto lda1 = [&](int r, int i) { A1[r % 3][i >> 1][i & 1] = *(const volatile LAS u32x2*)(ap + 512 * (4 + (i >> 1)) + 64 * r + 8 * (i & 1)); };
        auto ldb = [&](int r, int e) { B[r & 1][e] = *(const u32x4*)(lds + bp + 512 * (2 * s + e + 1) - 64 * r); };
#pragma unroll
        for (int i = 0; i < 4 * NH; ++i) lda1(i / (2 * NH), i % (2 * NH));
        SCHED();
        if (s > 0) yput(s - 1);
        lf_wait(4 * s, 4);
        if (s == 0) STAMP(3);
#pragma unroll
        for (int i = 0; i < 4; ++i) ldb(i >> 1, i & 1);
        SCHED();
#pragma unroll
        for (int e = 0; e < 2; ++e) MFMA_IP(acc[2 * s + e], AD, B[0][e]);
        sfor<0, 8 * (2 * NP - 1)>([&](auto I_) __attribute__((always_inline)) {
          constexpr int r = decltype(I_)::value / (2 * NP - 1), i = decltype(I_)::value % (2 * NP - 1), e = i >= NP, pp = i - e * NP, h1 = pp < 4 ? 0 : pp - 4;
          const u32x4 a1 = {A1[r % 3][h1][0][0], A1[r % 3][h1][0][1], A1[r % 3][h1][1][0], A1[r % 3][h1][1][1]};
          const u32x4 fa = pp < 4 ? A0[r][pp & 3] : a1;
          MFMA_IP(acc[2 * s + e + pp], fa, B[r & 1][e]);
          SCHED();
          if (r + 2 < 8 && i == 2 * NP - 3) ldb(r + 2, 0);
          if (r + 2 < 8 && i == 2 * NP - 2) ldb(r + 2, 1);
          if (r + 2 < 8 && i < 2 * NH) lda1(r + 2, i % (2 * NH));
          SCHED();
        });
      };
      stage(IC<0>());
      STAMP(4);
      stage(IC<1>());
      STAMPF(9);
      u32x2 fv;
      if (MSPLIT) {
        u32x4 B[8][2];
        auto ldb = [&](int r, int m0) {
#pragma unroll
          for (int m = 0; m < 2; ++m) B[r][m] = *(const u32x4*)(lds + bp + 512 * (m0 + m + 1) - 64 * r);
        };
        asm volatile("s_nop 1" : "+v"(acc[2]), "+v"(acc[3]));
        yput(1);
        if (w == 3) wo_ld();
        __builtin_amdgcn_s_setprio(MPR);
        lf_wait(8, 4);
        STAMP(22);
        auto ldb1 = [&](int i, int m0) { B[i >> 1][i & 1] = *(const u32x4*)(lds + bp + 512 * (m0 + (i & 1) + 1) - 64 * (i >> 1)); };
#pragma unroll
        for (int r = 0; r < 3; ++r) ldb(r, 4);
        SCHED();
#pragma unroll
        for (int m = 0; m < 2; ++m) MFMA_IP(acc[4 + m], AD, B[0][m]);
        sfor<0, 24>([&](auto I_) __attribute__((always_inline)) {
          constexpr int i = decltype(I_)::value, r = i / 3, k = i % 3;
          MFMA_IP(acc[4 + (k != 1)], A0[r][k == 0], B[r][k == 2]);
          SCHED();
          if (i < 10) ldb1(6 + i, 4);
          SCHED();
        });
        sfor<0, 32>([&](auto I_) __attribute__((always_inline)) {
          constexpr int i = decltype(I_)::value, r = i >> 2, k = i & 3;
          MFMA_IP(acc[6 + (k & 1)], A0[r][2 + (k & 1) - (k >> 1)], B[r][k >> 1]);
          SCHED();
          if (i == 7) yput(2);
          if (i == 4 * FVR) fv = ld8(rs, b2o);
          if (i == 19) lf_wait(12, 4);
          if (i >= 19 && i < 29) ldb1(i - 19, 6);
          SCHED();
        });
#pragma unroll
        for (int m = 0; m < 2; ++m) MFMA_IP(acc[6 + m], AD, B[0][m]);
        sfor<0, 24>([&](auto I_) __attribute__((always_inline)) {
          constexpr int i = decltype(I_)::value, r = i / 3, k = i % 3;
          MFMA_IP(acc[6 + (k != 1)], A0[r][k == 0], B[r][k == 2]);
          SCHED();
          if (i < 6) ldb1(10 + i, 6);
          SCHED();
        });
        asm volatile("s_nop 7" : "+v"(acc[6]), "+v"(acc[7]));
        yput(3);
      } else {
        u32x4 B[3][4];
        auto ldb = [&](int r, int m) { B[r % 3][m] = *(const u32x4*)(lds + bp + 512 * (5 + m) - 64 * r); };
        asm volatile("s_nop 1" : "+v"(acc[2]), "+v"(acc[3]));
        yput(1);
        if (w == 3) wo_ld();
        __builtin_amdgcn_s_setprio(MPR);
        lf_wait(8, 8);
        STAMP(22);
#pragma unroll
        for (int i = 0; i < 8; ++i) ldb(i >> 2, i & 3);
        SCHED();
#pragma unroll
        for (int m = 0; m < 4; ++m) MFMA_IP(acc[4 + m], AD, B[0][m]);
        sfor<0, 80>([&](auto I_) __attribute__((always_inline)) {
          constexpr int r = decltype(I_)::value / 10, i = decltype(I_)::value % 10, m = i < 4 ? 0 : i < 7 ? 1 : i < 9 ? 2 : 3, pp = i - (m * (9 - m)) / 2;
          MFMA_IP(acc[4 + m + pp], A0[r][pp], B[r % 3][m]);
          SCHED();
          if (r + 2 < 8 && i < 4) ldb(r + 2, i & 3);
          if (r == 1 && i == 9) fv = ld8(rs, b2o);
          SCHED();
        });
#pragma unroll
        for (int e = 0; e < 4; ++e) {
          if (e == 1) asm volatile("s_nop 4" : "+v"(acc[5]), "+v"(acc[6]), "+v"(acc[7]));
          *(u32x2*)(U + 2048 + 512 * e + (16 * a + 4 * h) * 2) = (u32x2){pack2(acc[4 + e][0], acc[4 + e][1]), pack2(acc[4 + e][2], acc[4 + e][3])};
          SCHED();
        }
        asm volatile("" ::: "memory");
        if (l == 0) lf[28 + ch] = tag;
      }
      STAMP(28);
      __builtin_amdgcn_s_setprio(0);
      b2_wait(fv, 0);
      lf_wait(20, 4);
      DRAIN();
    }
    __builtin_amdgcn_s_setprio(0);
  } else {
    const int at = idx - 20;
    char* const scr = lds + WBLK + 14848;
    auto gv_blk = [&](int j) { return j < 160 ? 30 + (j >> 3) : j < 400 ? (j >> 3) - 20 : j >> 3; };
    auto gv_fin = [&](const u32x4* pre, int j) {
      float acc[8];
      gemv_fin(acc, pre, 10, scr);
      gemv_put(acc, j < 160 ? WS_MODP + (unsigned)((j & 7) * 1280 + (j >> 3) * 64) * 8u : WS_MODQ + (unsigned)((j & 7) * 2560 + ((j >> 3) - 20) * 64) * 8u);
    };
    const int ja = xcd * 12 + at, ai = xcd * 8 + at, bi = xcd * 4 + at - 8, g = w >> 2, wj = w & 3;
    const int rj = w == 0 ? ja : w == 1 ? (at >= 4 ? 92 + ai : -1) : w == 4 ? 160 + ja : w == 5 && at < 8 ? 256 + ai : -1;
    char* const rst = lds + (g ? WBLK : 65536) + wj * 7424;
    u32x4 pre[11];
    if (at >= 8 || g == 0) {
      TOUCH_AT(2);
      const int T = at >= 8 ? 64 + 2 * bi + g : ai;
      unsigned* mds = (unsigned*)(lds + 2048) + 16 * g;
      char* qt = lds + L_RT + g * (16 * 1312);
      u32x4 ld[5];
      const bool e1 = at >= 8 && w == 3;
      if (e1) gemv_load(pre, gv_blk(128 + bi), bi & 7, 10);
      else __builtin_amdgcn_s_sleep(8);
#pragma unroll
      for (int j = 0; j < 5; ++j)
        ld[j] = __builtin_amdgcn_raw_buffer_load_b128(rq, (unsigned)(h4 * D_ * 2 + n16 * 16 + j * 256), (unsigned)((32 * (T >> 1) + 8 * wj + 4 * (T & 1)) * D_ * 2), QG ? 0 : NT);
      const int g2 = at < 8 || w < 2 || w == 4 || w == (G2E ? 3 : 2) ? 400 : w < 4 ? 384 + bi : 256 + wj * 32 + bi;
      SCHED();
      if (G2E && g2 < 400) gemv_load(pre, gv_blk(g2), g2 & 7, 10);
      TAG_READY();
      {
        const bf2_t ONE2 = {(__bf16)1.0f, (__bf16)1.0f};
        char* dst = qt + (4 * wj + h4) * 1312 + n16 * 16;
        float sa = 0.f, va = 0.f;
#pragma unroll
        for (int j = 0; j < 5; ++j) {
          if (!QG) *(u32x4*)(dst + j * 256) = ld[j];
#pragma unroll
          for (int d = 0; d < 4; ++d) {
            const unsigned a0 = ld[j][d];
            const bf2_t xa = __builtin_bit_cast(bf2_t, a0);
            sa = __builtin_amdgcn_fdot2_f32_bf16(xa, ONE2, sa, false); va = __builtin_amdgcn_fdot2_f32_bf16(xa, xa, va, false);
          }
        }
        if (!QG) lf_set(8 + w);
        const float ma = sum16(sa) * (1.0f / D_);
        va = __builtin_fmaxf(sum16(va) * (1.0f / D_) - ma * ma, 0.f);
        if (n16 == 0) mds[4 * wj + h4] = pack2(ma, __builtin_amdgcn_sqrtf(rbf(rbf(va) + 1e-5f)));
      }
      if (QG || QC) lf_set(16 + w);
      if (e1) gv_fin(pre, 128 + bi);
      if (!QG) lf_wait(8 + 4 * g, 4);
      STAMP(23);
      if (QG) {
        if (wj == 2) {
          lf_wait(16 + 4 * g, 4);
          st16(rs, WS_ST1 + (unsigned)(l >> 3) * 16384u + (unsigned)T * 128u + (unsigned)(l & 7) * 16u, (u32x4){mds[l & 7], tag, mds[8 + (l & 7)], tag});
          STAMP(22);
        }
      } else {
        const char* src = qt + (l & 7) * 1312 + (40 * wj + (l >> 3)) * 8;
        unsigned ckw = 0u;
#pragma unroll
        for (int j = 0; j < 5; ++j) {
          const u32x2 x = *(const u32x2*)(src + j * 64), y = *(const u32x2*)(src + j * 64 + 8 * 1312);
          st16(rs, WS_QB + (unsigned)T * 20480u + (unsigned)(5 * wj + j) * 1024u + l16, (u32x4){x[0], x[1], y[0], y[1]});
          if (QC) { const unsigned t = tag - sum8u((x[0] + x[1]) + (y[0] + y[1])); ckw = (l & 7) == j ? t : ckw; }
        }
        if (QC) {
          st4(rs, WS_QCK + (unsigned)(T * 4 + wj) * 256u + (unsigned)(l * 4), (unsigned)__builtin_amdgcn_ds_bpermute(((l & 7) * 8 + (l >> 3)) * 4, (int)ckw));
          if (wj == 2) {
            lf_wait(16 + 4 * g, 4);
            st16(rs, WS_ST1 + (unsigned)(l >> 3) * 16384u + (unsigned)T * 128u + (unsigned)(l & 7) * 16u, (u32x4){mds[l & 7], tag, mds[8 + (l & 7)], tag});
            STAMP(22);
          }
        }
      }
      SCHED();
      static_assert(!G2E || QC, "");
      if (g2 < 400) {
        if (!G2E) gemv_load(pre, gv_blk(g2), g2 & 7, 10);
        if (!QG && !QC) asm volatile("s_waitcnt vmcnt(10)" ::: "memory");
      } else if (w < 2 && rj >= 0) {
        rt_ld(rj, rst);
        if (!QG && !QC) asm volatile("s_waitcnt vmcnt(7)" ::: "memory");
      } else if (!QG && !QC) {
        DRAIN();
      }
      if (!QG && !QC) lf_set(16 + w);
      if (!QG && !QC && wj == 2) {
        lf_wait(16 + 4 * g, 4);
        st16(rs, WS_ST1 + (unsigned)(l >> 3) * 16384u + (unsigned)T * 128u + (unsigned)(l & 7) * 16u, (u32x4){mds[l & 7], tag, mds[8 + (l & 7)], tag});
        STAMP(22);
      }
      if (w >= 4 && w < 7) lf_set(w);
      if (g2 < 400) gv_fin(pre, g2);
      STAMP(25);
    } else {
      constexpr int KB = L_RT + 16 * 1312;
      TOUCH_AT(3);
      const int g1 = w < 6 ? 2 * ai + wj : 32 + wj * 64 + ai;
      if (w >= 6) {
        const rsrc_t rk = __builtin_amdgcn_make_buffer_rsrc((void*)(w == 6 ? p.k : p.v), 0, S_ * D_ * 2, 0x00020000);
        const unsigned ko = (unsigned)((1536 + 8 * ai) * (D_ * 2) - (w == 6 ? 4096 : 0));
        char* dst = lds + KB + (w == 6 ? 0 : 14336);
        __builtin_amdgcn_s_sleep(3);
#pragma unroll
        for (int n = 0; n < 10; ++n) __builtin_amdgcn_raw_ptr_buffer_load_lds(rk, (LAS void*)(dst + n * 1024), 16, l16, ko + (unsigned)n * 1024u, 0, NT);
        if (w == 6) {
#pragma unroll
          for (int n = 10; n < 14; ++n) __builtin_amdgcn_raw_ptr_buffer_load_lds(rk, (LAS void*)(dst + n * 1024), 16, l16, ko + (unsigned)n * 1024u, 0, NT);
        }
      }
      SCHED();
      u32x4 swt[2];
      swt[0] = *(const u32x4*)(p.shift_k + (tid - 256) * 8);
      swt[1] = *(const u32x4*)(p.shift_k + (256 + l) * 8);
      SCHED();
      if (w >= 6) __builtin_amdgcn_s_sleep(8);
      gemv_load(pre, gv_blk(g1), g1 & 7, 10);
      TAG_READY();
      asm volatile("s_waitcnt vmcnt(11)" : "+v"(swt[0]), "+v"(swt[1]) :: "memory");
      lf_set(8 + w);
      if (w < 6) gv_fin(pre, g1);
      STAMP(25);
      lf_wait(12, 4);
      STAMP(23);
#pragma unroll 1
      for (int n = 0; n < (w == 7 ? 2 : 1); ++n) {
        {
          const int pr = n ? 256 + l : tid - 256;
          const char* kp = lds + KB + 4096 + pr * 4;
          unsigned kk[11], vv[8];
#pragma unroll
          for (int i = 0; i < 11; ++i) kk[i] = *(const unsigned*)(kp + (i - 3) * 1280);
#pragma unroll
          for (int i = 0; i < 8; ++i) vv[i] = *(const unsigned*)(kp + 10240 + i * 1280);
          const u32x4 sw = n ? swt[1] : swt[0];
          const f32x2 wt[4] = {{bflo(sw[0]), bflo(sw[2])}, {bfhi(sw[0]), bfhi(sw[2])}, {bflo(sw[1]), bflo(sw[3])}, {bfhi(sw[1]), bfhi(sw[3])}};
          f32x2 o[8];
#pragma unroll
          for (int i = 0; i < 8; ++i) {
            f32x2 t = (f32x2){bflo(kk[i + 3]), bfhi(kk[i + 3])} * wt[0];
#pragma unroll
            for (int j = 1; j < 4; ++j) t = __builtin_elementwise_fma((f32x2){bflo(kk[i + 3 - j]), bfhi(kk[i + 3 - j])}, wt[j], t);
            o[i] = t * (f32x2){bflo(vv[i]), bfhi(vv[i])};
          }
#pragma unroll
          for (int e = 0; e < 4; ++e) {
            const int i = 7 - 4 * (e & 1);
            st16(rs, WS_KV3 + (unsigned)ai * 20480u + (unsigned)(e * 5120 + pr * 16),
                 (u32x4){pack2(o[i][e >> 1], o[i - 1][e >> 1]), tag, pack2(o[i - 2][e >> 1], o[i - 3][e >> 1]), tag});
          }
        }
      }
      STAMP(22);
      if (w < 7) lf_set(w);
      if (w >= 6) gv_fin(pre, g1);
      STAMPF(30);
    }
    if (w == 6 && ja < 80) {
      hold(AUX_T);
      gemv_load(pre, gv_blk(400 + ja), ja & 7, 10);
      gv_fin(pre, 400 + ja);
    }
    if (rj >= 0) {
      if (w >= 2) { hold(RT2_T); rt_ld(rj, rst); }
      rt_job(rj, rst);
    }
    STAMP(26);
    lf_set(w);
    if (w == 3) { hold(WA_T); wo_ld(); }
    STAMP(3);
    if (w < 4) {
      hold(W_T);
      b2_wait((u32x2){~tag, 0u}, 1);
      lf_wait(0, 8);
      STAMP(4);
    }
    DRAIN();
  }

  STAMPF(16);
  u32x4 wox[5];
  if (w == 3) {
    const char* bp = wo_st + (4 * h4 + (n16 >> 2)) * 160 + (n16 & 3) * 8;
#pragma unroll
    for (int dj = 0; dj < 5; ++dj) wox[dj] = trfrag(bp + dj * 32, 160);
    asm volatile("s_waitcnt lgkmcnt(0)" : "+v"(wox[0]), "+v"(wox[1]), "+v"(wox[2]), "+v"(wox[3]), "+v"(wox[4]) :: "memory");
  }
  auto wo_put = [&]() {
    if (w == 3 && R < 28) {
      const unsigned base = WS_WOUTF + (unsigned)c * WBLK + (unsigned)((R >> 1) * 10240 + (R & 1) * 1024);
#pragma unroll
      for (int dj = 0; dj < 5; ++dj) st16(rs, base + (unsigned)((R & 1) ? dj : (dj + 2) % 5) * 2048u + l16, wox[dj]);
    }
  };
  if (cs2) {
    cs_ld(csv, (8 + c) * 7 + R - 20);
    SCHED();
    load_b2(0, 12);
    SCHED();
    cs_fin(csv, (8 + c) * 7 + R - 20);
  } else if (w < 4) {
    asm volatile("" ::: "memory");
    load_b2(0, B0);
    if (B0 < 12 && !is_conv) load_b2(B0, 12);
  }
  const unsigned xfb = WS_FX + (unsigned)(((R >> 1) * 4 + (R & 1) * 2 + (c >> 2)) * 160 + l) * 128u;
  unsigned xf3[3];
  if (w == 7) {
    lf_wait(36, 4);
    int dt = (int)(lf[44 + ((l & 31) < 20 ? l & 31 : 0)] - t_start);
    dt = l < 32 ? dt : -dt;
    dt = max(dt, __builtin_amdgcn_update_dpp(0, dt, 0xB1, 0xf, 0xf, true));
    dt = max(dt, __builtin_amdgcn_update_dpp(0, dt, 0x4E, 0xf, 0xf, true));
    dt = max(dt, __builtin_amdgcn_update_dpp(0, dt, 0x124, 0xf, 0xf, true));
    dt = max(dt, __builtin_amdgcn_update_dpp(0, dt, 0x128, 0xf, 0xf, true));
    const int d1 = min(max(max(__builtin_amdgcn_readlane(dt, 0), __builtin_amdgcn_readlane(dt, 16)), 0), 300);
    const int d0 = min(max(max(__builtin_amdgcn_readlane(dt, 32), __builtin_amdgcn_readlane(dt, 48)), 0), 300);
    dt = ((d1 + d0) * YW >> 3) - d0;
    __builtin_amdgcn_s_setprio(YPR);
    STAMPF(24);
    hold((unsigned)(YG_T + dt));
    STAMPF(30);
    xf3[0] = ld4(rs, xfb); xf3[1] = ld4(rs, xfb + 8192u); xf3[2] = ld4(rs, l < 32 ? xfb + 16384u : ~0u);
    __builtin_amdgcn_s_setprio(0);
  }
  SCHED();
  u32x4 k_c;
  unsigned kc_o = w < 2 ? (unsigned)(tid < 112 ? tid : 0) * 16u : 0x80000000u, kc_d = (unsigned)(L_CONST + (tid & 127) * 8);
  auto kc_ld = [&](int z2) { k_c = __builtin_amdgcn_raw_buffer_load_b128(rs, kc_o, WS_CS + (unsigned)(8 * z2 + c) * 2048u, SC1); };
  auto consts_put = [&](int z2) {
    if (w < 2) {
      if (__any(ne2(k_c[0], k_c[2]) != 0u)) {
        _Pragma("unroll 1") for (unsigned spins = 0; spins < SPIN_CAP; ++spins) {
          __builtin_amdgcn_s_sleep(8);
          kc_ld(z2);
          if (!__any(ne2(k_c[0], k_c[2]) != 0u)) break;
        }
      }
      if (z2) *(u32x2*)(lds + kc_d + 1024) = (u32x2){k_c[1], k_c[3]};
      else { const unsigned d = kc_d - ((kc_d >> 1) & 4u); *(unsigned*)(lds + d) = k_c[1]; *(unsigned*)(lds + d + 8) = k_c[3]; }
    }
  };
  const int wq = w ^ rotr, rt1 = wq & 3, kh1 = w >> 2;
  unsigned bo0 = (unsigned)(kh1 * 71680) + l16, bo1 = bo0 + 35840u;
  asm volatile("" : "+v"(bo0), "+v"(bo1));
  unsigned pso[3], pqo[3], oto = (unsigned)(((w ^ 4) * 7 * 64 + l) * 8), rdo = (unsigned)((w * 7 * 64 + l) * 8), cso = (unsigned)(L_CONST + (128 + n16) * 8);
#pragma unroll
  for (int e = 0; e < 3; ++e) {
    const int row = rt1 * 16 + (e < 2 ? 4 * h4 + 2 * kh1 + e : n16);
    pso[e] = (unsigned)(L_PST + row * 4); pqo[e] = (unsigned)(L_PST + 512 + row * 16 + (row & 3) * 4);
  }
  unsigned spo = (unsigned)(L_Z + tid * 16), p6o = (unsigned)(L_PST + (wq * 16 + 4 * h4) * 4), pgo = (unsigned)(L_PST + 512 + (wq * 16 + n16) * 16);
  asm volatile("" : "+v"(spo), "+v"(p6o), "+v"(pgo));
  p6o = n16 == 12 ? p6o : spo;
  pgo = h4 == (n16 >> 2) ? pgo : spo;
  unsigned bsrc = (unsigned)(((l >> 1) & 3) * 64 + (l & 1) * 4), ppo = l < 8 ? l16 : 0x80000000u;
  asm volatile("" : "+v"(pso[0]), "+v"(pso[2]), "+v"(pqo[0]), "+v"(pqo[2]), "+v"(oto), "+v"(rdo), "+v"(cso), "+v"(p6o), "+v"(pgo), "+v"(bsrc), "+v"(ppo), "+v"(kc_o), "+v"(kc_d));
  pso[1] = pso[0] + 4u; pqo[1] = pqo[0] + 20u;
#define BFRAG(q, j) (*(const u32x4*)(lds + ((q) < 5 ? bo0 : bo1) + ((q) % 5) * 7168 + (j) * 1024))
  u32x4 h2[10];
  unsigned xv = (unsigned)((4 * ((l >> 3) & 1) + h4) * 128 + (l & 7) * 16), xs0 = WS_X1B + (unsigned)(R * 4 + rt1) * 20480u + (unsigned)(10 * kh1) * 1024u;
  unsigned wvo = wsp_vo, bso = (unsigned)tau(rt1, n16) * 2u, fgo = l < 32 ? (unsigned)(l >> 3) * F4S + (unsigned)(l & 7) * 16u : 0x80000000u;
  asm volatile("" : "+v"(xv), "+v"(wvo), "+v"(bso), "+v"(fgo));
#define WO3 (-92)
#define PINS(x) do { x = (unsigned)__builtin_amdgcn_readfirstlane((int)x); asm volatile("" : "+s"(x)); } while (0)
  PINS(xs0);
  unsigned zs[8], wx8, wx4, wgb, fzs, uo, ui, xs, wl2, os, zos, pos, s1, s3;
#define LDX(q) h2[q] = __builtin_amdgcn_raw_buffer_load_b128(rs, xv + (unsigned)((q) & 3) * 1024u, xs0 + (unsigned)((q) >> 2) * 4096u, BULK_AUX)
  auto flag4 = [&](unsigned off) { ST16S(((u32x4){tag, tag, tag, tag}), rs, fgo, WS_F4 - WS_FWI + off, SC1); };
  unsigned bs_raw, fwo[1];
  f32x4 acc[7];
  float mu_n, rs_n;
  {
    unsigned f1o[1] = {w == 1 && l < 20 ? fwi + (unsigned)(c * 20 + l) * 128u : ~0u}, f1v;
    fwo[0] = w == 3 && l < 28 ? fwi + (WS_FWO - WS_FWI) + (unsigned)(c * 32 + l) * 128u : ~0u;
    asm volatile("" : "+v"(f1o[0]), "+v"(fwo[0]));
    auto late_scalars = [&]() {
#pragma unroll
    for (int t = 0; t < 8; ++t) { zs[t] = WS_Z2F + (unsigned)(c * 64 + 8 * w + 4 * ((t >> 2) ^ rotg) + ((t + rotb) & 3)) * 7168u; PINS(zs[t]); }
    wx8 = (unsigned)((w + (idx >> 2)) & 7) * 1024u; wx4 = wx8 & 3072u; wgb = WS_WOUTF + (unsigned)c * WBLK; fzs = fz + (unsigned)(c * 32 + 4 * w) * 128u;
    uo = WS_UF + (unsigned)((R * 4 + rt1) * 8 + c) * 4096u + (unsigned)kh1 * 2048u; ui = WS_UF + (unsigned)((R * 4 + rt1) * 8 + 4 * kh1) * 4096u;
    xs = WS_X1B + (unsigned)(R * 4 + rt1) * 20480u + (unsigned)(5 * c + 2 * kh1) * 512u; wl2 = (unsigned)(120 + WO3 + (w < 4 ? w + 16 : w)) * 1024u;
    os = (unsigned)(((R * 64 + tau(rt1, 0)) * D_ + 80 * c + 32 * kh1) * 2);
    zos = WS_Z2F + (unsigned)((c * 64 + 4 * (R >> 1)) * 7 + (w < 6 ? w + 1 : 0)) * 1024u + (unsigned)(R & 1) * 512u; pos = WS_PART + (unsigned)(R * 64 + wq) * 128u;
    s1 = (unsigned)((w ^ 4) * 3072 + 1024); s3 = (unsigned)(w * 3072 + 1024);
    PINS(wx8); PINS(wx4); PINS(wgb); PINS(fzs); PINS(uo); PINS(ui); PINS(xs); PINS(wl2); PINS(os); PINS(zos); PINS(pos); PINS(s1); PINS(s3);
    SCHED();
    };
    if (GE) late_scalars();
  if (w < 4) {
    load_b2(12, BK);
    if (BG && is_conv) {
      lf_wait(40, 4);
      load_b2(B0, 12);
    }
    load_b2(BK, 35);
  }
  wo_put();
  STAMPF(29);
    if (w == 7) {
      asm volatile("" : "+v"(xf3[0]), "+v"(xf3[1]), "+v"(xf3[2]));
      if (__any(ne2(xf3[0], xf3[1]) != 0u) || __builtin_amdgcn_ballot_w64(xf3[2] == tag) != 0xffffffffull) {
        const unsigned fo[3] = {xf3[0] != tag ? xfb : ~0u, xf3[1] != tag ? xfb + 8192u : ~0u, l < 32 && xf3[2] != tag ? xfb + 16384u : ~0u};
        wait_lines<3, YSL>(rs, fo, tag);
      }
      if (l == 0) lf[33] = tag;
    }
    lf_wait(33, 1);
    STAMP(7);
    SCHED();
#pragma unroll
    for (int q = 0; q < 3; ++q) LDX(q);
    SCHED();
    __builtin_amdgcn_s_waitcnt(0x0F73);
    BAR();
    STAMP(8);
#pragma unroll
    for (int j = 0; j < 7; ++j) acc[j] = (f32x4){0.f, 0.f, 0.f, 0.f};
    f32x4 accg = {0.f, 0.f, 0.f, 0.f};
    u32x4 bf[7];
#pragma unroll
    for (int j = 0; j < 7; ++j) bf[j] = BFRAG(0, j);
    SCHED();
#pragma unroll
    for (int q = 3; q < 6; ++q) LDX(q);
    if (w == 3) flag4(WS_FWO + (unsigned)(c * 32 + R) * 128u);
    SCHED();
    if (!GE) late_scalars();
    auto xswap = [&](int q, int e) {
#pragma unroll
      for (int d = 0; d < 2; ++d) {
        if (e == 1) h2[q][2 + d] ^= (unsigned)__builtin_amdgcn_update_dpp(0, (int)h2[q][d], 0x128, 0xf, 0x3, false);
        else h2[q][d] ^= (unsigned)__builtin_amdgcn_update_dpp(0, (int)h2[q][2 + d], 0x128, 0xf, 0xC, false);
      }
      asm volatile("" : "+v"(h2[q]));
    };
#define XSW 1
#pragma unroll
    for (int q = 0; q < 10; ++q) {
      if (q < XSW) { xswap(q, 0); xswap(q, 1); xswap(q, 2); }
      if (q == 5 && w == 1 && __builtin_amdgcn_ballot_w64(f1v == tag) != 0xfffffull) wait_lines<1, 4>(rs, f1o, tag);
      if (q == 5) { __builtin_amdgcn_s_barrier(); asm volatile("" ::: "memory"); }
      SCHED();
      accg = MFMA16(h2[q], h2[q], accg);
      SCHED();
#pragma unroll
      for (int j = 0; j < 7; ++j) {
        acc[j] = MFMA16(h2[q], bf[j], acc[j]);
        if (q == 9 && j == 1) *(f32x4*)(lds + pgo) = accg;
        if (q < 9) bf[j] = BFRAG(q + 1, j);
        if (q < 9 && q + 1 >= XSW && j >= 3 && j < 6) xswap(q + 1, j - 3);
        if (q == 1 && j == 6) f1v = ld4(rs, f1o[0]);
        if (q == 2 && j == 0) bs_raw = __builtin_amdgcn_raw_buffer_load_b16(__builtin_amdgcn_make_buffer_rsrc((void*)p.b_sp, 0, S_ * 2, 0x00020000), bso, (unsigned)R * 128u, 0);
#define KCQ 4
#define WA 8
#define WB 12
#define WQ0 2
        if (WQ0 == 2 && q >= 2 && j == 2) LDW(q - 2);
        if (WQ0 == 6 && q >= 6 && (j == 2 || j == 4)) LDW(2 * (q - 6) + (j >> 2));
        if (WQ0 == 3 && q >= 2 && q < 5 && (j & 1) && 3 * (q - 2) + (j >> 1) < WA) LDW(3 * (q - 2) + (j >> 1));
        if (WQ0 == 4 && q >= 2 && !(q & 1) && j == 2) LDW((q - 2) >> 1);
        if ((j == 0 || j == 4) && 6 + 2 * q + (j >> 2) < 10) LDX(6 + 2 * q + (j >> 2));
        if (q == KCQ && j == 0) kc_ld(1);
        if (q == 2 && j == 1 && cs1) cs_ld(csv, c * 7 + R - 25);
        if (q >= 5 && j == 5) {
          const int i = (5 + q) * 7 + (w < 7 ? w : 0);
          dma1k(rs, lds + i * 1024, l16, WS_WINF + (unsigned)c * WBLK + (unsigned)i * 1024u);
          if (q == 9) { const int i2 = 28 + (w < 7 ? w : 0); dma1k(rs, lds + i2 * 1024, l16, WS_WINF + (unsigned)c * WBLK + (unsigned)i2 * 1024u); }
        }
        SCHED();
      }
    }
    STAMP(31);
  }
  unsigned sro, swo, zo, zro, po;
  auto gelu_n = [&](f32x2* x, auto N_, auto&& fill) {
    constexpr int N = decltype(N_)::value;
    const float c0 = -2.0f * 0.7978845608028654f * 1.4426950408889634f, c1 = c0 * 0.044715f;
    f32x2 k0 = {c0, c0}, k1 = {c1, c1}, k2 = {1.0f, 1.0f};
    asm volatile("" : "+v"(k0), "+v"(k1), "+v"(k2));
    f32x2 t[N];
#define GST(e) do { _Pragma("unroll") for (int i = 0; i < N; ++i) { t[i] = (e); asm volatile("" : "+v"(t[i])); } } while (0)
    GST(x[i] * x[i]);
    GST(__builtin_elementwise_fma(t[i], k1, k0));
    fill(IC<0>());
    GST(t[i] * x[i]);
    fill(IC<1>());
    GST(((f32x2){__builtin_amdgcn_exp2f(t[i][0]), __builtin_amdgcn_exp2f(t[i][1])}));
    fill(IC<2>());
    GST(t[i] + k2);
    fill(IC<3>());
    GST(((f32x2){__builtin_amdgcn_rcpf(t[i][0]), __builtin_amdgcn_rcpf(t[i][1])}));
    fill(IC<4>());
#pragma unroll
    for (int i = 0; i < N; ++i) { x[i] *= t[i]; asm volatile("" : "+v"(x[i])); }
  };
  {
    f32x2 mine[7];
    f32x2 csb[7];
    if (kh1) {
#pragma unroll
      for (int j = 0; j < 7; ++j) { *(f32x2*)(lds + rdo + j * 512) = (f32x2){acc[j][0], acc[j][1]}; mine[j] = (f32x2){acc[j][2], acc[j][3]}; }
      *(f32x4*)(lds + p6o) = acc[6];
    } else {
#pragma unroll
      for (int j = 0; j < 7; ++j) { *(f32x2*)(lds + rdo + j * 512) = (f32x2){acc[j][2], acc[j][3]}; mine[j] = (f32x2){acc[j][0], acc[j][1]}; }
      *(f32x4*)(lds + p6o) = acc[6];
      consts_put(1);
      if (cs1) cs_fin(csv, c * 7 + R - 25);
#pragma unroll
      for (int k = WA; k < WB; ++k) LDW(k);
    }
    BAR();
#define BY 0
    auto b1p = [&](int k) {
      const int i = 4 * k + (w & 3) + (k < 8 ? 35 : k > 8 ? 70 : (w & 3) < 3 ? 35 : 70) - (k == 17 ? (w & 2) : 0);
      dma1k(rs, lds + i * 1024, l16, WS_WINF + (unsigned)c * WBLK + (unsigned)i * 1024u);
    };
#define IB 5
    auto b18 = [&](int k) {
      const int i = 8 * k + w + (k < 4 ? 35 : k > 4 ? 70 : w < 3 ? 35 : 70) - (k == 8 && w > 5 ? 2 : 0);
      dma1k(rs, lds + i * 1024, l16, WS_WINF + (unsigned)c * WBLK + (unsigned)i * 1024u);
      SCHED();
    };
    if (kh1) {
#pragma unroll
      for (int k = 0; k < BY; ++k) b1p(k);
#pragma unroll
      for (int k = WA; k < WB; ++k) LDW(k);
    }
    __builtin_amdgcn_s_setprio(3);
    STAMPT(9);
    f32x2 oth[7];
    f32x2 pa[3], pb[3];
#pragma unroll
    for (int e = 0; e < 3; ++e) {
      pa[e] = (f32x2){*(const float*)(lds + pso[e]), *(const float*)(lds + pqo[e])};
      pb[e] = (f32x2){*(const float*)(lds + pso[e] + 256), *(const float*)(lds + pqo[e] + 1024)};
    }
#pragma unroll
    for (int j = 0; j < 7; ++j) { oth[j] = *(const f32x2*)(lds + oto + j * 512); csb[j] = *(const f32x2*)(lds + cso + 128 * j); }
    SCHED();
    if (IB > 6) b18(IB - 7);
    float mu2[2], rs2[2];
#pragma unroll
    for (int e = 0; e < 3; ++e) {
      const float mean = (pa[e][0] + pb[e][0]) * (1.0f / D_);
      const float var = __builtin_fmaxf((pa[e][1] + pb[e][1]) * (1.0f / D_) - mean * mean, 0.f);
      const float rstd = __builtin_amdgcn_rsqf(var + 1e-5f);
      if (e < 2) { mu2[e] = mean; rs2[e] = rstd; } else { mu_n = mean; rs_n = rstd; }
    }
    f32x2 zz[7];
    const f32x2 nmu = {-mu2[0], -mu2[1]}, rsv = {rs2[0], rs2[1]};
#pragma unroll
    for (int j = 0; j < 7; ++j)
      zz[j] = __builtin_elementwise_fma(__builtin_elementwise_fma(nmu, (f32x2){csb[j][0], csb[j][0]}, mine[j] + oth[j]), rsv, (f32x2){csb[j][1], csb[j][1]});
    if (IB > 5) {
#pragma unroll
      for (int j = 0; j < 7; ++j) asm volatile("" : "+v"(zz[j]));
      b18(IB - 6);
    }
    gelu_n(zz, IC<7>(), [&](auto K_) __attribute__((always_inline)) { if (IB - 5 + decltype(K_)::value >= 0 && IB) b18(IB - 5 + decltype(K_)::value); });
    if (n16 >= 12) zz[6] = (f32x2){0.f, 0.f};
    f32x2 s1 = ((zz[0] + zz[1]) + (zz[2] + zz[3])) + ((zz[4] + zz[5]) + zz[6]);
    f32x2 s2 = zz[0] * zz[0], s3 = zz[1] * zz[1];
#pragma unroll
    for (int j = 2; j < 7; ++j) { if (j & 1) s3 = __builtin_elementwise_fma(zz[j], zz[j], s3); else s2 = __builtin_elementwise_fma(zz[j], zz[j], s2); }
    s2 += s3;
    asm("" : "+v"(s1), "+v"(s2));
    float a0 = s1[0] + dpp_f<0xB1>(s1[0]), a1 = s1[1] + dpp_f<0xB1>(s1[1]), b0 = s2[0] + dpp_f<0xB1>(s2[0]);
    a0 = (l & 1) ? a1 : a0;
    const float b1 = s2[1] + dpp_f<0xB1>(s2[1]);
    b0 = (l & 1) ? b1 : b0;
    a0 += dpp_f<0x4E>(a0); b0 += dpp_f<0x4E>(b0);
    a0 += dpp_f<0x124>(a0); b0 += dpp_f<0x124>(b0);
    a0 += dpp_f<0x128>(a0); b0 += dpp_f<0x128>(b0);
    {
      const unsigned pa0 = __builtin_amdgcn_ds_bpermute(bsrc, __float_as_uint(a0)), pb0 = __builtin_amdgcn_ds_bpermute(bsrc, __float_as_uint(b0));
      ST16S(((u32x4){tag, pa0, tag, pb0}), rs, ppo, pos + (unsigned)c * 1024u, SC1);
    }
    SCHED();
    __builtin_amdgcn_s_setprio(0);
    STAMP(10);
#define BO 14
    if (kh1) {
#pragma unroll
      for (int k = 2 * IB + BY + BO; k < 18; ++k) b1p(k);
    } else {
#pragma unroll
      for (int k = 2 * IB + BY; k < 2 * IB + BY + BO && k < 18; ++k) b1p(k);
    }
#define WC 16
#define IT 0
#pragma unroll
    for (int k = WB; k < (IT ? WB : WC); ++k) LDW(k);
#pragma unroll
    for (int j = 0; j < 7; ++j)
      *(unsigned*)(lds + L_Z + (((rt1 >> 1) * 7 + j) * 64 + l) * 16 + (4 * (rt1 & 1) + 2 * kh1) * 2) = pack2(zz[j][0], zz[j][1]);
    SCHED();
#define TR0 2
#define TR1 TR0
#define TR2 TR1
#define TR3 5
    sfor<0, 4 * TR0>([&](auto X_) __attribute__((always_inline)) {
      constexpr int x = decltype(X_)::value, n = WC - WB, k0 = x * n / (4 * TR0), k1 = (x + 1) * n / (4 * TR0);
      if (IT) sfor<k0, k1>([&](auto K_) __attribute__((always_inline)) { LDW((WB + decltype(K_)::value) & 31); SCHED(); });
      WSP_TX(x);
      SCHED();
    });
    sro = (unsigned)(L_ST2 + h4 * 16);
    swo = (unsigned)(L_ST2 + (16 * rt1 + 4 * ((l >> 1) & 3) + 2 * kh1 + (l & 1)) * 4);
    zo = w < 7 ? (unsigned)(h4 * 7168 + n16 * 16) : 0x80000000u;
    zro = (unsigned)(L_Z + (w < 7 ? w : 0) * 1024) + l16;
    po = (unsigned)((l >> 3) * 1024 + (l & 7) * 16);
    asm volatile("" : "+v"(sro), "+v"(swo), "+v"(zo), "+v"(zro), "+v"(po));
    asm volatile("s_waitcnt vmcnt(%0)" :: "n"(WC - WB) : "memory");
    BAR();
    STAMPT(11);
  }
  unsigned z1d[7];
  constexpr int L_RED = 28672;
#define WOA(i) (((i) < 28 ? (i) : (i) < 108 ? (i) + 32 : (i) < 120 ? (i) + 37 : (i) - 92) * 1024)
#define RBAR() do { __builtin_amdgcn_s_barrier(); asm volatile("" ::: "memory"); } while (0)
#define ZAUX 16
#define NW 14
#define WOD(i0, x) dma1k(rs, lds + WOA(i0) + (x), l16, wgb + (unsigned)(i0) * 1024u + (x))
  auto wo_dma = [&](int k) {};
  u32x4 zw[NW];
#define LDZ(f) do { if (((f) >> 3) == 4) { zs[(f) & 7] += 4096u; asm volatile("" : "+s"(zs[(f) & 7])); } \
    zw[(f) % NW] = __builtin_amdgcn_raw_buffer_load_b128(rs, l16 + (unsigned)(((f) >> 3) & 3) * 1024u, zs[(f) & 7], ZAUX); } while (0)
#define WD 24
#define WE 32
#define QS 5
#define PQ 3
#define NW0 NW
#define VMW(n) asm volatile("s_waitcnt vmcnt(%0)" ::"n"(n) : "memory")
#define FQ 10
#define BFK 0
#define BFN 0
#define PP 1
#define WZ 2
#define WY 8
  static_assert((BFK == 0 || QS < 6) && QS >= 4 && QS <= 6 && PQ >= 2 && PQ < QS && (WZ < 4 || QS >= 5), "");
  {
    f32x4 acc[7];
#pragma unroll
    for (int j = 0; j < 7; ++j) acc[j] = (f32x4){0.f, 0.f, 0.f, 0.f};
    u32x4 bf[7];
#pragma unroll
    for (int j = 0; j < 7; ++j) bf[j] = BFRAG(4, j);
    u32x4 pv, zf[2];
    unsigned fwv;
    auto ld_pv = [&]() { pv = __builtin_amdgcn_raw_buffer_load_b128(rs, po, pos, SC1); };
    auto z1 = [&](auto Q0_, auto Q1_) __attribute__((always_inline)) {
#pragma unroll
      for (int qi = decltype(Q0_)::value; qi < decltype(Q1_)::value; ++qi) {
        const int q = (qi + 4) % 10, qn = (qi + 5) % 10;
        if (qi == FQ) {
          if (kh1) VMW(WE - WD + WZ); else VMW(0);
          RBAR();
          if (w == 3) flag4(WS_FZ + (unsigned)(c * 32 + R) * 128u);
        }
        SCHED();
#pragma unroll
        for (int j = 0; j < 7; ++j) {
          acc[j] = MFMA16(bf[j], h2[q], acc[j]);
          if (qi < 9 && (qi != QS - 1 || j < BFK)) bf[j] = BFRAG(qn, j);
          if (qi < 2 && (j == 1 || j == 4)) { const int i = (2 * qi + (j >> 2)) * 7 + (w < 7 ? w : 0); dma1k(rs, lds + i * 1024, l16, WS_WINF + (unsigned)c * WBLK + (unsigned)i * 1024u); }
          if (qi == 2 && j == 0) kc_ld(0);
          if (qi == 2 && j == 3) fwv = ld4(rs, fwo[0]);
          if (qi == PQ && j == 1) ld_pv();
          if (qi == QS - 1 && j < 2) zf[j] = *(const u32x4*)(lds + zro + j * 7168);
          if (!PP && qi >= QS && (j & 1) && WD + 3 * (qi - QS) + (j >> 1) < WE) LDW((WD + 3 * (qi - QS) + (j >> 1)) & 31);
          SCHED();
        }
      }
    };
    auto req1 = [&]() {
#pragma unroll
      for (int k = WC; k < WD; ++k) { LDW(k); SCHED(); }
    };
    auto req2 = [&]() {
#pragma unroll
      for (int k = WD; k < (PP ? WE : WD); ++k) { LDW(k); SCHED(); }
      sfor<0, WZ>([&](auto G_) __attribute__((always_inline)) { WOD(68 + 8 * decltype(G_)::value, wx8); SCHED(); });
    };
    if (kh1) req1();
    z1(IC<0>(), IC<QS>());
    STAMPT(29);
    f32x2 zv[2][4];
#pragma unroll
    for (int i = 0; i < 8; ++i) { zv[i >> 2][i & 3] = (f32x2){bflo(zf[i >> 2][i & 3]), bfhi(zf[i >> 2][i & 3])}; asm volatile("" : "+v"(zv[i >> 2][i & 3])); }
#pragma unroll
    for (int g = TR0; g < TR1; ++g) WSP_TR(g);
    __builtin_amdgcn_s_setprio(3);
    if (__any(ne2(pv[0], pv[2]) != 0u)) {
      _Pragma("unroll 1") for (unsigned spins = 0; spins < SPIN_CAP; ++spins) {
        ld_pv();
        if (!__any(ne2(pv[0], pv[2]) != 0u)) break;
        __builtin_amdgcn_s_sleep(1);
      }
    }
    {
      float t1 = __uint_as_float(pv[1]), t2 = __uint_as_float(pv[3]);
      t1 += dpp_f<0x128>(t1); t2 += dpp_f<0x128>(t2);
      auto x = __builtin_amdgcn_permlane16_swap(__float_as_uint(t1), __float_as_uint(t2), false, false);
      const unsigned y = __float_as_uint(__uint_as_float(x[0]) + __uint_as_float(x[1]));
      auto z = __builtin_amdgcn_permlane32_swap(y, y, false, false);
      const unsigned u = __float_as_uint(__uint_as_float(z[0]) + __uint_as_float(z[1]));
      auto v = __builtin_amdgcn_permlane16_swap(u, u, false, false);
      const float mean = __uint_as_float(v[0]) * (1.0f / FH);
      const float var = __builtin_fmaxf(__uint_as_float(v[1]) * (1.0f / FH) - mean * mean, 0.f);
      const float rstd = __builtin_amdgcn_rsqf(var + 1e-5f);
      *(float*)(lds + swo) = rstd;
      *(float*)(lds + swo + 256) = -mean * rstd;
    }
    SCHED();
    consts_put(0);
    if (w == 3 && __builtin_amdgcn_ballot_w64(fwv == tag) != 0xfffffffull) wait_lines<1, 1>(rs, fwo, tag);
    if (!kh1) req1();
    BAR();
    STAMP(12);
    {
      f32x4 sa[2][2], sb[2][2];
#pragma unroll
      for (int i = 0; i < 4; ++i) { sa[i >> 1][i & 1] = *(const f32x4*)(lds + sro + i * 64); sb[i >> 1][i & 1] = *(const f32x4*)(lds + sro + i * 64 + 256); }
      SCHED();
#pragma unroll
      for (int j = BFK; j < (BFN ? 7 : BFK); ++j) bf[j] = BFRAG((QS + 4) % 10, j);
      SCHED();
#pragma unroll
      for (int m = 0; m < 2; ++m) {
        u32x4 o;
#pragma unroll
        for (int e = 0; e < 4; ++e) {
          const f32x4 a = sa[m][e >> 1], b = sb[m][e >> 1];
          const f32x2 x = __builtin_elementwise_fma(zv[m][e], (f32x2){a[2 * (e & 1)], a[2 * (e & 1) + 1]}, (f32x2){b[2 * (e & 1)], b[2 * (e & 1) + 1]});
          o[e] = pack2(x[0], x[1]);
        }
        ST16S(o, rs, zo + (unsigned)m * 256u, zos, SC1);
      }
    }
    SCHED();
    __builtin_amdgcn_s_setprio(0);
    STAMPT(13);
#pragma unroll
    for (int j = BFK; j < (BFN ? BFK : 7); ++j) bf[j] = BFRAG((QS + 4) % 10, j);
    if (kh1) req2();
    z1(IC<QS>(), IC<10>());
    f32x2 mine[7];
    if (kh1) {
#pragma unroll
      for (int j = 0; j < 7; ++j) { *(f32x2*)(lds + rdo + L_RED + j * 512) = (f32x2){acc[j][0], acc[j][1]}; mine[j] = (f32x2){acc[j][2], acc[j][3]}; }
    } else {
#pragma unroll
      for (int j = 0; j < 7; ++j) { *(f32x2*)(lds + rdo + L_RED + j * 512) = (f32x2){acc[j][2], acc[j][3]}; mine[j] = (f32x2){acc[j][0], acc[j][1]}; }
    }
    if (!kh1) req2();
#pragma unroll
    for (int g = TR1; g < TR2; ++g) WSP_TR(g);
    if (FQ > 9) VMW(WE - WD + WZ);
    BAR();
    if (FQ > 9 && w == 3) flag4(WS_FZ + (unsigned)(c * 32 + R) * 128u);
    STAMP(6);
    auto req = [&](auto G0_, auto G1_) __attribute__((always_inline)) {
#pragma unroll
      for (int k = WE; k < 32; ++k) LDW(k);
      SCHED();
      sfor<decltype(G0_)::value, decltype(G1_)::value>([&](auto G_) __attribute__((always_inline)) {
        constexpr int i0 = 4 * decltype(G_)::value;
        WOD(i0 < 68 ? i0 : i0 + 8 * WZ, wx4); SCHED();
      });
    };
#define IL 1
#define XF 12
#define XC 18
    static_assert(!IL || (WZ == 2 && WE == 32), "");
    auto wo8 = [&](auto K_) __attribute__((always_inline)) {
      constexpr int k = decltype(K_)::value;
      if (k < 12) WOD(k < 3 ? 8 * k : k < 8 ? 8 * k + 4 : 8 * k + 20, wx8);
      else if (k == 12) dma1k(rs, lds + (kh1 ? WOA(116) : WOA(24)) + wx4, l16, wgb + (kh1 ? 116u : 24u) * 1024u + wx4);
      SCHED();
    };
    if (IL) { wo8(IC<0>()); wo8(IC<1>()); }
    if (!IL && kh1) req(IC<0>(), IC<WY>());
    u32x4 gv[8];
    auto gb_ld = [&]() {
#pragma unroll
      for (int k = 0; k < 8; ++k) gv[k] = __builtin_amdgcn_raw_buffer_load_b128(rs, l < 40 ? l16 : 0u, WS_MODQ + (unsigned)(k * 2560 + 1920 + 80 * c) * 8u, SC1);
    };
    if (w == 2) gb_ld();
    f32x2 z[7];
    {
      f32x2 oth[7];
      f32x4 cc[7];
#pragma unroll
      for (int j = 0; j < 7; ++j) {
        oth[j] = *(const f32x2*)(lds + oto + L_RED + j * 512);
        cc[j] = *(const f32x4*)(lds + L_CONST + (16 * j + 4 * h4 + 2 * kh1) * 8);
      }
      SCHED();
#pragma unroll
      for (int j = 0; j < 7; ++j)
        z[j] = __builtin_elementwise_fma(__builtin_elementwise_fma((f32x2){-mu_n, -mu_n}, (f32x2){cc[j][0], cc[j][1]}, oth[j] + mine[j]),
                                         (f32x2){rs_n, rs_n}, (f32x2){cc[j][2], cc[j][3]});
    }
    if (IL) {
#pragma unroll
      for (int j = 0; j < 7; ++j) asm volatile("" : "+v"(z[j]));
      wo8(IC<2>());
    }
    gelu_n(z, IC<7>(), [&](auto K_) __attribute__((always_inline)) { if (IL) wo8(IC<3 + decltype(K_)::value>()); });
#pragma unroll
    for (int j = 0; j < 7; ++j) z1d[j] = pack2(z[j][0], z[j][1]);
    asm volatile("" : "+v"(z1d[0]), "+v"(z1d[1]), "+v"(z1d[2]), "+v"(z1d[3]), "+v"(z1d[4]), "+v"(z1d[5]), "+v"(z1d[6]));
    SCHED();
    STAMPT(30);
    if (w == 2) {
      auto bad = [&]() {
        unsigned d = 0u;
#pragma unroll
        for (int k = 0; k < 8; ++k) d |= ne2(gv[k][0], gv[k][2]);
        return __any(d != 0u);
      };
      if (bad()) {
        _Pragma("unroll 1") for (unsigned spins = 0; spins < SPIN_CAP; ++spins) {
          __builtin_amdgcn_s_sleep(8);
          gb_ld();
          if (!bad()) break;
        }
      }
      f32x2 t[4];
#pragma unroll
      for (int k = 0; k < 4; ++k) t[k] = (f32x2){__uint_as_float(gv[2 * k][1]) + __uint_as_float(gv[2 * k + 1][1]), __uint_as_float(gv[2 * k][3]) + __uint_as_float(gv[2 * k + 1][3])};
      *(f32x2*)(lds + L_MISC + l * 8) = rbf2((t[0] + t[1]) + (t[2] + t[3]));
    }
    if (!IL && !kh1) req(IC<WY>(), IC<30 - 2 * WZ>());
    const unsigned fo[1] = {l < 4 ? fzs + (unsigned)l * 128u : ~0u};
#define FPY 1
#define FPO FPY
#define FSL 1
    unsigned fv;
    sfor<4 * TR2, 32>([&](auto X_) __attribute__((always_inline)) {
      constexpr int x = decltype(X_)::value;
      wo8(IC<8 + x - 4 * TR2>());
      if (x == XF) { fv = ld4(rs, fo[0]); SCHED(); }
      if (x == XC) {
        STAMP(14);
        if (__builtin_amdgcn_ballot_w64(fv == tag) != 15ull) wait_lines<1, FSL>(rs, fo, tag);
        STAMPT(23);
      }
      if (x >= XC && x - XC < NW) { LDZ((x - XC) & 15); SCHED(); }
      WSP_TX(x);
      SCHED();
    });
#pragma unroll
    for (int f = 32 - XC; f < NW; ++f) { LDZ(f); SCHED(); }
  }

#define WO2 15
#define TRC (IL ? 8 : TR3)
  {
    SCHED();
#pragma unroll
    for (int g = TRC; g < 8; ++g) WSP_TR(g);
    SCHED();
    STAMP(20);
    const int rt = rt1, kh = kh1;
    const unsigned rw = (unsigned)(L_RED + w * 512);
    unsigned rr = (unsigned)(L_RED + w * 4096 + l * 4);
    asm volatile("; m0 is set here, not in front of the first store (one wait state)" : "+v"(rr) : "{m0}"(rw));
    const float bs = bflo(bs_raw);
    unsigned long long hm = 0xffff000000000000ull;
    asm volatile("" : "+s"(hm));
    unsigned l8;
    f32x4 acc2[2][4];
    float rv[8][2];
    u32x4 ud;
    u32x4 P[2][8];
    u32x2 xp[3];
    auto ld_p = [&](int pt, int i) { P[pt][i] = __builtin_amdgcn_raw_buffer_load_b128(rs, l16 + (unsigned)pt * 1024u, ui + (unsigned)i * 2048u, SC1); };
#define RED_PUT1(j, i) asm volatile("ds_write_addtid_b32 %0 offset:%2" :: "v"(acc2[(j) & 1][((i) >> 2) & 3][(i) & 3]), "{m0}"(rw), "n"((((i) >> 2) & 3) * 4096 + (((i) >> 1) & 1) * 16384 + ((i) & 1) * 256) : "memory")
#define RED_PUT(j, r) do { RED_PUT1(j, 4 * (r)); RED_PUT1(j, 4 * (r) + 1); RED_PUT1(j, 4 * (r) + 2); RED_PUT1(j, 4 * (r) + 3); } while (0)
#define RED_GET(i) do { f32x2 x_; asm volatile("ds_read2st64_b32 %0, %1 offset0:%2 offset1:%3" : "=v"(x_) : "v"(rr), "n"(2 * (i)), "n"(2 * (i) + 1) : "memory"); rv[i][0] = x_[0]; rv[i][1] = x_[1]; } while (0)
    float zz[2], gg[2];
    unsigned cs;
    auto sum_rows_u = [&](unsigned v) {
      auto a = __builtin_amdgcn_permlane16_swap(v, v, false, false);
      v = a[0] + a[1];
      auto b = __builtin_amdgcn_permlane32_swap(v, v, false, false);
      return b[0] + b[1];
    };
    auto red_fin = [&](int j, int k, bool fuse = false) {
      const int e = k & 1, i = k >> 1;
#define VADD(d, x, y) asm volatile("v_add_f32 %0, %1, %2" : "=v"(d) : "v"(x), "v"(y))
      if (i < 4) VADD(rv[2 * i][e], rv[2 * i][e], rv[2 * i + 1][e]);
      if (i == 4 || i == 5) VADD(rv[4 * (i & 1)][e], rv[4 * (i & 1)][e], rv[4 * (i & 1) + 2][e]);
      if (i == 6) VADD(rv[0][e], rv[0][e], bs);
      if (i == 7) VADD(gg[e], rv[0][e], rv[4][e]);
      if (i == (fuse ? 9 : 8)) zz[e] = e ? bfhi(z1d[(j + 6) % 7]) : bflo(z1d[(j + 6) % 7]);
      if (i == 9) zz[e] *= gg[e];
      if (i == 9 || (i == 8 && !fuse)) asm volatile("" : "+v"(zz[e]));
      if (k == 20) { ud[j & 3] = pack2(zz[0], zz[1]); asm volatile("" : "+v"(ud[j & 3])); }
      if (k == 21) {
        if (j == 0) asm volatile("v_cndmask_b32 %0, %0, 0, %1" : "+v"(ud[0]) : "s"(hm));
        if (j == 2) asm volatile("v_add3_u32 %0, %1, %2, %3" : "=v"(cs) : "v"(ud[0]), "v"(ud[1]), "v"(ud[2]));
        if (j == 3) {
          cs = tag - sum_rows_u(cs + ud[3]);
          asm volatile("v_cndmask_b32 %0, %0, %1, %2" : "+v"(ud[0]) : "v"(cs), "s"(hm));
        }
        if (j >= 4) { ud[3] = (j == 4 ? tag : ud[3]) - ud[j & 3]; asm volatile("" : "+v"(ud[3])); }
        if (j % 3 == 0 && j) ST16S(ud, rs, l16 + (unsigned)(j / 6) * 1024u, uo, SC1);
      }
    };
    struct Sch { int b1, b2, wp[16], rp[8], vp[22], lp[8], pr[32]; };
    constexpr Sch S = {18, 28, {0, 1, 2, 3, 4, 5, 6, 7, 8, 9, 10, 11, 12, 12, 15, 15}, {19, 19, 20, 22, 23, 23, 24, 24}, {29, 29, 30, 30, 31, 31, 32, 33, 35, 36, 37, 38, 39, 40, 41, 42, 43, 46, 43, 46, 48, 48},
                       {1, 1, 1, 1, 1, 1, 1, 1}, {0, 0, 2, 1, 0, 0, 0, 0, 0, 0, 2, 2, 0, 0, 0, 0, 0, 0, 0, 1, 0, 0, 0, 1, 0, 0, 2, 1, 0, 0, 0, 0}};
    constexpr Sch F = {15, 23, {0, 1, 1, 2, 3, 4, 4, 5, 6, 7, 8, 9, 10, 11, 12, 12}, {15, 16, 16, 17, 17, 18, 18, 19}, {23, 23, 24, 24, 25, 26, 26, 27, 27, 28, 28, 29, 30, 30, 31, 31, 22, 22, 32, 33, 34, 35},
                       {1, 2, 1, 1, 1, 1, 1, 1}, {0, 2, 1, 0, 0, 1, 1, 0, 0, 2, 1, 0, 0, 2, 1, 0, 0, 2, 1, 0, 0, 2, 1, 0, 0, 2, 1, 0, 0, 2, 1, 0}};
#define VM 3
#define JF 4
#define RO 3
#define XE 1
#define CE 0
#define AP0 2
#define P1B 2
#define P1S 3
    unsigned wb[3], xo;
    const unsigned wl[3] = {(unsigned)(120 + WO3 + w) * 1024u, (unsigned)(128 + WO3 + w) * 1024u, wl2}, wg = wgb - (unsigned)(WO3 * 1024);
#define NAF 32
#define NA0 16
#define NA1 NA0
    u32x4 af[NAF];
#define LDA(m) af[(m) % NAF] = *(const u32x4*)(lds + wb[WOA(2 * (m)) >> 16] + (WOA(2 * (m)) & 65535))
    unsigned ck[2];
    const unsigned tag4 = tag * 4u;
    auto p_chk = [&](int pt, int pr, int k) {
      u32x4* q = P[pt] + 4 * pr;
      if (k < 2) ck[k & 1] = q[k & 1][0] + q[k & 1][1];
      else if (k < 8) ck[k & 1] = ck[k & 1] + q[(k & 1) + 2 * ((k >> 2) & 1)][k & 2] + q[(k & 1) + 2 * ((k >> 2) & 1)][(k & 2) + 1];
      else if (k == 8) ck[0] = pt ? ck[0] + ck[1] : sum_rows_u(ck[0] + ck[1]);
      if (k < 9) asm volatile("" : "+v"(ck[k & 1]));
      if (k == 9) {
        if (__builtin_expect(__any(ck[0] != tag4), 0)) {
          _Pragma("unroll 1") for (unsigned spins = 0; spins < SPIN_CAP; ++spins) {
            unsigned d = 0u;
#pragma unroll
            for (int i = 0; i < 4; ++i) ld_p(pt, 4 * pr + i);
#pragma unroll
            for (int i = 0; i < 4; ++i) d += q[i][0] + q[i][1] + q[i][2] + q[i][3];
            if (!pt) d = sum_rows_u(d);
            if (!__any(d != tag4)) break;
            __builtin_amdgcn_s_sleep(1);
          }
        }
      }
      if (k >= 10 && !pt) { unsigned x = q[(k - 10) & 3][0]; asm volatile("v_cndmask_b32 %0, %0, 0, %1" : "+v"(x) : "s"(hm)); q[(k - 10) & 3][0] = x; }
      if (k >= 10 && k < 13 && pt) { q[(k - 10) % 3][3] = q[3][(k - 10) % 3]; asm volatile("" : "+v"(q[(k - 10) % 3])); }
    };
    sfor<0, 224>([&](auto M_) __attribute__((always_inline)) {
      constexpr int mm = decltype(M_)::value, j = mm >> 5, m = mm & 31, y = m - (32 - 4 * RO);
      constexpr int t = j == 6 && y >= 0 ? 8 - RO + (y % (2 * RO + !RO)) / 2 : m >> 2, r = j == 6 && y >= 0 ? (y & 1) + 2 * (y / (2 * RO + !RO)) : m & 3, f = 8 * j + t;
      constexpr Sch C = j < JF ? S : F, B = j <= JF ? S : F;
      if (t == 0) asm volatile("v_mfma_f32_16x16x32_bf16 %0, %1, %2, 0" : "=&v"(acc2[j & 1][r]) : "v"(zw[f % NW]), "v"(wsp[r][t]), "v"(zw[(f | (f < 8 ? 1 : VM)) % NW]));
      else asm volatile("v_mfma_f32_16x16x32_bf16 %0, %1, %2, %0" : "+v"(acc2[j & 1][r]) : "v"(zw[f % NW]), "v"(wsp[r][t]), "v"(zw[(f | (f < 8 ? 1 : VM)) % NW]));
      SCHED();
      if (C.pr[m] == 1) __builtin_amdgcn_s_setprio(1);
      if ((m == C.b1 && j > 0) || (m == C.b2 && j > 0) || mm == 30) { asm volatile("" ::: "memory"); __builtin_amdgcn_s_waitcnt(0xC07F); RBAR(); SCHED(); }
      sfor<0, 16>([&](auto I_) __attribute__((always_inline)) {
        constexpr int i = decltype(I_)::value;
        if (j > 0 && C.wp[i] == m) { RED_PUT1(j + 1, i); SCHED(); }
        if (j > 0 && i < 8 && C.rp[i & 7] == m) { RED_GET(i & 7); SCHED(); }
      });
      if (j == 5 && m == 20) {
#pragma unroll
        for (int i = 0; i < 3; ++i) { wb[i] = (unsigned)(kh * 1024 + i * 65536) + l16; asm volatile("" : "+v"(wb[i])); }
      }
      if (XE && j == 5 && m == 13) {
        asm volatile("v_lshrrev_b32 %0, 1, %1" : "=v"(l8) : "v"(l16));
        xo = (l8 & 0x180u) + (unsigned)psi((l8 >> 3) & 15) * 8u;
        asm volatile("" : "+v"(xo));
      }
      if (RO && j == 6 && m >= 28) { RED_PUT1(6, 2 * (m - 28)); RED_PUT1(6, 2 * (m - 28) + 1); SCHED(); }
      if (CE == 1 && j == 6 && m >= 19 && m < 22) sfor<0, 3>([&](auto K_) __attribute__((always_inline)) { p_chk(0, 0, 3 * (m - 19) + decltype(K_)::value); SCHED(); });
      constexpr int ap[20] = {7, 8, 9, 10, 11, 13, 13, 14, 14, 19, 20, 21, 22, 23, 24, 25, 26, 27, 28, 29};
      sfor<0, NA0>([&](auto N_) __attribute__((always_inline)) { if (j == 6 && ap[AP0 + decltype(N_)::value] == m) { LDA(decltype(N_)::value); SCHED(); } });
      sfor<0, 22>([&](auto K_) __attribute__((always_inline)) {
        constexpr int k = decltype(K_)::value;
        if (j > 1 && B.vp[k] == m + 32) { red_fin(j > 1 ? j - 2 : 0, k, B.vp[16] == B.vp[18]); SCHED(); }
        if (j > 0 && C.vp[k] == m) { red_fin(j > 0 ? j - 1 : 0, k); SCHED(); }
      });
      if (C.pr[m] == 2) __builtin_amdgcn_s_setprio(0);
      if ((m & 3) == C.lp[m >> 2]) {
        if (f > 0 && f - 1 + NW < 56) LDZ(f - 1 + NW);
        if (j == 6 && (m >> 2) >= P1S) ld_p(0, ((m >> 2) - P1S) & 7);
        if (XE && j == 5 && m >= 20) xp[((m >> 2) - 5) % 3] = __builtin_amdgcn_raw_buffer_load_b64(rs, xo + (unsigned)((m >> 2) - 5) * 512u, xs, BULK_AUX);
        SCHED();
      }
    });
    STAMP(15);
#define NM0 0
#define OAUX 0
#define GM 19
#define MB 24
    f32x4 acc[5];
    f32x4 xf[3], gf[3];
    unsigned go, oo, eo, ho, tw, tr, o1, ta, tb;
    const unsigned s2 = (unsigned)(L_MISC + 128 * kh), s4 = s1 - (unsigned)kh * 1024u;
    unsigned long long om = kh ? ~0ull : 0xccccccccccccccccull;
#define PIN2(a, b) asm volatile("" : "+v"(a), "+v"(b))
    auto glue = [&](int k) {
      if (k == 0) { ta = l8 >> 5; tb = l16 & 48u; PIN2(ta, tb); }
      if (k == 1) { tr = __umul24(ta, 112u) + tb; oo = ta & 12u; PIN2(tr, oo); }
      if (k == 2) { tr += s1; oo += ta; PIN2(tr, oo); }
      if (k == 3) { oo = __umul24(oo, 1280u) + tb; ta = l8 >> 4; PIN2(oo, ta); }
      if (k == 4) { ta &= 24u; tb = l16 & 0xf0u; PIN2(ta, tb); }
      if (k == 5) { tw = __umul24(tb, 7u) + ta; go = (ta << 1) + s2; PIN2(tw, go); }
      if (k == 6) { tw += s1; asm volatile("v_cndmask_b32 %0, %1, -2.0, %2" : "=v"(o1) : "v"(oo), "s"(om)); asm volatile("" : "+v"(tw)); }
      if (k == 7) { eo = l16 + s3; ho = l16 + s4; PIN2(eo, ho); }
      if (k >= 8 && k < 14) {
        const unsigned x = xp[((k - 8) >> 1) % 3][k & 1];
        float lo = bflo(x), hi = bfhi(x);
        PIN2(lo, hi);
        xf[((k - 8) >> 1) % 3][2 * (k & 1)] = lo; xf[((k - 8) >> 1) % 3][2 * (k & 1) + 1] = hi;
      }
    };
    auto g3 = [&](auto M0_, auto M1_) __attribute__((always_inline)) {
      sfor<decltype(M0_)::value, decltype(M1_)::value>([&](auto M_) __attribute__((always_inline)) {
        constexpr int mi = decltype(M_)::value;
        constexpr int m = mi < 55 ? mi : mi < 64 ? 55 + 5 * ((mi - 55) / 3) + (mi - 55) % 3 : 58 + 5 * ((mi - 64) / 2) + (mi & 1), u2 = m / 5;
        const u32x4 fb = u2 < 8 ? P[0][u2 & 7] : P[1][u2 < 11 ? (u2 - 8) & 3 : (u2 - 7) & 7];
        if (m < 5) asm volatile("v_mfma_f32_16x16x32_bf16 %0, %1, %2, 0" : "=&v"(acc[m % 5]) : "v"(af[m % NAF]), "v"(fb));
        else asm volatile("v_mfma_f32_16x16x32_bf16 %0, %1, %2, %0" : "+v"(acc[m % 5]) : "v"(af[m % NAF]), "v"(fb));
        SCHED();
        if (mi == NM0) RBAR();
        if (mi >= NM0 && mi < NM0 + 3) dma1k(rs, lds + wl[(mi - NM0) % 3], l16, wg + wl[(mi - NM0) % 3]);
        if (mi >= 5 && mi < 19) p_chk(0, 1, mi - 5);
        if (mi >= GM && mi < GM + 14) glue(mi - GM);
        if (mi >= 47 && mi < 54) { p_chk(1, 1, 2 * (mi - 47)); SCHED(); p_chk(1, 1, 2 * (mi - 47) + 1); }
        if (!XE && mi == 3) { xo = (l8 & 0x180u) + (unsigned)psi((l8 >> 3) & 15) * 8u; asm volatile("" : "+v"(xo)); }
        if (!XE && mi >= 4 && mi < 7) xp[(mi - 4) % 3] = __builtin_amdgcn_raw_buffer_load_b64(rs, xo + (unsigned)(mi - 4) * 512u, xs, BULK_AUX);
        if (mi >= NM0 && mi < 40 && NA1 + mi - NM0 < 60) LDA(NA1 + mi - NM0);
        if (mi >= 66 && mi < 69) *(f32x4*)(lds + eo + (mi - 66) * 1024) = acc[(mi - 66) % 3];
        SCHED();
      });
    };
    __builtin_amdgcn_s_setprio(3);
#pragma unroll
    for (int k = 18; k < 22; ++k) red_fin(5, k);
    if (!XE) asm volatile("v_lshrrev_b32 %0, 1, %1" : "=v"(l8) : "v"(l16));
    if (RO) asm volatile("s_nop 2"); else { RED_PUT(6, 0); RED_PUT(6, 1); }
    RED_PUT(6, 2); RED_PUT(6, 3);
    red_fin(6, 16); red_fin(6, 17);
    SCHED();
#pragma unroll
    for (int i = 8 - P1S; i < 8 - P1S + P1B; ++i) ld_p(0, i);
    SCHED();
    BAR();
    sfor<0, 8>([&](auto I_) __attribute__((always_inline)) { RED_GET(decltype(I_)::value); });
    SCHED();
#pragma unroll
    for (int n = NA0; n < NA1; ++n) LDA(n);
#pragma unroll
    for (int i = 8 - P1S + P1B; i < 8; ++i) ld_p(0, i);
    SCHED();
#pragma unroll
    for (int k = 0; k < (CE == 2 ? 9 : 0); ++k) p_chk(0, 0, k);
    SCHED();
    sfor<0, 20>([&](auto K_) __attribute__((always_inline)) {
      constexpr int k = decltype(K_)::value;
      if (k < 8 && !(k & 1)) asm volatile("s_waitcnt lgkmcnt(%0)" :: "n"(6 - k + NA1 - NA0) : "memory");
      red_fin(6, k < 16 ? k : k + 2);
    });
    SCHED();
    __builtin_amdgcn_s_setprio(0);
    STAMPT(16);
#pragma unroll
    for (int k = CE ? 9 : 0; k < 14; ++k) p_chk(0, 0, k);
    SCHED();
    g3(IC<0>(), IC<MB>());
    STAMPT(22);
    DRAIN();
    RBAR();
    STAMPT(27);
#define MY 12
    if (MY == 0 || !kh) {
#pragma unroll
      for (int i = 0; i < 8; ++i) { ld_p(1, i); SCHED(); }
    }
    g3(IC<MB>(), IC<MB + MY>());
    if (MY > 0 && kh) {
#pragma unroll
      for (int i = 0; i < 8; ++i) { ld_p(1, i); SCHED(); }
    }
    STAMPT(26);
    g3(IC<MB + MY>(), IC<40>());
    STAMPT(24);
#pragma unroll
    for (int m = NA1 + 40 - NM0; m < 70; ++m) LDA(m);
#pragma unroll
    for (int x = 0; x < 3; ++x) gf[x] = *(const f32x4*)(lds + go + x * 64);
    SCHED();
    const rsrc_t ro = __builtin_amdgcn_make_buffer_rsrc((void*)p.out, 0, S_ * D_ * 2, 0x00020000);
    STAMPT(17);
#pragma unroll
    for (int k = 0; k < 14; ++k) p_chk(1, 0, k);
    asm volatile("s_nop 1");
    STAMPT(25);
    g3(IC<40>(), IC<70>());
    STAMPT(18);
    BAR();
    STAMPT(28);
    f32x4 o4[3];
    u32x4 ov[2];
#pragma unroll
    for (int j = 2; j >= 0; --j) o4[j] = *(const volatile LAS f32x4*)(lds + ho + j * 1024);
    asm volatile("" : "+v"(acc[2]), "+v"(acc[3]), "+v"(acc[4]));
    SCHED();
#pragma unroll
    for (int j = 2; j >= 0; --j) {
      const f32x4 v = __builtin_elementwise_fma(gf[j], o4[j] + acc[2 + j], xf[j]);
      *(volatile LAS u32x2*)(lds + tw + j * 32) = (u32x2){pack2(v[0], v[1]), pack2(v[2], v[3])};
      if (j < 2) ov[j] = *(const volatile LAS u32x4*)(lds + tr + j * 32);
    }
    ST16S(ov[1], ro, oo + 32u, os, OAUX);
    ST16S(ov[0], ro, o1, os, OAUX);
    if (bid == 0 && w == 5 && l8 == 0u) st4(rs, WS_EPOCH, tag);
  }
  STAMP(19);
}

extern "C" __device__ __attribute__((used, noinline, visibility("hidden"))) void mega_tail() {}

extern "C" void kernel_launch(void* const* d_in, const int* in_sizes, int n_in, void* d_out, int out_size,
                              void* d_ws, size_t ws_size, hipStream_t stream) {
  Params p;
  p.q = (const u16*)d_in[0];
  p.k = (const u16*)d_in[1];
  p.v = (const u16*)d_in[2];
  p.shift_k = (const u16*)d_in[3];
  p.ssm_k = (const u16*)d_in[4];
  p.ssm_D = (const u16*)d_in[5];
  p.w_in = (const u16*)d_in[7];
  p.w_sp = (const u16*)d_in[8];
  p.b_sp = (const u16*)d_in[9];
  p.w_out = (const u16*)d_in[10];
  p.cvec = (const u16*)d_in[11];
  p.w_mod = (const u16*)d_in[12];
  p.out = (u16*)d_out;
  p.ws = (char*)d_ws;
  mega<<<256, 512, 0, stream>>>(p);
}
```

```cpp
#include <hip/hip_runtime.h>
#include <stdint.h>

typedef unsigned short u16;
typedef __attribute__((ext_vector_type(8))) short bf16x8;
typedef __attribute__((ext_vector_type(4))) short bf16x4;
typedef __attribute__((ext_vector_type(4))) float f32x4;
typedef __attribute__((ext_vector_type(2))) float f32x2;
typedef __attribute__((ext_vector_type(4))) unsigned int u32x4;
typedef __attribute__((ext_vector_type(2))) unsigned int u32x2;
#define LAS __attribute__((address_space(3)))
typedef __amdgpu_buffer_rsrc_t rsrc_t;
typedef __attribute__((ext_vector_type(4))) short s16x4;

constexpr int S_ = 2048, D_ = 640, F_ = 1728, FH = 864, CB = 108;

constexpr unsigned WS_EPOCH = 0;
constexpr unsigned WS_FWI = 4096;
constexpr unsigned WS_FZ = WS_FWI + 384 * 128;
constexpr unsigned WS_FWO = WS_FZ + 256 * 128;
constexpr unsigned F4S = 896 * 128;
constexpr unsigned WS_CS = WS_FZ + 256 * 128;
constexpr unsigned WS_MOD = 131072;
constexpr unsigned WS_MODP = WS_MOD + 81920;
constexpr unsigned WS_ST1 = 393216;
constexpr unsigned WS_PART = 524288;
constexpr unsigned WS_CSP = WS_PART + 262144;
constexpr unsigned WS_FX = WS_CSP + 655360;
constexpr unsigned WS_X1B = WS_FX + 16 * 4 * 160 * 128;
constexpr unsigned WS_QB = WS_X1B + 2621440;
constexpr unsigned WS_KV3 = WS_QB + 2621440;
constexpr unsigned WS_MODQ = WS_KV3 + 1310720;
constexpr unsigned WBLK = 143360;
constexpr unsigned WS_WINF = WS_MODQ + 245760;
constexpr unsigned WS_WOUTF = WS_WINF + 16 * WBLK;
constexpr unsigned WS_Z2F = WS_WOUTF + 8 * WBLK;
constexpr unsigned WS_UF = WS_Z2F + 8 * 64 * 7168;
constexpr unsigned WS_F4 = WS_UF + 32 * 4 * 8 * 4096;
constexpr unsigned WS_QCK = WS_F4 + 4 * F4S;
constexpr unsigned WS_END = WS_QCK + 131072;
constexpr unsigned WS_STAMP = 200u << 20;

constexpr int G_COPY = 4160;
constexpr int L_U = WBLK;
constexpr int U_CH = (256 + S_) * 2 + 32;
constexpr int L_FLG = L_U + 4 * U_CH;
constexpr int L_RT = 16384;
constexpr int L_CONST = WBLK;
constexpr int L_PST = L_CONST + 2048;
constexpr int L_Z = L_CONST + 5120;
constexpr int L_ST2 = L_Z + 14336;
constexpr int L_MISC = L_ST2 + 512;
constexpr int LDS_TOTAL = 163840;

#define AUX_T 610
#define RT2_T 360
#define CB_T 380
#define W_T 700
#define YG_T 890
#define BG 1
#define BK 12
#define YW 4
#define NOPK 1
#define ME 1
#define MPR 3
#define G2E 1
#define B0 0
#define RTE 0
#define WC_T 0
#define QC 1
#define KE_T 460
#define QG 0
#define QA 4
#define QM 6
#define GE 0
#define YPR 0
#define YSL 2
#define K_T 480
#ifndef M_T
#define M_T 0
#endif
#define WA_T 580
constexpr int SC1 = 16, NT = 2;
constexpr unsigned SPIN_CAP = 1u << 16;

__device__ __forceinline__ float bflo(unsigned x) { return __uint_as_float(x << 16); }
__device__ __forceinline__ float bfhi(unsigned x) { return __uint_as_float(x & 0xffff0000u); }
__device__ __forceinline__ unsigned f2bf(float x) {
  __bf16 b = (__bf16)x;
  return (unsigned)__builtin_bit_cast(unsigned short, b);
}
__device__ __forceinline__ float rbf(float x) { return bflo(f2bf(x)); }
typedef __attribute__((ext_vector_type(2))) __bf16 bf2_t;
__device__ __forceinline__ unsigned pack2(float lo, float hi) {
  f32x2 v = {lo, hi};
  return __builtin_bit_cast(unsigned, __builtin_convertvector(v, bf2_t));
}
__device__ __forceinline__ f32x2 rbf2(f32x2 v) {
  const unsigned p = __builtin_bit_cast(unsigned, __builtin_convertvector(v, bf2_t));
  return (f32x2){__uint_as_float(p << 16), __uint_as_float(p & 0xffff0000u)};
}
__device__ __forceinline__ f32x4 rbf4(f32x4 v) {
  const f32x2 a = rbf2((f32x2){v[0], v[1]}), b = rbf2((f32x2){v[2], v[3]});
  return (f32x4){a[0], a[1], b[0], b[1]};
}
__device__ __forceinline__ f32x4 fma4(f32x4 a, f32x4 b, f32x4 c) {
  float r[4];
  asm("v_fma_f32 %0, %4, %8, %12\n\tv_fma_f32 %1, %5, %9, %13\n\tv_fma_f32 %2, %6, %10, %14\n\tv_fma_f32 %3, %7, %11, %15"
      : "=&v"(r[0]), "=&v"(r[1]), "=&v"(r[2]), "=&v"(r[3])
      : "v"(a[0]), "v"(a[1]), "v"(a[2]), "v"(a[3]), "v"(b[0]), "v"(b[1]), "v"(b[2]), "v"(b[3]), "v"(c[0]), "v"(c[1]), "v"(c[2]), "v"(c[3]));
  return (f32x4){r[0], r[1], r[2], r[3]};
}
template <int CTRL>
__device__ __forceinline__ float dpp_f(float v) {
  return __int_as_float(__builtin_amdgcn_update_dpp(0, __float_as_int(v), CTRL, 0xf, 0xf, true));
}
__device__ __forceinline__ float sum8(float v) {
  v += dpp_f<0xB1>(v);
  v += dpp_f<0x4E>(v);
  v += dpp_f<0x141>(v);
  return v;
}
__device__ __forceinline__ float sum16(float v) {
  v = sum8(v);
  v += dpp_f<0x140>(v);
  return v;
}
__device__ __forceinline__ float sum_rows(float v) {
  auto a = __builtin_amdgcn_permlane16_swap(__float_as_uint(v), __float_as_uint(v), false, false);
  v = __uint_as_float(a[0]) + __uint_as_float(a[1]);
  auto b = __builtin_amdgcn_permlane32_swap(__float_as_uint(v), __float_as_uint(v), false, false);
  return __uint_as_float(b[0]) + __uint_as_float(b[1]);
}
#define MFMA16(a, b, c) __builtin_amdgcn_mfma_f32_16x16x32_bf16(__builtin_bit_cast(bf16x8, a), __builtin_bit_cast(bf16x8, b), c, 0, 0, 0)
#define MFMA_IP(c, a, b) asm volatile("v_mfma_f32_16x16x32_bf16 %0, %1, %2, %0" : "+v"(c) : "v"(a), "v"(b))
#define MFMA_0(c, a, b) asm volatile("v_mfma_f32_16x16x32_bf16 %0, %1, %2, 0" : "=&v"(c) : "v"(a), "v"(b))
#define MFMA16K16(a, b, c) __builtin_amdgcn_mfma_f32_16x16x16bf16_1k(__builtin_bit_cast(bf16x4, a), __builtin_bit_cast(bf16x4, b), c, 0, 0, 0)
__device__ __forceinline__ u32x2 trd(const char* p) {
  return __builtin_bit_cast(u32x2, __builtin_amdgcn_ds_read_tr16_b64_v4i16((LAS s16x4*)p));
}
__device__ __forceinline__ u32x4 trfrag(const char* p, int pitch) {
  u32x2 a = trd(p), b = trd(p + 16 * pitch);
  return (u32x4){a[0], a[1], b[0], b[1]};
}
#define BAR() asm volatile("s_waitcnt lgkmcnt(0)\n\ts_barrier" ::: "memory")
#define DRAIN() do { __builtin_amdgcn_s_waitcnt(0x0F70); asm volatile("" ::: "memory"); } while (0)
#define SCHED() __builtin_amdgcn_sched_barrier(0)
template <int N> struct IC { static constexpr int value = N; };
template <int I, int N, class F>
__device__ __forceinline__ void sfor(F&& f) {
  if constexpr (I < N) { f(IC<I>()); sfor<I + 1, N>(f); }
}

__device__ __forceinline__ u32x4 ld16(rsrc_t r, unsigned off) { return __builtin_amdgcn_raw_buffer_load_b128(r, off, 0, SC1); }
__device__ __forceinline__ u32x2 ld8(rsrc_t r, unsigned off) { return __builtin_amdgcn_raw_buffer_load_b64(r, off, 0, SC1); }
__device__ __forceinline__ unsigned ld4(rsrc_t r, unsigned off) { return __builtin_amdgcn_raw_buffer_load_b32(r, off, 0, SC1); }
#define ST_AUX 16
__device__ __forceinline__ void st16(rsrc_t r, unsigned off, u32x4 v) { __builtin_amdgcn_raw_buffer_store_b128(v, r, off, 0, ST_AUX); }
#define ST16S(v, r, voff, soff, aux) do { const u32x4 v_ = (v); __builtin_amdgcn_raw_buffer_store_b128(v_, r, voff, soff, aux); asm volatile("s_nop 1" :: "v"(v_)); } while (0)
__device__ __forceinline__ void st8(rsrc_t r, unsigned off, u32x2 v) { __builtin_amdgcn_raw_buffer_store_b64(v, r, off, 0, SC1); }
__device__ __forceinline__ void st_flag4(rsrc_t r, unsigned off, unsigned tag, int l) {
  if (l < 32) st16(r, WS_F4 - WS_FWI + off + (unsigned)(l >> 3) * F4S + (unsigned)(l & 7) * 16u, (u32x4){tag, tag, tag, tag});
}
__device__ __forceinline__ void st4(rsrc_t r, unsigned off, unsigned v) { __builtin_amdgcn_raw_buffer_store_b32(v, r, off, 0, SC1); }
#define BULK_AUX 16
__device__ __forceinline__ u32x4 ld16p(rsrc_t r, unsigned off) { return __builtin_amdgcn_raw_buffer_load_b128(r, off, 0, BULK_AUX); }
__device__ __forceinline__ u32x2 ld8p(rsrc_t r, unsigned off) { return __builtin_amdgcn_raw_buffer_load_b64(r, off, 0, BULK_AUX); }
__device__ __forceinline__ void dma1k(rsrc_t r, char* lds_dst, unsigned l16, unsigned soff) {
  __builtin_amdgcn_raw_ptr_buffer_load_lds(r, (LAS void*)lds_dst, 16, l16, soff, 0, BULK_AUX);
}

#define STAMP(i)
#define STAMPF(i)
#define STAMPT(i) STAMP(i)

template <int N, int SLP>
__device__ __forceinline__ void wait_lines(rsrc_t r, const unsigned (&off)[N], unsigned tag) {
  bool need[N];
#pragma unroll
  for (int i = 0; i < N; ++i) need[i] = off[i] != ~0u;
  _Pragma("unroll 1") for (unsigned spins = 0; spins < SPIN_CAP; ++spins) {
    unsigned v[N];
#pragma unroll
    for (int i = 0; i < N; ++i) {
      v[i] = tag;
      if (need[i]) v[i] = ld4(r, off[i]);
    }
    bool any = false;
#pragma unroll
    for (int i = 0; i < N; ++i) { need[i] = v[i] != tag; any = any || need[i]; }
    if (!__any(any)) break;
    __builtin_amdgcn_s_sleep(SLP);
  }
  asm volatile("" ::: "memory");
}

__device__ __forceinline__ int tau(int rt, int n) { return 32 * (rt >> 1) + 8 * (n >> 2) + 4 * (rt & 1) + (n & 3); }
__device__ __forceinline__ int psi(int n) { return 2 * (n & 7) + (n >> 3); }
#define QX_CM(d, s0, s1, P) "v_cndmask_b32_dpp " d ", " s0 ", " s1 ", vcc quad_perm:" P " row_mask:0xf bank_mask:0xf\n\t"
#define QX_BODY(M, P) \
  asm("s_mov_b32 vcc_lo, " M "\n\ts_mov_b32 vcc_hi, " M "\n\t" \
      QX_CM("%0", "%12", "%8", P) "v_xor_b32 %4, %8, %0\n\t" QX_CM("%1", "%13", "%9", P) "v_xor_b32 %5, %9, %1\n\t" \
      QX_CM("%2", "%14", "%10", P) "v_xor_b32 %6, %10, %2\n\t" QX_CM("%3", "%15", "%11", P) "v_xor_b32 %7, %11, %3" \
      : "=&v"(t[0]), "=&v"(t[1]), "=&v"(t[2]), "=&v"(t[3]), "=&v"(u[0]), "=&v"(u[1]), "=&v"(u[2]), "=&v"(u[3]) \
      : "v"(a[0]), "v"(a[1]), "v"(a[2]), "v"(a[3]), "v"(b[0]), "v"(b[1]), "v"(b[2]), "v"(b[3]) : "vcc")
template <int BIT>
__device__ __forceinline__ void quad_xchg3(u32x4& a, u32x4& b) {
  unsigned t[4], u[4];
  if (BIT == 0) QX_BODY("0x55555555", "[1,0,3,2]");
  else QX_BODY("0x33333333", "[2,3,0,1]");
  a = (u32x4){t[0], t[1], t[2], t[3]};
#pragma unroll
  for (int d = 0; d < 4; ++d) b[d] ^= (unsigned)__builtin_amdgcn_update_dpp(0, (int)u[d], BIT == 0 ? 0xB1 : 0x4E, 0xf, 0xf, true);
}
__device__ __forceinline__ void quad_transpose(u32x4& x0, u32x4& x1, u32x4& x2, u32x4& x3) {
  quad_xchg3<0>(x0, x1);
  quad_xchg3<0>(x2, x3);
  quad_xchg3<1>(x0, x2);
  quad_xchg3<1>(x1, x3);
  asm volatile("" : "+v"(x0), "+v"(x1), "+v"(x2), "+v"(x3));
}

struct Params {
  const u16 *q, *k, *v, *shift_k, *ssm_k, *ssm_D, *w_in, *w_sp, *b_sp, *w_out, *cvec, *w_mod;
  u16* out;
  char* ws;
};

__global__ __launch_bounds__(512, 2) void mega(Params p) {
  __shared__ __attribute__((aligned(16))) char lds[LDS_TOTAL];
  {
    unsigned long long pc0;
    unsigned vt;
    asm volatile("v_readfirstlane_b32 vcc_lo, %3\n\ts_cmpk_lt_u32 %2, 192\n\ts_cbranch_scc1 4f\n\ts_cmpk_lt_u32 vcc_lo, 64\n\ts_cbranch_scc0 4f\n\t"
                 "s_lshr_b32 vcc_hi, %2, 3\n\ts_sub_u32 vcc_hi, vcc_hi, 24\n\ts_lshl_b32 vcc_hi, vcc_hi, 13\n\t"
                 "v_lshlrev_b32 %1, 7, %3\n\tv_add_u32 %1, vcc_hi, %1\n\ts_getpc_b64 %0\n.Lmega_here:\n\t"
                 "v_min_u32 %1, mega_tail-.Lmega_here-4, %1\n\ts_movk_i32 m0, 0x2000\n\ts_nop 1\n\tglobal_load_lds_dword %1, %0\n4:\n\t"
                 "s_cmpk_lt_u32 %2, 160\n\ts_cbranch_scc0 1f\n\ts_cmpk_lt_u32 vcc_lo, 256\n\ts_cbranch_scc1 3f\n\t"
                 "s_call_b64 vcc, .Ltouch1\n\ts_branch 3f\n1:\n\t"
                 "s_cmpk_lt_u32 vcc_lo, 256\n\ts_cbranch_scc1 2f\n\ts_cmpk_lt_u32 %2, 224\n\ts_cbranch_scc0 2f\n\ts_call_b64 vcc, .Ltouch3\n\ts_branch 3f\n"
                 "2:\n\ts_call_b64 vcc, .Ltouch2\n3:"
                 : "=&s"(pc0), "=&v"(vt) : "s"(blockIdx.x), "v"(threadIdx.x) : "vcc", "scc", "memory");
  }
  SCHED();
  const int tid = threadIdx.x, l = tid & 63;
  const int w = __builtin_amdgcn_readfirstlane(tid >> 6);
  const int bid = blockIdx.x, xcd = bid & 7, idx = bid >> 3;
#define TOUCH_AT(n) asm volatile("s_branch 1f\n.Ltouch" #n ":\n\ts_setpc_b64 vcc\n1:")
  char* const ws = p.ws;
  asm volatile("" ::"s"(p.q), "s"(p.k), "s"(p.v), "s"(p.shift_k), "s"(p.ssm_k), "s"(p.ssm_D), "s"(p.w_in), "s"(p.w_sp), "s"(p.b_sp),
               "s"(p.w_out), "s"(p.cvec), "s"(p.w_mod), "s"(p.out), "s"(p.ws));
  unsigned epoch, tag, t_start;
  unsigned long long t64;
  asm volatile("s_load_dword %0, %2, 0x0\n\ts_memrealtime %1" : "=&s"(epoch), "=&s"(t64) : "s"(ws));
#define TAG_READY() do { SCHED(); \
    asm volatile("s_waitcnt lgkmcnt(0)\n\ts_mul_i32 %0, %2, 0x9e3779b1\n\ts_add_i32 %0, %0, 0x9e3779b9\n\ts_mov_b64 %1, %3" \
                 : "=&s"(tag), "=&s"(t64r) : "s"(epoch), "s"(t64) : "scc"); t_start = (unsigned)t64r; SCHED(); } while (0)
  unsigned long long t64r;
  const rsrc_t rs = __builtin_amdgcn_make_buffer_rsrc(ws, 0, WS_END, 0x00020000);
  const unsigned l16 = (unsigned)l * 16u;
  STAMP(0);
  const bool is_conv = idx < 20;
  const int quad = xcd * 20 + idx;
  const int R = is_conv ? (xcd & 3) * 5 + (idx >> 2) : (xcd & 3) * 3 + 15 + (idx >> 2);
  const int c = (xcd >> 2) * 4 + (idx & 3);
  const unsigned fwi = WS_F4 + (unsigned)(xcd & 3) * F4S, fz = fwi + (WS_FZ - WS_FWI);
  const int n16 = l & 15, h4 = l >> 4;
  u32x4 wsp[4][8];
#define ROTC 2
#define ROTZ 3
#define WAUX 0
  const int rotg = (((ROTZ & 1) ? idx >> ((ROTZ & 4) ? 2 : 4) : 0) ^ ((ROTC & 2) ? idx >> 1 : 0)) & 1, rotb = (ROTZ & 2) ? (idx >> 2) & 3 : 0, rotr = (ROTC & 1) ? (idx & 1) * 2 : 0;
  const rsrc_t rsp = __builtin_amdgcn_make_buffer_rsrc((void*)p.w_sp, 0, S_ * S_ * 2, 0x00020000);
  const unsigned wsp_vo = (unsigned)(((l >> 2) & 3) * 8 * 4096 + h4 * 64 + ((l + rotb) & 3) * 16);
#define LDW(k) LDW_(k)
#define WSP_TR(g) WSP_TR_(g)
#define WROW R
#define LDW_(k) wsp[((k) >> 2) & 3][((k) >> 4) * 4 + ((k) & 3)] = __builtin_amdgcn_raw_buffer_load_b128(rsp, wvo, \
    (unsigned)((WROW * 64 + 16 * ((((k) >> 2) & 2) ^ rotr) + 4 * (((k) >> 2) & 1) + ((k) & 3)) * 4096 + w * 512 + ((((k) >> 4) ^ rotg) * 256)), WAUX)
#define WSP_TR_(g) quad_transpose(wsp[(g) & 3][((g) >> 2) * 4], wsp[(g) & 3][((g) >> 2) * 4 + 1], wsp[(g) & 3][((g) >> 2) * 4 + 2], wsp[(g) & 3][((g) >> 2) * 4 + 3])
#define WSP_Q(g, k) wsp[(g) & 3][(((g) >> 2) & 1) * 4 + (k)]
#define WSP_TX(x) do { if (((x) & 2) == 0) quad_xchg3<0>(WSP_Q((x) >> 2, 2 * ((x) & 1)), WSP_Q((x) >> 2, 2 * ((x) & 1) + 1)); \
    else quad_xchg3<1>(WSP_Q((x) >> 2, (x) & 1), WSP_Q((x) >> 2, ((x) & 1) + 2)); \
    asm volatile("" : "+v"(WSP_Q((x) >> 2, 0)), "+v"(WSP_Q((x) >> 2, 1)), "+v"(WSP_Q((x) >> 2, 2)), "+v"(WSP_Q((x) >> 2, 3))); } while (0)
  const rsrc_t rwi = __builtin_amdgcn_make_buffer_rsrc((void*)p.w_in, 0, D_ * F_ * 2, 0x00020000);
  const rsrc_t rwo = __builtin_amdgcn_make_buffer_rsrc((void*)p.w_out, 0, FH * D_ * 2, 0x00020000);
  const rsrc_t rq = __builtin_amdgcn_make_buffer_rsrc((void*)p.q, 0, S_ * D_ * 2, 0x00020000);
  auto hold = [&](unsigned tt) { while ((unsigned)__builtin_amdgcn_s_memrealtime() - t_start < tt) __builtin_amdgcn_s_sleep(2); };
  auto ne2 = [&](unsigned x, unsigned y) { unsigned d; asm("v_bitop3_b32 %0, %1, %2, %3 bitop3:0x7e" : "=v"(d) : "v"(x), "v"(y), "s"(tag)); return d; };
  auto sum8u = [&](unsigned v) {
    v += (unsigned)__builtin_amdgcn_update_dpp(0, (int)v, 0xB1, 0xf, 0xf, true);
    v += (unsigned)__builtin_amdgcn_update_dpp(0, (int)v, 0x4E, 0xf, 0xf, true);
    return v + (unsigned)__builtin_amdgcn_update_dpp(0, (int)v, 0x141, 0xf, 0xf, true);
  };
  volatile LAS unsigned* lf = (volatile LAS unsigned*)(lds + L_FLG);
  auto lf_set = [&](int i) { asm volatile("s_waitcnt lgkmcnt(0)" ::: "memory"); if (l == 0) lf[i] = tag; };
  auto lf_wait = [&](int i0, int n) {
    _Pragma("unroll 1") for (unsigned spins = 0; spins < (SPIN_CAP << 4); ++spins) {
      const unsigned v = lf[i0 + (l & (n - 1))];
      if (__all(v == tag)) break;
      __builtin_amdgcn_s_sleep(1);
    }
    asm volatile("" ::: "memory");
  };
  const unsigned b2o = fwi + (unsigned)((8 + c) * 20 + 5 * (w & 3) + (l < 5 ? l : 0)) * 128u;
  auto b2_wait = [&](u32x2 fv, int slp) {
    if (!__all(fv[0] == tag)) {
      _Pragma("unroll 1") for (unsigned spins = 0; spins < SPIN_CAP; ++spins) {
        if (fv[0] != tag) fv = ld8(rs, b2o);
        if (__all(fv[0] == tag)) break;
        if (slp) __builtin_amdgcn_s_sleep(8); else __builtin_amdgcn_s_sleep(4);
      }
    }
    asm volatile("" ::: "memory");
    if (l < 5) lf[44 + 5 * w + l] = fv[1];
    if (l == 0) lf[36 + w] = tag;
  };
  auto load_b2 = [&](int k0, int k1) {
    const unsigned a0 = (unsigned)(35 * w + (idx >> 2) * 4) * 1024u, gb = WS_WINF + (unsigned)(8 + c) * WBLK;
    unsigned a = a0, g = gb + a0;
#pragma unroll
    for (int k = k0; k < k1; ++k) {
      if (k == k0 || (k & 3) == 3) {
        a = k + (idx >> 2) * 4 >= 35 ? a0 - 35840u : a0;
        g = gb + a;
        asm volatile("" : "+s"(a), "+s"(g));
      }
      dma1k(rs, lds + a + k * 1024, l16, g + (unsigned)k * 1024u);
    }
  };
  char* const wo_st = lds + 122880;
  auto wo_ld = [&]() {
    const int U = R < 28 ? R : 0, u2 = U >> 1, fi = (u2 + 1) % 3;
    const int c0 = 4 * (U & 1) + (u2 < 8 ? u2 >> 1 : u2 < 11 ? 0 : 2);
    const int b0 = u2 < 8 ? CB * c0 + 2 * (u2 & 1) + 96 : CB * (c0 + (fi >> 1)) + 48 + 2 * (fi & 1), d1 = u2 < 8 ? -96 : 16, d2 = d1 + 16;
    const int b3 = u2 < 8 ? b0 - 64 : CB * (c0 + 1) + 50 + 16 * fi;
#pragma unroll
    for (int r = 0; r < 5; ++r) {
      const int g = r * 64 + l, rho = g / 10;
      const int fb = (rho & 16) ? ((rho & 2) ? b3 : b0 + d2) : ((rho & 2) ? b0 + d1 : b0);
      __builtin_amdgcn_raw_ptr_buffer_load_lds(rwo, (LAS void*)(wo_st + r * 1024), 16, (unsigned)((fb + (rho & 13)) * (D_ * 2) + (g % 10) * 16 + c * 160), 0, 0, 0);
    }
  };
  auto rt_ld = [&](int i0, char* st) {
    const int cb = (i0 < 160 ? 8 : 0) + (i0 & 7), Q = (i0 < 160 ? i0 : i0 - 160) >> 3;
#pragma unroll
    for (int r = 0; r < 7; ++r) {
      const int g = r * 64 + l;
      __builtin_amdgcn_raw_ptr_buffer_load_lds(rwi, (LAS void*)(st + r * 1024), 16, (unsigned)((g / 14) * (F_ * 2) + (g % 14) * 16 + Q * 32 * F_ * 2 + cb * CB * 2),
                                               0, 0, NT);
    }
  };
  auto rt_job = [&](int i0, char* st) {
    const int cb = (i0 < 160 ? 8 : 0) + (i0 & 7), Q = (i0 < 160 ? i0 : i0 - 160) >> 3;
    const int col = 640 * (l >> 5) + 32 * Q + (l & 31);
    u32x2 pp8[8];
    auto ld_pp = [&]() {
#pragma unroll
      for (int i = 0; i < 8; ++i) pp8[i] = __builtin_amdgcn_raw_buffer_load_b64(rs, (unsigned)col * 8u, WS_MODP + (unsigned)i * 10240u, SC1);
    };
    auto pp_bad = [&]() { return __any(((ne2(pp8[0][0], pp8[1][0]) | ne2(pp8[2][0], pp8[3][0]) | ne2(pp8[4][0], pp8[5][0])) | ne2(pp8[6][0], pp8[7][0])) != 0u); };
    hold(CB_T);
    ld_pp();
    SCHED();
    __builtin_amdgcn_s_waitcnt(0x0F78);
    asm volatile("" ::: "memory");
    u32x4 x[7], o[7];
    f32x2 xu[7][4];
    {
      const char* bp = st + (4 * h4 + (n16 >> 2)) * 224 + (n16 & 3) * 8;
#pragma unroll
      for (int j = 0; j < 7; ++j) x[j] = trfrag(bp + j * 32, 224);
#pragma unroll
      for (int j = 0; j < 7; ++j)
#pragma unroll
        for (int e = 0; e < 4; ++e) { xu[j][e] = (f32x2){bflo(x[j][e]), bfhi(x[j][e])}; asm volatile("" : "+v"(xu[j][e])); }
    }
    unsigned mk[4][2], cw = l >= 32 ? (unsigned)(7168 + (l - 32) * 4) : (unsigned)(7298 + l * 2 - 4), m12 = n16 == 12 ? ~0u : 0u;
    unsigned fgo = l < 32 ? (unsigned)(l >> 3) * F4S + (unsigned)(l & 7) * 16u : 0x80000000u;
#pragma unroll
    for (int i = 0; i < 4; ++i) {
      mk[i][0] = n16 == 4 * i ? 0x3F803F80u : 0u; mk[i][1] = n16 == 4 * i + 1 ? ~0u : 0u;
      asm volatile("" : "+v"(mk[i][0]), "+v"(mk[i][1]));
    }
    asm volatile("" : "+v"(cw), "+v"(m12), "+v"(fgo));
    unsigned ob = WS_WINF + (unsigned)cb * WBLK + (unsigned)(Q * 7) * 1024u;
    asm volatile("" : "+s"(ob));
    if (pp_bad()) {
      _Pragma("unroll 1") for (unsigned spins = 0; spins < SPIN_CAP; ++spins) {
        __builtin_amdgcn_s_sleep(4);
        ld_pp();
        if (!pp_bad()) break;
      }
    }
    STAMPF(9);
    __builtin_amdgcn_s_setprio(3);
    {
      auto pf = [&](int i) { return __uint_as_float(pp8[i][1]); };
      const float t = rbf(((pf(0) + pf(1)) + (pf(2) + pf(3))) + ((pf(4) + pf(5)) + (pf(6) + pf(7))));
      if (l >= 32) *(float*)(st + cw) = rbf(1.0f + t);
      else *(u16*)(st + cw + 2) = (u16)(__float_as_uint(t) >> 16);
    }
    asm volatile("" ::: "memory");
    const f32x4 ca = *(const volatile LAS f32x4*)(st + 7168 + h4 * 16), cc = *(const volatile LAS f32x4*)(st + 7168 + 64 + h4 * 16);
    const u32x2 e0 = *(const volatile LAS u32x2*)(st + 7296 + h4 * 8), e1 = *(const volatile LAS u32x2*)(st + 7328 + h4 * 8);
    SCHED();
#pragma unroll
    for (int j = 0; j < 7; ++j) {
      const f32x2 y0 = xu[j][0] * (f32x2){ca[0], ca[1]}, y1 = xu[j][1] * (f32x2){ca[2], ca[3]}, y2 = xu[j][2] * (f32x2){cc[0], cc[1]}, y3 = xu[j][3] * (f32x2){cc[2], cc[3]};
      o[j] = (u32x4){pack2(y0[0], y0[1]), pack2(y1[0], y1[1]), pack2(y2[0], y2[1]), pack2(y3[0], y3[1])};
      if (j == 6) {
#pragma unroll
        for (int e = 0; e < 4; ++e) o[j][e] = (o[j][e] & ~m12) | mk[3][0];
      }
      ST16S(o[j], rs, l16 + (unsigned)(j & 3) * 1024u, ob + (unsigned)(j >> 2) * 4096u, SC1);
      SCHED();
    }
    __builtin_amdgcn_s_setprio(0);
    STAMPF(11);
    f32x4 d[2] = {{0.f, 0.f, 0.f, 0.f}, {0.f, 0.f, 0.f, 0.f}};
#pragma unroll
    for (int j = 0; j < 7; ++j) {
      const unsigned one = mk[j & 3][0], sr = mk[j & 3][1];
      d[j >> 2] = MFMA16(((u32x4){one, one, one, one}), o[j], d[j >> 2]);
      d[j >> 2] = MFMA16(((u32x4){sr & e0[0], sr & e0[1], sr & e1[0], sr & e1[1]}), x[j], d[j >> 2]);
    }
#pragma unroll
    for (int m = 0; m < 2; ++m)
      st16(rs, WS_CSP + (unsigned)((cb * 20 + Q) * 2 + m) * 1024u + l16, (u32x4){tag, __float_as_uint(d[m][0]), tag, __float_as_uint(d[m][1])});
    STAMPF(13);
    u32x4 tg4 = {tag, t_start, tag, tag};
    asm volatile("s_waitcnt vmcnt(2)" : "+v"(tg4) :: "memory");
    ST16S(tg4, rs, fgo, WS_F4 + (unsigned)(cb * 20 + Q) * 128u, SC1);
  };

  auto red8 = [&](float v) { v += dpp_f<0x128>(v); return sum_rows(v); };
  const rsrc_t rwm = __builtin_amdgcn_make_buffer_rsrc((void*)p.w_mod, 0, D_ * 3840 * 2, 0x00020000);
  auto gemv_load = [&](u32x4* pre, int blk, int sl, int np) {
    pre[10][0] = *(const unsigned*)(p.cvec + 8 * np * sl + 2 * (l < 4 * np ? l : 0));
#pragma unroll
    for (int pp = 0; pp < np; ++pp)
      pre[pp] = __builtin_amdgcn_raw_buffer_load_b128(rwm, (unsigned)((l >> 3) * 7680 + (l & 7) * 16), (unsigned)((8 * np * sl + pp * 8) * 7680 + blk * 128), NT);
  };
  auto gemv_fin = [&](float* acc, const u32x4* pre, int np, char* scr) {
    const unsigned cv2 = pre[10][0];
    unsigned* svt = (unsigned*)scr + w * 96;
#pragma unroll
    for (int e = 0; e < 2; ++e) {
      const int row = 2 * l + e;
      const float cv = e ? bfhi(cv2) : bflo(cv2);
      if (l < 4 * np) svt[(row & 7) * 12 + (row >> 3)] = __float_as_uint(rbf(cv * __builtin_amdgcn_rcpf(1.0f + __expf(-cv))));
    }
    asm volatile("s_waitcnt lgkmcnt(0)" ::: "memory");
    u32x4 sv[3];
#pragma unroll
    for (int i = 0; i < (np + 3) / 4; ++i) sv[i] = *(const u32x4*)(svt + (l >> 3) * 12 + 4 * i);
    SCHED();
#pragma unroll
    for (int e = 0; e < 8; ++e) acc[e] = 0.f;
#pragma unroll
    for (int pp = 0; pp < np; ++pp) {
      const unsigned sh = sv[pp >> 2][pp & 3], sl2 = sh >> 16;
#pragma unroll
      for (int e = 0; e < 4; ++e) {
        const unsigned x = pre[pp][e];
        acc[2 * e] = __builtin_amdgcn_fdot2_f32_bf16(__builtin_bit_cast(bf2_t, x), __builtin_bit_cast(bf2_t, sl2), acc[2 * e], false);
        acc[2 * e + 1] = __builtin_amdgcn_fdot2_f32_bf16(__builtin_bit_cast(bf2_t, x), __builtin_bit_cast(bf2_t, sh), acc[2 * e + 1], false);
      }
    }
#pragma unroll
    for (int e = 0; e < 4; ++e) {
      auto x = __builtin_amdgcn_permlane32_swap(__float_as_uint(acc[e]), __float_as_uint(acc[e + 4]), false, false);
      acc[e] = __uint_as_float(x[0]) + __uint_as_float(x[1]);
    }
#pragma unroll
    for (int e = 0; e < 2; ++e) {
      auto x = __builtin_amdgcn_permlane16_swap(__float_as_uint(acc[e]), __float_as_uint(acc[e + 2]), false, false);
      acc[e] = __uint_as_float(x[0]) + __uint_as_float(x[1]);
    }
    const float t0 = acc[0] + dpp_f<0x128>(acc[0]), t1 = acc[1] + dpp_f<0x128>(acc[1]);
    acc[0] = (l & 8) ? t1 : t0;
  };
  auto gemv_put = [&](const float* acc, unsigned off) {
    const float v = acc[0];
    st8(rs, off + (unsigned)l * 8u, (u32x2){tag, (unsigned)__builtin_amdgcn_ds_bpermute(((l & 7) * 8 + (l >> 3)) * 4, (int)__float_as_uint(v))});
  };
  auto cs_ld = [&](u32x4* v, int cj) {
    const int cb = cj / 7, j = cj % 7;
#pragma unroll
    for (int k = 0; k < 5; ++k)
      v[k] = __builtin_amdgcn_raw_buffer_load_b128(rs, (unsigned)(h4 * 10240 + n16 * 16), WS_CSP + (unsigned)((cb * 40 + (j >> 2) + 2 * k) * 1024 + (j & 3) * 256), SC1);
  };
  auto cs_fin = [&](u32x4* v, int cj) {
    auto bad = [&]() { return __any(((ne2(v[0][0], v[0][2]) | ne2(v[1][0], v[1][2]) | ne2(v[2][0], v[2][2])) | ne2(v[3][0], v[3][2]) | ne2(v[4][0], v[4][2])) != 0u); };
    if (bad()) {
      _Pragma("unroll 1") for (unsigned spins = 0; spins < SPIN_CAP; ++spins) {
        __builtin_amdgcn_s_sleep(8);
        cs_ld(v, cj);
        if (!bad()) break;
      }
    }
    float t0 = 0.f, t1 = 0.f;
#pragma unroll
    for (int k = 0; k < 5; ++k) { t0 += __uint_as_float(v[k][1]); t1 += __uint_as_float(v[k][3]); }
    t0 = sum_rows(t0); t1 = sum_rows(t1);
    if (l < 16) st16(rs, WS_CS + (unsigned)((cj / 7) * 2048 + (cj % 7) * 256) + l16, (u32x4){tag, __float_as_uint(t0), tag, __float_as_uint(t1)});
  };
  const bool cs2 = w == 2 && R >= 20 && R < 27, cs1 = w == 2 && R >= 25;
  u32x4 csv[5];
  typedef const __attribute__((address_space(4))) unsigned* cptr_t;

  if (__builtin_expect(is_conv, 1)) {
    if (w >= 4) {
      TOUCH_AT(1);
      const int pw = w - 4;
      const rsrc_t ra = __builtin_amdgcn_make_buffer_rsrc((void*)((idx & 2) ? p.v : p.k), 0, S_ * D_ * 2, 0x00020000);
      const rsrc_t rb = __builtin_amdgcn_make_buffer_rsrc((void*)((idx & 2) ? p.k : p.v), 0, S_ * D_ * 2, 0x00020000);
      const rsrc_t rk = __builtin_amdgcn_make_buffer_rsrc((void*)p.k, 0, S_ * D_ * 2, 0x00020000);
      const rsrc_t rv = __builtin_amdgcn_make_buffer_rsrc((void*)p.v, 0, S_ * D_ * 2, 0x00020000);
#define NROT 2
      const unsigned vo = (unsigned)((128 * pw + 2 * l) * (D_ * 2));
      const unsigned so0 = (unsigned)((idx & 1) * (D_ * 2) + quad * 8), so1 = (unsigned)((1 - (idx & 1)) * (D_ * 2) + quad * 8);
      u32x2 g[3][4], kh;
      auto gl = [&](int s, int j0, int j1) {
        __builtin_amdgcn_s_setprio(0);
#pragma unroll
        for (int j = j0; j < j1; ++j) {
          if (s < NROT) g[s][j] = __builtin_amdgcn_raw_buffer_load_b64(j < 2 ? ra : rb, vo, ((j & 1) ? so1 : so0) + (unsigned)(s * 512 * D_ * 2), 0);
          else g[s][j] = __builtin_amdgcn_raw_buffer_load_b64(j < 2 ? rk : rv, vo + (unsigned)((j & 1) * D_ * 2), (unsigned)(quad * 8 + s * 512 * D_ * 2), 0);
          SCHED();
        }
        __builtin_amdgcn_s_setprio(3);
      };
      const cptr_t sp = (cptr_t)(p.shift_k + quad * 16);
      unsigned spv[8];
#pragma unroll
      for (int i = 0; i < 8; ++i) spv[i] = sp[i];
      __builtin_amdgcn_s_sleep(3);
      gl(0, 0, 4);
      {
        const int row = 512 * (l >> 2) + 128 * pw - 3 + (l & 3);
        kh = __builtin_amdgcn_raw_buffer_load_b64(rk, (l >= 12 || (l & 3) == 3 || row < 0) ? 0x80000000u : (unsigned)(row * D_ * 2 + quad * 8), 0, 0);
      }
      TAG_READY();
      f32x2 wt[2][4];
#pragma unroll
      for (int d = 0; d < 2; ++d) {
        wt[d][0] = (f32x2){bflo(spv[4 * d]), bflo(spv[4 * d + 2])}; wt[d][1] = (f32x2){bfhi(spv[4 * d]), bfhi(spv[4 * d + 2])};
        wt[d][2] = (f32x2){bflo(spv[4 * d + 1]), bflo(spv[4 * d + 3])}; wt[d][3] = (f32x2){bfhi(spv[4 * d + 1]), bfhi(spv[4 * d + 3])};
        asm volatile("" : "+s"(wt[d][0]), "+s"(wt[d][1]), "+s"(wt[d][2]), "+s"(wt[d][3]));
      }
      SCHED();
      {
        const int j = idx * 4 + pw;
        const unsigned o = (unsigned)((512 + 64 * (j >> 2) + l) * (D_ * 2) + (((160 * xcd) >> 7) + ((j >> 1) & 1)) * 128);
        __builtin_amdgcn_raw_ptr_buffer_load_lds((j & 1) ? ra : rb, (LAS void*)(lds + 98304), 4, j < 64 ? o : 0x80000000u, 0, 0, 0);
      }
      SCHED();
      gl(1, 0, 4);
      STAMP(21);
      unsigned up = (unsigned)(L_U + 512 + 256 * pw + (l >> 2) * 16 + (3 - (l & 3)) * 4);
      unsigned lfo = l == 0 ? (unsigned)(L_FLG + 4 * pw) : (unsigned)(100352 + tid * 4), lfr = (unsigned)(L_FLG + (l & 3) * 4);
      unsigned yr = (unsigned)(L_U + (n16 >> 2) * U_CH + (128 * pw + 32 * h4 + 8 * (n16 & 1) + 4 * ((n16 >> 1) & 1)) * 2);
      unsigned yo = (unsigned)(8 * pw + (l >> 3)) * 20480u + (unsigned)quad * 128u + (unsigned)(l & 7) * 16u;
      unsigned mo = (unsigned)((n16 < 12 ? n16 >> 2 : 0) * D_ + quad * 4 + (l & 3)) * 8u + (unsigned)h4 * 40960u;
      unsigned k3o = (unsigned)(16 * pw + (l >> 3)) * 20480u + (unsigned)(quad * 128 + (l & 7) * 16);
      unsigned k3d = (unsigned)(L_U + ((l >> 1) & 3) * U_CH + 512 + (1536 + 128 * pw + 8 * (l >> 3)) * 2 + (l & 1) * 8);
      unsigned xfo = (unsigned)(((4 * (l >> 5) + pw) * 4 + ((l >> 3) & 3)) * 160 + quad) * 128u + (unsigned)(l & 7) * 16u;
      u32x4 tg4 = {tag, tag, tag, tag};
      asm volatile("" : "+v"(up), "+v"(lfo), "+v"(lfr), "+v"(yr), "+v"(yo), "+v"(mo), "+v"(k3o), "+v"(k3d), "+v"(xfo), "+v"(tg4));
      unsigned so = WS_ST1 + (unsigned)xcd * 16384u + (unsigned)pw * 1024u;
      asm volatile("" : "+s"(so));
      auto pf_set = [&](int i) { asm volatile("" ::: "memory"); *(volatile LAS unsigned*)(lds + lfo + 4 * i) = tg4[0]; };
      auto pf_wait = [&](int i0) {
        _Pragma("unroll 1") for (unsigned spins = 0; spins < (SPIN_CAP << 4); ++spins) {
          if (__all(*(volatile LAS unsigned*)(lds + lfr + 4 * i0) == tag)) break;
          __builtin_amdgcn_s_sleep(1);
        }
        asm volatile("" ::: "memory");
      };
      unsigned hl[2][3];
      auto halo = [&](int s) {
#pragma unroll
        for (int i = 0; i < 6; ++i) { hl[i & 1][i >> 1] = (unsigned)__builtin_amdgcn_readlane(kh[i & 1], 4 * s + (i >> 1)); asm volatile("" : "+v"(hl[i & 1][i >> 1])); }
      };
      auto kv = [&](auto S__, auto N__) {
        constexpr int s = decltype(S__)::value;
        u32x2* x = g[s];
        SCHED();
        asm volatile("s_waitcnt vmcnt(%5)" : "+v"(x[0]), "+v"(x[1]), "+v"(x[2]), "+v"(x[3]), "+v"(kh) : "n"(decltype(N__)::value));
        if (s == 2) STAMP(27);
        if (s == 0) halo(0);
        if (s < NROT) {
          if (idx & 1) { asm volatile(""); const u32x2 t = x[0], u = x[2]; x[0] = x[1]; x[1] = t; x[2] = x[3]; x[3] = u; }
          asm volatile("" : "+v"(x[0]), "+v"(x[1]), "+v"(x[2]), "+v"(x[3]));
          if (idx & 2) { asm volatile(""); const u32x2 t = x[0], u = x[1]; x[0] = x[2]; x[1] = x[3]; x[2] = t; x[3] = u; }
          asm volatile("" : "+v"(x[0]), "+v"(x[1]), "+v"(x[2]), "+v"(x[3]));
        }
        f32x2 kf[2][5], o[2][2];
#pragma unroll
        for (int d = 0; d < 2; ++d) {
          unsigned kk[5];
          kk[3] = x[0][d]; kk[4] = x[1][d];
          kk[2] = __builtin_amdgcn_update_dpp(hl[d][2], kk[4], 0x138, 0xf, 0xf, false);
          kk[1] = __builtin_amdgcn_update_dpp(hl[d][1], kk[3], 0x138, 0xf, 0xf, false);
          kk[0] = __builtin_amdgcn_update_dpp(hl[d][0], kk[2], 0x138, 0xf, 0xf, false);
#pragma unroll
          for (int j = 0; j < 5; ++j) kf[d][j] = (f32x2){bflo(kk[j]), bfhi(kk[j])};
        }
#pragma unroll
        for (int t = 0; t < 5; ++t) {
#pragma unroll
          for (int i = 0; i < 4; ++i) {
            const int d = i >> 1, r = i & 1;
            if (t == 4) o[d][r] *= (f32x2){bflo(x[2 + r][d]), bfhi(x[2 + r][d])};
            else if (t == 0) o[d][r] = kf[d][r + 3] * wt[d][0];
            else o[d][r] = __builtin_elementwise_fma(kf[d][r + 3 - t], wt[d][t], o[d][r]);
          }
          asm volatile("" : "+v"(o[0][0]), "+v"(o[0][1]), "+v"(o[1][0]), "+v"(o[1][1]));
        }
#pragma unroll
        for (int i = 0; i < 4; ++i) *(unsigned*)(lds + up + i * U_CH + s * 1024) = pack2(o[i >> 1][1][i & 1], o[i >> 1][0][i & 1]);
        pf_set(4 * s);
        SCHED();
        if (s < 2) halo(s + 1);
        SCHED();
      };
      u32x4 sv[4], qv[4];
      u32x2 mv[2];
      auto ld_e = [&]() {
#pragma unroll
        for (int s = 0; s < 4; ++s) sv[s] = __builtin_amdgcn_raw_buffer_load_b128(rs, l16, so + (unsigned)s * 4096u, SC1);
        SCHED();
      };
      auto s_bad = [&]() { return __any(((ne2(sv[0][1], sv[0][3]) | ne2(sv[1][1], sv[1][3]) | ne2(sv[2][1], sv[2][3])) | ne2(sv[3][1], sv[3][3])) != 0u); };
      unsigned qo = (unsigned)((128 * pw + 32 * h4 + 4 * ((l >> 3) & 1) + 8 * ((l >> 2) & 1) + (l & 3)) * (D_ * 2));
      asm volatile("" : "+v"(qo));
      auto glq = [&](int j0, int j1) {
        __builtin_amdgcn_s_setprio(0);
#pragma unroll
        for (int j = j0; j < j1; ++j) {
          const u32x2 x = __builtin_amdgcn_raw_buffer_load_b64(rq, qo, (unsigned)(quad * 8 + (512 * (j >> 1) + 16 * (j & 1)) * D_ * 2), 0);
          qv[j >> 1][2 * (j & 1)] = x[0]; qv[j >> 1][2 * (j & 1) + 1] = x[1];
          SCHED();
        }
        __builtin_amdgcn_s_setprio(3);
      };
      kv(IC<0>(), IC<5>());
      STAMP(23);
      if (QG) {
        const int j = idx * 4 + pw;
        const unsigned o = (unsigned)((64 * (j >> 1) + l) * (D_ * 2) + (((160 * xcd) >> 7) + (j & 1)) * 128);
        __builtin_amdgcn_raw_ptr_buffer_load_lds(rq, (LAS void*)(lds + 98304), 4, j < 64 ? o : 0x80000000u, 0, 0, 0);
        SCHED();
      }
#define G2A 4
      gl(2, 0, G2A);
      kv(IC<1>(), IC<G2A + QG>());
      gl(2, G2A, 4);
      STAMPF(9);
      u32x4 k3[2];
      auto ld_k3 = [&]() {
        k3[0] = __builtin_amdgcn_raw_buffer_load_b128(rs, k3o, WS_KV3, SC1);
        k3[1] = __builtin_amdgcn_raw_buffer_load_b128(rs, k3o, WS_KV3 + 8u * 20480u, SC1);
      };
      auto ld_m = [&]() {
        mv[0] = __builtin_amdgcn_raw_buffer_load_b64(rs, mo, WS_MODQ, SC1);
        mv[1] = __builtin_amdgcn_raw_buffer_load_b64(rs, mo, WS_MODQ + 20480u, SC1);
      };
      auto ld_q = [&]() {
        asm volatile("" ::: "memory");
        if (!M_T) ld_m();
        SCHED();
#pragma unroll
        for (int s = 0; s < 4; ++s) qv[s] = __builtin_amdgcn_raw_buffer_load_b128(rs, yo, WS_QB + (unsigned)s * (32u * 20480u), BULK_AUX);
        SCHED();
      };
      auto k_bad = [&]() { return __any((ne2(k3[0][1], k3[0][3]) | ne2(k3[1][1], k3[1][3])) != 0u); };
      auto k_put = [&]() {
        *(u32x2*)(lds + k3d) = (u32x2){k3[0][0], k3[0][2]};
        *(u32x2*)(lds + k3d + 128) = (u32x2){k3[1][0], k3[1][2]};
        pf_set(12);
        SCHED();
      };
      float r1[4][2], m1[4][2];
      auto ln_pre = [&]() {
#pragma unroll
      for (int i = 0; i < 8; ++i) {
        const unsigned x = sv[i >> 1][2 * (i & 1)];
        r1[i >> 1][i & 1] = __builtin_amdgcn_rcpf(bfhi(x));
        m1[i >> 1][i & 1] = -bflo(x) * r1[i >> 1][i & 1];
        asm volatile("" : "+v"(r1[i >> 1][i & 1]), "+v"(m1[i >> 1][i & 1]));
      }
      };
      unsigned qck[4], qco = (unsigned)((8 * pw + (l >> 3)) * 1024 + (quad / 40) * 256 + (quad % 40) * 4);
      asm volatile("" : "+v"(qco));
      auto ld_qc = [&]() {
#pragma unroll
        for (int s = 0; s < 4; ++s) {
          qv[s] = __builtin_amdgcn_raw_buffer_load_b128(rs, yo, WS_QB + (unsigned)s * (32u * 20480u), SC1);
          qck[s] = __builtin_amdgcn_raw_buffer_load_b32(rs, qco, WS_QCK + (unsigned)s * 32768u, SC1);
        }
        SCHED();
      };
      auto q_bad = [&]() {
        unsigned d = 0u;
#pragma unroll
        for (int s = 0; s < 4; ++s) d += (qv[s][0] + qv[s][1]) + (qv[s][2] + qv[s][3]);
        return __any(sum8u(d) + ((qck[0] + qck[1]) + (qck[2] + qck[3])) != tag * 4u);
      };
      const bool early = (unsigned)__builtin_amdgcn_s_memrealtime() - t_start < KE_T;
      ld_e();
      if (!early) hold(K_T);
      {
        const unsigned em = early ? 0x80000000u : 0u;
        k3o |= em; yo |= em; qco |= em;
        ld_k3();
        SCHED();
        ld_qc();
        k3o ^= em; yo ^= em; qco ^= em;
      }
      kv(IC<2>(), IC<14>());
      STAMPF(11);
      auto s_get = [&]() {
        if (s_bad()) {
          _Pragma("unroll 1") for (unsigned spins = 0; spins < SPIN_CAP; ++spins) {
            __builtin_amdgcn_s_sleep(4);
            ld_e();
            if (!s_bad()) break;
          }
        }
        ln_pre();
      };
      bool sok = false;
      if (early) {
        sok = !s_bad();
        hold(K_T);
        if (!sok) ld_e();
        ld_k3();
        SCHED();
        ld_qc();
        if (sok) ln_pre();
      }
      if (k_bad()) {
        _Pragma("unroll 1") for (unsigned spins = 0; spins < SPIN_CAP; ++spins) {
          __builtin_amdgcn_s_sleep(4);
          ld_k3();
          if (!k_bad()) break;
        }
      }
      k_put();
      STAMPF(18);
      if (ME) { if (M_T) hold(M_T); ld_m(); SCHED(); }
      if (!sok) s_get();
      if (q_bad()) {
        _Pragma("unroll 1") for (unsigned spins = 0; spins < SPIN_CAP; ++spins) {
          __builtin_amdgcn_s_sleep(4);
          ld_qc();
          if (!q_bad()) break;
        }
      }
      STAMPF(17);
      if (!ME) { if (M_T) hold(M_T); ld_m(); SCHED(); }
      STAMP(2);
      u32x2 yv[4][2];
      auto ld_y = [&](int s) {
#pragma unroll
        for (int e = 0; e < 2; ++e) yv[s][e] = trd(lds + yr + 1024 * s + 32 * e);
      };
      if (!QC) ln_pre();
      f32x4 qf[4][2], P[4][2];
      auto xln = [&](int s) {
#pragma unroll
        for (int e = 0; e < 2; ++e) {
          const float r = r1[s][e], m = m1[s][e];
          const f32x4 qq = {bflo(qv[s][2 * e]), bfhi(qv[s][2 * e]), bflo(qv[s][2 * e + 1]), bfhi(qv[s][2 * e + 1])};
          qf[s][e] = qq;
          P[s][e] = (NOPK & 1) ? fma4(qq, (f32x4){r, r, r, r}, (f32x4){m, m, m, m}) : __builtin_elementwise_fma(qq, (f32x4){r, r, r, r}, (f32x4){m, m, m, m});
        }
        asm volatile("" : "+v"(qf[s][0]), "+v"(qf[s][1]), "+v"(P[s][0]), "+v"(P[s][1]));
      };
      if (QC) { xln(0); xln(1); xln(2); xln(3); }
      pf_wait(20);
      ld_y(0); ld_y(1);
      SCHED();
      if (__any(ne2(mv[0][0], mv[1][0]) != 0u)) {
        _Pragma("unroll 1") for (unsigned spins = 0; spins < SPIN_CAP; ++spins) {
          __builtin_amdgcn_s_sleep(4);
          ld_m();
          if (!__any(ne2(mv[0][0], mv[1][0]) != 0u)) break;
        }
      }
      STAMPF(13);
      f32x4 al, be;
      {
        const float mgf = rbf(sum_rows(__uint_as_float(mv[0][1]) + __uint_as_float(mv[1][1])));
        auto rl = [&](int i) { return __uint_as_float(__builtin_amdgcn_readlane(__float_as_uint(mgf), i)); };
        const f32x4 sa = {rl(0), rl(1), rl(2), rl(3)}, ca = {rl(4), rl(5), rl(6), rl(7)}, ga = {rl(8), rl(9), rl(10), rl(11)};
        al = rbf4(ca + 1.0f) * ga;
        be = sa * ga;
        asm volatile("" : "+v"(al), "+v"(be));
      }
      auto xpre = [&](int s) {
        if (!QC) xln(s);
#pragma unroll
        for (int e = 0; e < 2; ++e) P[s][e] = (NOPK & 2) ? fma4(P[s][e], al, be) : __builtin_elementwise_fma(P[s][e], al, be);
        asm volatile("" : "+v"(P[s][0]), "+v"(P[s][1]));
      };
      auto xfin = [&](int s) {
        u32x4 o;
#pragma unroll
        for (int e = 0; e < 2; ++e) {
          const u32x2 y = yv[s][e];
          const f32x4 yf = {bflo(y[0]), bfhi(y[0]), bflo(y[1]), bfhi(y[1])};
          const f32x4 x = ((NOPK & 2) && s < 2) ? fma4(P[s][e], yf, qf[s][e]) : __builtin_elementwise_fma(P[s][e], yf, qf[s][e]);
          o[2 * e] = pack2(x[0], x[1]);
          o[2 * e + 1] = pack2(x[2], x[3]);
        }
        ST16S(o, rs, yo, WS_X1B + (unsigned)s * (32u * 20480u), SC1);
      };
      xpre(0); xfin(0);
      xpre(1); xfin(1);
      SCHED();
      xpre(2); xpre(3);
#define FVR 5
#define MSPLIT 1
      if (MSPLIT) {
        pf_wait(24);
        ld_y(2);
        xfin(2);
        SCHED();
        pf_wait(28);
        ld_y(3);
        xfin(3);
      } else {
        pf_wait(28);
        ld_y(2); ld_y(3);
        xfin(2); xfin(3);
      }
      STAMP(28);
      if (BG == 1 && l == 0) lf[40 + pw] = tag;
      asm volatile("s_waitcnt vmcnt(2)" ::: "memory");
      ST16S(tg4, rs, xfo, WS_FX, SC1);
      SCHED();
      asm volatile("s_waitcnt vmcnt(1)" ::: "memory");
      ST16S(tg4, rs, xfo, WS_FX + 32u * 160u * 128u, SC1);
      if (BG == 2 && l == 0) lf[40 + pw] = tag;
      STAMP(5);
    } else {
      const int ch = w;
      char* U = lds + L_U + ch * U_CH;
      char* G = lds + ch * (4 * G_COPY);
      {
        const rsrc_t rf = __builtin_amdgcn_make_buffer_rsrc((void*)(p.ssm_k + (size_t)(quad * 4 + ch) * S_), 0, S_ * 2, 0x00020000);
        u32x4 fa[4];
        u32x2 fb[4];
        __builtin_amdgcn_s_setprio(3);
#pragma unroll
        for (int k = 0; k < 4; ++k) {
          fa[k] = __builtin_amdgcn_raw_buffer_load_b128(rf, l16 + (unsigned)k * 1024u, 0, NT);
        }
        __builtin_amdgcn_s_setprio(1);
        TAG_READY();
#pragma unroll
        for (int k = 0; k < 4; ++k) {
#pragma unroll
          for (int d = 0; d < 2; ++d)
            fb[k][d] = __builtin_amdgcn_update_dpp(k < 3 ? (unsigned)__builtin_amdgcn_readfirstlane(fa[k < 3 ? k + 1 : k][d]) : 0u, fa[k][d], 0x130, 0xf, 0xf, false);
          const unsigned x[6] = {fa[k][0], fa[k][1], fa[k][2], fa[k][3], fb[k][0], fb[k][1]};
          unsigned y[5];
#pragma unroll
          for (int d = 0; d < 5; ++d) y[d] = __builtin_amdgcn_alignbit(x[d + 1], x[d], 16);
          char* gp = G + 32 + k * 1024 + l16;
          *(u32x4*)(gp) = fa[k];
          *(u32x4*)(gp + G_COPY) = (u32x4){y[0], y[1], y[2], y[3]};
          *(u32x4*)(gp + 2 * G_COPY) = (u32x4){x[1], x[2], x[3], x[4]};
          *(u32x4*)(gp + 3 * G_COPY) = (u32x4){y[1], y[2], y[3], y[4]};
          if (k == 0) {
            const unsigned f0 = __builtin_amdgcn_readfirstlane(x[0]), f1 = __builtin_amdgcn_readfirstlane(x[1]);
            const unsigned long long f64 = (unsigned long long)f0 | ((unsigned long long)f1 << 32);
            const unsigned long long hi = l ? (f64 << (64 - 16 * (l & 3))) : 0ull;
            if (l < 4) {
              *(u32x4*)(G + l * G_COPY) = (u32x4){0u, 0u, 0u, 0u};
              *(u32x4*)(G + l * G_COPY + 16) = (u32x4){0u, 0u, (unsigned)hi, (unsigned)(hi >> 32)};
            }
            if (l >= 32) *(u32x4*)(U + (l - 32) * 16) = (u32x4){0u, 0u, 0u, 0u};
          }
        }
        asm volatile("s_waitcnt lgkmcnt(0)" ::: "memory");
        STAMP(1);
      }
      const int a = n16, h = h4, jj = h >> 1, e2 = h & 1;
      const char* ap = G + ((a + 1) & 3) * G_COPY + 2 * (16 * jj + a - 8 * e2 + 9 - ((a + 1) & 3));
      unsigned bp = (unsigned)(L_U + ch * U_CH + 32 * (a - jj) + 16 * e2);
      auto lda = [&](u32x4* A, int r, int p0, int np) {
#pragma unroll
        for (int pp = 0; pp < np; ++pp) {
          u32x2 lo = *(const volatile LAS u32x2*)(ap + 512 * (p0 + pp) + 64 * r);
          u32x2 hi = *(const volatile LAS u32x2*)(ap + 512 * (p0 + pp) + 64 * r + 8);
          A[pp] = (u32x4){lo[0], lo[1], hi[0], hi[1]};
        }
      };
      u32x4 AD;
      {
        const unsigned dDr = (*(cptr_t)(p.ssm_D + quad * 4 + (ch & 2)) >> (16 * (ch & 1))) & 0xffffu;
        const unsigned dv = h == (a >> 3) ? ((a & 1) ? dDr : dDr << 16) : 0u;
        const int dw = (7 - (a & 7)) >> 1;
#pragma unroll
        for (int d = 0; d < 4; ++d) AD[d] = dw == d ? dv : 0u;
      }
      u32x4 A0[8][4];
      f32x4 acc[8];
#pragma unroll
      for (int n = 0; n < 8; ++n) acc[n] = (f32x4){0.f, 0.f, 0.f, 0.f};
#pragma unroll
      for (int r = 0; r < 8; ++r) lda(A0[r], r, 0, 4);
      auto yput = [&](int s) {
#pragma unroll
        for (int e = 0; e < 2; ++e)
          *(u32x2*)(U + 1024 * s + 512 * e + (16 * a + 4 * h) * 2) = (u32x2){pack2(acc[2 * s + e][0], acc[2 * s + e][1]), pack2(acc[2 * s + e][2], acc[2 * s + e][3])};
        asm volatile("" ::: "memory");
        if (l == 0) lf[16 + 4 * s + ch] = tag;
      };
      asm volatile("" : "+v"(AD), "+v"(bp), "+v"(acc[0]), "+v"(acc[1]), "+v"(acc[2]), "+v"(acc[3]), "+v"(acc[4]), "+v"(acc[5]), "+v"(acc[6]), "+v"(acc[7]));
      auto stage = [&](auto S__) {
        constexpr int s = decltype(S__)::value, NH = 4 - 2 * s, NP = 8 - 2 * s;
        u32x2 A1[3][NH][2];
        u32x4 B[2][2];
        auto lda1 = [&](int r, int i) { A1[r % 3][i >> 1][i & 1] = *(const volatile LAS u32x2*)(ap + 512 * (4 + (i >> 1)) + 64 * r + 8 * (i & 1)); };
        auto ldb = [&](int r, int e) { B[r & 1][e] = *(const u32x4*)(lds + bp + 512 * (2 * s + e + 1) - 64 * r); };
#pragma unroll
        for (int i = 0; i < 4 * NH; ++i) lda1(i / (2 * NH), i % (2 * NH));
        SCHED();
        if (s > 0) yput(s - 1);
        lf_wait(4 * s, 4);
        if (s == 0) STAMP(3);
#pragma unroll
        for (int i = 0; i < 4; ++i) ldb(i >> 1, i & 1);
        SCHED();
#pragma unroll
        for (int e = 0; e < 2; ++e) MFMA_IP(acc[2 * s + e], AD, B[0][e]);
        sfor<0, 8 * (2 * NP - 1)>([&](auto I_) __attribute__((always_inline)) {
          constexpr int r = decltype(I_)::value / (2 * NP - 1), i = decltype(I_)::value % (2 * NP - 1), e = i >= NP, pp = i - e * NP, h1 = pp < 4 ? 0 : pp - 4;
          const u32x4 a1 = {A1[r % 3][h1][0][0], A1[r % 3][h1][0][1], A1[r % 3][h1][1][0], A1[r % 3][h1][1][1]};
          const u32x4 fa = pp < 4 ? A0[r][pp & 3] : a1;
          MFMA_IP(acc[2 * s + e + pp], fa, B[r & 1][e]);
          SCHED();
          if (r + 2 < 8 && i == 2 * NP - 3) ldb(r + 2, 0);
          if (r + 2 < 8 && i == 2 * NP - 2) ldb(r + 2, 1);
          if (r + 2 < 8 && i < 2 * NH) lda1(r + 2, i % (2 * NH));
          SCHED();
        });
      };
      stage(IC<0>());
      STAMP(4);
      stage(IC<1>());
      STAMPF(9);
      u32x2 fv;
      if (MSPLIT) {
        u32x4 B[8][2];
        auto ldb = [&](int r, int m0) {
#pragma unroll
          for (int m = 0; m < 2; ++m) B[r][m] = *(const u32x4*)(lds + bp + 512 * (m0 + m + 1) - 64 * r);
        };
        asm volatile("s_nop 1" : "+v"(acc[2]), "+v"(acc[3]));
        yput(1);
        if (w == 3) wo_ld();
        __builtin_amdgcn_s_setprio(MPR);
        lf_wait(8, 4);
        STAMP(22);
        auto ldb1 = [&](int i, int m0) { B[i >> 1][i & 1] = *(const u32x4*)(lds + bp + 512 * (m0 + (i & 1) + 1) - 64 * (i >> 1)); };
#pragma unroll
        for (int r = 0; r < 3; ++r) ldb(r, 4);
        SCHED();
#pragma unroll
        for (int m = 0; m < 2; ++m) MFMA_IP(acc[4 + m], AD, B[0][m]);
        sfor<0, 24>([&](auto I_) __attribute__((always_inline)) {
          constexpr int i = decltype(I_)::value, r = i / 3, k = i % 3;
          MFMA_IP(acc[4 + (k != 1)], A0[r][k == 0], B[r][k == 2]);
          SCHED();
          if (i < 10) ldb1(6 + i, 4);
          SCHED();
        });
        sfor<0, 32>([&](auto I_) __attribute__((always_inline)) {
          constexpr int i = decltype(I_)::value, r = i >> 2, k = i & 3;
          MFMA_IP(acc[6 + (k & 1)], A0[r][2 + (k & 1) - (k >> 1)], B[r][k >> 1]);
          SCHED();
          if (i == 7) yput(2);
          if (!WC_T && i == 4 * FVR) fv = ld8(rs, b2o);
          if (i == 19) lf_wait(12, 4);
          if (i >= 19 && i < 29) ldb1(i - 19, 6);
          SCHED();
        });
#pragma unroll
        for (int m = 0; m < 2; ++m) MFMA_IP(acc[6 + m], AD, B[0][m]);
        sfor<0, 24>([&](auto I_) __attribute__((always_inline)) {
          constexpr int i = decltype(I_)::value, r = i / 3, k = i % 3;
          MFMA_IP(acc[6 + (k != 1)], A0[r][k == 0], B[r][k == 2]);
          SCHED();
          if (i < 6) ldb1(10 + i, 6);
          SCHED();
        });
        asm volatile("s_nop 7" : "+v"(acc[6]), "+v"(acc[7]));
        yput(3);
      } else {
        u32x4 B[3][4];
        auto ldb = [&](int r, int m) { B[r % 3][m] = *(const u32x4*)(lds + bp + 512 * (5 + m) - 64 * r); };
        asm volatile("s_nop 1" : "+v"(acc[2]), "+v"(acc[3]));
        yput(1);
        if (w == 3) wo_ld();
        __builtin_amdgcn_s_setprio(MPR);
        lf_wait(8, 8);
        STAMP(22);
#pragma unroll
        for (int i = 0; i < 8; ++i) ldb(i >> 2, i & 3);
        SCHED();
#pragma unroll
        for (int m = 0; m < 4; ++m) MFMA_IP(acc[4 + m], AD, B[0][m]);
        sfor<0, 80>([&](auto I_) __attribute__((always_inline)) {
          constexpr int r = decltype(I_)::value / 10, i = decltype(I_)::value % 10, m = i < 4 ? 0 : i < 7 ? 1 : i < 9 ? 2 : 3, pp = i - (m * (9 - m)) / 2;
          MFMA_IP(acc[4 + m + pp], A0[r][pp], B[r % 3][m]);
          SCHED();
          if (r + 2 < 8 && i < 4) ldb(r + 2, i & 3);
          if (r == 1 && i == 9) fv = ld8(rs, b2o);
          SCHED();
        });
#pragma unroll
        for (int e = 0; e < 4; ++e) {
          if (e == 1) asm volatile("s_nop 4" : "+v"(acc[5]), "+v"(acc[6]), "+v"(acc[7]));
          *(u32x2*)(U + 2048 + 512 * e + (16 * a + 4 * h) * 2) = (u32x2){pack2(acc[4 + e][0], acc[4 + e][1]), pack2(acc[4 + e][2], acc[4 + e][3])};
          SCHED();
        }
        asm volatile("" ::: "memory");
        if (l == 0) lf[28 + ch] = tag;
      }
      STAMP(28);
      __builtin_amdgcn_s_setprio(0);
      if (WC_T) { hold(WC_T); fv = (u32x2){~tag, 0u}; }
      b2_wait(fv, 0);
      lf_wait(20, 4);
      DRAIN();
    }
    __builtin_amdgcn_s_setprio(0);
  } else {
    const int at = idx - 20;
    char* const scr = lds + WBLK + 14848;
    auto gv_blk = [&](int j) { return j < 160 ? 30 + (j >> 3) : j < 400 ? (j >> 3) - 20 : j >> 3; };
    auto gv_fin = [&](const u32x4* pre, int j) {
      float acc[8];
      gemv_fin(acc, pre, 10, scr);
      gemv_put(acc, j < 160 ? WS_MODP + (unsigned)((j & 7) * 1280 + (j >> 3) * 64) * 8u : WS_MODQ + (unsigned)((j & 7) * 2560 + ((j >> 3) - 20) * 64) * 8u);
    };
    const int ja = xcd * 12 + at, ai = xcd * 8 + at, bi = xcd * 4 + at - 8, g = w >> 2, wj = w & 3;
    const int rj = w == 0 ? ja : w == 1 ? (at >= 4 ? 92 + ai : -1) : w == 4 ? 160 + ja : w == 5 && at < 8 ? 256 + ai : -1;
    char* const rst = lds + (g ? WBLK : 65536) + wj * 7424;
    u32x4 pre[11];
    if (at >= 8 || g == 0) {
      TOUCH_AT(2);
      const int T = at >= 8 ? 64 + 2 * bi + g : ai;
      unsigned* mds = (unsigned*)(lds + 2048) + 16 * g;
      char* qt = lds + L_RT + g * (16 * 1312);
      u32x4 ld[5];
      const bool e1 = at >= 8 && w == 3;
      if (!e1) __builtin_amdgcn_s_sleep(8);
      auto q_ld = [&](auto N_) {
        asm volatile("; %0" :: "n"(decltype(N_)::value) : "memory");
#pragma unroll
      for (int j = 0; j < 5; ++j)
        ld[j] = __builtin_amdgcn_raw_buffer_load_b128(rq, (unsigned)(h4 * D_ * 2 + n16 * 16 + j * 256), (unsigned)((32 * (T >> 1) + 8 * wj + 4 * (T & 1)) * D_ * 2), QG ? 0 : NT);
        SCHED();
      };
      const int g2 = at < 8 || w < 2 || w == 4 || w == (G2E ? 3 : 2) ? 400 : w < 4 ? 384 + bi : 256 + wj * 32 + bi;
      SCHED();
      float sa = 0.f, va = 0.f;
      auto q_in = [&]() {
        const bf2_t ONE2 = {(__bf16)1.0f, (__bf16)1.0f};
        char* dst = qt + (4 * wj + h4) * 1312 + n16 * 16;
        SCHED();
#pragma unroll
        for (int j = 0; j < 5; ++j) {
          if (!QG) *(volatile LAS u32x4*)(dst + j * 256) = ld[j];
#pragma unroll
          for (int d = 0; d < 4; ++d) {
            const unsigned a0 = ld[j][d];
            const bf2_t xa = __builtin_bit_cast(bf2_t, a0);
            sa = __builtin_amdgcn_fdot2_f32_bf16(xa, ONE2, sa, false); va = __builtin_amdgcn_fdot2_f32_bf16(xa, xa, va, false);
          }
        }
      };
      u32x4 pre2[11];
      if (e1) {
        gemv_load(pre, gv_blk(128 + bi), bi & 7, 10);
        q_ld(IC<3>());
        TAG_READY();
        gv_fin(pre, 128 + bi);
        q_in();
        asm volatile("; d" : "+v"(sa), "+v"(va));
      } else if (G2E && g2 < 400) {
        q_ld(IC<0>());
        gemv_load(pre2, gv_blk(g2), g2 & 7, 10);
        q_in();
        asm volatile("; a" : "+v"(sa), "+v"(va));
      } else if (RTE && w < 2 && rj >= 0) {
        q_ld(IC<1>());
        if (RTE > 1) __builtin_amdgcn_s_sleep(RTE);
        rt_ld(rj, rst);
        q_in();
        asm volatile("; b" : "+v"(sa), "+v"(va));
      } else {
        q_ld(IC<2>());
        q_in();
        asm volatile("; c" : "+v"(sa), "+v"(va));
      }
      TAG_READY();
      {
        if (!QG) lf_set(8 + w);
        const float ma = sum16(sa) * (1.0f / D_);
        va = __builtin_fmaxf(sum16(va) * (1.0f / D_) - ma * ma, 0.f);
        if (n16 == 0) mds[4 * wj + h4] = pack2(ma, __builtin_amdgcn_sqrtf(rbf(rbf(va) + 1e-5f)));
      }
      if (QG || QC) lf_set(16 + w);
      if (!QG) lf_wait(8 + 4 * g, 4);
      STAMP(23);
      if (QG) {
        if (wj == 2) {
          lf_wait(16 + 4 * g, 4);
          st16(rs, WS_ST1 + (unsigned)(l >> 3) * 16384u + (unsigned)T * 128u + (unsigned)(l & 7) * 16u, (u32x4){mds[l & 7], tag, mds[8 + (l & 7)], tag});
          STAMP(22);
        }
      } else {
        const char* src = qt + (l & 7) * 1312 + (40 * wj + (l >> 3)) * 8;
        unsigned ckw = 0u;
        u32x2 qx[5][2];
#pragma unroll
        for (int j = 0; j < 5; ++j) { qx[j][0] = *(const volatile LAS u32x2*)(src + j * 64); qx[j][1] = *(const volatile LAS u32x2*)(src + j * 64 + 8 * 1312); }
        SCHED();
#pragma unroll
        for (int j = 0; j < 5; ++j) {
          const u32x2 x = qx[j][0], y = qx[j][1];
          st16(rs, WS_QB + (unsigned)T * 20480u + (unsigned)(5 * wj + j) * 1024u + l16, (u32x4){x[0], x[1], y[0], y[1]});
          if (QC) { const unsigned t = tag - sum8u((x[0] + x[1]) + (y[0] + y[1])); ckw = (l & 7) == j ? t : ckw; }
        }
        if (QC) {
          st4(rs, WS_QCK + (unsigned)(T * 4 + wj) * 256u + (unsigned)(l * 4), (unsigned)__builtin_amdgcn_ds_bpermute(((l & 7) * 8 + (l >> 3)) * 4, (int)ckw));
          if (wj == 2) {
            lf_wait(16 + 4 * g, 4);
            st16(rs, WS_ST1 + (unsigned)(l >> 3) * 16384u + (unsigned)T * 128u + (unsigned)(l & 7) * 16u, (u32x4){mds[l & 7], tag, mds[8 + (l & 7)], tag});
            STAMP(22);
          }
        }
      }
      SCHED();
      static_assert((!G2E && !RTE) || QC, "");
      if (g2 < 400) {
        if (!G2E) gemv_load(pre2, gv_blk(g2), g2 & 7, 10);
        if (!QG && !QC) asm volatile("s_waitcnt vmcnt(10)" ::: "memory");
      } else if (w < 2 && rj >= 0) {
        if (!RTE) rt_ld(rj, rst);
        if (!QG && !QC) asm volatile("s_waitcnt vmcnt(7)" ::: "memory");
      } else if (!QG && !QC) {
        DRAIN();
      }
      if (!QG && !QC) lf_set(16 + w);
      if (!QG && !QC && wj == 2) {
        lf_wait(16 + 4 * g, 4);
        st16(rs, WS_ST1 + (unsigned)(l >> 3) * 16384u + (unsigned)T * 128u + (unsigned)(l & 7) * 16u, (u32x4){mds[l & 7], tag, mds[8 + (l & 7)], tag});
        STAMP(22);
      }
      if (w >= 4 && w < 7) lf_set(w);
      if (g2 < 400) gv_fin(pre2, g2);
      STAMP(25);
    } else {
      constexpr int KB = L_RT + 16 * 1312;
      TOUCH_AT(3);
      const int g1 = w < 6 ? 2 * ai + wj : 32 + wj * 64 + ai;
      if (w >= 6) {
        const rsrc_t rk = __builtin_amdgcn_make_buffer_rsrc((void*)(w == 6 ? p.k : p.v), 0, S_ * D_ * 2, 0x00020000);
        const unsigned ko = (unsigned)((1536 + 8 * ai) * (D_ * 2) - (w == 6 ? 4096 : 0));
        char* dst = lds + KB + (w == 6 ? 0 : 14336);
        __builtin_amdgcn_s_sleep(3);
#pragma unroll
        for (int n = 0; n < 10; ++n) __builtin_amdgcn_raw_ptr_buffer_load_lds(rk, (LAS void*)(dst + n * 1024), 16, l16, ko + (unsigned)n * 1024u, 0, NT);
        if (w == 6) {
#pragma unroll
          for (int n = 10; n < 14; ++n) __builtin_amdgcn_raw_ptr_buffer_load_lds(rk, (LAS void*)(dst + n * 1024), 16, l16, ko + (unsigned)n * 1024u, 0, NT);
        }
      }
      SCHED();
      u32x4 swt[2];
      swt[0] = *(const u32x4*)(p.shift_k + (tid - 256) * 8);
      swt[1] = *(const u32x4*)(p.shift_k + (256 + l) * 8);
      SCHED();
      if (w >= 6) __builtin_amdgcn_s_sleep(8);
      gemv_load(pre, gv_blk(g1), g1 & 7, 10);
      TAG_READY();
      asm volatile("s_waitcnt vmcnt(11)" : "+v"(swt[0]), "+v"(swt[1]) :: "memory");
      lf_set(8 + w);
      if (w < 6) gv_fin(pre, g1);
      STAMP(25);
      lf_wait(12, 4);
      STAMP(23);
#pragma unroll 1
      for (int n = 0; n < (w == 7 ? 2 : 1); ++n) {
        {
          const int pr = n ? 256 + l : tid - 256;
          const char* kp = lds + KB + 4096 + pr * 4;
          unsigned kk[11], vv[8];
#pragma unroll
          for (int i = 0; i < 11; ++i) kk[i] = *(const unsigned*)(kp + (i - 3) * 1280);
#pragma unroll
          for (int i = 0; i < 8; ++i) vv[i] = *(const unsigned*)(kp + 10240 + i * 1280);
          const u32x4 sw = n ? swt[1] : swt[0];
          const f32x2 wt[4] = {{bflo(sw[0]), bflo(sw[2])}, {bfhi(sw[0]), bfhi(sw[2])}, {bflo(sw[1]), bflo(sw[3])}, {bfhi(sw[1]), bfhi(sw[3])}};
          f32x2 o[8];
#pragma unroll
          for (int i = 0; i < 8; ++i) {
            f32x2 t = (f32x2){bflo(kk[i + 3]), bfhi(kk[i + 3])} * wt[0];
#pragma unroll
            for (int j = 1; j < 4; ++j) t = __builtin_elementwise_fma((f32x2){bflo(kk[i + 3 - j]), bfhi(kk[i + 3 - j])}, wt[j], t);
            o[i] = t * (f32x2){bflo(vv[i]), bfhi(vv[i])};
          }
          char* sc = lds + 98304 + wj * 2048;
#pragma unroll
          for (int e = 0; e < 2; ++e)
            *(volatile LAS u32x4*)(sc + l * 32 + e * 16) = (u32x4){pack2(o[7][e], o[6][e]), pack2(o[5][e], o[4][e]), pack2(o[3][e], o[2][e]), pack2(o[1][e], o[0][e])};
          u32x2 x[4];
#pragma unroll
          for (int e = 0; e < 4; ++e) x[e] = *(const volatile LAS u32x2*)(sc + e * 512 + l * 8);
#pragma unroll
          for (int e = 0; e < 4; ++e)
            st16(rs, WS_KV3 + (unsigned)ai * 20480u + (unsigned)((n ? 4 : wj) * 4096 + e * 1024) + l16, (u32x4){x[e][0], tag, x[e][1], tag});
        }
      }
      STAMP(22);
      if (w < 7) lf_set(w);
      if (w >= 6) gv_fin(pre, g1);
      STAMPF(30);
    }
    if (w == 6 && ja < 80) {
      hold(AUX_T);
      gemv_load(pre, gv_blk(400 + ja), ja & 7, 10);
      gv_fin(pre, 400 + ja);
    }
    if (rj >= 0) {
      if (w >= 2) { hold(RT2_T); rt_ld(rj, rst); }
      rt_job(rj, rst);
    }
    STAMP(26);
    lf_set(w);
    if (w == 3) { hold(WA_T); wo_ld(); }
    STAMP(3);
    if (w < 4) {
      hold(W_T);
      b2_wait((u32x2){~tag, 0u}, 1);
      lf_wait(0, 8);
      STAMP(4);
    }
    DRAIN();
  }

  STAMPF(16);
  u32x4 wox[5];
  if (w == 3) {
    const char* bp = wo_st + (4 * h4 + (n16 >> 2)) * 160 + (n16 & 3) * 8;
#pragma unroll
    for (int dj = 0; dj < 5; ++dj) wox[dj] = trfrag(bp + dj * 32, 160);
    asm volatile("s_waitcnt lgkmcnt(0)" : "+v"(wox[0]), "+v"(wox[1]), "+v"(wox[2]), "+v"(wox[3]), "+v"(wox[4]) :: "memory");
  }
  auto wo_put = [&]() {
    if (w == 3 && R < 28) {
      const unsigned base = WS_WOUTF + (unsigned)c * WBLK + (unsigned)((R >> 1) * 10240 + (R & 1) * 1024);
#pragma unroll
      for (int dj = 0; dj < 5; ++dj) st16(rs, base + (unsigned)((R & 1) ? dj : (dj + 2) % 5) * 2048u + l16, wox[dj]);
    }
  };
  if (cs2) {
    cs_ld(csv, (8 + c) * 7 + R - 20);
    SCHED();
    load_b2(0, 12);
    SCHED();
    cs_fin(csv, (8 + c) * 7 + R - 20);
  } else if (w < 4) {
    asm volatile("" ::: "memory");
    load_b2(0, B0);
    if (B0 < 12 && !is_conv) load_b2(B0, 12);
  }
  const unsigned xfb = WS_FX + (unsigned)(((R >> 1) * 4 + (R & 1) * 2 + (c >> 2)) * 160 + l) * 128u;
  unsigned xf3[3];
  if (w == 7) {
    lf_wait(36, 4);
    int dt = (int)(lf[44 + ((l & 31) < 20 ? l & 31 : 0)] - t_start);
    dt = l < 32 ? dt : -dt;
    dt = max(dt, __builtin_amdgcn_update_dpp(0, dt, 0xB1, 0xf, 0xf, true));
    dt = max(dt, __builtin_amdgcn_update_dpp(0, dt, 0x4E, 0xf, 0xf, true));
    dt = max(dt, __builtin_amdgcn_update_dpp(0, dt, 0x124, 0xf, 0xf, true));
    dt = max(dt, __builtin_amdgcn_update_dpp(0, dt, 0x128, 0xf, 0xf, true));
    const int d1 = min(max(max(__builtin_amdgcn_readlane(dt, 0), __builtin_amdgcn_readlane(dt, 16)), 0), 300);
    const int d0 = min(max(max(__builtin_amdgcn_readlane(dt, 32), __builtin_amdgcn_readlane(dt, 48)), 0), 300);
    dt = ((d1 + d0) * YW >> 3) - d0;
    __builtin_amdgcn_s_setprio(YPR);
    STAMPF(24);
    hold((unsigned)(YG_T + dt));
    STAMPF(30);
    xf3[0] = ld4(rs, xfb); xf3[1] = ld4(rs, xfb + 8192u); xf3[2] = ld4(rs, l < 32 ? xfb + 16384u : ~0u);
    __builtin_amdgcn_s_setprio(0);
  }
  SCHED();
  u32x4 k_c;
  unsigned kc_o = w < 2 ? (unsigned)(tid < 112 ? tid : 0) * 16u : 0x80000000u, kc_d = (unsigned)(L_CONST + (tid & 127) * 8);
  auto kc_ld = [&](int z2) { k_c = __builtin_amdgcn_raw_buffer_load_b128(rs, kc_o, WS_CS + (unsigned)(8 * z2 + c) * 2048u, SC1); };
  auto consts_put = [&](int z2) {
    if (w < 2) {
      if (__any(ne2(k_c[0], k_c[2]) != 0u)) {
        _Pragma("unroll 1") for (unsigned spins = 0; spins < SPIN_CAP; ++spins) {
          __builtin_amdgcn_s_sleep(8);
          kc_ld(z2);
          if (!__any(ne2(k_c[0], k_c[2]) != 0u)) break;
        }
      }
      if (z2) *(u32x2*)(lds + kc_d + 1024) = (u32x2){k_c[1], k_c[3]};
      else { const unsigned d = kc_d - ((kc_d >> 1) & 4u); *(unsigned*)(lds + d) = k_c[1]; *(unsigned*)(lds + d + 8) = k_c[3]; }
    }
  };
  const int wq = w ^ rotr, rt1 = wq & 3, kh1 = w >> 2;
  unsigned bo0 = (unsigned)(kh1 * 71680) + l16, bo1 = bo0 + 35840u;
  asm volatile("" : "+v"(bo0), "+v"(bo1));
  unsigned pso[3], pqo[3], oto = (unsigned)(((w ^ 4) * 7 * 64 + l) * 8), rdo = (unsigned)((w * 7 * 64 + l) * 8), cso = (unsigned)(L_CONST + (128 + n16) * 8);
#pragma unroll
  for (int e = 0; e < 3; ++e) {
    const int row = rt1 * 16 + (e < 2 ? 4 * h4 + 2 * kh1 + e : n16);
    pso[e] = (unsigned)(L_PST + row * 4); pqo[e] = (unsigned)(L_PST + 512 + row * 16 + (row & 3) * 4);
  }
  unsigned spo = (unsigned)(L_Z + tid * 16), p6o = (unsigned)(L_PST + (wq * 16 + 4 * h4) * 4), pgo = (unsigned)(L_PST + 512 + (wq * 16 + n16) * 16);
  asm volatile("" : "+v"(spo), "+v"(p6o), "+v"(pgo));
  p6o = n16 == 12 ? p6o : spo;
  pgo = h4 == (n16 >> 2) ? pgo : spo;
  unsigned bsrc = (unsigned)(((l >> 1) & 3) * 64 + (l & 1) * 4), ppo = l < 8 ? l16 : 0x80000000u;
  asm volatile("" : "+v"(pso[0]), "+v"(pso[2]), "+v"(pqo[0]), "+v"(pqo[2]), "+v"(oto), "+v"(rdo), "+v"(cso), "+v"(p6o), "+v"(pgo), "+v"(bsrc), "+v"(ppo), "+v"(kc_o), "+v"(kc_d));
  pso[1] = pso[0] + 4u; pqo[1] = pqo[0] + 20u;
#define BFRAG(q, j) (*(const u32x4*)(lds + ((q) < 5 ? bo0 : bo1) + ((q) % 5) * 7168 + (j) * 1024))
  u32x4 h2[10];
  unsigned xv = (unsigned)((4 * ((l >> 3) & 1) + h4) * 128 + (l & 7) * 16), xs0 = WS_X1B + (unsigned)(R * 4 + rt1) * 20480u + (unsigned)(10 * kh1) * 1024u;
  unsigned wvo = wsp_vo, bso = (unsigned)tau(rt1, n16) * 2u, fgo = l < 32 ? (unsigned)(l >> 3) * F4S + (unsigned)(l & 7) * 16u : 0x80000000u;
  asm volatile("" : "+v"(xv), "+v"(wvo), "+v"(bso), "+v"(fgo));
#define WO3 (-92)
#define PINS(x) do { x = (unsigned)__builtin_amdgcn_readfirstlane((int)x); asm volatile("" : "+s"(x)); } while (0)
  PINS(xs0);
  unsigned zs[8], wx8, wx4, wgb, fzs, uo, ui, xs, wl2, os, zos, pos, s1, s3;
#define LDX(q) h2[q] = __builtin_amdgcn_raw_buffer_load_b128(rs, xv + (unsigned)((q) & 3) * 1024u, xs0 + (unsigned)((q) >> 2) * 4096u, BULK_AUX)
  auto flag4 = [&](unsigned off) { ST16S(((u32x4){tag, tag, tag, tag}), rs, fgo, WS_F4 - WS_FWI + off, SC1); };
  unsigned bs_raw, fwo[1];
  f32x4 acc[7];
  float mu_n, rs_n;
  {
    unsigned f1o[1] = {w == 1 && l < 20 ? fwi + (unsigned)(c * 20 + l) * 128u : ~0u}, f1v;
    fwo[0] = w == 3 && l < 28 ? fwi + (WS_FWO - WS_FWI) + (unsigned)(c * 32 + l) * 128u : ~0u;
    asm volatile("" : "+v"(f1o[0]), "+v"(fwo[0]));
    auto late_scalars = [&]() {
#pragma unroll
    for (int t = 0; t < 8; ++t) { zs[t] = WS_Z2F + (unsigned)(c * 64 + 8 * w + 4 * ((t >> 2) ^ rotg) + ((t + rotb) & 3)) * 7168u; PINS(zs[t]); }
    wx8 = (unsigned)((w + (idx >> 2)) & 7) * 1024u; wx4 = wx8 & 3072u; wgb = WS_WOUTF + (unsigned)c * WBLK; fzs = fz + (unsigned)(c * 32 + 4 * w) * 128u;
    uo = WS_UF + (unsigned)((R * 4 + rt1) * 8 + c) * 4096u + (unsigned)kh1 * 2048u; ui = WS_UF + (unsigned)((R * 4 + rt1) * 8 + 4 * kh1) * 4096u;
    xs = WS_X1B + (unsigned)(R * 4 + rt1) * 20480u + (unsigned)(5 * c + 2 * kh1) * 512u; wl2 = (unsigned)(120 + WO3 + (w < 4 ? w + 16 : w)) * 1024u;
    os = (unsigned)(((R * 64 + tau(rt1, 0)) * D_ + 80 * c + 32 * kh1) * 2);
    zos = WS_Z2F + (unsigned)((c * 64 + 4 * (R >> 1)) * 7 + (w < 6 ? w + 1 : 0)) * 1024u + (unsigned)(R & 1) * 512u; pos = WS_PART + (unsigned)(R * 64 + wq) * 128u;
    s1 = (unsigned)((w ^ 4) * 3072 + 1024); s3 = (unsigned)(w * 3072 + 1024);
    PINS(wx8); PINS(wx4); PINS(wgb); PINS(fzs); PINS(uo); PINS(ui); PINS(xs); PINS(wl2); PINS(os); PINS(zos); PINS(pos); PINS(s1); PINS(s3);
    SCHED();
    };
    if (GE) late_scalars();
  if (w < 4) {
    load_b2(12, BK);
    if (BG && is_conv) {
      lf_wait(40, 4);
      load_b2(B0, 12);
    }
    load_b2(BK, 35);
  }
  wo_put();
  STAMPF(29);
    if (w == 7) {
      asm volatile("" : "+v"(xf3[0]), "+v"(xf3[1]), "+v"(xf3[2]));
      if (__any(ne2(xf3[0], xf3[1]) != 0u) || __builtin_amdgcn_ballot_w64(xf3[2] == tag) != 0xffffffffull) {
        const unsigned fo[3] = {xf3[0] != tag ? xfb : ~0u, xf3[1] != tag ? xfb + 8192u : ~0u, l < 32 && xf3[2] != tag ? xfb + 16384u : ~0u};
        wait_lines<3, YSL>(rs, fo, tag);
      }
      if (l == 0) lf[33] = tag;
    }
    lf_wait(33, 1);
    STAMP(7);
    SCHED();
#pragma unroll
    for (int q = 0; q < 3; ++q) LDX(q);
    SCHED();
    __builtin_amdgcn_s_waitcnt(0x0F73);
    BAR();
    STAMP(8);
#pragma unroll
    for (int j = 0; j < 7; ++j) acc[j] = (f32x4){0.f, 0.f, 0.f, 0.f};
    f32x4 accg = {0.f, 0.f, 0.f, 0.f};
    u32x4 bf[7];
#pragma unroll
    for (int j = 0; j < 7; ++j) bf[j] = BFRAG(0, j);
    SCHED();
#pragma unroll
    for (int q = 3; q < 6; ++q) LDX(q);
    if (w == 3) flag4(WS_FWO + (unsigned)(c * 32 + R) * 128u);
    SCHED();
    if (!GE) late_scalars();
    auto xswap = [&](int q, int e) {
#pragma unroll
      for (int d = 0; d < 2; ++d) {
        if (e == 1) h2[q][2 + d] ^= (unsigned)__builtin_amdgcn_update_dpp(0, (int)h2[q][d], 0x128, 0xf, 0x3, false);
        else h2[q][d] ^= (unsigned)__builtin_amdgcn_update_dpp(0, (int)h2[q][2 + d], 0x128, 0xf, 0xC, false);
      }
      asm volatile("" : "+v"(h2[q]));
    };
#define XSW 1
#pragma unroll
    for (int q = 0; q < 10; ++q) {
      if (q < XSW) { xswap(q, 0); xswap(q, 1); xswap(q, 2); }
      if (q == 5 && w == 1 && __builtin_amdgcn_ballot_w64(f1v == tag) != 0xfffffull) wait_lines<1, 4>(rs, f1o, tag);
      if (q == 5) { __builtin_amdgcn_s_barrier(); asm volatile("" ::: "memory"); }
      SCHED();
      accg = MFMA16(h2[q], h2[q], accg);
      SCHED();
#pragma unroll
      for (int j = 0; j < 7; ++j) {
        acc[j] = MFMA16(h2[q], bf[j], acc[j]);
        if (q == 9 && j == 1) *(f32x4*)(lds + pgo) = accg;
        if (q < 9) bf[j] = BFRAG(q + 1, j);
        if (q < 9 && q + 1 >= XSW && j >= 3 && j < 6) xswap(q + 1, j - 3);
        if (q == 1 && j == 6) f1v = ld4(rs, f1o[0]);
        if (q == 2 && j == 0) bs_raw = __builtin_amdgcn_raw_buffer_load_b16(__builtin_amdgcn_make_buffer_rsrc((void*)p.b_sp, 0, S_ * 2, 0x00020000), bso, (unsigned)R * 128u, 0);
#define KCQ 4
#define WA 8
#define WB 12
#define WQ0 2
        if (WQ0 == 2 && q >= 2 && j == 2) LDW(q - 2);
        if (WQ0 == 6 && q >= 6 && (j == 2 || j == 4)) LDW(2 * (q - 6) + (j >> 2));
        if (WQ0 == 3 && q >= 2 && q < 5 && (j & 1) && 3 * (q - 2) + (j >> 1) < WA) LDW(3 * (q - 2) + (j >> 1));
        if (WQ0 == 4 && q >= 2 && !(q & 1) && j == 2) LDW((q - 2) >> 1);
        if ((j == 0 || j == 4) && 6 + 2 * q + (j >> 2) < 10) LDX(6 + 2 * q + (j >> 2));
        if (q == KCQ && j == 0) kc_ld(1);
        if (q == 2 && j == 1 && cs1) cs_ld(csv, c * 7 + R - 25);
        if (q >= 5 && j == 5) {
          const int i = (5 + q) * 7 + (w < 7 ? w : 0);
          dma1k(rs, lds + i * 1024, l16, WS_WINF + (unsigned)c * WBLK + (unsigned)i * 1024u);
          if (q == 9) { const int i2 = 28 + (w < 7 ? w : 0); dma1k(rs, lds + i2 * 1024, l16, WS_WINF + (unsigned)c * WBLK + (unsigned)i2 * 1024u); }
        }
        SCHED();
      }
    }
    STAMP(31);
  }
  unsigned sro, swo, zo, zro, po;
  auto gelu_n = [&](f32x2* x, auto N_, auto&& fill) {
    constexpr int N = decltype(N_)::value;
    const float c0 = -2.0f * 0.7978845608028654f * 1.4426950408889634f, c1 = c0 * 0.044715f;
    f32x2 k0 = {c0, c0}, k1 = {c1, c1}, k2 = {1.0f, 1.0f};
    asm volatile("" : "+v"(k0), "+v"(k1), "+v"(k2));
    f32x2 t[N];
#define GST(e) do { _Pragma("unroll") for (int i = 0; i < N; ++i) { t[i] = (e); asm volatile("" : "+v"(t[i])); } } while (0)
    GST(x[i] * x[i]);
    GST(__builtin_elementwise_fma(t[i], k1, k0));
    fill(IC<0>());
    GST(t[i] * x[i]);
    fill(IC<1>());
    GST(((f32x2){__builtin_amdgcn_exp2f(t[i][0]), __builtin_amdgcn_exp2f(t[i][1])}));
    fill(IC<2>());
    GST(t[i] + k2);
    fill(IC<3>());
    GST(((f32x2){__builtin_amdgcn_rcpf(t[i][0]), __builtin_amdgcn_rcpf(t[i][1])}));
    fill(IC<4>());
#pragma unroll
    for (int i = 0; i < N; ++i) { x[i] *= t[i]; asm volatile("" : "+v"(x[i])); }
  };
  {
    f32x2 mine[7];
    f32x2 csb[7];
    if (kh1) {
#pragma unroll
      for (int j = 0; j < 7; ++j) { *(f32x2*)(lds + rdo + j * 512) = (f32x2){acc[j][0], acc[j][1]}; mine[j] = (f32x2){acc[j][2], acc[j][3]}; }
      *(f32x4*)(lds + p6o) = acc[6];
    } else {
#pragma unroll
      for (int j = 0; j < 7; ++j) { *(f32x2*)(lds + rdo + j * 512) = (f32x2){acc[j][2], acc[j][3]}; mine[j] = (f32x2){acc[j][0], acc[j][1]}; }
      *(f32x4*)(lds + p6o) = acc[6];
      consts_put(1);
      if (cs1) cs_fin(csv, c * 7 + R - 25);
#pragma unroll
      for (int k = WA; k < WB; ++k) LDW(k);
    }
    BAR();
#define BY 0
    auto b1p = [&](int k) {
      const int i = 4 * k + (w & 3) + (k < 8 ? 35 : k > 8 ? 70 : (w & 3) < 3 ? 35 : 70) - (k == 17 ? (w & 2) : 0);
      dma1k(rs, lds + i * 1024, l16, WS_WINF + (unsigned)c * WBLK + (unsigned)i * 1024u);
    };
#define IB 5
    auto b18 = [&](int k) {
      const int i = 8 * k + w + (k < 4 ? 35 : k > 4 ? 70 : w < 3 ? 35 : 70) - (k == 8 && w > 5 ? 2 : 0);
      dma1k(rs, lds + i * 1024, l16, WS_WINF + (unsigned)c * WBLK + (unsigned)i * 1024u);
      SCHED();
    };
    if (kh1) {
#pragma unroll
      for (int k = 0; k < BY; ++k) b1p(k);
#pragma unroll
      for (int k = WA; k < WB; ++k) LDW(k);
    }
    __builtin_amdgcn_s_setprio(3);
    STAMPT(9);
    f32x2 oth[7];
    f32x2 pa[3], pb[3];
#pragma unroll
    for (int e = 0; e < 3; ++e) {
      pa[e] = (f32x2){*(const float*)(lds + pso[e]), *(const float*)(lds + pqo[e])};
      pb[e] = (f32x2){*(const float*)(lds + pso[e] + 256), *(const float*)(lds + pqo[e] + 1024)};
    }
#pragma unroll
    for (int j = 0; j < 7; ++j) { oth[j] = *(const f32x2*)(lds + oto + j * 512); csb[j] = *(const f32x2*)(lds + cso + 128 * j); }
    SCHED();
    if (IB > 6) b18(IB - 7);
    float mu2[2], rs2[2];
#pragma unroll
    for (int e = 0; e < 3; ++e) {
      const float mean = (pa[e][0] + pb[e][0]) * (1.0f / D_);
      const float var = __builtin_fmaxf((pa[e][1] + pb[e][1]) * (1.0f / D_) - mean * mean, 0.f);
      const float rstd = __builtin_amdgcn_rsqf(var + 1e-5f);
      if (e < 2) { mu2[e] = mean; rs2[e] = rstd; } else { mu_n = mean; rs_n = rstd; }
    }
    f32x2 zz[7];
    const f32x2 nmu = {-mu2[0], -mu2[1]}, rsv = {rs2[0], rs2[1]};
#pragma unroll
    for (int j = 0; j < 7; ++j)
      zz[j] = __builtin_elementwise_fma(__builtin_elementwise_fma(nmu, (f32x2){csb[j][0], csb[j][0]}, mine[j] + oth[j]), rsv, (f32x2){csb[j][1], csb[j][1]});
    if (IB > 5) {
#pragma unroll
      for (int j = 0; j < 7; ++j) asm volatile("" : "+v"(zz[j]));
      b18(IB - 6);
    }
    gelu_n(zz, IC<7>(), [&](auto K_) __attribute__((always_inline)) { if (IB - 5 + decltype(K_)::value >= 0 && IB) b18(IB - 5 + decltype(K_)::value); });
    if (n16 >= 12) zz[6] = (f32x2){0.f, 0.f};
    f32x2 s1 = ((zz[0] + zz[1]) + (zz[2] + zz[3])) + ((zz[4] + zz[5]) + zz[6]);
    f32x2 s2 = zz[0] * zz[0], s3 = zz[1] * zz[1];
#pragma unroll
    for (int j = 2; j < 7; ++j) { if (j & 1) s3 = __builtin_elementwise_fma(zz[j], zz[j], s3); else s2 = __builtin_elementwise_fma(zz[j], zz[j], s2); }
    s2 += s3;
    asm("" : "+v"(s1), "+v"(s2));
    float a0 = s1[0] + dpp_f<0xB1>(s1[0]), a1 = s1[1] + dpp_f<0xB1>(s1[1]), b0 = s2[0] + dpp_f<0xB1>(s2[0]);
    a0 = (l & 1) ? a1 : a0;
    const float b1 = s2[1] + dpp_f<0xB1>(s2[1]);
    b0 = (l & 1) ? b1 : b0;
    a0 += dpp_f<0x4E>(a0); b0 += dpp_f<0x4E>(b0);
    a0 += dpp_f<0x124>(a0); b0 += dpp_f<0x124>(b0);
    a0 += dpp_f<0x128>(a0); b0 += dpp_f<0x128>(b0);
    {
      const unsigned pa0 = __builtin_amdgcn_ds_bpermute(bsrc, __float_as_uint(a0)), pb0 = __builtin_amdgcn_ds_bpermute(bsrc, __float_as_uint(b0));
      ST16S(((u32x4){tag, pa0, tag, pb0}), rs, ppo, pos + (unsigned)c * 1024u, SC1);
    }
    SCHED();
    __builtin_amdgcn_s_setprio(0);
    STAMP(10);
#define BO 14
    if (kh1) {
#pragma unroll
      for (int k = 2 * IB + BY + BO; k < 18; ++k) b1p(k);
    } else {
#pragma unroll
      for (int k = 2 * IB + BY; k < 2 * IB + BY + BO && k < 18; ++k) b1p(k);
    }
#define WC 16
#define IT 0
#pragma unroll
    for (int k = WB; k < (IT ? WB : WC); ++k) LDW(k);
#pragma unroll
    for (int j = 0; j < 7; ++j)
      *(unsigned*)(lds + L_Z + (((rt1 >> 1) * 7 + j) * 64 + l) * 16 + (4 * (rt1 & 1) + 2 * kh1) * 2) = pack2(zz[j][0], zz[j][1]);
    SCHED();
#define TR0 2
#define TR1 TR0
#define TR2 TR1
#define TR3 5
    sfor<0, 4 * TR0>([&](auto X_) __attribute__((always_inline)) {
      constexpr int x = decltype(X_)::value, n = WC - WB, k0 = x * n / (4 * TR0), k1 = (x + 1) * n / (4 * TR0);
      if (IT) sfor<k0, k1>([&](auto K_) __attribute__((always_inline)) { LDW((WB + decltype(K_)::value) & 31); SCHED(); });
      WSP_TX(x);
      SCHED();
    });
    sro = (unsigned)(L_ST2 + h4 * 16);
    swo = (unsigned)(L_ST2 + (16 * rt1 + 4 * ((l >> 1) & 3) + 2 * kh1 + (l & 1)) * 4);
    zo = w < 7 ? (unsigned)(h4 * 7168 + n16 * 16) : 0x80000000u;
    zro = (unsigned)(L_Z + (w < 7 ? w : 0) * 1024) + l16;
    po = (unsigned)((l >> 3) * 1024 + (l & 7) * 16);
    asm volatile("" : "+v"(sro), "+v"(swo), "+v"(zo), "+v"(zro), "+v"(po));
    asm volatile("s_waitcnt vmcnt(%0)" :: "n"(WC - WB) : "memory");
    BAR();
    STAMPT(11);
  }
  unsigned z1d[7];
  constexpr int L_RED = 28672;
#define WOA(i) (((i) < 28 ? (i) : (i) < 108 ? (i) + 32 : (i) < 120 ? (i) + 37 : (i) - 92) * 1024)
#define RBAR() do { __builtin_amdgcn_s_barrier(); asm volatile("" ::: "memory"); } while (0)
#define ZAUX 16
#define NW 14
#define WOD(i0, x) dma1k(rs, lds + WOA(i0) + (x), l16, wgb + (unsigned)(i0) * 1024u + (x))
  auto wo_dma = [&](int k) {};
  u32x4 zw[NW];
#define LDZ(f) do { if (((f) >> 3) == 4) { zs[(f) & 7] += 4096u; asm volatile("" : "+s"(zs[(f) & 7])); } \
    zw[(f) % NW] = __builtin_amdgcn_raw_buffer_load_b128(rs, l16 + (unsigned)(((f) >> 3) & 3) * 1024u, zs[(f) & 7], ZAUX); } while (0)
#define WD 24
#define WE 32
#define QS 5
#define PQ 3
#define NW0 NW
#define VMW(n) asm volatile("s_waitcnt vmcnt(%0)" ::"n"(n) : "memory")
#define FQ 10
#define BFK 0
#define BFN 0
#define PP 1
#define WZ 2
#define WY 8
  static_assert((BFK == 0 || QS < 6) && QS >= 4 && QS <= 6 && PQ >= 2 && PQ < QS && (WZ < 4 || QS >= 5), "");
  {
    f32x4 acc[7];
#pragma unroll
    for (int j = 0; j < 7; ++j) acc[j] = (f32x4){0.f, 0.f, 0.f, 0.f};
    u32x4 bf[7];
#pragma unroll
    for (int j = 0; j < 7; ++j) bf[j] = BFRAG(4, j);
    u32x4 pv, zf[2];
    unsigned fwv;
    auto ld_pv = [&]() { pv = __builtin_amdgcn_raw_buffer_load_b128(rs, po, pos, SC1); };
    auto z1 = [&](auto Q0_, auto Q1_) __attribute__((always_inline)) {
#pragma unroll
      for (int qi = decltype(Q0_)::value; qi < decltype(Q1_)::value; ++qi) {
        const int q = (qi + 4) % 10, qn = (qi + 5) % 10;
        if (qi == FQ) {
          if (kh1) VMW(WE - WD + WZ); else VMW(0);
          RBAR();
          if (w == 3) flag4(WS_FZ + (unsigned)(c * 32 + R) * 128u);
        }
        SCHED();
#pragma unroll
        for (int j = 0; j < 7; ++j) {
          acc[j] = MFMA16(bf[j], h2[q], acc[j]);
          if (qi < 9 && (qi != QS - 1 || j < BFK)) bf[j] = BFRAG(qn, j);
          if (qi < 2 && (j == 1 || j == 4)) { const int i = (2 * qi + (j >> 2)) * 7 + (w < 7 ? w : 0); dma1k(rs, lds + i * 1024, l16, WS_WINF + (unsigned)c * WBLK + (unsigned)i * 1024u); }
          if (qi == 2 && j == 0) kc_ld(0);
          if (qi == 2 && j == 3) fwv = ld4(rs, fwo[0]);
          if (qi == PQ && j == 1) ld_pv();
          if (qi == QS - 1 && j < 2) zf[j] = *(const u32x4*)(lds + zro + j * 7168);
          if (!PP && qi >= QS && (j & 1) && WD + 3 * (qi - QS) + (j >> 1) < WE) LDW((WD + 3 * (qi - QS) + (j >> 1)) & 31);
          SCHED();
        }
      }
    };
    auto req1 = [&]() {
#pragma unroll
      for (int k = WC; k < WD; ++k) { LDW(k); SCHED(); }
    };
    auto req2 = [&]() {
#pragma unroll
      for (int k = WD; k < (PP ? WE : WD); ++k) { LDW(k); SCHED(); }
      sfor<0, WZ>([&](auto G_) __attribute__((always_inline)) { WOD(68 + 8 * decltype(G_)::value, wx8); SCHED(); });
    };
    if (kh1) req1();
    z1(IC<0>(), IC<QS>());
    STAMPT(29);
    f32x2 zv[2][4];
#pragma unroll
    for (int i = 0; i < 8; ++i) { zv[i >> 2][i & 3] = (f32x2){bflo(zf[i >> 2][i & 3]), bfhi(zf[i >> 2][i & 3])}; asm volatile("" : "+v"(zv[i >> 2][i & 3])); }
#pragma unroll
    for (int g = TR0; g < TR1; ++g) WSP_TR(g);
    __builtin_amdgcn_s_setprio(3);
    if (__any(ne2(pv[0], pv[2]) != 0u)) {
      _Pragma("unroll 1") for (unsigned spins = 0; spins < SPIN_CAP; ++spins) {
        ld_pv();
        if (!__any(ne2(pv[0], pv[2]) != 0u)) break;
        __builtin_amdgcn_s_sleep(1);
      }
    }
    {
      float t1 = __uint_as_float(pv[1]), t2 = __uint_as_float(pv[3]);
      t1 += dpp_f<0x128>(t1); t2 += dpp_f<0x128>(t2);
      auto x = __builtin_amdgcn_permlane16_swap(__float_as_uint(t1), __float_as_uint(t2), false, false);
      const unsigned y = __float_as_uint(__uint_as_float(x[0]) + __uint_as_float(x[1]));
      auto z = __builtin_amdgcn_permlane32_swap(y, y, false, false);
      const unsigned u = __float_as_uint(__uint_as_float(z[0]) + __uint_as_float(z[1]));
      auto v = __builtin_amdgcn_permlane16_swap(u, u, false, false);
      const float mean = __uint_as_float(v[0]) * (1.0f / FH);
      const float var = __builtin_fmaxf(__uint_as_float(v[1]) * (1.0f / FH) - mean * mean, 0.f);
      const float rstd = __builtin_amdgcn_rsqf(var + 1e-5f);
      *(float*)(lds + swo) = rstd;
      *(float*)(lds + swo + 256) = -mean * rstd;
    }
    SCHED();
    consts_put(0);
    if (w == 3 && __builtin_amdgcn_ballot_w64(fwv == tag) != 0xfffffffull) wait_lines<1, 1>(rs, fwo, tag);
    if (!kh1) req1();
    BAR();
    STAMP(12);
    {
      f32x4 sa[2][2], sb[2][2];
#pragma unroll
      for (int i = 0; i < 4; ++i) { sa[i >> 1][i & 1] = *(const f32x4*)(lds + sro + i * 64); sb[i >> 1][i & 1] = *(const f32x4*)(lds + sro + i * 64 + 256); }
      SCHED();
#pragma unroll
      for (int j = BFK; j < (BFN ? 7 : BFK); ++j) bf[j] = BFRAG((QS + 4) % 10, j);
      SCHED();
#pragma unroll
      for (int m = 0; m < 2; ++m) {
        u32x4 o;
#pragma unroll
        for (int e = 0; e < 4; ++e) {
          const f32x4 a = sa[m][e >> 1], b = sb[m][e >> 1];
          const f32x2 x = __builtin_elementwise_fma(zv[m][e], (f32x2){a[2 * (e & 1)], a[2 * (e & 1) + 1]}, (f32x2){b[2 * (e & 1)], b[2 * (e & 1) + 1]});
          o[e] = pack2(x[0], x[1]);
        }
        ST16S(o, rs, zo + (unsigned)m * 256u, zos, SC1);
      }
    }
    SCHED();
    __builtin_amdgcn_s_setprio(0);
    STAMPT(13);
#pragma unroll
    for (int j = BFK; j < (BFN ? BFK : 7); ++j) bf[j] = BFRAG((QS + 4) % 10, j);
    if (kh1) req2();
    z1(IC<QS>(), IC<10>());
    f32x2 mine[7];
    if (kh1) {
#pragma unroll
      for (int j = 0; j < 7; ++j) { *(f32x2*)(lds + rdo + L_RED + j * 512) = (f32x2){acc[j][0], acc[j][1]}; mine[j] = (f32x2){acc[j][2], acc[j][3]}; }
    } else {
#pragma unroll
      for (int j = 0; j < 7; ++j) { *(f32x2*)(lds + rdo + L_RED + j * 512) = (f32x2){acc[j][2], acc[j][3]}; mine[j] = (f32x2){acc[j][0], acc[j][1]}; }
    }
    if (!kh1) req2();
#pragma unroll
    for (int g = TR1; g < TR2; ++g) WSP_TR(g);
    if (FQ > 9) VMW(WE - WD + WZ);
    BAR();
    if (FQ > 9 && w == 3) flag4(WS_FZ + (unsigned)(c * 32 + R) * 128u);
    STAMP(6);
    auto req = [&](auto G0_, auto G1_) __attribute__((always_inline)) {
#pragma unroll
      for (int k = WE; k < 32; ++k) LDW(k);
      SCHED();
      sfor<decltype(G0_)::value, decltype(G1_)::value>([&](auto G_) __attribute__((always_inline)) {
        constexpr int i0 = 4 * decltype(G_)::value;
        WOD(i0 < 68 ? i0 : i0 + 8 * WZ, wx4); SCHED();
      });
    };
#define IL 1
#define XF 12
#define XC 18
    static_assert(!IL || (WZ == 2 && WE == 32), "");
    auto wo8 = [&](auto K_) __attribute__((always_inline)) {
      constexpr int k = decltype(K_)::value;
      if (k < 12) WOD(k < 3 ? 8 * k : k < 8 ? 8 * k + 4 : 8 * k + 20, wx8);
      else if (k == 12) dma1k(rs, lds + (kh1 ? WOA(116) : WOA(24)) + wx4, l16, wgb + (kh1 ? 116u : 24u) * 1024u + wx4);
      SCHED();
    };
    if (IL) { wo8(IC<0>()); wo8(IC<1>()); }
    if (!IL && kh1) req(IC<0>(), IC<WY>());
    u32x4 gv[8];
    auto gb_ld = [&]() {
#pragma unroll
      for (int k = 0; k < 8; ++k) gv[k] = __builtin_amdgcn_raw_buffer_load_b128(rs, l < 40 ? l16 : 0u, WS_MODQ + (unsigned)(k * 2560 + 1920 + 80 * c) * 8u, SC1);
    };
    if (w == 2) gb_ld();
    f32x2 z[7];
    {
      f32x2 oth[7];
      f32x4 cc[7];
#pragma unroll
      for (int j = 0; j < 7; ++j) {
        oth[j] = *(const f32x2*)(lds + oto + L_RED + j * 512);
        cc[j] = *(const f32x4*)(lds + L_CONST + (16 * j + 4 * h4 + 2 * kh1) * 8);
      }
      SCHED();
#pragma unroll
      for (int j = 0; j < 7; ++j)
        z[j] = __builtin_elementwise_fma(__builtin_elementwise_fma((f32x2){-mu_n, -mu_n}, (f32x2){cc[j][0], cc[j][1]}, oth[j] + mine[j]),
                                         (f32x2){rs_n, rs_n}, (f32x2){cc[j][2], cc[j][3]});
    }
    if (IL) {
#pragma unroll
      for (int j = 0; j < 7; ++j) asm volatile("" : "+v"(z[j]));
      wo8(IC<2>());
    }
    gelu_n(z, IC<7>(), [&](auto K_) __attribute__((always_inline)) { if (IL) wo8(IC<3 + decltype(K_)::value>()); });
#pragma unroll
    for (int j = 0; j < 7; ++j) z1d[j] = pack2(z[j][0], z[j][1]);
    asm volatile("" : "+v"(z1d[0]), "+v"(z1d[1]), "+v"(z1d[2]), "+v"(z1d[3]), "+v"(z1d[4]), "+v"(z1d[5]), "+v"(z1d[6]));
    SCHED();
    STAMPT(30);
    if (w == 2) {
      auto bad = [&]() {
        unsigned d = 0u;
#pragma unroll
        for (int k = 0; k < 8; ++k) d |= ne2(gv[k][0], gv[k][2]);
        return __any(d != 0u);
      };
      if (bad()) {
        _Pragma("unroll 1") for (unsigned spins = 0; spins < SPIN_CAP; ++spins) {
          __builtin_amdgcn_s_sleep(8);
          gb_ld();
          if (!bad()) break;
        }
      }
      f32x2 t[4];
#pragma unroll
      for (int k = 0; k < 4; ++k) t[k] = (f32x2){__uint_as_float(gv[2 * k][1]) + __uint_as_float(gv[2 * k + 1][1]), __uint_as_float(gv[2 * k][3]) + __uint_as_float(gv[2 * k + 1][3])};
      *(f32x2*)(lds + L_MISC + l * 8) = rbf2((t[0] + t[1]) + (t[2] + t[3]));
    }
    if (!IL && !kh1) req(IC<WY>(), IC<30 - 2 * WZ>());
    const unsigned fo[1] = {l < 4 ? fzs + (unsigned)l * 128u : ~0u};
#define FPY 1
#define FPO FPY
#define FSL 1
    unsigned fv;
    sfor<4 * TR2, 32>([&](auto X_) __attribute__((always_inline)) {
      constexpr int x = decltype(X_)::value;
      wo8(IC<8 + x - 4 * TR2>());
      if (x == XF) { fv = ld4(rs, fo[0]); SCHED(); }
      if (x == XC) {
        STAMP(14);
        if (__builtin_amdgcn_ballot_w64(fv == tag) != 15ull) wait_lines<1, FSL>(rs, fo, tag);
        STAMPT(23);
      }
      if (x >= XC && x - XC < NW) { LDZ((x - XC) & 15); SCHED(); }
      WSP_TX(x);
      SCHED();
    });
#pragma unroll
    for (int f = 32 - XC; f < NW; ++f) { LDZ(f); SCHED(); }
  }

#define WO2 15
#define TRC (IL ? 8 : TR3)
  {
    SCHED();
#pragma unroll
    for (int g = TRC; g < 8; ++g) WSP_TR(g);
    SCHED();
    STAMP(20);
    const int rt = rt1, kh = kh1;
    const unsigned rw = (unsigned)(L_RED + w * 512);
    unsigned rr = (unsigned)(L_RED + w * 4096 + l * 4);
    asm volatile("; m0 is set here, not in front of the first store (one wait state)" : "+v"(rr) : "{m0}"(rw));
    const float bs = bflo(bs_raw);
    unsigned long long hm = 0xffff000000000000ull;
    asm volatile("" : "+s"(hm));
    unsigned l8;
    f32x4 acc2[2][4];
    float rv[8][2];
    u32x4 ud;
    u32x4 P[2][8];
    u32x2 xp[3];
    auto ld_p = [&](int pt, int i) { P[pt][i] = __builtin_amdgcn_raw_buffer_load_b128(rs, l16 + (unsigned)pt * 1024u, ui + (unsigned)i * 2048u, SC1); };
#define RED_PUT1(j, i) asm volatile("ds_write_addtid_b32 %0 offset:%2" :: "v"(acc2[(j) & 1][((i) >> 2) & 3][(i) & 3]), "{m0}"(rw), "n"((((i) >> 2) & 3) * 4096 + (((i) >> 1) & 1) * 16384 + ((i) & 1) * 256) : "memory")
#define RED_PUT(j, r) do { RED_PUT1(j, 4 * (r)); RED_PUT1(j, 4 * (r) + 1); RED_PUT1(j, 4 * (r) + 2); RED_PUT1(j, 4 * (r) + 3); } while (0)
#define RED_GET(i) do { f32x2 x_; asm volatile("ds_read2st64_b32 %0, %1 offset0:%2 offset1:%3" : "=v"(x_) : "v"(rr), "n"(2 * (i)), "n"(2 * (i) + 1) : "memory"); rv[i][0] = x_[0]; rv[i][1] = x_[1]; } while (0)
    float zz[2], gg[2];
    unsigned cs;
    auto sum_rows_u = [&](unsigned v) {
      auto a = __builtin_amdgcn_permlane16_swap(v, v, false, false);
      v = a[0] + a[1];
      auto b = __builtin_amdgcn_permlane32_swap(v, v, false, false);
      return b[0] + b[1];
    };
    auto red_fin = [&](int j, int k, bool fuse = false) {
      const int e = k & 1, i = k >> 1;
#define VADD(d, x, y) asm volatile("v_add_f32 %0, %1, %2" : "=v"(d) : "v"(x), "v"(y))
      if (i < 4) VADD(rv[2 * i][e], rv[2 * i][e], rv[2 * i + 1][e]);
      if (i == 4 || i == 5) VADD(rv[4 * (i & 1)][e], rv[4 * (i & 1)][e], rv[4 * (i & 1) + 2][e]);
      if (i == 6) VADD(rv[0][e], rv[0][e], bs);
      if (i == 7) VADD(gg[e], rv[0][e], rv[4][e]);
      if (i == (fuse ? 9 : 8)) zz[e] = e ? bfhi(z1d[(j + 6) % 7]) : bflo(z1d[(j + 6) % 7]);
      if (i == 9) zz[e] *= gg[e];
      if (i == 9 || (i == 8 && !fuse)) asm volatile("" : "+v"(zz[e]));
      if (k == 20) { ud[j & 3] = pack2(zz[0], zz[1]); asm volatile("" : "+v"(ud[j & 3])); }
      if (k == 21) {
        if (j == 0) asm volatile("v_cndmask_b32 %0, %0, 0, %1" : "+v"(ud[0]) : "s"(hm));
        if (j == 2) asm volatile("v_add3_u32 %0, %1, %2, %3" : "=v"(cs) : "v"(ud[0]), "v"(ud[1]), "v"(ud[2]));
        if (j == 3) {
          cs = tag - sum_rows_u(cs + ud[3]);
          asm volatile("v_cndmask_b32 %0, %0, %1, %2" : "+v"(ud[0]) : "v"(cs), "s"(hm));
        }
        if (j >= 4) { ud[3] = (j == 4 ? tag : ud[3]) - ud[j & 3]; asm volatile("" : "+v"(ud[3])); }
        if (j % 3 == 0 && j) ST16S(ud, rs, l16 + (unsigned)(j / 6) * 1024u, uo, SC1);
      }
    };
    struct Sch { int b1, b2, wp[16], rp[8], vp[22], lp[8], pr[32]; };
    constexpr Sch S = {18, 28, {0, 1, 2, 3, 4, 5, 6, 7, 8, 9, 10, 11, 12, 12, 15, 15}, {19, 19, 20, 22, 23, 23, 24, 24}, {29, 29, 30, 30, 31, 31, 32, 33, 35, 36, 37, 38, 39, 40, 41, 42, 43, 46, 43, 46, 48, 48},
                       {1, 1, 1, 1, 1, 1, 1, 1}, {0, 0, 2, 1, 0, 0, 0, 0, 0, 0, 2, 2, 0, 0, 0, 0, 0, 0, 0, 1, 0, 0, 0, 1, 0, 0, 2, 1, 0, 0, 0, 0}};
    constexpr Sch F = {15, 23, {0, 1, 1, 2, 3, 4, 4, 5, 6, 7, 8, 9, 10, 11, 12, 12}, {15, 16, 16, 17, 17, 18, 18, 19}, {23, 23, 24, 24, 25, 26, 26, 27, 27, 28, 28, 29, 30, 30, 31, 31, 22, 22, 32, 33, 34, 35},
                       {1, 2, 1, 1, 1, 1, 1, 1}, {0, 2, 1, 0, 0, 1, 1, 0, 0, 2, 1, 0, 0, 2, 1, 0, 0, 2, 1, 0, 0, 2, 1, 0, 0, 2, 1, 0, 0, 2, 1, 0}};
#define VM 3
#define JF 4
#define RO 3
#define XE 1
#define CE 0
#define AP0 2
#define P1B 2
#define P1S 3
    unsigned wb[3], xo;
    const unsigned wl[3] = {(unsigned)(120 + WO3 + w) * 1024u, (unsigned)(128 + WO3 + w) * 1024u, wl2}, wg = wgb - (unsigned)(WO3 * 1024);
#define NAF 32
#define NA0 16
#define NA1 NA0
    u32x4 af[NAF];
#define LDA(m) af[(m) % NAF] = *(const u32x4*)(lds + wb[WOA(2 * (m)) >> 16] + (WOA(2 * (m)) & 65535))
    unsigned ck[2];
    const unsigned tag4 = tag * 4u;
    auto p_chk = [&](int pt, int pr, int k) {
      u32x4* q = P[pt] + 4 * pr;
      if (k < 2) ck[k & 1] = q[k & 1][0] + q[k & 1][1];
      else if (k < 8) ck[k & 1] = ck[k & 1] + q[(k & 1) + 2 * ((k >> 2) & 1)][k & 2] + q[(k & 1) + 2 * ((k >> 2) & 1)][(k & 2) + 1];
      else if (k == 8) ck[0] = pt ? ck[0] + ck[1] : sum_rows_u(ck[0] + ck[1]);
      if (k < 9) asm volatile("" : "+v"(ck[k & 1]));
      if (k == 9) {
        if (__builtin_expect(__any(ck[0] != tag4), 0)) {
          _Pragma("unroll 1") for (unsigned spins = 0; spins < SPIN_CAP; ++spins) {
            unsigned d = 0u;
#pragma unroll
            for (int i = 0; i < 4; ++i) ld_p(pt, 4 * pr + i);
#pragma unroll
            for (int i = 0; i < 4; ++i) d += q[i][0] + q[i][1] + q[i][2] + q[i][3];
            if (!pt) d = sum_rows_u(d);
            if (!__any(d != tag4)) break;
            __builtin_amdgcn_s_sleep(1);
          }
        }
      }
      if (k >= 10 && !pt) { unsigned x = q[(k - 10) & 3][0]; asm volatile("v_cndmask_b32 %0, %0, 0, %1" : "+v"(x) : "s"(hm)); q[(k - 10) & 3][0] = x; }
      if (k >= 10 && k < 13 && pt) { q[(k - 10) % 3][3] = q[3][(k - 10) % 3]; asm volatile("" : "+v"(q[(k - 10) % 3])); }
    };
    sfor<0, 224>([&](auto M_) __attribute__((always_inline)) {
      constexpr int mm = decltype(M_)::value, j = mm >> 5, m = mm & 31, y = m - (32 - 4 * RO);
      constexpr int t = j == 6 && y >= 0 ? 8 - RO + (y % (2 * RO + !RO)) / 2 : m >> 2, r = j == 6 && y >= 0 ? (y & 1) + 2 * (y / (2 * RO + !RO)) : m & 3, f = 8 * j + t;
      constexpr Sch C = j < JF ? S : F, B = j <= JF ? S : F;
      if (t == 0) asm volatile("v_mfma_f32_16x16x32_bf16 %0, %1, %2, 0" : "=&v"(acc2[j & 1][r]) : "v"(zw[f % NW]), "v"(wsp[r][t]), "v"(zw[(f | (f < 8 ? 1 : VM)) % NW]));
      else asm volatile("v_mfma_f32_16x16x32_bf16 %0, %1, %2, %0" : "+v"(acc2[j & 1][r]) : "v"(zw[f % NW]), "v"(wsp[r][t]), "v"(zw[(f | (f < 8 ? 1 : VM)) % NW]));
      SCHED();
      if (C.pr[m] == 1) __builtin_amdgcn_s_setprio(1);
      if ((m == C.b1 && j > 0) || (m == C.b2 && j > 0) || mm == 30) { asm volatile("" ::: "memory"); __builtin_amdgcn_s_waitcnt(0xC07F); RBAR(); SCHED(); }
      sfor<0, 16>([&](auto I_) __attribute__((always_inline)) {
        constexpr int i = decltype(I_)::value;
        if (j > 0 && C.wp[i] == m) { RED_PUT1(j + 1, i); SCHED(); }
        if (j > 0 && i < 8 && C.rp[i & 7] == m) { RED_GET(i & 7); SCHED(); }
      });
      if (j == 5 && m == 20) {
#pragma unroll
        for (int i = 0; i < 3; ++i) { wb[i] = (unsigned)(kh * 1024 + i * 65536) + l16; asm volatile("" : "+v"(wb[i])); }
      }
      if (XE && j == 5 && m == 13) {
        asm volatile("v_lshrrev_b32 %0, 1, %1" : "=v"(l8) : "v"(l16));
        xo = (l8 & 0x180u) + (unsigned)psi((l8 >> 3) & 15) * 8u;
        asm volatile("" : "+v"(xo));
      }
      if (RO && j == 6 && m >= 28) { RED_PUT1(6, 2 * (m - 28)); RED_PUT1(6, 2 * (m - 28) + 1); SCHED(); }
      if (CE == 1 && j == 6 && m >= 19 && m < 22) sfor<0, 3>([&](auto K_) __attribute__((always_inline)) { p_chk(0, 0, 3 * (m - 19) + decltype(K_)::value); SCHED(); });
      constexpr int ap[20] = {7, 8, 9, 10, 11, 13, 13, 14, 14, 19, 20, 21, 22, 23, 24, 25, 26, 27, 28, 29};
      sfor<0, NA0>([&](auto N_) __attribute__((always_inline)) { if (j == 6 && ap[AP0 + decltype(N_)::value] == m) { LDA(decltype(N_)::value); SCHED(); } });
      sfor<0, 22>([&](auto K_) __attribute__((always_inline)) {
        constexpr int k = decltype(K_)::value;
        if (j > 1 && B.vp[k] == m + 32) { red_fin(j > 1 ? j - 2 : 0, k, B.vp[16] == B.vp[18]); SCHED(); }
        if (j > 0 && C.vp[k] == m) { red_fin(j > 0 ? j - 1 : 0, k); SCHED(); }
      });
      if (C.pr[m] == 2) __builtin_amdgcn_s_setprio(0);
      if ((m & 3) == C.lp[m >> 2]) {
        if (f > 0 && f - 1 + NW < 56) LDZ(f - 1 + NW);
        if (j == 6 && (m >> 2) >= P1S) ld_p(0, ((m >> 2) - P1S) & 7);
        if (XE && j == 5 && m >= 20) xp[((m >> 2) - 5) % 3] = __builtin_amdgcn_raw_buffer_load_b64(rs, xo + (unsigned)((m >> 2) - 5) * 512u, xs, BULK_AUX);
        SCHED();
      }
    });
    STAMP(15);
#define NM0 0
#define OAUX 0
#define GM 19
#define MB 24
    f32x4 acc[5];
    f32x4 xf[3], gf[3];
    unsigned go, oo, eo, ho, tw, tr, o1, ta, tb;
    const unsigned s2 = (unsigned)(L_MISC + 128 * kh), s4 = s1 - (unsigned)kh * 1024u;
    unsigned long long om = kh ? ~0ull : 0xccccccccccccccccull;
#define PIN2(a, b) asm volatile("" : "+v"(a), "+v"(b))
    auto glue = [&](int k) {
      if (k == 0) { ta = l8 >> 5; tb = l16 & 48u; PIN2(ta, tb); }
      if (k == 1) { tr = __umul24(ta, 112u) + tb; oo = ta & 12u; PIN2(tr, oo); }
      if (k == 2) { tr += s1; oo += ta; PIN2(tr, oo); }
      if (k == 3) { oo = __umul24(oo, 1280u) + tb; ta = l8 >> 4; PIN2(oo, ta); }
      if (k == 4) { ta &= 24u; tb = l16 & 0xf0u; PIN2(ta, tb); }
      if (k == 5) { tw = __umul24(tb, 7u) + ta; go = (ta << 1) + s2; PIN2(tw, go); }
      if (k == 6) { tw += s1; asm volatile("v_cndmask_b32 %0, %1, -2.0, %2" : "=v"(o1) : "v"(oo), "s"(om)); asm volatile("" : "+v"(tw)); }
      if (k == 7) { eo = l16 + s3; ho = l16 + s4; PIN2(eo, ho); }
      if (k >= 8 && k < 14) {
        const unsigned x = xp[((k - 8) >> 1) % 3][k & 1];
        float lo = bflo(x), hi = bfhi(x);
        PIN2(lo, hi);
        xf[((k - 8) >> 1) % 3][2 * (k & 1)] = lo; xf[((k - 8) >> 1) % 3][2 * (k & 1) + 1] = hi;
      }
    };
    auto g3 = [&](auto M0_, auto M1_) __attribute__((always_inline)) {
      sfor<decltype(M0_)::value, decltype(M1_)::value>([&](auto M_) __attribute__((always_inline)) {
        constexpr int mi = decltype(M_)::value;
        constexpr int m = mi < 55 ? mi : mi < 64 ? 55 + 5 * ((mi - 55) / 3) + (mi - 55) % 3 : 58 + 5 * ((mi - 64) / 2) + (mi & 1), u2 = m / 5;
        const u32x4 fb = u2 < 8 ? P[0][u2 & 7] : P[1][u2 < 11 ? (u2 - 8) & 3 : (u2 - 7) & 7];
        if (m < 5) asm volatile("v_mfma_f32_16x16x32_bf16 %0, %1, %2, 0" : "=&v"(acc[m % 5]) : "v"(af[m % NAF]), "v"(fb));
        else asm volatile("v_mfma_f32_16x16x32_bf16 %0, %1, %2, %0" : "+v"(acc[m % 5]) : "v"(af[m % NAF]), "v"(fb));
        SCHED();
        if (mi == NM0) RBAR();
        if (mi >= NM0 && mi < NM0 + 3) dma1k(rs, lds + wl[(mi - NM0) % 3], l16, wg + wl[(mi - NM0) % 3]);
        if (mi >= 5 && mi < 19) p_chk(0, 1, mi - 5);
        if (mi >= GM && mi < GM + 14) glue(mi - GM);
        if (mi >= 47 && mi < 54) { p_chk(1, 1, 2 * (mi - 47)); SCHED(); p_chk(1, 1, 2 * (mi - 47) + 1); }
        if (!XE && mi == 3) { xo = (l8 & 0x180u) + (unsigned)psi((l8 >> 3) & 15) * 8u; asm volatile("" : "+v"(xo)); }
        if (!XE && mi >= 4 && mi < 7) xp[(mi - 4) % 3] = __builtin_amdgcn_raw_buffer_load_b64(rs, xo + (unsigned)(mi - 4) * 512u, xs, BULK_AUX);
        if (mi >= NM0 && mi < 40 && NA1 + mi - NM0 < 60) LDA(NA1 + mi - NM0);
        if (mi >= 66 && mi < 69) *(f32x4*)(lds + eo + (mi - 66) * 1024) = acc[(mi - 66) % 3];
        SCHED();
      });
    };
    __builtin_amdgcn_s_setprio(3);
#pragma unroll
    for (int k = 18; k < 22; ++k) red_fin(5, k);
    if (!XE) asm volatile("v_lshrrev_b32 %0, 1, %1" : "=v"(l8) : "v"(l16));
    if (RO) asm volatile("s_nop 2"); else { RED_PUT(6, 0); RED_PUT(6, 1); }
    RED_PUT(6, 2); RED_PUT(6, 3);
    red_fin(6, 16); red_fin(6, 17);
    SCHED();
#pragma unroll
    for (int i = 8 - P1S; i < 8 - P1S + P1B; ++i) ld_p(0, i);
    SCHED();
    BAR();
    sfor<0, 8>([&](auto I_) __attribute__((always_inline)) { RED_GET(decltype(I_)::value); });
    SCHED();
#pragma unroll
    for (int n = NA0; n < NA1; ++n) LDA(n);
#pragma unroll
    for (int i = 8 - P1S + P1B; i < 8; ++i) ld_p(0, i);
    SCHED();
#pragma unroll
    for (int k = 0; k < (CE == 2 ? 9 : 0); ++k) p_chk(0, 0, k);
    SCHED();
    sfor<0, 20>([&](auto K_) __attribute__((always_inline)) {
      constexpr int k = decltype(K_)::value;
      if (k < 8 && !(k & 1)) asm volatile("s_waitcnt lgkmcnt(%0)" :: "n"(6 - k + NA1 - NA0) : "memory");
      red_fin(6, k < 16 ? k : k + 2);
    });
    SCHED();
    __builtin_amdgcn_s_setprio(0);
    STAMPT(16);
#pragma unroll
    for (int k = CE ? 9 : 0; k < 14; ++k) p_chk(0, 0, k);
    SCHED();
    g3(IC<0>(), IC<MB>());
    STAMPT(22);
    DRAIN();
    RBAR();
    STAMPT(27);
#define MY 12
    if (MY == 0 || !kh) {
#pragma unroll
      for (int i = 0; i < 8; ++i) { ld_p(1, i); SCHED(); }
    }
    g3(IC<MB>(), IC<MB + MY>());
    if (MY > 0 && kh) {
#pragma unroll
      for (int i = 0; i < 8; ++i) { ld_p(1, i); SCHED(); }
    }
    STAMPT(26);
    g3(IC<MB + MY>(), IC<40>());
    STAMPT(24);
#pragma unroll
    for (int m = NA1 + 40 - NM0; m < 70; ++m) LDA(m);
#pragma unroll
    for (int x = 0; x < 3; ++x) gf[x] = *(const f32x4*)(lds + go + x * 64);
    SCHED();
    const rsrc_t ro = __builtin_amdgcn_make_buffer_rsrc((void*)p.out, 0, S_ * D_ * 2, 0x00020000);
    STAMPT(17);
#pragma unroll
    for (int k = 0; k < 14; ++k) p_chk(1, 0, k);
    asm volatile("s_nop 1");
    STAMPT(25);
    g3(IC<40>(), IC<70>());
    STAMPT(18);
    BAR();
    STAMPT(28);
    f32x4 o4[3];
    u32x4 ov[2];
#pragma unroll
    for (int j = 2; j >= 0; --j) o4[j] = *(const volatile LAS f32x4*)(lds + ho + j * 1024);
    asm volatile("" : "+v"(acc[2]), "+v"(acc[3]), "+v"(acc[4]));
    SCHED();
#pragma unroll
    for (int j = 2; j >= 0; --j) {
      const f32x4 v = __builtin_elementwise_fma(gf[j], o4[j] + acc[2 + j], xf[j]);
      *(volatile LAS u32x2*)(lds + tw + j * 32) = (u32x2){pack2(v[0], v[1]), pack2(v[2], v[3])};
      if (j < 2) ov[j] = *(const volatile LAS u32x4*)(lds + tr + j * 32);
    }
    ST16S(ov[1], ro, oo + 32u, os, OAUX);
    ST16S(ov[0], ro, o1, os, OAUX);
    if (bid == 0 && w == 5 && l8 == 0u) st4(rs, WS_EPOCH, tag);
  }
  STAMP(19);
}

extern "C" __device__ __attribute__((used, noinline, visibility("hidden"))) void mega_tail() {}

extern "C" void kernel_launch(void* const* d_in, const int* in_sizes, int n_in, void* d_out, int out_size,
                              void* d_ws, size_t ws_size, hipStream_t stream) {
  Params p;
  p.q = (const u16*)d_in[0];
  p.k = (const u16*)d_in[1];
  p.v = (const u16*)d_in[2];
  p.shift_k = (const u16*)d_in[3];
  p.ssm_k = (const u16*)d_in[4];
  p.ssm_D = (const u16*)d_in[5];
  p.w_in = (const u16*)d_in[7];
  p.w_sp = (const u16*)d_in[8];
  p.b_sp = (const u16*)d_in[9];
  p.w_out = (const u16*)d_in[10];
  p.cvec = (const u16*)d_in[11];
  p.w_mod = (const u16*)d_in[12];
  p.out = (u16*)d_out;
  p.ws = (char*)d_ws;
  mega<<<256, 512, 0, stream>>>(p);
}
```

```cpp
#include <hip/hip_runtime.h>
#include <stdint.h>

typedef unsigned short u16;
typedef __attribute__((ext_vector_type(8))) short bf16x8;
typedef __attribute__((ext_vector_type(4))) short bf16x4;
typedef __attribute__((ext_vector_type(4))) float f32x4;
typedef __attribute__((ext_vector_type(2))) float f32x2;
typedef __attribute__((ext_vector_type(4))) unsigned int u32x4;
typedef __attribute__((ext_vector_type(2))) unsigned int u32x2;
#define LAS __attribute__((address_space(3)))
typedef __amdgpu_buffer_rsrc_t rsrc_t;
typedef __attribute__((ext_vector_type(4))) short s16x4;

constexpr int S_ = 2048, D_ = 640, F_ = 1728, FH = 864, CB = 108;

constexpr unsigned WS_EPOCH = 0;
constexpr unsigned WS_FWI = 4096;
constexpr unsigned WS_FZ = WS_FWI + 384 * 128;
constexpr unsigned WS_FWO = WS_FZ + 256 * 128;
constexpr unsigned F4S = 896 * 128;
constexpr unsigned WS_CS = WS_FZ + 256 * 128;
constexpr unsigned WS_MOD = 131072;
constexpr unsigned WS_MODP = WS_MOD + 81920;
constexpr unsigned WS_ST1 = 393216;
constexpr unsigned WS_PART = 524288;
constexpr unsigned WS_CSP = WS_PART + 262144;
constexpr unsigned WS_FX = WS_CSP + 655360;
constexpr unsigned WS_X1B = WS_FX + 16 * 4 * 160 * 128;
constexpr unsigned WS_QB = WS_X1B + 2621440;
constexpr unsigned WS_KV3 = WS_QB + 2621440;
constexpr unsigned WS_MODQ = WS_KV3 + 1310720;
constexpr unsigned WBLK = 143360;
constexpr unsigned WS_WINF = WS_MODQ + 245760;
constexpr unsigned WS_WOUTF = WS_WINF + 16 * WBLK;
constexpr unsigned WS_Z2F = WS_WOUTF + 8 * WBLK;
constexpr unsigned WS_UF = WS_Z2F + 8 * 64 * 7168;
constexpr unsigned WS_F4 = WS_UF + 32 * 4 * 8 * 4096;
constexpr unsigned WS_QCK = WS_F4 + 4 * F4S;
constexpr unsigned WS_END = WS_QCK + 131072;
constexpr unsigned WS_STAMP = 200u << 20;

constexpr int G_COPY = 4160;
constexpr int L_U = WBLK;
constexpr int U_CH = (256 + S_) * 2 + 32;
constexpr int L_FLG = L_U + 4 * U_CH;
constexpr int L_RT = 16384;
constexpr int L_CONST = WBLK;
constexpr int L_PST = L_CONST + 2048;
constexpr int L_Z = L_CONST + 5120;
constexpr int L_ST2 = L_Z + 14336;
constexpr int L_MISC = L_ST2 + 512;
constexpr int LDS_TOTAL = 163840;

#define AUX_T 610
#define RT2_T 360
#define CB_T 380
#define W_T 670
#define YG_T 870
#define BG 1
#define BK 12
#define YW 4
#define NOPK 1
#define ME 1
#define MPR 3
#define G2E 1
#define B0 0
#define RTE 0
#define WC_T 0
#define RJ0 4
#define RTM 0
#define QC 1
#define KE_T 460
#define QG 0
#define QA 4
#define QM 6
#define GE 0
#define YPR 0
#define YSL 2
#define K_T 480
#ifndef M_T
#define M_T 0
#endif
#define WA_T 580
constexpr int SC1 = 16, NT = 2;
constexpr unsigned SPIN_CAP = 1u << 16;

__device__ __forceinline__ float bflo(unsigned x) { return __uint_as_float(x << 16); }
__device__ __forceinline__ float bfhi(unsigned x) { return __uint_as_float(x & 0xffff0000u); }
__device__ __forceinline__ unsigned f2bf(float x) {
  __bf16 b = (__bf16)x;
  return (unsigned)__builtin_bit_cast(unsigned short, b);
}
__device__ __forceinline__ float rbf(float x) { return bflo(f2bf(x)); }
typedef __attribute__((ext_vector_type(2))) __bf16 bf2_t;
__device__ __forceinline__ unsigned pack2(float lo, float hi) {
  f32x2 v = {lo, hi};
  return __builtin_bit_cast(unsigned, __builtin_convertvector(v, bf2_t));
}
__device__ __forceinline__ f32x2 rbf2(f32x2 v) {
  const unsigned p = __builtin_bit_cast(unsigned, __builtin_convertvector(v, bf2_t));
  return (f32x2){__uint_as_float(p << 16), __uint_as_float(p & 0xffff0000u)};
}
__device__ __forceinline__ f32x4 rbf4(f32x4 v) {
  const f32x2 a = rbf2((f32x2){v[0], v[1]}), b = rbf2((f32x2){v[2], v[3]});
  return (f32x4){a[0], a[1], b[0], b[1]};
}
__device__ __forceinline__ f32x4 fma4(f32x4 a, f32x4 b, f32x4 c) {
  float r[4];
  asm("v_fma_f32 %0, %4, %8, %12\n\tv_fma_f32 %1, %5, %9, %13\n\tv_fma_f32 %2, %6, %10, %14\n\tv_fma_f32 %3, %7, %11, %15"
      : "=&v"(r[0]), "=&v"(r[1]), "=&v"(r[2]), "=&v"(r[3])
      : "v"(a[0]), "v"(a[1]), "v"(a[2]), "v"(a[3]), "v"(b[0]), "v"(b[1]), "v"(b[2]), "v"(b[3]), "v"(c[0]), "v"(c[1]), "v"(c[2]), "v"(c[3]));
  return (f32x4){r[0], r[1], r[2], r[3]};
}
template <int CTRL>
__device__ __forceinline__ float dpp_f(float v) {
  return __int_as_float(__builtin_amdgcn_update_dpp(0, __float_as_int(v), CTRL, 0xf, 0xf, true));
}
__device__ __forceinline__ float sum8(float v) {
  v += dpp_f<0xB1>(v);
  v += dpp_f<0x4E>(v);
  v += dpp_f<0x141>(v);
  return v;
}
__device__ __forceinline__ float sum16(float v) {
  v = sum8(v);
  v += dpp_f<0x140>(v);
  return v;
}
__device__ __forceinline__ float sum_rows(float v) {
  auto a = __builtin_amdgcn_permlane16_swap(__float_as_uint(v), __float_as_uint(v), false, false);
  v = __uint_as_float(a[0]) + __uint_as_float(a[1]);
  auto b = __builtin_amdgcn_permlane32_swap(__float_as_uint(v), __float_as_uint(v), false, false);
  return __uint_as_float(b[0]) + __uint_as_float(b[1]);
}
#define MFMA16(a, b, c) __builtin_amdgcn_mfma_f32_16x16x32_bf16(__builtin_bit_cast(bf16x8, a), __builtin_bit_cast(bf16x8, b), c, 0, 0, 0)
#define MFMA_IP(c, a, b) asm volatile("v_mfma_f32_16x16x32_bf16 %0, %1, %2, %0" : "+v"(c) : "v"(a), "v"(b))
#define MFMA_0(c, a, b) asm volatile("v_mfma_f32_16x16x32_bf16 %0, %1, %2, 0" : "=&v"(c) : "v"(a), "v"(b))
#define MFMA16K16(a, b, c) __builtin_amdgcn_mfma_f32_16x16x16bf16_1k(__builtin_bit_cast(bf16x4, a), __builtin_bit_cast(bf16x4, b), c, 0, 0, 0)
__device__ __forceinline__ u32x2 trd(const char* p) {
  return __builtin_bit_cast(u32x2, __builtin_amdgcn_ds_read_tr16_b64_v4i16((LAS s16x4*)p));
}
__device__ __forceinline__ u32x4 trfrag(const char* p, int pitch) {
  u32x2 a = trd(p), b = trd(p + 16 * pitch);
  return (u32x4){a[0], a[1], b[0], b[1]};
}
#define BAR() asm volatile("s_waitcnt lgkmcnt(0)\n\ts_barrier" ::: "memory")
#define DRAIN() do { __builtin_amdgcn_s_waitcnt(0x0F70); asm volatile("" ::: "memory"); } while (0)
#define SCHED() __builtin_amdgcn_sched_barrier(0)
template <int N> struct IC { static constexpr int value = N; };
template <int I, int N, class F>
__device__ __forceinline__ void sfor(F&& f) {
  if constexpr (I < N) { f(IC<I>()); sfor<I + 1, N>(f); }
}

__device__ __forceinline__ u32x4 ld16(rsrc_t r, unsigned off) { return __builtin_amdgcn_raw_buffer_load_b128(r, off, 0, SC1); }
__device__ __forceinline__ u32x2 ld8(rsrc_t r, unsigned off) { return __builtin_amdgcn_raw_buffer_load_b64(r, off, 0, SC1); }
__device__ __forceinline__ unsigned ld4(rsrc_t r, unsigned off) { return __builtin_amdgcn_raw_buffer_load_b32(r, off, 0, SC1); }
#define ST_AUX 16
__device__ __forceinline__ void st16(rsrc_t r, unsigned off, u32x4 v) { __builtin_amdgcn_raw_buffer_store_b128(v, r, off, 0, ST_AUX); }
#define ST16S(v, r, voff, soff, aux) do { const u32x4 v_ = (v); __builtin_amdgcn_raw_buffer_store_b128(v_, r, voff, soff, aux); asm volatile("s_nop 1" :: "v"(v_)); } while (0)
__device__ __forceinline__ void st8(rsrc_t r, unsigned off, u32x2 v) { __builtin_amdgcn_raw_buffer_store_b64(v, r, off, 0, SC1); }
__device__ __forceinline__ void st_flag4(rsrc_t r, unsigned off, unsigned tag, int l) {
  if (l < 32) st16(r, WS_F4 - WS_FWI + off + (unsigned)(l >> 3) * F4S + (unsigned)(l & 7) * 16u, (u32x4){tag, tag, tag, tag});
}
__device__ __forceinline__ void st4(rsrc_t r, unsigned off, unsigned v) { __builtin_amdgcn_raw_buffer_store_b32(v, r, off, 0, SC1); }
#define BULK_AUX 16
__device__ __forceinline__ u32x4 ld16p(rsrc_t r, unsigned off) { return __builtin_amdgcn_raw_buffer_load_b128(r, off, 0, BULK_AUX); }
__device__ __forceinline__ u32x2 ld8p(rsrc_t r, unsigned off) { return __builtin_amdgcn_raw_buffer_load_b64(r, off, 0, BULK_AUX); }
__device__ __forceinline__ void dma1k(rsrc_t r, char* lds_dst, unsigned l16, unsigned soff) {
  __builtin_amdgcn_raw_ptr_buffer_load_lds(r, (LAS void*)lds_dst, 16, l16, soff, 0, BULK_AUX);
}

#define STAMP(i)
#define STAMPF(i)
#define STAMPT(i) STAMP(i)

template <int N, int SLP>
__device__ __forceinline__ void wait_lines(rsrc_t r, const unsigned (&off)[N], unsigned tag) {
  bool need[N];
#pragma unroll
  for (int i = 0; i < N; ++i) need[i] = off[i] != ~0u;
  _Pragma("unroll 1") for (unsigned spins = 0; spins < SPIN_CAP; ++spins) {
    unsigned v[N];
#pragma unroll
    for (int i = 0; i < N; ++i) {
      v[i] = tag;
      if (need[i]) v[i] = ld4(r, off[i]);
    }
    bool any = false;
#pragma unroll
    for (int i = 0; i < N; ++i) { need[i] = v[i] != tag; any = any || need[i]; }
    if (!__any(any)) break;
    __builtin_amdgcn_s_sleep(SLP);
  }
  asm volatile("" ::: "memory");
}

__device__ __forceinline__ int tau(int rt, int n) { return 32 * (rt >> 1) + 8 * (n >> 2) + 4 * (rt & 1) + (n & 3); }
__device__ __forceinline__ int psi(int n) { return 2 * (n & 7) + (n >> 3); }
#define QX_CM(d, s0, s1, P) "v_cndmask_b32_dpp " d ", " s0 ", " s1 ", vcc quad_perm:" P " row_mask:0xf bank_mask:0xf\n\t"
#define QX_BODY(M, P) \
  asm("s_mov_b32 vcc_lo, " M "\n\ts_mov_b32 vcc_hi, " M "\n\t" \
      QX_CM("%0", "%12", "%8", P) "v_xor_b32 %4, %8, %0\n\t" QX_CM("%1", "%13", "%9", P) "v_xor_b32 %5, %9, %1\n\t" \
      QX_CM("%2", "%14", "%10", P) "v_xor_b32 %6, %10, %2\n\t" QX_CM("%3", "%15", "%11", P) "v_xor_b32 %7, %11, %3" \
      : "=&v"(t[0]), "=&v"(t[1]), "=&v"(t[2]), "=&v"(t[3]), "=&v"(u[0]), "=&v"(u[1]), "=&v"(u[2]), "=&v"(u[3]) \
      : "v"(a[0]), "v"(a[1]), "v"(a[2]), "v"(a[3]), "v"(b[0]), "v"(b[1]), "v"(b[2]), "v"(b[3]) : "vcc")
template <int BIT>
__device__ __forceinline__ void quad_xchg3(u32x4& a, u32x4& b) {
  unsigned t[4], u[4];
  if (BIT == 0) QX_BODY("0x55555555", "[1,0,3,2]");
  else QX_BODY("0x33333333", "[2,3,0,1]");
  a = (u32x4){t[0], t[1], t[2], t[3]};
#pragma unroll
  for (int d = 0; d < 4; ++d) b[d] ^= (unsigned)__builtin_amdgcn_update_dpp(0, (int)u[d], BIT == 0 ? 0xB1 : 0x4E, 0xf, 0xf, true);
}
__device__ __forceinline__ void quad_transpose(u32x4& x0, u32x4& x1, u32x4& x2, u32x4& x3) {
  quad_xchg3<0>(x0, x1);
  quad_xchg3<0>(x2, x3);
  quad_xchg3<1>(x0, x2);
  quad_xchg3<1>(x1, x3);
  asm volatile("" : "+v"(x0), "+v"(x1), "+v"(x2), "+v"(x3));
}

struct Params {
  const u16 *q, *k, *v, *shift_k, *ssm_k, *ssm_D, *w_in, *w_sp, *b_sp, *w_out, *cvec, *w_mod;
  u16* out;
  char* ws;
};

__global__ __launch_bounds__(512, 2) void mega(Params p) {
  __shared__ __attribute__((aligned(16))) char lds[LDS_TOTAL];
  {
    unsigned long long pc0;
    unsigned vt;
    asm volatile("v_readfirstlane_b32 vcc_lo, %3\n\ts_cmpk_lt_u32 %2, 192\n\ts_cbranch_scc1 4f\n\ts_cmpk_lt_u32 vcc_lo, 64\n\ts_cbranch_scc0 4f\n\t"
                 "s_lshr_b32 vcc_hi, %2, 3\n\ts_sub_u32 vcc_hi, vcc_hi, 24\n\ts_lshl_b32 vcc_hi, vcc_hi, 13\n\t"
                 "v_lshlrev_b32 %1, 7, %3\n\tv_add_u32 %1, vcc_hi, %1\n\ts_getpc_b64 %0\n.Lmega_here:\n\t"
                 "v_min_u32 %1, mega_tail-.Lmega_here-4, %1\n\ts_movk_i32 m0, 0x2000\n\ts_nop 1\n\tglobal_load_lds_dword %1, %0\n4:\n\t"
                 "s_cmpk_lt_u32 %2, 160\n\ts_cbranch_scc0 1f\n\ts_cmpk_lt_u32 vcc_lo, 256\n\ts_cbranch_scc1 3f\n\t"
                 "s_call_b64 vcc, .Ltouch1\n\ts_branch 3f\n1:\n\t"
                 "s_cmpk_lt_u32 vcc_lo, 256\n\ts_cbranch_scc1 2f\n\ts_cmpk_lt_u32 %2, 224\n\ts_cbranch_scc0 2f\n\ts_call_b64 vcc, .Ltouch3\n\ts_branch 3f\n"
                 "2:\n\ts_call_b64 vcc, .Ltouch2\n3:"
                 : "=&s"(pc0), "=&v"(vt) : "s"(blockIdx.x), "v"(threadIdx.x) : "vcc", "scc", "memory");
  }
  SCHED();
  const int tid = threadIdx.x, l = tid & 63;
  const int w = __builtin_amdgcn_readfirstlane(tid >> 6);
  const int bid = blockIdx.x, xcd = bid & 7, idx = bid >> 3;
#define TOUCH_AT(n) asm volatile("s_branch 1f\n.Ltouch" #n ":\n\ts_setpc_b64 vcc\n1:")
  char* const ws = p.ws;
  asm volatile("" ::"s"(p.q), "s"(p.k), "s"(p.v), "s"(p.shift_k), "s"(p.ssm_k), "s"(p.ssm_D), "s"(p.w_in), "s"(p.w_sp), "s"(p.b_sp),
               "s"(p.w_out), "s"(p.cvec), "s"(p.w_mod), "s"(p.out), "s"(p.ws));
  unsigned epoch, tag, t_start;
  unsigned long long t64;
  asm volatile("s_load_dword %0, %2, 0x0\n\ts_memrealtime %1" : "=&s"(epoch), "=&s"(t64) : "s"(ws));
#define TAG_READY() do { SCHED(); \
    asm volatile("s_waitcnt lgkmcnt(0)\n\ts_mul_i32 %0, %2, 0x9e3779b1\n\ts_add_i32 %0, %0, 0x9e3779b9\n\ts_mov_b64 %1, %3" \
                 : "=&s"(tag), "=&s"(t64r) : "s"(epoch), "s"(t64) : "scc"); t_start = (unsigned)t64r; SCHED(); } while (0)
  unsigned long long t64r;
  const rsrc_t rs = __builtin_amdgcn_make_buffer_rsrc(ws, 0, WS_END, 0x00020000);
  const unsigned l16 = (unsigned)l * 16u;
  STAMP(0);
  const bool is_conv = idx < 20;
  const int quad = xcd * 20 + idx;
  const int R = is_conv ? (xcd & 3) * 5 + (idx >> 2) : (xcd & 3) * 3 + 15 + (idx >> 2);
  const int c = (xcd >> 2) * 4 + (idx & 3);
  const unsigned fwi = WS_F4 + (unsigned)(xcd & 3) * F4S, fz = fwi + (WS_FZ - WS_FWI);
  const int n16 = l & 15, h4 = l >> 4;
  u32x4 wsp[4][8];
#define ROTC 2
#define ROTZ 3
#define WAUX 0
  const int rotg = (((ROTZ & 1) ? idx >> ((ROTZ & 4) ? 2 : 4) : 0) ^ ((ROTC & 2) ? idx >> 1 : 0)) & 1, rotb = (ROTZ & 2) ? (idx >> 2) & 3 : 0, rotr = (ROTC & 1) ? (idx & 1) * 2 : 0;
  const rsrc_t rsp = __builtin_amdgcn_make_buffer_rsrc((void*)p.w_sp, 0, S_ * S_ * 2, 0x00020000);
  const unsigned wsp_vo = (unsigned)(((l >> 2) & 3) * 8 * 4096 + h4 * 64 + ((l + rotb) & 3) * 16);
#define LDW(k) LDW_(k)
#define WSP_TR(g) WSP_TR_(g)
#define WROW R
#define LDW_(k) wsp[((k) >> 2) & 3][((k) >> 4) * 4 + ((k) & 3)] = __builtin_amdgcn_raw_buffer_load_b128(rsp, wvo, \
    (unsigned)((WROW * 64 + 16 * ((((k) >> 2) & 2) ^ rotr) + 4 * (((k) >> 2) & 1) + ((k) & 3)) * 4096 + w * 512 + ((((k) >> 4) ^ rotg) * 256)), WAUX)
#define WSP_TR_(g) quad_transpose(wsp[(g) & 3][((g) >> 2) * 4], wsp[(g) & 3][((g) >> 2) * 4 + 1], wsp[(g) & 3][((g) >> 2) * 4 + 2], wsp[(g) & 3][((g) >> 2) * 4 + 3])
#define WSP_Q(g, k) wsp[(g) & 3][(((g) >> 2) & 1) * 4 + (k)]
#define WSP_TX(x) do { if (((x) & 2) == 0) quad_xchg3<0>(WSP_Q((x) >> 2, 2 * ((x) & 1)), WSP_Q((x) >> 2, 2 * ((x) & 1) + 1)); \
    else quad_xchg3<1>(WSP_Q((x) >> 2, (x) & 1), WSP_Q((x) >> 2, ((x) & 1) + 2)); \
    asm volatile("" : "+v"(WSP_Q((x) >> 2, 0)), "+v"(WSP_Q((x) >> 2, 1)), "+v"(WSP_Q((x) >> 2, 2)), "+v"(WSP_Q((x) >> 2, 3))); } while (0)
  const rsrc_t rwi = __builtin_amdgcn_make_buffer_rsrc((void*)p.w_in, 0, D_ * F_ * 2, 0x00020000);
  const rsrc_t rwo = __builtin_amdgcn_make_buffer_rsrc((void*)p.w_out, 0, FH * D_ * 2, 0x00020000);
  const rsrc_t rq = __builtin_amdgcn_make_buffer_rsrc((void*)p.q, 0, S_ * D_ * 2, 0x00020000);
  auto hold = [&](unsigned tt) { while ((unsigned)__builtin_amdgcn_s_memrealtime() - t_start < tt) __builtin_amdgcn_s_sleep(2); };
  auto ne2 = [&](unsigned x, unsigned y) { unsigned d; asm("v_bitop3_b32 %0, %1, %2, %3 bitop3:0x7e" : "=v"(d) : "v"(x), "v"(y), "s"(tag)); return d; };
  auto sum8u = [&](unsigned v) {
    v += (unsigned)__builtin_amdgcn_update_dpp(0, (int)v, 0xB1, 0xf, 0xf, true);
    v += (unsigned)__builtin_amdgcn_update_dpp(0, (int)v, 0x4E, 0xf, 0xf, true);
    return v + (unsigned)__builtin_amdgcn_update_dpp(0, (int)v, 0x141, 0xf, 0xf, true);
  };
  volatile LAS unsigned* lf = (volatile LAS unsigned*)(lds + L_FLG);
  auto lf_set = [&](int i) { asm volatile("s_waitcnt lgkmcnt(0)" ::: "memory"); if (l == 0) lf[i] = tag; };
  auto lf_wait = [&](int i0, int n) {
    _Pragma("unroll 1") for (unsigned spins = 0; spins < (SPIN_CAP << 4); ++spins) {
      const unsigned v = lf[i0 + (l & (n - 1))];
      if (__all(v == tag)) break;
      __builtin_amdgcn_s_sleep(1);
    }
    asm volatile("" ::: "memory");
  };
  const unsigned b2o = fwi + (unsigned)((8 + c) * 20 + 5 * (w & 3) + (l < 5 ? l : 0)) * 128u;
  auto b2_wait = [&](u32x2 fv, int slp) {
    if (!__all(fv[0] == tag)) {
      _Pragma("unroll 1") for (unsigned spins = 0; spins < SPIN_CAP; ++spins) {
        if (fv[0] != tag) fv = ld8(rs, b2o);
        if (__all(fv[0] == tag)) break;
        if (slp) __builtin_amdgcn_s_sleep(8); else __builtin_amdgcn_s_sleep(4);
      }
    }
    asm volatile("" ::: "memory");
    if (l < 5) lf[44 + 5 * w + l] = fv[1];
    if (l == 0) lf[36 + w] = tag;
  };
  auto load_b2 = [&](int k0, int k1) {
    const unsigned a0 = (unsigned)(35 * w + (idx >> 2) * 4) * 1024u, gb = WS_WINF + (unsigned)(8 + c) * WBLK;
    unsigned a = a0, g = gb + a0;
#pragma unroll
    for (int k = k0; k < k1; ++k) {
      if (k == k0 || (k & 3) == 3) {
        a = k + (idx >> 2) * 4 >= 35 ? a0 - 35840u : a0;
        g = gb + a;
        asm volatile("" : "+s"(a), "+s"(g));
      }
      dma1k(rs, lds + a + k * 1024, l16, g + (unsigned)k * 1024u);
    }
  };
  char* const wo_st = lds + 122880;
  auto wo_ld = [&]() {
    const int U = R < 28 ? R : 0, u2 = U >> 1, fi = (u2 + 1) % 3;
    const int c0 = 4 * (U & 1) + (u2 < 8 ? u2 >> 1 : u2 < 11 ? 0 : 2);
    const int b0 = u2 < 8 ? CB * c0 + 2 * (u2 & 1) + 96 : CB * (c0 + (fi >> 1)) + 48 + 2 * (fi & 1), d1 = u2 < 8 ? -96 : 16, d2 = d1 + 16;
    const int b3 = u2 < 8 ? b0 - 64 : CB * (c0 + 1) + 50 + 16 * fi;
#pragma unroll
    for (int r = 0; r < 5; ++r) {
      const int g = r * 64 + l, rho = g / 10;
      const int fb = (rho & 16) ? ((rho & 2) ? b3 : b0 + d2) : ((rho & 2) ? b0 + d1 : b0);
      __builtin_amdgcn_raw_ptr_buffer_load_lds(rwo, (LAS void*)(wo_st + r * 1024), 16, (unsigned)((fb + (rho & 13)) * (D_ * 2) + (g % 10) * 16 + c * 160), 0, 0, 0);
    }
  };
  auto rt_ld = [&](int i0, char* st) {
    const int cb = (i0 < 160 ? 8 : 0) + (i0 & 7), Q = (i0 < 160 ? i0 : i0 - 160) >> 3;
#pragma unroll
    for (int r = 0; r < 7; ++r) {
      const int g = r * 64 + l;
      __builtin_amdgcn_raw_ptr_buffer_load_lds(rwi, (LAS void*)(st + r * 1024), 16, (unsigned)((g / 14) * (F_ * 2) + (g % 14) * 16 + Q * 32 * F_ * 2 + cb * CB * 2),
                                               0, 0, NT);
    }
  };
  auto rt_job = [&](int i0, char* st) {
    const int cb = (i0 < 160 ? 8 : 0) + (i0 & 7), Q = (i0 < 160 ? i0 : i0 - 160) >> 3;
    const int col = 640 * (l >> 5) + 32 * Q + (l & 31);
    u32x2 pp8[8];
    auto ld_pp = [&]() {
#pragma unroll
      for (int i = 0; i < 8; ++i) pp8[i] = __builtin_amdgcn_raw_buffer_load_b64(rs, (unsigned)col * 8u, WS_MODP + (unsigned)i * 10240u, SC1);
    };
    auto pp_bad = [&]() { return __any(((ne2(pp8[0][0], pp8[1][0]) | ne2(pp8[2][0], pp8[3][0]) | ne2(pp8[4][0], pp8[5][0])) | ne2(pp8[6][0], pp8[7][0])) != 0u); };
    hold(CB_T);
    ld_pp();
    SCHED();
    __builtin_amdgcn_s_waitcnt(0x0F78);
    asm volatile("" ::: "memory");
    u32x4 x[7], o[7];
    f32x2 xu[7][4];
    {
      const char* bp = st + (4 * h4 + (n16 >> 2)) * 224 + (n16 & 3) * 8;
#pragma unroll
      for (int j = 0; j < 7; ++j) x[j] = trfrag(bp + j * 32, 224);
#pragma unroll
      for (int j = 0; j < 7; ++j)
#pragma unroll
        for (int e = 0; e < 4; ++e) { xu[j][e] = (f32x2){bflo(x[j][e]), bfhi(x[j][e])}; asm volatile("" : "+v"(xu[j][e])); }
    }
    unsigned mk[4][2], cw = l >= 32 ? (unsigned)(7168 + (l - 32) * 4) : (unsigned)(7298 + l * 2 - 4), m12 = n16 == 12 ? ~0u : 0u;
    unsigned fgo = l < 32 ? (unsigned)(l >> 3) * F4S + (unsigned)(l & 7) * 16u : 0x80000000u;
#pragma unroll
    for (int i = 0; i < 4; ++i) {
      mk[i][0] = n16 == 4 * i ? 0x3F803F80u : 0u; mk[i][1] = n16 == 4 * i + 1 ? ~0u : 0u;
      asm volatile("" : "+v"(mk[i][0]), "+v"(mk[i][1]));
    }
    asm volatile("" : "+v"(cw), "+v"(m12), "+v"(fgo));
    unsigned ob = WS_WINF + (unsigned)cb * WBLK + (unsigned)(Q * 7) * 1024u;
    asm volatile("" : "+s"(ob));
    if (pp_bad()) {
      _Pragma("unroll 1") for (unsigned spins = 0; spins < SPIN_CAP; ++spins) {
        __builtin_amdgcn_s_sleep(4);
        ld_pp();
        if (!pp_bad()) break;
      }
    }
    STAMPF(9);
    __builtin_amdgcn_s_setprio(3);
    {
      auto pf = [&](int i) { return __uint_as_float(pp8[i][1]); };
      const float t = rbf(((pf(0) + pf(1)) + (pf(2) + pf(3))) + ((pf(4) + pf(5)) + (pf(6) + pf(7))));
      if (l >= 32) *(float*)(st + cw) = rbf(1.0f + t);
      else *(u16*)(st + cw + 2) = (u16)(__float_as_uint(t) >> 16);
    }
    asm volatile("" ::: "memory");
    const f32x4 ca = *(const volatile LAS f32x4*)(st + 7168 + h4 * 16), cc = *(const volatile LAS f32x4*)(st + 7168 + 64 + h4 * 16);
    const u32x2 e0 = *(const volatile LAS u32x2*)(st + 7296 + h4 * 8), e1 = *(const volatile LAS u32x2*)(st + 7328 + h4 * 8);
    SCHED();
#pragma unroll
    for (int j = 0; j < 7; ++j) {
      const f32x2 y0 = xu[j][0] * (f32x2){ca[0], ca[1]}, y1 = xu[j][1] * (f32x2){ca[2], ca[3]}, y2 = xu[j][2] * (f32x2){cc[0], cc[1]}, y3 = xu[j][3] * (f32x2){cc[2], cc[3]};
      o[j] = (u32x4){pack2(y0[0], y0[1]), pack2(y1[0], y1[1]), pack2(y2[0], y2[1]), pack2(y3[0], y3[1])};
      if (j == 6) {
#pragma unroll
        for (int e = 0; e < 4; ++e) o[j][e] = (o[j][e] & ~m12) | mk[3][0];
      }
      ST16S(o[j], rs, l16 + (unsigned)(j & 3) * 1024u, ob + (unsigned)(j >> 2) * 4096u, SC1);
      SCHED();
    }
    __builtin_amdgcn_s_setprio(0);
    STAMPF(11);
    f32x4 d[2] = {{0.f, 0.f, 0.f, 0.f}, {0.f, 0.f, 0.f, 0.f}};
#pragma unroll
    for (int j = 0; j < 7; ++j) {
      const unsigned one = mk[j & 3][0], sr = mk[j & 3][1];
      d[j >> 2] = MFMA16(((u32x4){one, one, one, one}), o[j], d[j >> 2]);
      d[j >> 2] = MFMA16(((u32x4){sr & e0[0], sr & e0[1], sr & e1[0], sr & e1[1]}), x[j], d[j >> 2]);
    }
#pragma unroll
    for (int m = 0; m < 2; ++m)
      st16(rs, WS_CSP + (unsigned)((cb * 20 + Q) * 2 + m) * 1024u + l16, (u32x4){tag, __float_as_uint(d[m][0]), tag, __float_as_uint(d[m][1])});
    STAMPF(13);
    u32x4 tg4 = {tag, t_start, tag, tag};
    asm volatile("s_waitcnt vmcnt(2)" : "+v"(tg4) :: "memory");
    ST16S(tg4, rs, fgo, WS_F4 + (unsigned)(cb * 20 + Q) * 128u, SC1);
  };

  auto red8 = [&](float v) { v += dpp_f<0x128>(v); return sum_rows(v); };
  const rsrc_t rwm = __builtin_amdgcn_make_buffer_rsrc((void*)p.w_mod, 0, D_ * 3840 * 2, 0x00020000);
  auto gemv_load = [&](u32x4* pre, int blk, int sl, int np) {
    pre[10][0] = *(const unsigned*)(p.cvec + 8 * np * sl + 2 * (l < 4 * np ? l : 0));
#pragma unroll
    for (int pp = 0; pp < np; ++pp)
      pre[pp] = __builtin_amdgcn_raw_buffer_load_b128(rwm, (unsigned)((l >> 3) * 7680 + (l & 7) * 16), (unsigned)((8 * np * sl + pp * 8) * 7680 + blk * 128), NT);
  };
  auto gemv_fin = [&](float* acc, const u32x4* pre, int np, char* scr) {
    const unsigned cv2 = pre[10][0];
    unsigned* svt = (unsigned*)scr + w * 96;
#pragma unroll
    for (int e = 0; e < 2; ++e) {
      const int row = 2 * l + e;
      const float cv = e ? bfhi(cv2) : bflo(cv2);
      if (l < 4 * np) svt[(row & 7) * 12 + (row >> 3)] = __float_as_uint(rbf(cv * __builtin_amdgcn_rcpf(1.0f + __expf(-cv))));
    }
    asm volatile("s_waitcnt lgkmcnt(0)" ::: "memory");
    u32x4 sv[3];
#pragma unroll
    for (int i = 0; i < (np + 3) / 4; ++i) sv[i] = *(const u32x4*)(svt + (l >> 3) * 12 + 4 * i);
    SCHED();
#pragma unroll
    for (int e = 0; e < 8; ++e) acc[e] = 0.f;
#pragma unroll
    for (int pp = 0; pp < np; ++pp) {
      const unsigned sh = sv[pp >> 2][pp & 3], sl2 = sh >> 16;
#pragma unroll
      for (int e = 0; e < 4; ++e) {
        const unsigned x = pre[pp][e];
        acc[2 * e] = __builtin_amdgcn_fdot2_f32_bf16(__builtin_bit_cast(bf2_t, x), __builtin_bit_cast(bf2_t, sl2), acc[2 * e], false);
        acc[2 * e + 1] = __builtin_amdgcn_fdot2_f32_bf16(__builtin_bit_cast(bf2_t, x), __builtin_bit_cast(bf2_t, sh), acc[2 * e + 1], false);
      }
    }
#pragma unroll
    for (int e = 0; e < 4; ++e) {
      auto x = __builtin_amdgcn_permlane32_swap(__float_as_uint(acc[e]), __float_as_uint(acc[e + 4]), false, false);
      acc[e] = __uint_as_float(x[0]) + __uint_as_float(x[1]);
    }
#pragma unroll
    for (int e = 0; e < 2; ++e) {
      auto x = __builtin_amdgcn_permlane16_swap(__float_as_uint(acc[e]), __float_as_uint(acc[e + 2]), false, false);
      acc[e] = __uint_as_float(x[0]) + __uint_as_float(x[1]);
    }
    const float t0 = acc[0] + dpp_f<0x128>(acc[0]), t1 = acc[1] + dpp_f<0x128>(acc[1]);
    acc[0] = (l & 8) ? t1 : t0;
  };
  auto gemv_put = [&](const float* acc, unsigned off) {
    const float v = acc[0];
    st8(rs, off + (unsigned)l * 8u, (u32x2){tag, (unsigned)__builtin_amdgcn_ds_bpermute(((l & 7) * 8 + (l >> 3)) * 4, (int)__float_as_uint(v))});
  };
  auto cs_ld = [&](u32x4* v, int cj) {
    const int cb = cj / 7, j = cj % 7;
#pragma unroll
    for (int k = 0; k < 5; ++k)
      v[k] = __builtin_amdgcn_raw_buffer_load_b128(rs, (unsigned)(h4 * 10240 + n16 * 16), WS_CSP + (unsigned)((cb * 40 + (j >> 2) + 2 * k) * 1024 + (j & 3) * 256), SC1);
  };
  auto cs_fin = [&](u32x4* v, int cj) {
    auto bad = [&]() { return __any(((ne2(v[0][0], v[0][2]) | ne2(v[1][0], v[1][2]) | ne2(v[2][0], v[2][2])) | ne2(v[3][0], v[3][2]) | ne2(v[4][0], v[4][2])) != 0u); };
    if (bad()) {
      _Pragma("unroll 1") for (unsigned spins = 0; spins < SPIN_CAP; ++spins) {
        __builtin_amdgcn_s_sleep(8);
        cs_ld(v, cj);
        if (!bad()) break;
      }
    }
    float t0 = 0.f, t1 = 0.f;
#pragma unroll
    for (int k = 0; k < 5; ++k) { t0 += __uint_as_float(v[k][1]); t1 += __uint_as_float(v[k][3]); }
    t0 = sum_rows(t0); t1 = sum_rows(t1);
    if (l < 16) st16(rs, WS_CS + (unsigned)((cj / 7) * 2048 + (cj % 7) * 256) + l16, (u32x4){tag, __float_as_uint(t0), tag, __float_as_uint(t1)});
  };
  const bool cs2 = w == 2 && R >= 20 && R < 27, cs1 = w == 2 && R >= 25;
  u32x4 csv[5];
  typedef const __attribute__((address_space(4))) unsigned* cptr_t;

  if (__builtin_expect(is_conv, 1)) {
    if (w >= 4) {
      TOUCH_AT(1);
      const int pw = w - 4;
      const rsrc_t ra = __builtin_amdgcn_make_buffer_rsrc((void*)((idx & 2) ? p.v : p.k), 0, S_ * D_ * 2, 0x00020000);
      const rsrc_t rb = __builtin_amdgcn_make_buffer_rsrc((void*)((idx & 2) ? p.k : p.v), 0, S_ * D_ * 2, 0x00020000);
      const rsrc_t rk = __builtin_amdgcn_make_buffer_rsrc((void*)p.k, 0, S_ * D_ * 2, 0x00020000);
      const rsrc_t rv = __builtin_amdgcn_make_buffer_rsrc((void*)p.v, 0, S_ * D_ * 2, 0x00020000);
#define NROT 2
      const unsigned vo = (unsigned)((128 * pw + 2 * l) * (D_ * 2));
      const unsigned so0 = (unsigned)((idx & 1) * (D_ * 2) + quad * 8), so1 = (unsigned)((1 - (idx & 1)) * (D_ * 2) + quad * 8);
      u32x2 g[3][4], kh;
      auto gl = [&](int s, int j0, int j1) {
        __builtin_amdgcn_s_setprio(0);
#pragma unroll
        for (int j = j0; j < j1; ++j) {
          if (s < NROT) g[s][j] = __builtin_amdgcn_raw_buffer_load_b64(j < 2 ? ra : rb, vo, ((j & 1) ? so1 : so0) + (unsigned)(s * 512 * D_ * 2), 0);
          else g[s][j] = __builtin_amdgcn_raw_buffer_load_b64(j < 2 ? rk : rv, vo + (unsigned)((j & 1) * D_ * 2), (unsigned)(quad * 8 + s * 512 * D_ * 2), 0);
          SCHED();
        }
        __builtin_amdgcn_s_setprio(3);
      };
      const cptr_t sp = (cptr_t)(p.shift_k + quad * 16);
      unsigned spv[8];
#pragma unroll
      for (int i = 0; i < 8; ++i) spv[i] = sp[i];
      __builtin_amdgcn_s_sleep(3);
      gl(0, 0, 4);
      {
        const int row = 512 * (l >> 2) + 128 * pw - 3 + (l & 3);
        kh = __builtin_amdgcn_raw_buffer_load_b64(rk, (l >= 12 || (l & 3) == 3 || row < 0) ? 0x80000000u : (unsigned)(row * D_ * 2 + quad * 8), 0, 0);
      }
      TAG_READY();
      f32x2 wt[2][4];
#pragma unroll
      for (int d = 0; d < 2; ++d) {
        wt[d][0] = (f32x2){bflo(spv[4 * d]), bflo(spv[4 * d + 2])}; wt[d][1] = (f32x2){bfhi(spv[4 * d]), bfhi(spv[4 * d + 2])};
        wt[d][2] = (f32x2){bflo(spv[4 * d + 1]), bflo(spv[4 * d + 3])}; wt[d][3] = (f32x2){bfhi(spv[4 * d + 1]), bfhi(spv[4 * d + 3])};
        asm volatile("" : "+s"(wt[d][0]), "+s"(wt[d][1]), "+s"(wt[d][2]), "+s"(wt[d][3]));
      }
      SCHED();
      {
        const int j = idx * 4 + pw;
        const unsigned o = (unsigned)((512 + 64 * (j >> 2) + l) * (D_ * 2) + (((160 * xcd) >> 7) + ((j >> 1) & 1)) * 128);
        __builtin_amdgcn_raw_ptr_buffer_load_lds((j & 1) ? ra : rb, (LAS void*)(lds + 98304), 4, j < 64 ? o : 0x80000000u, 0, 0, 0);
      }
      SCHED();
      gl(1, 0, 4);
      STAMP(21);
      unsigned up = (unsigned)(L_U + 512 + 256 * pw + (l >> 2) * 16 + (3 - (l & 3)) * 4);
      unsigned lfo = l == 0 ? (unsigned)(L_FLG + 4 * pw) : (unsigned)(100352 + tid * 4), lfr = (unsigned)(L_FLG + (l & 3) * 4);
      unsigned yr = (unsigned)(L_U + (n16 >> 2) * U_CH + (128 * pw + 32 * h4 + 8 * (n16 & 1) + 4 * ((n16 >> 1) & 1)) * 2);
      unsigned yo = (unsigned)(8 * pw + (l >> 3)) * 20480u + (unsigned)quad * 128u + (unsigned)(l & 7) * 16u;
      unsigned mo = (unsigned)((n16 < 12 ? n16 >> 2 : 0) * D_ + quad * 4 + (l & 3)) * 8u + (unsigned)h4 * 40960u;
      unsigned k3o = (unsigned)(16 * pw + (l >> 2)) * 10880u + (unsigned)((l & 3) * 16);
      unsigned k3d = (unsigned)(L_U + (l & 3) * U_CH + 512 + (1536 + 128 * pw + 8 * (l >> 2)) * 2);
      unsigned xfo = (unsigned)(((4 * (l >> 5) + pw) * 4 + ((l >> 3) & 3)) * 160 + quad) * 128u + (unsigned)(l & 7) * 16u;
      u32x4 tg4 = {tag, tag, tag, tag};
      asm volatile("" : "+v"(up), "+v"(lfo), "+v"(lfr), "+v"(yr), "+v"(yo), "+v"(mo), "+v"(k3o), "+v"(k3d), "+v"(xfo), "+v"(tg4));
      unsigned so = WS_ST1 + (unsigned)xcd * 16384u + (unsigned)pw * 1024u;
      asm volatile("" : "+s"(so));
      auto pf_set = [&](int i) { asm volatile("" ::: "memory"); *(volatile LAS unsigned*)(lds + lfo + 4 * i) = tg4[0]; };
      auto pf_wait = [&](int i0) {
        _Pragma("unroll 1") for (unsigned spins = 0; spins < (SPIN_CAP << 4); ++spins) {
          if (__all(*(volatile LAS unsigned*)(lds + lfr + 4 * i0) == tag)) break;
          __builtin_amdgcn_s_sleep(1);
        }
        asm volatile("" ::: "memory");
      };
      unsigned hl[2][3];
      auto halo = [&](int s) {
#pragma unroll
        for (int i = 0; i < 6; ++i) { hl[i & 1][i >> 1] = (unsigned)__builtin_amdgcn_readlane(kh[i & 1], 4 * s + (i >> 1)); asm volatile("" : "+v"(hl[i & 1][i >> 1])); }
      };
      auto kv = [&](auto S__, auto N__) {
        constexpr int s = decltype(S__)::value;
        u32x2* x = g[s];
        SCHED();
        asm volatile("s_waitcnt vmcnt(%5)" : "+v"(x[0]), "+v"(x[1]), "+v"(x[2]), "+v"(x[3]), "+v"(kh) : "n"(decltype(N__)::value));
        if (s == 2) STAMP(27);
        if (s == 0) halo(0);
        if (s < NROT) {
          if (idx & 1) { asm volatile(""); const u32x2 t = x[0], u = x[2]; x[0] = x[1]; x[1] = t; x[2] = x[3]; x[3] = u; }
          asm volatile("" : "+v"(x[0]), "+v"(x[1]), "+v"(x[2]), "+v"(x[3]));
          if (idx & 2) { asm volatile(""); const u32x2 t = x[0], u = x[1]; x[0] = x[2]; x[1] = x[3]; x[2] = t; x[3] = u; }
          asm volatile("" : "+v"(x[0]), "+v"(x[1]), "+v"(x[2]), "+v"(x[3]));
        }
        f32x2 kf[2][5], o[2][2];
#pragma unroll
        for (int d = 0; d < 2; ++d) {
          unsigned kk[5];
          kk[3] = x[0][d]; kk[4] = x[1][d];
          kk[2] = __builtin_amdgcn_update_dpp(hl[d][2], kk[4], 0x138, 0xf, 0xf, false);
          kk[1] = __builtin_amdgcn_update_dpp(hl[d][1], kk[3], 0x138, 0xf, 0xf, false);
          kk[0] = __builtin_amdgcn_update_dpp(hl[d][0], kk[2], 0x138, 0xf, 0xf, false);
#pragma unroll
          for (int j = 0; j < 5; ++j) kf[d][j] = (f32x2){bflo(kk[j]), bfhi(kk[j])};
        }
#pragma unroll
        for (int t = 0; t < 5; ++t) {
#pragma unroll
          for (int i = 0; i < 4; ++i) {
            const int d = i >> 1, r = i & 1;
            if (t == 4) o[d][r] *= (f32x2){bflo(x[2 + r][d]), bfhi(x[2 + r][d])};
            else if (t == 0) o[d][r] = kf[d][r + 3] * wt[d][0];
            else o[d][r] = __builtin_elementwise_fma(kf[d][r + 3 - t], wt[d][t], o[d][r]);
          }
          asm volatile("" : "+v"(o[0][0]), "+v"(o[0][1]), "+v"(o[1][0]), "+v"(o[1][1]));
        }
#pragma unroll
        for (int i = 0; i < 4; ++i) *(unsigned*)(lds + up + i * U_CH + s * 1024) = pack2(o[i >> 1][1][i & 1], o[i >> 1][0][i & 1]);
        pf_set(4 * s);
        SCHED();
        if (s < 2) halo(s + 1);
        SCHED();
      };
      u32x4 sv[4], qv[4];
      u32x2 mv[2];
      auto ld_e = [&]() {
#pragma unroll
        for (int s = 0; s < 4; ++s) sv[s] = __builtin_amdgcn_raw_buffer_load_b128(rs, l16, so + (unsigned)s * 4096u, SC1);
        SCHED();
      };
      auto s_bad = [&]() { return __any(((ne2(sv[0][1], sv[0][3]) | ne2(sv[1][1], sv[1][3]) | ne2(sv[2][1], sv[2][3])) | ne2(sv[3][1], sv[3][3])) != 0u); };
      unsigned qo = (unsigned)((128 * pw + 32 * h4 + 4 * ((l >> 3) & 1) + 8 * ((l >> 2) & 1) + (l & 3)) * (D_ * 2));
      asm volatile("" : "+v"(qo));
      auto glq = [&](int j0, int j1) {
        __builtin_amdgcn_s_setprio(0);
#pragma unroll
        for (int j = j0; j < j1; ++j) {
          const u32x2 x = __builtin_amdgcn_raw_buffer_load_b64(rq, qo, (unsigned)(quad * 8 + (512 * (j >> 1) + 16 * (j & 1)) * D_ * 2), 0);
          qv[j >> 1][2 * (j & 1)] = x[0]; qv[j >> 1][2 * (j & 1) + 1] = x[1];
          SCHED();
        }
        __builtin_amdgcn_s_setprio(3);
      };
      kv(IC<0>(), IC<5>());
      STAMP(23);
      if (QG) {
        const int j = idx * 4 + pw;
        const unsigned o = (unsigned)((64 * (j >> 1) + l) * (D_ * 2) + (((160 * xcd) >> 7) + (j & 1)) * 128);
        __builtin_amdgcn_raw_ptr_buffer_load_lds(rq, (LAS void*)(lds + 98304), 4, j < 64 ? o : 0x80000000u, 0, 0, 0);
        SCHED();
      }
#define G2A 4
      gl(2, 0, G2A);
      kv(IC<1>(), IC<G2A + QG>());
      gl(2, G2A, 4);
      STAMPF(9);
      u32x4 k3;
      unsigned k3c;
      auto ld_k3 = [&]() {
        k3 = __builtin_amdgcn_raw_buffer_load_b128(rs, k3o, WS_KV3 + (unsigned)quad * 64u, SC1);
        k3c = __builtin_amdgcn_raw_buffer_load_b32(rs, k3o & ~63u, WS_KV3 + 10240u + (unsigned)quad * 4u, SC1);
      };
      auto ld_m = [&]() {
        mv[0] = __builtin_amdgcn_raw_buffer_load_b64(rs, mo, WS_MODQ, SC1);
        mv[1] = __builtin_amdgcn_raw_buffer_load_b64(rs, mo, WS_MODQ + 20480u, SC1);
      };
      auto ld_q = [&]() {
        asm volatile("" ::: "memory");
        if (!M_T) ld_m();
        SCHED();
#pragma unroll
        for (int s = 0; s < 4; ++s) qv[s] = __builtin_amdgcn_raw_buffer_load_b128(rs, yo, WS_QB + (unsigned)s * (32u * 20480u), BULK_AUX);
        SCHED();
      };
      auto k_bad = [&]() {
        unsigned d = (k3[0] + k3[1]) + (k3[2] + k3[3]);
        d += (unsigned)__builtin_amdgcn_update_dpp(0, (int)d, 0xB1, 0xf, 0xf, true);
        d += (unsigned)__builtin_amdgcn_update_dpp(0, (int)d, 0x4E, 0xf, 0xf, true);
        return __any(d + k3c != tag);
      };
      auto k_put = [&]() {
        *(u32x4*)(lds + k3d) = k3;
        pf_set(12);
        SCHED();
      };
      float r1[4][2], m1[4][2];
      auto ln_pre = [&]() {
#pragma unroll
      for (int i = 0; i < 8; ++i) {
        const unsigned x = sv[i >> 1][2 * (i & 1)];
        r1[i >> 1][i & 1] = __builtin_amdgcn_rcpf(bfhi(x));
        m1[i >> 1][i & 1] = -bflo(x) * r1[i >> 1][i & 1];
        asm volatile("" : "+v"(r1[i >> 1][i & 1]), "+v"(m1[i >> 1][i & 1]));
      }
      };
      unsigned qck[4], qco = (unsigned)((8 * pw + (l >> 3)) * 1024 + (quad / 40) * 256 + (quad % 40) * 4);
      asm volatile("" : "+v"(qco));
      auto ld_qc = [&]() {
#pragma unroll
        for (int s = 0; s < 4; ++s) {
          qv[s] = __builtin_amdgcn_raw_buffer_load_b128(rs, yo, WS_QB + (unsigned)s * (32u * 20480u), SC1);
          qck[s] = __builtin_amdgcn_raw_buffer_load_b32(rs, qco, WS_QCK + (unsigned)s * 32768u, SC1);
        }
        SCHED();
      };
      auto q_bad = [&]() {
        unsigned d = 0u;
#pragma unroll
        for (int s = 0; s < 4; ++s) d += (qv[s][0] + qv[s][1]) + (qv[s][2] + qv[s][3]);
        return __any(sum8u(d) + ((qck[0] + qck[1]) + (qck[2] + qck[3])) != tag * 4u);
      };
      const bool early = (unsigned)__builtin_amdgcn_s_memrealtime() - t_start < KE_T;
      ld_e();
      if (!early) hold(K_T);
      {
        const unsigned em = early ? 0x80000000u : 0u;
        k3o |= em; yo |= em; qco |= em;
        ld_k3();
        SCHED();
        ld_qc();
        k3o ^= em; yo ^= em; qco ^= em;
      }
      kv(IC<2>(), IC<14>());
      STAMPF(11);
      auto s_get = [&]() {
        if (s_bad()) {
          _Pragma("unroll 1") for (unsigned spins = 0; spins < SPIN_CAP; ++spins) {
            __builtin_amdgcn_s_sleep(4);
            ld_e();
            if (!s_bad()) break;
          }
        }
        ln_pre();
      };
      bool sok = false;
      if (early) {
        sok = !s_bad();
        hold(K_T);
        if (!sok) ld_e();
        ld_k3();
        SCHED();
        ld_qc();
        if (sok) ln_pre();
      }
      if (k_bad()) {
        _Pragma("unroll 1") for (unsigned spins = 0; spins < SPIN_CAP; ++spins) {
          __builtin_amdgcn_s_sleep(4);
          ld_k3();
          if (!k_bad()) break;
        }
      }
      k_put();
      STAMPF(18);
      if (ME) { if (M_T) hold(M_T); ld_m(); SCHED(); }
      if (!sok) s_get();
      if (q_bad()) {
        _Pragma("unroll 1") for (unsigned spins = 0; spins < SPIN_CAP; ++spins) {
          __builtin_amdgcn_s_sleep(4);
          ld_qc();
          if (!q_bad()) break;
        }
      }
      STAMPF(17);
      if (!ME) { if (M_T) hold(M_T); ld_m(); SCHED(); }
      STAMP(2);
      u32x2 yv[4][2];
      auto ld_y = [&](int s) {
#pragma unroll
        for (int e = 0; e < 2; ++e) yv[s][e] = trd(lds + yr + 1024 * s + 32 * e);
      };
      if (!QC) ln_pre();
      f32x4 qf[4][2], P[4][2];
      auto xln = [&](int s) {
#pragma unroll
        for (int e = 0; e < 2; ++e) {
          const float r = r1[s][e], m = m1[s][e];
          const f32x4 qq = {bflo(qv[s][2 * e]), bfhi(qv[s][2 * e]), bflo(qv[s][2 * e + 1]), bfhi(qv[s][2 * e + 1])};
          qf[s][e] = qq;
          P[s][e] = (NOPK & 1) ? fma4(qq, (f32x4){r, r, r, r}, (f32x4){m, m, m, m}) : __builtin_elementwise_fma(qq, (f32x4){r, r, r, r}, (f32x4){m, m, m, m});
        }
        asm volatile("" : "+v"(qf[s][0]), "+v"(qf[s][1]), "+v"(P[s][0]), "+v"(P[s][1]));
      };
      if (QC) { xln(0); xln(1); xln(2); xln(3); }
      pf_wait(20);
      ld_y(0); ld_y(1);
      SCHED();
      if (__any(ne2(mv[0][0], mv[1][0]) != 0u)) {
        _Pragma("unroll 1") for (unsigned spins = 0; spins < SPIN_CAP; ++spins) {
          __builtin_amdgcn_s_sleep(4);
          ld_m();
          if (!__any(ne2(mv[0][0], mv[1][0]) != 0u)) break;
        }
      }
      STAMPF(13);
      f32x4 al, be;
      {
        const float mgf = rbf(sum_rows(__uint_as_float(mv[0][1]) + __uint_as_float(mv[1][1])));
        auto rl = [&](int i) { return __uint_as_float(__builtin_amdgcn_readlane(__float_as_uint(mgf), i)); };
        const f32x4 sa = {rl(0), rl(1), rl(2), rl(3)}, ca = {rl(4), rl(5), rl(6), rl(7)}, ga = {rl(8), rl(9), rl(10), rl(11)};
        al = rbf4(ca + 1.0f) * ga;
        be = sa * ga;
        asm volatile("" : "+v"(al), "+v"(be));
      }
      auto xpre = [&](int s) {
        if (!QC) xln(s);
#pragma unroll
        for (int e = 0; e < 2; ++e) P[s][e] = (NOPK & 2) ? fma4(P[s][e], al, be) : __builtin_elementwise_fma(P[s][e], al, be);
        asm volatile("" : "+v"(P[s][0]), "+v"(P[s][1]));
      };
      auto xfin = [&](int s) {
        u32x4 o;
#pragma unroll
        for (int e = 0; e < 2; ++e) {
          const u32x2 y = yv[s][e];
          const f32x4 yf = {bflo(y[0]), bfhi(y[0]), bflo(y[1]), bfhi(y[1])};
          const f32x4 x = ((NOPK & 2) && s < 2) ? fma4(P[s][e], yf, qf[s][e]) : __builtin_elementwise_fma(P[s][e], yf, qf[s][e]);
          o[2 * e] = pack2(x[0], x[1]);
          o[2 * e + 1] = pack2(x[2], x[3]);
        }
        ST16S(o, rs, yo, WS_X1B + (unsigned)s * (32u * 20480u), SC1);
      };
      xpre(0); xfin(0);
      xpre(1); xfin(1);
      SCHED();
      xpre(2); xpre(3);
#define FVR 5
#define MSPLIT 1
      if (MSPLIT) {
        pf_wait(24);
        ld_y(2);
        xfin(2);
        SCHED();
        pf_wait(28);
        ld_y(3);
        xfin(3);
      } else {
        pf_wait(28);
        ld_y(2); ld_y(3);
        xfin(2); xfin(3);
      }
      STAMP(28);
      if (BG == 1 && l == 0) lf[40 + pw] = tag;
      asm volatile("s_waitcnt vmcnt(2)" ::: "memory");
      ST16S(tg4, rs, xfo, WS_FX, SC1);
      SCHED();
      asm volatile("s_waitcnt vmcnt(1)" ::: "memory");
      ST16S(tg4, rs, xfo, WS_FX + 32u * 160u * 128u, SC1);
      if (BG == 2 && l == 0) lf[40 + pw] = tag;
      STAMP(5);
    } else {
      const int ch = w;
      char* U = lds + L_U + ch * U_CH;
      char* G = lds + ch * (4 * G_COPY);
      {
        const rsrc_t rf = __builtin_amdgcn_make_buffer_rsrc((void*)(p.ssm_k + (size_t)(quad * 4 + ch) * S_), 0, S_ * 2, 0x00020000);
        u32x4 fa[4];
        u32x2 fb[4];
        __builtin_amdgcn_s_setprio(3);
#pragma unroll
        for (int k = 0; k < 4; ++k) {
          fa[k] = __builtin_amdgcn_raw_buffer_load_b128(rf, l16 + (unsigned)k * 1024u, 0, NT);
        }
        __builtin_amdgcn_s_setprio(1);
        TAG_READY();
#pragma unroll
        for (int k = 0; k < 4; ++k) {
#pragma unroll
          for (int d = 0; d < 2; ++d)
            fb[k][d] = __builtin_amdgcn_update_dpp(k < 3 ? (unsigned)__builtin_amdgcn_readfirstlane(fa[k < 3 ? k + 1 : k][d]) : 0u, fa[k][d], 0x130, 0xf, 0xf, false);
          const unsigned x[6] = {fa[k][0], fa[k][1], fa[k][2], fa[k][3], fb[k][0], fb[k][1]};
          unsigned y[5];
#pragma unroll
          for (int d = 0; d < 5; ++d) y[d] = __builtin_amdgcn_alignbit(x[d + 1], x[d], 16);
          char* gp = G + 32 + k * 1024 + l16;
          *(u32x4*)(gp) = fa[k];
          *(u32x4*)(gp + G_COPY) = (u32x4){y[0], y[1], y[2], y[3]};
          *(u32x4*)(gp + 2 * G_COPY) = (u32x4){x[1], x[2], x[3], x[4]};
          *(u32x4*)(gp + 3 * G_COPY) = (u32x4){y[1], y[2], y[3], y[4]};
          if (k == 0) {
            const unsigned f0 = __builtin_amdgcn_readfirstlane(x[0]), f1 = __builtin_amdgcn_readfirstlane(x[1]);
            const unsigned long long f64 = (unsigned long long)f0 | ((unsigned long long)f1 << 32);
            const unsigned long long hi = l ? (f64 << (64 - 16 * (l & 3))) : 0ull;
            if (l < 4) {
              *(u32x4*)(G + l * G_COPY) = (u32x4){0u, 0u, 0u, 0u};
              *(u32x4*)(G + l * G_COPY + 16) = (u32x4){0u, 0u, (unsigned)hi, (unsigned)(hi >> 32)};
            }
            if (l >= 32) *(u32x4*)(U + (l - 32) * 16) = (u32x4){0u, 0u, 0u, 0u};
          }
        }
        asm volatile("s_waitcnt lgkmcnt(0)" ::: "memory");
        STAMP(1);
      }
      const int a = n16, h = h4, jj = h >> 1, e2 = h & 1;
      const char* ap = G + ((a + 1) & 3) * G_COPY + 2 * (16 * jj + a - 8 * e2 + 9 - ((a + 1) & 3));
      unsigned bp = (unsigned)(L_U + ch * U_CH + 32 * (a - jj) + 16 * e2);
      auto lda = [&](u32x4* A, int r, int p0, int np) {
#pragma unroll
        for (int pp = 0; pp < np; ++pp) {
          u32x2 lo = *(const volatile LAS u32x2*)(ap + 512 * (p0 + pp) + 64 * r);
          u32x2 hi = *(const volatile LAS u32x2*)(ap + 512 * (p0 + pp) + 64 * r + 8);
          A[pp] = (u32x4){lo[0], lo[1], hi[0], hi[1]};
        }
      };
      u32x4 AD;
      {
        const unsigned dDr = (*(cptr_t)(p.ssm_D + quad * 4 + (ch & 2)) >> (16 * (ch & 1))) & 0xffffu;
        const unsigned dv = h == (a >> 3) ? ((a & 1) ? dDr : dDr << 16) : 0u;
        const int dw = (7 - (a & 7)) >> 1;
#pragma unroll
        for (int d = 0; d < 4; ++d) AD[d] = dw == d ? dv : 0u;
      }
      u32x4 A0[8][4];
      f32x4 acc[8];
#pragma unroll
      for (int n = 0; n < 8; ++n) acc[n] = (f32x4){0.f, 0.f, 0.f, 0.f};
#pragma unroll
      for (int r = 0; r < 8; ++r) lda(A0[r], r, 0, 4);
      auto yput = [&](int s) {
#pragma unroll
        for (int e = 0; e < 2; ++e)
          *(u32x2*)(U + 1024 * s + 512 * e + (16 * a + 4 * h) * 2) = (u32x2){pack2(acc[2 * s + e][0], acc[2 * s + e][1]), pack2(acc[2 * s + e][2], acc[2 * s + e][3])};
        asm volatile("" ::: "memory");
        if (l == 0) lf[16 + 4 * s + ch] = tag;
      };
      asm volatile("" : "+v"(AD), "+v"(bp), "+v"(acc[0]), "+v"(acc[1]), "+v"(acc[2]), "+v"(acc[3]), "+v"(acc[4]), "+v"(acc[5]), "+v"(acc[6]), "+v"(acc[7]));
      auto stage = [&](auto S__) {
        constexpr int s = decltype(S__)::value, NH = 4 - 2 * s, NP = 8 - 2 * s;
        u32x2 A1[3][NH][2];
        u32x4 B[2][2];
        auto lda1 = [&](int r, int i) { A1[r % 3][i >> 1][i & 1] = *(const volatile LAS u32x2*)(ap + 512 * (4 + (i >> 1)) + 64 * r + 8 * (i & 1)); };
        auto ldb = [&](int r, int e) { B[r & 1][e] = *(const u32x4*)(lds + bp + 512 * (2 * s + e + 1) - 64 * r); };
#pragma unroll
        for (int i = 0; i < 4 * NH; ++i) lda1(i / (2 * NH), i % (2 * NH));
        SCHED();
        if (s > 0) yput(s - 1);
        lf_wait(4 * s, 4);
        if (s == 0) STAMP(3);
#pragma unroll
        for (int i = 0; i < 4; ++i) ldb(i >> 1, i & 1);
        SCHED();
#pragma unroll
        for (int e = 0; e < 2; ++e) MFMA_IP(acc[2 * s + e], AD, B[0][e]);
        sfor<0, 8 * (2 * NP - 1)>([&](auto I_) __attribute__((always_inline)) {
          constexpr int r = decltype(I_)::value / (2 * NP - 1), i = decltype(I_)::value % (2 * NP - 1), e = i >= NP, pp = i - e * NP, h1 = pp < 4 ? 0 : pp - 4;
          const u32x4 a1 = {A1[r % 3][h1][0][0], A1[r % 3][h1][0][1], A1[r % 3][h1][1][0], A1[r % 3][h1][1][1]};
          const u32x4 fa = pp < 4 ? A0[r][pp & 3] : a1;
          MFMA_IP(acc[2 * s + e + pp], fa, B[r & 1][e]);
          SCHED();
          if (r + 2 < 8 && i == 2 * NP - 3) ldb(r + 2, 0);
          if (r + 2 < 8 && i == 2 * NP - 2) ldb(r + 2, 1);
          if (r + 2 < 8 && i < 2 * NH) lda1(r + 2, i % (2 * NH));
          SCHED();
        });
      };
      stage(IC<0>());
      STAMP(4);
      stage(IC<1>());
      STAMPF(9);
      u32x2 fv;
      if (MSPLIT) {
        u32x4 B[8][2];
        auto ldb = [&](int r, int m0) {
#pragma unroll
          for (int m = 0; m < 2; ++m) B[r][m] = *(const u32x4*)(lds + bp + 512 * (m0 + m + 1) - 64 * r);
        };
        asm volatile("s_nop 1" : "+v"(acc[2]), "+v"(acc[3]));
        yput(1);
        if (w == 3) wo_ld();
        __builtin_amdgcn_s_setprio(MPR);
        lf_wait(8, 4);
        STAMP(22);
        auto ldb1 = [&](int i, int m0) { B[i >> 1][i & 1] = *(const u32x4*)(lds + bp + 512 * (m0 + (i & 1) + 1) - 64 * (i >> 1)); };
#pragma unroll
        for (int r = 0; r < 3; ++r) ldb(r, 4);
        SCHED();
#pragma unroll
        for (int m = 0; m < 2; ++m) MFMA_IP(acc[4 + m], AD, B[0][m]);
        sfor<0, 24>([&](auto I_) __attribute__((always_inline)) {
          constexpr int i = decltype(I_)::value, r = i / 3, k = i % 3;
          MFMA_IP(acc[4 + (k != 1)], A0[r][k == 0], B[r][k == 2]);
          SCHED();
          if (i < 10) ldb1(6 + i, 4);
          SCHED();
        });
        sfor<0, 32>([&](auto I_) __attribute__((always_inline)) {
          constexpr int i = decltype(I_)::value, r = i >> 2, k = i & 3;
          MFMA_IP(acc[6 + (k & 1)], A0[r][2 + (k & 1) - (k >> 1)], B[r][k >> 1]);
          SCHED();
          if (i == 7) yput(2);
          if (!WC_T && i == 4 * FVR) fv = ld8(rs, b2o);
          if (i == 19) lf_wait(12, 4);
          if (i >= 19 && i < 29) ldb1(i - 19, 6);
          SCHED();
        });
#pragma unroll
        for (int m = 0; m < 2; ++m) MFMA_IP(acc[6 + m], AD, B[0][m]);
        sfor<0, 24>([&](auto I_) __attribute__((always_inline)) {
          constexpr int i = decltype(I_)::value, r = i / 3, k = i % 3;
          MFMA_IP(acc[6 + (k != 1)], A0[r][k == 0], B[r][k == 2]);
          SCHED();
          if (i < 6) ldb1(10 + i, 6);
          SCHED();
        });
        asm volatile("s_nop 7" : "+v"(acc[6]), "+v"(acc[7]));
        yput(3);
      } else {
        u32x4 B[3][4];
        auto ldb = [&](int r, int m) { B[r % 3][m] = *(const u32x4*)(lds + bp + 512 * (5 + m) - 64 * r); };
        asm volatile("s_nop 1" : "+v"(acc[2]), "+v"(acc[3]));
        yput(1);
        if (w == 3) wo_ld();
        __builtin_amdgcn_s_setprio(MPR);
        lf_wait(8, 8);
        STAMP(22);
#pragma unroll
        for (int i = 0; i < 8; ++i) ldb(i >> 2, i & 3);
        SCHED();
#pragma unroll
        for (int m = 0; m < 4; ++m) MFMA_IP(acc[4 + m], AD, B[0][m]);
        sfor<0, 80>([&](auto I_) __attribute__((always_inline)) {
          constexpr int r = decltype(I_)::value / 10, i = decltype(I_)::value % 10, m = i < 4 ? 0 : i < 7 ? 1 : i < 9 ? 2 : 3, pp = i - (m * (9 - m)) / 2;
          MFMA_IP(acc[4 + m + pp], A0[r][pp], B[r % 3][m]);
          SCHED();
          if (r + 2 < 8 && i < 4) ldb(r + 2, i & 3);
          if (r == 1 && i == 9) fv = ld8(rs, b2o);
          SCHED();
        });
#pragma unroll
        for (int e = 0; e < 4; ++e) {
          if (e == 1) asm volatile("s_nop 4" : "+v"(acc[5]), "+v"(acc[6]), "+v"(acc[7]));
          *(u32x2*)(U + 2048 + 512 * e + (16 * a + 4 * h) * 2) = (u32x2){pack2(acc[4 + e][0], acc[4 + e][1]), pack2(acc[4 + e][2], acc[4 + e][3])};
          SCHED();
        }
        asm volatile("" ::: "memory");
        if (l == 0) lf[28 + ch] = tag;
      }
      STAMP(28);
      __builtin_amdgcn_s_setprio(0);
      if (WC_T) { hold(WC_T); fv = (u32x2){~tag, 0u}; }
      b2_wait(fv, 0);
      lf_wait(20, 4);
      DRAIN();
    }
    __builtin_amdgcn_s_setprio(0);
  } else {
    const int at = idx - 20;
    char* const scr = lds + WBLK + 14848;
    auto gv_blk = [&](int j) { return j < 160 ? 30 + (j >> 3) : j < 400 ? (j >> 3) - 20 : j >> 3; };
    auto gv_fin = [&](const u32x4* pre, int j) {
      float acc[8];
      gemv_fin(acc, pre, 10, scr);
      gemv_put(acc, j < 160 ? WS_MODP + (unsigned)((j & 7) * 1280 + (j >> 3) * 64) * 8u : WS_MODQ + (unsigned)((j & 7) * 2560 + ((j >> 3) - 20) * 64) * 8u);
    };
    const int ja = xcd * 12 + at, ai = xcd * 8 + at, bi = xcd * 4 + at - 8, g = w >> 2, wj = w & 3;
    const int rj = w == 0 ? ja : w == 1 ? (at >= RJ0 && at < RJ0 + 8 ? 96 - RJ0 + ai : -1) : w == 4 ? 160 + ja : w == 5 && at < 8 ? 256 + ai : -1;
    char* const rst = lds + (g ? WBLK : 65536) + wj * 7424;
    u32x4 pre[11];
    if (at >= 8 || g == 0) {
      TOUCH_AT(2);
      const int T = at >= 8 ? 64 + 2 * bi + g : ai;
      unsigned* mds = (unsigned*)(lds + 2048) + 16 * g;
      char* qt = lds + L_RT + g * (16 * 1312);
      u32x4 ld[5];
      const bool e1 = at >= 8 && w == 3;
      if (!e1) __builtin_amdgcn_s_sleep(8);
      auto q_ld = [&](auto N_) {
        asm volatile("; %0" :: "n"(decltype(N_)::value) : "memory");
#pragma unroll
      for (int j = 0; j < 5; ++j)
        ld[j] = __builtin_amdgcn_raw_buffer_load_b128(rq, (unsigned)(h4 * D_ * 2 + n16 * 16 + j * 256), (unsigned)((32 * (T >> 1) + 8 * wj + 4 * (T & 1)) * D_ * 2), QG ? 0 : NT);
        SCHED();
      };
      const int g2 = at < 8 || w < 2 || w == 4 || w == (G2E ? 3 : 2) ? 400 : w < 4 ? 384 + bi : 256 + wj * 32 + bi;
      SCHED();
      float sa = 0.f, va = 0.f;
      auto q_in = [&]() {
        const bf2_t ONE2 = {(__bf16)1.0f, (__bf16)1.0f};
        char* dst = qt + (4 * wj + h4) * 1312 + n16 * 16;
        SCHED();
#pragma unroll
        for (int j = 0; j < 5; ++j) {
          if (!QG) *(volatile LAS u32x4*)(dst + j * 256) = ld[j];
#pragma unroll
          for (int d = 0; d < 4; ++d) {
            const unsigned a0 = ld[j][d];
            const bf2_t xa = __builtin_bit_cast(bf2_t, a0);
            sa = __builtin_amdgcn_fdot2_f32_bf16(xa, ONE2, sa, false); va = __builtin_amdgcn_fdot2_f32_bf16(xa, xa, va, false);
          }
        }
      };
      u32x4 pre2[11];
      if (e1) {
        gemv_load(pre, gv_blk(128 + bi), bi & 7, 10);
        q_ld(IC<3>());
        TAG_READY();
        gv_fin(pre, 128 + bi);
        q_in();
        asm volatile("; d" : "+v"(sa), "+v"(va));
      } else if (G2E && g2 < 400) {
        q_ld(IC<0>());
        gemv_load(pre2, gv_blk(g2), g2 & 7, 10);
        q_in();
        asm volatile("; a" : "+v"(sa), "+v"(va));
      } else if (RTE && w < 2 && rj >= 0) {
        q_ld(IC<1>());
        if (RTE > 1) __builtin_amdgcn_s_sleep(RTE);
        rt_ld(rj, rst);
        q_in();
        asm volatile("; b" : "+v"(sa), "+v"(va));
      } else {
        q_ld(IC<2>());
        q_in();
        asm volatile("; c" : "+v"(sa), "+v"(va));
      }
      TAG_READY();
      {
        if (!QG) lf_set(8 + w);
        const float ma = sum16(sa) * (1.0f / D_);
        va = __builtin_fmaxf(sum16(va) * (1.0f / D_) - ma * ma, 0.f);
        if (n16 == 0) mds[4 * wj + h4] = pack2(ma, __builtin_amdgcn_sqrtf(rbf(rbf(va) + 1e-5f)));
      }
      if (QG || QC) lf_set(16 + w);
      if (RTM && w < 2 && rj >= 0) { rt_ld(rj, rst); SCHED(); }
      if (!QG) lf_wait(8 + 4 * g, 4);
      STAMP(23);
      if (QG) {
        if (wj == 2) {
          lf_wait(16 + 4 * g, 4);
          st16(rs, WS_ST1 + (unsigned)(l >> 3) * 16384u + (unsigned)T * 128u + (unsigned)(l & 7) * 16u, (u32x4){mds[l & 7], tag, mds[8 + (l & 7)], tag});
          STAMP(22);
        }
      } else {
        const char* src = qt + (l & 7) * 1312 + (40 * wj + (l >> 3)) * 8;
        unsigned ckw = 0u;
        u32x2 qx[5][2];
#pragma unroll
        for (int j = 0; j < 5; ++j) { qx[j][0] = *(const volatile LAS u32x2*)(src + j * 64); qx[j][1] = *(const volatile LAS u32x2*)(src + j * 64 + 8 * 1312); }
        SCHED();
#pragma unroll
        for (int j = 0; j < 5; ++j) {
          const u32x2 x = qx[j][0], y = qx[j][1];
          st16(rs, WS_QB + (unsigned)T * 20480u + (unsigned)(5 * wj + j) * 1024u + l16, (u32x4){x[0], x[1], y[0], y[1]});
          if (QC) { const unsigned t = tag - sum8u((x[0] + x[1]) + (y[0] + y[1])); ckw = (l & 7) == j ? t : ckw; }
        }
        if (QC) {
          st4(rs, WS_QCK + (unsigned)(T * 4 + wj) * 256u + (unsigned)(l * 4), (unsigned)__builtin_amdgcn_ds_bpermute(((l & 7) * 8 + (l >> 3)) * 4, (int)ckw));
          if (wj == 2) {
            lf_wait(16 + 4 * g, 4);
            st16(rs, WS_ST1 + (unsigned)(l >> 3) * 16384u + (unsigned)T * 128u + (unsigned)(l & 7) * 16u, (u32x4){mds[l & 7], tag, mds[8 + (l & 7)], tag});
            STAMP(22);
          }
        }
      }
      SCHED();
      static_assert((!G2E && !RTE) || QC, "");
      if (g2 < 400) {
        if (!G2E) gemv_load(pre2, gv_blk(g2), g2 & 7, 10);
        if (!QG && !QC) asm volatile("s_waitcnt vmcnt(10)" ::: "memory");
      } else if (w < 2 && rj >= 0) {
        if (!RTE && !RTM) rt_ld(rj, rst);
        if (!QG && !QC) asm volatile("s_waitcnt vmcnt(7)" ::: "memory");
      } else if (!QG && !QC) {
        DRAIN();
      }
      if (!QG && !QC) lf_set(16 + w);
      if (!QG && !QC && wj == 2) {
        lf_wait(16 + 4 * g, 4);
        st16(rs, WS_ST1 + (unsigned)(l >> 3) * 16384u + (unsigned)T * 128u + (unsigned)(l & 7) * 16u, (u32x4){mds[l & 7], tag, mds[8 + (l & 7)], tag});
        STAMP(22);
      }
      if (w >= 4 && w < 7) lf_set(w);
      if (g2 < 400) gv_fin(pre2, g2);
      STAMP(25);
    } else {
      constexpr int KB = L_RT + 16 * 1312;
      TOUCH_AT(3);
      const int g1 = w < 6 ? 2 * ai + wj : 32 + wj * 64 + ai;
      if (w >= 6) {
        const rsrc_t rk = __builtin_amdgcn_make_buffer_rsrc((void*)(w == 6 ? p.k : p.v), 0, S_ * D_ * 2, 0x00020000);
        const unsigned ko = (unsigned)((1536 + 8 * ai) * (D_ * 2) - (w == 6 ? 4096 : 0));
        char* dst = lds + KB + (w == 6 ? 0 : 14336);
        __builtin_amdgcn_s_sleep(3);
#pragma unroll
        for (int n = 0; n < 10; ++n) __builtin_amdgcn_raw_ptr_buffer_load_lds(rk, (LAS void*)(dst + n * 1024), 16, l16, ko + (unsigned)n * 1024u, 0, NT);
        if (w == 6) {
#pragma unroll
          for (int n = 10; n < 14; ++n) __builtin_amdgcn_raw_ptr_buffer_load_lds(rk, (LAS void*)(dst + n * 1024), 16, l16, ko + (unsigned)n * 1024u, 0, NT);
        }
      }
      SCHED();
      u32x4 swt[2];
      swt[0] = *(const u32x4*)(p.shift_k + (tid - 256) * 8);
      swt[1] = *(const u32x4*)(p.shift_k + (256 + l) * 8);
      SCHED();
      if (w >= 6) __builtin_amdgcn_s_sleep(8);
      gemv_load(pre, gv_blk(g1), g1 & 7, 10);
      TAG_READY();
      asm volatile("s_waitcnt vmcnt(11)" : "+v"(swt[0]), "+v"(swt[1]) :: "memory");
      lf_set(8 + w);
      if (w < 6) gv_fin(pre, g1);
      STAMP(25);
      lf_wait(12, 4);
      STAMP(23);
#pragma unroll 1
      for (int n = 0; n < (w == 7 ? 2 : 1); ++n) {
        {
          const int pr = n ? 256 + l : tid - 256;
          const char* kp = lds + KB + 4096 + pr * 4;
          unsigned kk[11], vv[8];
#pragma unroll
          for (int i = 0; i < 11; ++i) kk[i] = *(const unsigned*)(kp + (i - 3) * 1280);
#pragma unroll
          for (int i = 0; i < 8; ++i) vv[i] = *(const unsigned*)(kp + 10240 + i * 1280);
          const u32x4 sw = n ? swt[1] : swt[0];
          const f32x2 wt[4] = {{bflo(sw[0]), bflo(sw[2])}, {bfhi(sw[0]), bfhi(sw[2])}, {bflo(sw[1]), bflo(sw[3])}, {bfhi(sw[1]), bfhi(sw[3])}};
          f32x2 o[8];
#pragma unroll
          for (int i = 0; i < 8; ++i) {
            f32x2 t = (f32x2){bflo(kk[i + 3]), bfhi(kk[i + 3])} * wt[0];
#pragma unroll
            for (int j = 1; j < 4; ++j) t = __builtin_elementwise_fma((f32x2){bflo(kk[i + 3 - j]), bfhi(kk[i + 3 - j])}, wt[j], t);
            o[i] = t * (f32x2){bflo(vv[i]), bfhi(vv[i])};
          }
          char* sc = lds + 98304 + wj * 2304;
          unsigned t = 0u;
#pragma unroll
          for (int e = 0; e < 2; ++e) {
            const u32x4 x = {pack2(o[7][e], o[6][e]), pack2(o[5][e], o[4][e]), pack2(o[3][e], o[2][e]), pack2(o[1][e], o[0][e])};
            *(volatile LAS u32x4*)(sc + l * 32 + e * 16) = x;
            t += (x[0] + x[1]) + (x[2] + x[3]);
          }
          *(volatile LAS unsigned*)(sc + 2048 + (l >> 1) * 4 + (l & 1) * 128) = tag - t - (unsigned)__builtin_amdgcn_update_dpp(0, (int)t, 0xB1, 0xf, 0xf, true);
          u32x4 x[3];
#pragma unroll
          for (int e = 0; e < 3; ++e) x[e] = *(const volatile LAS u32x4*)(sc + e * 1024 + (e < 2 ? l : l & 7) * 16);
          const unsigned ko = WS_KV3 + (unsigned)ai * 10880u;
#pragma unroll
          for (int e = 0; e < 2; ++e) st16(rs, ko + (unsigned)((n ? 4 : wj) * 2048 + e * 1024) + l16, x[e]);
          if (l < 8) st16(rs, ko + (unsigned)(10240 + (n ? 4 : wj) * 128) + l16, x[2]);
        }
      }
      STAMP(22);
      if (w < 7) lf_set(w);
      if (w >= 6) gv_fin(pre, g1);
      STAMPF(30);
    }
    if (w == 6 && ja < 80) {
      hold(AUX_T);
      gemv_load(pre, gv_blk(400 + ja), ja & 7, 10);
      gv_fin(pre, 400 + ja);
    }
    if (rj >= 0) {
      if (w >= 2) { hold(RT2_T); rt_ld(rj, rst); }
      rt_job(rj, rst);
    }
    STAMP(26);
    lf_set(w);
    if (w == 3) { hold(WA_T); wo_ld(); }
    STAMP(3);
    if (w < 4) {
      hold(W_T);
      b2_wait((u32x2){~tag, 0u}, 1);
      lf_wait(0, 8);
      STAMP(4);
    }
    DRAIN();
  }

  STAMPF(16);
  u32x4 wox[5];
  if (w == 3) {
    const char* bp = wo_st + (4 * h4 + (n16 >> 2)) * 160 + (n16 & 3) * 8;
#pragma unroll
    for (int dj = 0; dj < 5; ++dj) wox[dj] = trfrag(bp + dj * 32, 160);
    asm volatile("s_waitcnt lgkmcnt(0)" : "+v"(wox[0]), "+v"(wox[1]), "+v"(wox[2]), "+v"(wox[3]), "+v"(wox[4]) :: "memory");
  }
  auto wo_put = [&]() {
    if (w == 3 && R < 28) {
      const unsigned base = WS_WOUTF + (unsigned)c * WBLK + (unsigned)((R >> 1) * 10240 + (R & 1) * 1024);
#pragma unroll
      for (int dj = 0; dj < 5; ++dj) st16(rs, base + (unsigned)((R & 1) ? dj : (dj + 2) % 5) * 2048u + l16, wox[dj]);
    }
  };
  if (cs2) {
    cs_ld(csv, (8 + c) * 7 + R - 20);
    SCHED();
    load_b2(0, 12);
    SCHED();
    cs_fin(csv, (8 + c) * 7 + R - 20);
  } else if (w < 4) {
    asm volatile("" ::: "memory");
    load_b2(0, B0);
    if (B0 < 12 && !is_conv) load_b2(B0, 12);
  }
  const unsigned xfb = WS_FX + (unsigned)(((R >> 1) * 4 + (R & 1) * 2 + (c >> 2)) * 160 + l) * 128u;
  unsigned xf3[3];
  if (w == 7) {
    lf_wait(36, 4);
    int dt = (int)(lf[44 + ((l & 31) < 20 ? l & 31 : 0)] - t_start);
    dt = l < 32 ? dt : -dt;
    dt = max(dt, __builtin_amdgcn_update_dpp(0, dt, 0xB1, 0xf, 0xf, true));
    dt = max(dt, __builtin_amdgcn_update_dpp(0, dt, 0x4E, 0xf, 0xf, true));
    dt = max(dt, __builtin_amdgcn_update_dpp(0, dt, 0x124, 0xf, 0xf, true));
    dt = max(dt, __builtin_amdgcn_update_dpp(0, dt, 0x128, 0xf, 0xf, true));
    const int d1 = min(max(max(__builtin_amdgcn_readlane(dt, 0), __builtin_amdgcn_readlane(dt, 16)), 0), 300);
    const int d0 = min(max(max(__builtin_amdgcn_readlane(dt, 32), __builtin_amdgcn_readlane(dt, 48)), 0), 300);
    dt = ((d1 + d0) * YW >> 3) - d0;
    __builtin_amdgcn_s_setprio(YPR);
    STAMPF(24);
    hold((unsigned)(YG_T + dt));
    STAMPF(30);
    xf3[0] = ld4(rs, xfb); xf3[1] = ld4(rs, xfb + 8192u); xf3[2] = ld4(rs, l < 32 ? xfb + 16384u : ~0u);
    __builtin_amdgcn_s_setprio(0);
  }
  SCHED();
  u32x4 k_c;
  unsigned kc_o = w < 2 ? (unsigned)(tid < 112 ? tid : 0) * 16u : 0x80000000u, kc_d = (unsigned)(L_CONST + (tid & 127) * 8);
  auto kc_ld = [&](int z2) { k_c = __builtin_amdgcn_raw_buffer_load_b128(rs, kc_o, WS_CS + (unsigned)(8 * z2 + c) * 2048u, SC1); };
  auto consts_put = [&](int z2) {
    if (w < 2) {
      if (__any(ne2(k_c[0], k_c[2]) != 0u)) {
        _Pragma("unroll 1") for (unsigned spins = 0; spins < SPIN_CAP; ++spins) {
          __builtin_amdgcn_s_sleep(8);
          kc_ld(z2);
          if (!__any(ne2(k_c[0], k_c[2]) != 0u)) break;
        }
      }
      if (z2) *(u32x2*)(lds + kc_d + 1024) = (u32x2){k_c[1], k_c[3]};
      else { const unsigned d = kc_d - ((kc_d >> 1) & 4u); *(unsigned*)(lds + d) = k_c[1]; *(unsigned*)(lds + d + 8) = k_c[3]; }
    }
  };
  const int wq = w ^ rotr, rt1 = wq & 3, kh1 = w >> 2;
  unsigned bo0 = (unsigned)(kh1 * 71680) + l16, bo1 = bo0 + 35840u;
  asm volatile("" : "+v"(bo0), "+v"(bo1));
  unsigned pso[3], pqo[3], oto = (unsigned)(((w ^ 4) * 7 * 64 + l) * 8), rdo = (unsigned)((w * 7 * 64 + l) * 8), cso = (unsigned)(L_CONST + (128 + n16) * 8);
#pragma unroll
  for (int e = 0; e < 3; ++e) {
    const int row = rt1 * 16 + (e < 2 ? 4 * h4 + 2 * kh1 + e : n16);
    pso[e] = (unsigned)(L_PST + row * 4); pqo[e] = (unsigned)(L_PST + 512 + row * 16 + (row & 3) * 4);
  }
  unsigned spo = (unsigned)(L_Z + tid * 16), p6o = (unsigned)(L_PST + (wq * 16 + 4 * h4) * 4), pgo = (unsigned)(L_PST + 512 + (wq * 16 + n16) * 16);
  asm volatile("" : "+v"(spo), "+v"(p6o), "+v"(pgo));
  p6o = n16 == 12 ? p6o : spo;
  pgo = h4 == (n16 >> 2) ? pgo : spo;
  unsigned bsrc = (unsigned)(((l >> 1) & 3) * 64 + (l & 1) * 4), ppo = l < 8 ? l16 : 0x80000000u;
  asm volatile("" : "+v"(pso[0]), "+v"(pso[2]), "+v"(pqo[0]), "+v"(pqo[2]), "+v"(oto), "+v"(rdo), "+v"(cso), "+v"(p6o), "+v"(pgo), "+v"(bsrc), "+v"(ppo), "+v"(kc_o), "+v"(kc_d));
  pso[1] = pso[0] + 4u; pqo[1] = pqo[0] + 20u;
#define BFRAG(q, j) (*(const u32x4*)(lds + ((q) < 5 ? bo0 : bo1) + ((q) % 5) * 7168 + (j) * 1024))
  u32x4 h2[10];
  unsigned xv = (unsigned)((4 * ((l >> 3) & 1) + h4) * 128 + (l & 7) * 16), xs0 = WS_X1B + (unsigned)(R * 4 + rt1) * 20480u + (unsigned)(10 * kh1) * 1024u;
  unsigned wvo = wsp_vo, bso = (unsigned)tau(rt1, n16) * 2u, fgo = l < 32 ? (unsigned)(l >> 3) * F4S + (unsigned)(l & 7) * 16u : 0x80000000u;
  asm volatile("" : "+v"(xv), "+v"(wvo), "+v"(bso), "+v"(fgo));
#define WO3 (-92)
#define PINS(x) do { x = (unsigned)__builtin_amdgcn_readfirstlane((int)x); asm volatile("" : "+s"(x)); } while (0)
  PINS(xs0);
  unsigned zs[8], wx8, wx4, wgb, fzs, uo, ui, xs, wl2, os, zos, pos, s1, s3;
#define LDX(q) h2[q] = __builtin_amdgcn_raw_buffer_load_b128(rs, xv + (unsigned)((q) & 3) * 1024u, xs0 + (unsigned)((q) >> 2) * 4096u, BULK_AUX)
  auto flag4 = [&](unsigned off) { ST16S(((u32x4){tag, tag, tag, tag}), rs, fgo, WS_F4 - WS_FWI + off, SC1); };
  unsigned bs_raw, fwo[1];
  f32x4 acc[7];
  float mu_n, rs_n;
  {
    unsigned f1o[1] = {w == 1 && l < 20 ? fwi + (unsigned)(c * 20 + l) * 128u : ~0u}, f1v;
    fwo[0] = w == 3 && l < 28 ? fwi + (WS_FWO - WS_FWI) + (unsigned)(c * 32 + l) * 128u : ~0u;
    asm volatile("" : "+v"(f1o[0]), "+v"(fwo[0]));
    auto late_scalars = [&]() {
#pragma unroll
    for (int t = 0; t < 8; ++t) { zs[t] = WS_Z2F + (unsigned)(c * 64 + 8 * w + 4 * ((t >> 2) ^ rotg) + ((t + rotb) & 3)) * 7168u; PINS(zs[t]); }
    wx8 = (unsigned)((w + (idx >> 2)) & 7) * 1024u; wx4 = wx8 & 3072u; wgb = WS_WOUTF + (unsigned)c * WBLK; fzs = fz + (unsigned)(c * 32 + 4 * w) * 128u;
    uo = WS_UF + (unsigned)((R * 4 + rt1) * 8 + c) * 4096u + (unsigned)kh1 * 2048u; ui = WS_UF + (unsigned)((R * 4 + rt1) * 8 + 4 * kh1) * 4096u;
    xs = WS_X1B + (unsigned)(R * 4 + rt1) * 20480u + (unsigned)(5 * c + 2 * kh1) * 512u; wl2 = (unsigned)(120 + WO3 + (w < 4 ? w + 16 : w)) * 1024u;
    os = (unsigned)(((R * 64 + tau(rt1, 0)) * D_ + 80 * c + 32 * kh1) * 2);
    zos = WS_Z2F + (unsigned)((c * 64 + 4 * (R >> 1)) * 7 + (w < 6 ? w + 1 : 0)) * 1024u + (unsigned)(R & 1) * 512u; pos = WS_PART + (unsigned)(R * 64 + wq) * 128u;
    s1 = (unsigned)((w ^ 4) * 3072 + 1024); s3 = (unsigned)(w * 3072 + 1024);
    PINS(wx8); PINS(wx4); PINS(wgb); PINS(fzs); PINS(uo); PINS(ui); PINS(xs); PINS(wl2); PINS(os); PINS(zos); PINS(pos); PINS(s1); PINS(s3);
    SCHED();
    };
    if (GE) late_scalars();
  if (w < 4) {
    load_b2(12, BK);
    if (BG && is_conv) {
      lf_wait(40, 4);
      load_b2(B0, 12);
    }
    load_b2(BK, 35);
  }
  wo_put();
  STAMPF(29);
    if (w == 7) {
      asm volatile("" : "+v"(xf3[0]), "+v"(xf3[1]), "+v"(xf3[2]));
      if (__any(ne2(xf3[0], xf3[1]) != 0u) || __builtin_amdgcn_ballot_w64(xf3[2] == tag) != 0xffffffffull) {
        const unsigned fo[3] = {xf3[0] != tag ? xfb : ~0u, xf3[1] != tag ? xfb + 8192u : ~0u, l < 32 && xf3[2] != tag ? xfb + 16384u : ~0u};
        wait_lines<3, YSL>(rs, fo, tag);
      }
      if (l == 0) lf[33] = tag;
    }
    lf_wait(33, 1);
    STAMP(7);
    SCHED();
#pragma unroll
    for (int q = 0; q < 3; ++q) LDX(q);
    SCHED();
    __builtin_amdgcn_s_waitcnt(0x0F73);
    BAR();
    STAMP(8);
#pragma unroll
    for (int j = 0; j < 7; ++j) acc[j] = (f32x4){0.f, 0.f, 0.f, 0.f};
    f32x4 accg = {0.f, 0.f, 0.f, 0.f};
    u32x4 bf[7];
#pragma unroll
    for (int j = 0; j < 7; ++j) bf[j] = BFRAG(0, j);
    SCHED();
#pragma unroll
    for (int q = 3; q < 6; ++q) LDX(q);
    if (w == 3) flag4(WS_FWO + (unsigned)(c * 32 + R) * 128u);
    SCHED();
    if (!GE) late_scalars();
    auto xswap = [&](int q, int e) {
#pragma unroll
      for (int d = 0; d < 2; ++d) {
        if (e == 1) h2[q][2 + d] ^= (unsigned)__builtin_amdgcn_update_dpp(0, (int)h2[q][d], 0x128, 0xf, 0x3, false);
        else h2[q][d] ^= (unsigned)__builtin_amdgcn_update_dpp(0, (int)h2[q][2 + d], 0x128, 0xf, 0xC, false);
      }
      asm volatile("" : "+v"(h2[q]));
    };
#define XSW 1
#pragma unroll
    for (int q = 0; q < 10; ++q) {
      if (q < XSW) { xswap(q, 0); xswap(q, 1); xswap(q, 2); }
      if (q == 5 && w == 1 && __builtin_amdgcn_ballot_w64(f1v == tag) != 0xfffffull) wait_lines<1, 4>(rs, f1o, tag);
      if (q == 5) { __builtin_amdgcn_s_barrier(); asm volatile("" ::: "memory"); }
      SCHED();
      accg = MFMA16(h2[q], h2[q], accg);
      SCHED();
#pragma unroll
      for (int j = 0; j < 7; ++j) {
        acc[j] = MFMA16(h2[q], bf[j], acc[j]);
        if (q == 9 && j == 1) *(f32x4*)(lds + pgo) = accg;
        if (q < 9) bf[j] = BFRAG(q + 1, j);
        if (q < 9 && q + 1 >= XSW && j >= 3 && j < 6) xswap(q + 1, j - 3);
        if (q == 1 && j == 6) f1v = ld4(rs, f1o[0]);
        if (q == 2 && j == 0) bs_raw = __builtin_amdgcn_raw_buffer_load_b16(__builtin_amdgcn_make_buffer_rsrc((void*)p.b_sp, 0, S_ * 2, 0x00020000), bso, (unsigned)R * 128u, 0);
#define KCQ 4
#define WA 8
#define WB 12
#define WQ0 2
        if (WQ0 == 2 && q >= 2 && j == 2) LDW(q - 2);
        if (WQ0 == 6 && q >= 6 && (j == 2 || j == 4)) LDW(2 * (q - 6) + (j >> 2));
        if (WQ0 == 3 && q >= 2 && q < 5 && (j & 1) && 3 * (q - 2) + (j >> 1) < WA) LDW(3 * (q - 2) + (j >> 1));
        if (WQ0 == 4 && q >= 2 && !(q & 1) && j == 2) LDW((q - 2) >> 1);
        if ((j == 0 || j == 4) && 6 + 2 * q + (j >> 2) < 10) LDX(6 + 2 * q + (j >> 2));
        if (q == KCQ && j == 0) kc_ld(1);
        if (q == 2 && j == 1 && cs1) cs_ld(csv, c * 7 + R - 25);
        if (q >= 5 && j == 5) {
          const int i = (5 + q) * 7 + (w < 7 ? w : 0);
          dma1k(rs, lds + i * 1024, l16, WS_WINF + (unsigned)c * WBLK + (unsigned)i * 1024u);
          if (q == 9) { const int i2 = 28 + (w < 7 ? w : 0); dma1k(rs, lds + i2 * 1024, l16, WS_WINF + (unsigned)c * WBLK + (unsigned)i2 * 1024u); }
        }
        SCHED();
      }
    }
    STAMP(31);
  }
  unsigned sro, swo, zo, zro, po;
  auto gelu_n = [&](f32x2* x, auto N_, auto&& fill) {
    constexpr int N = decltype(N_)::value;
    const float c0 = -2.0f * 0.7978845608028654f * 1.4426950408889634f, c1 = c0 * 0.044715f;
    f32x2 k0 = {c0, c0}, k1 = {c1, c1}, k2 = {1.0f, 1.0f};
    asm volatile("" : "+v"(k0), "+v"(k1), "+v"(k2));
    f32x2 t[N];
#define GST(e) do { _Pragma("unroll") for (int i = 0; i < N; ++i) { t[i] = (e); asm volatile("" : "+v"(t[i])); } } while (0)
    GST(x[i] * x[i]);
    GST(__builtin_elementwise_fma(t[i], k1, k0));
    fill(IC<0>());
    GST(t[i] * x[i]);
    fill(IC<1>());
    GST(((f32x2){__builtin_amdgcn_exp2f(t[i][0]), __builtin_amdgcn_exp2f(t[i][1])}));
    fill(IC<2>());
    GST(t[i] + k2);
    fill(IC<3>());
    GST(((f32x2){__builtin_amdgcn_rcpf(t[i][0]), __builtin_amdgcn_rcpf(t[i][1])}));
    fill(IC<4>());
#pragma unroll
    for (int i = 0; i < N; ++i) { x[i] *= t[i]; asm volatile("" : "+v"(x[i])); }
  };
  {
    f32x2 mine[7];
    f32x2 csb[7];
    if (kh1) {
#pragma unroll
      for (int j = 0; j < 7; ++j) { *(f32x2*)(lds + rdo + j * 512) = (f32x2){acc[j][0], acc[j][1]}; mine[j] = (f32x2){acc[j][2], acc[j][3]}; }
      *(f32x4*)(lds + p6o) = acc[6];
    } else {
#pragma unroll
      for (int j = 0; j < 7; ++j) { *(f32x2*)(lds + rdo + j * 512) = (f32x2){acc[j][2], acc[j][3]}; mine[j] = (f32x2){acc[j][0], acc[j][1]}; }
      *(f32x4*)(lds + p6o) = acc[6];
      consts_put(1);
      if (cs1) cs_fin(csv, c * 7 + R - 25);
#pragma unroll
      for (int k = WA; k < WB; ++k) LDW(k);
    }
    BAR();
#define BY 0
    auto b1p = [&](int k) {
      const int i = 4 * k + (w & 3) + (k < 8 ? 35 : k > 8 ? 70 : (w & 3) < 3 ? 35 : 70) - (k == 17 ? (w & 2) : 0);
      dma1k(rs, lds + i * 1024, l16, WS_WINF + (unsigned)c * WBLK + (unsigned)i * 1024u);
    };
#define IB 5
    auto b18 = [&](int k) {
      const int i = 8 * k + w + (k < 4 ? 35 : k > 4 ? 70 : w < 3 ? 35 : 70) - (k == 8 && w > 5 ? 2 : 0);
      dma1k(rs, lds + i * 1024, l16, WS_WINF + (unsigned)c * WBLK + (unsigned)i * 1024u);
      SCHED();
    };
    if (kh1) {
#pragma unroll
      for (int k = 0; k < BY; ++k) b1p(k);
#pragma unroll
      for (int k = WA; k < WB; ++k) LDW(k);
    }
    __builtin_amdgcn_s_setprio(3);
    STAMPT(9);
    f32x2 oth[7];
    f32x2 pa[3], pb[3];
#pragma unroll
    for (int e = 0; e < 3; ++e) {
      pa[e] = (f32x2){*(const float*)(lds + pso[e]), *(const float*)(lds + pqo[e])};
      pb[e] = (f32x2){*(const float*)(lds + pso[e] + 256), *(const float*)(lds + pqo[e] + 1024)};
    }
#pragma unroll
    for (int j = 0; j < 7; ++j) { oth[j] = *(const f32x2*)(lds + oto + j * 512); csb[j] = *(const f32x2*)(lds + cso + 128 * j); }
    SCHED();
    if (IB > 6) b18(IB - 7);
    float mu2[2], rs2[2];
#pragma unroll
    for (int e = 0; e < 3; ++e) {
      const float mean = (pa[e][0] + pb[e][0]) * (1.0f / D_);
      const float var = __builtin_fmaxf((pa[e][1] + pb[e][1]) * (1.0f / D_) - mean * mean, 0.f);
      const float rstd = __builtin_amdgcn_rsqf(var + 1e-5f);
      if (e < 2) { mu2[e] = mean; rs2[e] = rstd; } else { mu_n = mean; rs_n = rstd; }
    }
    f32x2 zz[7];
    const f32x2 nmu = {-mu2[0], -mu2[1]}, rsv = {rs2[0], rs2[1]};
#pragma unroll
    for (int j = 0; j < 7; ++j)
      zz[j] = __builtin_elementwise_fma(__builtin_elementwise_fma(nmu, (f32x2){csb[j][0], csb[j][0]}, mine[j] + oth[j]), rsv, (f32x2){csb[j][1], csb[j][1]});
    if (IB > 5) {
#pragma unroll
      for (int j = 0; j < 7; ++j) asm volatile("" : "+v"(zz[j]));
      b18(IB - 6);
    }
    gelu_n(zz, IC<7>(), [&](auto K_) __attribute__((always_inline)) { if (IB - 5 + decltype(K_)::value >= 0 && IB) b18(IB - 5 + decltype(K_)::value); });
    if (n16 >= 12) zz[6] = (f32x2){0.f, 0.f};
    f32x2 s1 = ((zz[0] + zz[1]) + (zz[2] + zz[3])) + ((zz[4] + zz[5]) + zz[6]);
    f32x2 s2 = zz[0] * zz[0], s3 = zz[1] * zz[1];
#pragma unroll
    for (int j = 2; j < 7; ++j) { if (j & 1) s3 = __builtin_elementwise_fma(zz[j], zz[j], s3); else s2 = __builtin_elementwise_fma(zz[j], zz[j], s2); }
    s2 += s3;
    asm("" : "+v"(s1), "+v"(s2));
    float a0 = s1[0] + dpp_f<0xB1>(s1[0]), a1 = s1[1] + dpp_f<0xB1>(s1[1]), b0 = s2[0] + dpp_f<0xB1>(s2[0]);
    a0 = (l & 1) ? a1 : a0;
    const float b1 = s2[1] + dpp_f<0xB1>(s2[1]);
    b0 = (l & 1) ? b1 : b0;
    a0 += dpp_f<0x4E>(a0); b0 += dpp_f<0x4E>(b0);
    a0 += dpp_f<0x124>(a0); b0 += dpp_f<0x124>(b0);
    a0 += dpp_f<0x128>(a0); b0 += dpp_f<0x128>(b0);
    {
      const unsigned pa0 = __builtin_amdgcn_ds_bpermute(bsrc, __float_as_uint(a0)), pb0 = __builtin_amdgcn_ds_bpermute(bsrc, __float_as_uint(b0));
      ST16S(((u32x4){tag, pa0, tag, pb0}), rs, ppo, pos + (unsigned)c * 1024u, SC1);
    }
    SCHED();
    __builtin_amdgcn_s_setprio(0);
    STAMP(10);
#define BO 14
    if (kh1) {
#pragma unroll
      for (int k = 2 * IB + BY + BO; k < 18; ++k) b1p(k);
    } else {
#pragma unroll
      for (int k = 2 * IB + BY; k < 2 * IB + BY + BO && k < 18; ++k) b1p(k);
    }
#define WC 16
#define IT 0
#pragma unroll
    for (int k = WB; k < (IT ? WB : WC); ++k) LDW(k);
#pragma unroll
    for (int j = 0; j < 7; ++j)
      *(unsigned*)(lds + L_Z + (((rt1 >> 1) * 7 + j) * 64 + l) * 16 + (4 * (rt1 & 1) + 2 * kh1) * 2) = pack2(zz[j][0], zz[j][1]);
    SCHED();
#define TR0 2
#define TR1 TR0
#define TR2 TR1
#define TR3 5
    sfor<0, 4 * TR0>([&](auto X_) __attribute__((always_inline)) {
      constexpr int x = decltype(X_)::value, n = WC - WB, k0 = x * n / (4 * TR0), k1 = (x + 1) * n / (4 * TR0);
      if (IT) sfor<k0, k1>([&](auto K_) __attribute__((always_inline)) { LDW((WB + decltype(K_)::value) & 31); SCHED(); });
      WSP_TX(x);
      SCHED();
    });
    sro = (unsigned)(L_ST2 + h4 * 16);
    swo = (unsigned)(L_ST2 + (16 * rt1 + 4 * ((l >> 1) & 3) + 2 * kh1 + (l & 1)) * 4);
    zo = w < 7 ? (unsigned)(h4 * 7168 + n16 * 16) : 0x80000000u;
    zro = (unsigned)(L_Z + (w < 7 ? w : 0) * 1024) + l16;
    po = (unsigned)((l >> 3) * 1024 + (l & 7) * 16);
    asm volatile("" : "+v"(sro), "+v"(swo), "+v"(zo), "+v"(zro), "+v"(po));
    asm volatile("s_waitcnt vmcnt(%0)" :: "n"(WC - WB) : "memory");
    BAR();
    STAMPT(11);
  }
  unsigned z1d[7];
  constexpr int L_RED = 28672;
#define WOA(i) (((i) < 28 ? (i) : (i) < 108 ? (i) + 32 : (i) < 120 ? (i) + 37 : (i) - 92) * 1024)
#define RBAR() do { __builtin_amdgcn_s_barrier(); asm volatile("" ::: "memory"); } while (0)
#define ZAUX 16
#define NW 14
#define WOD(i0, x) dma1k(rs, lds + WOA(i0) + (x), l16, wgb + (unsigned)(i0) * 1024u + (x))
  auto wo_dma = [&](int k) {};
  u32x4 zw[NW];
#define LDZ(f) do { if (((f) >> 3) == 4) { zs[(f) & 7] += 4096u; asm volatile("" : "+s"(zs[(f) & 7])); } \
    zw[(f) % NW] = __builtin_amdgcn_raw_buffer_load_b128(rs, l16 + (unsigned)(((f) >> 3) & 3) * 1024u, zs[(f) & 7], ZAUX); } while (0)
#define WD 24
#define WE 32
#define QS 5
#define PQ 3
#define NW0 NW
#define VMW(n) asm volatile("s_waitcnt vmcnt(%0)" ::"n"(n) : "memory")
#define FQ 10
#define BFK 0
#define BFN 0
#define PP 1
#define WZ 2
#define WY 8
  static_assert((BFK == 0 || QS < 6) && QS >= 4 && QS <= 6 && PQ >= 2 && PQ < QS && (WZ < 4 || QS >= 5), "");
  {
    f32x4 acc[7];
#pragma unroll
    for (int j = 0; j < 7; ++j) acc[j] = (f32x4){0.f, 0.f, 0.f, 0.f};
    u32x4 bf[7];
#pragma unroll
    for (int j = 0; j < 7; ++j) bf[j] = BFRAG(4, j);
    u32x4 pv, zf[2];
    unsigned fwv;
    auto ld_pv = [&]() { pv = __builtin_amdgcn_raw_buffer_load_b128(rs, po, pos, SC1); };
    auto z1 = [&](auto Q0_, auto Q1_) __attribute__((always_inline)) {
#pragma unroll
      for (int qi = decltype(Q0_)::value; qi < decltype(Q1_)::value; ++qi) {
        const int q = (qi + 4) % 10, qn = (qi + 5) % 10;
        if (qi == FQ) {
          if (kh1) VMW(WE - WD + WZ); else VMW(0);
          RBAR();
          if (w == 3) flag4(WS_FZ + (unsigned)(c * 32 + R) * 128u);
        }
        SCHED();
#pragma unroll
        for (int j = 0; j < 7; ++j) {
          acc[j] = MFMA16(bf[j], h2[q], acc[j]);
          if (qi < 9 && (qi != QS - 1 || j < BFK)) bf[j] = BFRAG(qn, j);
          if (qi < 2 && (j == 1 || j == 4)) { const int i = (2 * qi + (j >> 2)) * 7 + (w < 7 ? w : 0); dma1k(rs, lds + i * 1024, l16, WS_WINF + (unsigned)c * WBLK + (unsigned)i * 1024u); }
          if (qi == 2 && j == 0) kc_ld(0);
          if (qi == 2 && j == 3) fwv = ld4(rs, fwo[0]);
          if (qi == PQ && j == 1) ld_pv();
          if (qi == QS - 1 && j < 2) zf[j] = *(const u32x4*)(lds + zro + j * 7168);
          if (!PP && qi >= QS && (j & 1) && WD + 3 * (qi - QS) + (j >> 1) < WE) LDW((WD + 3 * (qi - QS) + (j >> 1)) & 31);
          SCHED();
        }
      }
    };
    auto req1 = [&]() {
#pragma unroll
      for (int k = WC; k < WD; ++k) { LDW(k); SCHED(); }
    };
    auto req2 = [&]() {
#pragma unroll
      for (int k = WD; k < (PP ? WE : WD); ++k) { LDW(k); SCHED(); }
      sfor<0, WZ>([&](auto G_) __attribute__((always_inline)) { WOD(68 + 8 * decltype(G_)::value, wx8); SCHED(); });
    };
    if (kh1) req1();
    z1(IC<0>(), IC<QS>());
    STAMPT(29);
    f32x2 zv[2][4];
#pragma unroll
    for (int i = 0; i < 8; ++i) { zv[i >> 2][i & 3] = (f32x2){bflo(zf[i >> 2][i & 3]), bfhi(zf[i >> 2][i & 3])}; asm volatile("" : "+v"(zv[i >> 2][i & 3])); }
#pragma unroll
    for (int g = TR0; g < TR1; ++g) WSP_TR(g);
    __builtin_amdgcn_s_setprio(3);
    if (__any(ne2(pv[0], pv[2]) != 0u)) {
      _Pragma("unroll 1") for (unsigned spins = 0; spins < SPIN_CAP; ++spins) {
        ld_pv();
        if (!__any(ne2(pv[0], pv[2]) != 0u)) break;
        __builtin_amdgcn_s_sleep(1);
      }
    }
    {
      float t1 = __uint_as_float(pv[1]), t2 = __uint_as_float(pv[3]);
      t1 += dpp_f<0x128>(t1); t2 += dpp_f<0x128>(t2);
      auto x = __builtin_amdgcn_permlane16_swap(__float_as_uint(t1), __float_as_uint(t2), false, false);
      const unsigned y = __float_as_uint(__uint_as_float(x[0]) + __uint_as_float(x[1]));
      auto z = __builtin_amdgcn_permlane32_swap(y, y, false, false);
      const unsigned u = __float_as_uint(__uint_as_float(z[0]) + __uint_as_float(z[1]));
      auto v = __builtin_amdgcn_permlane16_swap(u, u, false, false);
      const float mean = __uint_as_float(v[0]) * (1.0f / FH);
      const float var = __builtin_fmaxf(__uint_as_float(v[1]) * (1.0f / FH) - mean * mean, 0.f);
      const float rstd = __builtin_amdgcn_rsqf(var + 1e-5f);
      *(float*)(lds + swo) = rstd;
      *(float*)(lds + swo + 256) = -mean * rstd;
    }
    SCHED();
    consts_put(0);
    if (w == 3 && __builtin_amdgcn_ballot_w64(fwv == tag) != 0xfffffffull) wait_lines<1, 1>(rs, fwo, tag);
    if (!kh1) req1();
    BAR();
    STAMP(12);
    {
      f32x4 sa[2][2], sb[2][2];
#pragma unroll
      for (int i = 0; i < 4; ++i) { sa[i >> 1][i & 1] = *(const f32x4*)(lds + sro + i * 64); sb[i >> 1][i & 1] = *(const f32x4*)(lds + sro + i * 64 + 256); }
      SCHED();
#pragma unroll
      for (int j = BFK; j < (BFN ? 7 : BFK); ++j) bf[j] = BFRAG((QS + 4) % 10, j);
      SCHED();
#pragma unroll
      for (int m = 0; m < 2; ++m) {
        u32x4 o;
#pragma unroll
        for (int e = 0; e < 4; ++e) {
          const f32x4 a = sa[m][e >> 1], b = sb[m][e >> 1];
          const f32x2 x = __builtin_elementwise_fma(zv[m][e], (f32x2){a[2 * (e & 1)], a[2 * (e & 1) + 1]}, (f32x2){b[2 * (e & 1)], b[2 * (e & 1) + 1]});
          o[e] = pack2(x[0], x[1]);
        }
        ST16S(o, rs, zo + (unsigned)m * 256u, zos, SC1);
      }
    }
    SCHED();
    __builtin_amdgcn_s_setprio(0);
    STAMPT(13);
#pragma unroll
    for (int j = BFK; j < (BFN ? BFK : 7); ++j) bf[j] = BFRAG((QS + 4) % 10, j);
    if (kh1) req2();
    z1(IC<QS>(), IC<10>());
    f32x2 mine[7];
    if (kh1) {
#pragma unroll
      for (int j = 0; j < 7; ++j) { *(f32x2*)(lds + rdo + L_RED + j * 512) = (f32x2){acc[j][0], acc[j][1]}; mine[j] = (f32x2){acc[j][2], acc[j][3]}; }
    } else {
#pragma unroll
      for (int j = 0; j < 7; ++j) { *(f32x2*)(lds + rdo + L_RED + j * 512) = (f32x2){acc[j][2], acc[j][3]}; mine[j] = (f32x2){acc[j][0], acc[j][1]}; }
    }
    if (!kh1) req2();
#pragma unroll
    for (int g = TR1; g < TR2; ++g) WSP_TR(g);
    if (FQ > 9) VMW(WE - WD + WZ);
    BAR();
    if (FQ > 9 && w == 3) flag4(WS_FZ + (unsigned)(c * 32 + R) * 128u);
    STAMP(6);
    auto req = [&](auto G0_, auto G1_) __attribute__((always_inline)) {
#pragma unroll
      for (int k = WE; k < 32; ++k) LDW(k);
      SCHED();
      sfor<decltype(G0_)::value, decltype(G1_)::value>([&](auto G_) __attribute__((always_inline)) {
        constexpr int i0 = 4 * decltype(G_)::value;
        WOD(i0 < 68 ? i0 : i0 + 8 * WZ, wx4); SCHED();
      });
    };
#define IL 1
#define XF 12
#define XC 18
    static_assert(!IL || (WZ == 2 && WE == 32), "");
    auto wo8 = [&](auto K_) __attribute__((always_inline)) {
      constexpr int k = decltype(K_)::value;
      if (k < 12) WOD(k < 3 ? 8 * k : k < 8 ? 8 * k + 4 : 8 * k + 20, wx8);
      else if (k == 12) dma1k(rs, lds + (kh1 ? WOA(116) : WOA(24)) + wx4, l16, wgb + (kh1 ? 116u : 24u) * 1024u + wx4);
      SCHED();
    };
    if (IL) { wo8(IC<0>()); wo8(IC<1>()); }
    if (!IL && kh1) req(IC<0>(), IC<WY>());
    u32x4 gv[8];
    auto gb_ld = [&]() {
#pragma unroll
      for (int k = 0; k < 8; ++k) gv[k] = __builtin_amdgcn_raw_buffer_load_b128(rs, l < 40 ? l16 : 0u, WS_MODQ + (unsigned)(k * 2560 + 1920 + 80 * c) * 8u, SC1);
    };
    if (w == 2) gb_ld();
    f32x2 z[7];
    {
      f32x2 oth[7];
      f32x4 cc[7];
#pragma unroll
      for (int j = 0; j < 7; ++j) {
        oth[j] = *(const f32x2*)(lds + oto + L_RED + j * 512);
        cc[j] = *(const f32x4*)(lds + L_CONST + (16 * j + 4 * h4 + 2 * kh1) * 8);
      }
      SCHED();
#pragma unroll
      for (int j = 0; j < 7; ++j)
        z[j] = __builtin_elementwise_fma(__builtin_elementwise_fma((f32x2){-mu_n, -mu_n}, (f32x2){cc[j][0], cc[j][1]}, oth[j] + mine[j]),
                                         (f32x2){rs_n, rs_n}, (f32x2){cc[j][2], cc[j][3]});
    }
    if (IL) {
#pragma unroll
      for (int j = 0; j < 7; ++j) asm volatile("" : "+v"(z[j]));
      wo8(IC<2>());
    }
    gelu_n(z, IC<7>(), [&](auto K_) __attribute__((always_inline)) { if (IL) wo8(IC<3 + decltype(K_)::value>()); });
#pragma unroll
    for (int j = 0; j < 7; ++j) z1d[j] = pack2(z[j][0], z[j][1]);
    asm volatile("" : "+v"(z1d[0]), "+v"(z1d[1]), "+v"(z1d[2]), "+v"(z1d[3]), "+v"(z1d[4]), "+v"(z1d[5]), "+v"(z1d[6]));
    SCHED();
    STAMPT(30);
    if (w == 2) {
      auto bad = [&]() {
        unsigned d = 0u;
#pragma unroll
        for (int k = 0; k < 8; ++k) d |= ne2(gv[k][0], gv[k][2]);
        return __any(d != 0u);
      };
      if (bad()) {
        _Pragma("unroll 1") for (unsigned spins = 0; spins < SPIN_CAP; ++spins) {
          __builtin_amdgcn_s_sleep(8);
          gb_ld();
          if (!bad()) break;
        }
      }
      f32x2 t[4];
#pragma unroll
      for (int k = 0; k < 4; ++k) t[k] = (f32x2){__uint_as_float(gv[2 * k][1]) + __uint_as_float(gv[2 * k + 1][1]), __uint_as_float(gv[2 * k][3]) + __uint_as_float(gv[2 * k + 1][3])};
      *(f32x2*)(lds + L_MISC + l * 8) = rbf2((t[0] + t[1]) + (t[2] + t[3]));
    }
    if (!IL && !kh1) req(IC<WY>(), IC<30 - 2 * WZ>());
    const unsigned fo[1] = {l < 4 ? fzs + (unsigned)l * 128u : ~0u};
#define FPY 1
#define FPO FPY
#define FSL 1
    unsigned fv;
    sfor<4 * TR2, 32>([&](auto X_) __attribute__((always_inline)) {
      constexpr int x = decltype(X_)::value;
      wo8(IC<8 + x - 4 * TR2>());
      if (x == XF) { fv = ld4(rs, fo[0]); SCHED(); }
      if (x == XC) {
        STAMP(14);
        if (__builtin_amdgcn_ballot_w64(fv == tag) != 15ull) wait_lines<1, FSL>(rs, fo, tag);
        STAMPT(23);
      }
      if (x >= XC && x - XC < NW) { LDZ((x - XC) & 15); SCHED(); }
      WSP_TX(x);
      SCHED();
    });
#pragma unroll
    for (int f = 32 - XC; f < NW; ++f) { LDZ(f); SCHED(); }
  }

#define WO2 15
#define TRC (IL ? 8 : TR3)
  {
    SCHED();
#pragma unroll
    for (int g = TRC; g < 8; ++g) WSP_TR(g);
    SCHED();
    STAMP(20);
    const int rt = rt1, kh = kh1;
    const unsigned rw = (unsigned)(L_RED + w * 512);
    unsigned rr = (unsigned)(L_RED + w * 4096 + l * 4);
    asm volatile("; m0 is set here, not in front of the first store (one wait state)" : "+v"(rr) : "{m0}"(rw));
    const float bs = bflo(bs_raw);
    unsigned long long hm = 0xffff000000000000ull;
    asm volatile("" : "+s"(hm));
    unsigned l8;
    f32x4 acc2[2][4];
    float rv[8][2];
    u32x4 ud;
    u32x4 P[2][8];
    u32x2 xp[3];
    auto ld_p = [&](int pt, int i) { P[pt][i] = __builtin_amdgcn_raw_buffer_load_b128(rs, l16 + (unsigned)pt * 1024u, ui + (unsigned)i * 2048u, SC1); };
#define RED_PUT1(j, i) asm volatile("ds_write_addtid_b32 %0 offset:%2" :: "v"(acc2[(j) & 1][((i) >> 2) & 3][(i) & 3]), "{m0}"(rw), "n"((((i) >> 2) & 3) * 4096 + (((i) >> 1) & 1) * 16384 + ((i) & 1) * 256) : "memory")
#define RED_PUT(j, r) do { RED_PUT1(j, 4 * (r)); RED_PUT1(j, 4 * (r) + 1); RED_PUT1(j, 4 * (r) + 2); RED_PUT1(j, 4 * (r) + 3); } while (0)
#define RED_GET(i) do { f32x2 x_; asm volatile("ds_read2st64_b32 %0, %1 offset0:%2 offset1:%3" : "=v"(x_) : "v"(rr), "n"(2 * (i)), "n"(2 * (i) + 1) : "memory"); rv[i][0] = x_[0]; rv[i][1] = x_[1]; } while (0)
    float zz[2], gg[2];
    unsigned cs;
    auto sum_rows_u = [&](unsigned v) {
      auto a = __builtin_amdgcn_permlane16_swap(v, v, false, false);
      v = a[0] + a[1];
      auto b = __builtin_amdgcn_permlane32_swap(v, v, false, false);
      return b[0] + b[1];
    };
    auto red_fin = [&](int j, int k, bool fuse = false) {
      const int e = k & 1, i = k >> 1;
#define VADD(d, x, y) asm volatile("v_add_f32 %0, %1, %2" : "=v"(d) : "v"(x), "v"(y))
      if (i < 4) VADD(rv[2 * i][e], rv[2 * i][e], rv[2 * i + 1][e]);
      if (i == 4 || i == 5) VADD(rv[4 * (i & 1)][e], rv[4 * (i & 1)][e], rv[4 * (i & 1) + 2][e]);
      if (i == 6) VADD(rv[0][e], rv[0][e], bs);
      if (i == 7) VADD(gg[e], rv[0][e], rv[4][e]);
      if (i == (fuse ? 9 : 8)) zz[e] = e ? bfhi(z1d[(j + 6) % 7]) : bflo(z1d[(j + 6) % 7]);
      if (i == 9) zz[e] *= gg[e];
      if (i == 9 || (i == 8 && !fuse)) asm volatile("" : "+v"(zz[e]));
      if (k == 20) { ud[j & 3] = pack2(zz[0], zz[1]); asm volatile("" : "+v"(ud[j & 3])); }
      if (k == 21) {
        if (j == 0) asm volatile("v_cndmask_b32 %0, %0, 0, %1" : "+v"(ud[0]) : "s"(hm));
        if (j == 2) asm volatile("v_add3_u32 %0, %1, %2, %3" : "=v"(cs) : "v"(ud[0]), "v"(ud[1]), "v"(ud[2]));
        if (j == 3) {
          cs = tag - sum_rows_u(cs + ud[3]);
          asm volatile("v_cndmask_b32 %0, %0, %1, %2" : "+v"(ud[0]) : "v"(cs), "s"(hm));
        }
        if (j >= 4) { ud[3] = (j == 4 ? tag : ud[3]) - ud[j & 3]; asm volatile("" : "+v"(ud[3])); }
        if (j % 3 == 0 && j) ST16S(ud, rs, l16 + (unsigned)(j / 6) * 1024u, uo, SC1);
      }
    };
    struct Sch { int b1, b2, wp[16], rp[8], vp[22], lp[8], pr[32]; };
    constexpr Sch S = {18, 28, {0, 1, 2, 3, 4, 5, 6, 7, 8, 9, 10, 11, 12, 12, 15, 15}, {19, 19, 20, 22, 23, 23, 24, 24}, {29, 29, 30, 30, 31, 31, 32, 33, 35, 36, 37, 38, 39, 40, 41, 42, 43, 46, 43, 46, 48, 48},
                       {1, 1, 1, 1, 1, 1, 1, 1}, {0, 0, 2, 1, 0, 0, 0, 0, 0, 0, 2, 2, 0, 0, 0, 0, 0, 0, 0, 1, 0, 0, 0, 1, 0, 0, 2, 1, 0, 0, 0, 0}};
    constexpr Sch F = {15, 23, {0, 1, 1, 2, 3, 4, 4, 5, 6, 7, 8, 9, 10, 11, 12, 12}, {15, 16, 16, 17, 17, 18, 18, 19}, {23, 23, 24, 24, 25, 26, 26, 27, 27, 28, 28, 29, 30, 30, 31, 31, 22, 22, 32, 33, 34, 35},
                       {1, 2, 1, 1, 1, 1, 1, 1}, {0, 2, 1, 0, 0, 1, 1, 0, 0, 2, 1, 0, 0, 2, 1, 0, 0, 2, 1, 0, 0, 2, 1, 0, 0, 2, 1, 0, 0, 2, 1, 0}};
#define VM 3
#define JF 4
#define RO 3
#define XE 1
#define CE 0
#define AP0 2
#define P1B 2
#define P1S 3
    unsigned wb[3], xo;
    const unsigned wl[3] = {(unsigned)(120 + WO3 + w) * 1024u, (unsigned)(128 + WO3 + w) * 1024u, wl2}, wg = wgb - (unsigned)(WO3 * 1024);
#define NAF 32
#define NA0 16
#define NA1 NA0
    u32x4 af[NAF];
#define LDA(m) af[(m) % NAF] = *(const u32x4*)(lds + wb[WOA(2 * (m)) >> 16] + (WOA(2 * (m)) & 65535))
    unsigned ck[2];
    const unsigned tag4 = tag * 4u;
    auto p_chk = [&](int pt, int pr, int k) {
      u32x4* q = P[pt] + 4 * pr;
      if (k < 2) ck[k & 1] = q[k & 1][0] + q[k & 1][1];
      else if (k < 8) ck[k & 1] = ck[k & 1] + q[(k & 1) + 2 * ((k >> 2) & 1)][k & 2] + q[(k & 1) + 2 * ((k >> 2) & 1)][(k & 2) + 1];
      else if (k == 8) ck[0] = pt ? ck[0] + ck[1] : sum_rows_u(ck[0] + ck[1]);
      if (k < 9) asm volatile("" : "+v"(ck[k & 1]));
      if (k == 9) {
        if (__builtin_expect(__any(ck[0] != tag4), 0)) {
          _Pragma("unroll 1") for (unsigned spins = 0; spins < SPIN_CAP; ++spins) {
            unsigned d = 0u;
#pragma unroll
            for (int i = 0; i < 4; ++i) ld_p(pt, 4 * pr + i);
#pragma unroll
            for (int i = 0; i < 4; ++i) d += q[i][0] + q[i][1] + q[i][2] + q[i][3];
            if (!pt) d = sum_rows_u(d);
            if (!__any(d != tag4)) break;
            __builtin_amdgcn_s_sleep(1);
          }
        }
      }
      if (k >= 10 && !pt) { unsigned x = q[(k - 10) & 3][0]; asm volatile("v_cndmask_b32 %0, %0, 0, %1" : "+v"(x) : "s"(hm)); q[(k - 10) & 3][0] = x; }
      if (k >= 10 && k < 13 && pt) { q[(k - 10) % 3][3] = q[3][(k - 10) % 3]; asm volatile("" : "+v"(q[(k - 10) % 3])); }
    };
    sfor<0, 224>([&](auto M_) __attribute__((always_inline)) {
      constexpr int mm = decltype(M_)::value, j = mm >> 5, m = mm & 31, y = m - (32 - 4 * RO);
      constexpr int t = j == 6 && y >= 0 ? 8 - RO + (y % (2 * RO + !RO)) / 2 : m >> 2, r = j == 6 && y >= 0 ? (y & 1) + 2 * (y / (2 * RO + !RO)) : m & 3, f = 8 * j + t;
      constexpr Sch C = j < JF ? S : F, B = j <= JF ? S : F;
      if (t == 0) asm volatile("v_mfma_f32_16x16x32_bf16 %0, %1, %2, 0" : "=&v"(acc2[j & 1][r]) : "v"(zw[f % NW]), "v"(wsp[r][t]), "v"(zw[(f | (f < 8 ? 1 : VM)) % NW]));
      else asm volatile("v_mfma_f32_16x16x32_bf16 %0, %1, %2, %0" : "+v"(acc2[j & 1][r]) : "v"(zw[f % NW]), "v"(wsp[r][t]), "v"(zw[(f | (f < 8 ? 1 : VM)) % NW]));
      SCHED();
      if (C.pr[m] == 1) __builtin_amdgcn_s_setprio(1);
      if ((m == C.b1 && j > 0) || (m == C.b2 && j > 0) || mm == 30) { asm volatile("" ::: "memory"); __builtin_amdgcn_s_waitcnt(0xC07F); RBAR(); SCHED(); }
      sfor<0, 16>([&](auto I_) __attribute__((always_inline)) {
        constexpr int i = decltype(I_)::value;
        if (j > 0 && C.wp[i] == m) { RED_PUT1(j + 1, i); SCHED(); }
        if (j > 0 && i < 8 && C.rp[i & 7] == m) { RED_GET(i & 7); SCHED(); }
      });
      if (j == 5 && m == 20) {
#pragma unroll
        for (int i = 0; i < 3; ++i) { wb[i] = (unsigned)(kh * 1024 + i * 65536) + l16; asm volatile("" : "+v"(wb[i])); }
      }
      if (XE && j == 5 && m == 13) {
        asm volatile("v_lshrrev_b32 %0, 1, %1" : "=v"(l8) : "v"(l16));
        xo = (l8 & 0x180u) + (unsigned)psi((l8 >> 3) & 15) * 8u;
        asm volatile("" : "+v"(xo));
      }
      if (RO && j == 6 && m >= 28) { RED_PUT1(6, 2 * (m - 28)); RED_PUT1(6, 2 * (m - 28) + 1); SCHED(); }
      if (CE == 1 && j == 6 && m >= 19 && m < 22) sfor<0, 3>([&](auto K_) __attribute__((always_inline)) { p_chk(0, 0, 3 * (m - 19) + decltype(K_)::value); SCHED(); });
      constexpr int ap[20] = {7, 8, 9, 10, 11, 13, 13, 14, 14, 19, 20, 21, 22, 23, 24, 25, 26, 27, 28, 29};
      sfor<0, NA0>([&](auto N_) __attribute__((always_inline)) { if (j == 6 && ap[AP0 + decltype(N_)::value] == m) { LDA(decltype(N_)::value); SCHED(); } });
      sfor<0, 22>([&](auto K_) __attribute__((always_inline)) {
        constexpr int k = decltype(K_)::value;
        if (j > 1 && B.vp[k] == m + 32) { red_fin(j > 1 ? j - 2 : 0, k, B.vp[16] == B.vp[18]); SCHED(); }
        if (j > 0 && C.vp[k] == m) { red_fin(j > 0 ? j - 1 : 0, k); SCHED(); }
      });
      if (C.pr[m] == 2) __builtin_amdgcn_s_setprio(0);
      if ((m & 3) == C.lp[m >> 2]) {
        if (f > 0 && f - 1 + NW < 56) LDZ(f - 1 + NW);
        if (j == 6 && (m >> 2) >= P1S) ld_p(0, ((m >> 2) - P1S) & 7);
        if (XE && j == 5 && m >= 20) xp[((m >> 2) - 5) % 3] = __builtin_amdgcn_raw_buffer_load_b64(rs, xo + (unsigned)((m >> 2) - 5) * 512u, xs, BULK_AUX);
        SCHED();
      }
    });
    STAMP(15);
#define NM0 0
#define OAUX 0
#define GM 19
#define MB 24
    f32x4 acc[5];
    f32x4 xf[3], gf[3];
    unsigned go, oo, eo, ho, tw, tr, o1, ta, tb;
    const unsigned s2 = (unsigned)(L_MISC + 128 * kh), s4 = s1 - (unsigned)kh * 1024u;
    unsigned long long om = kh ? ~0ull : 0xccccccccccccccccull;
#define PIN2(a, b) asm volatile("" : "+v"(a), "+v"(b))
    auto glue = [&](int k) {
      if (k == 0) { ta = l8 >> 5; tb = l16 & 48u; PIN2(ta, tb); }
      if (k == 1) { tr = __umul24(ta, 112u) + tb; oo = ta & 12u; PIN2(tr, oo); }
      if (k == 2) { tr += s1; oo += ta; PIN2(tr, oo); }
      if (k == 3) { oo = __umul24(oo, 1280u) + tb; ta = l8 >> 4; PIN2(oo, ta); }
      if (k == 4) { ta &= 24u; tb = l16 & 0xf0u; PIN2(ta, tb); }
      if (k == 5) { tw = __umul24(tb, 7u) + ta; go = (ta << 1) + s2; PIN2(tw, go); }
      if (k == 6) { tw += s1; asm volatile("v_cndmask_b32 %0, %1, -2.0, %2" : "=v"(o1) : "v"(oo), "s"(om)); asm volatile("" : "+v"(tw)); }
      if (k == 7) { eo = l16 + s3; ho = l16 + s4; PIN2(eo, ho); }
      if (k >= 8 && k < 14) {
        const unsigned x = xp[((k - 8) >> 1) % 3][k & 1];
        float lo = bflo(x), hi = bfhi(x);
        PIN2(lo, hi);
        xf[((k - 8) >> 1) % 3][2 * (k & 1)] = lo; xf[((k - 8) >> 1) % 3][2 * (k & 1) + 1] = hi;
      }
    };
    auto g3 = [&](auto M0_, auto M1_) __attribute__((always_inline)) {
      sfor<decltype(M0_)::value, decltype(M1_)::value>([&](auto M_) __attribute__((always_inline)) {
        constexpr int mi = decltype(M_)::value;
        constexpr int m = mi < 55 ? mi : mi < 64 ? 55 + 5 * ((mi - 55) / 3) + (mi - 55) % 3 : 58 + 5 * ((mi - 64) / 2) + (mi & 1), u2 = m / 5;
        const u32x4 fb = u2 < 8 ? P[0][u2 & 7] : P[1][u2 < 11 ? (u2 - 8) & 3 : (u2 - 7) & 7];
        if (m < 5) asm volatile("v_mfma_f32_16x16x32_bf16 %0, %1, %2, 0" : "=&v"(acc[m % 5]) : "v"(af[m % NAF]), "v"(fb));
        else asm volatile("v_mfma_f32_16x16x32_bf16 %0, %1, %2, %0" : "+v"(acc[m % 5]) : "v"(af[m % NAF]), "v"(fb));
        SCHED();
        if (mi == NM0) RBAR();
        if (mi >= NM0 && mi < NM0 + 3) dma1k(rs, lds + wl[(mi - NM0) % 3], l16, wg + wl[(mi - NM0) % 3]);
        if (mi >= 5 && mi < 19) p_chk(0, 1, mi - 5);
        if (mi >= GM && mi < GM + 14) glue(mi - GM);
        if (mi >= 47 && mi < 54) { p_chk(1, 1, 2 * (mi - 47)); SCHED(); p_chk(1, 1, 2 * (mi - 47) + 1); }
        if (!XE && mi == 3) { xo = (l8 & 0x180u) + (unsigned)psi((l8 >> 3) & 15) * 8u; asm volatile("" : "+v"(xo)); }
        if (!XE && mi >= 4 && mi < 7) xp[(mi - 4) % 3] = __builtin_amdgcn_raw_buffer_load_b64(rs, xo + (unsigned)(mi - 4) * 512u, xs, BULK_AUX);
        if (mi >= NM0 && mi < 40 && NA1 + mi - NM0 < 60) LDA(NA1 + mi - NM0);
        if (mi >= 66 && mi < 69) *(f32x4*)(lds + eo + (mi - 66) * 1024) = acc[(mi - 66) % 3];
        SCHED();
      });
    };
    __builtin_amdgcn_s_setprio(3);
#pragma unroll
    for (int k = 18; k < 22; ++k) red_fin(5, k);
    if (!XE) asm volatile("v_lshrrev_b32 %0, 1, %1" : "=v"(l8) : "v"(l16));
    if (RO) asm volatile("s_nop 2"); else { RED_PUT(6, 0); RED_PUT(6, 1); }
    RED_PUT(6, 2); RED_PUT(6, 3);
    red_fin(6, 16); red_fin(6, 17);
    SCHED();
#pragma unroll
    for (int i = 8 - P1S; i < 8 - P1S + P1B; ++i) ld_p(0, i);
    SCHED();
    BAR();
    sfor<0, 8>([&](auto I_) __attribute__((always_inline)) { RED_GET(decltype(I_)::value); });
    SCHED();
#pragma unroll
    for (int n = NA0; n < NA1; ++n) LDA(n);
#pragma unroll
    for (int i = 8 - P1S + P1B; i < 8; ++i) ld_p(0, i);
    SCHED();
#pragma unroll
    for (int k = 0; k < (CE == 2 ? 9 : 0); ++k) p_chk(0, 0, k);
    SCHED();
    sfor<0, 20>([&](auto K_) __attribute__((always_inline)) {
      constexpr int k = decltype(K_)::value;
      if (k < 8 && !(k & 1)) asm volatile("s_waitcnt lgkmcnt(%0)" :: "n"(6 - k + NA1 - NA0) : "memory");
      red_fin(6, k < 16 ? k : k + 2);
    });
    SCHED();
    __builtin_amdgcn_s_setprio(0);
    STAMPT(16);
#pragma unroll
    for (int k = CE ? 9 : 0; k < 14; ++k) p_chk(0, 0, k);
    SCHED();
    g3(IC<0>(), IC<MB>());
    STAMPT(22);
    DRAIN();
    RBAR();
    STAMPT(27);
#define MY 12
    if (MY == 0 || !kh) {
#pragma unroll
      for (int i = 0; i < 8; ++i) { ld_p(1, i); SCHED(); }
    }
    g3(IC<MB>(), IC<MB + MY>());
    if (MY > 0 && kh) {
#pragma unroll
      for (int i = 0; i < 8; ++i) { ld_p(1, i); SCHED(); }
    }
    STAMPT(26);
    g3(IC<MB + MY>(), IC<40>());
    STAMPT(24);
#pragma unroll
    for (int m = NA1 + 40 - NM0; m < 70; ++m) LDA(m);
#pragma unroll
    for (int x = 0; x < 3; ++x) gf[x] = *(const f32x4*)(lds + go + x * 64);
    SCHED();
    const rsrc_t ro = __builtin_amdgcn_make_buffer_rsrc((void*)p.out, 0, S_ * D_ * 2, 0x00020000);
    STAMPT(17);
#pragma unroll
    for (int k = 0; k < 14; ++k) p_chk(1, 0, k);
    asm volatile("s_nop 1");
    STAMPT(25);
    g3(IC<40>(), IC<70>());
    STAMPT(18);
    BAR();
    STAMPT(28);
    f32x4 o4[3];
    u32x4 ov[2];
#pragma unroll
    for (int j = 2; j >= 0; --j) o4[j] = *(const volatile LAS f32x4*)(lds + ho + j * 1024);
    asm volatile("" : "+v"(acc[2]), "+v"(acc[3]), "+v"(acc[4]));
    SCHED();
#pragma unroll
    for (int j = 2; j >= 0; --j) {
      const f32x4 v = __builtin_elementwise_fma(gf[j], o4[j] + acc[2 + j], xf[j]);
      *(volatile LAS u32x2*)(lds + tw + j * 32) = (u32x2){pack2(v[0], v[1]), pack2(v[2], v[3])};
      if (j < 2) ov[j] = *(const volatile LAS u32x4*)(lds + tr + j * 32);
    }
    ST16S(ov[1], ro, oo + 32u, os, OAUX);
    ST16S(ov[0], ro, o1, os, OAUX);
    if (bid == 0 && w == 5 && l8 == 0u) st4(rs, WS_EPOCH, tag);
  }
  STAMP(19);
}

extern "C" __device__ __attribute__((used, noinline, visibility("hidden"))) void mega_tail() {}

extern "C" void kernel_launch(void* const* d_in, const int* in_sizes, int n_in, void* d_out, int out_size,
                              void* d_ws, size_t ws_size, hipStream_t stream) {
  Params p;
  p.q = (const u16*)d_in[0];
  p.k = (const u16*)d_in[1];
  p.v = (const u16*)d_in[2];
  p.shift_k = (const u16*)d_in[3];
  p.ssm_k = (const u16*)d_in[4];
  p.ssm_D = (const u16*)d_in[5];
  p.w_in = (const u16*)d_in[7];
  p.w_sp = (const u16*)d_in[8];
  p.b_sp = (const u16*)d_in[9];
  p.w_out = (const u16*)d_in[10];
  p.cvec = (const u16*)d_in[11];
  p.w_mod = (const u16*)d_in[12];
  p.out = (u16*)d_out;
  p.ws = (char*)d_ws;
  mega<<<256, 512, 0, stream>>>(p);
}
```

```cpp
#include <hip/hip_runtime.h>
#include <stdint.h>

typedef unsigned short u16;
typedef __attribute__((ext_vector_type(8))) short bf16x8;
typedef __attribute__((ext_vector_type(4))) float f32x4;
typedef __attribute__((ext_vector_type(2))) float f32x2;
typedef __attribute__((ext_vector_type(4))) unsigned int u32x4;
typedef __attribute__((ext_vector_type(2))) unsigned int u32x2;
#define LAS __attribute__((address_space(3)))
typedef __amdgpu_buffer_rsrc_t rsrc_t;
typedef __attribute__((ext_vector_type(4))) short s16x4;

constexpr int S_ = 2048, D_ = 640, F_ = 1728, FH = 864, CB = 108;

constexpr unsigned WS_EPOCH = 0;
constexpr unsigned WS_FWI = 4096;
constexpr unsigned WS_FZ = WS_FWI + 384 * 128;
constexpr unsigned WS_FWO = WS_FZ + 256 * 128;
constexpr unsigned F4S = 896 * 128;
constexpr unsigned WS_CS = WS_FZ + 256 * 128;
constexpr unsigned WS_MODP = 212992;
constexpr unsigned WS_ST1 = 393216;
constexpr unsigned WS_PART = 524288;
constexpr unsigned WS_CSP = WS_PART + 262144;
constexpr unsigned WS_FX = WS_CSP + 655360;
constexpr unsigned WS_X1B = WS_FX + 16 * 4 * 160 * 128;
constexpr unsigned WS_QB = WS_X1B + 2621440;
constexpr unsigned WS_KV3 = WS_QB + 2621440;
constexpr unsigned WS_MODQ = WS_KV3 + 1310720;
constexpr unsigned WBLK = 143360;
constexpr unsigned WS_WINF = WS_MODQ + 245760;
constexpr unsigned WS_WOUTF = WS_WINF + 16 * WBLK;
constexpr unsigned WS_Z2F = WS_WOUTF + 8 * WBLK;
constexpr unsigned WS_UF = WS_Z2F + 8 * 64 * 7168;
constexpr unsigned WS_F4 = WS_UF + 32 * 4 * 8 * 4096;
constexpr unsigned WS_QCK = WS_F4 + 4 * F4S;
constexpr unsigned WS_END = WS_QCK + 131072;
constexpr unsigned WS_STAMP = 200u << 20;

constexpr int G_COPY = 4160;
constexpr int L_U = WBLK;
constexpr int U_CH = (256 + S_) * 2 + 32;
constexpr int L_FLG = L_U + 4 * U_CH;
constexpr int L_RT = 16384;
constexpr int L_CONST = WBLK;
constexpr int L_PST = L_CONST + 2048;
constexpr int L_Z = L_CONST + 5120;
constexpr int L_ST2 = L_Z + 14336;
constexpr int L_MISC = L_ST2 + 512;
constexpr int LDS_TOTAL = 163840;

#define AUX_T 610
#define RT2_T 360
#define CB_T 380
#define W_T 670
#define YG_T 870
#define KE_T 460
#define K_T 480
#define WA_T 580
constexpr int SC1 = 16, NT = 2;
constexpr unsigned SPIN_CAP = 1u << 16;

__device__ __forceinline__ float bflo(unsigned x) { return __uint_as_float(x << 16); }
__device__ __forceinline__ float bfhi(unsigned x) { return __uint_as_float(x & 0xffff0000u); }
__device__ __forceinline__ unsigned f2bf(float x) {
  __bf16 b = (__bf16)x;
  return (unsigned)__builtin_bit_cast(unsigned short, b);
}
__device__ __forceinline__ float rbf(float x) { return bflo(f2bf(x)); }
typedef __attribute__((ext_vector_type(2))) __bf16 bf2_t;
__device__ __forceinline__ unsigned pack2(float lo, float hi) {
  f32x2 v = {lo, hi};
  return __builtin_bit_cast(unsigned, __builtin_convertvector(v, bf2_t));
}
__device__ __forceinline__ f32x2 rbf2(f32x2 v) {
  const unsigned p = __builtin_bit_cast(unsigned, __builtin_convertvector(v, bf2_t));
  return (f32x2){__uint_as_float(p << 16), __uint_as_float(p & 0xffff0000u)};
}
__device__ __forceinline__ f32x4 rbf4(f32x4 v) {
  const f32x2 a = rbf2((f32x2){v[0], v[1]}), b = rbf2((f32x2){v[2], v[3]});
  return (f32x4){a[0], a[1], b[0], b[1]};
}
__device__ __forceinline__ f32x4 fma4(f32x4 a, f32x4 b, f32x4 c) {
  float r[4];
  asm("v_fma_f32 %0, %4, %8, %12\n\tv_fma_f32 %1, %5, %9, %13\n\tv_fma_f32 %2, %6, %10, %14\n\tv_fma_f32 %3, %7, %11, %15"
      : "=&v"(r[0]), "=&v"(r[1]), "=&v"(r[2]), "=&v"(r[3])
      : "v"(a[0]), "v"(a[1]), "v"(a[2]), "v"(a[3]), "v"(b[0]), "v"(b[1]), "v"(b[2]), "v"(b[3]), "v"(c[0]), "v"(c[1]), "v"(c[2]), "v"(c[3]));
  return (f32x4){r[0], r[1], r[2], r[3]};
}
template <int CTRL>
__device__ __forceinline__ float dpp_f(float v) {
  return __int_as_float(__builtin_amdgcn_update_dpp(0, __float_as_int(v), CTRL, 0xf, 0xf, true));
}
__device__ __forceinline__ float sum8(float v) {
  v += dpp_f<0xB1>(v);
  v += dpp_f<0x4E>(v);
  v += dpp_f<0x141>(v);
  return v;
}
__device__ __forceinline__ float sum16(float v) {
  v = sum8(v);
  v += dpp_f<0x140>(v);
  return v;
}
__device__ __forceinline__ float sum_rows(float v) {
  auto a = __builtin_amdgcn_permlane16_swap(__float_as_uint(v), __float_as_uint(v), false, false);
  v = __uint_as_float(a[0]) + __uint_as_float(a[1]);
  auto b = __builtin_amdgcn_permlane32_swap(__float_as_uint(v), __float_as_uint(v), false, false);
  return __uint_as_float(b[0]) + __uint_as_float(b[1]);
}
#define MFMA16(a, b, c) __builtin_amdgcn_mfma_f32_16x16x32_bf16(__builtin_bit_cast(bf16x8, a), __builtin_bit_cast(bf16x8, b), c, 0, 0, 0)
#define MFMA_IP(c, a, b) asm volatile("v_mfma_f32_16x16x32_bf16 %0, %1, %2, %0" : "+v"(c) : "v"(a), "v"(b))
__device__ __forceinline__ u32x2 trd(const char* p) {
  return __builtin_bit_cast(u32x2, __builtin_amdgcn_ds_read_tr16_b64_v4i16((LAS s16x4*)p));
}
__device__ __forceinline__ u32x4 trfrag(const char* p, int pitch) {
  u32x2 a = trd(p), b = trd(p + 16 * pitch);
  return (u32x4){a[0], a[1], b[0], b[1]};
}
#define BAR() asm volatile("s_waitcnt lgkmcnt(0)\n\ts_barrier" ::: "memory")
#define DRAIN() do { __builtin_amdgcn_s_waitcnt(0x0F70); asm volatile("" ::: "memory"); } while (0)
#define SCHED() __builtin_amdgcn_sched_barrier(0)
template <int N> struct IC { static constexpr int value = N; };
template <int I, int N, class F>
__device__ __forceinline__ void sfor(F&& f) {
  if constexpr (I < N) { f(IC<I>()); sfor<I + 1, N>(f); }
}

__device__ __forceinline__ u32x2 ld8(rsrc_t r, unsigned off) { return __builtin_amdgcn_raw_buffer_load_b64(r, off, 0, SC1); }
__device__ __forceinline__ unsigned ld4(rsrc_t r, unsigned off) { return __builtin_amdgcn_raw_buffer_load_b32(r, off, 0, SC1); }
__device__ __forceinline__ void st16(rsrc_t r, unsigned off, u32x4 v) { __builtin_amdgcn_raw_buffer_store_b128(v, r, off, 0, SC1); }
#define ST16S(v, r, voff, soff, aux) do { const u32x4 v_ = (v); __builtin_amdgcn_raw_buffer_store_b128(v_, r, voff, soff, aux); asm volatile("s_nop 1" :: "v"(v_)); } while (0)
__device__ __forceinline__ void st4(rsrc_t r, unsigned off, unsigned v) { __builtin_amdgcn_raw_buffer_store_b32(v, r, off, 0, SC1); }
__device__ __forceinline__ void dma1k(rsrc_t r, char* lds_dst, unsigned l16, unsigned soff) {
  __builtin_amdgcn_raw_ptr_buffer_load_lds(r, (LAS void*)lds_dst, 16, l16, soff, 0, SC1);
}

#define STAMP(i)
#define STAMPF(i)
#define STAMPT(i) STAMP(i)

template <int N, int SLP>
__device__ __forceinline__ void wait_lines(rsrc_t r, const unsigned (&off)[N], unsigned tag) {
  bool need[N];
#pragma unroll
  for (int i = 0; i < N; ++i) need[i] = off[i] != ~0u;
  _Pragma("unroll 1") for (unsigned spins = 0; spins < SPIN_CAP; ++spins) {
    unsigned v[N];
#pragma unroll
    for (int i = 0; i < N; ++i) {
      v[i] = tag;
      if (need[i]) v[i] = ld4(r, off[i]);
    }
    bool any = false;
#pragma unroll
    for (int i = 0; i < N; ++i) { need[i] = v[i] != tag; any = any || need[i]; }
    if (!__any(any)) break;
    __builtin_amdgcn_s_sleep(SLP);
  }
  asm volatile("" ::: "memory");
}

__device__ __forceinline__ int tau(int rt, int n) { return 32 * (rt >> 1) + 8 * (n >> 2) + 4 * (rt & 1) + (n & 3); }
__device__ __forceinline__ int psi(int n) { return 2 * (n & 7) + (n >> 3); }
#define QX_CM(d, s0, s1, P) "v_cndmask_b32_dpp " d ", " s0 ", " s1 ", vcc quad_perm:" P " row_mask:0xf bank_mask:0xf\n\t"
#define QX_BODY(M, P) \
  asm("s_mov_b32 vcc_lo, " M "\n\ts_mov_b32 vcc_hi, " M "\n\t" \
      QX_CM("%0", "%12", "%8", P) "v_xor_b32 %4, %8, %0\n\t" QX_CM("%1", "%13", "%9", P) "v_xor_b32 %5, %9, %1\n\t" \
      QX_CM("%2", "%14", "%10", P) "v_xor_b32 %6, %10, %2\n\t" QX_CM("%3", "%15", "%11", P) "v_xor_b32 %7, %11, %3" \
      : "=&v"(t[0]), "=&v"(t[1]), "=&v"(t[2]), "=&v"(t[3]), "=&v"(u[0]), "=&v"(u[1]), "=&v"(u[2]), "=&v"(u[3]) \
      : "v"(a[0]), "v"(a[1]), "v"(a[2]), "v"(a[3]), "v"(b[0]), "v"(b[1]), "v"(b[2]), "v"(b[3]) : "vcc")
template <int BIT>
__device__ __forceinline__ void quad_xchg3(u32x4& a, u32x4& b) {
  unsigned t[4], u[4];
  if (BIT == 0) QX_BODY("0x55555555", "[1,0,3,2]");
  else QX_BODY("0x33333333", "[2,3,0,1]");
  a = (u32x4){t[0], t[1], t[2], t[3]};
#pragma unroll
  for (int d = 0; d < 4; ++d) b[d] ^= (unsigned)__builtin_amdgcn_update_dpp(0, (int)u[d], BIT == 0 ? 0xB1 : 0x4E, 0xf, 0xf, true);
}
struct Params {
  const u16 *q, *k, *v, *shift_k, *ssm_k, *ssm_D, *w_in, *w_sp, *b_sp, *w_out, *cvec, *w_mod;
  u16* out;
  char* ws;
};

__global__ __launch_bounds__(512, 2) void mega(Params p) {
  __shared__ __attribute__((aligned(16))) char lds[LDS_TOTAL];
  {
    unsigned long long pc0;
    unsigned vt;
    asm volatile("v_readfirstlane_b32 vcc_lo, %3\n\ts_cmpk_lt_u32 %2, 192\n\ts_cbranch_scc1 4f\n\ts_cmpk_lt_u32 vcc_lo, 64\n\ts_cbranch_scc0 4f\n\t"
                 "s_lshr_b32 vcc_hi, %2, 3\n\ts_sub_u32 vcc_hi, vcc_hi, 24\n\ts_lshl_b32 vcc_hi, vcc_hi, 13\n\t"
                 "v_lshlrev_b32 %1, 7, %3\n\tv_add_u32 %1, vcc_hi, %1\n\ts_getpc_b64 %0\n.Lmega_here:\n\t"
                 "v_min_u32 %1, mega_tail-.Lmega_here-4, %1\n\ts_movk_i32 m0, 0x2000\n\ts_nop 1\n\tglobal_load_lds_dword %1, %0\n4:\n\t"
                 "s_cmpk_lt_u32 %2, 160\n\ts_cbranch_scc0 1f\n\ts_cmpk_lt_u32 vcc_lo, 256\n\ts_cbranch_scc1 3f\n\t"
                 "s_call_b64 vcc, .Ltouch1\n\ts_branch 3f\n1:\n\t"
                 "s_cmpk_lt_u32 vcc_lo, 256\n\ts_cbranch_scc1 2f\n\ts_cmpk_lt_u32 %2, 224\n\ts_cbranch_scc0 2f\n\ts_call_b64 vcc, .Ltouch3\n\ts_branch 3f\n"
                 "2:\n\ts_call_b64 vcc, .Ltouch2\n3:"
                 : "=&s"(pc0), "=&v"(vt) : "s"(blockIdx.x), "v"(threadIdx.x) : "vcc", "scc", "memory");
  }
  SCHED();
  const int tid = threadIdx.x, l = tid & 63;
  const int w = __builtin_amdgcn_readfirstlane(tid >> 6);
  const int bid = blockIdx.x, xcd = bid & 7, idx = bid >> 3;
#define TOUCH_AT(n) asm volatile("s_branch 1f\n.Ltouch" #n ":\n\ts_setpc_b64 vcc\n1:")
  char* const ws = p.ws;
  asm volatile("" ::"s"(p.q), "s"(p.k), "s"(p.v), "s"(p.shift_k), "s"(p.ssm_k), "s"(p.ssm_D), "s"(p.w_in), "s"(p.w_sp), "s"(p.b_sp),
               "s"(p.w_out), "s"(p.cvec), "s"(p.w_mod), "s"(p.out), "s"(p.ws));
  unsigned epoch, tag, t_start;
  unsigned long long t64;
  asm volatile("s_load_dword %0, %2, 0x0\n\ts_memrealtime %1" : "=&s"(epoch), "=&s"(t64) : "s"(ws));
#define TAG_READY() do { SCHED(); \
    asm volatile("s_waitcnt lgkmcnt(0)\n\ts_mul_i32 %0, %2, 0x9e3779b1\n\ts_add_i32 %0, %0, 0x9e3779b9\n\ts_mov_b64 %1, %3" \
                 : "=&s"(tag), "=&s"(t64r) : "s"(epoch), "s"(t64) : "scc"); t_start = (unsigned)t64r; SCHED(); } while (0)
  unsigned long long t64r;
  const rsrc_t rs = __builtin_amdgcn_make_buffer_rsrc(ws, 0, WS_END, 0x00020000);
  const unsigned l16 = (unsigned)l * 16u;
  STAMP(0);
  const bool is_conv = idx < 20;
  const int quad = xcd * 20 + idx;
  const int R = is_conv ? (xcd & 3) * 5 + (idx >> 2) : (xcd & 3) * 3 + 15 + (idx >> 2);
  const int c = (xcd >> 2) * 4 + (idx & 3);
  const unsigned fwi = WS_F4 + (unsigned)(xcd & 3) * F4S, fz = fwi + (WS_FZ - WS_FWI);
  const int n16 = l & 15, h4 = l >> 4;
  u32x4 wsp[4][8];
  const int rotg = ((idx >> 4) ^ (idx >> 1)) & 1, rotb = (idx >> 2) & 3;
  const rsrc_t rsp = __builtin_amdgcn_make_buffer_rsrc((void*)p.w_sp, 0, S_ * S_ * 2, 0x00020000);
  const unsigned wsp_vo = (unsigned)(((l >> 2) & 3) * 8 * 4096 + h4 * 64 + ((l + rotb) & 3) * 16);
#define LDW(k) wsp[((k) >> 2) & 3][((k) >> 4) * 4 + ((k) & 3)] = __builtin_amdgcn_raw_buffer_load_b128(rsp, wvo, \
    (unsigned)((R * 64 + 16 * (((k) >> 2) & 2) + 4 * (((k) >> 2) & 1) + ((k) & 3)) * 4096 + w * 512 + ((((k) >> 4) ^ rotg) * 256)), 0)
#define WSP_Q(g, k) wsp[(g) & 3][(((g) >> 2) & 1) * 4 + (k)]
#define WSP_TX(x) do { if (((x) & 2) == 0) quad_xchg3<0>(WSP_Q((x) >> 2, 2 * ((x) & 1)), WSP_Q((x) >> 2, 2 * ((x) & 1) + 1)); \
    else quad_xchg3<1>(WSP_Q((x) >> 2, (x) & 1), WSP_Q((x) >> 2, ((x) & 1) + 2)); \
    asm volatile("" : "+v"(WSP_Q((x) >> 2, 0)), "+v"(WSP_Q((x) >> 2, 1)), "+v"(WSP_Q((x) >> 2, 2)), "+v"(WSP_Q((x) >> 2, 3))); } while (0)
  const rsrc_t rwi = __builtin_amdgcn_make_buffer_rsrc((void*)p.w_in, 0, D_ * F_ * 2, 0x00020000);
  const rsrc_t rwo = __builtin_amdgcn_make_buffer_rsrc((void*)p.w_out, 0, FH * D_ * 2, 0x00020000);
  const rsrc_t rq = __builtin_amdgcn_make_buffer_rsrc((void*)p.q, 0, S_ * D_ * 2, 0x00020000);
  auto hold = [&](unsigned tt) { while ((unsigned)__builtin_amdgcn_s_memrealtime() - t_start < tt) __builtin_amdgcn_s_sleep(2); };
  auto ne2 = [&](unsigned x, unsigned y) { unsigned d; asm("v_bitop3_b32 %0, %1, %2, %3 bitop3:0x7e" : "=v"(d) : "v"(x), "v"(y), "s"(tag)); return d; };
  auto sum8u = [&](unsigned v) {
    v += (unsigned)__builtin_amdgcn_update_dpp(0, (int)v, 0xB1, 0xf, 0xf, true);
    v += (unsigned)__builtin_amdgcn_update_dpp(0, (int)v, 0x4E, 0xf, 0xf, true);
    return v + (unsigned)__builtin_amdgcn_update_dpp(0, (int)v, 0x141, 0xf, 0xf, true);
  };
  volatile LAS unsigned* lf = (volatile LAS unsigned*)(lds + L_FLG);
  auto lf_set = [&](int i) { asm volatile("s_waitcnt lgkmcnt(0)" ::: "memory"); if (l == 0) lf[i] = tag; };
  auto lf_wait = [&](int i0, int n) {
    _Pragma("unroll 1") for (unsigned spins = 0; spins < (SPIN_CAP << 4); ++spins) {
      const unsigned v = lf[i0 + (l & (n - 1))];
      if (__all(v == tag)) break;
      __builtin_amdgcn_s_sleep(1);
    }
    asm volatile("" ::: "memory");
  };
  const unsigned b2o = fwi + (unsigned)((8 + c) * 20 + 5 * (w & 3) + (l < 5 ? l : 0)) * 128u;
  auto b2_wait = [&](u32x2 fv, int slp) {
    if (!__all(fv[0] == tag)) {
      _Pragma("unroll 1") for (unsigned spins = 0; spins < SPIN_CAP; ++spins) {
        if (fv[0] != tag) fv = ld8(rs, b2o);
        if (__all(fv[0] == tag)) break;
        if (slp) __builtin_amdgcn_s_sleep(8); else __builtin_amdgcn_s_sleep(4);
      }
    }
    asm volatile("" ::: "memory");
    if (l < 5) lf[44 + 5 * w + l] = fv[1];
    if (l == 0) lf[36 + w] = tag;
  };
  auto load_b2 = [&](int k0, int k1) {
    const unsigned a0 = (unsigned)(35 * w + (idx >> 2) * 4) * 1024u, gb = WS_WINF + (unsigned)(8 + c) * WBLK;
    unsigned a = a0, g = gb + a0;
#pragma unroll
    for (int k = k0; k < k1; ++k) {
      if (k == k0 || (k & 3) == 3) {
        a = k + (idx >> 2) * 4 >= 35 ? a0 - 35840u : a0;
        g = gb + a;
        asm volatile("" : "+s"(a), "+s"(g));
      }
      dma1k(rs, lds + a + k * 1024, l16, g + (unsigned)k * 1024u);
    }
  };
  char* const wo_st = lds + 122880;
  auto wo_ld = [&]() {
    const int U = R < 28 ? R : 0, u2 = U >> 1, fi = (u2 + 1) % 3;
    const int c0 = 4 * (U & 1) + (u2 < 8 ? u2 >> 1 : u2 < 11 ? 0 : 2);
    const int b0 = u2 < 8 ? CB * c0 + 2 * (u2 & 1) + 96 : CB * (c0 + (fi >> 1)) + 48 + 2 * (fi & 1), d1 = u2 < 8 ? -96 : 16, d2 = d1 + 16;
    const int b3 = u2 < 8 ? b0 - 64 : CB * (c0 + 1) + 50 + 16 * fi;
#pragma unroll
    for (int r = 0; r < 5; ++r) {
      const int g = r * 64 + l, rho = g / 10;
      const int fb = (rho & 16) ? ((rho & 2) ? b3 : b0 + d2) : ((rho & 2) ? b0 + d1 : b0);
      __builtin_amdgcn_raw_ptr_buffer_load_lds(rwo, (LAS void*)(wo_st + r * 1024), 16, (unsigned)((fb + (rho & 13)) * (D_ * 2) + (g % 10) * 16 + c * 160), 0, 0, 0);
    }
  };
  auto rt_ld = [&](int i0, char* st) {
    const int cb = (i0 < 160 ? 8 : 0) + (i0 & 7), Q = (i0 < 160 ? i0 : i0 - 160) >> 3;
#pragma unroll
    for (int r = 0; r < 7; ++r) {
      const int g = r * 64 + l;
      __builtin_amdgcn_raw_ptr_buffer_load_lds(rwi, (LAS void*)(st + r * 1024), 16, (unsigned)((g / 14) * (F_ * 2) + (g % 14) * 16 + Q * 32 * F_ * 2 + cb * CB * 2),
                                               0, 0, NT);
    }
  };
  auto rt_job = [&](int i0, char* st) {
    const int cb = (i0 < 160 ? 8 : 0) + (i0 & 7), Q = (i0 < 160 ? i0 : i0 - 160) >> 3;
    const int col = 640 * (l >> 5) + 32 * Q + (l & 31);
    u32x2 pp8[8];
    auto ld_pp = [&]() {
#pragma unroll
      for (int i = 0; i < 8; ++i) pp8[i] = __builtin_amdgcn_raw_buffer_load_b64(rs, (unsigned)col * 8u, WS_MODP + (unsigned)i * 10240u, SC1);
    };
    auto pp_bad = [&]() { return __any(((ne2(pp8[0][0], pp8[1][0]) | ne2(pp8[2][0], pp8[3][0]) | ne2(pp8[4][0], pp8[5][0])) | ne2(pp8[6][0], pp8[7][0])) != 0u); };
    hold(CB_T);
    ld_pp();
    SCHED();
    __builtin_amdgcn_s_waitcnt(0x0F78);
    asm volatile("" ::: "memory");
    u32x4 x[7], o[7];
    f32x2 xu[7][4];
    {
      const char* bp = st + (4 * h4 + (n16 >> 2)) * 224 + (n16 & 3) * 8;
#pragma unroll
      for (int j = 0; j < 7; ++j) x[j] = trfrag(bp + j * 32, 224);
#pragma unroll
      for (int j = 0; j < 7; ++j)
#pragma unroll
        for (int e = 0; e < 4; ++e) { xu[j][e] = (f32x2){bflo(x[j][e]), bfhi(x[j][e])}; asm volatile("" : "+v"(xu[j][e])); }
    }
    unsigned mk[4][2], cw = l >= 32 ? (unsigned)(7168 + (l - 32) * 4) : (unsigned)(7298 + l * 2 - 4), m12 = n16 == 12 ? ~0u : 0u;
    unsigned fgo = l < 32 ? (unsigned)(l >> 3) * F4S + (unsigned)(l & 7) * 16u : 0x80000000u;
#pragma unroll
    for (int i = 0; i < 4; ++i) {
      mk[i][0] = n16 == 4 * i ? 0x3F803F80u : 0u; mk[i][1] = n16 == 4 * i + 1 ? ~0u : 0u;
      asm volatile("" : "+v"(mk[i][0]), "+v"(mk[i][1]));
    }
    asm volatile("" : "+v"(cw), "+v"(m12), "+v"(fgo));
    unsigned ob = WS_WINF + (unsigned)cb * WBLK + (unsigned)(Q * 7) * 1024u;
    asm volatile("" : "+s"(ob));
    if (pp_bad()) {
      _Pragma("unroll 1") for (unsigned spins = 0; spins < SPIN_CAP; ++spins) {
        __builtin_amdgcn_s_sleep(4);
        ld_pp();
        if (!pp_bad()) break;
      }
    }
    STAMPF(9);
    __builtin_amdgcn_s_setprio(3);
    {
      auto pf = [&](int i) { return __uint_as_float(pp8[i][1]); };
      const float t = rbf(((pf(0) + pf(1)) + (pf(2) + pf(3))) + ((pf(4) + pf(5)) + (pf(6) + pf(7))));
      if (l >= 32) *(float*)(st + cw) = rbf(1.0f + t);
      else *(u16*)(st + cw + 2) = (u16)(__float_as_uint(t) >> 16);
    }
    asm volatile("" ::: "memory");
    const f32x4 ca = *(const volatile LAS f32x4*)(st + 7168 + h4 * 16), cc = *(const volatile LAS f32x4*)(st + 7168 + 64 + h4 * 16);
    const u32x2 e0 = *(const volatile LAS u32x2*)(st + 7296 + h4 * 8), e1 = *(const volatile LAS u32x2*)(st + 7328 + h4 * 8);
    SCHED();
#pragma unroll
    for (int j = 0; j < 7; ++j) {
      const f32x2 y0 = xu[j][0] * (f32x2){ca[0], ca[1]}, y1 = xu[j][1] * (f32x2){ca[2], ca[3]}, y2 = xu[j][2] * (f32x2){cc[0], cc[1]}, y3 = xu[j][3] * (f32x2){cc[2], cc[3]};
      o[j] = (u32x4){pack2(y0[0], y0[1]), pack2(y1[0], y1[1]), pack2(y2[0], y2[1]), pack2(y3[0], y3[1])};
      if (j == 6) {
#pragma unroll
        for (int e = 0; e < 4; ++e) o[j][e] = (o[j][e] & ~m12) | mk[3][0];
      }
      ST16S(o[j], rs, l16 + (unsigned)(j & 3) * 1024u, ob + (unsigned)(j >> 2) * 4096u, SC1);
      SCHED();
    }
    __builtin_amdgcn_s_setprio(0);
    STAMPF(11);
    f32x4 d[2] = {{0.f, 0.f, 0.f, 0.f}, {0.f, 0.f, 0.f, 0.f}};
#pragma unroll
    for (int j = 0; j < 7; ++j) {
      const unsigned one = mk[j & 3][0], sr = mk[j & 3][1];
      d[j >> 2] = MFMA16(((u32x4){one, one, one, one}), o[j], d[j >> 2]);
      d[j >> 2] = MFMA16(((u32x4){sr & e0[0], sr & e0[1], sr & e1[0], sr & e1[1]}), x[j], d[j >> 2]);
    }
#pragma unroll
    for (int m = 0; m < 2; ++m)
      st16(rs, WS_CSP + (unsigned)((cb * 20 + Q) * 2 + m) * 1024u + l16, (u32x4){tag, __float_as_uint(d[m][0]), tag, __float_as_uint(d[m][1])});
    STAMPF(13);
    u32x4 tg4 = {tag, t_start, tag, tag};
    asm volatile("s_waitcnt vmcnt(2)" : "+v"(tg4) :: "memory");
    ST16S(tg4, rs, fgo, WS_F4 + (unsigned)(cb * 20 + Q) * 128u, SC1);
  };

  const rsrc_t rwm = __builtin_amdgcn_make_buffer_rsrc((void*)p.w_mod, 0, D_ * 3840 * 2, 0x00020000);
  auto gemv_load = [&](u32x4* pre, int blk, int sl, int np) {
    pre[10][0] = *(const unsigned*)(p.cvec + 8 * np * sl + 2 * (l < 4 * np ? l : 0));
#pragma unroll
    for (int pp = 0; pp < np; ++pp)
      pre[pp] = __builtin_amdgcn_raw_buffer_load_b128(rwm, (unsigned)((l >> 3) * 7680 + (l & 7) * 16), (unsigned)((8 * np * sl + pp * 8) * 7680 + blk * 128), NT);
  };
  auto gemv_fin = [&](float* acc, const u32x4* pre, int np, char* scr) {
    const unsigned cv2 = pre[10][0];
    unsigned* svt = (unsigned*)scr + w * 96;
#pragma unroll
    for (int e = 0; e < 2; ++e) {
      const int row = 2 * l + e;
      const float cv = e ? bfhi(cv2) : bflo(cv2);
      if (l < 4 * np) svt[(row & 7) * 12 + (row >> 3)] = __float_as_uint(rbf(cv * __builtin_amdgcn_rcpf(1.0f + __expf(-cv))));
    }
    asm volatile("s_waitcnt lgkmcnt(0)" ::: "memory");
    u32x4 sv[3];
#pragma unroll
    for (int i = 0; i < (np + 3) / 4; ++i) sv[i] = *(const u32x4*)(svt + (l >> 3) * 12 + 4 * i);
    SCHED();
#pragma unroll
    for (int e = 0; e < 8; ++e) acc[e] = 0.f;
#pragma unroll
    for (int pp = 0; pp < np; ++pp) {
      const unsigned sh = sv[pp >> 2][pp & 3], sl2 = sh >> 16;
#pragma unroll
      for (int e = 0; e < 4; ++e) {
        const unsigned x = pre[pp][e];
        acc[2 * e] = __builtin_amdgcn_fdot2_f32_bf16(__builtin_bit_cast(bf2_t, x), __builtin_bit_cast(bf2_t, sl2), acc[2 * e], false);
        acc[2 * e + 1] = __builtin_amdgcn_fdot2_f32_bf16(__builtin_bit_cast(bf2_t, x), __builtin_bit_cast(bf2_t, sh), acc[2 * e + 1], false);
      }
    }
#pragma unroll
    for (int e = 0; e < 4; ++e) {
      auto x = __builtin_amdgcn_permlane32_swap(__float_as_uint(acc[e]), __float_as_uint(acc[e + 4]), false, false);
      acc[e] = __uint_as_float(x[0]) + __uint_as_float(x[1]);
    }
#pragma unroll
    for (int e = 0; e < 2; ++e) {
      auto x = __builtin_amdgcn_permlane16_swap(__float_as_uint(acc[e]), __float_as_uint(acc[e + 2]), false, false);
      acc[e] = __uint_as_float(x[0]) + __uint_as_float(x[1]);
    }
    const float t0 = acc[0] + dpp_f<0x128>(acc[0]), t1 = acc[1] + dpp_f<0x128>(acc[1]);
    acc[0] = (l & 8) ? t1 : t0;
  };
  auto gemv_put = [&](const float* acc, unsigned off) {
    const float v = acc[0];
    const int src = ((l & 3) * 16 + ((l >> 2) & 7)) * 4;
    const unsigned v0 = (unsigned)__builtin_amdgcn_ds_bpermute(src, (int)__float_as_uint(v)), v1 = (unsigned)__builtin_amdgcn_ds_bpermute(src + 32, (int)__float_as_uint(v));
    if (l < 32) st16(rs, off + l16, (u32x4){tag, v0, tag, v1});
  };
  auto cs_ld = [&](u32x4* v, int cj) {
    const int cb = cj / 7, j = cj % 7;
#pragma unroll
    for (int k = 0; k < 5; ++k)
      v[k] = __builtin_amdgcn_raw_buffer_load_b128(rs, (unsigned)(h4 * 10240 + n16 * 16), WS_CSP + (unsigned)((cb * 40 + (j >> 2) + 2 * k) * 1024 + (j & 3) * 256), SC1);
  };
  auto cs_fin = [&](u32x4* v, int cj) {
    auto bad = [&]() { return __any(((ne2(v[0][0], v[0][2]) | ne2(v[1][0], v[1][2]) | ne2(v[2][0], v[2][2])) | ne2(v[3][0], v[3][2]) | ne2(v[4][0], v[4][2])) != 0u); };
    if (bad()) {
      _Pragma("unroll 1") for (unsigned spins = 0; spins < SPIN_CAP; ++spins) {
        __builtin_amdgcn_s_sleep(8);
        cs_ld(v, cj);
        if (!bad()) break;
      }
    }
    float t0 = 0.f, t1 = 0.f;
#pragma unroll
    for (int k = 0; k < 5; ++k) { t0 += __uint_as_float(v[k][1]); t1 += __uint_as_float(v[k][3]); }
    t0 = sum_rows(t0); t1 = sum_rows(t1);
    if (l < 16) st16(rs, WS_CS + (unsigned)((cj / 7) * 2048 + (cj % 7) * 256) + l16, (u32x4){tag, __float_as_uint(t0), tag, __float_as_uint(t1)});
  };
  const bool cs2 = w == 2 && R >= 20 && R < 27, cs1 = w == 2 && R >= 25;
  u32x4 csv[5];
  typedef const __attribute__((address_space(4))) unsigned* cptr_t;

  if (__builtin_expect(is_conv, 1)) {
    if (w >= 4) {
      TOUCH_AT(1);
      const int pw = w - 4;
      const rsrc_t ra = __builtin_amdgcn_make_buffer_rsrc((void*)((idx & 2) ? p.v : p.k), 0, S_ * D_ * 2, 0x00020000);
      const rsrc_t rb = __builtin_amdgcn_make_buffer_rsrc((void*)((idx & 2) ? p.k : p.v), 0, S_ * D_ * 2, 0x00020000);
      const rsrc_t rk = __builtin_amdgcn_make_buffer_rsrc((void*)p.k, 0, S_ * D_ * 2, 0x00020000);
      const rsrc_t rv = __builtin_amdgcn_make_buffer_rsrc((void*)p.v, 0, S_ * D_ * 2, 0x00020000);
      const unsigned vo = (unsigned)((128 * pw + 2 * l) * (D_ * 2));
      const unsigned so0 = (unsigned)((idx & 1) * (D_ * 2) + quad * 8), so1 = (unsigned)((1 - (idx & 1)) * (D_ * 2) + quad * 8);
      u32x2 g[3][4], kh;
      auto gl = [&](int s, int j0, int j1) {
        __builtin_amdgcn_s_setprio(0);
#pragma unroll
        for (int j = j0; j < j1; ++j) {
          if (s < 2) g[s][j] = __builtin_amdgcn_raw_buffer_load_b64(j < 2 ? ra : rb, vo, ((j & 1) ? so1 : so0) + (unsigned)(s * 512 * D_ * 2), 0);
          else g[s][j] = __builtin_amdgcn_raw_buffer_load_b64(j < 2 ? rk : rv, vo + (unsigned)((j & 1) * D_ * 2), (unsigned)(quad * 8 + s * 512 * D_ * 2), 0);
          SCHED();
        }
        __builtin_amdgcn_s_setprio(3);
      };
      const cptr_t sp = (cptr_t)(p.shift_k + quad * 16);
      unsigned spv[8];
#pragma unroll
      for (int i = 0; i < 8; ++i) spv[i] = sp[i];
      __builtin_amdgcn_s_sleep(3);
      gl(0, 0, 4);
      {
        const int row = 512 * (l >> 2) + 128 * pw - 3 + (l & 3);
        kh = __builtin_amdgcn_raw_buffer_load_b64(rk, (l >= 12 || (l & 3) == 3 || row < 0) ? 0x80000000u : (unsigned)(row * D_ * 2 + quad * 8), 0, 0);
      }
      TAG_READY();
      f32x2 wt[2][4];
#pragma unroll
      for (int d = 0; d < 2; ++d) {
        wt[d][0] = (f32x2){bflo(spv[4 * d]), bflo(spv[4 * d + 2])}; wt[d][1] = (f32x2){bfhi(spv[4 * d]), bfhi(spv[4 * d + 2])};
        wt[d][2] = (f32x2){bflo(spv[4 * d + 1]), bflo(spv[4 * d + 3])}; wt[d][3] = (f32x2){bfhi(spv[4 * d + 1]), bfhi(spv[4 * d + 3])};
        asm volatile("" : "+s"(wt[d][0]), "+s"(wt[d][1]), "+s"(wt[d][2]), "+s"(wt[d][3]));
      }
      SCHED();
      {
        const int j = idx * 4 + pw;
        const unsigned o = (unsigned)((512 + 64 * (j >> 2) + l) * (D_ * 2) + (((160 * xcd) >> 7) + ((j >> 1) & 1)) * 128);
        __builtin_amdgcn_raw_ptr_buffer_load_lds((j & 1) ? ra : rb, (LAS void*)(lds + 98304), 4, j < 64 ? o : 0x80000000u, 0, 0, 0);
      }
      SCHED();
      gl(1, 0, 4);
      STAMP(21);
      unsigned up = (unsigned)(L_U + 512 + 256 * pw + (l >> 2) * 16 + (3 - (l & 3)) * 4);
      unsigned lfo = l == 0 ? (unsigned)(L_FLG + 4 * pw) : (unsigned)(100352 + tid * 4), lfr = (unsigned)(L_FLG + (l & 3) * 4);
      unsigned yr = (unsigned)(L_U + (n16 >> 2) * U_CH + (128 * pw + 32 * h4 + 8 * (n16 & 1) + 4 * ((n16 >> 1) & 1)) * 2);
      unsigned yo = (unsigned)(8 * pw + (l >> 3)) * 20480u + (unsigned)quad * 128u + (unsigned)(l & 7) * 16u;
      unsigned mo = (unsigned)((n16 < 12 ? n16 >> 2 : 0) * D_ + quad * 4 + (l & 3)) * 8u + (unsigned)h4 * 40960u;
      unsigned k3o = (unsigned)(16 * pw + (l >> 2)) * 10880u + (unsigned)((l & 3) * 16);
      unsigned k3d = (unsigned)(L_U + (l & 3) * U_CH + 512 + (1536 + 128 * pw + 8 * (l >> 2)) * 2);
      unsigned xfo = (unsigned)(((4 * (l >> 5) + pw) * 4 + ((l >> 3) & 3)) * 160 + quad) * 128u + (unsigned)(l & 7) * 16u;
      u32x4 tg4 = {tag, tag, tag, tag};
      asm volatile("" : "+v"(up), "+v"(lfo), "+v"(lfr), "+v"(yr), "+v"(yo), "+v"(mo), "+v"(k3o), "+v"(k3d), "+v"(xfo), "+v"(tg4));
      unsigned so = WS_ST1 + (unsigned)xcd * 16384u + (unsigned)pw * 1024u;
      asm volatile("" : "+s"(so));
      auto pf_set = [&](int i) { asm volatile("" ::: "memory"); *(volatile LAS unsigned*)(lds + lfo + 4 * i) = tg4[0]; };
      auto pf_wait = [&](int i0) {
        _Pragma("unroll 1") for (unsigned spins = 0; spins < (SPIN_CAP << 4); ++spins) {
          if (__all(*(volatile LAS unsigned*)(lds + lfr + 4 * i0) == tag)) break;
          __builtin_amdgcn_s_sleep(1);
        }
        asm volatile("" ::: "memory");
      };
      unsigned hl[2][3];
      auto halo = [&](int s) {
#pragma unroll
        for (int i = 0; i < 6; ++i) { hl[i & 1][i >> 1] = (unsigned)__builtin_amdgcn_readlane(kh[i & 1], 4 * s + (i >> 1)); asm volatile("" : "+v"(hl[i & 1][i >> 1])); }
      };
      auto kv = [&](auto S__, auto N__) {
        constexpr int s = decltype(S__)::value;
        u32x2* x = g[s];
        SCHED();
        asm volatile("s_waitcnt vmcnt(%5)" : "+v"(x[0]), "+v"(x[1]), "+v"(x[2]), "+v"(x[3]), "+v"(kh) : "n"(decltype(N__)::value));
        if (s == 2) STAMP(27);
        if (s == 0) halo(0);
        if (s < 2) {
          if (idx & 1) { asm volatile(""); const u32x2 t = x[0], u = x[2]; x[0] = x[1]; x[1] = t; x[2] = x[3]; x[3] = u; }
          asm volatile("" : "+v"(x[0]), "+v"(x[1]), "+v"(x[2]), "+v"(x[3]));
          if (idx & 2) { asm volatile(""); const u32x2 t = x[0], u = x[1]; x[0] = x[2]; x[1] = x[3]; x[2] = t; x[3] = u; }
          asm volatile("" : "+v"(x[0]), "+v"(x[1]), "+v"(x[2]), "+v"(x[3]));
        }
        f32x2 kf[2][5], o[2][2];
#pragma unroll
        for (int d = 0; d < 2; ++d) {
          unsigned kk[5];
          kk[3] = x[0][d]; kk[4] = x[1][d];
          kk[2] = __builtin_amdgcn_update_dpp(hl[d][2], kk[4], 0x138, 0xf, 0xf, false);
          kk[1] = __builtin_amdgcn_update_dpp(hl[d][1], kk[3], 0x138, 0xf, 0xf, false);
          kk[0] = __builtin_amdgcn_update_dpp(hl[d][0], kk[2], 0x138, 0xf, 0xf, false);
#pragma unroll
          for (int j = 0; j < 5; ++j) kf[d][j] = (f32x2){bflo(kk[j]), bfhi(kk[j])};
        }
#pragma unroll
        for (int t = 0; t < 5; ++t) {
#pragma unroll
          for (int i = 0; i < 4; ++i) {
            const int d = i >> 1, r = i & 1;
            if (t == 4) o[d][r] *= (f32x2){bflo(x[2 + r][d]), bfhi(x[2 + r][d])};
            else if (t == 0) o[d][r] = kf[d][r + 3] * wt[d][0];
            else o[d][r] = __builtin_elementwise_fma(kf[d][r + 3 - t], wt[d][t], o[d][r]);
          }
          asm volatile("" : "+v"(o[0][0]), "+v"(o[0][1]), "+v"(o[1][0]), "+v"(o[1][1]));
        }
#pragma unroll
        for (int i = 0; i < 4; ++i) *(unsigned*)(lds + up + i * U_CH + s * 1024) = pack2(o[i >> 1][1][i & 1], o[i >> 1][0][i & 1]);
        pf_set(4 * s);
        SCHED();
        if (s < 2) halo(s + 1);
        SCHED();
      };
      u32x4 sv[4], qv[4];
      u32x2 mv[2];
      auto ld_e = [&]() {
#pragma unroll
        for (int s = 0; s < 4; ++s) sv[s] = __builtin_amdgcn_raw_buffer_load_b128(rs, l16, so + (unsigned)s * 4096u, SC1);
        SCHED();
      };
      auto s_bad = [&]() { return __any(((ne2(sv[0][1], sv[0][3]) | ne2(sv[1][1], sv[1][3]) | ne2(sv[2][1], sv[2][3])) | ne2(sv[3][1], sv[3][3])) != 0u); };
      kv(IC<0>(), IC<5>());
      STAMP(23);
      gl(2, 0, 4);
      kv(IC<1>(), IC<4>());
      STAMPF(9);
      u32x4 k3;
      unsigned k3c;
      auto ld_k3 = [&]() {
        k3 = __builtin_amdgcn_raw_buffer_load_b128(rs, k3o, WS_KV3 + (unsigned)quad * 64u, SC1);
        k3c = __builtin_amdgcn_raw_buffer_load_b32(rs, k3o & ~63u, WS_KV3 + 10240u + (unsigned)quad * 4u, SC1);
      };
      auto ld_m = [&]() {
        mv[0] = __builtin_amdgcn_raw_buffer_load_b64(rs, mo, WS_MODQ, SC1);
        mv[1] = __builtin_amdgcn_raw_buffer_load_b64(rs, mo, WS_MODQ + 20480u, SC1);
      };
      auto k_bad = [&]() {
        unsigned d = (k3[0] + k3[1]) + (k3[2] + k3[3]);
        d += (unsigned)__builtin_amdgcn_update_dpp(0, (int)d, 0xB1, 0xf, 0xf, true);
        d += (unsigned)__builtin_amdgcn_update_dpp(0, (int)d, 0x4E, 0xf, 0xf, true);
        return __any(d + k3c != tag);
      };
      float r1[4][2], m1[4][2];
      auto ln_pre = [&]() {
#pragma unroll
        for (int i = 0; i < 8; ++i) {
          const unsigned x = sv[i >> 1][2 * (i & 1)];
          r1[i >> 1][i & 1] = __builtin_amdgcn_rcpf(bfhi(x));
          m1[i >> 1][i & 1] = -bflo(x) * r1[i >> 1][i & 1];
          asm volatile("" : "+v"(r1[i >> 1][i & 1]), "+v"(m1[i >> 1][i & 1]));
        }
      };
      unsigned qck[4], qco = (unsigned)((8 * pw + (l >> 3)) * 1024 + (quad / 40) * 256 + (quad % 40) * 4);
      asm volatile("" : "+v"(qco));
      auto ld_qc = [&]() {
#pragma unroll
        for (int s = 0; s < 4; ++s) {
          qv[s] = __builtin_amdgcn_raw_buffer_load_b128(rs, yo, WS_QB + (unsigned)s * (32u * 20480u), SC1);
          qck[s] = __builtin_amdgcn_raw_buffer_load_b32(rs, qco, WS_QCK + (unsigned)s * 32768u, SC1);
        }
        SCHED();
      };
      auto q_bad = [&]() {
        unsigned d = 0u;
#pragma unroll
        for (int s = 0; s < 4; ++s) d += (qv[s][0] + qv[s][1]) + (qv[s][2] + qv[s][3]);
        return __any(sum8u(d) + ((qck[0] + qck[1]) + (qck[2] + qck[3])) != tag * 4u);
      };
      const bool early = (unsigned)__builtin_amdgcn_s_memrealtime() - t_start < KE_T;
      ld_e();
      if (!early) hold(K_T);
      {
        const unsigned em = early ? 0x80000000u : 0u;
        k3o |= em; yo |= em; qco |= em;
        ld_k3();
        SCHED();
        ld_qc();
        k3o ^= em; yo ^= em; qco ^= em;
      }
      kv(IC<2>(), IC<14>());
      STAMPF(11);
      auto s_get = [&]() {
        if (s_bad()) {
          _Pragma("unroll 1") for (unsigned spins = 0; spins < SPIN_CAP; ++spins) {
            __builtin_amdgcn_s_sleep(4);
            ld_e();
            if (!s_bad()) break;
          }
        }
        ln_pre();
      };
      bool sok = false;
      if (early) {
        sok = !s_bad();
        hold(K_T);
        if (!sok) ld_e();
        ld_k3();
        SCHED();
        ld_qc();
        if (sok) ln_pre();
      }
      if (k_bad()) {
        _Pragma("unroll 1") for (unsigned spins = 0; spins < SPIN_CAP; ++spins) {
          __builtin_amdgcn_s_sleep(4);
          ld_k3();
          if (!k_bad()) break;
        }
      }
      *(u32x4*)(lds + k3d) = k3;
      pf_set(12);
      SCHED();
      STAMPF(18);
      ld_m();
      SCHED();
      if (!sok) s_get();
      if (q_bad()) {
        _Pragma("unroll 1") for (unsigned spins = 0; spins < SPIN_CAP; ++spins) {
          __builtin_amdgcn_s_sleep(4);
          ld_qc();
          if (!q_bad()) break;
        }
      }
      STAMPF(17);
      STAMP(2);
      u32x2 yv[4][2];
      auto ld_y = [&](int s) {
#pragma unroll
        for (int e = 0; e < 2; ++e) yv[s][e] = trd(lds + yr + 1024 * s + 32 * e);
      };
      f32x4 qf[4][2], P[4][2];
      auto xln = [&](int s) {
#pragma unroll
        for (int e = 0; e < 2; ++e) {
          const float r = r1[s][e], m = m1[s][e];
          const f32x4 qq = {bflo(qv[s][2 * e]), bfhi(qv[s][2 * e]), bflo(qv[s][2 * e + 1]), bfhi(qv[s][2 * e + 1])};
          qf[s][e] = qq;
          P[s][e] = fma4(qq, (f32x4){r, r, r, r}, (f32x4){m, m, m, m});
        }
        asm volatile("" : "+v"(qf[s][0]), "+v"(qf[s][1]), "+v"(P[s][0]), "+v"(P[s][1]));
      };
      xln(0); xln(1); xln(2); xln(3);
      pf_wait(20);
      ld_y(0); ld_y(1);
      SCHED();
      if (__any(ne2(mv[0][0], mv[1][0]) != 0u)) {
        _Pragma("unroll 1") for (unsigned spins = 0; spins < SPIN_CAP; ++spins) {
          __builtin_amdgcn_s_sleep(4);
          ld_m();
          if (!__any(ne2(mv[0][0], mv[1][0]) != 0u)) break;
        }
      }
      STAMPF(13);
      f32x4 al, be;
      {
        const float mgf = rbf(sum_rows(__uint_as_float(mv[0][1]) + __uint_as_float(mv[1][1])));
        auto rl = [&](int i) { return __uint_as_float(__builtin_amdgcn_readlane(__float_as_uint(mgf), i)); };
        const f32x4 sa = {rl(0), rl(1), rl(2), rl(3)}, ca = {rl(4), rl(5), rl(6), rl(7)}, ga = {rl(8), rl(9), rl(10), rl(11)};
        al = rbf4(ca + 1.0f) * ga;
        be = sa * ga;
        asm volatile("" : "+v"(al), "+v"(be));
      }
      auto xpre = [&](int s) {
#pragma unroll
        for (int e = 0; e < 2; ++e) P[s][e] = __builtin_elementwise_fma(P[s][e], al, be);
        asm volatile("" : "+v"(P[s][0]), "+v"(P[s][1]));
      };
      auto xfin = [&](int s) {
        u32x4 o;
#pragma unroll
        for (int e = 0; e < 2; ++e) {
          const u32x2 y = yv[s][e];
          const f32x4 yf = {bflo(y[0]), bfhi(y[0]), bflo(y[1]), bfhi(y[1])};
          const f32x4 x = __builtin_elementwise_fma(P[s][e], yf, qf[s][e]);
          o[2 * e] = pack2(x[0], x[1]);
          o[2 * e + 1] = pack2(x[2], x[3]);
        }
        ST16S(o, rs, yo, WS_X1B + (unsigned)s * (32u * 20480u), SC1);
      };
      xpre(0); xfin(0);
      xpre(1); xfin(1);
      SCHED();
      xpre(2); xpre(3);
      pf_wait(24);
      ld_y(2);
      xfin(2);
      SCHED();
      pf_wait(28);
      ld_y(3);
      xfin(3);
      STAMP(28);
      if (l == 0) lf[40 + pw] = tag;
      asm volatile("s_waitcnt vmcnt(2)" ::: "memory");
      ST16S(tg4, rs, xfo, WS_FX, SC1);
      SCHED();
      asm volatile("s_waitcnt vmcnt(1)" ::: "memory");
      ST16S(tg4, rs, xfo, WS_FX + 32u * 160u * 128u, SC1);
      STAMP(5);
    } else {
      const int ch = w;
      char* U = lds + L_U + ch * U_CH;
      char* G = lds + ch * (4 * G_COPY);
      {
        const rsrc_t rf = __builtin_amdgcn_make_buffer_rsrc((void*)(p.ssm_k + (size_t)(quad * 4 + ch) * S_), 0, S_ * 2, 0x00020000);
        u32x4 fa[4];
        u32x2 fb[4];
        __builtin_amdgcn_s_setprio(3);
#pragma unroll
        for (int k = 0; k < 4; ++k) {
          fa[k] = __builtin_amdgcn_raw_buffer_load_b128(rf, l16 + (unsigned)k * 1024u, 0, NT);
        }
        __builtin_amdgcn_s_setprio(1);
        TAG_READY();
#pragma unroll
        for (int k = 0; k < 4; ++k) {
#pragma unroll
          for (int d = 0; d < 2; ++d)
            fb[k][d] = __builtin_amdgcn_update_dpp(k < 3 ? (unsigned)__builtin_amdgcn_readfirstlane(fa[k < 3 ? k + 1 : k][d]) : 0u, fa[k][d], 0x130, 0xf, 0xf, false);
          const unsigned x[6] = {fa[k][0], fa[k][1], fa[k][2], fa[k][3], fb[k][0], fb[k][1]};
          unsigned y[5];
#pragma unroll
          for (int d = 0; d < 5; ++d) y[d] = __builtin_amdgcn_alignbit(x[d + 1], x[d], 16);
          char* gp = G + 32 + k * 1024 + l16;
          *(u32x4*)(gp) = fa[k];
          *(u32x4*)(gp + G_COPY) = (u32x4){y[0], y[1], y[2], y[3]};
          *(u32x4*)(gp + 2 * G_COPY) = (u32x4){x[1], x[2], x[3], x[4]};
          *(u32x4*)(gp + 3 * G_COPY) = (u32x4){y[1], y[2], y[3], y[4]};
          if (k == 0) {
            const unsigned f0 = __builtin_amdgcn_readfirstlane(x[0]), f1 = __builtin_amdgcn_readfirstlane(x[1]);
            const unsigned long long f64 = (unsigned long long)f0 | ((unsigned long long)f1 << 32);
            const unsigned long long hi = l ? (f64 << (64 - 16 * (l & 3))) : 0ull;
            if (l < 4) {
              *(u32x4*)(G + l * G_COPY) = (u32x4){0u, 0u, 0u, 0u};
              *(u32x4*)(G + l * G_COPY + 16) = (u32x4){0u, 0u, (unsigned)hi, (unsigned)(hi >> 32)};
            }
            if (l >= 32) *(u32x4*)(U + (l - 32) * 16) = (u32x4){0u, 0u, 0u, 0u};
          }
        }
        asm volatile("s_waitcnt lgkmcnt(0)" ::: "memory");
        STAMP(1);
      }
      const int a = n16, h = h4, jj = h >> 1, e2 = h & 1;
      const char* ap = G + ((a + 1) & 3) * G_COPY + 2 * (16 * jj + a - 8 * e2 + 9 - ((a + 1) & 3));
      unsigned bp = (unsigned)(L_U + ch * U_CH + 32 * (a - jj) + 16 * e2);
      auto lda = [&](u32x4* A, int r, int p0, int np) {
#pragma unroll
        for (int pp = 0; pp < np; ++pp) {
          u32x2 lo = *(const volatile LAS u32x2*)(ap + 512 * (p0 + pp) + 64 * r);
          u32x2 hi = *(const volatile LAS u32x2*)(ap + 512 * (p0 + pp) + 64 * r + 8);
          A[pp] = (u32x4){lo[0], lo[1], hi[0], hi[1]};
        }
      };
      u32x4 AD;
      {
        const unsigned dDr = (*(cptr_t)(p.ssm_D + quad * 4 + (ch & 2)) >> (16 * (ch & 1))) & 0xffffu;
        const unsigned dv = h == (a >> 3) ? ((a & 1) ? dDr : dDr << 16) : 0u;
        const int dw = (7 - (a & 7)) >> 1;
#pragma unroll
        for (int d = 0; d < 4; ++d) AD[d] = dw == d ? dv : 0u;
      }
      u32x4 A0[8][4];
      f32x4 acc[8];
#pragma unroll
      for (int n = 0; n < 8; ++n) acc[n] = (f32x4){0.f, 0.f, 0.f, 0.f};
#pragma unroll
      for (int r = 0; r < 8; ++r) lda(A0[r], r, 0, 4);
      auto yput = [&](int s) {
#pragma unroll
        for (int e = 0; e < 2; ++e)
          *(u32x2*)(U + 1024 * s + 512 * e + (16 * a + 4 * h) * 2) = (u32x2){pack2(acc[2 * s + e][0], acc[2 * s + e][1]), pack2(acc[2 * s + e][2], acc[2 * s + e][3])};
        asm volatile("" ::: "memory");
        if (l == 0) lf[16 + 4 * s + ch] = tag;
      };
      asm volatile("" : "+v"(AD), "+v"(bp), "+v"(acc[0]), "+v"(acc[1]), "+v"(acc[2]), "+v"(acc[3]), "+v"(acc[4]), "+v"(acc[5]), "+v"(acc[6]), "+v"(acc[7]));
      auto stage = [&](auto S__) {
        constexpr int s = decltype(S__)::value, NH = 4 - 2 * s, NP = 8 - 2 * s;
        u32x2 A1[3][NH][2];
        u32x4 B[2][2];
        auto lda1 = [&](int r, int i) { A1[r % 3][i >> 1][i & 1] = *(const volatile LAS u32x2*)(ap + 512 * (4 + (i >> 1)) + 64 * r + 8 * (i & 1)); };
        auto ldb = [&](int r, int e) { B[r & 1][e] = *(const u32x4*)(lds + bp + 512 * (2 * s + e + 1) - 64 * r); };
#pragma unroll
        for (int i = 0; i < 4 * NH; ++i) lda1(i / (2 * NH), i % (2 * NH));
        SCHED();
        if (s > 0) yput(s - 1);
        lf_wait(4 * s, 4);
        if (s == 0) STAMP(3);
#pragma unroll
        for (int i = 0; i < 4; ++i) ldb(i >> 1, i & 1);
        SCHED();
#pragma unroll
        for (int e = 0; e < 2; ++e) MFMA_IP(acc[2 * s + e], AD, B[0][e]);
        sfor<0, 8 * (2 * NP - 1)>([&](auto I_) __attribute__((always_inline)) {
          constexpr int r = decltype(I_)::value / (2 * NP - 1), i = decltype(I_)::value % (2 * NP - 1), e = i >= NP, pp = i - e * NP, h1 = pp < 4 ? 0 : pp - 4;
          const u32x4 a1 = {A1[r % 3][h1][0][0], A1[r % 3][h1][0][1], A1[r % 3][h1][1][0], A1[r % 3][h1][1][1]};
          const u32x4 fa = pp < 4 ? A0[r][pp & 3] : a1;
          MFMA_IP(acc[2 * s + e + pp], fa, B[r & 1][e]);
          SCHED();
          if (r + 2 < 8 && i == 2 * NP - 3) ldb(r + 2, 0);
          if (r + 2 < 8 && i == 2 * NP - 2) ldb(r + 2, 1);
          if (r + 2 < 8 && i < 2 * NH) lda1(r + 2, i % (2 * NH));
          SCHED();
        });
      };
      stage(IC<0>());
      STAMP(4);
      stage(IC<1>());
      STAMPF(9);
      u32x2 fv;
      {
        u32x4 B[8][2];
        auto ldb = [&](int r, int m0) {
#pragma unroll
          for (int m = 0; m < 2; ++m) B[r][m] = *(const u32x4*)(lds + bp + 512 * (m0 + m + 1) - 64 * r);
        };
        asm volatile("s_nop 1" : "+v"(acc[2]), "+v"(acc[3]));
        yput(1);
        if (w == 3) wo_ld();
        __builtin_amdgcn_s_setprio(3);
        lf_wait(8, 4);
        STAMP(22);
        auto ldb1 = [&](int i, int m0) { B[i >> 1][i & 1] = *(const u32x4*)(lds + bp + 512 * (m0 + (i & 1) + 1) - 64 * (i >> 1)); };
#pragma unroll
        for (int r = 0; r < 3; ++r) ldb(r, 4);
        SCHED();
#pragma unroll
        for (int m = 0; m < 2; ++m) MFMA_IP(acc[4 + m], AD, B[0][m]);
        sfor<0, 24>([&](auto I_) __attribute__((always_inline)) {
          constexpr int i = decltype(I_)::value, r = i / 3, k = i % 3;
          MFMA_IP(acc[4 + (k != 1)], A0[r][k == 0], B[r][k == 2]);
          SCHED();
          if (i < 10) ldb1(6 + i, 4);
          SCHED();
        });
        sfor<0, 32>([&](auto I_) __attribute__((always_inline)) {
          constexpr int i = decltype(I_)::value, r = i >> 2, k = i & 3;
          MFMA_IP(acc[6 + (k & 1)], A0[r][2 + (k & 1) - (k >> 1)], B[r][k >> 1]);
          SCHED();
          if (i == 7) yput(2);
          if (i == 20) fv = ld8(rs, b2o);
          if (i == 19) lf_wait(12, 4);
          if (i >= 19 && i < 29) ldb1(i - 19, 6);
          SCHED();
        });
#pragma unroll
        for (int m = 0; m < 2; ++m) MFMA_IP(acc[6 + m], AD, B[0][m]);
        sfor<0, 24>([&](auto I_) __attribute__((always_inline)) {
          constexpr int i = decltype(I_)::value, r = i / 3, k = i % 3;
          MFMA_IP(acc[6 + (k != 1)], A0[r][k == 0], B[r][k == 2]);
          SCHED();
          if (i < 6) ldb1(10 + i, 6);
          SCHED();
        });
        asm volatile("s_nop 7" : "+v"(acc[6]), "+v"(acc[7]));
        yput(3);
      }
      STAMP(28);
      __builtin_amdgcn_s_setprio(0);
      b2_wait(fv, 0);
      lf_wait(20, 4);
      DRAIN();
    }
    __builtin_amdgcn_s_setprio(0);
  } else {
    const int at = idx - 20;
    char* const scr = lds + WBLK + 14848;
    auto gv_blk = [&](int j) { return j < 160 ? 30 + (j >> 3) : j < 400 ? (j >> 3) - 20 : j >> 3; };
    auto gv_fin = [&](const u32x4* pre, int j) {
      float acc[8];
      gemv_fin(acc, pre, 10, scr);
      gemv_put(acc, j < 160 ? WS_MODP + (unsigned)((j & 7) * 1280 + (j >> 3) * 64) * 8u : WS_MODQ + (unsigned)((j & 7) * 2560 + ((j >> 3) - 20) * 64) * 8u);
    };
    const int ja = xcd * 12 + at, ai = xcd * 8 + at, bi = xcd * 4 + at - 8, g = w >> 2, wj = w & 3;
    const int rj = w == 0 ? ja : w == 1 ? (at >= 4 && at < 12 ? 92 + ai : -1) : w == 4 ? 160 + ja : w == 5 && at < 8 ? 256 + ai : -1;
    char* const rst = lds + (g ? WBLK : 65536) + wj * 7424;
    u32x4 pre[11];
    if (at >= 8 || g == 0) {
      TOUCH_AT(2);
      const int T = at >= 8 ? 64 + 2 * bi + g : ai;
      unsigned* mds = (unsigned*)(lds + 2048) + 16 * g;
      char* qt = lds + L_RT + g * (16 * 1312);
      u32x4 ld[5];
      const bool e1 = at >= 8 && w == 3;
      if (!e1) __builtin_amdgcn_s_sleep(8);
      auto q_ld = [&](auto N_) {
        asm volatile("; %0" :: "n"(decltype(N_)::value) : "memory");
#pragma unroll
        for (int j = 0; j < 5; ++j)
          ld[j] = __builtin_amdgcn_raw_buffer_load_b128(rq, (unsigned)(h4 * D_ * 2 + n16 * 16 + j * 256), (unsigned)((32 * (T >> 1) + 8 * wj + 4 * (T & 1)) * D_ * 2), NT);
        SCHED();
      };
      const int g2 = at < 8 || w < 2 || w == 4 || w == 3 ? 400 : w < 4 ? 384 + bi : 256 + wj * 32 + bi;
      SCHED();
      float sa = 0.f, va = 0.f;
      auto q_in = [&]() {
        const bf2_t ONE2 = {(__bf16)1.0f, (__bf16)1.0f};
        char* dst = qt + (4 * wj + h4) * 1312 + n16 * 16;
        SCHED();
#pragma unroll
        for (int j = 0; j < 5; ++j) {
          *(volatile LAS u32x4*)(dst + j * 256) = ld[j];
#pragma unroll
          for (int d = 0; d < 4; ++d) {
            const unsigned a0 = ld[j][d];
            const bf2_t xa = __builtin_bit_cast(bf2_t, a0);
            sa = __builtin_amdgcn_fdot2_f32_bf16(xa, ONE2, sa, false); va = __builtin_amdgcn_fdot2_f32_bf16(xa, xa, va, false);
          }
        }
      };
      u32x4 pre2[11];
      if (e1) {
        gemv_load(pre, gv_blk(128 + bi), bi & 7, 10);
        q_ld(IC<3>());
        TAG_READY();
        gv_fin(pre, 128 + bi);
        q_in();
        asm volatile("; d" : "+v"(sa), "+v"(va));
      } else if (g2 < 400) {
        q_ld(IC<0>());
        gemv_load(pre2, gv_blk(g2), g2 & 7, 10);
        q_in();
        asm volatile("; a" : "+v"(sa), "+v"(va));
      } else {
        q_ld(IC<2>());
        q_in();
        asm volatile("; c" : "+v"(sa), "+v"(va));
      }
      TAG_READY();
      {
        lf_set(8 + w);
        const float ma = sum16(sa) * (1.0f / D_);
        va = __builtin_fmaxf(sum16(va) * (1.0f / D_) - ma * ma, 0.f);
        if (n16 == 0) mds[4 * wj + h4] = pack2(ma, __builtin_amdgcn_sqrtf(rbf(rbf(va) + 1e-5f)));
      }
      lf_set(16 + w);
      lf_wait(8 + 4 * g, 4);
      STAMP(23);
      {
        const char* src = qt + (l & 7) * 1312 + (40 * wj + (l >> 3)) * 8;
        unsigned ckw = 0u;
        u32x2 qx[5][2];
#pragma unroll
        for (int j = 0; j < 5; ++j) { qx[j][0] = *(const volatile LAS u32x2*)(src + j * 64); qx[j][1] = *(const volatile LAS u32x2*)(src + j * 64 + 8 * 1312); }
        SCHED();
        if (wj == 2) {
          lf_wait(16 + 4 * g, 4);
          st16(rs, WS_ST1 + (unsigned)(l >> 3) * 16384u + (unsigned)T * 128u + (unsigned)(l & 7) * 16u, (u32x4){mds[l & 7], tag, mds[8 + (l & 7)], tag});
          STAMP(22);
        }
#pragma unroll
        for (int j = 0; j < 5; ++j) {
          const u32x2 x = qx[j][0], y = qx[j][1];
          st16(rs, WS_QB + (unsigned)T * 20480u + (unsigned)(5 * wj + j) * 1024u + l16, (u32x4){x[0], x[1], y[0], y[1]});
          const unsigned t = tag - sum8u((x[0] + x[1]) + (y[0] + y[1]));
          ckw = (l & 7) == j ? t : ckw;
        }
        char* sc = lds + 110592 + w * 256;
        *(volatile LAS unsigned*)(sc + ((l & 7) * 8 + (l >> 3)) * 4) = ckw;
        const u32x4 x = *(const volatile LAS u32x4*)(sc + n16 * 16);
        if (l < 16) st16(rs, WS_QCK + (unsigned)(T * 4 + wj) * 256u + l16, x);
      }
      SCHED();
      if (g2 >= 400 && w < 2 && rj >= 0 && rj < 160) rt_ld(rj, rst);
      if (w >= 4 && w < 7) lf_set(w);
      if (g2 < 400) gv_fin(pre2, g2);
      STAMP(25);
    } else {
      constexpr int KB = L_RT + 16 * 1312;
      TOUCH_AT(3);
      const int g1 = w < 6 ? 2 * ai + wj : 32 + wj * 64 + ai;
      if (w >= 6) {
        const rsrc_t rk = __builtin_amdgcn_make_buffer_rsrc((void*)(w == 6 ? p.k : p.v), 0, S_ * D_ * 2, 0x00020000);
        const unsigned ko = (unsigned)((1536 + 8 * ai) * (D_ * 2) - (w == 6 ? 4096 : 0));
        char* dst = lds + KB + (w == 6 ? 0 : 14336);
        __builtin_amdgcn_s_sleep(3);
#pragma unroll
        for (int n = 0; n < 10; ++n) __builtin_amdgcn_raw_ptr_buffer_load_lds(rk, (LAS void*)(dst + n * 1024), 16, l16, ko + (unsigned)n * 1024u, 0, NT);
        if (w == 6) {
#pragma unroll
          for (int n = 10; n < 14; ++n) __builtin_amdgcn_raw_ptr_buffer_load_lds(rk, (LAS void*)(dst + n * 1024), 16, l16, ko + (unsigned)n * 1024u, 0, NT);
        }
      }
      SCHED();
      u32x4 swt[2];
      swt[0] = *(const u32x4*)(p.shift_k + (tid - 256) * 8);
      swt[1] = *(const u32x4*)(p.shift_k + (256 + l) * 8);
      SCHED();
      if (w >= 6) __builtin_amdgcn_s_sleep(8);
      gemv_load(pre, gv_blk(g1), g1 & 7, 10);
      TAG_READY();
      asm volatile("s_waitcnt vmcnt(11)" : "+v"(swt[0]), "+v"(swt[1]) :: "memory");
      lf_set(8 + w);
      if (w < 6) gv_fin(pre, g1);
      STAMP(25);
      lf_wait(12, 4);
      STAMP(23);
#pragma unroll 1
      for (int n = 0; n < (w == 7 ? 2 : 1); ++n) {
        {
          const int pr = n ? 256 + l : tid - 256;
          const char* kp = lds + KB + 4096 + pr * 4;
          unsigned kk[11], vv[8];
#pragma unroll
          for (int i = 0; i < 11; ++i) kk[i] = *(const unsigned*)(kp + (i - 3) * 1280);
#pragma unroll
          for (int i = 0; i < 8; ++i) vv[i] = *(const unsigned*)(kp + 10240 + i * 1280);
          const u32x4 sw = n ? swt[1] : swt[0];
          const f32x2 wt[4] = {{bflo(sw[0]), bflo(sw[2])}, {bfhi(sw[0]), bfhi(sw[2])}, {bflo(sw[1]), bflo(sw[3])}, {bfhi(sw[1]), bfhi(sw[3])}};
          f32x2 o[8];
#pragma unroll
          for (int i = 0; i < 8; ++i) {
            f32x2 t = (f32x2){bflo(kk[i + 3]), bfhi(kk[i + 3])} * wt[0];
#pragma unroll
            for (int j = 1; j < 4; ++j) t = __builtin_elementwise_fma((f32x2){bflo(kk[i + 3 - j]), bfhi(kk[i + 3 - j])}, wt[j], t);
            o[i] = t * (f32x2){bflo(vv[i]), bfhi(vv[i])};
          }
          char* sc = lds + 98304 + wj * 2304;
          unsigned t = 0u;
#pragma unroll
          for (int e = 0; e < 2; ++e) {
            const u32x4 x = {pack2(o[7][e], o[6][e]), pack2(o[5][e], o[4][e]), pack2(o[3][e], o[2][e]), pack2(o[1][e], o[0][e])};
            *(volatile LAS u32x4*)(sc + l * 32 + e * 16) = x;
            t += (x[0] + x[1]) + (x[2] + x[3]);
          }
          *(volatile LAS unsigned*)(sc + 2048 + (l >> 1) * 4 + (l & 1) * 128) = tag - t - (unsigned)__builtin_amdgcn_update_dpp(0, (int)t, 0xB1, 0xf, 0xf, true);
          u32x4 x[3];
#pragma unroll
          for (int e = 0; e < 3; ++e) x[e] = *(const volatile LAS u32x4*)(sc + e * 1024 + (e < 2 ? l : l & 7) * 16);
          const unsigned ko = WS_KV3 + (unsigned)ai * 10880u;
#pragma unroll
          for (int e = 0; e < 2; ++e) st16(rs, ko + (unsigned)((n ? 4 : wj) * 2048 + e * 1024) + l16, x[e]);
          if (l < 8) st16(rs, ko + (unsigned)(10240 + (n ? 4 : wj) * 128) + l16, x[2]);
        }
      }
      STAMP(22);
      if (w < 7) lf_set(w);
      if (w >= 6) gv_fin(pre, g1);
      STAMPF(30);
    }
    if (w == 6 && ja < 80) {
      hold(AUX_T);
      gemv_load(pre, gv_blk(400 + ja), ja & 7, 10);
      gv_fin(pre, 400 + ja);
    }
    if (rj >= 0) {
      if (rj >= 160) { hold(RT2_T); rt_ld(rj, rst); }
      rt_job(rj, rst);
    }
    STAMP(26);
    lf_set(w);
    if (w == 3) { hold(WA_T); wo_ld(); }
    STAMP(3);
    if (w < 4) {
      hold(W_T);
      b2_wait((u32x2){~tag, 0u}, 1);
      lf_wait(0, 8);
      STAMP(4);
    }
    DRAIN();
  }

  STAMPF(16);
  u32x4 wox[5];
  if (w == 3) {
    const unsigned bp = (unsigned)(122880 + (4 * h4 + (n16 >> 2)) * 160 + (n16 & 3) * 8);
    u32x2 x[10];
    asm volatile("ds_read_b64_tr_b16 %0, %10\n\tds_read_b64_tr_b16 %1, %10 offset:2560\n\tds_read_b64_tr_b16 %2, %10 offset:32\n\tds_read_b64_tr_b16 %3, %10 offset:2592\n\t"
                 "ds_read_b64_tr_b16 %4, %10 offset:64\n\tds_read_b64_tr_b16 %5, %10 offset:2624\n\tds_read_b64_tr_b16 %6, %10 offset:96\n\tds_read_b64_tr_b16 %7, %10 offset:2656\n\t"
                 "ds_read_b64_tr_b16 %8, %10 offset:128\n\tds_read_b64_tr_b16 %9, %10 offset:2688\n\ts_waitcnt lgkmcnt(0)"
                 : "=&v"(x[0]), "=&v"(x[1]), "=&v"(x[2]), "=&v"(x[3]), "=&v"(x[4]), "=&v"(x[5]), "=&v"(x[6]), "=&v"(x[7]), "=&v"(x[8]), "=&v"(x[9]) : "v"(bp) : "memory");
#pragma unroll
    for (int dj = 0; dj < 5; ++dj) wox[dj] = (u32x4){x[2 * dj][0], x[2 * dj][1], x[2 * dj + 1][0], x[2 * dj + 1][1]};
  }
  auto wo_put = [&]() {
    if (w == 3 && R < 28) {
      const unsigned base = WS_WOUTF + (unsigned)c * WBLK + (unsigned)((R >> 1) * 10240 + (R & 1) * 1024);
#pragma unroll
      for (int dj = 0; dj < 5; ++dj) st16(rs, base + (unsigned)((R & 1) ? dj : (dj + 2) % 5) * 2048u + l16, wox[dj]);
    }
  };
  if (cs2) {
    cs_ld(csv, (8 + c) * 7 + R - 20);
    SCHED();
    load_b2(0, 12);
    SCHED();
    cs_fin(csv, (8 + c) * 7 + R - 20);
  } else if (w < 4) {
    asm volatile("" ::: "memory");
    if (!is_conv) load_b2(0, 12);
  }
  const unsigned xfb = WS_FX + (unsigned)(((R >> 1) * 4 + (R & 1) * 2 + (c >> 2)) * 160 + l) * 128u;
  unsigned xf3[3];
  if (w == 7) {
    lf_wait(36, 4);
    int dt = (int)(lf[44 + ((l & 31) < 20 ? l & 31 : 0)] - t_start);
    dt = l < 32 ? dt : -dt;
    dt = max(dt, __builtin_amdgcn_update_dpp(0, dt, 0xB1, 0xf, 0xf, true));
    dt = max(dt, __builtin_amdgcn_update_dpp(0, dt, 0x4E, 0xf, 0xf, true));
    dt = max(dt, __builtin_amdgcn_update_dpp(0, dt, 0x124, 0xf, 0xf, true));
    dt = max(dt, __builtin_amdgcn_update_dpp(0, dt, 0x128, 0xf, 0xf, true));
    const int d1 = min(max(max(__builtin_amdgcn_readlane(dt, 0), __builtin_amdgcn_readlane(dt, 16)), 0), 300);
    const int d0 = min(max(max(__builtin_amdgcn_readlane(dt, 32), __builtin_amdgcn_readlane(dt, 48)), 0), 300);
    dt = ((d1 + d0) >> 1) - d0;
    STAMPF(24);
    hold((unsigned)(YG_T + dt));
    STAMPF(30);
    xf3[0] = ld4(rs, xfb); xf3[1] = ld4(rs, xfb + 8192u); xf3[2] = ld4(rs, l < 32 ? xfb + 16384u : ~0u);
  }
  SCHED();
  u32x4 k_c;
  unsigned kc_o = w < 2 ? (unsigned)(tid < 112 ? tid : 0) * 16u : 0x80000000u, kc_d = (unsigned)(L_CONST + (tid & 127) * 8);
  auto kc_ld = [&](int z2) { k_c = __builtin_amdgcn_raw_buffer_load_b128(rs, kc_o, WS_CS + (unsigned)(8 * z2 + c) * 2048u, SC1); };
  auto consts_put = [&](int z2) {
    if (w < 2) {
      if (__any(ne2(k_c[0], k_c[2]) != 0u)) {
        _Pragma("unroll 1") for (unsigned spins = 0; spins < SPIN_CAP; ++spins) {
          __builtin_amdgcn_s_sleep(8);
          kc_ld(z2);
          if (!__any(ne2(k_c[0], k_c[2]) != 0u)) break;
        }
      }
      if (z2) *(u32x2*)(lds + kc_d + 1024) = (u32x2){k_c[1], k_c[3]};
      else { const unsigned d = kc_d - ((kc_d >> 1) & 4u); *(unsigned*)(lds + d) = k_c[1]; *(unsigned*)(lds + d + 8) = k_c[3]; }
    }
  };
  const int rt1 = w & 3, kh1 = w >> 2;
  unsigned bo0 = (unsigned)(kh1 * 71680) + l16, bo1 = bo0 + 35840u;
  asm volatile("" : "+v"(bo0), "+v"(bo1));
  unsigned pso[3], pqo[3], oto = (unsigned)(((w ^ 4) * 7 * 64 + l) * 8), rdo = (unsigned)((w * 7 * 64 + l) * 8), cso = (unsigned)(L_CONST + (128 + n16) * 8);
#pragma unroll
  for (int e = 0; e < 3; ++e) {
    const int row = rt1 * 16 + (e < 2 ? 4 * h4 + 2 * kh1 + e : n16);
    pso[e] = (unsigned)(L_PST + row * 4); pqo[e] = (unsigned)(L_PST + 512 + row * 16 + (row & 3) * 4);
  }
  unsigned spo = (unsigned)(L_Z + tid * 16), p6o = (unsigned)(L_PST + (w * 16 + 4 * h4) * 4), pgo = (unsigned)(L_PST + 512 + (w * 16 + n16) * 16);
  asm volatile("" : "+v"(spo), "+v"(p6o), "+v"(pgo));
  p6o = n16 == 12 ? p6o : spo;
  pgo = h4 == (n16 >> 2) ? pgo : spo;
  unsigned bsrc = (unsigned)(((l >> 1) & 3) * 64 + (l & 1) * 4), ppo = l < 8 ? l16 : 0x80000000u;
  asm volatile("" : "+v"(pso[0]), "+v"(pso[2]), "+v"(pqo[0]), "+v"(pqo[2]), "+v"(oto), "+v"(rdo), "+v"(cso), "+v"(p6o), "+v"(pgo), "+v"(bsrc), "+v"(ppo), "+v"(kc_o), "+v"(kc_d));
  pso[1] = pso[0] + 4u; pqo[1] = pqo[0] + 20u;
#define BFRAG(q, j) (*(const u32x4*)(lds + ((q) < 5 ? bo0 : bo1) + ((q) % 5) * 7168 + (j) * 1024))
  u32x4 h2[10];
  unsigned xv = (unsigned)((4 * ((l >> 3) & 1) + h4) * 128 + (l & 7) * 16), xs0 = WS_X1B + (unsigned)(R * 4 + rt1) * 20480u + (unsigned)(10 * kh1) * 1024u;
  unsigned wvo = wsp_vo, bso = (unsigned)tau(rt1, n16) * 2u, fgo = l < 32 ? (unsigned)(l >> 3) * F4S + (unsigned)(l & 7) * 16u : 0x80000000u;
  asm volatile("" : "+v"(xv), "+v"(wvo), "+v"(bso), "+v"(fgo));
#define WO3 (-92)
#define PINS(x) do { x = (unsigned)__builtin_amdgcn_readfirstlane((int)x); asm volatile("" : "+s"(x)); } while (0)
  PINS(xs0);
  unsigned zs[8], wx8, wx4, wgb, fzs, uo, ui, xs, wl2, os, zos, pos, s1, s3;
#define LDX(q) h2[q] = __builtin_amdgcn_raw_buffer_load_b128(rs, xv + (unsigned)((q) & 3) * 1024u, xs0 + (unsigned)((q) >> 2) * 4096u, SC1)
  auto flag4 = [&](unsigned off) { ST16S(((u32x4){tag, tag, tag, tag}), rs, fgo, WS_F4 - WS_FWI + off, SC1); };
  unsigned bs_raw, fwo[1];
  f32x4 acc[7];
  float mu_n, rs_n;
  {
    unsigned f1o[1] = {w == 1 && l < 20 ? fwi + (unsigned)(c * 20 + l) * 128u : ~0u}, f1v;
    fwo[0] = w == 3 && l < 28 ? fwi + (WS_FWO - WS_FWI) + (unsigned)(c * 32 + l) * 128u : ~0u;
    asm volatile("" : "+v"(f1o[0]), "+v"(fwo[0]));
    if (w < 4) {
      if (is_conv) {
        lf_wait(40, 4);
        load_b2(0, 12);
      }
      load_b2(12, 35);
    }
    wo_put();
    STAMPF(29);
    if (w == 7) {
      asm volatile("" : "+v"(xf3[0]), "+v"(xf3[1]), "+v"(xf3[2]));
      if (__any(ne2(xf3[0], xf3[1]) != 0u) || __builtin_amdgcn_ballot_w64(xf3[2] == tag) != 0xffffffffull) {
        const unsigned fo[3] = {xf3[0] != tag ? xfb : ~0u, xf3[1] != tag ? xfb + 8192u : ~0u, l < 32 && xf3[2] != tag ? xfb + 16384u : ~0u};
        wait_lines<3, 2>(rs, fo, tag);
      }
      if (l == 0) lf[33] = tag;
    }
    lf_wait(33, 1);
    STAMP(7);
    SCHED();
#pragma unroll
    for (int q = 0; q < 3; ++q) LDX(q);
    SCHED();
    __builtin_amdgcn_s_waitcnt(0x0F73);
    BAR();
    STAMP(8);
#pragma unroll
    for (int j = 0; j < 7; ++j) acc[j] = (f32x4){0.f, 0.f, 0.f, 0.f};
    f32x4 accg = {0.f, 0.f, 0.f, 0.f};
    u32x4 bf[7];
#pragma unroll
    for (int j = 0; j < 7; ++j) bf[j] = BFRAG(0, j);
    SCHED();
#pragma unroll
    for (int q = 3; q < 6; ++q) LDX(q);
    if (w == 3) flag4(WS_FWO + (unsigned)(c * 32 + R) * 128u);
    SCHED();
#pragma unroll
    for (int t = 0; t < 8; ++t) { zs[t] = WS_Z2F + (unsigned)(c * 64 + 8 * w + 4 * ((t >> 2) ^ rotg) + ((t + rotb) & 3)) * 7168u; PINS(zs[t]); }
    wx8 = (unsigned)((w + (idx >> 2)) & 7) * 1024u; wx4 = wx8 & 3072u; wgb = WS_WOUTF + (unsigned)c * WBLK; fzs = fz + (unsigned)(c * 32 + 4 * w) * 128u;
    uo = WS_UF + (unsigned)((R * 4 + rt1) * 8 + c) * 4096u + (unsigned)kh1 * 2048u; ui = WS_UF + (unsigned)((R * 4 + rt1) * 8 + 4 * kh1) * 4096u;
    xs = WS_X1B + (unsigned)(R * 4 + rt1) * 20480u + (unsigned)(5 * c + 2 * kh1) * 512u; wl2 = (unsigned)(120 + WO3 + (w < 4 ? w + 16 : w)) * 1024u;
    os = (unsigned)(((R * 64 + tau(rt1, 0)) * D_ + 80 * c + 32 * kh1) * 2);
    zos = WS_Z2F + (unsigned)((c * 64 + 4 * (R >> 1)) * 7 + (w < 6 ? w + 1 : 0)) * 1024u + (unsigned)(R & 1) * 512u; pos = WS_PART + (unsigned)(R * 64 + w) * 128u;
    s1 = (unsigned)((w ^ 4) * 3072 + 1024); s3 = (unsigned)(w * 3072 + 1024);
    PINS(wx8); PINS(wx4); PINS(wgb); PINS(fzs); PINS(uo); PINS(ui); PINS(xs); PINS(wl2); PINS(os); PINS(zos); PINS(pos); PINS(s1); PINS(s3);
    SCHED();
    auto xswap = [&](int q, int e) {
#pragma unroll
      for (int d = 0; d < 2; ++d) {
        if (e == 1) h2[q][2 + d] ^= (unsigned)__builtin_amdgcn_update_dpp(0, (int)h2[q][d], 0x128, 0xf, 0x3, false);
        else h2[q][d] ^= (unsigned)__builtin_amdgcn_update_dpp(0, (int)h2[q][2 + d], 0x128, 0xf, 0xC, false);
      }
      asm volatile("" : "+v"(h2[q]));
    };
#pragma unroll
    for (int q = 0; q < 10; ++q) {
      if (q == 0) { xswap(q, 0); xswap(q, 1); xswap(q, 2); }
      if (q == 5 && w == 1 && __builtin_amdgcn_ballot_w64(f1v == tag) != 0xfffffull) wait_lines<1, 4>(rs, f1o, tag);
      if (q == 5) { __builtin_amdgcn_s_barrier(); asm volatile("" ::: "memory"); }
      SCHED();
      accg = MFMA16(h2[q], h2[q], accg);
      SCHED();
#pragma unroll
      for (int j = 0; j < 7; ++j) {
        acc[j] = MFMA16(h2[q], bf[j], acc[j]);
        if (q == 9 && j == 1) *(f32x4*)(lds + pgo) = accg;
        if (q < 9) bf[j] = BFRAG(q + 1, j);
        if (q < 9 && j >= 3 && j < 6) xswap(q + 1, j - 3);
        if (q == 1 && j == 6) f1v = ld4(rs, f1o[0]);
        if (q == 2 && j == 0) bs_raw = __builtin_amdgcn_raw_buffer_load_b16(__builtin_amdgcn_make_buffer_rsrc((void*)p.b_sp, 0, S_ * 2, 0x00020000), bso, (unsigned)R * 128u, 0);
        if (q >= 2 && j == 2) LDW(q - 2);
        if ((j == 0 || j == 4) && 6 + 2 * q + (j >> 2) < 10) LDX(6 + 2 * q + (j >> 2));
        if (q == 4 && j == 0) kc_ld(1);
        if (q == 2 && j == 1 && cs1) cs_ld(csv, c * 7 + R - 25);
        if (q >= 5 && j == 5) {
          const int i = (5 + q) * 7 + (w < 7 ? w : 0);
          dma1k(rs, lds + i * 1024, l16, WS_WINF + (unsigned)c * WBLK + (unsigned)i * 1024u);
          if (q == 9) { const int i2 = 28 + (w < 7 ? w : 0); dma1k(rs, lds + i2 * 1024, l16, WS_WINF + (unsigned)c * WBLK + (unsigned)i2 * 1024u); }
        }
        SCHED();
      }
    }
    STAMP(31);
  }
  unsigned sro, swo, zo, zro, po;
  auto gelu_n = [&](f32x2* x, auto N_, auto&& fill) {
    constexpr int N = decltype(N_)::value;
    const float c0 = -2.0f * 0.7978845608028654f * 1.4426950408889634f, c1 = c0 * 0.044715f;
    f32x2 k0 = {c0, c0}, k1 = {c1, c1}, k2 = {1.0f, 1.0f};
    asm volatile("" : "+v"(k0), "+v"(k1), "+v"(k2));
    f32x2 t[N];
#define GST(e) do { _Pragma("unroll") for (int i = 0; i < N; ++i) { t[i] = (e); asm volatile("" : "+v"(t[i])); } } while (0)
    GST(x[i] * x[i]);
    GST(__builtin_elementwise_fma(t[i], k1, k0));
    fill(IC<0>());
    GST(t[i] * x[i]);
    fill(IC<1>());
    GST(((f32x2){__builtin_amdgcn_exp2f(t[i][0]), __builtin_amdgcn_exp2f(t[i][1])}));
    fill(IC<2>());
    GST(t[i] + k2);
    fill(IC<3>());
    GST(((f32x2){__builtin_amdgcn_rcpf(t[i][0]), __builtin_amdgcn_rcpf(t[i][1])}));
    fill(IC<4>());
#pragma unroll
    for (int i = 0; i < N; ++i) { x[i] *= t[i]; asm volatile("" : "+v"(x[i])); }
  };
  {
    f32x2 mine[7];
    f32x2 csb[7];
    if (kh1) {
#pragma unroll
      for (int j = 0; j < 7; ++j) { *(f32x2*)(lds + rdo + j * 512) = (f32x2){acc[j][0], acc[j][1]}; mine[j] = (f32x2){acc[j][2], acc[j][3]}; }
      *(f32x4*)(lds + p6o) = acc[6];
    } else {
#pragma unroll
      for (int j = 0; j < 7; ++j) { *(f32x2*)(lds + rdo + j * 512) = (f32x2){acc[j][2], acc[j][3]}; mine[j] = (f32x2){acc[j][0], acc[j][1]}; }
      *(f32x4*)(lds + p6o) = acc[6];
      consts_put(1);
      if (cs1) cs_fin(csv, c * 7 + R - 25);
#pragma unroll
      for (int k = 8; k < 12; ++k) LDW(k);
    }
    BAR();
    auto b1p = [&](int i) { dma1k(rs, lds + i * 1024, l16, WS_WINF + (unsigned)c * WBLK + (unsigned)i * 1024u); };
    if (kh1) {
#pragma unroll
      for (int k = 8; k < 12; ++k) LDW(k);
    }
    __builtin_amdgcn_s_setprio(3);
    STAMPT(9);
    f32x2 oth[7];
    f32x2 pa[3], pb[3];
#pragma unroll
    for (int e = 0; e < 3; ++e) {
      pa[e] = (f32x2){*(const float*)(lds + pso[e]), *(const float*)(lds + pqo[e])};
      pb[e] = (f32x2){*(const float*)(lds + pso[e] + 256), *(const float*)(lds + pqo[e] + 1024)};
    }
#pragma unroll
    for (int j = 0; j < 7; ++j) { oth[j] = *(const f32x2*)(lds + oto + j * 512); csb[j] = *(const f32x2*)(lds + cso + 128 * j); }
    SCHED();
    float mu2[2], rs2[2];
#pragma unroll
    for (int e = 0; e < 3; ++e) {
      const float mean = (pa[e][0] + pb[e][0]) * (1.0f / D_);
      const float var = __builtin_fmaxf((pa[e][1] + pb[e][1]) * (1.0f / D_) - mean * mean, 0.f);
      const float rstd = __builtin_amdgcn_rsqf(var + 1e-5f);
      if (e < 2) { mu2[e] = mean; rs2[e] = rstd; } else { mu_n = mean; rs_n = rstd; }
    }
    f32x2 zz[7];
    const f32x2 nmu = {-mu2[0], -mu2[1]}, rsv = {rs2[0], rs2[1]};
#pragma unroll
    for (int j = 0; j < 7; ++j)
      zz[j] = __builtin_elementwise_fma(__builtin_elementwise_fma(nmu, (f32x2){csb[j][0], csb[j][0]}, mine[j] + oth[j]), rsv, (f32x2){csb[j][1], csb[j][1]});
    gelu_n(zz, IC<7>(), [&](auto K_) __attribute__((always_inline)) { constexpr int k = decltype(K_)::value; b1p(8 * k + w + (k < 4 || w < 3 ? 35 : 70)); SCHED(); });
    if (n16 >= 12) zz[6] = (f32x2){0.f, 0.f};
    f32x2 s1 = ((zz[0] + zz[1]) + (zz[2] + zz[3])) + ((zz[4] + zz[5]) + zz[6]);
    f32x2 s2 = zz[0] * zz[0], s3 = zz[1] * zz[1];
#pragma unroll
    for (int j = 2; j < 7; ++j) { if (j & 1) s3 = __builtin_elementwise_fma(zz[j], zz[j], s3); else s2 = __builtin_elementwise_fma(zz[j], zz[j], s2); }
    s2 += s3;
    asm("" : "+v"(s1), "+v"(s2));
    float a0 = s1[0] + dpp_f<0xB1>(s1[0]), a1 = s1[1] + dpp_f<0xB1>(s1[1]), b0 = s2[0] + dpp_f<0xB1>(s2[0]);
    a0 = (l & 1) ? a1 : a0;
    const float b1 = s2[1] + dpp_f<0xB1>(s2[1]);
    b0 = (l & 1) ? b1 : b0;
    a0 += dpp_f<0x4E>(a0); b0 += dpp_f<0x4E>(b0);
    a0 += dpp_f<0x124>(a0); b0 += dpp_f<0x124>(b0);
    a0 += dpp_f<0x128>(a0); b0 += dpp_f<0x128>(b0);
    {
      const unsigned pa0 = __builtin_amdgcn_ds_bpermute(bsrc, __float_as_uint(a0)), pb0 = __builtin_amdgcn_ds_bpermute(bsrc, __float_as_uint(b0));
      ST16S(((u32x4){tag, pa0, tag, pb0}), rs, ppo, pos + (unsigned)c * 1024u, SC1);
    }
    SCHED();
    __builtin_amdgcn_s_setprio(0);
    STAMP(10);
    if (!kh1) {
#pragma unroll
      for (int k = 10; k < 18; ++k) b1p(4 * k + (w & 3) + 70 - (k == 17 ? (w & 2) : 0));
    }
#pragma unroll
    for (int k = 12; k < 16; ++k) LDW(k);
#pragma unroll
    for (int j = 0; j < 7; ++j)
      *(unsigned*)(lds + L_Z + (((rt1 >> 1) * 7 + j) * 64 + l) * 16 + (4 * (rt1 & 1) + 2 * kh1) * 2) = pack2(zz[j][0], zz[j][1]);
    SCHED();
    sfor<0, 8>([&](auto X_) __attribute__((always_inline)) { WSP_TX(decltype(X_)::value); SCHED(); });
    sro = (unsigned)(L_ST2 + h4 * 16);
    swo = (unsigned)(L_ST2 + (16 * rt1 + 4 * ((l >> 1) & 3) + 2 * kh1 + (l & 1)) * 4);
    zo = w < 7 ? (unsigned)(h4 * 7168 + n16 * 16) : 0x80000000u;
    zro = (unsigned)(L_Z + (w < 7 ? w : 0) * 1024) + l16;
    po = (unsigned)((l >> 3) * 1024 + (l & 7) * 16);
    asm volatile("" : "+v"(sro), "+v"(swo), "+v"(zo), "+v"(zro), "+v"(po));
    asm volatile("s_waitcnt vmcnt(4)" ::: "memory");
    BAR();
    STAMPT(11);
  }
  unsigned z1d[7];
  constexpr int L_RED = 28672;
#define WOA(i) (((i) < 28 ? (i) : (i) < 108 ? (i) + 32 : (i) < 120 ? (i) + 37 : (i) - 92) * 1024)
#define RBAR() do { __builtin_amdgcn_s_barrier(); asm volatile("" ::: "memory"); } while (0)
#define WOD(i0, x) dma1k(rs, lds + WOA(i0) + (x), l16, wgb + (unsigned)(i0) * 1024u + (x))
  constexpr int NW = 14;
  u32x4 zw[NW];
#define LDZ(f) do { if (((f) >> 3) == 4) { zs[(f) & 7] += 4096u; asm volatile("" : "+s"(zs[(f) & 7])); } \
    zw[(f) % NW] = __builtin_amdgcn_raw_buffer_load_b128(rs, l16 + (unsigned)(((f) >> 3) & 3) * 1024u, zs[(f) & 7], SC1); } while (0)
  {
    f32x4 acc[7];
#pragma unroll
    for (int j = 0; j < 7; ++j) acc[j] = (f32x4){0.f, 0.f, 0.f, 0.f};
    u32x4 bf[7];
#pragma unroll
    for (int j = 0; j < 7; ++j) bf[j] = BFRAG(4, j);
    u32x4 pv, zf[2];
    unsigned fwv;
    auto ld_pv = [&]() { pv = __builtin_amdgcn_raw_buffer_load_b128(rs, po, pos, SC1); };
    auto z1 = [&](auto Q0_, auto Q1_) __attribute__((always_inline)) {
#pragma unroll
      for (int qi = decltype(Q0_)::value; qi < decltype(Q1_)::value; ++qi) {
        const int q = (qi + 4) % 10, qn = (qi + 5) % 10;
        SCHED();
#pragma unroll
        for (int j = 0; j < 7; ++j) {
          acc[j] = MFMA16(bf[j], h2[q], acc[j]);
          if (qi < 9 && qi != 4) bf[j] = BFRAG(qn, j);
          if (qi < 2 && (j == 1 || j == 4)) { const int i = (2 * qi + (j >> 2)) * 7 + (w < 7 ? w : 0); dma1k(rs, lds + i * 1024, l16, WS_WINF + (unsigned)c * WBLK + (unsigned)i * 1024u); }
          if (qi == 2 && j == 0) kc_ld(0);
          if (qi == 2 && j == 3) fwv = ld4(rs, fwo[0]);
          if (qi == 3 && j == 1) ld_pv();
          if (qi == 4 && j < 2) zf[j] = *(const u32x4*)(lds + zro + j * 7168);
          SCHED();
        }
      }
    };
    auto req1 = [&]() {
#pragma unroll
      for (int k = 16; k < 24; ++k) { LDW(k); SCHED(); }
    };
    auto req2 = [&]() {
#pragma unroll
      for (int k = 24; k < 32; ++k) { LDW(k); SCHED(); }
      sfor<0, 2>([&](auto G_) __attribute__((always_inline)) { WOD(68 + 8 * decltype(G_)::value, wx8); SCHED(); });
    };
    if (kh1) req1();
    z1(IC<0>(), IC<5>());
    STAMPT(29);
    f32x2 zv[2][4];
#pragma unroll
    for (int i = 0; i < 8; ++i) { zv[i >> 2][i & 3] = (f32x2){bflo(zf[i >> 2][i & 3]), bfhi(zf[i >> 2][i & 3])}; asm volatile("" : "+v"(zv[i >> 2][i & 3])); }
    __builtin_amdgcn_s_setprio(3);
    if (__any(ne2(pv[0], pv[2]) != 0u)) {
      _Pragma("unroll 1") for (unsigned spins = 0; spins < SPIN_CAP; ++spins) {
        ld_pv();
        if (!__any(ne2(pv[0], pv[2]) != 0u)) break;
        __builtin_amdgcn_s_sleep(1);
      }
    }
    {
      float t1 = __uint_as_float(pv[1]), t2 = __uint_as_float(pv[3]);
      t1 += dpp_f<0x128>(t1); t2 += dpp_f<0x128>(t2);
      auto x = __builtin_amdgcn_permlane16_swap(__float_as_uint(t1), __float_as_uint(t2), false, false);
      const unsigned y = __float_as_uint(__uint_as_float(x[0]) + __uint_as_float(x[1]));
      auto z = __builtin_amdgcn_permlane32_swap(y, y, false, false);
      const unsigned u = __float_as_uint(__uint_as_float(z[0]) + __uint_as_float(z[1]));
      auto v = __builtin_amdgcn_permlane16_swap(u, u, false, false);
      const float mean = __uint_as_float(v[0]) * (1.0f / FH);
      const float var = __builtin_fmaxf(__uint_as_float(v[1]) * (1.0f / FH) - mean * mean, 0.f);
      const float rstd = __builtin_amdgcn_rsqf(var + 1e-5f);
      *(float*)(lds + swo) = rstd;
      *(float*)(lds + swo + 256) = -mean * rstd;
    }
    SCHED();
    consts_put(0);
    if (w == 3 && __builtin_amdgcn_ballot_w64(fwv == tag) != 0xfffffffull) wait_lines<1, 1>(rs, fwo, tag);
    if (!kh1) req1();
    BAR();
    STAMP(12);
    {
      f32x4 sa[2][2], sb[2][2];
#pragma unroll
      for (int i = 0; i < 4; ++i) { sa[i >> 1][i & 1] = *(const f32x4*)(lds + sro + i * 64); sb[i >> 1][i & 1] = *(const f32x4*)(lds + sro + i * 64 + 256); }
      SCHED();
#pragma unroll
      for (int m = 0; m < 2; ++m) {
        u32x4 o;
#pragma unroll
        for (int e = 0; e < 4; ++e) {
          const f32x4 a = sa[m][e >> 1], b = sb[m][e >> 1];
          const f32x2 x = __builtin_elementwise_fma(zv[m][e], (f32x2){a[2 * (e & 1)], a[2 * (e & 1) + 1]}, (f32x2){b[2 * (e & 1)], b[2 * (e & 1) + 1]});
          o[e] = pack2(x[0], x[1]);
        }
        ST16S(o, rs, zo + (unsigned)m * 256u, zos, SC1);
      }
    }
    SCHED();
    __builtin_amdgcn_s_setprio(0);
    STAMPT(13);
#pragma unroll
    for (int j = 0; j < 7; ++j) bf[j] = BFRAG(9, j);
    if (kh1) req2();
    z1(IC<5>(), IC<10>());
    f32x2 mine[7];
    if (kh1) {
#pragma unroll
      for (int j = 0; j < 7; ++j) { *(f32x2*)(lds + rdo + L_RED + j * 512) = (f32x2){acc[j][0], acc[j][1]}; mine[j] = (f32x2){acc[j][2], acc[j][3]}; }
    } else {
#pragma unroll
      for (int j = 0; j < 7; ++j) { *(f32x2*)(lds + rdo + L_RED + j * 512) = (f32x2){acc[j][2], acc[j][3]}; mine[j] = (f32x2){acc[j][0], acc[j][1]}; }
    }
    if (!kh1) req2();
    asm volatile("s_waitcnt vmcnt(10)" ::: "memory");
    BAR();
    if (w == 3) flag4(WS_FZ + (unsigned)(c * 32 + R) * 128u);
    STAMP(6);
    auto wo8 = [&](auto K_) __attribute__((always_inline)) {
      constexpr int k = decltype(K_)::value;
      if (k < 12) WOD(k < 3 ? 8 * k : k < 8 ? 8 * k + 4 : 8 * k + 20, wx8);
      else if (k == 12) dma1k(rs, lds + (kh1 ? WOA(116) : WOA(24)) + wx4, l16, wgb + (kh1 ? 116u : 24u) * 1024u + wx4);
      SCHED();
    };
    wo8(IC<0>()); wo8(IC<1>());
    u32x4 gv[8];
    auto gb_ld = [&]() {
#pragma unroll
      for (int k = 0; k < 8; ++k) gv[k] = __builtin_amdgcn_raw_buffer_load_b128(rs, l < 40 ? l16 : 0u, WS_MODQ + (unsigned)(k * 2560 + 1920 + 80 * c) * 8u, SC1);
    };
    if (w == 2) gb_ld();
    f32x2 z[7];
    {
      f32x2 oth[7];
      f32x4 cc[7];
#pragma unroll
      for (int j = 0; j < 7; ++j) {
        oth[j] = *(const f32x2*)(lds + oto + L_RED + j * 512);
        cc[j] = *(const f32x4*)(lds + L_CONST + (16 * j + 4 * h4 + 2 * kh1) * 8);
      }
      SCHED();
#pragma unroll
      for (int j = 0; j < 7; ++j)
        z[j] = __builtin_elementwise_fma(__builtin_elementwise_fma((f32x2){-mu_n, -mu_n}, (f32x2){cc[j][0], cc[j][1]}, oth[j] + mine[j]),
                                         (f32x2){rs_n, rs_n}, (f32x2){cc[j][2], cc[j][3]});
    }
#pragma unroll
    for (int j = 0; j < 7; ++j) asm volatile("" : "+v"(z[j]));
    wo8(IC<2>());
    gelu_n(z, IC<7>(), [&](auto K_) __attribute__((always_inline)) { wo8(IC<3 + decltype(K_)::value>()); });
#pragma unroll
    for (int j = 0; j < 7; ++j) z1d[j] = pack2(z[j][0], z[j][1]);
    asm volatile("" : "+v"(z1d[0]), "+v"(z1d[1]), "+v"(z1d[2]), "+v"(z1d[3]), "+v"(z1d[4]), "+v"(z1d[5]), "+v"(z1d[6]));
    SCHED();
    STAMPT(30);
    if (w == 2) {
      auto bad = [&]() {
        unsigned d = 0u;
#pragma unroll
        for (int k = 0; k < 8; ++k) d |= ne2(gv[k][0], gv[k][2]);
        return __any(d != 0u);
      };
      if (bad()) {
        _Pragma("unroll 1") for (unsigned spins = 0; spins < SPIN_CAP; ++spins) {
          __builtin_amdgcn_s_sleep(8);
          gb_ld();
          if (!bad()) break;
        }
      }
      f32x2 t[4];
#pragma unroll
      for (int k = 0; k < 4; ++k) t[k] = (f32x2){__uint_as_float(gv[2 * k][1]) + __uint_as_float(gv[2 * k + 1][1]), __uint_as_float(gv[2 * k][3]) + __uint_as_float(gv[2 * k + 1][3])};
      *(f32x2*)(lds + L_MISC + l * 8) = rbf2((t[0] + t[1]) + (t[2] + t[3]));
    }
    const unsigned fo[1] = {l < 4 ? fzs + (unsigned)l * 128u : ~0u};
    unsigned fv;
    sfor<8, 32>([&](auto X_) __attribute__((always_inline)) {
      constexpr int x = decltype(X_)::value;
      wo8(IC<x>());
      if (x == 12) { fv = ld4(rs, fo[0]); SCHED(); }
      if (x == 18) {
        STAMP(14);
        if (__builtin_amdgcn_ballot_w64(fv == tag) != 15ull) wait_lines<1, 1>(rs, fo, tag);
        asm volatile("" ::: "memory");
        STAMPT(23);
      }
      if (x >= 18) { LDZ((x - 18) & 15); SCHED(); }
      WSP_TX(x);
      SCHED();
    });
  }

  {
    SCHED();
    STAMP(20);
    const int kh = kh1;
    const unsigned rw = (unsigned)(L_RED + w * 512);
    unsigned rr = (unsigned)(L_RED + w * 4096 + l * 4);
    asm volatile("; m0 is set here, not in front of the first store (one wait state)" : "+v"(rr) : "{m0}"(rw));
    const float bs = bflo(bs_raw);
    unsigned long long hm = 0xffff000000000000ull;
    asm volatile("" : "+s"(hm));
    unsigned l8;
    f32x4 acc2[2][4];
    float rv[8][2];
    u32x4 ud;
    u32x4 P[2][8];
    u32x2 xp[3];
    auto ld_p = [&](int pt, int i) { P[pt][i] = __builtin_amdgcn_raw_buffer_load_b128(rs, l16 + (unsigned)pt * 1024u, ui + (unsigned)i * 2048u, SC1); };
#define RED_PUT1(j, i) asm volatile("ds_write_addtid_b32 %0 offset:%2" :: "v"(acc2[(j) & 1][((i) >> 2) & 3][(i) & 3]), "{m0}"(rw), "n"((((i) >> 2) & 3) * 4096 + (((i) >> 1) & 1) * 16384 + ((i) & 1) * 256) : "memory")
#define RED_PUT(j, r) do { RED_PUT1(j, 4 * (r)); RED_PUT1(j, 4 * (r) + 1); RED_PUT1(j, 4 * (r) + 2); RED_PUT1(j, 4 * (r) + 3); } while (0)
#define RED_GET(i) do { f32x2 x_; asm volatile("ds_read2st64_b32 %0, %1 offset0:%2 offset1:%3" : "=v"(x_) : "v"(rr), "n"(2 * (i)), "n"(2 * (i) + 1) : "memory"); rv[i][0] = x_[0]; rv[i][1] = x_[1]; } while (0)
    float zz[2], gg[2];
    unsigned cs;
    auto sum_rows_u = [&](unsigned v) {
      auto a = __builtin_amdgcn_permlane16_swap(v, v, false, false);
      v = a[0] + a[1];
      auto b = __builtin_amdgcn_permlane32_swap(v, v, false, false);
      return b[0] + b[1];
    };
    auto red_fin = [&](int j, int k, bool fuse = false) {
      const int e = k & 1, i = k >> 1;
#define VADD(d, x, y) asm volatile("v_add_f32 %0, %1, %2" : "=v"(d) : "v"(x), "v"(y))
      if (i < 4) VADD(rv[2 * i][e], rv[2 * i][e], rv[2 * i + 1][e]);
      if (i == 4 || i == 5) VADD(rv[4 * (i & 1)][e], rv[4 * (i & 1)][e], rv[4 * (i & 1) + 2][e]);
      if (i == 6) VADD(rv[0][e], rv[0][e], bs);
      if (i == 7) VADD(gg[e], rv[0][e], rv[4][e]);
      if (i == (fuse ? 9 : 8)) zz[e] = e ? bfhi(z1d[(j + 6) % 7]) : bflo(z1d[(j + 6) % 7]);
      if (i == 9) zz[e] *= gg[e];
      if (i == 9 || (i == 8 && !fuse)) asm volatile("" : "+v"(zz[e]));
      if (k == 20) { ud[j & 3] = pack2(zz[0], zz[1]); asm volatile("" : "+v"(ud[j & 3])); }
      if (k == 21) {
        if (j == 0) asm volatile("v_cndmask_b32 %0, %0, 0, %1" : "+v"(ud[0]) : "s"(hm));
        if (j == 2) asm volatile("v_add3_u32 %0, %1, %2, %3" : "=v"(cs) : "v"(ud[0]), "v"(ud[1]), "v"(ud[2]));
        if (j == 3) {
          cs = tag - sum_rows_u(cs + ud[3]);
          asm volatile("v_cndmask_b32 %0, %0, %1, %2" : "+v"(ud[0]) : "v"(cs), "s"(hm));
        }
        if (j >= 4) { ud[3] = (j == 4 ? tag : ud[3]) - ud[j & 3]; asm volatile("" : "+v"(ud[3])); }
        if (j % 3 == 0 && j) ST16S(ud, rs, l16 + (unsigned)(j / 6) * 1024u, uo, SC1);
      }
    };
    struct Sch { int b1, b2, wp[16], rp[8], vp[22], lp[8], pr[32]; };
    constexpr Sch S = {18, 28, {0, 1, 2, 3, 4, 5, 6, 7, 8, 9, 10, 11, 12, 12, 15, 15}, {19, 19, 20, 22, 23, 23, 24, 24}, {29, 29, 30, 30, 31, 31, 32, 33, 35, 36, 37, 38, 39, 40, 41, 42, 43, 46, 43, 46, 48, 48},
                       {1, 1, 1, 1, 1, 1, 1, 1}, {0, 0, 2, 1, 0, 0, 0, 0, 0, 0, 2, 2, 0, 0, 0, 0, 0, 0, 0, 1, 0, 0, 0, 1, 0, 0, 2, 1, 0, 0, 0, 0}};
    constexpr Sch F = {15, 23, {0, 1, 1, 2, 3, 4, 4, 5, 6, 7, 8, 9, 10, 11, 12, 12}, {15, 16, 16, 17, 17, 18, 18, 19}, {23, 23, 24, 24, 25, 26, 26, 27, 27, 28, 28, 29, 30, 30, 31, 31, 22, 22, 32, 33, 34, 35},
                       {1, 2, 1, 1, 1, 1, 1, 1}, {0, 2, 1, 0, 0, 1, 1, 0, 0, 2, 1, 0, 0, 2, 1, 0, 0, 2, 1, 0, 0, 2, 1, 0, 0, 2, 1, 0, 0, 2, 1, 0}};
    unsigned wb[3], xo;
    const unsigned wl[3] = {(unsigned)(120 + WO3 + w) * 1024u, (unsigned)(128 + WO3 + w) * 1024u, wl2}, wg = wgb - (unsigned)(WO3 * 1024);
    constexpr int NAF = 32, NA0 = 16;
    u32x4 af[NAF];
#define LDA(m) af[(m) % NAF] = *(const u32x4*)(lds + wb[WOA(2 * (m)) >> 16] + (WOA(2 * (m)) & 65535))
    unsigned ck[2];
    const unsigned tag4 = tag * 4u;
    auto p_chk = [&](int pt, int pr, int k) {
      u32x4* q = P[pt] + 4 * pr;
      if (k < 2) ck[k & 1] = q[k & 1][0] + q[k & 1][1];
      else if (k < 8) ck[k & 1] = ck[k & 1] + q[(k & 1) + 2 * ((k >> 2) & 1)][k & 2] + q[(k & 1) + 2 * ((k >> 2) & 1)][(k & 2) + 1];
      else if (k == 8) ck[0] = pt ? ck[0] + ck[1] : sum_rows_u(ck[0] + ck[1]);
      if (k < 9) asm volatile("" : "+v"(ck[k & 1]));
      if (k == 9) {
        if (__builtin_expect(__any(ck[0] != tag4), 0)) {
          _Pragma("unroll 1") for (unsigned spins = 0; spins < SPIN_CAP; ++spins) {
            unsigned d = 0u;
#pragma unroll
            for (int i = 0; i < 4; ++i) ld_p(pt, 4 * pr + i);
#pragma unroll
            for (int i = 0; i < 4; ++i) d += q[i][0] + q[i][1] + q[i][2] + q[i][3];
            if (!pt) d = sum_rows_u(d);
            if (!__any(d != tag4)) break;
            __builtin_amdgcn_s_sleep(1);
          }
        }
      }
      if (k >= 10 && !pt) { unsigned x = q[(k - 10) & 3][0]; asm volatile("v_cndmask_b32 %0, %0, 0, %1" : "+v"(x) : "s"(hm)); q[(k - 10) & 3][0] = x; }
      if (k >= 10 && k < 13 && pt) { q[(k - 10) % 3][3] = q[3][(k - 10) % 3]; asm volatile("" : "+v"(q[(k - 10) % 3])); }
    };
    sfor<0, 224>([&](auto M_) __attribute__((always_inline)) {
      constexpr int mm = decltype(M_)::value, j = mm >> 5, m = mm & 31, y = m - 20;
      constexpr int t = j == 6 && y >= 0 ? 5 + (y % 6) / 2 : m >> 2, r = j == 6 && y >= 0 ? (y & 1) + 2 * (y / 6) : m & 3, f = 8 * j + t;
      constexpr Sch C = j < 4 ? S : F, B = j <= 4 ? S : F;
      if (t == 0) asm volatile("v_mfma_f32_16x16x32_bf16 %0, %1, %2, 0" : "=&v"(acc2[j & 1][r]) : "v"(zw[f % NW]), "v"(wsp[r][t]), "v"(zw[(f | (f < 8 ? 1 : 3)) % NW]));
      else asm volatile("v_mfma_f32_16x16x32_bf16 %0, %1, %2, %0" : "+v"(acc2[j & 1][r]) : "v"(zw[f % NW]), "v"(wsp[r][t]), "v"(zw[(f | (f < 8 ? 1 : 3)) % NW]));
      SCHED();
      if (C.pr[m] == 1) __builtin_amdgcn_s_setprio(1);
      if ((m == C.b1 && j > 0) || (m == C.b2 && j > 0) || mm == 30) { asm volatile("" ::: "memory"); __builtin_amdgcn_s_waitcnt(0xC07F); RBAR(); SCHED(); }
      sfor<0, 16>([&](auto I_) __attribute__((always_inline)) {
        constexpr int i = decltype(I_)::value;
        if (j > 0 && C.wp[i] == m) { RED_PUT1(j + 1, i); SCHED(); }
        if (j > 0 && i < 8 && C.rp[i & 7] == m) { RED_GET(i & 7); SCHED(); }
      });
      if (j == 5 && m == 20) {
#pragma unroll
        for (int i = 0; i < 3; ++i) { wb[i] = (unsigned)(kh * 1024 + i * 65536) + l16; asm volatile("" : "+v"(wb[i])); }
      }
      if (j == 5 && m == 13) {
        asm volatile("v_lshrrev_b32 %0, 1, %1" : "=v"(l8) : "v"(l16));
        xo = (l8 & 0x180u) + (unsigned)psi((l8 >> 3) & 15) * 8u;
        asm volatile("" : "+v"(xo));
      }
      if (j == 6 && m >= 28) { RED_PUT1(6, 2 * (m - 28)); RED_PUT1(6, 2 * (m - 28) + 1); SCHED(); }
      constexpr int ap[NA0] = {9, 10, 11, 13, 13, 14, 14, 19, 20, 21, 22, 23, 24, 25, 26, 27};
      sfor<0, NA0>([&](auto N_) __attribute__((always_inline)) { if (j == 6 && ap[decltype(N_)::value] == m) { LDA(decltype(N_)::value); SCHED(); } });
      sfor<0, 22>([&](auto K_) __attribute__((always_inline)) {
        constexpr int k = decltype(K_)::value;
        if (j > 1 && B.vp[k] == m + 32) { red_fin(j > 1 ? j - 2 : 0, k, B.vp[16] == B.vp[18]); SCHED(); }
        if (j > 0 && C.vp[k] == m) { red_fin(j > 0 ? j - 1 : 0, k); SCHED(); }
      });
      if (C.pr[m] == 2) __builtin_amdgcn_s_setprio(0);
      if ((m & 3) == C.lp[m >> 2]) {
        if (f > 0 && f - 1 + NW < 56) LDZ(f - 1 + NW);
        if (j == 6 && m >= 12) ld_p(0, ((m >> 2) - 3) & 7);
        if (j == 5 && m >= 20) xp[((m >> 2) - 5) % 3] = __builtin_amdgcn_raw_buffer_load_b64(rs, xo + (unsigned)((m >> 2) - 5) * 512u, xs, SC1);
        SCHED();
      }
    });
    STAMP(15);
    f32x4 acc[5];
    f32x4 xf[3], gf[3];
    unsigned go, oo, eo, ho, tw, tr, o1, ta, tb;
    const unsigned s2 = (unsigned)(L_MISC + 128 * kh), s4 = s1 - (unsigned)kh * 1024u;
    unsigned long long om = kh ? ~0ull : 0xccccccccccccccccull;
#define PIN2(a, b) asm volatile("" : "+v"(a), "+v"(b))
    auto glue = [&](int k) {
      if (k == 0) { ta = l8 >> 5; tb = l16 & 48u; PIN2(ta, tb); }
      if (k == 1) { tr = __umul24(ta, 112u) + tb; oo = ta & 12u; PIN2(tr, oo); }
      if (k == 2) { tr += s1; oo += ta; PIN2(tr, oo); }
      if (k == 3) { oo = __umul24(oo, 1280u) + tb; ta = l8 >> 4; PIN2(oo, ta); }
      if (k == 4) { ta &= 24u; tb = l16 & 0xf0u; PIN2(ta, tb); }
      if (k == 5) { tw = __umul24(tb, 7u) + ta; go = (ta << 1) + s2; PIN2(tw, go); }
      if (k == 6) { tw += s1; asm volatile("v_cndmask_b32 %0, %1, -2.0, %2" : "=v"(o1) : "v"(oo), "s"(om)); asm volatile("" : "+v"(tw)); }
      if (k == 7) { eo = l16 + s3; ho = l16 + s4; PIN2(eo, ho); }
      if (k >= 8 && k < 14) {
        const unsigned x = xp[((k - 8) >> 1) % 3][k & 1];
        float lo = bflo(x), hi = bfhi(x);
        PIN2(lo, hi);
        xf[((k - 8) >> 1) % 3][2 * (k & 1)] = lo; xf[((k - 8) >> 1) % 3][2 * (k & 1) + 1] = hi;
      }
    };
    auto g3 = [&](auto M0_, auto M1_) __attribute__((always_inline)) {
      sfor<decltype(M0_)::value, decltype(M1_)::value>([&](auto M_) __attribute__((always_inline)) {
        constexpr int mi = decltype(M_)::value;
        constexpr int m = mi < 55 ? mi : mi < 64 ? 55 + 5 * ((mi - 55) / 3) + (mi - 55) % 3 : 58 + 5 * ((mi - 64) / 2) + (mi & 1), u2 = m / 5;
        const u32x4 fb = u2 < 8 ? P[0][u2 & 7] : P[1][u2 < 11 ? (u2 - 8) & 3 : (u2 - 7) & 7];
        if (m < 5) asm volatile("v_mfma_f32_16x16x32_bf16 %0, %1, %2, 0" : "=&v"(acc[m % 5]) : "v"(af[m % NAF]), "v"(fb));
        else asm volatile("v_mfma_f32_16x16x32_bf16 %0, %1, %2, %0" : "+v"(acc[m % 5]) : "v"(af[m % NAF]), "v"(fb));
        SCHED();
        if (mi == 0) RBAR();
        if (mi < 3) dma1k(rs, lds + wl[mi % 3], l16, wg + wl[mi % 3]);
        if (mi >= 5 && mi < 19) p_chk(0, 1, mi - 5);
        if (mi >= 19 && mi < 33) glue(mi - 19);
        if (mi >= 47 && mi < 54) { p_chk(1, 1, 2 * (mi - 47)); SCHED(); p_chk(1, 1, 2 * (mi - 47) + 1); }
        if (mi < 40) LDA(NA0 + mi);
        if (mi >= 66 && mi < 69) *(f32x4*)(lds + eo + (mi - 66) * 1024) = acc[(mi - 66) % 3];
        SCHED();
      });
    };
    __builtin_amdgcn_s_setprio(3);
#pragma unroll
    for (int k = 18; k < 22; ++k) red_fin(5, k);
    asm volatile("s_nop 2");
    RED_PUT(6, 2); RED_PUT(6, 3);
    red_fin(6, 16); red_fin(6, 17);
    SCHED();
    ld_p(0, 5); ld_p(0, 6);
    SCHED();
    BAR();
    sfor<0, 8>([&](auto I_) __attribute__((always_inline)) { RED_GET(decltype(I_)::value); });
    SCHED();
    ld_p(0, 7);
    SCHED();
    sfor<0, 20>([&](auto K_) __attribute__((always_inline)) {
      constexpr int k = decltype(K_)::value;
      if (k < 8 && !(k & 1)) asm volatile("s_waitcnt lgkmcnt(%0)" :: "n"(6 - k) : "memory");
      red_fin(6, k < 16 ? k : k + 2);
    });
    SCHED();
    __builtin_amdgcn_s_setprio(0);
    STAMPT(16);
#pragma unroll
    for (int k = 0; k < 14; ++k) p_chk(0, 0, k);
    SCHED();
    g3(IC<0>(), IC<24>());
    STAMPT(22);
    DRAIN();
    RBAR();
    STAMPT(27);
    if (!kh) {
#pragma unroll
      for (int i = 0; i < 8; ++i) { ld_p(1, i); SCHED(); }
    }
    g3(IC<24>(), IC<36>());
    if (kh) {
#pragma unroll
      for (int i = 0; i < 8; ++i) { ld_p(1, i); SCHED(); }
    }
    STAMPT(26);
    g3(IC<36>(), IC<40>());
    STAMPT(24);
#pragma unroll
    for (int m = NA0 + 40; m < 70; ++m) LDA(m);
#pragma unroll
    for (int x = 0; x < 3; ++x) gf[x] = *(const f32x4*)(lds + go + x * 64);
    SCHED();
    const rsrc_t ro = __builtin_amdgcn_make_buffer_rsrc((void*)p.out, 0, S_ * D_ * 2, 0x00020000);
    STAMPT(17);
#pragma unroll
    for (int k = 0; k < 14; ++k) p_chk(1, 0, k);
    asm volatile("s_nop 1");
    STAMPT(25);
    g3(IC<40>(), IC<70>());
    STAMPT(18);
    BAR();
    STAMPT(28);
    f32x4 o4[3];
    u32x4 ov[2];
#pragma unroll
    for (int j = 2; j >= 0; --j) o4[j] = *(const volatile LAS f32x4*)(lds + ho + j * 1024);
    asm volatile("" : "+v"(acc[2]), "+v"(acc[3]), "+v"(acc[4]));
    SCHED();
#pragma unroll
    for (int j = 2; j >= 0; --j) {
      const f32x4 v = __builtin_elementwise_fma(gf[j], o4[j] + acc[2 + j], xf[j]);
      *(volatile LAS u32x2*)(lds + tw + j * 32) = (u32x2){pack2(v[0], v[1]), pack2(v[2], v[3])};
      if (j < 2) ov[j] = *(const volatile LAS u32x4*)(lds + tr + j * 32);
    }
    ST16S(ov[1], ro, oo + 32u, os, 0);
    ST16S(ov[0], ro, o1, os, 0);
    if (bid == 0 && w == 5 && l8 == 0u) st4(rs, WS_EPOCH, tag);
  }
  STAMP(19);
}

extern "C" __device__ __attribute__((used, noinline, visibility("hidden"))) void mega_tail() {}

extern "C" void kernel_launch(void* const* d_in, const int* in_sizes, int n_in, void* d_out, int out_size,
                              void* d_ws, size_t ws_size, hipStream_t stream) {
  Params p;
  p.q = (const u16*)d_in[0];
  p.k = (const u16*)d_in[1];
  p.v = (const u16*)d_in[2];
  p.shift_k = (const u16*)d_in[3];
  p.ssm_k = (const u16*)d_in[4];
  p.ssm_D = (const u16*)d_in[5];
  p.w_in = (const u16*)d_in[7];
  p.w_sp = (const u16*)d_in[8];
  p.b_sp = (const u16*)d_in[9];
  p.w_out = (const u16*)d_in[10];
  p.cvec = (const u16*)d_in[11];
  p.w_mod = (const u16*)d_in[12];
  p.out = (u16*)d_out;
  p.ws = (char*)d_ws;
  mega<<<256, 512, 0, stream>>>(p);
}
```

```cpp
#define GT 790
#include <hip/hip_runtime.h>
#include <stdint.h>

typedef unsigned short u16;
typedef __attribute__((ext_vector_type(8))) short bf16x8;
typedef __attribute__((ext_vector_type(4))) float f32x4;
typedef __attribute__((ext_vector_type(2))) float f32x2;
typedef __attribute__((ext_vector_type(4))) unsigned int u32x4;
typedef __attribute__((ext_vector_type(2))) unsigned int u32x2;
#define LAS __attribute__((address_space(3)))
typedef __amdgpu_buffer_rsrc_t rsrc_t;
typedef __attribute__((ext_vector_type(4))) short s16x4;

constexpr int S_ = 2048, D_ = 640, F_ = 1728, FH = 864, CB = 108;

constexpr unsigned WS_EPOCH = 0;
constexpr unsigned WS_FWI = 4096;
constexpr unsigned WS_FZ = WS_FWI + 384 * 128;
constexpr unsigned WS_FWO = WS_FZ + 256 * 128;
constexpr unsigned F4S = 896 * 128;
constexpr unsigned WS_CS = WS_FZ + 256 * 128;
constexpr unsigned WS_MODP = 212992;
constexpr unsigned WS_ST1 = 393216;
constexpr unsigned WS_PART = 524288;
constexpr unsigned WS_CSP = WS_PART + 262144;
constexpr unsigned WS_FX = WS_CSP + 655360;
constexpr unsigned WS_X1B = WS_FX + 16 * 4 * 160 * 128;
constexpr unsigned WS_QB = WS_X1B + 2621440;
constexpr unsigned WS_KV3 = WS_QB + 2621440;
constexpr unsigned WS_MODQ = WS_KV3 + 1310720;
constexpr unsigned WBLK = 143360;
constexpr unsigned WS_WINF = WS_MODQ + 245760;
constexpr unsigned WS_WOUTF = WS_WINF + 16 * WBLK;
constexpr unsigned WS_Z2F = WS_WOUTF + 8 * WBLK;
constexpr unsigned WS_UF = WS_Z2F + 8 * 64 * 7168;
constexpr unsigned WS_F4 = WS_UF + 32 * 4 * 8 * 4096;
constexpr unsigned WS_QCK = WS_F4 + 4 * F4S;
constexpr unsigned WS_END = WS_QCK + 131072;
constexpr unsigned WS_STAMP = 200u << 20;

constexpr int G_COPY = 4160;
constexpr int L_U = WBLK;
constexpr int U_CH = (256 + S_) * 2 + 32;
constexpr int L_FLG = L_U + 4 * U_CH;
constexpr int L_RT = 16384;
constexpr int L_CONST = WBLK;
constexpr int L_PST = L_CONST + 2048;
constexpr int L_Z = L_CONST + 5120;
constexpr int L_ST2 = L_Z + 14336;
constexpr int L_MISC = L_ST2 + 512;
constexpr int LDS_TOTAL = 163840;

#define AUX_T 610
#define RT2_T 360
#define CB_T 380
#define W_T 670
#define YG_T 870
#define KE_T 460
#define K_T 480
#define GA 0
#define WA_T 580
constexpr int SC1 = 16, NT = 2;
constexpr unsigned SPIN_CAP = 1u << 16;

__device__ __forceinline__ float bflo(unsigned x) { return __uint_as_float(x << 16); }
__device__ __forceinline__ float bfhi(unsigned x) { return __uint_as_float(x & 0xffff0000u); }
__device__ __forceinline__ unsigned f2bf(float x) {
  __bf16 b = (__bf16)x;
  return (unsigned)__builtin_bit_cast(unsigned short, b);
}
__device__ __forceinline__ float rbf(float x) { return bflo(f2bf(x)); }
typedef __attribute__((ext_vector_type(2))) __bf16 bf2_t;
__device__ __forceinline__ unsigned pack2(float lo, float hi) {
  f32x2 v = {lo, hi};
  return __builtin_bit_cast(unsigned, __builtin_convertvector(v, bf2_t));
}
__device__ __forceinline__ f32x2 rbf2(f32x2 v) {
  const unsigned p = __builtin_bit_cast(unsigned, __builtin_convertvector(v, bf2_t));
  return (f32x2){__uint_as_float(p << 16), __uint_as_float(p & 0xffff0000u)};
}
__device__ __forceinline__ f32x4 rbf4(f32x4 v) {
  const f32x2 a = rbf2((f32x2){v[0], v[1]}), b = rbf2((f32x2){v[2], v[3]});
  return (f32x4){a[0], a[1], b[0], b[1]};
}
__device__ __forceinline__ f32x4 fma4(f32x4 a, f32x4 b, f32x4 c) {
  float r[4];
  asm("v_fma_f32 %0, %4, %8, %12\n\tv_fma_f32 %1, %5, %9, %13\n\tv_fma_f32 %2, %6, %10, %14\n\tv_fma_f32 %3, %7, %11, %15"
      : "=&v"(r[0]), "=&v"(r[1]), "=&v"(r[2]), "=&v"(r[3])
      : "v"(a[0]), "v"(a[1]), "v"(a[2]), "v"(a[3]), "v"(b[0]), "v"(b[1]), "v"(b[2]), "v"(b[3]), "v"(c[0]), "v"(c[1]), "v"(c[2]), "v"(c[3]));
  return (f32x4){r[0], r[1], r[2], r[3]};
}
template <int CTRL>
__device__ __forceinline__ float dpp_f(float v) {
  return __int_as_float(__builtin_amdgcn_update_dpp(0, __float_as_int(v), CTRL, 0xf, 0xf, true));
}
__device__ __forceinline__ float sum8(float v) {
  v += dpp_f<0xB1>(v);
  v += dpp_f<0x4E>(v);
  v += dpp_f<0x141>(v);
  return v;
}
__device__ __forceinline__ float sum16(float v) {
  v = sum8(v);
  v += dpp_f<0x140>(v);
  return v;
}
__device__ __forceinline__ float sum_rows(float v) {
  auto a = __builtin_amdgcn_permlane16_swap(__float_as_uint(v), __float_as_uint(v), false, false);
  v = __uint_as_float(a[0]) + __uint_as_float(a[1]);
  auto b = __builtin_amdgcn_permlane32_swap(__float_as_uint(v), __float_as_uint(v), false, false);
  return __uint_as_float(b[0]) + __uint_as_float(b[1]);
}
#define MFMA16(a, b, c) __builtin_amdgcn_mfma_f32_16x16x32_bf16(__builtin_bit_cast(bf16x8, a), __builtin_bit_cast(bf16x8, b), c, 0, 0, 0)
#define MFMA_IP(c, a, b) asm volatile("v_mfma_f32_16x16x32_bf16 %0, %1, %2, %0" : "+v"(c) : "v"(a), "v"(b))
__device__ __forceinline__ u32x2 trd(const char* p) {
  return __builtin_bit_cast(u32x2, __builtin_amdgcn_ds_read_tr16_b64_v4i16((LAS s16x4*)p));
}
__device__ __forceinline__ u32x4 trfrag(const char* p, int pitch) {
  u32x2 a = trd(p), b = trd(p + 16 * pitch);
  return (u32x4){a[0], a[1], b[0], b[1]};
}
#define BAR() asm volatile("s_waitcnt lgkmcnt(0)\n\ts_barrier" ::: "memory")
#define DRAIN() do { __builtin_amdgcn_s_waitcnt(0x0F70); asm volatile("" ::: "memory"); } while (0)
#define SCHED() __builtin_amdgcn_sched_barrier(0)
template <int N> struct IC { static constexpr int value = N; };
template <int I, int N, class F>
__device__ __forceinline__ void sfor(F&& f) {
  if constexpr (I < N) { f(IC<I>()); sfor<I + 1, N>(f); }
}

__device__ __forceinline__ u32x2 ld8(rsrc_t r, unsigned off) { return __builtin_amdgcn_raw_buffer_load_b64(r, off, 0, SC1); }
__device__ __forceinline__ unsigned ld4(rsrc_t r, unsigned off) { return __builtin_amdgcn_raw_buffer_load_b32(r, off, 0, SC1); }
__device__ __forceinline__ void st16(rsrc_t r, unsigned off, u32x4 v) { __builtin_amdgcn_raw_buffer_store_b128(v, r, off, 0, SC1); }
#define ST16S(v, r, voff, soff, aux) do { const u32x4 v_ = (v); __builtin_amdgcn_raw_buffer_store_b128(v_, r, voff, soff, aux); asm volatile("s_nop 1" :: "v"(v_)); } while (0)
__device__ __forceinline__ void st4(rsrc_t r, unsigned off, unsigned v) { __builtin_amdgcn_raw_buffer_store_b32(v, r, off, 0, SC1); }
__device__ __forceinline__ void dma1k(rsrc_t r, char* lds_dst, unsigned l16, unsigned soff) {
  __builtin_amdgcn_raw_ptr_buffer_load_lds(r, (LAS void*)lds_dst, 16, l16, soff, 0, SC1);
}

#define STAMP(i)
#define STAMPF(i)
#define STAMPT(i) STAMP(i)

template <int N, int SLP>
__device__ __forceinline__ void wait_lines(rsrc_t r, const unsigned (&off)[N], unsigned tag) {
  bool need[N];
#pragma unroll
  for (int i = 0; i < N; ++i) need[i] = off[i] != ~0u;
  _Pragma("unroll 1") for (unsigned spins = 0; spins < SPIN_CAP; ++spins) {
    unsigned v[N];
#pragma unroll
    for (int i = 0; i < N; ++i) {
      v[i] = tag;
      if (need[i]) v[i] = ld4(r, off[i]);
    }
    bool any = false;
#pragma unroll
    for (int i = 0; i < N; ++i) { need[i] = v[i] != tag; any = any || need[i]; }
    if (!__any(any)) break;
    __builtin_amdgcn_s_sleep(SLP);
  }
  asm volatile("" ::: "memory");
}

__device__ __forceinline__ int tau(int rt, int n) { return 32 * (rt >> 1) + 8 * (n >> 2) + 4 * (rt & 1) + (n & 3); }
__device__ __forceinline__ int psi(int n) { return 2 * (n & 7) + (n >> 3); }
#define QX_CM(d, s0, s1, P) "v_cndmask_b32_dpp " d ", " s0 ", " s1 ", vcc quad_perm:" P " row_mask:0xf bank_mask:0xf\n\t"
#define QX_BODY(M, P) \
  asm("s_mov_b32 vcc_lo, " M "\n\ts_mov_b32 vcc_hi, " M "\n\t" \
      QX_CM("%0", "%12", "%8", P) "v_xor_b32 %4, %8, %0\n\t" QX_CM("%1", "%13", "%9", P) "v_xor_b32 %5, %9, %1\n\t" \
      QX_CM("%2", "%14", "%10", P) "v_xor_b32 %6, %10, %2\n\t" QX_CM("%3", "%15", "%11", P) "v_xor_b32 %7, %11, %3" \
      : "=&v"(t[0]), "=&v"(t[1]), "=&v"(t[2]), "=&v"(t[3]), "=&v"(u[0]), "=&v"(u[1]), "=&v"(u[2]), "=&v"(u[3]) \
      : "v"(a[0]), "v"(a[1]), "v"(a[2]), "v"(a[3]), "v"(b[0]), "v"(b[1]), "v"(b[2]), "v"(b[3]) : "vcc")
template <int BIT>
__device__ __forceinline__ void quad_xchg3(u32x4& a, u32x4& b) {
  unsigned t[4], u[4];
  if (BIT == 0) QX_BODY("0x55555555", "[1,0,3,2]");
  else QX_BODY("0x33333333", "[2,3,0,1]");
  a = (u32x4){t[0], t[1], t[2], t[3]};
#pragma unroll
  for (int d = 0; d < 4; ++d) b[d] ^= (unsigned)__builtin_amdgcn_update_dpp(0, (int)u[d], BIT == 0 ? 0xB1 : 0x4E, 0xf, 0xf, true);
}
struct Params {
  const u16 *q, *k, *v, *shift_k, *ssm_k, *ssm_D, *w_in, *w_sp, *b_sp, *w_out, *cvec, *w_mod;
  u16* out;
  char* ws;
};

__global__ __launch_bounds__(512, 2) void mega(Params p) {
  __shared__ __attribute__((aligned(16))) char lds[LDS_TOTAL];
  {
    unsigned long long pc0;
    unsigned vt;
    asm volatile("v_readfirstlane_b32 vcc_lo, %3\n\ts_cmpk_lt_u32 %2, 192\n\ts_cbranch_scc1 4f\n\ts_cmpk_lt_u32 vcc_lo, 64\n\ts_cbranch_scc0 4f\n\t"
                 "s_lshr_b32 vcc_hi, %2, 3\n\ts_sub_u32 vcc_hi, vcc_hi, 24\n\ts_lshl_b32 vcc_hi, vcc_hi, 13\n\t"
                 "v_lshlrev_b32 %1, 7, %3\n\tv_add_u32 %1, vcc_hi, %1\n\ts_getpc_b64 %0\n.Lmega_here:\n\t"
                 "v_min_u32 %1, mega_tail-.Lmega_here-4, %1\n\ts_movk_i32 m0, 0x2000\n\ts_nop 1\n\tglobal_load_lds_dword %1, %0\n4:\n\t"
                 "s_cmpk_lt_u32 %2, 160\n\ts_cbranch_scc0 1f\n\ts_cmpk_lt_u32 vcc_lo, 256\n\ts_cbranch_scc1 3f\n\t"
                 "s_call_b64 vcc, .Ltouch1\n\ts_branch 3f\n1:\n\t"
                 "s_cmpk_lt_u32 vcc_lo, 256\n\ts_cbranch_scc1 2f\n\ts_cmpk_lt_u32 %2, 224\n\ts_cbranch_scc0 2f\n\ts_call_b64 vcc, .Ltouch3\n\ts_branch 3f\n"
                 "2:\n\ts_call_b64 vcc, .Ltouch2\n3:"
                 : "=&s"(pc0), "=&v"(vt) : "s"(blockIdx.x), "v"(threadIdx.x) : "vcc", "scc", "memory");
  }
  SCHED();
  const int tid = threadIdx.x, l = tid & 63;
  const int w = __builtin_amdgcn_readfirstlane(tid >> 6);
  const int bid = blockIdx.x, xcd = bid & 7, idx = bid >> 3;
#define TOUCH_AT(n) asm volatile("s_branch 1f\n.Ltouch" #n ":\n\ts_setpc_b64 vcc\n1:")
  char* const ws = p.ws;
  asm volatile("" ::"s"(p.q), "s"(p.k), "s"(p.v), "s"(p.shift_k), "s"(p.ssm_k), "s"(p.ssm_D), "s"(p.w_in), "s"(p.w_sp), "s"(p.b_sp),
               "s"(p.w_out), "s"(p.cvec), "s"(p.w_mod), "s"(p.out), "s"(p.ws));
  unsigned epoch, tag, t_start;
  unsigned long long t64;
  asm volatile("s_load_dword %0, %2, 0x0\n\ts_memrealtime %1" : "=&s"(epoch), "=&s"(t64) : "s"(ws));
#define TAG_READY() do { SCHED(); \
    asm volatile("s_waitcnt lgkmcnt(0)\n\ts_mul_i32 %0, %2, 0x9e3779b1\n\ts_add_i32 %0, %0, 0x9e3779b9\n\ts_mov_b64 %1, %3" \
                 : "=&s"(tag), "=&s"(t64r) : "s"(epoch), "s"(t64) : "scc"); t_start = (unsigned)t64r; SCHED(); } while (0)
  unsigned long long t64r;
  const rsrc_t rs = __builtin_amdgcn_make_buffer_rsrc(ws, 0, WS_END, 0x00020000);
  const unsigned l16 = (unsigned)l * 16u;
  STAMP(0);
  const bool is_conv = idx < 20;
  const int quad = xcd * 20 + idx;
  const int R = is_conv ? (xcd & 3) * 5 + (idx >> 2) : (xcd & 3) * 3 + 15 + (idx >> 2);
  const int c = (xcd >> 2) * 4 + (idx & 3);
  const unsigned fwi = WS_F4 + (unsigned)(xcd & 3) * F4S, fz = fwi + (WS_FZ - WS_FWI);
  const int n16 = l & 15, h4 = l >> 4;
  u32x4 wsp[4][8];
  const int rotg = ((idx >> 4) ^ (idx >> 1)) & 1, rotb = (idx >> 2) & 3;
  const rsrc_t rsp = __builtin_amdgcn_make_buffer_rsrc((void*)p.w_sp, 0, S_ * S_ * 2, 0x00020000);
  const unsigned wsp_vo = (unsigned)(((l >> 2) & 3) * 8 * 4096 + h4 * 64 + ((l + rotb) & 3) * 16);
#define LDW(k) wsp[((k) >> 2) & 3][((k) >> 4) * 4 + ((k) & 3)] = __builtin_amdgcn_raw_buffer_load_b128(rsp, wvo, \
    (unsigned)((R * 64 + 16 * (((k) >> 2) & 2) + 4 * (((k) >> 2) & 1) + ((k) & 3)) * 4096 + w * 512 + ((((k) >> 4) ^ rotg) * 256)), 0)
#define WSP_Q(g, k) wsp[(g) & 3][(((g) >> 2) & 1) * 4 + (k)]
#define WSP_TX(x) do { if (((x) & 2) == 0) quad_xchg3<0>(WSP_Q((x) >> 2, 2 * ((x) & 1)), WSP_Q((x) >> 2, 2 * ((x) & 1) + 1)); \
    else quad_xchg3<1>(WSP_Q((x) >> 2, (x) & 1), WSP_Q((x) >> 2, ((x) & 1) + 2)); \
    asm volatile("" : "+v"(WSP_Q((x) >> 2, 0)), "+v"(WSP_Q((x) >> 2, 1)), "+v"(WSP_Q((x) >> 2, 2)), "+v"(WSP_Q((x) >> 2, 3))); } while (0)
  const rsrc_t rwi = __builtin_amdgcn_make_buffer_rsrc((void*)p.w_in, 0, D_ * F_ * 2, 0x00020000);
  const rsrc_t rwo = __builtin_amdgcn_make_buffer_rsrc((void*)p.w_out, 0, FH * D_ * 2, 0x00020000);
  const rsrc_t rq = __builtin_amdgcn_make_buffer_rsrc((void*)p.q, 0, S_ * D_ * 2, 0x00020000);
  auto hold = [&](unsigned tt) { while ((unsigned)__builtin_amdgcn_s_memrealtime() - t_start < tt) __builtin_amdgcn_s_sleep(2); };
  auto ne2 = [&](unsigned x, unsigned y) { unsigned d; asm("v_bitop3_b32 %0, %1, %2, %3 bitop3:0x7e" : "=v"(d) : "v"(x), "v"(y), "s"(tag)); return d; };
  auto sum8u = [&](unsigned v) {
    v += (unsigned)__builtin_amdgcn_update_dpp(0, (int)v, 0xB1, 0xf, 0xf, true);
    v += (unsigned)__builtin_amdgcn_update_dpp(0, (int)v, 0x4E, 0xf, 0xf, true);
    return v + (unsigned)__builtin_amdgcn_update_dpp(0, (int)v, 0x141, 0xf, 0xf, true);
  };
  volatile LAS unsigned* lf = (volatile LAS unsigned*)(lds + L_FLG);
  auto lf_set = [&](int i) { asm volatile("s_waitcnt lgkmcnt(0)" ::: "memory"); if (l == 0) lf[i] = tag; };
  auto lf_wait = [&](int i0, int n) {
    _Pragma("unroll 1") for (unsigned spins = 0; spins < (SPIN_CAP << 4); ++spins) {
      const unsigned v = lf[i0 + (l & (n - 1))];
      if (__all(v == tag)) break;
      __builtin_amdgcn_s_sleep(1);
    }
    asm volatile("" ::: "memory");
  };
  const unsigned b2o = fwi + (unsigned)((8 + c) * 20 + 5 * (w & 3) + (l < 5 ? l : 0)) * 128u;
  auto b2_wait = [&](u32x2 fv, int slp) {
    if (!__all(fv[0] == tag)) {
      _Pragma("unroll 1") for (unsigned spins = 0; spins < SPIN_CAP; ++spins) {
        if (fv[0] != tag) fv = ld8(rs, b2o);
        if (__all(fv[0] == tag)) break;
        if (slp) __builtin_amdgcn_s_sleep(8); else __builtin_amdgcn_s_sleep(4);
      }
    }
    asm volatile("" ::: "memory");
    if (l < 5) lf[44 + 5 * w + l] = fv[1];
    if (l == 0) lf[36 + w] = tag;
  };
  auto load_b2 = [&](int k0, int k1) {
    const unsigned a0 = (unsigned)(35 * w + (idx >> 2) * 4) * 1024u, gb = WS_WINF + (unsigned)(8 + c) * WBLK;
    unsigned a = a0, g = gb + a0;
#pragma unroll
    for (int k = k0; k < k1; ++k) {
      if (k == k0 || (k & 3) == 3) {
        a = k + (idx >> 2) * 4 >= 35 ? a0 - 35840u : a0;
        g = gb + a;
        asm volatile("" : "+s"(a), "+s"(g));
      }
      dma1k(rs, lds + a + k * 1024, l16, g + (unsigned)k * 1024u);
    }
  };
  char* const wo_st = lds + 122880;
  auto wo_ld = [&]() {
    const int U = R < 28 ? R : 0, u2 = U >> 1, fi = (u2 + 1) % 3;
    const int c0 = 4 * (U & 1) + (u2 < 8 ? u2 >> 1 : u2 < 11 ? 0 : 2);
    const int b0 = u2 < 8 ? CB * c0 + 2 * (u2 & 1) + 96 : CB * (c0 + (fi >> 1)) + 48 + 2 * (fi & 1), d1 = u2 < 8 ? -96 : 16, d2 = d1 + 16;
    const int b3 = u2 < 8 ? b0 - 64 : CB * (c0 + 1) + 50 + 16 * fi;
#pragma unroll
    for (int r = 0; r < 5; ++r) {
      const int g = r * 64 + l, rho = g / 10;
      const int fb = (rho & 16) ? ((rho & 2) ? b3 : b0 + d2) : ((rho & 2) ? b0 + d1 : b0);
      __builtin_amdgcn_raw_ptr_buffer_load_lds(rwo, (LAS void*)(wo_st + r * 1024), 16, (unsigned)((fb + (rho & 13)) * (D_ * 2) + (g % 10) * 16 + c * 160), 0, 0, 0);
    }
  };
  auto rt_ld = [&](int i0, char* st) {
    const int cb = (i0 < 160 ? 8 : 0) + (i0 & 7), Q = (i0 < 160 ? i0 : i0 - 160) >> 3;
#pragma unroll
    for (int r = 0; r < 7; ++r) {
      const int g = r * 64 + l;
      __builtin_amdgcn_raw_ptr_buffer_load_lds(rwi, (LAS void*)(st + r * 1024), 16, (unsigned)((g / 14) * (F_ * 2) + (g % 14) * 16 + Q * 32 * F_ * 2 + cb * CB * 2),
                                               0, 0, NT);
    }
  };
  auto rt_job = [&](int i0, char* st) {
    const int cb = (i0 < 160 ? 8 : 0) + (i0 & 7), Q = (i0 < 160 ? i0 : i0 - 160) >> 3;
    const int col = 640 * (l >> 5) + 32 * Q + (l & 31);
    u32x2 pp8[8];
    auto ld_pp = [&]() {
#pragma unroll
      for (int i = 0; i < 8; ++i) pp8[i] = __builtin_amdgcn_raw_buffer_load_b64(rs, (unsigned)col * 8u, WS_MODP + (unsigned)i * 10240u, SC1);
    };
    auto pp_bad = [&]() { return __any(((ne2(pp8[0][0], pp8[1][0]) | ne2(pp8[2][0], pp8[3][0]) | ne2(pp8[4][0], pp8[5][0])) | ne2(pp8[6][0], pp8[7][0])) != 0u); };
    hold(CB_T);
    ld_pp();
    SCHED();
    __builtin_amdgcn_s_waitcnt(0x0F78);
    asm volatile("" ::: "memory");
    u32x4 x[7], o[7];
    f32x2 xu[7][4];
    {
      const char* bp = st + (4 * h4 + (n16 >> 2)) * 224 + (n16 & 3) * 8;
#pragma unroll
      for (int j = 0; j < 7; ++j) x[j] = trfrag(bp + j * 32, 224);
#pragma unroll
      for (int j = 0; j < 7; ++j)
#pragma unroll
        for (int e = 0; e < 4; ++e) { xu[j][e] = (f32x2){bflo(x[j][e]), bfhi(x[j][e])}; asm volatile("" : "+v"(xu[j][e])); }
    }
    unsigned mk[4][2], cw = l >= 32 ? (unsigned)(7168 + (l - 32) * 4) : (unsigned)(7298 + l * 2 - 4), m12 = n16 == 12 ? ~0u : 0u;
    unsigned fgo = l < 32 ? (unsigned)(l >> 3) * F4S + (unsigned)(l & 7) * 16u : 0x80000000u;
#pragma unroll
    for (int i = 0; i < 4; ++i) {
      mk[i][0] = n16 == 4 * i ? 0x3F803F80u : 0u; mk[i][1] = n16 == 4 * i + 1 ? ~0u : 0u;
      asm volatile("" : "+v"(mk[i][0]), "+v"(mk[i][1]));
    }
    asm volatile("" : "+v"(cw), "+v"(m12), "+v"(fgo));
    unsigned ob = WS_WINF + (unsigned)cb * WBLK + (unsigned)(Q * 7) * 1024u;
    asm volatile("" : "+s"(ob));
    if (pp_bad()) {
      _Pragma("unroll 1") for (unsigned spins = 0; spins < SPIN_CAP; ++spins) {
        __builtin_amdgcn_s_sleep(4);
        ld_pp();
        if (!pp_bad()) break;
      }
    }
    STAMPF(9);
    __builtin_amdgcn_s_setprio(3);
    {
      auto pf = [&](int i) { return __uint_as_float(pp8[i][1]); };
      const float t = rbf(((pf(0) + pf(1)) + (pf(2) + pf(3))) + ((pf(4) + pf(5)) + (pf(6) + pf(7))));
      if (l >= 32) *(float*)(st + cw) = rbf(1.0f + t);
      else *(u16*)(st + cw + 2) = (u16)(__float_as_uint(t) >> 16);
    }
    asm volatile("" ::: "memory");
    const f32x4 ca = *(const volatile LAS f32x4*)(st + 7168 + h4 * 16), cc = *(const volatile LAS f32x4*)(st + 7168 + 64 + h4 * 16);
    const u32x2 e0 = *(const volatile LAS u32x2*)(st + 7296 + h4 * 8), e1 = *(const volatile LAS u32x2*)(st + 7328 + h4 * 8);
    SCHED();
#pragma unroll
    for (int j = 0; j < 7; ++j) {
      const f32x2 y0 = xu[j][0] * (f32x2){ca[0], ca[1]}, y1 = xu[j][1] * (f32x2){ca[2], ca[3]}, y2 = xu[j][2] * (f32x2){cc[0], cc[1]}, y3 = xu[j][3] * (f32x2){cc[2], cc[3]};
      o[j] = (u32x4){pack2(y0[0], y0[1]), pack2(y1[0], y1[1]), pack2(y2[0], y2[1]), pack2(y3[0], y3[1])};
      if (j == 6) {
#pragma unroll
        for (int e = 0; e < 4; ++e) o[j][e] = (o[j][e] & ~m12) | mk[3][0];
      }
      ST16S(o[j], rs, l16 + (unsigned)(j & 3) * 1024u, ob + (unsigned)(j >> 2) * 4096u, SC1);
      SCHED();
    }
    __builtin_amdgcn_s_setprio(0);
    STAMPF(11);
    f32x4 d[2] = {{0.f, 0.f, 0.f, 0.f}, {0.f, 0.f, 0.f, 0.f}};
#pragma unroll
    for (int j = 0; j < 7; ++j) {
      const unsigned one = mk[j & 3][0], sr = mk[j & 3][1];
      d[j >> 2] = MFMA16(((u32x4){one, one, one, one}), o[j], d[j >> 2]);
      d[j >> 2] = MFMA16(((u32x4){sr & e0[0], sr & e0[1], sr & e1[0], sr & e1[1]}), x[j], d[j >> 2]);
    }
#pragma unroll
    for (int m = 0; m < 2; ++m)
      st16(rs, WS_CSP + (unsigned)((cb * 20 + Q) * 2 + m) * 1024u + l16, (u32x4){tag, __float_as_uint(d[m][0]), tag, __float_as_uint(d[m][1])});
    STAMPF(13);
    u32x4 tg4 = {tag, t_start, tag, tag};
    asm volatile("s_waitcnt vmcnt(2)" : "+v"(tg4) :: "memory");
    ST16S(tg4, rs, fgo, WS_F4 + (unsigned)(cb * 20 + Q) * 128u, SC1);
  };

  const rsrc_t rwm = __builtin_amdgcn_make_buffer_rsrc((void*)p.w_mod, 0, D_ * 3840 * 2, 0x00020000);
  auto gemv_load = [&](u32x4* pre, int blk, int sl, int np) {
    pre[10][0] = *(const unsigned*)(p.cvec + 8 * np * sl + 2 * (l < 4 * np ? l : 0));
#pragma unroll
    for (int pp = 0; pp < np; ++pp)
      pre[pp] = __builtin_amdgcn_raw_buffer_load_b128(rwm, (unsigned)((l >> 3) * 7680 + (l & 7) * 16), (unsigned)((8 * np * sl + pp * 8) * 7680 + blk * 128), NT);
  };
  auto gemv_fin = [&](float* acc, const u32x4* pre, int np, char* scr) {
    const unsigned cv2 = pre[10][0];
    unsigned* svt = (unsigned*)scr + w * 96;
#pragma unroll
    for (int e = 0; e < 2; ++e) {
      const int row = 2 * l + e;
      const float cv = e ? bfhi(cv2) : bflo(cv2);
      if (l < 4 * np) svt[(row & 7) * 12 + (row >> 3)] = __float_as_uint(rbf(cv * __builtin_amdgcn_rcpf(1.0f + __expf(-cv))));
    }
    asm volatile("s_waitcnt lgkmcnt(0)" ::: "memory");
    u32x4 sv[3];
#pragma unroll
    for (int i = 0; i < (np + 3) / 4; ++i) sv[i] = *(const u32x4*)(svt + (l >> 3) * 12 + 4 * i);
    SCHED();
#pragma unroll
    for (int e = 0; e < 8; ++e) acc[e] = 0.f;
#pragma unroll
    for (int pp = 0; pp < np; ++pp) {
      const unsigned sh = sv[pp >> 2][pp & 3], sl2 = sh >> 16;
#pragma unroll
      for (int e = 0; e < 4; ++e) {
        const unsigned x = pre[pp][e];
        acc[2 * e] = __builtin_amdgcn_fdot2_f32_bf16(__builtin_bit_cast(bf2_t, x), __builtin_bit_cast(bf2_t, sl2), acc[2 * e], false);
        acc[2 * e + 1] = __builtin_amdgcn_fdot2_f32_bf16(__builtin_bit_cast(bf2_t, x), __builtin_bit_cast(bf2_t, sh), acc[2 * e + 1], false);
      }
    }
#pragma unroll
    for (int e = 0; e < 4; ++e) {
      auto x = __builtin_amdgcn_permlane32_swap(__float_as_uint(acc[e]), __float_as_uint(acc[e + 4]), false, false);
      acc[e] = __uint_as_float(x[0]) + __uint_as_float(x[1]);
    }
#pragma unroll
    for (int e = 0; e < 2; ++e) {
      auto x = __builtin_amdgcn_permlane16_swap(__float_as_uint(acc[e]), __float_as_uint(acc[e + 2]), false, false);
      acc[e] = __uint_as_float(x[0]) + __uint_as_float(x[1]);
    }
    const float t0 = acc[0] + dpp_f<0x128>(acc[0]), t1 = acc[1] + dpp_f<0x128>(acc[1]);
    acc[0] = (l & 8) ? t1 : t0;
  };
  auto gemv_put = [&](const float* acc, unsigned off) {
    const float v = acc[0];
    const int src = ((l & 3) * 16 + ((l >> 2) & 7)) * 4;
    const unsigned v0 = (unsigned)__builtin_amdgcn_ds_bpermute(src, (int)__float_as_uint(v)), v1 = (unsigned)__builtin_amdgcn_ds_bpermute(src + 32, (int)__float_as_uint(v));
    if (l < 32) st16(rs, off + l16, (u32x4){tag, v0, tag, v1});
  };
  auto cs_ld = [&](u32x4* v, int cj) {
    const int cb = cj / 7, j = cj % 7;
#pragma unroll
    for (int k = 0; k < 5; ++k)
      v[k] = __builtin_amdgcn_raw_buffer_load_b128(rs, (unsigned)(h4 * 10240 + n16 * 16), WS_CSP + (unsigned)((cb * 40 + (j >> 2) + 2 * k) * 1024 + (j & 3) * 256), SC1);
  };
  auto cs_fin = [&](u32x4* v, int cj) {
    auto bad = [&]() { return __any(((ne2(v[0][0], v[0][2]) | ne2(v[1][0], v[1][2]) | ne2(v[2][0], v[2][2])) | ne2(v[3][0], v[3][2]) | ne2(v[4][0], v[4][2])) != 0u); };
    if (bad()) {
      _Pragma("unroll 1") for (unsigned spins = 0; spins < SPIN_CAP; ++spins) {
        __builtin_amdgcn_s_sleep(8);
        cs_ld(v, cj);
        if (!bad()) break;
      }
    }
    float t0 = 0.f, t1 = 0.f;
#pragma unroll
    for (int k = 0; k < 5; ++k) { t0 += __uint_as_float(v[k][1]); t1 += __uint_as_float(v[k][3]); }
    t0 = sum_rows(t0); t1 = sum_rows(t1);
    if (l < 16) st16(rs, WS_CS + (unsigned)((cj / 7) * 2048 + (cj % 7) * 256) + l16, (u32x4){tag, __float_as_uint(t0), tag, __float_as_uint(t1)});
  };
  const bool cs2 = w == 2 && R >= 20 && R < 27, cs1 = w == 2 && R >= 25;
  u32x4 csv[5];
  typedef const __attribute__((address_space(4))) unsigned* cptr_t;

  if (__builtin_expect(is_conv, 1)) {
    if (w >= 4) {
      TOUCH_AT(1);
      const int pw = w - 4;
      const rsrc_t ra = __builtin_amdgcn_make_buffer_rsrc((void*)((idx & 2) ? p.v : p.k), 0, S_ * D_ * 2, 0x00020000);
      const rsrc_t rb = __builtin_amdgcn_make_buffer_rsrc((void*)((idx & 2) ? p.k : p.v), 0, S_ * D_ * 2, 0x00020000);
      const rsrc_t rk = __builtin_amdgcn_make_buffer_rsrc((void*)p.k, 0, S_ * D_ * 2, 0x00020000);
      const rsrc_t rv = __builtin_amdgcn_make_buffer_rsrc((void*)p.v, 0, S_ * D_ * 2, 0x00020000);
      const unsigned vo = (unsigned)((128 * pw + 2 * l) * (D_ * 2));
      const unsigned so0 = (unsigned)((idx & 1) * (D_ * 2) + quad * 8), so1 = (unsigned)((1 - (idx & 1)) * (D_ * 2) + quad * 8);
      u32x2 g[3][4], kh;
      auto gl = [&](int s, int j0, int j1) {
        __builtin_amdgcn_s_setprio(0);
#pragma unroll
        for (int j = j0; j < j1; ++j) {
          if (s < 2) g[s][j] = __builtin_amdgcn_raw_buffer_load_b64(j < 2 ? ra : rb, vo, ((j & 1) ? so1 : so0) + (unsigned)(s * 512 * D_ * 2), 0);
          else g[s][j] = __builtin_amdgcn_raw_buffer_load_b64(j < 2 ? rk : rv, vo + (unsigned)((j & 1) * D_ * 2), (unsigned)(quad * 8 + s * 512 * D_ * 2), 0);
          SCHED();
        }
        __builtin_amdgcn_s_setprio(3);
      };
      const cptr_t sp = (cptr_t)(p.shift_k + quad * 16);
      unsigned spv[8];
#pragma unroll
      for (int i = 0; i < 8; ++i) spv[i] = sp[i];
      __builtin_amdgcn_s_sleep(3);
      gl(0, 0, 4);
      {
        const int row = 512 * (l >> 2) + 128 * pw - 3 + (l & 3);
        kh = __builtin_amdgcn_raw_buffer_load_b64(rk, (l >= 12 || (l & 3) == 3 || row < 0) ? 0x80000000u : (unsigned)(row * D_ * 2 + quad * 8), 0, 0);
      }
      TAG_READY();
      f32x2 wt[2][4];
#pragma unroll
      for (int d = 0; d < 2; ++d) {
        wt[d][0] = (f32x2){bflo(spv[4 * d]), bflo(spv[4 * d + 2])}; wt[d][1] = (f32x2){bfhi(spv[4 * d]), bfhi(spv[4 * d + 2])};
        wt[d][2] = (f32x2){bflo(spv[4 * d + 1]), bflo(spv[4 * d + 3])}; wt[d][3] = (f32x2){bfhi(spv[4 * d + 1]), bfhi(spv[4 * d + 3])};
        asm volatile("" : "+s"(wt[d][0]), "+s"(wt[d][1]), "+s"(wt[d][2]), "+s"(wt[d][3]));
      }
      SCHED();
      {
        const int j = idx * 4 + pw;
        const unsigned o = (unsigned)((512 + 64 * (j >> 2) + l) * (D_ * 2) + (((160 * xcd) >> 7) + ((j >> 1) & 1)) * 128);
        __builtin_amdgcn_raw_ptr_buffer_load_lds((j & 1) ? ra : rb, (LAS void*)(lds + 98304), 4, j < 64 ? o : 0x80000000u, 0, 0, 0);
      }
      SCHED();
      gl(1, 0, 4);
      STAMP(21);
      unsigned up = (unsigned)(L_U + 512 + 256 * pw + (l >> 2) * 16 + (3 - (l & 3)) * 4);
      unsigned lfo = l == 0 ? (unsigned)(L_FLG + 4 * pw) : (unsigned)(100352 + tid * 4), lfr = (unsigned)(L_FLG + (l & 3) * 4);
      unsigned yr = (unsigned)(L_U + (n16 >> 2) * U_CH + (128 * pw + 32 * h4 + 8 * (n16 & 1) + 4 * ((n16 >> 1) & 1)) * 2);
      unsigned yo = (unsigned)(8 * pw + (l >> 3)) * 20480u + (unsigned)quad * 128u + (unsigned)(l & 7) * 16u;
      unsigned mo = (unsigned)((n16 < 12 ? n16 >> 2 : 0) * D_ + quad * 4 + (l & 3)) * 8u + (unsigned)h4 * 40960u;
      unsigned k3o = (unsigned)(16 * pw + (l >> 2)) * 10880u + (unsigned)((l & 3) * 16);
      unsigned k3d = (unsigned)(L_U + (l & 3) * U_CH + 512 + (1536 + 128 * pw + 8 * (l >> 2)) * 2);
      unsigned xfo = (unsigned)(((4 * (l >> 5) + pw) * 4 + ((l >> 3) & 3)) * 160 + quad) * 128u + (unsigned)(l & 7) * 16u;
      u32x4 tg4 = {tag, tag, tag, tag};
      asm volatile("" : "+v"(up), "+v"(lfo), "+v"(lfr), "+v"(yr), "+v"(yo), "+v"(mo), "+v"(k3o), "+v"(k3d), "+v"(xfo), "+v"(tg4));
      unsigned so = WS_ST1 + (unsigned)xcd * 16384u + (unsigned)pw * 1024u;
      asm volatile("" : "+s"(so));
      auto pf_set = [&](int i) { asm volatile("" ::: "memory"); *(volatile LAS unsigned*)(lds + lfo + 4 * i) = tg4[0]; };
      auto pf_wait = [&](int i0) {
        _Pragma("unroll 1") for (unsigned spins = 0; spins < (SPIN_CAP << 4); ++spins) {
          if (__all(*(volatile LAS unsigned*)(lds + lfr + 4 * i0) == tag)) break;
          __builtin_amdgcn_s_sleep(1);
        }
        asm volatile("" ::: "memory");
      };
      unsigned hl[2][3];
      auto halo = [&](int s) {
#pragma unroll
        for (int i = 0; i < 6; ++i) { hl[i & 1][i >> 1] = (unsigned)__builtin_amdgcn_readlane(kh[i & 1], 4 * s + (i >> 1)); asm volatile("" : "+v"(hl[i & 1][i >> 1])); }
      };
      auto kv = [&](auto S__, auto N__) {
        constexpr int s = decltype(S__)::value;
        u32x2* x = g[s];
        SCHED();
        asm volatile("s_waitcnt vmcnt(%5)" : "+v"(x[0]), "+v"(x[1]), "+v"(x[2]), "+v"(x[3]), "+v"(kh) : "n"(decltype(N__)::value));
        if (s == 2) STAMP(27);
        if (s == 0) halo(0);
        if (s < 2) {
          if (idx & 1) { asm volatile(""); const u32x2 t = x[0], u = x[2]; x[0] = x[1]; x[1] = t; x[2] = x[3]; x[3] = u; }
          asm volatile("" : "+v"(x[0]), "+v"(x[1]), "+v"(x[2]), "+v"(x[3]));
          if (idx & 2) { asm volatile(""); const u32x2 t = x[0], u = x[1]; x[0] = x[2]; x[1] = x[3]; x[2] = t; x[3] = u; }
          asm volatile("" : "+v"(x[0]), "+v"(x[1]), "+v"(x[2]), "+v"(x[3]));
        }
        f32x2 kf[2][5], o[2][2];
#pragma unroll
        for (int d = 0; d < 2; ++d) {
          unsigned kk[5];
          kk[3] = x[0][d]; kk[4] = x[1][d];
          kk[2] = __builtin_amdgcn_update_dpp(hl[d][2], kk[4], 0x138, 0xf, 0xf, false);
          kk[1] = __builtin_amdgcn_update_dpp(hl[d][1], kk[3], 0x138, 0xf, 0xf, false);
          kk[0] = __builtin_amdgcn_update_dpp(hl[d][0], kk[2], 0x138, 0xf, 0xf, false);
#pragma unroll
          for (int j = 0; j < 5; ++j) kf[d][j] = (f32x2){bflo(kk[j]), bfhi(kk[j])};
        }
#pragma unroll
        for (int t = 0; t < 5; ++t) {
#pragma unroll
          for (int i = 0; i < 4; ++i) {
            const int d = i >> 1, r = i & 1;
            if (t == 4) o[d][r] *= (f32x2){bflo(x[2 + r][d]), bfhi(x[2 + r][d])};
            else if (t == 0) o[d][r] = kf[d][r + 3] * wt[d][0];
            else o[d][r] = __builtin_elementwise_fma(kf[d][r + 3 - t], wt[d][t], o[d][r]);
          }
          asm volatile("" : "+v"(o[0][0]), "+v"(o[0][1]), "+v"(o[1][0]), "+v"(o[1][1]));
        }
#pragma unroll
        for (int i = 0; i < 4; ++i) *(unsigned*)(lds + up + i * U_CH + s * 1024) = pack2(o[i >> 1][1][i & 1], o[i >> 1][0][i & 1]);
        pf_set(4 * s);
        SCHED();
        if (s < 2) halo(s + 1);
        SCHED();
      };
      u32x4 sv[4], qv[4];
      u32x2 mv[2];
      auto ld_e = [&]() {
#pragma unroll
        for (int s = 0; s < 4; ++s) sv[s] = __builtin_amdgcn_raw_buffer_load_b128(rs, l16, so + (unsigned)s * 4096u, SC1);
        SCHED();
      };
      auto s_bad = [&]() { return __any(((ne2(sv[0][1], sv[0][3]) | ne2(sv[1][1], sv[1][3]) | ne2(sv[2][1], sv[2][3])) | ne2(sv[3][1], sv[3][3])) != 0u); };
      kv(IC<0>(), IC<5>());
      STAMP(23);
      gl(2, 0, 4);
      kv(IC<1>(), IC<4>());
      STAMPF(9);
      u32x4 k3;
      unsigned k3c;
      auto ld_k3 = [&]() {
        k3 = __builtin_amdgcn_raw_buffer_load_b128(rs, k3o, WS_KV3 + (unsigned)quad * 64u, SC1);
        k3c = __builtin_amdgcn_raw_buffer_load_b32(rs, k3o & ~63u, WS_KV3 + 10240u + (unsigned)quad * 4u, SC1);
      };
      auto ld_m = [&]() {
        mv[0] = __builtin_amdgcn_raw_buffer_load_b64(rs, mo, WS_MODQ, SC1);
        mv[1] = __builtin_amdgcn_raw_buffer_load_b64(rs, mo, WS_MODQ + 20480u, SC1);
      };
      auto k_bad = [&]() {
        unsigned d = (k3[0] + k3[1]) + (k3[2] + k3[3]);
        d += (unsigned)__builtin_amdgcn_update_dpp(0, (int)d, 0xB1, 0xf, 0xf, true);
        d += (unsigned)__builtin_amdgcn_update_dpp(0, (int)d, 0x4E, 0xf, 0xf, true);
        return __any(d + k3c != tag);
      };
      float r1[4][2], m1[4][2];
      auto ln_pre = [&]() {
#pragma unroll
        for (int i = 0; i < 8; ++i) {
          const unsigned x = sv[i >> 1][2 * (i & 1)];
          r1[i >> 1][i & 1] = __builtin_amdgcn_rcpf(bfhi(x));
          m1[i >> 1][i & 1] = -bflo(x) * r1[i >> 1][i & 1];
          asm volatile("" : "+v"(r1[i >> 1][i & 1]), "+v"(m1[i >> 1][i & 1]));
        }
      };
      unsigned qck[4], qco = (unsigned)((8 * pw + (l >> 3)) * 1024 + (quad / 40) * 256 + (quad % 40) * 4);
      asm volatile("" : "+v"(qco));
      auto ld_qc = [&]() {
#pragma unroll
        for (int s = 0; s < 4; ++s) {
          qv[s] = __builtin_amdgcn_raw_buffer_load_b128(rs, yo, WS_QB + (unsigned)s * (32u * 20480u), SC1);
          qck[s] = __builtin_amdgcn_raw_buffer_load_b32(rs, qco, WS_QCK + (unsigned)s * 32768u, SC1);
        }
        SCHED();
      };
      auto q_bad = [&]() {
        unsigned d = 0u;
#pragma unroll
        for (int s = 0; s < 4; ++s) d += (qv[s][0] + qv[s][1]) + (qv[s][2] + qv[s][3]);
        return __any(sum8u(d) + ((qck[0] + qck[1]) + (qck[2] + qck[3])) != tag * 4u);
      };
      const bool early = (unsigned)__builtin_amdgcn_s_memrealtime() - t_start < KE_T;
      ld_e();
      if (!early) hold(K_T);
      {
        const unsigned em = early ? 0x80000000u : 0u;
        k3o |= em; yo |= em; qco |= em;
        ld_k3();
        SCHED();
        ld_qc();
        k3o ^= em; yo ^= em; qco ^= em;
      }
      kv(IC<2>(), IC<14>());
      STAMPF(11);
      auto s_get = [&]() {
        if (s_bad()) {
          _Pragma("unroll 1") for (unsigned spins = 0; spins < SPIN_CAP; ++spins) {
            __builtin_amdgcn_s_sleep(4);
            ld_e();
            if (!s_bad()) break;
          }
        }
        ln_pre();
      };
      bool sok = false;
      if (early) {
        sok = !s_bad();
        hold(K_T);
        if (!sok) ld_e();
        ld_k3();
        SCHED();
        ld_qc();
        if (sok) ln_pre();
      }
      if (k_bad()) {
        _Pragma("unroll 1") for (unsigned spins = 0; spins < SPIN_CAP; ++spins) {
          __builtin_amdgcn_s_sleep(4);
          ld_k3();
          if (!k_bad()) break;
        }
      }
      *(u32x4*)(lds + k3d) = k3;
      pf_set(12);
      SCHED();
      STAMPF(18);
      ld_m();
      SCHED();
      if (!sok) s_get();
      if (q_bad()) {
        _Pragma("unroll 1") for (unsigned spins = 0; spins < SPIN_CAP; ++spins) {
          __builtin_amdgcn_s_sleep(4);
          ld_qc();
          if (!q_bad()) break;
        }
      }
      STAMPF(17);
      STAMP(2);
      u32x2 yv[4][2];
      auto ld_y = [&](int s) {
#pragma unroll
        for (int e = 0; e < 2; ++e) yv[s][e] = trd(lds + yr + 1024 * s + 32 * e);
      };
      f32x4 qf[4][2], P[4][2];
      auto xln = [&](int s) {
#pragma unroll
        for (int e = 0; e < 2; ++e) {
          const float r = r1[s][e], m = m1[s][e];
          const f32x4 qq = {bflo(qv[s][2 * e]), bfhi(qv[s][2 * e]), bflo(qv[s][2 * e + 1]), bfhi(qv[s][2 * e + 1])};
          qf[s][e] = qq;
          P[s][e] = fma4(qq, (f32x4){r, r, r, r}, (f32x4){m, m, m, m});
        }
        asm volatile("" : "+v"(qf[s][0]), "+v"(qf[s][1]), "+v"(P[s][0]), "+v"(P[s][1]));
      };
      xln(0); xln(1); xln(2); xln(3);
      pf_wait(20);
      ld_y(0); ld_y(1);
      SCHED();
      if (__any(ne2(mv[0][0], mv[1][0]) != 0u)) {
        _Pragma("unroll 1") for (unsigned spins = 0; spins < SPIN_CAP; ++spins) {
          __builtin_amdgcn_s_sleep(4);
          ld_m();
          if (!__any(ne2(mv[0][0], mv[1][0]) != 0u)) break;
        }
      }
      STAMPF(13);
      if (GA && l == 0) lf[40 + pw] = ~tag;
      f32x4 al, be;
      {
        const float mgf = rbf(sum_rows(__uint_as_float(mv[0][1]) + __uint_as_float(mv[1][1])));
        auto rl = [&](int i) { return __uint_as_float(__builtin_amdgcn_readlane(__float_as_uint(mgf), i)); };
        const f32x4 sa = {rl(0), rl(1), rl(2), rl(3)}, ca = {rl(4), rl(5), rl(6), rl(7)}, ga = {rl(8), rl(9), rl(10), rl(11)};
        al = rbf4(ca + 1.0f) * ga;
        be = sa * ga;
        asm volatile("" : "+v"(al), "+v"(be));
      }
      auto xpre = [&](int s) {
#pragma unroll
        for (int e = 0; e < 2; ++e) P[s][e] = __builtin_elementwise_fma(P[s][e], al, be);
        asm volatile("" : "+v"(P[s][0]), "+v"(P[s][1]));
      };
      auto xfin = [&](int s) {
        u32x4 o;
#pragma unroll
        for (int e = 0; e < 2; ++e) {
          const u32x2 y = yv[s][e];
          const f32x4 yf = {bflo(y[0]), bfhi(y[0]), bflo(y[1]), bfhi(y[1])};
          const f32x4 x = __builtin_elementwise_fma(P[s][e], yf, qf[s][e]);
          o[2 * e] = pack2(x[0], x[1]);
          o[2 * e + 1] = pack2(x[2], x[3]);
        }
        ST16S(o, rs, yo, WS_X1B + (unsigned)s * (32u * 20480u), SC1);
      };
      xpre(0); xfin(0);
      xpre(1); xfin(1);
      SCHED();
      xpre(2); xpre(3);
      pf_wait(24);
      ld_y(2);
      xfin(2);
      SCHED();
      pf_wait(28);
      ld_y(3);
      xfin(3);
      STAMP(28);
      if (l == 0) lf[40 + pw] = tag;
      asm volatile("s_waitcnt vmcnt(2)" ::: "memory");
      ST16S(tg4, rs, xfo, WS_FX, SC1);
      SCHED();
      asm volatile("s_waitcnt vmcnt(1)" ::: "memory");
      ST16S(tg4, rs, xfo, WS_FX + 32u * 160u * 128u, SC1);
      STAMP(5);
    } else {
      const int ch = w;
      char* U = lds + L_U + ch * U_CH;
      char* G = lds + ch * (4 * G_COPY);
      {
        const rsrc_t rf = __builtin_amdgcn_make_buffer_rsrc((void*)(p.ssm_k + (size_t)(quad * 4 + ch) * S_), 0, S_ * 2, 0x00020000);
        u32x4 fa[4];
        u32x2 fb[4];
        __builtin_amdgcn_s_setprio(3);
#pragma unroll
        for (int k = 0; k < 4; ++k) {
          fa[k] = __builtin_amdgcn_raw_buffer_load_b128(rf, l16 + (unsigned)k * 1024u, 0, NT);
        }
        __builtin_amdgcn_s_setprio(1);
        TAG_READY();
#pragma unroll
        for (int k = 0; k < 4; ++k) {
#pragma unroll
          for (int d = 0; d < 2; ++d)
            fb[k][d] = __builtin_amdgcn_update_dpp(k < 3 ? (unsigned)__builtin_amdgcn_readfirstlane(fa[k < 3 ? k + 1 : k][d]) : 0u, fa[k][d], 0x130, 0xf, 0xf, false);
          const unsigned x[6] = {fa[k][0], fa[k][1], fa[k][2], fa[k][3], fb[k][0], fb[k][1]};
          unsigned y[5];
#pragma unroll
          for (int d = 0; d < 5; ++d) y[d] = __builtin_amdgcn_alignbit(x[d + 1], x[d], 16);
          char* gp = G + 32 + k * 1024 + l16;
          *(u32x4*)(gp) = fa[k];
          *(u32x4*)(gp + G_COPY) = (u32x4){y[0], y[1], y[2], y[3]};
          *(u32x4*)(gp + 2 * G_COPY) = (u32x4){x[1], x[2], x[3], x[4]};
          *(u32x4*)(gp + 3 * G_COPY) = (u32x4){y[1], y[2], y[3], y[4]};
          if (k == 0) {
            const unsigned f0 = __builtin_amdgcn_readfirstlane(x[0]), f1 = __builtin_amdgcn_readfirstlane(x[1]);
            const unsigned long long f64 = (unsigned long long)f0 | ((unsigned long long)f1 << 32);
            const unsigned long long hi = l ? (f64 << (64 - 16 * (l & 3))) : 0ull;
            if (l < 4) {
              *(u32x4*)(G + l * G_COPY) = (u32x4){0u, 0u, 0u, 0u};
              *(u32x4*)(G + l * G_COPY + 16) = (u32x4){0u, 0u, (unsigned)hi, (unsigned)(hi >> 32)};
            }
            if (l >= 32) *(u32x4*)(U + (l - 32) * 16) = (u32x4){0u, 0u, 0u, 0u};
          }
        }
        asm volatile("s_waitcnt lgkmcnt(0)" ::: "memory");
        STAMP(1);
      }
      const int a = n16, h = h4, jj = h >> 1, e2 = h & 1;
      const char* ap = G + ((a + 1) & 3) * G_COPY + 2 * (16 * jj + a - 8 * e2 + 9 - ((a + 1) & 3));
      unsigned bp = (unsigned)(L_U + ch * U_CH + 32 * (a - jj) + 16 * e2);
      auto lda = [&](u32x4* A, int r, int p0, int np) {
#pragma unroll
        for (int pp = 0; pp < np; ++pp) {
          u32x2 lo = *(const volatile LAS u32x2*)(ap + 512 * (p0 + pp) + 64 * r);
          u32x2 hi = *(const volatile LAS u32x2*)(ap + 512 * (p0 + pp) + 64 * r + 8);
          A[pp] = (u32x4){lo[0], lo[1], hi[0], hi[1]};
        }
      };
      u32x4 AD;
      {
        const unsigned dDr = (*(cptr_t)(p.ssm_D + quad * 4 + (ch & 2)) >> (16 * (ch & 1))) & 0xffffu;
        const unsigned dv = h == (a >> 3) ? ((a & 1) ? dDr : dDr << 16) : 0u;
        const int dw = (7 - (a & 7)) >> 1;
#pragma unroll
        for (int d = 0; d < 4; ++d) AD[d] = dw == d ? dv : 0u;
      }
      u32x4 A0[8][4];
      f32x4 acc[8];
#pragma unroll
      for (int n = 0; n < 8; ++n) acc[n] = (f32x4){0.f, 0.f, 0.f, 0.f};
#pragma unroll
      for (int r = 0; r < 8; ++r) lda(A0[r], r, 0, 4);
      auto yput = [&](int s) {
#pragma unroll
        for (int e = 0; e < 2; ++e)
          *(u32x2*)(U + 1024 * s + 512 * e + (16 * a + 4 * h) * 2) = (u32x2){pack2(acc[2 * s + e][0], acc[2 * s + e][1]), pack2(acc[2 * s + e][2], acc[2 * s + e][3])};
        asm volatile("" ::: "memory");
        if (l == 0) lf[16 + 4 * s + ch] = tag;
      };
      asm volatile("" : "+v"(AD), "+v"(bp), "+v"(acc[0]), "+v"(acc[1]), "+v"(acc[2]), "+v"(acc[3]), "+v"(acc[4]), "+v"(acc[5]), "+v"(acc[6]), "+v"(acc[7]));
      auto stage = [&](auto S__) {
        constexpr int s = decltype(S__)::value, NH = 4 - 2 * s, NP = 8 - 2 * s;
        u32x2 A1[3][NH][2];
        u32x4 B[2][2];
        auto lda1 = [&](int r, int i) { A1[r % 3][i >> 1][i & 1] = *(const volatile LAS u32x2*)(ap + 512 * (4 + (i >> 1)) + 64 * r + 8 * (i & 1)); };
        auto ldb = [&](int r, int e) { B[r & 1][e] = *(const u32x4*)(lds + bp + 512 * (2 * s + e + 1) - 64 * r); };
#pragma unroll
        for (int i = 0; i < 4 * NH; ++i) lda1(i / (2 * NH), i % (2 * NH));
        SCHED();
        if (s > 0) yput(s - 1);
        lf_wait(4 * s, 4);
        if (s == 0) STAMP(3);
#pragma unroll
        for (int i = 0; i < 4; ++i) ldb(i >> 1, i & 1);
        SCHED();
#pragma unroll
        for (int e = 0; e < 2; ++e) MFMA_IP(acc[2 * s + e], AD, B[0][e]);
        sfor<0, 8 * (2 * NP - 1)>([&](auto I_) __attribute__((always_inline)) {
          constexpr int r = decltype(I_)::value / (2 * NP - 1), i = decltype(I_)::value % (2 * NP - 1), e = i >= NP, pp = i - e * NP, h1 = pp < 4 ? 0 : pp - 4;
          const u32x4 a1 = {A1[r % 3][h1][0][0], A1[r % 3][h1][0][1], A1[r % 3][h1][1][0], A1[r % 3][h1][1][1]};
          const u32x4 fa = pp < 4 ? A0[r][pp & 3] : a1;
          MFMA_IP(acc[2 * s + e + pp], fa, B[r & 1][e]);
          SCHED();
          if (r + 2 < 8 && i == 2 * NP - 3) ldb(r + 2, 0);
          if (r + 2 < 8 && i == 2 * NP - 2) ldb(r + 2, 1);
          if (r + 2 < 8 && i < 2 * NH) lda1(r + 2, i % (2 * NH));
          SCHED();
        });
      };
      stage(IC<0>());
      STAMP(4);
      stage(IC<1>());
      STAMPF(9);
      u32x2 fv;
      {
        u32x4 B[8][2];
        auto ldb = [&](int r, int m0) {
#pragma unroll
          for (int m = 0; m < 2; ++m) B[r][m] = *(const u32x4*)(lds + bp + 512 * (m0 + m + 1) - 64 * r);
        };
        asm volatile("s_nop 1" : "+v"(acc[2]), "+v"(acc[3]));
        yput(1);
        if (w == 3) wo_ld();
        __builtin_amdgcn_s_setprio(3);
        lf_wait(8, 4);
        STAMP(22);
        auto ldb1 = [&](int i, int m0) { B[i >> 1][i & 1] = *(const u32x4*)(lds + bp + 512 * (m0 + (i & 1) + 1) - 64 * (i >> 1)); };
#pragma unroll
        for (int r = 0; r < 3; ++r) ldb(r, 4);
        SCHED();
#pragma unroll
        for (int m = 0; m < 2; ++m) MFMA_IP(acc[4 + m], AD, B[0][m]);
        sfor<0, 24>([&](auto I_) __attribute__((always_inline)) {
          constexpr int i = decltype(I_)::value, r = i / 3, k = i % 3;
          MFMA_IP(acc[4 + (k != 1)], A0[r][k == 0], B[r][k == 2]);
          SCHED();
          if (i < 10) ldb1(6 + i, 4);
          SCHED();
        });
        sfor<0, 32>([&](auto I_) __attribute__((always_inline)) {
          constexpr int i = decltype(I_)::value, r = i >> 2, k = i & 3;
          MFMA_IP(acc[6 + (k & 1)], A0[r][2 + (k & 1) - (k >> 1)], B[r][k >> 1]);
          SCHED();
          if (i == 7) yput(2);
          if (i == 20) fv = ld8(rs, b2o);
          if (i == 19) lf_wait(12, 4);
          if (i >= 19 && i < 29) ldb1(i - 19, 6);
          SCHED();
        });
#pragma unroll
        for (int m = 0; m < 2; ++m) MFMA_IP(acc[6 + m], AD, B[0][m]);
        sfor<0, 24>([&](auto I_) __attribute__((always_inline)) {
          constexpr int i = decltype(I_)::value, r = i / 3, k = i % 3;
          MFMA_IP(acc[6 + (k != 1)], A0[r][k == 0], B[r][k == 2]);
          SCHED();
          if (i < 6) ldb1(10 + i, 6);
          SCHED();
        });
        asm volatile("s_nop 7" : "+v"(acc[6]), "+v"(acc[7]));
        yput(3);
      }
      STAMP(28);
      __builtin_amdgcn_s_setprio(0);
      b2_wait(fv, 0);
      lf_wait(20, 4);
      DRAIN();
    }
    __builtin_amdgcn_s_setprio(0);
  } else {
    const int at = idx - 20;
    char* const scr = lds + WBLK + 14848;
    auto gv_blk = [&](int j) { return j < 160 ? 30 + (j >> 3) : j < 400 ? (j >> 3) - 20 : j >> 3; };
    auto gv_fin = [&](const u32x4* pre, int j) {
      float acc[8];
      gemv_fin(acc, pre, 10, scr);
      gemv_put(acc, j < 160 ? WS_MODP + (unsigned)((j & 7) * 1280 + (j >> 3) * 64) * 8u : WS_MODQ + (unsigned)((j & 7) * 2560 + ((j >> 3) - 20) * 64) * 8u);
    };
    const int ja = xcd * 12 + at, ai = xcd * 8 + at, bi = xcd * 4 + at - 8, g = w >> 2, wj = w & 3;
    const int rj = w == 0 ? ja : w == 1 ? (at >= 4 && at < 12 ? 92 + ai : -1) : w == 4 ? 160 + ja : w == 5 && at < 8 ? 256 + ai : -1;
    char* const rst = lds + (g ? WBLK : 65536) + wj * 7424;
    u32x4 pre[11];
    if (at >= 8 || g == 0) {
      TOUCH_AT(2);
      const int T = at >= 8 ? 64 + 2 * bi + g : ai;
      unsigned* mds = (unsigned*)(lds + 2048) + 16 * g;
      char* qt = lds + L_RT + g * (16 * 1312);
      u32x4 ld[5];
      const bool e1 = at >= 8 && w == 3;
      if (!e1) __builtin_amdgcn_s_sleep(8);
      auto q_ld = [&](auto N_) {
        asm volatile("; %0" :: "n"(decltype(N_)::value) : "memory");
#pragma unroll
        for (int j = 0; j < 5; ++j)
          ld[j] = __builtin_amdgcn_raw_buffer_load_b128(rq, (unsigned)(h4 * D_ * 2 + n16 * 16 + j * 256), (unsigned)((32 * (T >> 1) + 8 * wj + 4 * (T & 1)) * D_ * 2), NT);
        SCHED();
      };
      const int g2 = at < 8 || w < 2 || w == 4 || w == 3 ? 400 : w < 4 ? 384 + bi : 256 + wj * 32 + bi;
      SCHED();
      float sa = 0.f, va = 0.f;
      auto q_in = [&]() {
        const bf2_t ONE2 = {(__bf16)1.0f, (__bf16)1.0f};
        char* dst = qt + (4 * wj + h4) * 1312 + n16 * 16;
        SCHED();
#pragma unroll
        for (int j = 0; j < 5; ++j) {
          *(volatile LAS u32x4*)(dst + j * 256) = ld[j];
#pragma unroll
          for (int d = 0; d < 4; ++d) {
            const unsigned a0 = ld[j][d];
            const bf2_t xa = __builtin_bit_cast(bf2_t, a0);
            sa = __builtin_amdgcn_fdot2_f32_bf16(xa, ONE2, sa, false); va = __builtin_amdgcn_fdot2_f32_bf16(xa, xa, va, false);
          }
        }
      };
      u32x4 pre2[11];
      if (e1) {
        gemv_load(pre, gv_blk(128 + bi), bi & 7, 10);
        q_ld(IC<3>());
        TAG_READY();
        gv_fin(pre, 128 + bi);
        q_in();
        asm volatile("; d" : "+v"(sa), "+v"(va));
      } else if (g2 < 400) {
        q_ld(IC<0>());
        gemv_load(pre2, gv_blk(g2), g2 & 7, 10);
        q_in();
        asm volatile("; a" : "+v"(sa), "+v"(va));
      } else {
        q_ld(IC<2>());
        q_in();
        asm volatile("; c" : "+v"(sa), "+v"(va));
      }
      TAG_READY();
      {
        lf_set(8 + w);
        const float ma = sum16(sa) * (1.0f / D_);
        va = __builtin_fmaxf(sum16(va) * (1.0f / D_) - ma * ma, 0.f);
        if (n16 == 0) mds[4 * wj + h4] = pack2(ma, __builtin_amdgcn_sqrtf(rbf(rbf(va) + 1e-5f)));
      }
      lf_set(16 + w);
      lf_wait(8 + 4 * g, 4);
      STAMP(23);
      {
        const char* src = qt + (l & 7) * 1312 + (40 * wj + (l >> 3)) * 8;
        unsigned ckw = 0u;
        u32x2 qx[5][2];
#pragma unroll
        for (int j = 0; j < 5; ++j) { qx[j][0] = *(const volatile LAS u32x2*)(src + j * 64); qx[j][1] = *(const volatile LAS u32x2*)(src + j * 64 + 8 * 1312); }
        SCHED();
        if (wj == 2) {
          lf_wait(16 + 4 * g, 4);
          st16(rs, WS_ST1 + (unsigned)(l >> 3) * 16384u + (unsigned)T * 128u + (unsigned)(l & 7) * 16u, (u32x4){mds[l & 7], tag, mds[8 + (l & 7)], tag});
          STAMP(22);
        }
#pragma unroll
        for (int j = 0; j < 5; ++j) {
          const u32x2 x = qx[j][0], y = qx[j][1];
          st16(rs, WS_QB + (unsigned)T * 20480u + (unsigned)(5 * wj + j) * 1024u + l16, (u32x4){x[0], x[1], y[0], y[1]});
          const unsigned t = tag - sum8u((x[0] + x[1]) + (y[0] + y[1]));
          ckw = (l & 7) == j ? t : ckw;
        }
        char* sc = lds + 110592 + w * 256;
        *(volatile LAS unsigned*)(sc + ((l & 7) * 8 + (l >> 3)) * 4) = ckw;
        const u32x4 x = *(const volatile LAS u32x4*)(sc + n16 * 16);
        if (l < 16) st16(rs, WS_QCK + (unsigned)(T * 4 + wj) * 256u + l16, x);
      }
      SCHED();
      if (g2 >= 400 && w < 2 && rj >= 0 && rj < 160) rt_ld(rj, rst);
      if (w >= 4 && w < 7) lf_set(w);
      if (g2 < 400) gv_fin(pre2, g2);
      STAMP(25);
    } else {
      constexpr int KB = L_RT + 16 * 1312;
      TOUCH_AT(3);
      const int g1 = w < 6 ? 2 * ai + wj : 32 + wj * 64 + ai;
      if (w >= 6) {
        const rsrc_t rk = __builtin_amdgcn_make_buffer_rsrc((void*)(w == 6 ? p.k : p.v), 0, S_ * D_ * 2, 0x00020000);
        const unsigned ko = (unsigned)((1536 + 8 * ai) * (D_ * 2) - (w == 6 ? 4096 : 0));
        char* dst = lds + KB + (w == 6 ? 0 : 14336);
        __builtin_amdgcn_s_sleep(3);
#pragma unroll
        for (int n = 0; n < 10; ++n) __builtin_amdgcn_raw_ptr_buffer_load_lds(rk, (LAS void*)(dst + n * 1024), 16, l16, ko + (unsigned)n * 1024u, 0, NT);
        if (w == 6) {
#pragma unroll
          for (int n = 10; n < 14; ++n) __builtin_amdgcn_raw_ptr_buffer_load_lds(rk, (LAS void*)(dst + n * 1024), 16, l16, ko + (unsigned)n * 1024u, 0, NT);
        }
      }
      SCHED();
      u32x4 swt[2];
      swt[0] = *(const u32x4*)(p.shift_k + (tid - 256) * 8);
      swt[1] = *(const u32x4*)(p.shift_k + (256 + l) * 8);
      SCHED();
      if (w >= 6) __builtin_amdgcn_s_sleep(8);
      gemv_load(pre, gv_blk(g1), g1 & 7, 10);
      TAG_READY();
      asm volatile("s_waitcnt vmcnt(11)" : "+v"(swt[0]), "+v"(swt[1]) :: "memory");
      lf_set(8 + w);
      if (w < 6) gv_fin(pre, g1);
      STAMP(25);
      lf_wait(12, 4);
      STAMP(23);
#pragma unroll 1
      for (int n = 0; n < (w == 7 ? 2 : 1); ++n) {
        {
          const int pr = n ? 256 + l : tid - 256;
          const char* kp = lds + KB + 4096 + pr * 4;
          unsigned kk[11], vv[8];
#pragma unroll
          for (int i = 0; i < 11; ++i) kk[i] = *(const unsigned*)(kp + (i - 3) * 1280);
#pragma unroll
          for (int i = 0; i < 8; ++i) vv[i] = *(const unsigned*)(kp + 10240 + i * 1280);
          const u32x4 sw = n ? swt[1] : swt[0];
          const f32x2 wt[4] = {{bflo(sw[0]), bflo(sw[2])}, {bfhi(sw[0]), bfhi(sw[2])}, {bflo(sw[1]), bflo(sw[3])}, {bfhi(sw[1]), bfhi(sw[3])}};
          f32x2 o[8];
#pragma unroll
          for (int i = 0; i < 8; ++i) {
            f32x2 t = (f32x2){bflo(kk[i + 3]), bfhi(kk[i + 3])} * wt[0];
#pragma unroll
            for (int j = 1; j < 4; ++j) t = __builtin_elementwise_fma((f32x2){bflo(kk[i + 3 - j]), bfhi(kk[i + 3 - j])}, wt[j], t);
            o[i] = t * (f32x2){bflo(vv[i]), bfhi(vv[i])};
          }
          char* sc = lds + 98304 + wj * 2304;
          unsigned t = 0u;
#pragma unroll
          for (int e = 0; e < 2; ++e) {
            const u32x4 x = {pack2(o[7][e], o[6][e]), pack2(o[5][e], o[4][e]), pack2(o[3][e], o[2][e]), pack2(o[1][e], o[0][e])};
            *(volatile LAS u32x4*)(sc + l * 32 + e * 16) = x;
            t += (x[0] + x[1]) + (x[2] + x[3]);
          }
          *(volatile LAS unsigned*)(sc + 2048 + (l >> 1) * 4 + (l & 1) * 128) = tag - t - (unsigned)__builtin_amdgcn_update_dpp(0, (int)t, 0xB1, 0xf, 0xf, true);
          u32x4 x[3];
#pragma unroll
          for (int e = 0; e < 3; ++e) x[e] = *(const volatile LAS u32x4*)(sc + e * 1024 + (e < 2 ? l : l & 7) * 16);
          const unsigned ko = WS_KV3 + (unsigned)ai * 10880u;
#pragma unroll
          for (int e = 0; e < 2; ++e) st16(rs, ko + (unsigned)((n ? 4 : wj) * 2048 + e * 1024) + l16, x[e]);
          if (l < 8) st16(rs, ko + (unsigned)(10240 + (n ? 4 : wj) * 128) + l16, x[2]);
        }
      }
      STAMP(22);
      if (w < 7) lf_set(w);
      if (w >= 6) gv_fin(pre, g1);
      STAMPF(30);
    }
    if (w == 6 && ja < 80) {
      hold(AUX_T);
      gemv_load(pre, gv_blk(400 + ja), ja & 7, 10);
      gv_fin(pre, 400 + ja);
    }
    if (rj >= 0) {
      if (rj >= 160) { hold(RT2_T); rt_ld(rj, rst); }
      rt_job(rj, rst);
    }
    STAMP(26);
    lf_set(w);
    if (w == 3) { hold(WA_T); wo_ld(); }
    STAMP(3);
    if (w < 4) {
      hold(W_T);
      b2_wait((u32x2){~tag, 0u}, 1);
      lf_wait(0, 8);
      STAMP(4);
    }
    DRAIN();
  }

  STAMPF(16);
  u32x4 wox[5];
  if (w == 3) {
    const unsigned bp = (unsigned)(122880 + (4 * h4 + (n16 >> 2)) * 160 + (n16 & 3) * 8);
    u32x2 x[10];
    asm volatile("ds_read_b64_tr_b16 %0, %10\n\tds_read_b64_tr_b16 %1, %10 offset:2560\n\tds_read_b64_tr_b16 %2, %10 offset:32\n\tds_read_b64_tr_b16 %3, %10 offset:2592\n\t"
                 "ds_read_b64_tr_b16 %4, %10 offset:64\n\tds_read_b64_tr_b16 %5, %10 offset:2624\n\tds_read_b64_tr_b16 %6, %10 offset:96\n\tds_read_b64_tr_b16 %7, %10 offset:2656\n\t"
                 "ds_read_b64_tr_b16 %8, %10 offset:128\n\tds_read_b64_tr_b16 %9, %10 offset:2688\n\ts_waitcnt lgkmcnt(0)"
                 : "=&v"(x[0]), "=&v"(x[1]), "=&v"(x[2]), "=&v"(x[3]), "=&v"(x[4]), "=&v"(x[5]), "=&v"(x[6]), "=&v"(x[7]), "=&v"(x[8]), "=&v"(x[9]) : "v"(bp) : "memory");
#pragma unroll
    for (int dj = 0; dj < 5; ++dj) wox[dj] = (u32x4){x[2 * dj][0], x[2 * dj][1], x[2 * dj + 1][0], x[2 * dj + 1][1]};
  }
  auto wo_put = [&]() {
    if (w == 3 && R < 28) {
      const unsigned base = WS_WOUTF + (unsigned)c * WBLK + (unsigned)((R >> 1) * 10240 + (R & 1) * 1024);
#pragma unroll
      for (int dj = 0; dj < 5; ++dj) st16(rs, base + (unsigned)((R & 1) ? dj : (dj + 2) % 5) * 2048u + l16, wox[dj]);
    }
  };
  if (cs2) {
    cs_ld(csv, (8 + c) * 7 + R - 20);
    SCHED();
    load_b2(0, 12);
    SCHED();
    cs_fin(csv, (8 + c) * 7 + R - 20);
  } else if (w < 4) {
    asm volatile("" ::: "memory");
    if (!is_conv) load_b2(0, 12);
  }
  const unsigned xfb = WS_FX + (unsigned)(((R >> 1) * 4 + (R & 1) * 2 + (c >> 2)) * 160 + l) * 128u;
  unsigned xf3[3];
  if (w == 7) {
    lf_wait(36, 4);
    int dt = (int)(lf[44 + ((l & 31) < 20 ? l & 31 : 0)] - t_start);
    dt = l < 32 ? dt : -dt;
    dt = max(dt, __builtin_amdgcn_update_dpp(0, dt, 0xB1, 0xf, 0xf, true));
    dt = max(dt, __builtin_amdgcn_update_dpp(0, dt, 0x4E, 0xf, 0xf, true));
    dt = max(dt, __builtin_amdgcn_update_dpp(0, dt, 0x124, 0xf, 0xf, true));
    dt = max(dt, __builtin_amdgcn_update_dpp(0, dt, 0x128, 0xf, 0xf, true));
    const int d1 = min(max(max(__builtin_amdgcn_readlane(dt, 0), __builtin_amdgcn_readlane(dt, 16)), 0), 300);
    const int d0 = min(max(max(__builtin_amdgcn_readlane(dt, 32), __builtin_amdgcn_readlane(dt, 48)), 0), 300);
    dt = ((d1 + d0) >> 1) - d0;
    STAMPF(24);
    hold((unsigned)(YG_T + dt));
    STAMPF(30);
    xf3[0] = ld4(rs, xfb); xf3[1] = ld4(rs, xfb + 8192u); xf3[2] = ld4(rs, l < 32 ? xfb + 16384u : ~0u);
  }
  SCHED();
  u32x4 k_c;
  unsigned kc_o = w < 2 ? (unsigned)(tid < 112 ? tid : 0) * 16u : 0x80000000u, kc_d = (unsigned)(L_CONST + (tid & 127) * 8);
  auto kc_ld = [&](int z2) { k_c = __builtin_amdgcn_raw_buffer_load_b128(rs, kc_o, WS_CS + (unsigned)(8 * z2 + c) * 2048u, SC1); };
  auto consts_put = [&](int z2) {
    if (w < 2) {
      if (__any(ne2(k_c[0], k_c[2]) != 0u)) {
        _Pragma("unroll 1") for (unsigned spins = 0; spins < SPIN_CAP; ++spins) {
          __builtin_amdgcn_s_sleep(8);
          kc_ld(z2);
          if (!__any(ne2(k_c[0], k_c[2]) != 0u)) break;
        }
      }
      if (z2) *(u32x2*)(lds + kc_d + 1024) = (u32x2){k_c[1], k_c[3]};
      else { const unsigned d = kc_d - ((kc_d >> 1) & 4u); *(unsigned*)(lds + d) = k_c[1]; *(unsigned*)(lds + d + 8) = k_c[3]; }
    }
  };
  const int rt1 = w & 3, kh1 = w >> 2;
  unsigned bo0 = (unsigned)(kh1 * 71680) + l16, bo1 = bo0 + 35840u;
  asm volatile("" : "+v"(bo0), "+v"(bo1));
  unsigned pso[3], pqo[3], oto = (unsigned)(((w ^ 4) * 7 * 64 + l) * 8), rdo = (unsigned)((w * 7 * 64 + l) * 8), cso = (unsigned)(L_CONST + (128 + n16) * 8);
#pragma unroll
  for (int e = 0; e < 3; ++e) {
    const int row = rt1 * 16 + (e < 2 ? 4 * h4 + 2 * kh1 + e : n16);
    pso[e] = (unsigned)(L_PST + row * 4); pqo[e] = (unsigned)(L_PST + 512 + row * 16 + (row & 3) * 4);
  }
  unsigned spo = (unsigned)(L_Z + tid * 16), p6o = (unsigned)(L_PST + (w * 16 + 4 * h4) * 4), pgo = (unsigned)(L_PST + 512 + (w * 16 + n16) * 16);
  asm volatile("" : "+v"(spo), "+v"(p6o), "+v"(pgo));
  p6o = n16 == 12 ? p6o : spo;
  pgo = h4 == (n16 >> 2) ? pgo : spo;
  unsigned bsrc = (unsigned)(((l >> 1) & 3) * 64 + (l & 1) * 4), ppo = l < 8 ? l16 : 0x80000000u;
  asm volatile("" : "+v"(pso[0]), "+v"(pso[2]), "+v"(pqo[0]), "+v"(pqo[2]), "+v"(oto), "+v"(rdo), "+v"(cso), "+v"(p6o), "+v"(pgo), "+v"(bsrc), "+v"(ppo), "+v"(kc_o), "+v"(kc_d));
  pso[1] = pso[0] + 4u; pqo[1] = pqo[0] + 20u;
#define BFRAG(q, j) (*(const u32x4*)(lds + ((q) < 5 ? bo0 : bo1) + ((q) % 5) * 7168 + (j) * 1024))
  u32x4 h2[10];
  unsigned xv = (unsigned)((4 * ((l >> 3) & 1) + h4) * 128 + (l & 7) * 16), xs0 = WS_X1B + (unsigned)(R * 4 + rt1) * 20480u + (unsigned)(10 * kh1) * 1024u;
  unsigned wvo = wsp_vo, bso = (unsigned)tau(rt1, n16) * 2u, fgo = l < 32 ? (unsigned)(l >> 3) * F4S + (unsigned)(l & 7) * 16u : 0x80000000u;
  asm volatile("" : "+v"(xv), "+v"(wvo), "+v"(bso), "+v"(fgo));
#define WO3 (-92)
#define PINS(x) do { x = (unsigned)__builtin_amdgcn_readfirstlane((int)x); asm volatile("" : "+s"(x)); } while (0)
  PINS(xs0);
  unsigned zs[8], wx8, wx4, wgb, fzs, uo, ui, xs, wl2, os, zos, pos, s1, s3;
#define LDX(q) h2[q] = __builtin_amdgcn_raw_buffer_load_b128(rs, xv + (unsigned)((q) & 3) * 1024u, xs0 + (unsigned)((q) >> 2) * 4096u, SC1)
  auto flag4 = [&](unsigned off) { ST16S(((u32x4){tag, tag, tag, tag}), rs, fgo, WS_F4 - WS_FWI + off, SC1); };
  unsigned bs_raw, fwo[1];
  f32x4 acc[7];
  float mu_n, rs_n;
  {
    unsigned f1o[1] = {w == 1 && l < 20 ? fwi + (unsigned)(c * 20 + l) * 128u : ~0u}, f1v;
    fwo[0] = w == 3 && l < 28 ? fwi + (WS_FWO - WS_FWI) + (unsigned)(c * 32 + l) * 128u : ~0u;
    asm volatile("" : "+v"(f1o[0]), "+v"(fwo[0]));
    if (w < 4) {
      auto gate = [&](bool all) {
        _Pragma("unroll 1") for (unsigned spins = 0; spins < (SPIN_CAP << 4); ++spins) {
          const unsigned v = lf[40 + (l & 3)];
          if (all ? __all(v == tag) || (GT && (unsigned)__builtin_amdgcn_s_memrealtime() - t_start > GT) : !__any(v == ~tag)) break;
          __builtin_amdgcn_s_sleep(1);
        }
      };
      if (is_conv) {
        if (!GA || GT) gate(true);
#pragma unroll
        for (int k = 0; k < 12; k += GA ? GA : 12) { if (GA) gate(false); load_b2(k, k + (GA ? GA : 12)); }
      }
#pragma unroll
      for (int k = 12; k < 35; k += GA ? GA : 23) { if (GA && is_conv) gate(false); load_b2(k, GA && k + GA < 35 ? k + GA : 35); }
    }
    wo_put();
    STAMPF(29);
    if (w == 7) {
      asm volatile("" : "+v"(xf3[0]), "+v"(xf3[1]), "+v"(xf3[2]));
      if (__any(ne2(xf3[0], xf3[1]) != 0u) || __builtin_amdgcn_ballot_w64(xf3[2] == tag) != 0xffffffffull) {
        const unsigned fo[3] = {xf3[0] != tag ? xfb : ~0u, xf3[1] != tag ? xfb + 8192u : ~0u, l < 32 && xf3[2] != tag ? xfb + 16384u : ~0u};
        wait_lines<3, 2>(rs, fo, tag);
      }
      if (l == 0) lf[33] = tag;
    }
    lf_wait(33, 1);
    STAMP(7);
    SCHED();
#pragma unroll
    for (int q = 0; q < 3; ++q) LDX(q);
    SCHED();
    __builtin_amdgcn_s_waitcnt(0x0F73);
    BAR();
    STAMP(8);
#pragma unroll
    for (int j = 0; j < 7; ++j) acc[j] = (f32x4){0.f, 0.f, 0.f, 0.f};
    f32x4 accg = {0.f, 0.f, 0.f, 0.f};
    u32x4 bf[7];
#pragma unroll
    for (int j = 0; j < 7; ++j) bf[j] = BFRAG(0, j);
    SCHED();
#pragma unroll
    for (int q = 3; q < 6; ++q) LDX(q);
    if (w == 3) flag4(WS_FWO + (unsigned)(c * 32 + R) * 128u);
    SCHED();
#pragma unroll
    for (int t = 0; t < 8; ++t) { zs[t] = WS_Z2F + (unsigned)(c * 64 + 8 * w + 4 * ((t >> 2) ^ rotg) + ((t + rotb) & 3)) * 7168u; PINS(zs[t]); }
    wx8 = (unsigned)((w + (idx >> 2)) & 7) * 1024u; wx4 = wx8 & 3072u; wgb = WS_WOUTF + (unsigned)c * WBLK; fzs = fz + (unsigned)(c * 32 + 4 * w) * 128u;
    uo = WS_UF + (unsigned)((R * 4 + rt1) * 8 + c) * 4096u + (unsigned)kh1 * 2048u; ui = WS_UF + (unsigned)((R * 4 + rt1) * 8 + 4 * kh1) * 4096u;
    xs = WS_X1B + (unsigned)(R * 4 + rt1) * 20480u + (unsigned)(5 * c + 2 * kh1) * 512u; wl2 = (unsigned)(120 + WO3 + (w < 4 ? w + 16 : w)) * 1024u;
    os = (unsigned)(((R * 64 + tau(rt1, 0)) * D_ + 80 * c + 32 * kh1) * 2);
    zos = WS_Z2F + (unsigned)((c * 64 + 4 * (R >> 1)) * 7 + (w < 6 ? w + 1 : 0)) * 1024u + (unsigned)(R & 1) * 512u; pos = WS_PART + (unsigned)(R * 64 + w) * 128u;
    s1 = (unsigned)((w ^ 4) * 3072 + 1024); s3 = (unsigned)(w * 3072 + 1024);
    PINS(wx8); PINS(wx4); PINS(wgb); PINS(fzs); PINS(uo); PINS(ui); PINS(xs); PINS(wl2); PINS(os); PINS(zos); PINS(pos); PINS(s1); PINS(s3);
    SCHED();
    auto xswap = [&](int q, int e) {
#pragma unroll
      for (int d = 0; d < 2; ++d) {
        if (e == 1) h2[q][2 + d] ^= (unsigned)__builtin_amdgcn_update_dpp(0, (int)h2[q][d], 0x128, 0xf, 0x3, false);
        else h2[q][d] ^= (unsigned)__builtin_amdgcn_update_dpp(0, (int)h2[q][2 + d], 0x128, 0xf, 0xC, false);
      }
      asm volatile("" : "+v"(h2[q]));
    };
#pragma unroll
    for (int q = 0; q < 10; ++q) {
      if (q == 0) { xswap(q, 0); xswap(q, 1); xswap(q, 2); }
      if (q == 5 && w == 1 && __builtin_amdgcn_ballot_w64(f1v == tag) != 0xfffffull) wait_lines<1, 4>(rs, f1o, tag);
      if (q == 5) { __builtin_amdgcn_s_barrier(); asm volatile("" ::: "memory"); }
      SCHED();
      accg = MFMA16(h2[q], h2[q], accg);
      SCHED();
#pragma unroll
      for (int j = 0; j < 7; ++j) {
        acc[j] = MFMA16(h2[q], bf[j], acc[j]);
        if (q == 9 && j == 1) *(f32x4*)(lds + pgo) = accg;
        if (q < 9) bf[j] = BFRAG(q + 1, j);
        if (q < 9 && j >= 3 && j < 6) xswap(q + 1, j - 3);
        if (q == 1 && j == 6) f1v = ld4(rs, f1o[0]);
        if (q == 2 && j == 0) bs_raw = __builtin_amdgcn_raw_buffer_load_b16(__builtin_amdgcn_make_buffer_rsrc((void*)p.b_sp, 0, S_ * 2, 0x00020000), bso, (unsigned)R * 128u, 0);
        if (q >= 2 && j == 2) LDW(q - 2);
        if ((j == 0 || j == 4) && 6 + 2 * q + (j >> 2) < 10) LDX(6 + 2 * q + (j >> 2));
        if (q == 4 && j == 0) kc_ld(1);
        if (q == 2 && j == 1 && cs1) cs_ld(csv, c * 7 + R - 25);
        if (q >= 5 && j == 5) {
          const int i = (5 + q) * 7 + (w < 7 ? w : 0);
          dma1k(rs, lds + i * 1024, l16, WS_WINF + (unsigned)c * WBLK + (unsigned)i * 1024u);
          if (q == 9) { const int i2 = 28 + (w < 7 ? w : 0); dma1k(rs, lds + i2 * 1024, l16, WS_WINF + (unsigned)c * WBLK + (unsigned)i2 * 1024u); }
        }
        SCHED();
      }
    }
    STAMP(31);
  }
  unsigned sro, swo, zo, zro, po;
  auto gelu_n = [&](f32x2* x, auto N_, auto&& fill) {
    constexpr int N = decltype(N_)::value;
    const float c0 = -2.0f * 0.7978845608028654f * 1.4426950408889634f, c1 = c0 * 0.044715f;
    f32x2 k0 = {c0, c0}, k1 = {c1, c1}, k2 = {1.0f, 1.0f};
    asm volatile("" : "+v"(k0), "+v"(k1), "+v"(k2));
    f32x2 t[N];
#define GST(e) do { _Pragma("unroll") for (int i = 0; i < N; ++i) { t[i] = (e); asm volatile("" : "+v"(t[i])); } } while (0)
    GST(x[i] * x[i]);
    GST(__builtin_elementwise_fma(t[i], k1, k0));
    fill(IC<0>());
    GST(t[i] * x[i]);
    fill(IC<1>());
    GST(((f32x2){__builtin_amdgcn_exp2f(t[i][0]), __builtin_amdgcn_exp2f(t[i][1])}));
    fill(IC<2>());
    GST(t[i] + k2);
    fill(IC<3>());
    GST(((f32x2){__builtin_amdgcn_rcpf(t[i][0]), __builtin_amdgcn_rcpf(t[i][1])}));
    fill(IC<4>());
#pragma unroll
    for (int i = 0; i < N; ++i) { x[i] *= t[i]; asm volatile("" : "+v"(x[i])); }
  };
  {
    f32x2 mine[7];
    f32x2 csb[7];
    if (kh1) {
#pragma unroll
      for (int j = 0; j < 7; ++j) { *(f32x2*)(lds + rdo + j * 512) = (f32x2){acc[j][0], acc[j][1]}; mine[j] = (f32x2){acc[j][2], acc[j][3]}; }
      *(f32x4*)(lds + p6o) = acc[6];
    } else {
#pragma unroll
      for (int j = 0; j < 7; ++j) { *(f32x2*)(lds + rdo + j * 512) = (f32x2){acc[j][2], acc[j][3]}; mine[j] = (f32x2){acc[j][0], acc[j][1]}; }
      *(f32x4*)(lds + p6o) = acc[6];
      consts_put(1);
      if (cs1) cs_fin(csv, c * 7 + R - 25);
#pragma unroll
      for (int k = 8; k < 12; ++k) LDW(k);
    }
    BAR();
    auto b1p = [&](int i) { dma1k(rs, lds + i * 1024, l16, WS_WINF + (unsigned)c * WBLK + (unsigned)i * 1024u); };
    if (kh1) {
#pragma unroll
      for (int k = 8; k < 12; ++k) LDW(k);
    }
    __builtin_amdgcn_s_setprio(3);
    STAMPT(9);
    f32x2 oth[7];
    f32x2 pa[3], pb[3];
#pragma unroll
    for (int e = 0; e < 3; ++e) {
      pa[e] = (f32x2){*(const float*)(lds + pso[e]), *(const float*)(lds + pqo[e])};
      pb[e] = (f32x2){*(const float*)(lds + pso[e] + 256), *(const float*)(lds + pqo[e] + 1024)};
    }
#pragma unroll
    for (int j = 0; j < 7; ++j) { oth[j] = *(const f32x2*)(lds + oto + j * 512); csb[j] = *(const f32x2*)(lds + cso + 128 * j); }
    SCHED();
    float mu2[2], rs2[2];
#pragma unroll
    for (int e = 0; e < 3; ++e) {
      const float mean = (pa[e][0] + pb[e][0]) * (1.0f / D_);
      const float var = __builtin_fmaxf((pa[e][1] + pb[e][1]) * (1.0f / D_) - mean * mean, 0.f);
      const float rstd = __builtin_amdgcn_rsqf(var + 1e-5f);
      if (e < 2) { mu2[e] = mean; rs2[e] = rstd; } else { mu_n = mean; rs_n = rstd; }
    }
    f32x2 zz[7];
    const f32x2 nmu = {-mu2[0], -mu2[1]}, rsv = {rs2[0], rs2[1]};
#pragma unroll
    for (int j = 0; j < 7; ++j)
      zz[j] = __builtin_elementwise_fma(__builtin_elementwise_fma(nmu, (f32x2){csb[j][0], csb[j][0]}, mine[j] + oth[j]), rsv, (f32x2){csb[j][1], csb[j][1]});
    gelu_n(zz, IC<7>(), [&](auto K_) __attribute__((always_inline)) { constexpr int k = decltype(K_)::value; b1p(8 * k + w + (k < 4 || w < 3 ? 35 : 70)); SCHED(); });
    if (n16 >= 12) zz[6] = (f32x2){0.f, 0.f};
    f32x2 s1 = ((zz[0] + zz[1]) + (zz[2] + zz[3])) + ((zz[4] + zz[5]) + zz[6]);
    f32x2 s2 = zz[0] * zz[0], s3 = zz[1] * zz[1];
#pragma unroll
    for (int j = 2; j < 7; ++j) { if (j & 1) s3 = __builtin_elementwise_fma(zz[j], zz[j], s3); else s2 = __builtin_elementwise_fma(zz[j], zz[j], s2); }
    s2 += s3;
    asm("" : "+v"(s1), "+v"(s2));
    float a0 = s1[0] + dpp_f<0xB1>(s1[0]), a1 = s1[1] + dpp_f<0xB1>(s1[1]), b0 = s2[0] + dpp_f<0xB1>(s2[0]);
    a0 = (l & 1) ? a1 : a0;
    const float b1 = s2[1] + dpp_f<0xB1>(s2[1]);
    b0 = (l & 1) ? b1 : b0;
    a0 += dpp_f<0x4E>(a0); b0 += dpp_f<0x4E>(b0);
    a0 += dpp_f<0x124>(a0); b0 += dpp_f<0x124>(b0);
    a0 += dpp_f<0x128>(a0); b0 += dpp_f<0x128>(b0);
    {
      const unsigned pa0 = __builtin_amdgcn_ds_bpermute(bsrc, __float_as_uint(a0)), pb0 = __builtin_amdgcn_ds_bpermute(bsrc, __float_as_uint(b0));
      ST16S(((u32x4){tag, pa0, tag, pb0}), rs, ppo, pos + (unsigned)c * 1024u, SC1);
    }
    SCHED();
    __builtin_amdgcn_s_setprio(0);
    STAMP(10);
    if (!kh1) {
#pragma unroll
      for (int k = 10; k < 18; ++k) b1p(4 * k + (w & 3) + 70 - (k == 17 ? (w & 2) : 0));
    }
#pragma unroll
    for (int k = 12; k < 16; ++k) LDW(k);
#pragma unroll
    for (int j = 0; j < 7; ++j)
      *(unsigned*)(lds + L_Z + (((rt1 >> 1) * 7 + j) * 64 + l) * 16 + (4 * (rt1 & 1) + 2 * kh1) * 2) = pack2(zz[j][0], zz[j][1]);
    SCHED();
    sfor<0, 8>([&](auto X_) __attribute__((always_inline)) { WSP_TX(decltype(X_)::value); SCHED(); });
    sro = (unsigned)(L_ST2 + h4 * 16);
    swo = (unsigned)(L_ST2 + (16 * rt1 + 4 * ((l >> 1) & 3) + 2 * kh1 + (l & 1)) * 4);
    zo = w < 7 ? (unsigned)(h4 * 7168 + n16 * 16) : 0x80000000u;
    zro = (unsigned)(L_Z + (w < 7 ? w : 0) * 1024) + l16;
    po = (unsigned)((l >> 3) * 1024 + (l & 7) * 16);
    asm volatile("" : "+v"(sro), "+v"(swo), "+v"(zo), "+v"(zro), "+v"(po));
    asm volatile("s_waitcnt vmcnt(4)" ::: "memory");
    BAR();
    STAMPT(11);
  }
  unsigned z1d[7];
  constexpr int L_RED = 28672;
#define WOA(i) (((i) < 28 ? (i) : (i) < 108 ? (i) + 32 : (i) < 120 ? (i) + 37 : (i) - 92) * 1024)
#define RBAR() do { __builtin_amdgcn_s_barrier(); asm volatile("" ::: "memory"); } while (0)
#define WOD(i0, x) dma1k(rs, lds + WOA(i0) + (x), l16, wgb + (unsigned)(i0) * 1024u + (x))
  constexpr int NW = 14;
  u32x4 zw[NW];
#define LDZ(f) do { if (((f) >> 3) == 4) { zs[(f) & 7] += 4096u; asm volatile("" : "+s"(zs[(f) & 7])); } \
    zw[(f) % NW] = __builtin_amdgcn_raw_buffer_load_b128(rs, l16 + (unsigned)(((f) >> 3) & 3) * 1024u, zs[(f) & 7], SC1); } while (0)
  {
    f32x4 acc[7];
#pragma unroll
    for (int j = 0; j < 7; ++j) acc[j] = (f32x4){0.f, 0.f, 0.f, 0.f};
    u32x4 bf[7];
#pragma unroll
    for (int j = 0; j < 7; ++j) bf[j] = BFRAG(4, j);
    u32x4 pv, zf[2];
    unsigned fwv;
    auto ld_pv = [&]() { pv = __builtin_amdgcn_raw_buffer_load_b128(rs, po, pos, SC1); };
    auto z1 = [&](auto Q0_, auto Q1_) __attribute__((always_inline)) {
#pragma unroll
      for (int qi = decltype(Q0_)::value; qi < decltype(Q1_)::value; ++qi) {
        const int q = (qi + 4) % 10, qn = (qi + 5) % 10;
        SCHED();
#pragma unroll
        for (int j = 0; j < 7; ++j) {
          acc[j] = MFMA16(bf[j], h2[q], acc[j]);
          if (qi < 9 && qi != 4) bf[j] = BFRAG(qn, j);
          if (qi < 2 && (j == 1 || j == 4)) { const int i = (2 * qi + (j >> 2)) * 7 + (w < 7 ? w : 0); dma1k(rs, lds + i * 1024, l16, WS_WINF + (unsigned)c * WBLK + (unsigned)i * 1024u); }
          if (qi == 2 && j == 0) kc_ld(0);
          if (qi == 2 && j == 3) fwv = ld4(rs, fwo[0]);
          if (qi == 3 && j == 1) ld_pv();
          if (qi == 4 && j < 2) zf[j] = *(const u32x4*)(lds + zro + j * 7168);
          SCHED();
        }
      }
    };
    auto req1 = [&]() {
#pragma unroll
      for (int k = 16; k < 24; ++k) { LDW(k); SCHED(); }
    };
    auto req2 = [&]() {
#pragma unroll
      for (int k = 24; k < 32; ++k) { LDW(k); SCHED(); }
      sfor<0, 2>([&](auto G_) __attribute__((always_inline)) { WOD(68 + 8 * decltype(G_)::value, wx8); SCHED(); });
    };
    if (kh1) req1();
    z1(IC<0>(), IC<5>());
    STAMPT(29);
    f32x2 zv[2][4];
#pragma unroll
    for (int i = 0; i < 8; ++i) { zv[i >> 2][i & 3] = (f32x2){bflo(zf[i >> 2][i & 3]), bfhi(zf[i >> 2][i & 3])}; asm volatile("" : "+v"(zv[i >> 2][i & 3])); }
    __builtin_amdgcn_s_setprio(3);
    if (__any(ne2(pv[0], pv[2]) != 0u)) {
      _Pragma("unroll 1") for (unsigned spins = 0; spins < SPIN_CAP; ++spins) {
        ld_pv();
        if (!__any(ne2(pv[0], pv[2]) != 0u)) break;
        __builtin_amdgcn_s_sleep(1);
      }
    }
    {
      float t1 = __uint_as_float(pv[1]), t2 = __uint_as_float(pv[3]);
      t1 += dpp_f<0x128>(t1); t2 += dpp_f<0x128>(t2);
      auto x = __builtin_amdgcn_permlane16_swap(__float_as_uint(t1), __float_as_uint(t2), false, false);
      const unsigned y = __float_as_uint(__uint_as_float(x[0]) + __uint_as_float(x[1]));
      auto z = __builtin_amdgcn_permlane32_swap(y, y, false, false);
      const unsigned u = __float_as_uint(__uint_as_float(z[0]) + __uint_as_float(z[1]));
      auto v = __builtin_amdgcn_permlane16_swap(u, u, false, false);
      const float mean = __uint_as_float(v[0]) * (1.0f / FH);
      const float var = __builtin_fmaxf(__uint_as_float(v[1]) * (1.0f / FH) - mean * mean, 0.f);
      const float rstd = __builtin_amdgcn_rsqf(var + 1e-5f);
      *(float*)(lds + swo) = rstd;
      *(float*)(lds + swo + 256) = -mean * rstd;
    }
    SCHED();
    consts_put(0);
    if (w == 3 && __builtin_amdgcn_ballot_w64(fwv == tag) != 0xfffffffull) wait_lines<1, 1>(rs, fwo, tag);
    if (!kh1) req1();
    BAR();
    STAMP(12);
    {
      f32x4 sa[2][2], sb[2][2];
#pragma unroll
      for (int i = 0; i < 4; ++i) { sa[i >> 1][i & 1] = *(const f32x4*)(lds + sro + i * 64); sb[i >> 1][i & 1] = *(const f32x4*)(lds + sro + i * 64 + 256); }
      SCHED();
#pragma unroll
      for (int m = 0; m < 2; ++m) {
        u32x4 o;
#pragma unroll
        for (int e = 0; e < 4; ++e) {
          const f32x4 a = sa[m][e >> 1], b = sb[m][e >> 1];
          const f32x2 x = __builtin_elementwise_fma(zv[m][e], (f32x2){a[2 * (e & 1)], a[2 * (e & 1) + 1]}, (f32x2){b[2 * (e & 1)], b[2 * (e & 1) + 1]});
          o[e] = pack2(x[0], x[1]);
        }
        ST16S(o, rs, zo + (unsigned)m * 256u, zos, SC1);
      }
    }
    SCHED();
    __builtin_amdgcn_s_setprio(0);
    STAMPT(13);
#pragma unroll
    for (int j = 0; j < 7; ++j) bf[j] = BFRAG(9, j);
    if (kh1) req2();
    z1(IC<5>(), IC<10>());
    f32x2 mine[7];
    if (kh1) {
#pragma unroll
      for (int j = 0; j < 7; ++j) { *(f32x2*)(lds + rdo + L_RED + j * 512) = (f32x2){acc[j][0], acc[j][1]}; mine[j] = (f32x2){acc[j][2], acc[j][3]}; }
    } else {
#pragma unroll
      for (int j = 0; j < 7; ++j) { *(f32x2*)(lds + rdo + L_RED + j * 512) = (f32x2){acc[j][2], acc[j][3]}; mine[j] = (f32x2){acc[j][0], acc[j][1]}; }
    }
    if (!kh1) req2();
    asm volatile("s_waitcnt vmcnt(10)" ::: "memory");
    BAR();
    if (w == 3) flag4(WS_FZ + (unsigned)(c * 32 + R) * 128u);
    STAMP(6);
    auto wo8 = [&](auto K_) __attribute__((always_inline)) {
      constexpr int k = decltype(K_)::value;
      if (k < 12) WOD(k < 3 ? 8 * k : k < 8 ? 8 * k + 4 : 8 * k + 20, wx8);
      else if (k == 12) dma1k(rs, lds + (kh1 ? WOA(116) : WOA(24)) + wx4, l16, wgb + (kh1 ? 116u : 24u) * 1024u + wx4);
      SCHED();
    };
    wo8(IC<0>()); wo8(IC<1>());
    u32x4 gv[8];
    auto gb_ld = [&]() {
#pragma unroll
      for (int k = 0; k < 8; ++k) gv[k] = __builtin_amdgcn_raw_buffer_load_b128(rs, l < 40 ? l16 : 0u, WS_MODQ + (unsigned)(k * 2560 + 1920 + 80 * c) * 8u, SC1);
    };
    if (w == 2) gb_ld();
    f32x2 z[7];
    {
      f32x2 oth[7];
      f32x4 cc[7];
#pragma unroll
      for (int j = 0; j < 7; ++j) {
        oth[j] = *(const f32x2*)(lds + oto + L_RED + j * 512);
        cc[j] = *(const f32x4*)(lds + L_CONST + (16 * j + 4 * h4 + 2 * kh1) * 8);
      }
      SCHED();
#pragma unroll
      for (int j = 0; j < 7; ++j)
        z[j] = __builtin_elementwise_fma(__builtin_elementwise_fma((f32x2){-mu_n, -mu_n}, (f32x2){cc[j][0], cc[j][1]}, oth[j] + mine[j]),
                                         (f32x2){rs_n, rs_n}, (f32x2){cc[j][2], cc[j][3]});
    }
#pragma unroll
    for (int j = 0; j < 7; ++j) asm volatile("" : "+v"(z[j]));
    wo8(IC<2>());
    gelu_n(z, IC<7>(), [&](auto K_) __attribute__((always_inline)) { wo8(IC<3 + decltype(K_)::value>()); });
#pragma unroll
    for (int j = 0; j < 7; ++j) z1d[j] = pack2(z[j][0], z[j][1]);
    asm volatile("" : "+v"(z1d[0]), "+v"(z1d[1]), "+v"(z1d[2]), "+v"(z1d[3]), "+v"(z1d[4]), "+v"(z1d[5]), "+v"(z1d[6]));
    SCHED();
    STAMPT(30);
    if (w == 2) {
      auto bad = [&]() {
        unsigned d = 0u;
#pragma unroll
        for (int k = 0; k < 8; ++k) d |= ne2(gv[k][0], gv[k][2]);
        return __any(d != 0u);
      };
      if (bad()) {
        _Pragma("unroll 1") for (unsigned spins = 0; spins < SPIN_CAP; ++spins) {
          __builtin_amdgcn_s_sleep(8);
          gb_ld();
          if (!bad()) break;
        }
      }
      f32x2 t[4];
#pragma unroll
      for (int k = 0; k < 4; ++k) t[k] = (f32x2){__uint_as_float(gv[2 * k][1]) + __uint_as_float(gv[2 * k + 1][1]), __uint_as_float(gv[2 * k][3]) + __uint_as_float(gv[2 * k + 1][3])};
      *(f32x2*)(lds + L_MISC + l * 8) = rbf2((t[0] + t[1]) + (t[2] + t[3]));
    }
    const unsigned fo[1] = {l < 4 ? fzs + (unsigned)l * 128u : ~0u};
    unsigned fv;
    sfor<8, 32>([&](auto X_) __attribute__((always_inline)) {
      constexpr int x = decltype(X_)::value;
      wo8(IC<x>());
      if (x == 12) { fv = ld4(rs, fo[0]); SCHED(); }
      if (x == 18) {
        STAMP(14);
        if (__builtin_amdgcn_ballot_w64(fv == tag) != 15ull) wait_lines<1, 1>(rs, fo, tag);
        asm volatile("" ::: "memory");
        STAMPT(23);
      }
      if (x >= 18) { LDZ((x - 18) & 15); SCHED(); }
      WSP_TX(x);
      SCHED();
    });
  }

  {
    SCHED();
    STAMP(20);
    const int kh = kh1;
    const unsigned rw = (unsigned)(L_RED + w * 512);
    unsigned rr = (unsigned)(L_RED + w * 4096 + l * 4);
    asm volatile("; m0 is set here, not in front of the first store (one wait state)" : "+v"(rr) : "{m0}"(rw));
    const float bs = bflo(bs_raw);
    unsigned long long hm = 0xffff000000000000ull;
    asm volatile("" : "+s"(hm));
    unsigned l8;
    f32x4 acc2[2][4];
    float rv[8][2];
    u32x4 ud;
    u32x4 P[2][8];
    u32x2 xp[3];
    auto ld_p = [&](int pt, int i) { P[pt][i] = __builtin_amdgcn_raw_buffer_load_b128(rs, l16 + (unsigned)pt * 1024u, ui + (unsigned)i * 2048u, SC1); };
#define RED_PUT1(j, i) asm volatile("ds_write_addtid_b32 %0 offset:%2" :: "v"(acc2[(j) & 1][((i) >> 2) & 3][(i) & 3]), "{m0}"(rw), "n"((((i) >> 2) & 3) * 4096 + (((i) >> 1) & 1) * 16384 + ((i) & 1) * 256) : "memory")
#define RED_PUT(j, r) do { RED_PUT1(j, 4 * (r)); RED_PUT1(j, 4 * (r) + 1); RED_PUT1(j, 4 * (r) + 2); RED_PUT1(j, 4 * (r) + 3); } while (0)
#define RED_GET(i) do { f32x2 x_; asm volatile("ds_read2st64_b32 %0, %1 offset0:%2 offset1:%3" : "=v"(x_) : "v"(rr), "n"(2 * (i)), "n"(2 * (i) + 1) : "memory"); rv[i][0] = x_[0]; rv[i][1] = x_[1]; } while (0)
    float zz[2], gg[2];
    unsigned cs;
    auto sum_rows_u = [&](unsigned v) {
      auto a = __builtin_amdgcn_permlane16_swap(v, v, false, false);
      v = a[0] + a[1];
      auto b = __builtin_amdgcn_permlane32_swap(v, v, false, false);
      return b[0] + b[1];
    };
    auto red_fin = [&](int j, int k, bool fuse = false) {
      const int e = k & 1, i = k >> 1;
#define VADD(d, x, y) asm volatile("v_add_f32 %0, %1, %2" : "=v"(d) : "v"(x), "v"(y))
      if (i < 4) VADD(rv[2 * i][e], rv[2 * i][e], rv[2 * i + 1][e]);
      if (i == 4 || i == 5) VADD(rv[4 * (i & 1)][e], rv[4 * (i & 1)][e], rv[4 * (i & 1) + 2][e]);
      if (i == 6) VADD(rv[0][e], rv[0][e], bs);
      if (i == 7) VADD(gg[e], rv[0][e], rv[4][e]);
      if (i == (fuse ? 9 : 8)) zz[e] = e ? bfhi(z1d[(j + 6) % 7]) : bflo(z1d[(j + 6) % 7]);
      if (i == 9) zz[e] *= gg[e];
      if (i == 9 || (i == 8 && !fuse)) asm volatile("" : "+v"(zz[e]));
      if (k == 20) { ud[j & 3] = pack2(zz[0], zz[1]); asm volatile("" : "+v"(ud[j & 3])); }
      if (k == 21) {
        if (j == 0) asm volatile("v_cndmask_b32 %0, %0, 0, %1" : "+v"(ud[0]) : "s"(hm));
        if (j == 2) asm volatile("v_add3_u32 %0, %1, %2, %3" : "=v"(cs) : "v"(ud[0]), "v"(ud[1]), "v"(ud[2]));
        if (j == 3) {
          cs = tag - sum_rows_u(cs + ud[3]);
          asm volatile("v_cndmask_b32 %0, %0, %1, %2" : "+v"(ud[0]) : "v"(cs), "s"(hm));
        }
        if (j >= 4) { ud[3] = (j == 4 ? tag : ud[3]) - ud[j & 3]; asm volatile("" : "+v"(ud[3])); }
        if (j % 3 == 0 && j) ST16S(ud, rs, l16 + (unsigned)(j / 6) * 1024u, uo, SC1);
      }
    };
    struct Sch { int b1, b2, wp[16], rp[8], vp[22], lp[8], pr[32]; };
    constexpr Sch S = {18, 28, {0, 1, 2, 3, 4, 5, 6, 7, 8, 9, 10, 11, 12, 12, 15, 15}, {19, 19, 20, 22, 23, 23, 24, 24}, {29, 29, 30, 30, 31, 31, 32, 33, 35, 36, 37, 38, 39, 40, 41, 42, 43, 46, 43, 46, 48, 48},
                       {1, 1, 1, 1, 1, 1, 1, 1}, {0, 0, 2, 1, 0, 0, 0, 0, 0, 0, 2, 2, 0, 0, 0, 0, 0, 0, 0, 1, 0, 0, 0, 1, 0, 0, 2, 1, 0, 0, 0, 0}};
    constexpr Sch F = {15, 23, {0, 1, 1, 2, 3, 4, 4, 5, 6, 7, 8, 9, 10, 11, 12, 12}, {15, 16, 16, 17, 17, 18, 18, 19}, {23, 23, 24, 24, 25, 26, 26, 27, 27, 28, 28, 29, 30, 30, 31, 31, 22, 22, 32, 33, 34, 35},
                       {1, 2, 1, 1, 1, 1, 1, 1}, {0, 2, 1, 0, 0, 1, 1, 0, 0, 2, 1, 0, 0, 2, 1, 0, 0, 2, 1, 0, 0, 2, 1, 0, 0, 2, 1, 0, 0, 2, 1, 0}};
    unsigned wb[3], xo;
    const unsigned wl[3] = {(unsigned)(120 + WO3 + w) * 1024u, (unsigned)(128 + WO3 + w) * 1024u, wl2}, wg = wgb - (unsigned)(WO3 * 1024);
    constexpr int NAF = 32, NA0 = 16;
    u32x4 af[NAF];
#define LDA(m) af[(m) % NAF] = *(const u32x4*)(lds + wb[WOA(2 * (m)) >> 16] + (WOA(2 * (m)) & 65535))
    unsigned ck[2];
    const unsigned tag4 = tag * 4u;
    auto p_chk = [&](int pt, int pr, int k) {
      u32x4* q = P[pt] + 4 * pr;
      if (k < 2) ck[k & 1] = q[k & 1][0] + q[k & 1][1];
      else if (k < 8) ck[k & 1] = ck[k & 1] + q[(k & 1) + 2 * ((k >> 2) & 1)][k & 2] + q[(k & 1) + 2 * ((k >> 2) & 1)][(k & 2) + 1];
      else if (k == 8) ck[0] = pt ? ck[0] + ck[1] : sum_rows_u(ck[0] + ck[1]);
      if (k < 9) asm volatile("" : "+v"(ck[k & 1]));
      if (k == 9) {
        if (__builtin_expect(__any(ck[0] != tag4), 0)) {
          _Pragma("unroll 1") for (unsigned spins = 0; spins < SPIN_CAP; ++spins) {
            unsigned d = 0u;
#pragma unroll
            for (int i = 0; i < 4; ++i) ld_p(pt, 4 * pr + i);
#pragma unroll
            for (int i = 0; i < 4; ++i) d += q[i][0] + q[i][1] + q[i][2] + q[i][3];
            if (!pt) d = sum_rows_u(d);
            if (!__any(d != tag4)) break;
            __builtin_amdgcn_s_sleep(1);
          }
        }
      }
      if (k >= 10 && !pt) { unsigned x = q[(k - 10) & 3][0]; asm volatile("v_cndmask_b32 %0, %0, 0, %1" : "+v"(x) : "s"(hm)); q[(k - 10) & 3][0] = x; }
      if (k >= 10 && k < 13 && pt) { q[(k - 10) % 3][3] = q[3][(k - 10) % 3]; asm volatile("" : "+v"(q[(k - 10) % 3])); }
    };
    sfor<0, 224>([&](auto M_) __attribute__((always_inline)) {
      constexpr int mm = decltype(M_)::value, j = mm >> 5, m = mm & 31, y = m - 20;
      constexpr int t = j == 6 && y >= 0 ? 5 + (y % 6) / 2 : m >> 2, r = j == 6 && y >= 0 ? (y & 1) + 2 * (y / 6) : m & 3, f = 8 * j + t;
      constexpr Sch C = j < 4 ? S : F, B = j <= 4 ? S : F;
      if (t == 0) asm volatile("v_mfma_f32_16x16x32_bf16 %0, %1, %2, 0" : "=&v"(acc2[j & 1][r]) : "v"(zw[f % NW]), "v"(wsp[r][t]), "v"(zw[(f | (f < 8 ? 1 : 3)) % NW]));
      else asm volatile("v_mfma_f32_16x16x32_bf16 %0, %1, %2, %0" : "+v"(acc2[j & 1][r]) : "v"(zw[f % NW]), "v"(wsp[r][t]), "v"(zw[(f | (f < 8 ? 1 : 3)) % NW]));
      SCHED();
      if (C.pr[m] == 1) __builtin_amdgcn_s_setprio(1);
      if ((m == C.b1 && j > 0) || (m == C.b2 && j > 0) || mm == 30) { asm volatile("" ::: "memory"); __builtin_amdgcn_s_waitcnt(0xC07F); RBAR(); SCHED(); }
      sfor<0, 16>([&](auto I_) __attribute__((always_inline)) {
        constexpr int i = decltype(I_)::value;
        if (j > 0 && C.wp[i] == m) { RED_PUT1(j + 1, i); SCHED(); }
        if (j > 0 && i < 8 && C.rp[i & 7] == m) { RED_GET(i & 7); SCHED(); }
      });
      if (j == 5 && m == 20) {
#pragma unroll
        for (int i = 0; i < 3; ++i) { wb[i] = (unsigned)(kh * 1024 + i * 65536) + l16; asm volatile("" : "+v"(wb[i])); }
      }
      if (j == 5 && m == 13) {
        asm volatile("v_lshrrev_b32 %0, 1, %1" : "=v"(l8) : "v"(l16));
        xo = (l8 & 0x180u) + (unsigned)psi((l8 >> 3) & 15) * 8u;
        asm volatile("" : "+v"(xo));
      }
      if (j == 6 && m >= 28) { RED_PUT1(6, 2 * (m - 28)); RED_PUT1(6, 2 * (m - 28) + 1); SCHED(); }
      constexpr int ap[NA0] = {9, 10, 11, 13, 13, 14, 14, 19, 20, 21, 22, 23, 24, 25, 26, 27};
      sfor<0, NA0>([&](auto N_) __attribute__((always_inline)) { if (j == 6 && ap[decltype(N_)::value] == m) { LDA(decltype(N_)::value); SCHED(); } });
      sfor<0, 22>([&](auto K_) __attribute__((always_inline)) {
        constexpr int k = decltype(K_)::value;
        if (j > 1 && B.vp[k] == m + 32) { red_fin(j > 1 ? j - 2 : 0, k, B.vp[16] == B.vp[18]); SCHED(); }
        if (j > 0 && C.vp[k] == m) { red_fin(j > 0 ? j - 1 : 0, k); SCHED(); }
      });
      if (C.pr[m] == 2) __builtin_amdgcn_s_setprio(0);
      if ((m & 3) == C.lp[m >> 2]) {
        if (f > 0 && f - 1 + NW < 56) LDZ(f - 1 + NW);
        if (j == 6 && m >= 12) ld_p(0, ((m >> 2) - 3) & 7);
        if (j == 5 && m >= 20) xp[((m >> 2) - 5) % 3] = __builtin_amdgcn_raw_buffer_load_b64(rs, xo + (unsigned)((m >> 2) - 5) * 512u, xs, SC1);
        SCHED();
      }
    });
    STAMP(15);
    f32x4 acc[5];
    f32x4 xf[3], gf[3];
    unsigned go, oo, eo, ho, tw, tr, o1, ta, tb;
    const unsigned s2 = (unsigned)(L_MISC + 128 * kh), s4 = s1 - (unsigned)kh * 1024u;
    unsigned long long om = kh ? ~0ull : 0xccccccccccccccccull;
#define PIN2(a, b) asm volatile("" : "+v"(a), "+v"(b))
    auto glue = [&](int k) {
      if (k == 0) { ta = l8 >> 5; tb = l16 & 48u; PIN2(ta, tb); }
      if (k == 1) { tr = __umul24(ta, 112u) + tb; oo = ta & 12u; PIN2(tr, oo); }
      if (k == 2) { tr += s1; oo += ta; PIN2(tr, oo); }
      if (k == 3) { oo = __umul24(oo, 1280u) + tb; ta = l8 >> 4; PIN2(oo, ta); }
      if (k == 4) { ta &= 24u; tb = l16 & 0xf0u; PIN2(ta, tb); }
      if (k == 5) { tw = __umul24(tb, 7u) + ta; go = (ta << 1) + s2; PIN2(tw, go); }
      if (k == 6) { tw += s1; asm volatile("v_cndmask_b32 %0, %1, -2.0, %2" : "=v"(o1) : "v"(oo), "s"(om)); asm volatile("" : "+v"(tw)); }
      if (k == 7) { eo = l16 + s3; ho = l16 + s4; PIN2(eo, ho); }
      if (k >= 8 && k < 14) {
        const unsigned x = xp[((k - 8) >> 1) % 3][k & 1];
        float lo = bflo(x), hi = bfhi(x);
        PIN2(lo, hi);
        xf[((k - 8) >> 1) % 3][2 * (k & 1)] = lo; xf[((k - 8) >> 1) % 3][2 * (k & 1) + 1] = hi;
      }
    };
    auto g3 = [&](auto M0_, auto M1_) __attribute__((always_inline)) {
      sfor<decltype(M0_)::value, decltype(M1_)::value>([&](auto M_) __attribute__((always_inline)) {
        constexpr int mi = decltype(M_)::value;
        constexpr int m = mi < 55 ? mi : mi < 64 ? 55 + 5 * ((mi - 55) / 3) + (mi - 55) % 3 : 58 + 5 * ((mi - 64) / 2) + (mi & 1), u2 = m / 5;
        const u32x4 fb = u2 < 8 ? P[0][u2 & 7] : P[1][u2 < 11 ? (u2 - 8) & 3 : (u2 - 7) & 7];
        if (m < 5) asm volatile("v_mfma_f32_16x16x32_bf16 %0, %1, %2, 0" : "=&v"(acc[m % 5]) : "v"(af[m % NAF]), "v"(fb));
        else asm volatile("v_mfma_f32_16x16x32_bf16 %0, %1, %2, %0" : "+v"(acc[m % 5]) : "v"(af[m % NAF]), "v"(fb));
        SCHED();
        if (mi == 0) RBAR();
        if (mi < 3) dma1k(rs, lds + wl[mi % 3], l16, wg + wl[mi % 3]);
        if (mi >= 5 && mi < 19) p_chk(0, 1, mi - 5);
        if (mi >= 19 && mi < 33) glue(mi - 19);
        if (mi >= 47 && mi < 54) { p_chk(1, 1, 2 * (mi - 47)); SCHED(); p_chk(1, 1, 2 * (mi - 47) + 1); }
        if (mi < 40) LDA(NA0 + mi);
        if (mi >= 66 && mi < 69) *(f32x4*)(lds + eo + (mi - 66) * 1024) = acc[(mi - 66) % 3];
        SCHED();
      });
    };
    __builtin_amdgcn_s_setprio(3);
#pragma unroll
    for (int k = 18; k < 22; ++k) red_fin(5, k);
    asm volatile("s_nop 2");
    RED_PUT(6, 2); RED_PUT(6, 3);
    red_fin(6, 16); red_fin(6, 17);
    SCHED();
    ld_p(0, 5); ld_p(0, 6);
    SCHED();
    BAR();
    sfor<0, 8>([&](auto I_) __attribute__((always_inline)) { RED_GET(decltype(I_)::value); });
    SCHED();
    ld_p(0, 7);
    SCHED();
    sfor<0, 20>([&](auto K_) __attribute__((always_inline)) {
      constexpr int k = decltype(K_)::value;
      if (k < 8 && !(k & 1)) asm volatile("s_waitcnt lgkmcnt(%0)" :: "n"(6 - k) : "memory");
      red_fin(6, k < 16 ? k : k + 2);
    });
    SCHED();
    __builtin_amdgcn_s_setprio(0);
    STAMPT(16);
#pragma unroll
    for (int k = 0; k < 14; ++k) p_chk(0, 0, k);
    SCHED();
    g3(IC<0>(), IC<24>());
    STAMPT(22);
    DRAIN();
    RBAR();
    STAMPT(27);
    if (!kh) {
#pragma unroll
      for (int i = 0; i < 8; ++i) { ld_p(1, i); SCHED(); }
    }
    g3(IC<24>(), IC<36>());
    if (kh) {
#pragma unroll
      for (int i = 0; i < 8; ++i) { ld_p(1, i); SCHED(); }
    }
    STAMPT(26);
    g3(IC<36>(), IC<40>());
    STAMPT(24);
#pragma unroll
    for (int m = NA0 + 40; m < 70; ++m) LDA(m);
#pragma unroll
    for (int x = 0; x < 3; ++x) gf[x] = *(const f32x4*)(lds + go + x * 64);
    SCHED();
    const rsrc_t ro = __builtin_amdgcn_make_buffer_rsrc((void*)p.out, 0, S_ * D_ * 2, 0x00020000);
    STAMPT(17);
#pragma unroll
    for (int k = 0; k < 14; ++k) p_chk(1, 0, k);
    asm volatile("s_nop 1");
    STAMPT(25);
    g3(IC<40>(), IC<70>());
    STAMPT(18);
    BAR();
    STAMPT(28);
    f32x4 o4[3];
    u32x4 ov[2];
#pragma unroll
    for (int j = 2; j >= 0; --j) o4[j] = *(const volatile LAS f32x4*)(lds + ho + j * 1024);
    asm volatile("" : "+v"(acc[2]), "+v"(acc[3]), "+v"(acc[4]));
    SCHED();
#pragma unroll
    for (int j = 2; j >= 0; --j) {
      const f32x4 v = __builtin_elementwise_fma(gf[j], o4[j] + acc[2 + j], xf[j]);
      *(volatile LAS u32x2*)(lds + tw + j * 32) = (u32x2){pack2(v[0], v[1]), pack2(v[2], v[3])};
      if (j < 2) ov[j] = *(const volatile LAS u32x4*)(lds + tr + j * 32);
    }
    ST16S(ov[1], ro, oo + 32u, os, 0);
    ST16S(ov[0], ro, o1, os, 0);
    if (bid == 0 && w == 5 && l8 == 0u) st4(rs, WS_EPOCH, tag);
  }
  STAMP(19);
}

extern "C" __device__ __attribute__((used, noinline, visibility("hidden"))) void mega_tail() {}

extern "C" void kernel_launch(void* const* d_in, const int* in_sizes, int n_in, void* d_out, int out_size,
                              void* d_ws, size_t ws_size, hipStream_t stream) {
  Params p;
  p.q = (const u16*)d_in[0];
  p.k = (const u16*)d_in[1];
  p.v = (const u16*)d_in[2];
  p.shift_k = (const u16*)d_in[3];
  p.ssm_k = (const u16*)d_in[4];
  p.ssm_D = (const u16*)d_in[5];
  p.w_in = (const u16*)d_in[7];
  p.w_sp = (const u16*)d_in[8];
  p.b_sp = (const u16*)d_in[9];
  p.w_out = (const u16*)d_in[10];
  p.cvec = (const u16*)d_in[11];
  p.w_mod = (const u16*)d_in[12];
  p.out = (u16*)d_out;
  p.ws = (char*)d_ws;
  mega<<<256, 512, 0, stream>>>(p);
}
```

```cpp
#include <hip/hip_runtime.h>
#include <stdint.h>

typedef unsigned short u16;
typedef __attribute__((ext_vector_type(8))) short bf16x8;
typedef __attribute__((ext_vector_type(4))) float f32x4;
typedef __attribute__((ext_vector_type(2))) float f32x2;
typedef __attribute__((ext_vector_type(4))) unsigned int u32x4;
typedef __attribute__((ext_vector_type(2))) unsigned int u32x2;
#define LAS __attribute__((address_space(3)))
typedef __amdgpu_buffer_rsrc_t rsrc_t;
typedef __attribute__((ext_vector_type(4))) short s16x4;

constexpr int S_ = 2048, D_ = 640, F_ = 1728, FH = 864, CB = 108;

constexpr unsigned WS_EPOCH = 0;
constexpr unsigned WS_FWI = 4096;
constexpr unsigned WS_FZ = WS_FWI + 384 * 128;
constexpr unsigned WS_FWO = WS_FZ + 256 * 128;
constexpr unsigned F4S = 896 * 128;
constexpr unsigned WS_CS = WS_FZ + 256 * 128;
constexpr unsigned WS_MODP = 212992;
constexpr unsigned WS_ST1 = 393216;
constexpr unsigned WS_PART = 524288;
constexpr unsigned WS_CSP = WS_PART + 262144;
constexpr unsigned WS_FX = WS_CSP + 655360;
constexpr unsigned WS_X1B = WS_FX + 16 * 4 * 160 * 128;
constexpr unsigned WS_QB = WS_X1B + 2621440;
constexpr unsigned WS_KV3 = WS_QB + 2621440;
constexpr unsigned WS_MODQ = WS_KV3 + 1310720;
constexpr unsigned WBLK = 143360;
constexpr unsigned WS_WINF = WS_MODQ + 245760;
constexpr unsigned WS_WOUTF = WS_WINF + 16 * WBLK;
constexpr unsigned WS_Z2F = WS_WOUTF + 8 * WBLK;
constexpr unsigned WS_UF = WS_Z2F + 8 * 64 * 7168;
constexpr unsigned WS_F4 = WS_UF + 32 * 4 * 8 * 4096;
constexpr unsigned WS_QCK = WS_F4 + 4 * F4S;
constexpr unsigned WS_END = WS_QCK + 131072;
constexpr unsigned WS_STAMP = 200u << 20;

constexpr int G_COPY = 4160;
constexpr int L_U = WBLK;
constexpr int U_CH = (256 + S_) * 2 + 32;
constexpr int L_FLG = L_U + 4 * U_CH;
constexpr int L_RT = 16384;
constexpr int L_CONST = WBLK;
constexpr int L_PST = L_CONST + 2048;
constexpr int L_Z = L_CONST + 5120;
constexpr int L_ST2 = L_Z + 14336;
constexpr int L_MISC = L_ST2 + 512;
constexpr int LDS_TOTAL = 163840;

#define AUX_T 610
#define RT2_T 360
#define CB_T 380
#define W_T 670
#define YG_T 870
#define KE_T 460
#define K_T 480
#define G_T 790
#define WA_T 580
constexpr int SC1 = 16, NT = 2;
constexpr unsigned SPIN_CAP = 1u << 16;

__device__ __forceinline__ float bflo(unsigned x) { return __uint_as_float(x << 16); }
__device__ __forceinline__ float bfhi(unsigned x) { return __uint_as_float(x & 0xffff0000u); }
__device__ __forceinline__ unsigned f2bf(float x) {
  __bf16 b = (__bf16)x;
  return (unsigned)__builtin_bit_cast(unsigned short, b);
}
__device__ __forceinline__ float rbf(float x) { return bflo(f2bf(x)); }
typedef __attribute__((ext_vector_type(2))) __bf16 bf2_t;
__device__ __forceinline__ unsigned pack2(float lo, float hi) {
  f32x2 v = {lo, hi};
  return __builtin_bit_cast(unsigned, __builtin_convertvector(v, bf2_t));
}
__device__ __forceinline__ f32x2 rbf2(f32x2 v) {
  const unsigned p = __builtin_bit_cast(unsigned, __builtin_convertvector(v, bf2_t));
  return (f32x2){__uint_as_float(p << 16), __uint_as_float(p & 0xffff0000u)};
}
__device__ __forceinline__ f32x4 rbf4(f32x4 v) {
  const f32x2 a = rbf2((f32x2){v[0], v[1]}), b = rbf2((f32x2){v[2], v[3]});
  return (f32x4){a[0], a[1], b[0], b[1]};
}
__device__ __forceinline__ f32x4 fma4(f32x4 a, f32x4 b, f32x4 c) {
  float r[4];
  asm("v_fma_f32 %0, %4, %8, %12\n\tv_fma_f32 %1, %5, %9, %13\n\tv_fma_f32 %2, %6, %10, %14\n\tv_fma_f32 %3, %7, %11, %15"
      : "=&v"(r[0]), "=&v"(r[1]), "=&v"(r[2]), "=&v"(r[3])
      : "v"(a[0]), "v"(a[1]), "v"(a[2]), "v"(a[3]), "v"(b[0]), "v"(b[1]), "v"(b[2]), "v"(b[3]), "v"(c[0]), "v"(c[1]), "v"(c[2]), "v"(c[3]));
  return (f32x4){r[0], r[1], r[2], r[3]};
}
template <int CTRL>
__device__ __forceinline__ float dpp_f(float v) {
  return __int_as_float(__builtin_amdgcn_update_dpp(0, __float_as_int(v), CTRL, 0xf, 0xf, true));
}
__device__ __forceinline__ float sum8(float v) {
  v += dpp_f<0xB1>(v);
  v += dpp_f<0x4E>(v);
  v += dpp_f<0x141>(v);
  return v;
}
__device__ __forceinline__ float sum16(float v) {
  v = sum8(v);
  v += dpp_f<0x140>(v);
  return v;
}
__device__ __forceinline__ float sum_rows(float v) {
  auto a = __builtin_amdgcn_permlane16_swap(__float_as_uint(v), __float_as_uint(v), false, false);
  v = __uint_as_float(a[0]) + __uint_as_float(a[1]);
  auto b = __builtin_amdgcn_permlane32_swap(__float_as_uint(v), __float_as_uint(v), false, false);
  return __uint_as_float(b[0]) + __uint_as_float(b[1]);
}
#define MFMA16(a, b, c) __builtin_amdgcn_mfma_f32_16x16x32_bf16(__builtin_bit_cast(bf16x8, a), __builtin_bit_cast(bf16x8, b), c, 0, 0, 0)
#define MFMA_IP(c, a, b) asm volatile("v_mfma_f32_16x16x32_bf16 %0, %1, %2, %0" : "+v"(c) : "v"(a), "v"(b))
__device__ __forceinline__ u32x2 trd(const char* p) {
  return __builtin_bit_cast(u32x2, __builtin_amdgcn_ds_read_tr16_b64_v4i16((LAS s16x4*)p));
}
__device__ __forceinline__ u32x4 trfrag(const char* p, int pitch) {
  u32x2 a = trd(p), b = trd(p + 16 * pitch);
  return (u32x4){a[0], a[1], b[0], b[1]};
}
#define BAR() asm volatile("s_waitcnt lgkmcnt(0)\n\ts_barrier" ::: "memory")
#define DRAIN() do { __builtin_amdgcn_s_waitcnt(0x0F70); asm volatile("" ::: "memory"); } while (0)
#define SCHED() __builtin_amdgcn_sched_barrier(0)
template <int N> struct IC { static constexpr int value = N; };
template <int I, int N, class F>
__device__ __forceinline__ void sfor(F&& f) {
  if constexpr (I < N) { f(IC<I>()); sfor<I + 1, N>(f); }
}

__device__ __forceinline__ u32x2 ld8(rsrc_t r, unsigned off) { return __builtin_amdgcn_raw_buffer_load_b64(r, off, 0, SC1); }
__device__ __forceinline__ unsigned ld4(rsrc_t r, unsigned off) { return __builtin_amdgcn_raw_buffer_load_b32(r, off, 0, SC1); }
__device__ __forceinline__ void st16(rsrc_t r, unsigned off, u32x4 v) { __builtin_amdgcn_raw_buffer_store_b128(v, r, off, 0, SC1); }
#define ST16S(v, r, voff, soff, aux) do { const u32x4 v_ = (v); __builtin_amdgcn_raw_buffer_store_b128(v_, r, voff, soff, aux); asm volatile("s_nop 1" :: "v"(v_)); } while (0)
__device__ __forceinline__ void st4(rsrc_t r, unsigned off, unsigned v) { __builtin_amdgcn_raw_buffer_store_b32(v, r, off, 0, SC1); }
__device__ __forceinline__ void dma1k(rsrc_t r, char* lds_dst, unsigned l16, unsigned soff) {
  __builtin_amdgcn_raw_ptr_buffer_load_lds(r, (LAS void*)lds_dst, 16, l16, soff, 0, SC1);
}

#define STAMP(i)
#define STAMPF(i)
#define STAMPT(i) STAMP(i)

template <int N, int SLP>
__device__ __forceinline__ void wait_lines(rsrc_t r, const unsigned (&off)[N], unsigned tag) {
  bool need[N];
#pragma unroll
  for (int i = 0; i < N; ++i) need[i] = off[i] != ~0u;
  _Pragma("unroll 1") for (unsigned spins = 0; spins < SPIN_CAP; ++spins) {
    unsigned v[N];
#pragma unroll
    for (int i = 0; i < N; ++i) {
      v[i] = tag;
      if (need[i]) v[i] = ld4(r, off[i]);
    }
    bool any = false;
#pragma unroll
    for (int i = 0; i < N; ++i) { need[i] = v[i] != tag; any = any || need[i]; }
    if (!__any(any)) break;
    __builtin_amdgcn_s_sleep(SLP);
  }
  asm volatile("" ::: "memory");
}

__device__ __forceinline__ int tau(int rt, int n) { return 32 * (rt >> 1) + 8 * (n >> 2) + 4 * (rt & 1) + (n & 3); }
__device__ __forceinline__ int psi(int n) { return 2 * (n & 7) + (n >> 3); }
#define QX_CM(d, s0, s1, P) "v_cndmask_b32_dpp " d ", " s0 ", " s1 ", vcc quad_perm:" P " row_mask:0xf bank_mask:0xf\n\t"
#define QX_BODY(M, P) \
  asm("s_mov_b32 vcc_lo, " M "\n\ts_mov_b32 vcc_hi, " M "\n\t" \
      QX_CM("%0", "%12", "%8", P) "v_xor_b32 %4, %8, %0\n\t" QX_CM("%1", "%13", "%9", P) "v_xor_b32 %5, %9, %1\n\t" \
      QX_CM("%2", "%14", "%10", P) "v_xor_b32 %6, %10, %2\n\t" QX_CM("%3", "%15", "%11", P) "v_xor_b32 %7, %11, %3" \
      : "=&v"(t[0]), "=&v"(t[1]), "=&v"(t[2]), "=&v"(t[3]), "=&v"(u[0]), "=&v"(u[1]), "=&v"(u[2]), "=&v"(u[3]) \
      : "v"(a[0]), "v"(a[1]), "v"(a[2]), "v"(a[3]), "v"(b[0]), "v"(b[1]), "v"(b[2]), "v"(b[3]) : "vcc")
template <int BIT>
__device__ __forceinline__ void quad_xchg3(u32x4& a, u32x4& b) {
  unsigned t[4], u[4];
  if (BIT == 0) QX_BODY("0x55555555", "[1,0,3,2]");
  else QX_BODY("0x33333333", "[2,3,0,1]");
  a = (u32x4){t[0], t[1], t[2], t[3]};
#pragma unroll
  for (int d = 0; d < 4; ++d) b[d] ^= (unsigned)__builtin_amdgcn_update_dpp(0, (int)u[d], BIT == 0 ? 0xB1 : 0x4E, 0xf, 0xf, true);
}
struct Params {
  const u16 *q, *k, *v, *shift_k, *ssm_k, *ssm_D, *w_in, *w_sp, *b_sp, *w_out, *cvec, *w_mod;
  u16* out;
  char* ws;
};

__global__ __launch_bounds__(512, 2) void mega(Params p) {
  __shared__ __attribute__((aligned(16))) char lds[LDS_TOTAL];
  {
    unsigned long long pc0;
    unsigned vt;
    asm volatile("v_readfirstlane_b32 vcc_lo, %3\n\ts_cmpk_lt_u32 %2, 192\n\ts_cbranch_scc1 4f\n\ts_cmpk_lt_u32 vcc_lo, 64\n\ts_cbranch_scc0 4f\n\t"
                 "s_lshr_b32 vcc_hi, %2, 3\n\ts_sub_u32 vcc_hi, vcc_hi, 24\n\ts_lshl_b32 vcc_hi, vcc_hi, 13\n\t"
                 "v_lshlrev_b32 %1, 7, %3\n\tv_add_u32 %1, vcc_hi, %1\n\ts_getpc_b64 %0\n.Lmega_here:\n\t"
                 "v_min_u32 %1, mega_tail-.Lmega_here-4, %1\n\ts_movk_i32 m0, 0x2000\n\ts_nop 1\n\tglobal_load_lds_dword %1, %0\n4:\n\t"
                 "s_cmpk_lt_u32 %2, 160\n\ts_cbranch_scc0 1f\n\ts_cmpk_lt_u32 vcc_lo, 256\n\ts_cbranch_scc1 3f\n\t"
                 "s_call_b64 vcc, .Ltouch1\n\ts_branch 3f\n1:\n\t"
                 "s_cmpk_lt_u32 vcc_lo, 256\n\ts_cbranch_scc1 2f\n\ts_cmpk_lt_u32 %2, 224\n\ts_cbranch_scc0 2f\n\ts_call_b64 vcc, .Ltouch3\n\ts_branch 3f\n"
                 "2:\n\ts_call_b64 vcc, .Ltouch2\n3:"
                 : "=&s"(pc0), "=&v"(vt) : "s"(blockIdx.x), "v"(threadIdx.x) : "vcc", "scc", "memory");
  }
  SCHED();
  const int tid = threadIdx.x, l = tid & 63;
  const int w = __builtin_amdgcn_readfirstlane(tid >> 6);
  const int bid = blockIdx.x, xcd = bid & 7, idx = bid >> 3;
#define TOUCH_AT(n) asm volatile("s_branch 1f\n.Ltouch" #n ":\n\ts_setpc_b64 vcc\n1:")
  char* const ws = p.ws;
  asm volatile("" ::"s"(p.q), "s"(p.k), "s"(p.v), "s"(p.shift_k), "s"(p.ssm_k), "s"(p.ssm_D), "s"(p.w_in), "s"(p.w_sp), "s"(p.b_sp),
               "s"(p.w_out), "s"(p.cvec), "s"(p.w_mod), "s"(p.out), "s"(p.ws));
  unsigned epoch, tag, t_start;
  unsigned long long t64;
  asm volatile("s_load_dword %0, %2, 0x0\n\ts_memrealtime %1" : "=&s"(epoch), "=&s"(t64) : "s"(ws));
#define TAG_READY() do { SCHED(); \
    asm volatile("s_waitcnt lgkmcnt(0)\n\ts_mul_i32 %0, %2, 0x9e3779b1\n\ts_add_i32 %0, %0, 0x9e3779b9\n\ts_mov_b64 %1, %3" \
                 : "=&s"(tag), "=&s"(t64r) : "s"(epoch), "s"(t64) : "scc"); t_start = (unsigned)t64r; SCHED(); } while (0)
  unsigned long long t64r;
  const rsrc_t rs = __builtin_amdgcn_make_buffer_rsrc(ws, 0, WS_END, 0x00020000);
  const unsigned l16 = (unsigned)l * 16u;
  STAMP(0);
  const bool is_conv = idx < 20;
  const int quad = xcd * 20 + idx;
  const int R = is_conv ? (xcd & 3) * 5 + (idx >> 2) : (xcd & 3) * 3 + 15 + (idx >> 2);
  const int c = (xcd >> 2) * 4 + (idx & 3);
  const unsigned fwi = WS_F4 + (unsigned)(xcd & 3) * F4S, fz = fwi + (WS_FZ - WS_FWI);
  const int n16 = l & 15, h4 = l >> 4;
  u32x4 wsp[4][8];
  const int rotg = ((idx >> 4) ^ (idx >> 1)) & 1, rotb = (idx >> 2) & 3;
  const rsrc_t rsp = __builtin_amdgcn_make_buffer_rsrc((void*)p.w_sp, 0, S_ * S_ * 2, 0x00020000);
  const unsigned wsp_vo = (unsigned)(((l >> 2) & 3) * 8 * 4096 + h4 * 64 + ((l + rotb) & 3) * 16);
#define LDW(k) wsp[((k) >> 2) & 3][((k) >> 4) * 4 + ((k) & 3)] = __builtin_amdgcn_raw_buffer_load_b128(rsp, wvo, \
    (unsigned)((R * 64 + 16 * (((k) >> 2) & 2) + 4 * (((k) >> 2) & 1) + ((k) & 3)) * 4096 + w * 512 + ((((k) >> 4) ^ rotg) * 256)), 0)
#define WSP_Q(g, k) wsp[(g) & 3][(((g) >> 2) & 1) * 4 + (k)]
#define WSP_TX(x) do { if (((x) & 2) == 0) quad_xchg3<0>(WSP_Q((x) >> 2, 2 * ((x) & 1)), WSP_Q((x) >> 2, 2 * ((x) & 1) + 1)); \
    else quad_xchg3<1>(WSP_Q((x) >> 2, (x) & 1), WSP_Q((x) >> 2, ((x) & 1) + 2)); \
    asm volatile("" : "+v"(WSP_Q((x) >> 2, 0)), "+v"(WSP_Q((x) >> 2, 1)), "+v"(WSP_Q((x) >> 2, 2)), "+v"(WSP_Q((x) >> 2, 3))); } while (0)
  const rsrc_t rwi = __builtin_amdgcn_make_buffer_rsrc((void*)p.w_in, 0, D_ * F_ * 2, 0x00020000);
  const rsrc_t rwo = __builtin_amdgcn_make_buffer_rsrc((void*)p.w_out, 0, FH * D_ * 2, 0x00020000);
  const rsrc_t rq = __builtin_amdgcn_make_buffer_rsrc((void*)p.q, 0, S_ * D_ * 2, 0x00020000);
  auto hold = [&](unsigned tt) { while ((unsigned)__builtin_amdgcn_s_memrealtime() - t_start < tt) __builtin_amdgcn_s_sleep(2); };
  auto ne2 = [&](unsigned x, unsigned y) { unsigned d; asm("v_bitop3_b32 %0, %1, %2, %3 bitop3:0x7e" : "=v"(d) : "v"(x), "v"(y), "s"(tag)); return d; };
  auto sum8u = [&](unsigned v) {
    v += (unsigned)__builtin_amdgcn_update_dpp(0, (int)v, 0xB1, 0xf, 0xf, true);
    v += (unsigned)__builtin_amdgcn_update_dpp(0, (int)v, 0x4E, 0xf, 0xf, true);
    return v + (unsigned)__builtin_amdgcn_update_dpp(0, (int)v, 0x141, 0xf, 0xf, true);
  };
  volatile LAS unsigned* lf = (volatile LAS unsigned*)(lds + L_FLG);
  auto lf_set = [&](int i) { asm volatile("s_waitcnt lgkmcnt(0)" ::: "memory"); if (l == 0) lf[i] = tag; };
  auto lf_wait = [&](int i0, int n) {
    _Pragma("unroll 1") for (unsigned spins = 0; spins < (SPIN_CAP << 4); ++spins) {
      const unsigned v = lf[i0 + (l & (n - 1))];
      if (__all(v == tag)) break;
      __builtin_amdgcn_s_sleep(1);
    }
    asm volatile("" ::: "memory");
  };
  const unsigned b2o = fwi + (unsigned)((8 + c) * 20 + 5 * (w & 3) + (l < 5 ? l : 0)) * 128u;
  auto b2_wait = [&](u32x2 fv, int slp) {
    if (!__all(fv[0] == tag)) {
      _Pragma("unroll 1") for (unsigned spins = 0; spins < SPIN_CAP; ++spins) {
        if (fv[0] != tag) fv = ld8(rs, b2o);
        if (__all(fv[0] == tag)) break;
        if (slp) __builtin_amdgcn_s_sleep(8); else __builtin_amdgcn_s_sleep(4);
      }
    }
    asm volatile("" ::: "memory");
    if (l < 5) lf[44 + 5 * w + l] = fv[1];
    if (l == 0) lf[36 + w] = tag;
  };
  auto load_b2 = [&](int k0, int k1) {
    const unsigned a0 = (unsigned)(35 * w + (idx >> 2) * 4) * 1024u, gb = WS_WINF + (unsigned)(8 + c) * WBLK;
    unsigned a = a0, g = gb + a0;
#pragma unroll
    for (int k = k0; k < k1; ++k) {
      if (k == k0 || (k & 3) == 3) {
        a = k + (idx >> 2) * 4 >= 35 ? a0 - 35840u : a0;
        g = gb + a;
        asm volatile("" : "+s"(a), "+s"(g));
      }
      dma1k(rs, lds + a + k * 1024, l16, g + (unsigned)k * 1024u);
    }
  };
  char* const wo_st = lds + 122880;
  auto wo_ld = [&]() {
    const int U = R < 28 ? R : 0, u2 = U >> 1, fi = (u2 + 1) % 3;
    const int c0 = 4 * (U & 1) + (u2 < 8 ? u2 >> 1 : u2 < 11 ? 0 : 2);
    const int b0 = u2 < 8 ? CB * c0 + 2 * (u2 & 1) + 96 : CB * (c0 + (fi >> 1)) + 48 + 2 * (fi & 1), d1 = u2 < 8 ? -96 : 16, d2 = d1 + 16;
    const int b3 = u2 < 8 ? b0 - 64 : CB * (c0 + 1) + 50 + 16 * fi;
#pragma unroll
    for (int r = 0; r < 5; ++r) {
      const int g = r * 64 + l, rho = g / 10;
      const int fb = (rho & 16) ? ((rho & 2) ? b3 : b0 + d2) : ((rho & 2) ? b0 + d1 : b0);
      __builtin_amdgcn_raw_ptr_buffer_load_lds(rwo, (LAS void*)(wo_st + r * 1024), 16, (unsigned)((fb + (rho & 13)) * (D_ * 2) + (g % 10) * 16 + c * 160), 0, 0, 0);
    }
  };
  auto rt_ld = [&](int i0, char* st) {
    const int cb = (i0 < 160 ? 8 : 0) + (i0 & 7), Q = (i0 < 160 ? i0 : i0 - 160) >> 3;
#pragma unroll
    for (int r = 0; r < 7; ++r) {
      const int g = r * 64 + l;
      __builtin_amdgcn_raw_ptr_buffer_load_lds(rwi, (LAS void*)(st + r * 1024), 16, (unsigned)((g / 14) * (F_ * 2) + (g % 14) * 16 + Q * 32 * F_ * 2 + cb * CB * 2),
                                               0, 0, NT);
    }
  };
  auto rt_job = [&](int i0, char* st) {
    const int cb = (i0 < 160 ? 8 : 0) + (i0 & 7), Q = (i0 < 160 ? i0 : i0 - 160) >> 3;
    const int col = 640 * (l >> 5) + 32 * Q + (l & 31);
    u32x2 pp8[8];
    auto ld_pp = [&]() {
#pragma unroll
      for (int i = 0; i < 8; ++i) pp8[i] = __builtin_amdgcn_raw_buffer_load_b64(rs, (unsigned)col * 8u, WS_MODP + (unsigned)i * 10240u, SC1);
    };
    auto pp_bad = [&]() { return __any(((ne2(pp8[0][0], pp8[1][0]) | ne2(pp8[2][0], pp8[3][0]) | ne2(pp8[4][0], pp8[5][0])) | ne2(pp8[6][0], pp8[7][0])) != 0u); };
    hold(CB_T);
    ld_pp();
    SCHED();
    __builtin_amdgcn_s_waitcnt(0x0F78);
    asm volatile("" ::: "memory");
    u32x4 x[7], o[7];
    f32x2 xu[7][4];
    {
      const char* bp = st + (4 * h4 + (n16 >> 2)) * 224 + (n16 & 3) * 8;
#pragma unroll
      for (int j = 0; j < 7; ++j) x[j] = trfrag(bp + j * 32, 224);
#pragma unroll
      for (int j = 0; j < 7; ++j)
#pragma unroll
        for (int e = 0; e < 4; ++e) { xu[j][e] = (f32x2){bflo(x[j][e]), bfhi(x[j][e])}; asm volatile("" : "+v"(xu[j][e])); }
    }
    unsigned mk[4][2], cw = l >= 32 ? (unsigned)(7168 + (l - 32) * 4) : (unsigned)(7298 + l * 2 - 4), m12 = n16 == 12 ? ~0u : 0u;
    unsigned fgo = l < 32 ? (unsigned)(l >> 3) * F4S + (unsigned)(l & 7) * 16u : 0x80000000u;
#pragma unroll
    for (int i = 0; i < 4; ++i) {
      mk[i][0] = n16 == 4 * i ? 0x3F803F80u : 0u; mk[i][1] = n16 == 4 * i + 1 ? ~0u : 0u;
      asm volatile("" : "+v"(mk[i][0]), "+v"(mk[i][1]));
    }
    asm volatile("" : "+v"(cw), "+v"(m12), "+v"(fgo));
    unsigned ob = WS_WINF + (unsigned)cb * WBLK + (unsigned)(Q * 7) * 1024u;
    asm volatile("" : "+s"(ob));
    if (pp_bad()) {
      _Pragma("unroll 1") for (unsigned spins = 0; spins < SPIN_CAP; ++spins) {
        __builtin_amdgcn_s_sleep(4);
        ld_pp();
        if (!pp_bad()) break;
      }
    }
    STAMPF(9);
    __builtin_amdgcn_s_setprio(3);
    {
      auto pf = [&](int i) { return __uint_as_float(pp8[i][1]); };
      const float t = rbf(((pf(0) + pf(1)) + (pf(2) + pf(3))) + ((pf(4) + pf(5)) + (pf(6) + pf(7))));
      if (l >= 32) *(float*)(st + cw) = rbf(1.0f + t);
      else *(u16*)(st + cw + 2) = (u16)(__float_as_uint(t) >> 16);
    }
    asm volatile("" ::: "memory");
    const f32x4 ca = *(const volatile LAS f32x4*)(st + 7168 + h4 * 16), cc = *(const volatile LAS f32x4*)(st + 7168 + 64 + h4 * 16);
    const u32x2 e0 = *(const volatile LAS u32x2*)(st + 7296 + h4 * 8), e1 = *(const volatile LAS u32x2*)(st + 7328 + h4 * 8);
    SCHED();
#pragma unroll
    for (int j = 0; j < 7; ++j) {
      const f32x2 y0 = xu[j][0] * (f32x2){ca[0], ca[1]}, y1 = xu[j][1] * (f32x2){ca[2], ca[3]}, y2 = xu[j][2] * (f32x2){cc[0], cc[1]}, y3 = xu[j][3] * (f32x2){cc[2], cc[3]};
      o[j] = (u32x4){pack2(y0[0], y0[1]), pack2(y1[0], y1[1]), pack2(y2[0], y2[1]), pack2(y3[0], y3[1])};
      if (j == 6) {
#pragma unroll
        for (int e = 0; e < 4; ++e) o[j][e] = (o[j][e] & ~m12) | mk[3][0];
      }
      ST16S(o[j], rs, l16 + (unsigned)(j & 3) * 1024u, ob + (unsigned)(j >> 2) * 4096u, SC1);
      SCHED();
    }
    __builtin_amdgcn_s_setprio(0);
    STAMPF(11);
    f32x4 d[2] = {{0.f, 0.f, 0.f, 0.f}, {0.f, 0.f, 0.f, 0.f}};
#pragma unroll
    for (int j = 0; j < 7; ++j) {
      const unsigned one = mk[j & 3][0], sr = mk[j & 3][1];
      d[j >> 2] = MFMA16(((u32x4){one, one, one, one}), o[j], d[j >> 2]);
      d[j >> 2] = MFMA16(((u32x4){sr & e0[0], sr & e0[1], sr & e1[0], sr & e1[1]}), x[j], d[j >> 2]);
    }
#pragma unroll
    for (int m = 0; m < 2; ++m)
      st16(rs, WS_CSP + (unsigned)((cb * 20 + Q) * 2 + m) * 1024u + l16, (u32x4){tag, __float_as_uint(d[m][0]), tag, __float_as_uint(d[m][1])});
    STAMPF(13);
    u32x4 tg4 = {tag, t_start, tag, tag};
    asm volatile("s_waitcnt vmcnt(2)" : "+v"(tg4) :: "memory");
    ST16S(tg4, rs, fgo, WS_F4 + (unsigned)(cb * 20 + Q) * 128u, SC1);
  };

  const rsrc_t rwm = __builtin_amdgcn_make_buffer_rsrc((void*)p.w_mod, 0, D_ * 3840 * 2, 0x00020000);
  auto gemv_load = [&](u32x4* pre, int blk, int sl, int np) {
    pre[10][0] = *(const unsigned*)(p.cvec + 8 * np * sl + 2 * (l < 4 * np ? l : 0));
#pragma unroll
    for (int pp = 0; pp < np; ++pp)
      pre[pp] = __builtin_amdgcn_raw_buffer_load_b128(rwm, (unsigned)((l >> 3) * 7680 + (l & 7) * 16), (unsigned)((8 * np * sl + pp * 8) * 7680 + blk * 128), NT);
  };
  auto gemv_fin = [&](float* acc, const u32x4* pre, int np, char* scr) {
    const unsigned cv2 = pre[10][0];
    unsigned* svt = (unsigned*)scr + w * 96;
#pragma unroll
    for (int e = 0; e < 2; ++e) {
      const int row = 2 * l + e;
      const float cv = e ? bfhi(cv2) : bflo(cv2);
      if (l < 4 * np) svt[(row & 7) * 12 + (row >> 3)] = __float_as_uint(rbf(cv * __builtin_amdgcn_rcpf(1.0f + __expf(-cv))));
    }
    asm volatile("s_waitcnt lgkmcnt(0)" ::: "memory");
    u32x4 sv[3];
#pragma unroll
    for (int i = 0; i < (np + 3) / 4; ++i) sv[i] = *(const u32x4*)(svt + (l >> 3) * 12 + 4 * i);
    SCHED();
#pragma unroll
    for (int e = 0; e < 8; ++e) acc[e] = 0.f;
#pragma unroll
    for (int pp = 0; pp < np; ++pp) {
      const unsigned sh = sv[pp >> 2][pp & 3], sl2 = sh >> 16;
#pragma unroll
      for (int e = 0; e < 4; ++e) {
        const unsigned x = pre[pp][e];
        acc[2 * e] = __builtin_amdgcn_fdot2_f32_bf16(__builtin_bit_cast(bf2_t, x), __builtin_bit_cast(bf2_t, sl2), acc[2 * e], false);
        acc[2 * e + 1] = __builtin_amdgcn_fdot2_f32_bf16(__builtin_bit_cast(bf2_t, x), __builtin_bit_cast(bf2_t, sh), acc[2 * e + 1], false);
      }
    }
#pragma unroll
    for (int e = 0; e < 4; ++e) {
      auto x = __builtin_amdgcn_permlane32_swap(__float_as_uint(acc[e]), __float_as_uint(acc[e + 4]), false, false);
      acc[e] = __uint_as_float(x[0]) + __uint_as_float(x[1]);
    }
#pragma unroll
    for (int e = 0; e < 2; ++e) {
      auto x = __builtin_amdgcn_permlane16_swap(__float_as_uint(acc[e]), __float_as_uint(acc[e + 2]), false, false);
      acc[e] = __uint_as_float(x[0]) + __uint_as_float(x[1]);
    }
    const float t0 = acc[0] + dpp_f<0x128>(acc[0]), t1 = acc[1] + dpp_f<0x128>(acc[1]);
    acc[0] = (l & 8) ? t1 : t0;
  };
  auto gemv_put = [&](const float* acc, unsigned off) {
    const float v = acc[0];
    const int src = ((l & 3) * 16 + ((l >> 2) & 7)) * 4;
    const unsigned v0 = (unsigned)__builtin_amdgcn_ds_bpermute(src, (int)__float_as_uint(v)), v1 = (unsigned)__builtin_amdgcn_ds_bpermute(src + 32, (int)__float_as_uint(v));
    if (l < 32) st16(rs, off + l16, (u32x4){tag, v0, tag, v1});
  };
  auto cs_ld = [&](u32x4* v, int cj) {
    const int cb = cj / 7, j = cj % 7;
#pragma unroll
    for (int k = 0; k < 5; ++k)
      v[k] = __builtin_amdgcn_raw_buffer_load_b128(rs, (unsigned)(h4 * 10240 + n16 * 16), WS_CSP + (unsigned)((cb * 40 + (j >> 2) + 2 * k) * 1024 + (j & 3) * 256), SC1);
  };
  auto cs_fin = [&](u32x4* v, int cj) {
    auto bad = [&]() { return __any(((ne2(v[0][0], v[0][2]) | ne2(v[1][0], v[1][2]) | ne2(v[2][0], v[2][2])) | ne2(v[3][0], v[3][2]) | ne2(v[4][0], v[4][2])) != 0u); };
    if (bad()) {
      _Pragma("unroll 1") for (unsigned spins = 0; spins < SPIN_CAP; ++spins) {
        __builtin_amdgcn_s_sleep(8);
        cs_ld(v, cj);
        if (!bad()) break;
      }
    }
    float t0 = 0.f, t1 = 0.f;
#pragma unroll
    for (int k = 0; k < 5; ++k) { t0 += __uint_as_float(v[k][1]); t1 += __uint_as_float(v[k][3]); }
    t0 = sum_rows(t0); t1 = sum_rows(t1);
    if (l < 16) st16(rs, WS_CS + (unsigned)((cj / 7) * 2048 + (cj % 7) * 256) + l16, (u32x4){tag, __float_as_uint(t0), tag, __float_as_uint(t1)});
  };
  const bool cs2 = w == 2 && R >= 20 && R < 27, cs1 = w == 2 && R >= 25;
  u32x4 csv[5];
  typedef const __attribute__((address_space(4))) unsigned* cptr_t;

  if (__builtin_expect(is_conv, 1)) {
    if (w >= 4) {
      TOUCH_AT(1);
      const int pw = w - 4;
      const rsrc_t ra = __builtin_amdgcn_make_buffer_rsrc((void*)((idx & 2) ? p.v : p.k), 0, S_ * D_ * 2, 0x00020000);
      const rsrc_t rb = __builtin_amdgcn_make_buffer_rsrc((void*)((idx & 2) ? p.k : p.v), 0, S_ * D_ * 2, 0x00020000);
      const rsrc_t rk = __builtin_amdgcn_make_buffer_rsrc((void*)p.k, 0, S_ * D_ * 2, 0x00020000);
      const rsrc_t rv = __builtin_amdgcn_make_buffer_rsrc((void*)p.v, 0, S_ * D_ * 2, 0x00020000);
      const unsigned vo = (unsigned)((128 * pw + 2 * l) * (D_ * 2));
      const unsigned so0 = (unsigned)((idx & 1) * (D_ * 2) + quad * 8), so1 = (unsigned)((1 - (idx & 1)) * (D_ * 2) + quad * 8);
      u32x2 g[3][4], kh;
      auto gl = [&](int s, int j0, int j1) {
        __builtin_amdgcn_s_setprio(0);
#pragma unroll
        for (int j = j0; j < j1; ++j) {
          if (s < 2) g[s][j] = __builtin_amdgcn_raw_buffer_load_b64(j < 2 ? ra : rb, vo, ((j & 1) ? so1 : so0) + (unsigned)(s * 512 * D_ * 2), 0);
          else g[s][j] = __builtin_amdgcn_raw_buffer_load_b64(j < 2 ? rk : rv, vo + (unsigned)((j & 1) * D_ * 2), (unsigned)(quad * 8 + s * 512 * D_ * 2), 0);
          SCHED();
        }
        __builtin_amdgcn_s_setprio(3);
      };
      const cptr_t sp = (cptr_t)(p.shift_k + quad * 16);
      unsigned spv[8];
#pragma unroll
      for (int i = 0; i < 8; ++i) spv[i] = sp[i];
      __builtin_amdgcn_s_sleep(3);
      gl(0, 0, 4);
      {
        const int row = 512 * (l >> 2) + 128 * pw - 3 + (l & 3);
        kh = __builtin_amdgcn_raw_buffer_load_b64(rk, (l >= 12 || (l & 3) == 3 || row < 0) ? 0x80000000u : (unsigned)(row * D_ * 2 + quad * 8), 0, 0);
      }
      TAG_READY();
      f32x2 wt[2][4];
#pragma unroll
      for (int d = 0; d < 2; ++d) {
        wt[d][0] = (f32x2){bflo(spv[4 * d]), bflo(spv[4 * d + 2])}; wt[d][1] = (f32x2){bfhi(spv[4 * d]), bfhi(spv[4 * d + 2])};
        wt[d][2] = (f32x2){bflo(spv[4 * d + 1]), bflo(spv[4 * d + 3])}; wt[d][3] = (f32x2){bfhi(spv[4 * d + 1]), bfhi(spv[4 * d + 3])};
        asm volatile("" : "+s"(wt[d][0]), "+s"(wt[d][1]), "+s"(wt[d][2]), "+s"(wt[d][3]));
      }
      SCHED();
      {
        const int j = idx * 4 + pw;
        const unsigned o = (unsigned)((512 + 64 * (j >> 2) + l) * (D_ * 2) + (((160 * xcd) >> 7) + ((j >> 1) & 1)) * 128);
        __builtin_amdgcn_raw_ptr_buffer_load_lds((j & 1) ? ra : rb, (LAS void*)(lds + 98304), 4, j < 64 ? o : 0x80000000u, 0, 0, 0);
      }
      SCHED();
      gl(1, 0, 4);
      STAMP(21);
      unsigned up = (unsigned)(L_U + 512 + 256 * pw + (l >> 2) * 16 + (3 - (l & 3)) * 4);
      unsigned lfo = l == 0 ? (unsigned)(L_FLG + 4 * pw) : (unsigned)(100352 + tid * 4), lfr = (unsigned)(L_FLG + (l & 3) * 4);
      unsigned yr = (unsigned)(L_U + (n16 >> 2) * U_CH + (128 * pw + 32 * h4 + 8 * (n16 & 1) + 4 * ((n16 >> 1) & 1)) * 2);
      unsigned yo = (unsigned)(8 * pw + (l >> 3)) * 20480u + (unsigned)quad * 128u + (unsigned)(l & 7) * 16u;
      unsigned mo = (unsigned)((n16 < 12 ? n16 >> 2 : 0) * D_ + quad * 4 + (l & 3)) * 8u + (unsigned)h4 * 40960u;
      unsigned k3o = (unsigned)(16 * pw + (l >> 2)) * 10880u + (unsigned)((l & 3) * 16);
      unsigned k3d = (unsigned)(L_U + (l & 3) * U_CH + 512 + (1536 + 128 * pw + 8 * (l >> 2)) * 2);
      unsigned xfo = (unsigned)(((4 * (l >> 5) + pw) * 4 + ((l >> 3) & 3)) * 160 + quad) * 128u + (unsigned)(l & 7) * 16u;
      u32x4 tg4 = {tag, tag, tag, tag};
      asm volatile("" : "+v"(up), "+v"(lfo), "+v"(lfr), "+v"(yr), "+v"(yo), "+v"(mo), "+v"(k3o), "+v"(k3d), "+v"(xfo), "+v"(tg4));
      unsigned so = WS_ST1 + (unsigned)xcd * 16384u + (unsigned)pw * 1024u;
      asm volatile("" : "+s"(so));
      auto pf_set = [&](int i) { asm volatile("" ::: "memory"); *(volatile LAS unsigned*)(lds + lfo + 4 * i) = tg4[0]; };
      auto pf_wait = [&](int i0) {
        _Pragma("unroll 1") for (unsigned spins = 0; spins < (SPIN_CAP << 4); ++spins) {
          if (__all(*(volatile LAS unsigned*)(lds + lfr + 4 * i0) == tag)) break;
          __builtin_amdgcn_s_sleep(1);
        }
        asm volatile("" ::: "memory");
      };
      unsigned hl[2][3];
      auto halo = [&](int s) {
#pragma unroll
        for (int i = 0; i < 6; ++i) { hl[i & 1][i >> 1] = (unsigned)__builtin_amdgcn_readlane(kh[i & 1], 4 * s + (i >> 1)); asm volatile("" : "+v"(hl[i & 1][i >> 1])); }
      };
      auto kv = [&](auto S__, auto N__) {
        constexpr int s = decltype(S__)::value;
        u32x2* x = g[s];
        SCHED();
        asm volatile("s_waitcnt vmcnt(%5)" : "+v"(x[0]), "+v"(x[1]), "+v"(x[2]), "+v"(x[3]), "+v"(kh) : "n"(decltype(N__)::value));
        if (s == 2) STAMP(27);
        if (s == 0) halo(0);
        if (s < 2) {
          if (idx & 1) { asm volatile(""); const u32x2 t = x[0], u = x[2]; x[0] = x[1]; x[1] = t; x[2] = x[3]; x[3] = u; }
          asm volatile("" : "+v"(x[0]), "+v"(x[1]), "+v"(x[2]), "+v"(x[3]));
          if (idx & 2) { asm volatile(""); const u32x2 t = x[0], u = x[1]; x[0] = x[2]; x[1] = x[3]; x[2] = t; x[3] = u; }
          asm volatile("" : "+v"(x[0]), "+v"(x[1]), "+v"(x[2]), "+v"(x[3]));
        }
        f32x2 kf[2][5], o[2][2];
#pragma unroll
        for (int d = 0; d < 2; ++d) {
          unsigned kk[5];
          kk[3] = x[0][d]; kk[4] = x[1][d];
          kk[2] = __builtin_amdgcn_update_dpp(hl[d][2], kk[4], 0x138, 0xf, 0xf, false);
          kk[1] = __builtin_amdgcn_update_dpp(hl[d][1], kk[3], 0x138, 0xf, 0xf, false);
          kk[0] = __builtin_amdgcn_update_dpp(hl[d][0], kk[2], 0x138, 0xf, 0xf, false);
#pragma unroll
          for (int j = 0; j < 5; ++j) kf[d][j] = (f32x2){bflo(kk[j]), bfhi(kk[j])};
        }
#pragma unroll
        for (int t = 0; t < 5; ++t) {
#pragma unroll
          for (int i = 0; i < 4; ++i) {
            const int d = i >> 1, r = i & 1;
            if (t == 4) o[d][r] *= (f32x2){bflo(x[2 + r][d]), bfhi(x[2 + r][d])};
            else if (t == 0) o[d][r] = kf[d][r + 3] * wt[d][0];
            else o[d][r] = __builtin_elementwise_fma(kf[d][r + 3 - t], wt[d][t], o[d][r]);
          }
          asm volatile("" : "+v"(o[0][0]), "+v"(o[0][1]), "+v"(o[1][0]), "+v"(o[1][1]));
        }
#pragma unroll
        for (int i = 0; i < 4; ++i) *(unsigned*)(lds + up + i * U_CH + s * 1024) = pack2(o[i >> 1][1][i & 1], o[i >> 1][0][i & 1]);
        pf_set(4 * s);
        SCHED();
        if (s < 2) halo(s + 1);
        SCHED();
      };
      u32x4 sv[4], qv[4];
      u32x2 mv[2];
      auto ld_e = [&]() {
#pragma unroll
        for (int s = 0; s < 4; ++s) sv[s] = __builtin_amdgcn_raw_buffer_load_b128(rs, l16, so + (unsigned)s * 4096u, SC1);
        SCHED();
      };
      auto s_bad = [&]() { return __any(((ne2(sv[0][1], sv[0][3]) | ne2(sv[1][1], sv[1][3]) | ne2(sv[2][1], sv[2][3])) | ne2(sv[3][1], sv[3][3])) != 0u); };
      kv(IC<0>(), IC<5>());
      STAMP(23);
      gl(2, 0, 4);
      kv(IC<1>(), IC<4>());
      STAMPF(9);
      u32x4 k3;
      unsigned k3c;
      auto ld_k3 = [&]() {
        k3 = __builtin_amdgcn_raw_buffer_load_b128(rs, k3o, WS_KV3 + (unsigned)quad * 64u, SC1);
        k3c = __builtin_amdgcn_raw_buffer_load_b32(rs, k3o & ~63u, WS_KV3 + 10240u + (unsigned)quad * 4u, SC1);
      };
      auto ld_m = [&]() {
        mv[0] = __builtin_amdgcn_raw_buffer_load_b64(rs, mo, WS_MODQ, SC1);
        mv[1] = __builtin_amdgcn_raw_buffer_load_b64(rs, mo, WS_MODQ + 20480u, SC1);
      };
      auto k_bad = [&]() {
        unsigned d = (k3[0] + k3[1]) + (k3[2] + k3[3]);
        d += (unsigned)__builtin_amdgcn_update_dpp(0, (int)d, 0xB1, 0xf, 0xf, true);
        d += (unsigned)__builtin_amdgcn_update_dpp(0, (int)d, 0x4E, 0xf, 0xf, true);
        return __any(d + k3c != tag);
      };
      float r1[4][2], m1[4][2];
      auto ln_pre = [&]() {
#pragma unroll
        for (int i = 0; i < 8; ++i) {
          const unsigned x = sv[i >> 1][2 * (i & 1)];
          r1[i >> 1][i & 1] = __builtin_amdgcn_rcpf(bfhi(x));
          m1[i >> 1][i & 1] = -bflo(x) * r1[i >> 1][i & 1];
          asm volatile("" : "+v"(r1[i >> 1][i & 1]), "+v"(m1[i >> 1][i & 1]));
        }
      };
      unsigned qck[4], qco = (unsigned)((8 * pw + (l >> 3)) * 1024 + (quad / 40) * 256 + (quad % 40) * 4);
      asm volatile("" : "+v"(qco));
      auto ld_qc = [&]() {
#pragma unroll
        for (int s = 0; s < 4; ++s) {
          qv[s] = __builtin_amdgcn_raw_buffer_load_b128(rs, yo, WS_QB + (unsigned)s * (32u * 20480u), SC1);
          qck[s] = __builtin_amdgcn_raw_buffer_load_b32(rs, qco, WS_QCK + (unsigned)s * 32768u, SC1);
        }
        SCHED();
      };
      auto q_bad = [&]() {
        unsigned d = 0u;
#pragma unroll
        for (int s = 0; s < 4; ++s) d += (qv[s][0] + qv[s][1]) + (qv[s][2] + qv[s][3]);
        return __any(sum8u(d) + ((qck[0] + qck[1]) + (qck[2] + qck[3])) != tag * 4u);
      };
      const bool early = (unsigned)__builtin_amdgcn_s_memrealtime() - t_start < KE_T;
      ld_e();
      if (!early) hold(K_T);
      {
        const unsigned em = early ? 0x80000000u : 0u;
        k3o |= em; yo |= em; qco |= em;
        ld_k3();
        SCHED();
        ld_qc();
        k3o ^= em; yo ^= em; qco ^= em;
      }
      kv(IC<2>(), IC<14>());
      STAMPF(11);
      auto s_get = [&]() {
        if (s_bad()) {
          _Pragma("unroll 1") for (unsigned spins = 0; spins < SPIN_CAP; ++spins) {
            __builtin_amdgcn_s_sleep(4);
            ld_e();
            if (!s_bad()) break;
          }
        }
        ln_pre();
      };
      bool sok = false;
      if (early) {
        sok = !s_bad();
        hold(K_T);
        if (!sok) ld_e();
        ld_k3();
        SCHED();
        ld_qc();
        if (sok) ln_pre();
      }
      if (k_bad()) {
        _Pragma("unroll 1") for (unsigned spins = 0; spins < SPIN_CAP; ++spins) {
          __builtin_amdgcn_s_sleep(4);
          ld_k3();
          if (!k_bad()) break;
        }
      }
      *(u32x4*)(lds + k3d) = k3;
      pf_set(12);
      SCHED();
      STAMPF(18);
      ld_m();
      SCHED();
      if (!sok) s_get();
      if (q_bad()) {
        _Pragma("unroll 1") for (unsigned spins = 0; spins < SPIN_CAP; ++spins) {
          __builtin_amdgcn_s_sleep(4);
          ld_qc();
          if (!q_bad()) break;
        }
      }
      STAMPF(17);
      STAMP(2);
      u32x2 yv[4][2];
      auto ld_y = [&](int s) {
#pragma unroll
        for (int e = 0; e < 2; ++e) yv[s][e] = trd(lds + yr + 1024 * s + 32 * e);
      };
      f32x4 qf[4][2], P[4][2];
      auto xln = [&](int s) {
#pragma unroll
        for (int e = 0; e < 2; ++e) {
          const float r = r1[s][e], m = m1[s][e];
          const f32x4 qq = {bflo(qv[s][2 * e]), bfhi(qv[s][2 * e]), bflo(qv[s][2 * e + 1]), bfhi(qv[s][2 * e + 1])};
          qf[s][e] = qq;
          P[s][e] = fma4(qq, (f32x4){r, r, r, r}, (f32x4){m, m, m, m});
        }
        asm volatile("" : "+v"(qf[s][0]), "+v"(qf[s][1]), "+v"(P[s][0]), "+v"(P[s][1]));
      };
      xln(0); xln(1); xln(2); xln(3);
      pf_wait(20);
      ld_y(0); ld_y(1);
      SCHED();
      if (__any(ne2(mv[0][0], mv[1][0]) != 0u)) {
        _Pragma("unroll 1") for (unsigned spins = 0; spins < SPIN_CAP; ++spins) {
          __builtin_amdgcn_s_sleep(4);
          ld_m();
          if (!__any(ne2(mv[0][0], mv[1][0]) != 0u)) break;
        }
      }
      STAMPF(13);
      f32x4 al, be;
      {
        const float mgf = rbf(sum_rows(__uint_as_float(mv[0][1]) + __uint_as_float(mv[1][1])));
        auto rl = [&](int i) { return __uint_as_float(__builtin_amdgcn_readlane(__float_as_uint(mgf), i)); };
        const f32x4 sa = {rl(0), rl(1), rl(2), rl(3)}, ca = {rl(4), rl(5), rl(6), rl(7)}, ga = {rl(8), rl(9), rl(10), rl(11)};
        al = rbf4(ca + 1.0f) * ga;
        be = sa * ga;
        asm volatile("" : "+v"(al), "+v"(be));
      }
      auto xpre = [&](int s) {
#pragma unroll
        for (int e = 0; e < 2; ++e) P[s][e] = __builtin_elementwise_fma(P[s][e], al, be);
        asm volatile("" : "+v"(P[s][0]), "+v"(P[s][1]));
      };
      auto xfin = [&](int s) {
        u32x4 o;
#pragma unroll
        for (int e = 0; e < 2; ++e) {
          const u32x2 y = yv[s][e];
          const f32x4 yf = {bflo(y[0]), bfhi(y[0]), bflo(y[1]), bfhi(y[1])};
          const f32x4 x = __builtin_elementwise_fma(P[s][e], yf, qf[s][e]);
          o[2 * e] = pack2(x[0], x[1]);
          o[2 * e + 1] = pack2(x[2], x[3]);
        }
        ST16S(o, rs, yo, WS_X1B + (unsigned)s * (32u * 20480u), SC1);
      };
      xpre(0); xfin(0);
      xpre(1); xfin(1);
      SCHED();
      xpre(2); xpre(3);
      pf_wait(24);
      ld_y(2);
      xfin(2);
      SCHED();
      pf_wait(28);
      ld_y(3);
      xfin(3);
      STAMP(28);
      if (l == 0) lf[40 + pw] = tag;
      asm volatile("s_waitcnt vmcnt(2)" ::: "memory");
      ST16S(tg4, rs, xfo, WS_FX, SC1);
      SCHED();
      asm volatile("s_waitcnt vmcnt(1)" ::: "memory");
      ST16S(tg4, rs, xfo, WS_FX + 32u * 160u * 128u, SC1);
      STAMP(5);
    } else {
      const int ch = w;
      char* U = lds + L_U + ch * U_CH;
      char* G = lds + ch * (4 * G_COPY);
      {
        const rsrc_t rf = __builtin_amdgcn_make_buffer_rsrc((void*)(p.ssm_k + (size_t)(quad * 4 + ch) * S_), 0, S_ * 2, 0x00020000);
        u32x4 fa[4];
        u32x2 fb[4];
        __builtin_amdgcn_s_setprio(3);
#pragma unroll
        for (int k = 0; k < 4; ++k) {
          fa[k] = __builtin_amdgcn_raw_buffer_load_b128(rf, l16 + (unsigned)k * 1024u, 0, NT);
        }
        __builtin_amdgcn_s_setprio(1);
        TAG_READY();
#pragma unroll
        for (int k = 0; k < 4; ++k) {
#pragma unroll
          for (int d = 0; d < 2; ++d)
            fb[k][d] = __builtin_amdgcn_update_dpp(k < 3 ? (unsigned)__builtin_amdgcn_readfirstlane(fa[k < 3 ? k + 1 : k][d]) : 0u, fa[k][d], 0x130, 0xf, 0xf, false);
          const unsigned x[6] = {fa[k][0], fa[k][1], fa[k][2], fa[k][3], fb[k][0], fb[k][1]};
          unsigned y[5];
#pragma unroll
          for (int d = 0; d < 5; ++d) y[d] = __builtin_amdgcn_alignbit(x[d + 1], x[d], 16);
          char* gp = G + 32 + k * 1024 + l16;
          *(u32x4*)(gp) = fa[k];
          *(u32x4*)(gp + G_COPY) = (u32x4){y[0], y[1], y[2], y[3]};
          *(u32x4*)(gp + 2 * G_COPY) = (u32x4){x[1], x[2], x[3], x[4]};
          *(u32x4*)(gp + 3 * G_COPY) = (u32x4){y[1], y[2], y[3], y[4]};
          if (k == 0) {
            const unsigned f0 = __builtin_amdgcn_readfirstlane(x[0]), f1 = __builtin_amdgcn_readfirstlane(x[1]);
            const unsigned long long f64 = (unsigned long long)f0 | ((unsigned long long)f1 << 32);
            const unsigned long long hi = l ? (f64 << (64 - 16 * (l & 3))) : 0ull;
            if (l < 4) {
              *(u32x4*)(G + l * G_COPY) = (u32x4){0u, 0u, 0u, 0u};
              *(u32x4*)(G + l * G_COPY + 16) = (u32x4){0u, 0u, (unsigned)hi, (unsigned)(hi >> 32)};
            }
            if (l >= 32) *(u32x4*)(U + (l - 32) * 16) = (u32x4){0u, 0u, 0u, 0u};
          }
        }
        asm volatile("s_waitcnt lgkmcnt(0)" ::: "memory");
        STAMP(1);
      }
      const int a = n16, h = h4, jj = h >> 1, e2 = h & 1;
      const char* ap = G + ((a + 1) & 3) * G_COPY + 2 * (16 * jj + a - 8 * e2 + 9 - ((a + 1) & 3));
      unsigned bp = (unsigned)(L_U + ch * U_CH + 32 * (a - jj) + 16 * e2);
      auto lda = [&](u32x4* A, int r, int p0, int np) {
#pragma unroll
        for (int pp = 0; pp < np; ++pp) {
          u32x2 lo = *(const volatile LAS u32x2*)(ap + 512 * (p0 + pp) + 64 * r);
          u32x2 hi = *(const volatile LAS u32x2*)(ap + 512 * (p0 + pp) + 64 * r + 8);
          A[pp] = (u32x4){lo[0], lo[1], hi[0], hi[1]};
        }
      };
      u32x4 AD;
      {
        const unsigned dDr = (*(cptr_t)(p.ssm_D + quad * 4 + (ch & 2)) >> (16 * (ch & 1))) & 0xffffu;
        const unsigned dv = h == (a >> 3) ? ((a & 1) ? dDr : dDr << 16) : 0u;
        const int dw = (7 - (a & 7)) >> 1;
#pragma unroll
        for (int d = 0; d < 4; ++d) AD[d] = dw == d ? dv : 0u;
      }
      u32x4 A0[8][4];
      f32x4 acc[8];
#pragma unroll
      for (int n = 0; n < 8; ++n) acc[n] = (f32x4){0.f, 0.f, 0.f, 0.f};
#pragma unroll
      for (int r = 0; r < 8; ++r) lda(A0[r], r, 0, 4);
      auto yput = [&](int s) {
#pragma unroll
        for (int e = 0; e < 2; ++e)
          *(u32x2*)(U + 1024 * s + 512 * e + (16 * a + 4 * h) * 2) = (u32x2){pack2(acc[2 * s + e][0], acc[2 * s + e][1]), pack2(acc[2 * s + e][2], acc[2 * s + e][3])};
        asm volatile("" ::: "memory");
        if (l == 0) lf[16 + 4 * s + ch] = tag;
      };
      asm volatile("" : "+v"(AD), "+v"(bp), "+v"(acc[0]), "+v"(acc[1]), "+v"(acc[2]), "+v"(acc[3]), "+v"(acc[4]), "+v"(acc[5]), "+v"(acc[6]), "+v"(acc[7]));
      auto stage = [&](auto S__) {
        constexpr int s = decltype(S__)::value, NH = 4 - 2 * s, NP = 8 - 2 * s;
        u32x2 A1[3][NH][2];
        u32x4 B[2][2];
        auto lda1 = [&](int r, int i) { A1[r % 3][i >> 1][i & 1] = *(const volatile LAS u32x2*)(ap + 512 * (4 + (i >> 1)) + 64 * r + 8 * (i & 1)); };
        auto ldb = [&](int r, int e) { B[r & 1][e] = *(const u32x4*)(lds + bp + 512 * (2 * s + e + 1) - 64 * r); };
#pragma unroll
        for (int i = 0; i < 4 * NH; ++i) lda1(i / (2 * NH), i % (2 * NH));
        SCHED();
        if (s > 0) yput(s - 1);
        lf_wait(4 * s, 4);
        if (s == 0) STAMP(3);
#pragma unroll
        for (int i = 0; i < 4; ++i) ldb(i >> 1, i & 1);
        SCHED();
#pragma unroll
        for (int e = 0; e < 2; ++e) MFMA_IP(acc[2 * s + e], AD, B[0][e]);
        sfor<0, 8 * (2 * NP - 1)>([&](auto I_) __attribute__((always_inline)) {
          constexpr int r = decltype(I_)::value / (2 * NP - 1), i = decltype(I_)::value % (2 * NP - 1), e = i >= NP, pp = i - e * NP, h1 = pp < 4 ? 0 : pp - 4;
          const u32x4 a1 = {A1[r % 3][h1][0][0], A1[r % 3][h1][0][1], A1[r % 3][h1][1][0], A1[r % 3][h1][1][1]};
          const u32x4 fa = pp < 4 ? A0[r][pp & 3] : a1;
          MFMA_IP(acc[2 * s + e + pp], fa, B[r & 1][e]);
          SCHED();
          if (r + 2 < 8 && i == 2 * NP - 3) ldb(r + 2, 0);
          if (r + 2 < 8 && i == 2 * NP - 2) ldb(r + 2, 1);
          if (r + 2 < 8 && i < 2 * NH) lda1(r + 2, i % (2 * NH));
          SCHED();
        });
      };
      stage(IC<0>());
      STAMP(4);
      stage(IC<1>());
      STAMPF(9);
      u32x2 fv;
      {
        u32x4 B[8][2];
        auto ldb = [&](int r, int m0) {
#pragma unroll
          for (int m = 0; m < 2; ++m) B[r][m] = *(const u32x4*)(lds + bp + 512 * (m0 + m + 1) - 64 * r);
        };
        asm volatile("s_nop 1" : "+v"(acc[2]), "+v"(acc[3]));
        yput(1);
        if (w == 3) wo_ld();
        __builtin_amdgcn_s_setprio(3);
        lf_wait(8, 4);
        STAMP(22);
        auto ldb1 = [&](int i, int m0) { B[i >> 1][i & 1] = *(const u32x4*)(lds + bp + 512 * (m0 + (i & 1) + 1) - 64 * (i >> 1)); };
#pragma unroll
        for (int r = 0; r < 3; ++r) ldb(r, 4);
        SCHED();
#pragma unroll
        for (int m = 0; m < 2; ++m) MFMA_IP(acc[4 + m], AD, B[0][m]);
        sfor<0, 24>([&](auto I_) __attribute__((always_inline)) {
          constexpr int i = decltype(I_)::value, r = i / 3, k = i % 3;
          MFMA_IP(acc[4 + (k != 1)], A0[r][k == 0], B[r][k == 2]);
          SCHED();
          if (i < 10) ldb1(6 + i, 4);
          SCHED();
        });
        sfor<0, 32>([&](auto I_) __attribute__((always_inline)) {
          constexpr int i = decltype(I_)::value, r = i >> 2, k = i & 3;
          MFMA_IP(acc[6 + (k & 1)], A0[r][2 + (k & 1) - (k >> 1)], B[r][k >> 1]);
          SCHED();
          if (i == 7) yput(2);
          if (i == 20) fv = ld8(rs, b2o);
          if (i == 19) lf_wait(12, 4);
          if (i >= 19 && i < 29) ldb1(i - 19, 6);
          SCHED();
        });
#pragma unroll
        for (int m = 0; m < 2; ++m) MFMA_IP(acc[6 + m], AD, B[0][m]);
        sfor<0, 24>([&](auto I_) __attribute__((always_inline)) {
          constexpr int i = decltype(I_)::value, r = i / 3, k = i % 3;
          MFMA_IP(acc[6 + (k != 1)], A0[r][k == 0], B[r][k == 2]);
          SCHED();
          if (i < 6) ldb1(10 + i, 6);
          SCHED();
        });
        asm volatile("s_nop 7" : "+v"(acc[6]), "+v"(acc[7]));
        yput(3);
      }
      STAMP(28);
      __builtin_amdgcn_s_setprio(0);
      b2_wait(fv, 0);
      lf_wait(20, 4);
      DRAIN();
    }
    __builtin_amdgcn_s_setprio(0);
  } else {
    const int at = idx - 20;
    char* const scr = lds + WBLK + 14848;
    auto gv_blk = [&](int j) { return j < 160 ? 30 + (j >> 3) : j < 400 ? (j >> 3) - 20 : j >> 3; };
    auto gv_fin = [&](const u32x4* pre, int j) {
      float acc[8];
      gemv_fin(acc, pre, 10, scr);
      gemv_put(acc, j < 160 ? WS_MODP + (unsigned)((j & 7) * 1280 + (j >> 3) * 64) * 8u : WS_MODQ + (unsigned)((j & 7) * 2560 + ((j >> 3) - 20) * 64) * 8u);
    };
    const int ja = xcd * 12 + at, ai = xcd * 8 + at, bi = xcd * 4 + at - 8, g = w >> 2, wj = w & 3;
    const int rj = w == 0 ? ja : w == 1 ? (at >= 4 && at < 12 ? 92 + ai : -1) : w == 4 ? 160 + ja : w == 5 && at < 8 ? 256 + ai : -1;
    char* const rst = lds + (g ? WBLK : 65536) + wj * 7424;
    u32x4 pre[11];
    if (at >= 8 || g == 0) {
      TOUCH_AT(2);
      const int T = at >= 8 ? 64 + 2 * bi + g : ai;
      unsigned* mds = (unsigned*)(lds + 2048) + 16 * g;
      char* qt = lds + L_RT + g * (16 * 1312);
      u32x4 ld[5];
      const bool e1 = at >= 8 && w == 3;
      if (!e1) __builtin_amdgcn_s_sleep(8);
      auto q_ld = [&](auto N_) {
        asm volatile("; %0" :: "n"(decltype(N_)::value) : "memory");
#pragma unroll
        for (int j = 0; j < 5; ++j)
          ld[j] = __builtin_amdgcn_raw_buffer_load_b128(rq, (unsigned)(h4 * D_ * 2 + n16 * 16 + j * 256), (unsigned)((32 * (T >> 1) + 8 * wj + 4 * (T & 1)) * D_ * 2), NT);
        SCHED();
      };
      const int g2 = at < 8 || w < 2 || w == 4 || w == 3 ? 400 : w < 4 ? 384 + bi : 256 + wj * 32 + bi;
      SCHED();
      float sa = 0.f, va = 0.f;
      auto q_in = [&]() {
        const bf2_t ONE2 = {(__bf16)1.0f, (__bf16)1.0f};
        char* dst = qt + (4 * wj + h4) * 1312 + n16 * 16;
        SCHED();
#pragma unroll
        for (int j = 0; j < 5; ++j) {
          *(volatile LAS u32x4*)(dst + j * 256) = ld[j];
#pragma unroll
          for (int d = 0; d < 4; ++d) {
            const unsigned a0 = ld[j][d];
            const bf2_t xa = __builtin_bit_cast(bf2_t, a0);
            sa = __builtin_amdgcn_fdot2_f32_bf16(xa, ONE2, sa, false); va = __builtin_amdgcn_fdot2_f32_bf16(xa, xa, va, false);
          }
        }
      };
      u32x4 pre2[11];
      if (e1) {
        gemv_load(pre, gv_blk(128 + bi), bi & 7, 10);
        q_ld(IC<3>());
        TAG_READY();
        gv_fin(pre, 128 + bi);
        q_in();
        asm volatile("; d" : "+v"(sa), "+v"(va));
      } else if (g2 < 400) {
        q_ld(IC<0>());
        gemv_load(pre2, gv_blk(g2), g2 & 7, 10);
        q_in();
        asm volatile("; a" : "+v"(sa), "+v"(va));
      } else {
        q_ld(IC<2>());
        q_in();
        asm volatile("; c" : "+v"(sa), "+v"(va));
      }
      TAG_READY();
      {
        lf_set(8 + w);
        const float ma = sum16(sa) * (1.0f / D_);
        va = __builtin_fmaxf(sum16(va) * (1.0f / D_) - ma * ma, 0.f);
        if (n16 == 0) mds[4 * wj + h4] = pack2(ma, __builtin_amdgcn_sqrtf(rbf(rbf(va) + 1e-5f)));
      }
      lf_set(16 + w);
      lf_wait(8 + 4 * g, 4);
      STAMP(23);
      {
        const char* src = qt + (l & 7) * 1312 + (40 * wj + (l >> 3)) * 8;
        unsigned ckw = 0u;
        u32x2 qx[5][2];
#pragma unroll
        for (int j = 0; j < 5; ++j) { qx[j][0] = *(const volatile LAS u32x2*)(src + j * 64); qx[j][1] = *(const volatile LAS u32x2*)(src + j * 64 + 8 * 1312); }
        SCHED();
        if (wj == 2) {
          lf_wait(16 + 4 * g, 4);
          st16(rs, WS_ST1 + (unsigned)(l >> 3) * 16384u + (unsigned)T * 128u + (unsigned)(l & 7) * 16u, (u32x4){mds[l & 7], tag, mds[8 + (l & 7)], tag});
          STAMP(22);
        }
#pragma unroll
        for (int j = 0; j < 5; ++j) {
          const u32x2 x = qx[j][0], y = qx[j][1];
          st16(rs, WS_QB + (unsigned)T * 20480u + (unsigned)(5 * wj + j) * 1024u + l16, (u32x4){x[0], x[1], y[0], y[1]});
          const unsigned t = tag - sum8u((x[0] + x[1]) + (y[0] + y[1]));
          ckw = (l & 7) == j ? t : ckw;
        }
        char* sc = lds + 110592 + w * 256;
        *(volatile LAS unsigned*)(sc + ((l & 7) * 8 + (l >> 3)) * 4) = ckw;
        const u32x4 x = *(const volatile LAS u32x4*)(sc + n16 * 16);
        if (l < 16) st16(rs, WS_QCK + (unsigned)(T * 4 + wj) * 256u + l16, x);
      }
      SCHED();
      if (g2 >= 400 && w < 2 && rj >= 0 && rj < 160) rt_ld(rj, rst);
      if (w >= 4 && w < 7) lf_set(w);
      if (g2 < 400) gv_fin(pre2, g2);
      STAMP(25);
    } else {
      constexpr int KB = L_RT + 16 * 1312;
      TOUCH_AT(3);
      const int g1 = w < 6 ? 2 * ai + wj : 32 + wj * 64 + ai;
      if (w >= 6) {
        const rsrc_t rk = __builtin_amdgcn_make_buffer_rsrc((void*)(w == 6 ? p.k : p.v), 0, S_ * D_ * 2, 0x00020000);
        const unsigned ko = (unsigned)((1536 + 8 * ai) * (D_ * 2) - (w == 6 ? 4096 : 0));
        char* dst = lds + KB + (w == 6 ? 0 : 14336);
        __builtin_amdgcn_s_sleep(3);
#pragma unroll
        for (int n = 0; n < 10; ++n) __builtin_amdgcn_raw_ptr_buffer_load_lds(rk, (LAS void*)(dst + n * 1024), 16, l16, ko + (unsigned)n * 1024u, 0, NT);
        if (w == 6) {
#pragma unroll
          for (int n = 10; n < 14; ++n) __builtin_amdgcn_raw_ptr_buffer_load_lds(rk, (LAS void*)(dst + n * 1024), 16, l16, ko + (unsigned)n * 1024u, 0, NT);
        }
      }
      SCHED();
      u32x4 swt[2];
      swt[0] = *(const u32x4*)(p.shift_k + (tid - 256) * 8);
      swt[1] = *(const u32x4*)(p.shift_k + (256 + l) * 8);
      SCHED();
      if (w >= 6) __builtin_amdgcn_s_sleep(8);
      gemv_load(pre, gv_blk(g1), g1 & 7, 10);
      TAG_READY();
      asm volatile("s_waitcnt vmcnt(11)" : "+v"(swt[0]), "+v"(swt[1]) :: "memory");
      lf_set(8 + w);
      if (w < 6) gv_fin(pre, g1);
      STAMP(25);
      lf_wait(12, 4);
      STAMP(23);
#pragma unroll 1
      for (int n = 0; n < (w == 7 ? 2 : 1); ++n) {
        {
          const int pr = n ? 256 + l : tid - 256;
          const char* kp = lds + KB + 4096 + pr * 4;
          unsigned kk[11], vv[8];
#pragma unroll
          for (int i = 0; i < 11; ++i) kk[i] = *(const unsigned*)(kp + (i - 3) * 1280);
#pragma unroll
          for (int i = 0; i < 8; ++i) vv[i] = *(const unsigned*)(kp + 10240 + i * 1280);
          const u32x4 sw = n ? swt[1] : swt[0];
          const f32x2 wt[4] = {{bflo(sw[0]), bflo(sw[2])}, {bfhi(sw[0]), bfhi(sw[2])}, {bflo(sw[1]), bflo(sw[3])}, {bfhi(sw[1]), bfhi(sw[3])}};
          f32x2 o[8];
#pragma unroll
          for (int i = 0; i < 8; ++i) {
            f32x2 t = (f32x2){bflo(kk[i + 3]), bfhi(kk[i + 3])} * wt[0];
#pragma unroll
            for (int j = 1; j < 4; ++j) t = __builtin_elementwise_fma((f32x2){bflo(kk[i + 3 - j]), bfhi(kk[i + 3 - j])}, wt[j], t);
            o[i] = t * (f32x2){bflo(vv[i]), bfhi(vv[i])};
          }
          char* sc = lds + 98304 + wj * 2304;
          unsigned t = 0u;
#pragma unroll
          for (int e = 0; e < 2; ++e) {
            const u32x4 x = {pack2(o[7][e], o[6][e]), pack2(o[5][e], o[4][e]), pack2(o[3][e], o[2][e]), pack2(o[1][e], o[0][e])};
            *(volatile LAS u32x4*)(sc + l * 32 + e * 16) = x;
            t += (x[0] + x[1]) + (x[2] + x[3]);
          }
          *(volatile LAS unsigned*)(sc + 2048 + (l >> 1) * 4 + (l & 1) * 128) = tag - t - (unsigned)__builtin_amdgcn_update_dpp(0, (int)t, 0xB1, 0xf, 0xf, true);
          u32x4 x[3];
#pragma unroll
          for (int e = 0; e < 3; ++e) x[e] = *(const volatile LAS u32x4*)(sc + e * 1024 + (e < 2 ? l : l & 7) * 16);
          const unsigned ko = WS_KV3 + (unsigned)ai * 10880u;
#pragma unroll
          for (int e = 0; e < 2; ++e) st16(rs, ko + (unsigned)((n ? 4 : wj) * 2048 + e * 1024) + l16, x[e]);
          if (l < 8) st16(rs, ko + (unsigned)(10240 + (n ? 4 : wj) * 128) + l16, x[2]);
        }
      }
      STAMP(22);
      if (w < 7) lf_set(w);
      if (w >= 6) gv_fin(pre, g1);
      STAMPF(30);
    }
    if (w == 6 && ja < 80) {
      hold(AUX_T);
      gemv_load(pre, gv_blk(400 + ja), ja & 7, 10);
      gv_fin(pre, 400 + ja);
    }
    if (rj >= 0) {
      if (rj >= 160) { hold(RT2_T); rt_ld(rj, rst); }
      rt_job(rj, rst);
    }
    STAMP(26);
    lf_set(w);
    if (w == 3) { hold(WA_T); wo_ld(); }
    STAMP(3);
    if (w < 4) {
      hold(W_T);
      b2_wait((u32x2){~tag, 0u}, 1);
      lf_wait(0, 8);
      STAMP(4);
    }
    DRAIN();
  }

  STAMPF(16);
  u32x4 wox[5];
  if (w == 3) {
    const unsigned bp = (unsigned)(122880 + (4 * h4 + (n16 >> 2)) * 160 + (n16 & 3) * 8);
    u32x2 x[10];
    asm volatile("ds_read_b64_tr_b16 %0, %10\n\tds_read_b64_tr_b16 %1, %10 offset:2560\n\tds_read_b64_tr_b16 %2, %10 offset:32\n\tds_read_b64_tr_b16 %3, %10 offset:2592\n\t"
                 "ds_read_b64_tr_b16 %4, %10 offset:64\n\tds_read_b64_tr_b16 %5, %10 offset:2624\n\tds_read_b64_tr_b16 %6, %10 offset:96\n\tds_read_b64_tr_b16 %7, %10 offset:2656\n\t"
                 "ds_read_b64_tr_b16 %8, %10 offset:128\n\tds_read_b64_tr_b16 %9, %10 offset:2688\n\ts_waitcnt lgkmcnt(0)"
                 : "=&v"(x[0]), "=&v"(x[1]), "=&v"(x[2]), "=&v"(x[3]), "=&v"(x[4]), "=&v"(x[5]), "=&v"(x[6]), "=&v"(x[7]), "=&v"(x[8]), "=&v"(x[9]) : "v"(bp) : "memory");
#pragma unroll
    for (int dj = 0; dj < 5; ++dj) wox[dj] = (u32x4){x[2 * dj][0], x[2 * dj][1], x[2 * dj + 1][0], x[2 * dj + 1][1]};
  }
  auto wo_put = [&]() {
    if (w == 3 && R < 28) {
      const unsigned base = WS_WOUTF + (unsigned)c * WBLK + (unsigned)((R >> 1) * 10240 + (R & 1) * 1024);
#pragma unroll
      for (int dj = 0; dj < 5; ++dj) st16(rs, base + (unsigned)((R & 1) ? dj : (dj + 2) % 5) * 2048u + l16, wox[dj]);
    }
  };
  if (cs2) {
    cs_ld(csv, (8 + c) * 7 + R - 20);
    SCHED();
    load_b2(0, 12);
    SCHED();
    cs_fin(csv, (8 + c) * 7 + R - 20);
  } else if (w < 4) {
    asm volatile("" ::: "memory");
    if (!is_conv) load_b2(0, 12);
  }
  const unsigned xfb = WS_FX + (unsigned)(((R >> 1) * 4 + (R & 1) * 2 + (c >> 2)) * 160 + l) * 128u;
  unsigned xf3[3];
  if (w == 7) {
    lf_wait(36, 4);
    int dt = (int)(lf[44 + ((l & 31) < 20 ? l & 31 : 0)] - t_start);
    dt = l < 32 ? dt : -dt;
    dt = max(dt, __builtin_amdgcn_update_dpp(0, dt, 0xB1, 0xf, 0xf, true));
    dt = max(dt, __builtin_amdgcn_update_dpp(0, dt, 0x4E, 0xf, 0xf, true));
    dt = max(dt, __builtin_amdgcn_update_dpp(0, dt, 0x124, 0xf, 0xf, true));
    dt = max(dt, __builtin_amdgcn_update_dpp(0, dt, 0x128, 0xf, 0xf, true));
    const int d1 = min(max(max(__builtin_amdgcn_readlane(dt, 0), __builtin_amdgcn_readlane(dt, 16)), 0), 300);
    const int d0 = min(max(max(__builtin_amdgcn_readlane(dt, 32), __builtin_amdgcn_readlane(dt, 48)), 0), 300);
    dt = ((d1 + d0) >> 1) - d0;
    STAMPF(24);
    hold((unsigned)(YG_T + dt));
    STAMPF(30);
    xf3[0] = ld4(rs, xfb); xf3[1] = ld4(rs, xfb + 8192u); xf3[2] = ld4(rs, l < 32 ? xfb + 16384u : ~0u);
  }
  SCHED();
  u32x4 k_c;
  unsigned kc_o = w < 2 ? (unsigned)(tid < 112 ? tid : 0) * 16u : 0x80000000u, kc_d = (unsigned)(L_CONST + (tid & 127) * 8);
  auto kc_ld = [&](int z2) { k_c = __builtin_amdgcn_raw_buffer_load_b128(rs, kc_o, WS_CS + (unsigned)(8 * z2 + c) * 2048u, SC1); };
  auto consts_put = [&](int z2) {
    if (w < 2) {
      if (__any(ne2(k_c[0], k_c[2]) != 0u)) {
        _Pragma("unroll 1") for (unsigned spins = 0; spins < SPIN_CAP; ++spins) {
          __builtin_amdgcn_s_sleep(8);
          kc_ld(z2);
          if (!__any(ne2(k_c[0], k_c[2]) != 0u)) break;
        }
      }
      if (z2) *(u32x2*)(lds + kc_d + 1024) = (u32x2){k_c[1], k_c[3]};
      else { const unsigned d = kc_d - ((kc_d >> 1) & 4u); *(unsigned*)(lds + d) = k_c[1]; *(unsigned*)(lds + d + 8) = k_c[3]; }
    }
  };
  const int rt1 = w & 3, kh1 = w >> 2;
  unsigned bo0 = (unsigned)(kh1 * 71680) + l16, bo1 = bo0 + 35840u;
  asm volatile("" : "+v"(bo0), "+v"(bo1));
  unsigned pso[3], pqo[3], oto = (unsigned)(((w ^ 4) * 7 * 64 + l) * 8), rdo = (unsigned)((w * 7 * 64 + l) * 8), cso = (unsigned)(L_CONST + (128 + n16) * 8);
#pragma unroll
  for (int e = 0; e < 3; ++e) {
    const int row = rt1 * 16 + (e < 2 ? 4 * h4 + 2 * kh1 + e : n16);
    pso[e] = (unsigned)(L_PST + row * 4); pqo[e] = (unsigned)(L_PST + 512 + row * 16 + (row & 3) * 4);
  }
  unsigned spo = (unsigned)(L_Z + tid * 16), p6o = (unsigned)(L_PST + (w * 16 + 4 * h4) * 4), pgo = (unsigned)(L_PST + 512 + (w * 16 + n16) * 16);
  asm volatile("" : "+v"(spo), "+v"(p6o), "+v"(pgo));
  p6o = n16 == 12 ? p6o : spo;
  pgo = h4 == (n16 >> 2) ? pgo : spo;
  unsigned bsrc = (unsigned)(((l >> 1) & 3) * 64 + (l & 1) * 4), ppo = l < 8 ? l16 : 0x80000000u;
  asm volatile("" : "+v"(pso[0]), "+v"(pso[2]), "+v"(pqo[0]), "+v"(pqo[2]), "+v"(oto), "+v"(rdo), "+v"(cso), "+v"(p6o), "+v"(pgo), "+v"(bsrc), "+v"(ppo), "+v"(kc_o), "+v"(kc_d));
  pso[1] = pso[0] + 4u; pqo[1] = pqo[0] + 20u;
#define BFRAG(q, j) (*(const u32x4*)(lds + ((q) < 5 ? bo0 : bo1) + ((q) % 5) * 7168 + (j) * 1024))
  u32x4 h2[10];
  unsigned xv = (unsigned)((4 * ((l >> 3) & 1) + h4) * 128 + (l & 7) * 16), xs0 = WS_X1B + (unsigned)(R * 4 + rt1) * 20480u + (unsigned)(10 * kh1) * 1024u;
  unsigned wvo = wsp_vo, bso = (unsigned)tau(rt1, n16) * 2u, fgo = l < 32 ? (unsigned)(l >> 3) * F4S + (unsigned)(l & 7) * 16u : 0x80000000u;
  asm volatile("" : "+v"(xv), "+v"(wvo), "+v"(bso), "+v"(fgo));
#define WO3 (-92)
#define PINS(x) do { x = (unsigned)__builtin_amdgcn_readfirstlane((int)x); asm volatile("" : "+s"(x)); } while (0)
  PINS(xs0);
  unsigned zs[8], wx8, wx4, wgb, fzs, uo, ui, xs, wl2, os, zos, pos, s1, s3;
#define LDX(q) h2[q] = __builtin_amdgcn_raw_buffer_load_b128(rs, xv + (unsigned)((q) & 3) * 1024u, xs0 + (unsigned)((q) >> 2) * 4096u, SC1)
  auto flag4 = [&](unsigned off) { ST16S(((u32x4){tag, tag, tag, tag}), rs, fgo, WS_F4 - WS_FWI + off, SC1); };
  unsigned bs_raw, fwo[1];
  f32x4 acc[7];
  float mu_n, rs_n;
  {
    unsigned f1o[1] = {w == 1 && l < 20 ? fwi + (unsigned)(c * 20 + l) * 128u : ~0u}, f1v;
    fwo[0] = w == 3 && l < 28 ? fwi + (WS_FWO - WS_FWI) + (unsigned)(c * 32 + l) * 128u : ~0u;
    asm volatile("" : "+v"(f1o[0]), "+v"(fwo[0]));
    if (w < 4) {
      if (is_conv) {
        while (!__all(lf[40 + (l & 3)] == tag) && (unsigned)__builtin_amdgcn_s_memrealtime() - t_start < G_T) __builtin_amdgcn_s_sleep(1);
        load_b2(0, 12);
      }
      load_b2(12, 35);
    }
    wo_put();
    STAMPF(29);
    if (w == 7) {
      asm volatile("" : "+v"(xf3[0]), "+v"(xf3[1]), "+v"(xf3[2]));
      if (__any(ne2(xf3[0], xf3[1]) != 0u) || __builtin_amdgcn_ballot_w64(xf3[2] == tag) != 0xffffffffull) {
        const unsigned fo[3] = {xf3[0] != tag ? xfb : ~0u, xf3[1] != tag ? xfb + 8192u : ~0u, l < 32 && xf3[2] != tag ? xfb + 16384u : ~0u};
        wait_lines<3, 2>(rs, fo, tag);
      }
      if (l == 0) lf[33] = tag;
    }
    lf_wait(33, 1);
    STAMP(7);
    SCHED();
#pragma unroll
    for (int q = 0; q < 3; ++q) LDX(q);
    SCHED();
    __builtin_amdgcn_s_waitcnt(0x0F73);
    BAR();
    STAMP(8);
#pragma unroll
    for (int j = 0; j < 7; ++j) acc[j] = (f32x4){0.f, 0.f, 0.f, 0.f};
    f32x4 accg = {0.f, 0.f, 0.f, 0.f};
    u32x4 bf[7];
#pragma unroll
    for (int j = 0; j < 7; ++j) bf[j] = BFRAG(0, j);
    SCHED();
#pragma unroll
    for (int q = 3; q < 6; ++q) LDX(q);
    if (w == 3) flag4(WS_FWO + (unsigned)(c * 32 + R) * 128u);
    SCHED();
#pragma unroll
    for (int t = 0; t < 8; ++t) { zs[t] = WS_Z2F + (unsigned)(c * 64 + 8 * w + 4 * ((t >> 2) ^ rotg) + ((t + rotb) & 3)) * 7168u; PINS(zs[t]); }
    wx8 = (unsigned)((w + (idx >> 2)) & 7) * 1024u; wx4 = wx8 & 3072u; wgb = WS_WOUTF + (unsigned)c * WBLK; fzs = fz + (unsigned)(c * 32 + 4 * w) * 128u;
    uo = WS_UF + (unsigned)((R * 4 + rt1) * 8 + c) * 4096u + (unsigned)kh1 * 2048u; ui = WS_UF + (unsigned)((R * 4 + rt1) * 8 + 4 * kh1) * 4096u;
    xs = WS_X1B + (unsigned)(R * 4 + rt1) * 20480u + (unsigned)(5 * c + 2 * kh1) * 512u; wl2 = (unsigned)(120 + WO3 + (w < 4 ? w + 16 : w)) * 1024u;
    os = (unsigned)(((R * 64 + tau(rt1, 0)) * D_ + 80 * c + 32 * kh1) * 2);
    zos = WS_Z2F + (unsigned)((c * 64 + 4 * (R >> 1)) * 7 + (w < 6 ? w + 1 : 0)) * 1024u + (unsigned)(R & 1) * 512u; pos = WS_PART + (unsigned)(R * 64 + w) * 128u;
    s1 = (unsigned)((w ^ 4) * 3072 + 1024); s3 = (unsigned)(w * 3072 + 1024);
    PINS(wx8); PINS(wx4); PINS(wgb); PINS(fzs); PINS(uo); PINS(ui); PINS(xs); PINS(wl2); PINS(os); PINS(zos); PINS(pos); PINS(s1); PINS(s3);
    SCHED();
    auto xswap = [&](int q, int e) {
#pragma unroll
      for (int d = 0; d < 2; ++d) {
        if (e == 1) h2[q][2 + d] ^= (unsigned)__builtin_amdgcn_update_dpp(0, (int)h2[q][d], 0x128, 0xf, 0x3, false);
        else h2[q][d] ^= (unsigned)__builtin_amdgcn_update_dpp(0, (int)h2[q][2 + d], 0x128, 0xf, 0xC, false);
      }
      asm volatile("" : "+v"(h2[q]));
    };
#pragma unroll
    for (int q = 0; q < 10; ++q) {
      if (q == 0) { xswap(q, 0); xswap(q, 1); xswap(q, 2); }
      if (q == 5 && w == 1 && __builtin_amdgcn_ballot_w64(f1v == tag) != 0xfffffull) wait_lines<1, 4>(rs, f1o, tag);
      if (q == 5) { __builtin_amdgcn_s_barrier(); asm volatile("" ::: "memory"); }
      SCHED();
      accg = MFMA16(h2[q], h2[q], accg);
      SCHED();
#pragma unroll
      for (int j = 0; j < 7; ++j) {
        acc[j] = MFMA16(h2[q], bf[j], acc[j]);
        if (q == 9 && j == 1) *(f32x4*)(lds + pgo) = accg;
        if (q < 9) bf[j] = BFRAG(q + 1, j);
        if (q < 9 && j >= 3 && j < 6) xswap(q + 1, j - 3);
        if (q == 1 && j == 6) f1v = ld4(rs, f1o[0]);
        if (q == 2 && j == 0) bs_raw = __builtin_amdgcn_raw_buffer_load_b16(__builtin_amdgcn_make_buffer_rsrc((void*)p.b_sp, 0, S_ * 2, 0x00020000), bso, (unsigned)R * 128u, 0);
        if (q >= 2 && j == 2) LDW(q - 2);
        if ((j == 0 || j == 4) && 6 + 2 * q + (j >> 2) < 10) LDX(6 + 2 * q + (j >> 2));
        if (q == 4 && j == 0) kc_ld(1);
        if (q == 2 && j == 1 && cs1) cs_ld(csv, c * 7 + R - 25);
        if (q >= 5 && j == 5) {
          const int i = (5 + q) * 7 + (w < 7 ? w : 0);
          dma1k(rs, lds + i * 1024, l16, WS_WINF + (unsigned)c * WBLK + (unsigned)i * 1024u);
          if (q == 9) { const int i2 = 28 + (w < 7 ? w : 0); dma1k(rs, lds + i2 * 1024, l16, WS_WINF + (unsigned)c * WBLK + (unsigned)i2 * 1024u); }
        }
        SCHED();
      }
    }
    STAMP(31);
  }
  unsigned sro, swo, zo, zro, po;
  auto gelu_n = [&](f32x2* x, auto N_, auto&& fill) {
    constexpr int N = decltype(N_)::value;
    const float c0 = -2.0f * 0.7978845608028654f * 1.4426950408889634f, c1 = c0 * 0.044715f;
    f32x2 k0 = {c0, c0}, k1 = {c1, c1}, k2 = {1.0f, 1.0f};
    asm volatile("" : "+v"(k0), "+v"(k1), "+v"(k2));
    f32x2 t[N];
#define GST(e) do { _Pragma("unroll") for (int i = 0; i < N; ++i) { t[i] = (e); asm volatile("" : "+v"(t[i])); } } while (0)
    GST(x[i] * x[i]);
    GST(__builtin_elementwise_fma(t[i], k1, k0));
    fill(IC<0>());
    GST(t[i] * x[i]);
    fill(IC<1>());
    GST(((f32x2){__builtin_amdgcn_exp2f(t[i][0]), __builtin_amdgcn_exp2f(t[i][1])}));
    fill(IC<2>());
    GST(t[i] + k2);
    fill(IC<3>());
    GST(((f32x2){__builtin_amdgcn_rcpf(t[i][0]), __builtin_amdgcn_rcpf(t[i][1])}));
    fill(IC<4>());
#pragma unroll
    for (int i = 0; i < N; ++i) { x[i] *= t[i]; asm volatile("" : "+v"(x[i])); }
  };
  {
    f32x2 mine[7];
    f32x2 csb[7];
    if (kh1) {
#pragma unroll
      for (int j = 0; j < 7; ++j) { *(f32x2*)(lds + rdo + j * 512) = (f32x2){acc[j][0], acc[j][1]}; mine[j] = (f32x2){acc[j][2], acc[j][3]}; }
      *(f32x4*)(lds + p6o) = acc[6];
    } else {
#pragma unroll
      for (int j = 0; j < 7; ++j) { *(f32x2*)(lds + rdo + j * 512) = (f32x2){acc[j][2], acc[j][3]}; mine[j] = (f32x2){acc[j][0], acc[j][1]}; }
      *(f32x4*)(lds + p6o) = acc[6];
      consts_put(1);
      if (cs1) cs_fin(csv, c * 7 + R - 25);
#pragma unroll
      for (int k = 8; k < 12; ++k) LDW(k);
    }
    BAR();
    auto b1p = [&](int i) { dma1k(rs, lds + i * 1024, l16, WS_WINF + (unsigned)c * WBLK + (unsigned)i * 1024u); };
    if (kh1) {
#pragma unroll
      for (int k = 8; k < 12; ++k) LDW(k);
    }
    __builtin_amdgcn_s_setprio(3);
    STAMPT(9);
    f32x2 oth[7];
    f32x2 pa[3], pb[3];
#pragma unroll
    for (int e = 0; e < 3; ++e) {
      pa[e] = (f32x2){*(const float*)(lds + pso[e]), *(const float*)(lds + pqo[e])};
      pb[e] = (f32x2){*(const float*)(lds + pso[e] + 256), *(const float*)(lds + pqo[e] + 1024)};
    }
#pragma unroll
    for (int j = 0; j < 7; ++j) { oth[j] = *(const f32x2*)(lds + oto + j * 512); csb[j] = *(const f32x2*)(lds + cso + 128 * j); }
    SCHED();
    float mu2[2], rs2[2];
#pragma unroll
    for (int e = 0; e < 3; ++e) {
      const float mean = (pa[e][0] + pb[e][0]) * (1.0f / D_);
      const float var = __builtin_fmaxf((pa[e][1] + pb[e][1]) * (1.0f / D_) - mean * mean, 0.f);
      const float rstd = __builtin_amdgcn_rsqf(var + 1e-5f);
      if (e < 2) { mu2[e] = mean; rs2[e] = rstd; } else { mu_n = mean; rs_n = rstd; }
    }
    f32x2 zz[7];
    const f32x2 nmu = {-mu2[0], -mu2[1]}, rsv = {rs2[0], rs2[1]};
#pragma unroll
    for (int j = 0; j < 7; ++j)
      zz[j] = __builtin_elementwise_fma(__builtin_elementwise_fma(nmu, (f32x2){csb[j][0], csb[j][0]}, mine[j] + oth[j]), rsv, (f32x2){csb[j][1], csb[j][1]});
    gelu_n(zz, IC<7>(), [&](auto K_) __attribute__((always_inline)) { constexpr int k = decltype(K_)::value; b1p(8 * k + w + (k < 4 || w < 3 ? 35 : 70)); SCHED(); });
    if (n16 >= 12) zz[6] = (f32x2){0.f, 0.f};
    f32x2 s1 = ((zz[0] + zz[1]) + (zz[2] + zz[3])) + ((zz[4] + zz[5]) + zz[6]);
    f32x2 s2 = zz[0] * zz[0], s3 = zz[1] * zz[1];
#pragma unroll
    for (int j = 2; j < 7; ++j) { if (j & 1) s3 = __builtin_elementwise_fma(zz[j], zz[j], s3); else s2 = __builtin_elementwise_fma(zz[j], zz[j], s2); }
    s2 += s3;
    asm("" : "+v"(s1), "+v"(s2));
    float a0 = s1[0] + dpp_f<0xB1>(s1[0]), a1 = s1[1] + dpp_f<0xB1>(s1[1]), b0 = s2[0] + dpp_f<0xB1>(s2[0]);
    a0 = (l & 1) ? a1 : a0;
    const float b1 = s2[1] + dpp_f<0xB1>(s2[1]);
    b0 = (l & 1) ? b1 : b0;
    a0 += dpp_f<0x4E>(a0); b0 += dpp_f<0x4E>(b0);
    a0 += dpp_f<0x124>(a0); b0 += dpp_f<0x124>(b0);
    a0 += dpp_f<0x128>(a0); b0 += dpp_f<0x128>(b0);
    {
      const unsigned pa0 = __builtin_amdgcn_ds_bpermute(bsrc, __float_as_uint(a0)), pb0 = __builtin_amdgcn_ds_bpermute(bsrc, __float_as_uint(b0));
      ST16S(((u32x4){tag, pa0, tag, pb0}), rs, ppo, pos + (unsigned)c * 1024u, SC1);
    }
    SCHED();
    __builtin_amdgcn_s_setprio(0);
    STAMP(10);
    if (!kh1) {
#pragma unroll
      for (int k = 10; k < 18; ++k) b1p(4 * k + (w & 3) + 70 - (k == 17 ? (w & 2) : 0));
    }
#pragma unroll
    for (int k = 12; k < 16; ++k) LDW(k);
#pragma unroll
    for (int j = 0; j < 7; ++j)
      *(unsigned*)(lds + L_Z + (((rt1 >> 1) * 7 + j) * 64 + l) * 16 + (4 * (rt1 & 1) + 2 * kh1) * 2) = pack2(zz[j][0], zz[j][1]);
    SCHED();
    sfor<0, 8>([&](auto X_) __attribute__((always_inline)) { WSP_TX(decltype(X_)::value); SCHED(); });
    sro = (unsigned)(L_ST2 + h4 * 16);
    swo = (unsigned)(L_ST2 + (16 * rt1 + 4 * ((l >> 1) & 3) + 2 * kh1 + (l & 1)) * 4);
    zo = w < 7 ? (unsigned)(h4 * 7168 + n16 * 16) : 0x80000000u;
    zro = (unsigned)(L_Z + (w < 7 ? w : 0) * 1024) + l16;
    po = (unsigned)((l >> 3) * 1024 + (l & 7) * 16);
    asm volatile("" : "+v"(sro), "+v"(swo), "+v"(zo), "+v"(zro), "+v"(po));
    asm volatile("s_waitcnt vmcnt(4)" ::: "memory");
    BAR();
    STAMPT(11);
  }
  unsigned z1d[7];
  constexpr int L_RED = 28672;
#define WOA(i) (((i) < 28 ? (i) : (i) < 108 ? (i) + 32 : (i) < 120 ? (i) + 37 : (i) - 92) * 1024)
#define RBAR() do { __builtin_amdgcn_s_barrier(); asm volatile("" ::: "memory"); } while (0)
#define WOD(i0, x) dma1k(rs, lds + WOA(i0) + (x), l16, wgb + (unsigned)(i0) * 1024u + (x))
  constexpr int NW = 14;
  u32x4 zw[NW];
#define LDZ(f) do { if (((f) >> 3) == 4) { zs[(f) & 7] += 4096u; asm volatile("" : "+s"(zs[(f) & 7])); } \
    zw[(f) % NW] = __builtin_amdgcn_raw_buffer_load_b128(rs, l16 + (unsigned)(((f) >> 3) & 3) * 1024u, zs[(f) & 7], SC1); } while (0)
  {
    f32x4 acc[7];
#pragma unroll
    for (int j = 0; j < 7; ++j) acc[j] = (f32x4){0.f, 0.f, 0.f, 0.f};
    u32x4 bf[7];
#pragma unroll
    for (int j = 0; j < 7; ++j) bf[j] = BFRAG(4, j);
    u32x4 pv, zf[2];
    unsigned fwv;
    auto ld_pv = [&]() { pv = __builtin_amdgcn_raw_buffer_load_b128(rs, po, pos, SC1); };
    auto z1 = [&](auto Q0_, auto Q1_) __attribute__((always_inline)) {
#pragma unroll
      for (int qi = decltype(Q0_)::value; qi < decltype(Q1_)::value; ++qi) {
        const int q = (qi + 4) % 10, qn = (qi + 5) % 10;
        SCHED();
#pragma unroll
        for (int j = 0; j < 7; ++j) {
          acc[j] = MFMA16(bf[j], h2[q], acc[j]);
          if (qi < 9 && qi != 4) bf[j] = BFRAG(qn, j);
          if (qi < 2 && (j == 1 || j == 4)) { const int i = (2 * qi + (j >> 2)) * 7 + (w < 7 ? w : 0); dma1k(rs, lds + i * 1024, l16, WS_WINF + (unsigned)c * WBLK + (unsigned)i * 1024u); }
          if (qi == 2 && j == 0) kc_ld(0);
          if (qi == 2 && j == 3) fwv = ld4(rs, fwo[0]);
          if (qi == 3 && j == 1) ld_pv();
          if (qi == 4 && j < 2) zf[j] = *(const u32x4*)(lds + zro + j * 7168);
          SCHED();
        }
      }
    };
    auto req1 = [&]() {
#pragma unroll
      for (int k = 16; k < 24; ++k) { LDW(k); SCHED(); }
    };
    auto req2 = [&]() {
#pragma unroll
      for (int k = 24; k < 32; ++k) { LDW(k); SCHED(); }
      sfor<0, 2>([&](auto G_) __attribute__((always_inline)) { WOD(68 + 8 * decltype(G_)::value, wx8); SCHED(); });
    };
    if (kh1) req1();
    z1(IC<0>(), IC<5>());
    STAMPT(29);
    f32x2 zv[2][4];
#pragma unroll
    for (int i = 0; i < 8; ++i) { zv[i >> 2][i & 3] = (f32x2){bflo(zf[i >> 2][i & 3]), bfhi(zf[i >> 2][i & 3])}; asm volatile("" : "+v"(zv[i >> 2][i & 3])); }
    __builtin_amdgcn_s_setprio(3);
    if (__any(ne2(pv[0], pv[2]) != 0u)) {
      _Pragma("unroll 1") for (unsigned spins = 0; spins < SPIN_CAP; ++spins) {
        ld_pv();
        if (!__any(ne2(pv[0], pv[2]) != 0u)) break;
        __builtin_amdgcn_s_sleep(1);
      }
    }
    {
      float t1 = __uint_as_float(pv[1]), t2 = __uint_as_float(pv[3]);
      t1 += dpp_f<0x128>(t1); t2 += dpp_f<0x128>(t2);
      auto x = __builtin_amdgcn_permlane16_swap(__float_as_uint(t1), __float_as_uint(t2), false, false);
      const unsigned y = __float_as_uint(__uint_as_float(x[0]) + __uint_as_float(x[1]));
      auto z = __builtin_amdgcn_permlane32_swap(y, y, false, false);
      const unsigned u = __float_as_uint(__uint_as_float(z[0]) + __uint_as_float(z[1]));
      auto v = __builtin_amdgcn_permlane16_swap(u, u, false, false);
      const float mean = __uint_as_float(v[0]) * (1.0f / FH);
      const float var = __builtin_fmaxf(__uint_as_float(v[1]) * (1.0f / FH) - mean * mean, 0.f);
      const float rstd = __builtin_amdgcn_rsqf(var + 1e-5f);
      *(float*)(lds + swo) = rstd;
      *(float*)(lds + swo + 256) = -mean * rstd;
    }
    SCHED();
    consts_put(0);
    if (w == 3 && __builtin_amdgcn_ballot_w64(fwv == tag) != 0xfffffffull) wait_lines<1, 1>(rs, fwo, tag);
    if (!kh1) req1();
    BAR();
    STAMP(12);
    {
      f32x4 sa[2][2], sb[2][2];
#pragma unroll
      for (int i = 0; i < 4; ++i) { sa[i >> 1][i & 1] = *(const f32x4*)(lds + sro + i * 64); sb[i >> 1][i & 1] = *(const f32x4*)(lds + sro + i * 64 + 256); }
      SCHED();
#pragma unroll
      for (int m = 0; m < 2; ++m) {
        u32x4 o;
#pragma unroll
        for (int e = 0; e < 4; ++e) {
          const f32x4 a = sa[m][e >> 1], b = sb[m][e >> 1];
          const f32x2 x = __builtin_elementwise_fma(zv[m][e], (f32x2){a[2 * (e & 1)], a[2 * (e & 1) + 1]}, (f32x2){b[2 * (e & 1)], b[2 * (e & 1) + 1]});
          o[e] = pack2(x[0], x[1]);
        }
        ST16S(o, rs, zo + (unsigned)m * 256u, zos, SC1);
      }
    }
    SCHED();
    __builtin_amdgcn_s_setprio(0);
    STAMPT(13);
#pragma unroll
    for (int j = 0; j < 7; ++j) bf[j] = BFRAG(9, j);
    if (kh1) req2();
    z1(IC<5>(), IC<10>());
    f32x2 mine[7];
    if (kh1) {
#pragma unroll
      for (int j = 0; j < 7; ++j) { *(f32x2*)(lds + rdo + L_RED + j * 512) = (f32x2){acc[j][0], acc[j][1]}; mine[j] = (f32x2){acc[j][2], acc[j][3]}; }
    } else {
#pragma unroll
      for (int j = 0; j < 7; ++j) { *(f32x2*)(lds + rdo + L_RED + j * 512) = (f32x2){acc[j][2], acc[j][3]}; mine[j] = (f32x2){acc[j][0], acc[j][1]}; }
    }
    if (!kh1) req2();
    asm volatile("s_waitcnt vmcnt(10)" ::: "memory");
    BAR();
    if (w == 3) flag4(WS_FZ + (unsigned)(c * 32 + R) * 128u);
    STAMP(6);
    auto wo8 = [&](auto K_) __attribute__((always_inline)) {
      constexpr int k = decltype(K_)::value;
      if (k < 12) WOD(k < 3 ? 8 * k : k < 8 ? 8 * k + 4 : 8 * k + 20, wx8);
      else if (k == 12) dma1k(rs, lds + (kh1 ? WOA(116) : WOA(24)) + wx4, l16, wgb + (kh1 ? 116u : 24u) * 1024u + wx4);
      SCHED();
    };
    wo8(IC<0>()); wo8(IC<1>());
    u32x4 gv[8];
    auto gb_ld = [&]() {
#pragma unroll
      for (int k = 0; k < 8; ++k) gv[k] = __builtin_amdgcn_raw_buffer_load_b128(rs, l < 40 ? l16 : 0u, WS_MODQ + (unsigned)(k * 2560 + 1920 + 80 * c) * 8u, SC1);
    };
    if (w == 2) gb_ld();
    f32x2 z[7];
    {
      f32x2 oth[7];
      f32x4 cc[7];
#pragma unroll
      for (int j = 0; j < 7; ++j) {
        oth[j] = *(const f32x2*)(lds + oto + L_RED + j * 512);
        cc[j] = *(const f32x4*)(lds + L_CONST + (16 * j + 4 * h4 + 2 * kh1) * 8);
      }
      SCHED();
#pragma unroll
      for (int j = 0; j < 7; ++j)
        z[j] = __builtin_elementwise_fma(__builtin_elementwise_fma((f32x2){-mu_n, -mu_n}, (f32x2){cc[j][0], cc[j][1]}, oth[j] + mine[j]),
                                         (f32x2){rs_n, rs_n}, (f32x2){cc[j][2], cc[j][3]});
    }
#pragma unroll
    for (int j = 0; j < 7; ++j) asm volatile("" : "+v"(z[j]));
    wo8(IC<2>());
    gelu_n(z, IC<7>(), [&](auto K_) __attribute__((always_inline)) { wo8(IC<3 + decltype(K_)::value>()); });
#pragma unroll
    for (int j = 0; j < 7; ++j) z1d[j] = pack2(z[j][0], z[j][1]);
    asm volatile("" : "+v"(z1d[0]), "+v"(z1d[1]), "+v"(z1d[2]), "+v"(z1d[3]), "+v"(z1d[4]), "+v"(z1d[5]), "+v"(z1d[6]));
    SCHED();
    STAMPT(30);
    if (w == 2) {
      auto bad = [&]() {
        unsigned d = 0u;
#pragma unroll
        for (int k = 0; k < 8; ++k) d |= ne2(gv[k][0], gv[k][2]);
        return __any(d != 0u);
      };
      if (bad()) {
        _Pragma("unroll 1") for (unsigned spins = 0; spins < SPIN_CAP; ++spins) {
          __builtin_amdgcn_s_sleep(8);
          gb_ld();
          if (!bad()) break;
        }
      }
      f32x2 t[4];
#pragma unroll
      for (int k = 0; k < 4; ++k) t[k] = (f32x2){__uint_as_float(gv[2 * k][1]) + __uint_as_float(gv[2 * k + 1][1]), __uint_as_float(gv[2 * k][3]) + __uint_as_float(gv[2 * k + 1][3])};
      *(f32x2*)(lds + L_MISC + l * 8) = rbf2((t[0] + t[1]) + (t[2] + t[3]));
    }
    const unsigned fo[1] = {l < 4 ? fzs + (unsigned)l * 128u : ~0u};
    unsigned fv;
    sfor<8, 32>([&](auto X_) __attribute__((always_inline)) {
      constexpr int x = decltype(X_)::value;
      wo8(IC<x>());
      if (x == 12) { fv = ld4(rs, fo[0]); SCHED(); }
      if (x == 18) {
        STAMP(14);
        if (__builtin_amdgcn_ballot_w64(fv == tag) != 15ull) wait_lines<1, 1>(rs, fo, tag);
        asm volatile("" ::: "memory");
        STAMPT(23);
      }
      if (x >= 18) { LDZ((x - 18) & 15); SCHED(); }
      WSP_TX(x);
      SCHED();
    });
  }

  {
    SCHED();
    STAMP(20);
    const int kh = kh1;
    const unsigned rw = (unsigned)(L_RED + w * 512);
    unsigned rr = (unsigned)(L_RED + w * 4096 + l * 4);
    asm volatile("; m0 is set here, not in front of the first store (one wait state)" : "+v"(rr) : "{m0}"(rw));
    const float bs = bflo(bs_raw);
    unsigned long long hm = 0xffff000000000000ull;
    asm volatile("" : "+s"(hm));
    unsigned l8;
    f32x4 acc2[2][4];
    float rv[8][2];
    u32x4 ud;
    u32x4 P[2][8];
    u32x2 xp[3];
    auto ld_p = [&](int pt, int i) { P[pt][i] = __builtin_amdgcn_raw_buffer_load_b128(rs, l16 + (unsigned)pt * 1024u, ui + (unsigned)i * 2048u, SC1); };
#define RED_PUT1(j, i) asm volatile("ds_write_addtid_b32 %0 offset:%2" :: "v"(acc2[(j) & 1][((i) >> 2) & 3][(i) & 3]), "{m0}"(rw), "n"((((i) >> 2) & 3) * 4096 + (((i) >> 1) & 1) * 16384 + ((i) & 1) * 256) : "memory")
#define RED_PUT(j, r) do { RED_PUT1(j, 4 * (r)); RED_PUT1(j, 4 * (r) + 1); RED_PUT1(j, 4 * (r) + 2); RED_PUT1(j, 4 * (r) + 3); } while (0)
#define RED_GET(i) do { f32x2 x_; asm volatile("ds_read2st64_b32 %0, %1 offset0:%2 offset1:%3" : "=v"(x_) : "v"(rr), "n"(2 * (i)), "n"(2 * (i) + 1) : "memory"); rv[i][0] = x_[0]; rv[i][1] = x_[1]; } while (0)
    float zz[2], gg[2];
    unsigned cs;
    auto sum_rows_u = [&](unsigned v) {
      auto a = __builtin_amdgcn_permlane16_swap(v, v, false, false);
      v = a[0] + a[1];
      auto b = __builtin_amdgcn_permlane32_swap(v, v, false, false);
      return b[0] + b[1];
    };
    auto red_fin = [&](int j, int k, bool fuse = false) {
      const int e = k & 1, i = k >> 1;
#define VADD(d, x, y) asm volatile("v_add_f32 %0, %1, %2" : "=v"(d) : "v"(x), "v"(y))
      if (i < 4) VADD(rv[2 * i][e], rv[2 * i][e], rv[2 * i + 1][e]);
      if (i == 4 || i == 5) VADD(rv[4 * (i & 1)][e], rv[4 * (i & 1)][e], rv[4 * (i & 1) + 2][e]);
      if (i == 6) VADD(rv[0][e], rv[0][e], bs);
      if (i == 7) VADD(gg[e], rv[0][e], rv[4][e]);
      if (i == (fuse ? 9 : 8)) zz[e] = e ? bfhi(z1d[(j + 6) % 7]) : bflo(z1d[(j + 6) % 7]);
      if (i == 9) zz[e] *= gg[e];
      if (i == 9 || (i == 8 && !fuse)) asm volatile("" : "+v"(zz[e]));
      if (k == 20) { ud[j & 3] = pack2(zz[0], zz[1]); asm volatile("" : "+v"(ud[j & 3])); }
      if (k == 21) {
        if (j == 0) asm volatile("v_cndmask_b32 %0, %0, 0, %1" : "+v"(ud[0]) : "s"(hm));
        if (j == 2) asm volatile("v_add3_u32 %0, %1, %2, %3" : "=v"(cs) : "v"(ud[0]), "v"(ud[1]), "v"(ud[2]));
        if (j == 3) {
          cs = tag - sum_rows_u(cs + ud[3]);
          asm volatile("v_cndmask_b32 %0, %0, %1, %2" : "+v"(ud[0]) : "v"(cs), "s"(hm));
        }
        if (j >= 4) { ud[3] = (j == 4 ? tag : ud[3]) - ud[j & 3]; asm volatile("" : "+v"(ud[3])); }
        if (j % 3 == 0 && j) ST16S(ud, rs, l16 + (unsigned)(j / 6) * 1024u, uo, SC1);
      }
    };
    struct Sch { int b1, b2, wp[16], rp[8], vp[22], lp[8], pr[32]; };
    constexpr Sch S = {18, 28, {0, 1, 2, 3, 4, 5, 6, 7, 8, 9, 10, 11, 12, 12, 15, 15}, {19, 19, 20, 22, 23, 23, 24, 24}, {29, 29, 30, 30, 31, 31, 32, 33, 35, 36, 37, 38, 39, 40, 41, 42, 43, 46, 43, 46, 48, 48},
                       {1, 1, 1, 1, 1, 1, 1, 1}, {0, 0, 2, 1, 0, 0, 0, 0, 0, 0, 2, 2, 0, 0, 0, 0, 0, 0, 0, 1, 0, 0, 0, 1, 0, 0, 2, 1, 0, 0, 0, 0}};
    constexpr Sch F = {15, 23, {0, 1, 1, 2, 3, 4, 4, 5, 6, 7, 8, 9, 10, 11, 12, 12}, {15, 16, 16, 17, 17, 18, 18, 19}, {23, 23, 24, 24, 25, 26, 26, 27, 27, 28, 28, 29, 30, 30, 31, 31, 22, 22, 32, 33, 34, 35},
                       {1, 2, 1, 1, 1, 1, 1, 1}, {0, 2, 1, 0, 0, 1, 1, 0, 0, 2, 1, 0, 0, 2, 1, 0, 0, 2, 1, 0, 0, 2, 1, 0, 0, 2, 1, 0, 0, 2, 1, 0}};
    unsigned wb[3], xo;
    const unsigned wl[3] = {(unsigned)(120 + WO3 + w) * 1024u, (unsigned)(128 + WO3 + w) * 1024u, wl2}, wg = wgb - (unsigned)(WO3 * 1024);
    constexpr int NAF = 32, NA0 = 16;
    u32x4 af[NAF];
#define LDA(m) af[(m) % NAF] = *(const u32x4*)(lds + wb[WOA(2 * (m)) >> 16] + (WOA(2 * (m)) & 65535))
    unsigned ck[2];
    const unsigned tag4 = tag * 4u;
    auto p_chk = [&](int pt, int pr, int k) {
      u32x4* q = P[pt] + 4 * pr;
      if (k < 2) ck[k & 1] = q[k & 1][0] + q[k & 1][1];
      else if (k < 8) ck[k & 1] = ck[k & 1] + q[(k & 1) + 2 * ((k >> 2) & 1)][k & 2] + q[(k & 1) + 2 * ((k >> 2) & 1)][(k & 2) + 1];
      else if (k == 8) ck[0] = pt ? ck[0] + ck[1] : sum_rows_u(ck[0] + ck[1]);
      if (k < 9) asm volatile("" : "+v"(ck[k & 1]));
      if (k == 9) {
        if (__builtin_expect(__any(ck[0] != tag4), 0)) {
          _Pragma("unroll 1") for (unsigned spins = 0; spins < SPIN_CAP; ++spins) {
            unsigned d = 0u;
#pragma unroll
            for (int i = 0; i < 4; ++i) ld_p(pt, 4 * pr + i);
#pragma unroll
            for (int i = 0; i < 4; ++i) d += q[i][0] + q[i][1] + q[i][2] + q[i][3];
            if (!pt) d = sum_rows_u(d);
            if (!__any(d != tag4)) break;
            __builtin_amdgcn_s_sleep(1);
          }
        }
      }
      if (k >= 10 && !pt) { unsigned x = q[(k - 10) & 3][0]; asm volatile("v_cndmask_b32 %0, %0, 0, %1" : "+v"(x) : "s"(hm)); q[(k - 10) & 3][0] = x; }
      if (k >= 10 && k < 13 && pt) { q[(k - 10) % 3][3] = q[3][(k - 10) % 3]; asm volatile("" : "+v"(q[(k - 10) % 3])); }
    };
    sfor<0, 224>([&](auto M_) __attribute__((always_inline)) {
      constexpr int mm = decltype(M_)::value, j = mm >> 5, m = mm & 31, y = m - 20;
      constexpr int t = j == 6 && y >= 0 ? 5 + (y % 6) / 2 : m >> 2, r = j == 6 && y >= 0 ? (y & 1) + 2 * (y / 6) : m & 3, f = 8 * j + t;
      constexpr Sch C = j < 4 ? S : F, B = j <= 4 ? S : F;
      if (t == 0) asm volatile("v_mfma_f32_16x16x32_bf16 %0, %1, %2, 0" : "=&v"(acc2[j & 1][r]) : "v"(zw[f % NW]), "v"(wsp[r][t]), "v"(zw[(f | (f < 8 ? 1 : 3)) % NW]));
      else asm volatile("v_mfma_f32_16x16x32_bf16 %0, %1, %2, %0" : "+v"(acc2[j & 1][r]) : "v"(zw[f % NW]), "v"(wsp[r][t]), "v"(zw[(f | (f < 8 ? 1 : 3)) % NW]));
      SCHED();
      if (C.pr[m] == 1) __builtin_amdgcn_s_setprio(1);
      if ((m == C.b1 && j > 0) || (m == C.b2 && j > 0) || mm == 30) { asm volatile("" ::: "memory"); __builtin_amdgcn_s_waitcnt(0xC07F); RBAR(); SCHED(); }
      sfor<0, 16>([&](auto I_) __attribute__((always_inline)) {
        constexpr int i = decltype(I_)::value;
        if (j > 0 && C.wp[i] == m) { RED_PUT1(j + 1, i); SCHED(); }
        if (j > 0 && i < 8 && C.rp[i & 7] == m) { RED_GET(i & 7); SCHED(); }
      });
      if (j == 5 && m == 20) {
#pragma unroll
        for (int i = 0; i < 3; ++i) { wb[i] = (unsigned)(kh * 1024 + i * 65536) + l16; asm volatile("" : "+v"(wb[i])); }
      }
      if (j == 5 && m == 13) {
        asm volatile("v_lshrrev_b32 %0, 1, %1" : "=v"(l8) : "v"(l16));
        xo = (l8 & 0x180u) + (unsigned)psi((l8 >> 3) & 15) * 8u;
        asm volatile("" : "+v"(xo));
      }
      if (j == 6 && m >= 28) { RED_PUT1(6, 2 * (m - 28)); RED_PUT1(6, 2 * (m - 28) + 1); SCHED(); }
      constexpr int ap[NA0] = {9, 10, 11, 13, 13, 14, 14, 19, 20, 21, 22, 23, 24, 25, 26, 27};
      sfor<0, NA0>([&](auto N_) __attribute__((always_inline)) { if (j == 6 && ap[decltype(N_)::value] == m) { LDA(decltype(N_)::value); SCHED(); } });
      sfor<0, 22>([&](auto K_) __attribute__((always_inline)) {
        constexpr int k = decltype(K_)::value;
        if (j > 1 && B.vp[k] == m + 32) { red_fin(j > 1 ? j - 2 : 0, k, B.vp[16] == B.vp[18]); SCHED(); }
        if (j > 0 && C.vp[k] == m) { red_fin(j > 0 ? j - 1 : 0, k); SCHED(); }
      });
      if (C.pr[m] == 2) __builtin_amdgcn_s_setprio(0);
      if ((m & 3) == C.lp[m >> 2]) {
        if (f > 0 && f - 1 + NW < 56) LDZ(f - 1 + NW);
        if (j == 6 && m >= 12) ld_p(0, ((m >> 2) - 3) & 7);
        if (j == 5 && m >= 20) xp[((m >> 2) - 5) % 3] = __builtin_amdgcn_raw_buffer_load_b64(rs, xo + (unsigned)((m >> 2) - 5) * 512u, xs, SC1);
        SCHED();
      }
    });
    STAMP(15);
    f32x4 acc[5];
    f32x4 xf[3], gf[3];
    unsigned go, oo, eo, ho, tw, tr, o1, ta, tb;
    const unsigned s2 = (unsigned)(L_MISC + 128 * kh), s4 = s1 - (unsigned)kh * 1024u;
    unsigned long long om = kh ? ~0ull : 0xccccccccccccccccull;
#define PIN2(a, b) asm volatile("" : "+v"(a), "+v"(b))
    auto glue = [&](int k) {
      if (k == 0) { ta = l8 >> 5; tb = l16 & 48u; PIN2(ta, tb); }
      if (k == 1) { tr = __umul24(ta, 112u) + tb; oo = ta & 12u; PIN2(tr, oo); }
      if (k == 2) { tr += s1; oo += ta; PIN2(tr, oo); }
      if (k == 3) { oo = __umul24(oo, 1280u) + tb; ta = l8 >> 4; PIN2(oo, ta); }
      if (k == 4) { ta &= 24u; tb = l16 & 0xf0u; PIN2(ta, tb); }
      if (k == 5) { tw = __umul24(tb, 7u) + ta; go = (ta << 1) + s2; PIN2(tw, go); }
      if (k == 6) { tw += s1; asm volatile("v_cndmask_b32 %0, %1, -2.0, %2" : "=v"(o1) : "v"(oo), "s"(om)); asm volatile("" : "+v"(tw)); }
      if (k == 7) { eo = l16 + s3; ho = l16 + s4; PIN2(eo, ho); }
      if (k >= 8 && k < 14) {
        const unsigned x = xp[((k - 8) >> 1) % 3][k & 1];
        float lo = bflo(x), hi = bfhi(x);
        PIN2(lo, hi);
        xf[((k - 8) >> 1) % 3][2 * (k & 1)] = lo; xf[((k - 8) >> 1) % 3][2 * (k & 1) + 1] = hi;
      }
    };
    auto g3 = [&](auto M0_, auto M1_) __attribute__((always_inline)) {
      sfor<decltype(M0_)::value, decltype(M1_)::value>([&](auto M_) __attribute__((always_inline)) {
        constexpr int mi = decltype(M_)::value;
        constexpr int m = mi < 55 ? mi : mi < 64 ? 55 + 5 * ((mi - 55) / 3) + (mi - 55) % 3 : 58 + 5 * ((mi - 64) / 2) + (mi & 1), u2 = m / 5;
        const u32x4 fb = u2 < 8 ? P[0][u2 & 7] : P[1][u2 < 11 ? (u2 - 8) & 3 : (u2 - 7) & 7];
        if (m < 5) asm volatile("v_mfma_f32_16x16x32_bf16 %0, %1, %2, 0" : "=&v"(acc[m % 5]) : "v"(af[m % NAF]), "v"(fb));
        else asm volatile("v_mfma_f32_16x16x32_bf16 %0, %1, %2, %0" : "+v"(acc[m % 5]) : "v"(af[m % NAF]), "v"(fb));
        SCHED();
        if (mi == 0) RBAR();
        if (mi < 3) dma1k(rs, lds + wl[mi % 3], l16, wg + wl[mi % 3]);
        if (mi >= 5 && mi < 19) p_chk(0, 1, mi - 5);
        if (mi >= 19 && mi < 33) glue(mi - 19);
        if (mi >= 47 && mi < 54) { p_chk(1, 1, 2 * (mi - 47)); SCHED(); p_chk(1, 1, 2 * (mi - 47) + 1); }
        if (mi < 40) LDA(NA0 + mi);
        if (mi >= 66 && mi < 69) *(f32x4*)(lds + eo + (mi - 66) * 1024) = acc[(mi - 66) % 3];
        SCHED();
      });
    };
    __builtin_amdgcn_s_setprio(3);
#pragma unroll
    for (int k = 18; k < 22; ++k) red_fin(5, k);
    asm volatile("s_nop 2");
    RED_PUT(6, 2); RED_PUT(6, 3);
    red_fin(6, 16); red_fin(6, 17);
    SCHED();
    ld_p(0, 5); ld_p(0, 6);
    SCHED();
    BAR();
    sfor<0, 8>([&](auto I_) __attribute__((always_inline)) { RED_GET(decltype(I_)::value); });
    SCHED();
    ld_p(0, 7);
    SCHED();
    sfor<0, 20>([&](auto K_) __attribute__((always_inline)) {
      constexpr int k = decltype(K_)::value;
      if (k < 8 && !(k & 1)) asm volatile("s_waitcnt lgkmcnt(%0)" :: "n"(6 - k) : "memory");
      red_fin(6, k < 16 ? k : k + 2);
    });
    SCHED();
    __builtin_amdgcn_s_setprio(0);
    STAMPT(16);
#pragma unroll
    for (int k = 0; k < 14; ++k) p_chk(0, 0, k);
    SCHED();
    g3(IC<0>(), IC<24>());
    STAMPT(22);
    DRAIN();
    RBAR();
    STAMPT(27);
    if (!kh) {
#pragma unroll
      for (int i = 0; i < 8; ++i) { ld_p(1, i); SCHED(); }
    }
    g3(IC<24>(), IC<36>());
    if (kh) {
#pragma unroll
      for (int i = 0; i < 8; ++i) { ld_p(1, i); SCHED(); }
    }
    STAMPT(26);
    g3(IC<36>(), IC<40>());
    STAMPT(24);
#pragma unroll
    for (int m = NA0 + 40; m < 70; ++m) LDA(m);
#pragma unroll
    for (int x = 0; x < 3; ++x) gf[x] = *(const f32x4*)(lds + go + x * 64);
    SCHED();
    const rsrc_t ro = __builtin_amdgcn_make_buffer_rsrc((void*)p.out, 0, S_ * D_ * 2, 0x00020000);
    STAMPT(17);
#pragma unroll
    for (int k = 0; k < 14; ++k) p_chk(1, 0, k);
    asm volatile("s_nop 1");
    STAMPT(25);
    g3(IC<40>(), IC<70>());
    STAMPT(18);
    BAR();
    STAMPT(28);
    f32x4 o4[3];
    u32x4 ov[2];
#pragma unroll
    for (int j = 2; j >= 0; --j) o4[j] = *(const volatile LAS f32x4*)(lds + ho + j * 1024);
    asm volatile("" : "+v"(acc[2]), "+v"(acc[3]), "+v"(acc[4]));
    SCHED();
#pragma unroll
    for (int j = 2; j >= 0; --j) {
      const f32x4 v = __builtin_elementwise_fma(gf[j], o4[j] + acc[2 + j], xf[j]);
      *(volatile LAS u32x2*)(lds + tw + j * 32) = (u32x2){pack2(v[0], v[1]), pack2(v[2], v[3])};
      if (j < 2) ov[j] = *(const volatile LAS u32x4*)(lds + tr + j * 32);
    }
    ST16S(ov[1], ro, oo + 32u, os, 0);
    ST16S(ov[0], ro, o1, os, 0);
    if (bid == 0 && w == 5 && l8 == 0u) st4(rs, WS_EPOCH, tag);
  }
  STAMP(19);
}

extern "C" __device__ __attribute__((used, noinline, visibility("hidden"))) void mega_tail() {}

extern "C" void kernel_launch(void* const* d_in, const int* in_sizes, int n_in, void* d_out, int out_size,
                              void* d_ws, size_t ws_size, hipStream_t stream) {
  Params p;
  p.q = (const u16*)d_in[0];
  p.k = (const u16*)d_in[1];
  p.v = (const u16*)d_in[2];
  p.shift_k = (const u16*)d_in[3];
  p.ssm_k = (const u16*)d_in[4];
  p.ssm_D = (const u16*)d_in[5];
  p.w_in = (const u16*)d_in[7];
  p.w_sp = (const u16*)d_in[8];
  p.b_sp = (const u16*)d_in[9];
  p.w_out = (const u16*)d_in[10];
  p.cvec = (const u16*)d_in[11];
  p.w_mod = (const u16*)d_in[12];
  p.out = (u16*)d_out;
  p.ws = (char*)d_ws;
  mega<<<256, 512, 0, stream>>>(p);
}
```
